```python
import torch

def _softmax(x, axis=-1):
    return torch.softmax(x, dim=axis)
def _gelu(x):
    return torch.nn.functional.gelu(x, approximate="tanh")
def _silu(x):
    return torch.nn.functional.silu(x)
def _sigmoid(x):
    return torch.sigmoid(x)
def _layernorm(x, axis=-1, eps=1e-5):
    m = x.mean(dim=axis, keepdim=True)
    v = x.var(dim=axis, keepdim=True, unbiased=False)
    return (x - m) / torch.sqrt(v + eps)
def _scatter_add(tgt, idx, src, axis=0):
    return tgt.clone().index_add_(axis, idx, src)
def _segment_sum(src, seg, n):
    out = torch.zeros((n,) + src.shape[1:], dtype=src.dtype)
    return out.index_add_(0, seg, src)
def _pad(x, pw):
    flat = tuple(v for pair in reversed(pw) for v in pair)
    return torch.nn.functional.pad(x, flat)

kernel_name = 'standalone__hybrid_graph_tf_exphormer__graph'

def setup_inputs():

    _w = (lambda a: (lambda t: t.to(torch.bfloat16) if t.is_floating_point() else t)(torch.as_tensor(a)))
    N, deg, D = 10000, 16, 256
    E = N * deg
    exp_deg = 4
    Ee = N * exp_deg
    src = (lambda lo, hi, *s: torch.randint(lo, hi, s))(0, N, E).to(torch.int64)
    dst = (lambda lo, hi, *s: torch.randint(lo, hi, s))(0, N, E).to(torch.int64)
    esrc = (lambda lo, hi, *s: torch.randint(lo, hi, s))(0, N, Ee).to(torch.int64)
    edst = torch.repeat_interleave(torch.arange(N), exp_deg).to(torch.int64)
    deg_cnt = torch.bincount(dst, minlength=N).to(float) + 1.0
    return {
        "x": _w((lambda *s: torch.randn(*s, dtype=torch.bfloat16) * 0.05)(N, D)),
        "src": _w(src),
        "dst": _w(dst),
        "esrc": _w(esrc),
        "edst": _w(edst),
        "deg_inv_sqrt": _w(1.0 / (lambda _x: torch.sqrt(torch.as_tensor(_x)))(deg_cnt)),
        "w_l": _w((lambda *s: torch.randn(*s, dtype=torch.bfloat16) * 0.05)(D, D)),
        "wq": _w((lambda *s: torch.randn(*s, dtype=torch.bfloat16) * 0.05)(D, D)),
        "wk": _w((lambda *s: torch.randn(*s, dtype=torch.bfloat16) * 0.05)(D, D)),
        "wv": _w((lambda *s: torch.randn(*s, dtype=torch.bfloat16) * 0.05)(D, D)),
    }

def reference(x, src, dst, esrc, edst, deg_inv_sqrt, w_l, wq, wk, wv):
    N, D = x.shape
    h = x @ w_l
    msg = h[src] * deg_inv_sqrt[src, None] * deg_inv_sqrt[dst, None]
    h_local = _segment_sum(msg, dst, N)
    q = x @ wq
    k = x @ wk
    v = x @ wv
    e = (q[edst] * k[esrc]).sum(-1) * (D ** -0.5)
    exp_e = torch.exp(e)
    denom = _segment_sum(exp_e[:, None], edst, N)[:, 0] + 1e-9
    alpha = exp_e / denom[edst]
    h_attn = _segment_sum(alpha[:, None] * v[esrc], edst, N)
    return h_local + h_attn
```

```cpp
#include <hip/hip_runtime.h>
#include <hip/hip_bf16.h>
#include <stdint.h>

typedef unsigned short u16;
typedef unsigned int u32;
typedef __attribute__((ext_vector_type(4))) u32 u32x4;
typedef __attribute__((ext_vector_type(2))) u32 u32x2;
struct Desc { const void* p; u32 bytes, flags; };
struct Args {
    Desc x, ws, wl, wv, wq, wk, src, dst, esrc, edst, dis, out;
    u32 N, TN, M, irr;
    const u16* xall;
    const u16* disall;
    int E, Ee, nbase, pad;
};
constexpr u32 NMAX = 10240, EMAX = 163840;
constexpr u32 WS_END = 9437184;
constexpr int LDS_BYTES = 163840;
constexpr u32 L_INV = 512, L_MAP = 1792, L_QK = 2048, L_AL = 20480, L_AA = 43008, S_QK = 16, S_Z = 40, S_T0 = 48;

__device__ __forceinline__ float bf2f(u32 h) { return __uint_as_float(h << 16); }
__device__ __forceinline__ float lo(u32 v) { return __uint_as_float(v << 16); }
__device__ __forceinline__ float hi(u32 v) { return __uint_as_float(v & 0xFFFF0000u); }
__device__ __forceinline__ u32 pack2(float a, float b) {
    return (u32)__builtin_bit_cast(u16, (__bf16)a) | ((u32)__builtin_bit_cast(u16, (__bf16)b) << 16);
}
__device__ __forceinline__ void lds_signal(const u32 addr, const u32 v) {
    asm volatile("s_mov_b64 exec, 1\n\tds_add_u32 %0, %1\n\ts_mov_b64 exec, -1" :: "v"(addr), "v"(v) : "memory");
}

extern "C" __global__ void __launch_bounds__(1024, 4)
agg_kernel(const Args a) {
    extern __shared__ __attribute__((aligned(16))) char smem[];
    u32 tid = threadIdx.x, wv;
    asm volatile(
        "s_memrealtime s[74:75]\n\t"
        "s_load_dwordx16 s[16:31], %2, 0\n\t"
        "s_load_dwordx16 s[32:47], %2, 64\n\t"
        "s_load_dwordx16 s[48:63], %2, 128\n\t"
        "s_load_dwordx4 s[64:67], %2, 192\n\t"
        "v_readfirstlane_b32 s69, v0\n\t"
        "v_and_b32 v1, 63, v0\n\t"
        "v_mov_b32 v2, 0\n\t"
        "v_mov_b32 v3, 1\n\t"
        "s_lshr_b32 s76, %3, 3\n\t"
        "s_and_b32 s70, %3, 1\n\t"
        "s_lshr_b32 s71, %3, 1\n\t"
        "s_mov_b32 s83, 0x2000000\n\t"
        "s_lshr_b32 s69, s69, 6\n\t"
        "s_cbranch_scc0 .L7_U\n\t"
        "s_cmp_lt_u32 s69, 8\n\t"
        "s_cbranch_scc1 .L7_L\n\t"
        "s_sub_u32 s69, s69, 8\n\t"
        "v_lshlrev_b32 v4, 2, v1\n\t"
        "s_lshl_b32 s77, s70, 2\n\t"
        "s_xor_b32 s77, s77, s69\n\t"
        "s_xor_b32 s77, s77, 4\n\t"
        "s_mul_i32 s80, s77, 10\n\t"
        "s_waitcnt lgkmcnt(0)\n\t"
        "s_mul_i32 s72, s71, s65\n\t"
        "s_add_u32 s78, s72, s80\n\t"
        "s_lshl_b32 s78, s78, 4\n\t"
        "s_mov_b32 exec_hi, 0xff\n\t"
        "buffer_load_dword v5, v4, s[48:51], s78 offen\n\t"
        "s_mov_b64 exec, -1\n\t"
        "s_branch .L7_O2\n"
        ".L7_U:\n\t"
        "s_setprio 3\n\t"
        "s_cmp_eq_u32 s70, 0\n\t"
        "s_cbranch_scc0 .L7_V\n\t"
        "v_lshrrev_b32 v4, 3, v1\n\t"
        "v_and_b32 v5, 7, v1\n\t"
        "v_lshlrev_b32 v4, 14, v4\n\t"
        "v_lshl_add_u32 v4, v5, 4, v4\n\t"
        "s_lshl_b32 s78, s71, 7\n\t"
        "v_add_u32 v4, s78, v4\n\t"
        "v_add_u32 v5, 0x20000, v4\n\t"
        "v_add_u32 v6, 0x40000, v4\n\t"
        "v_add_u32 v7, 0x60000, v4\n\t"
        "v_add_u32 v8, 0x80000, v4\n\t"
        "s_waitcnt lgkmcnt(0)\n\t"
        "s_mul_i32 s72, s71, s65\n\t"
        "buffer_load_dwordx4 v[16:19], v4, s[44:47], 0 offen nt\n\t"
        "buffer_load_dwordx4 v[20:23], v5, s[44:47], 0 offen nt\n\t"
        "buffer_load_dwordx4 v[24:27], v6, s[44:47], 0 offen nt\n\t"
        "buffer_load_dwordx4 v[28:31], v7, s[44:47], 0 offen nt\n\t"
        "buffer_load_dwordx4 v[32:35], v8, s[44:47], 0 offen nt\n\t"
        "s_buffer_load_dword s68, s[20:23], 0\n\t"
        "buffer_load_dwordx4 v[36:39], v4, s[40:43], 0 offen nt\n\t"
        "buffer_load_dwordx4 v[40:43], v5, s[40:43], 0 offen nt\n\t"
        "buffer_load_dwordx4 v[44:47], v6, s[40:43], 0 offen nt\n\t"
        "buffer_load_dwordx4 v[48:51], v7, s[40:43], 0 offen nt\n\t"
        "buffer_load_dwordx4 v[52:55], v8, s[40:43], 0 offen nt\n\t"
        "s_mul_i32 s80, s71, 0x50\n\t"
        "s_lshl_b32 s78, s80, 4\n\t"
        "v_lshlrev_b32 v12, 4, v1\n\t"
        "buffer_load_dwordx4 v[116:119], v12, s[52:55], s78 offen nt\n\t"
        "s_mov_b64 exec, 0xffff\n\t"
        "buffer_load_dwordx4 v[120:123], v12, s[52:55], s78 offen offset:1024 nt\n\t"
        "s_mov_b64 exec, -1\n\t"
        "s_branch .L7_U2\n"
        ".L7_V:\n\t"
        "s_and_b32 s78, s71, 1\n\t"
        "s_and_b32 s79, s76, 3\n\t"
        "s_lshl2_add_u32 s80, s78, s79\n\t"
        "s_and_b32 s78, s71, 2\n\t"
        "s_lshr_b32 s79, s76, 2\n\t"
        "s_lshl2_add_u32 s81, s78, s79\n\t"
        "v_lshlrev_b32 v12, 4, v1\n\t"
        "v_and_b32 v5, 0x200, v12\n\t"
        "v_mad_u32_u24 v4, v5, 15, v12\n\t"
        "s_lshl_b32 s82, s80, 14\n\t"
        "s_lshr_b32 s78, s81, 1\n\t"
        "s_lshl_b32 s78, s78, 14\n\t"
        "s_and_b32 s79, s81, 1\n\t"
        "s_lshl_b32 s79, s79, 11\n\t"
        "s_add_u32 s77, s78, s79\n\t"
        "s_mov_b32 m0, 83968\n\t"
        "s_waitcnt lgkmcnt(0)\n\t"
        "s_mul_i32 s72, s71, s65\n\t"
        "s_buffer_load_dword s68, s[20:23], 0\n\t"
        "buffer_load_dwordx4 v4, s[36:39], s77 offen offset:0 lds\n\t"
        "s_add_u32 m0, m0, 544\n\t"
        "s_nop 0\n\t"
        "buffer_load_dwordx4 v4, s[36:39], s77 offen offset:512 lds\n\t"
        "s_add_u32 m0, m0, 544\n\t"
        "s_nop 0\n\t"
        "buffer_load_dwordx4 v4, s[36:39], s77 offen offset:1024 lds\n\t"
        "s_add_u32 m0, m0, 544\n\t"
        "s_nop 0\n\t"
        "buffer_load_dwordx4 v4, s[36:39], s77 offen offset:1536 lds\n\t"
        "s_add_u32 m0, m0, 2592\n\t"
        "s_add_u32 s77, s77, 0x1000\n\t"
        "buffer_load_dwordx4 v4, s[36:39], s77 offen offset:0 lds\n\t"
        "s_add_u32 m0, m0, 544\n\t"
        "s_nop 0\n\t"
        "buffer_load_dwordx4 v4, s[36:39], s77 offen offset:512 lds\n\t"
        "s_add_u32 m0, m0, 544\n\t"
        "s_nop 0\n\t"
        "buffer_load_dwordx4 v4, s[36:39], s77 offen offset:1024 lds\n\t"
        "s_add_u32 m0, m0, 544\n\t"
        "s_nop 0\n\t"
        "buffer_load_dwordx4 v4, s[36:39], s77 offen offset:1536 lds\n\t"
        "s_add_u32 m0, m0, 2592\n\t"
        "s_nop 0\n\t"
        "buffer_load_dwordx4 v4, s[32:35], s82 offen offset:0 lds\n\t"
        "s_add_u32 m0, m0, 544\n\t"
        "s_nop 0\n\t"
        "buffer_load_dwordx4 v4, s[32:35], s82 offen offset:512 lds\n\t"
        "s_add_u32 m0, m0, 544\n\t"
        "s_nop 0\n\t"
        "buffer_load_dwordx4 v4, s[32:35], s82 offen offset:1024 lds\n\t"
        "s_add_u32 m0, m0, 544\n\t"
        "s_nop 0\n\t"
        "buffer_load_dwordx4 v4, s[32:35], s82 offen offset:1536 lds\n\t"
        "s_add_u32 m0, m0, 2592\n\t"
        "s_add_u32 s82, s82, 0x1000\n\t"
        "buffer_load_dwordx4 v4, s[32:35], s82 offen offset:0 lds\n\t"
        "s_add_u32 m0, m0, 544\n\t"
        "s_nop 0\n\t"
        "buffer_load_dwordx4 v4, s[32:35], s82 offen offset:512 lds\n\t"
        "s_add_u32 m0, m0, 544\n\t"
        "s_nop 0\n\t"
        "buffer_load_dwordx4 v4, s[32:35], s82 offen offset:1024 lds\n\t"
        "s_add_u32 m0, m0, 544\n\t"
        "s_nop 0\n\t"
        "buffer_load_dwordx4 v4, s[32:35], s82 offen offset:1536 lds\n\t"
        "s_add_u32 m0, m0, 2592\n\t"
        "s_sub_u32 s82, s82, 0x800\n\t"
        "buffer_load_dwordx4 v4, s[32:35], s82 offen offset:0 lds\n\t"
        "s_add_u32 m0, m0, 544\n\t"
        "s_nop 0\n\t"
        "buffer_load_dwordx4 v4, s[32:35], s82 offen offset:512 lds\n\t"
        "s_add_u32 m0, m0, 544\n\t"
        "s_nop 0\n\t"
        "buffer_load_dwordx4 v4, s[32:35], s82 offen offset:1024 lds\n\t"
        "s_add_u32 m0, m0, 544\n\t"
        "s_nop 0\n\t"
        "buffer_load_dwordx4 v4, s[32:35], s82 offen offset:1536 lds\n\t"
        "s_add_u32 m0, m0, 2592\n\t"
        "s_add_u32 s82, s82, 0x1000\n\t"
        "buffer_load_dwordx4 v4, s[32:35], s82 offen offset:0 lds\n\t"
        "s_add_u32 m0, m0, 544\n\t"
        "s_nop 0\n\t"
        "buffer_load_dwordx4 v4, s[32:35], s82 offen offset:512 lds\n\t"
        "s_add_u32 m0, m0, 544\n\t"
        "s_nop 0\n\t"
        "buffer_load_dwordx4 v4, s[32:35], s82 offen offset:1024 lds\n\t"
        "s_add_u32 m0, m0, 544\n\t"
        "s_nop 0\n\t"
        "buffer_load_dwordx4 v4, s[32:35], s82 offen offset:1536 lds\n\t"
        "s_branch .L7_V2\n"
        ".L7_L:\n\t"
        "s_cmp_lt_u32 s69, 4\n\t"
        "s_cbranch_scc0 .L7_G\n\t"
        "s_cmp_eq_u32 s69, 2\n\t"
        "s_cbranch_scc1 .L7_P2\n\t"
        "s_cmp_eq_u32 s69, 1\n\t"
        "s_cbranch_scc0 .L7_T\n\t"
        "v_lshlrev_b32 v4, 3, v1\n\t"
        "v_mov_b64 v[8:9], 0\n\t"
        "ds_write_b64 v4, v[8:9]\n\t"
        "s_waitcnt lgkmcnt(0)\n\t"
        "s_mul_i32 s72, s71, s65\n\t"
        "v_add_lshl_u32 v4, v1, s72, 1\n\t"
        "buffer_load_ushort v16, v4, s[56:59], 0 offen\n\t"
        "buffer_load_ushort v17, v4, s[56:59], 0 offen offset:128\n\t"
        "s_branch .L7_R2\n"
        ".L7_T:\n\t"
        "v_lshlrev_b32 v6, 7, v1\n\t"
        "s_mov_b32 m0, 149504\n\t"
        "s_lshl_b32 s78, s70, 16\n\t"
        "s_lshl_b32 s79, s76, 11\n\t"
        "s_add_u32 s80, s78, s79\n\t"
        "s_mul_i32 s81, s76, 0x300\n\t"
        "s_waitcnt lgkmcnt(0)\n\t"
        "s_mul_i32 s72, s71, s65\n\t"
        "s_getpc_b64 s[32:33]\n\t"
        "s_add_u32 s32, s32, agg_kernel@rel32@lo+4\n\t"
        "s_addc_u32 s33, s33, agg_kernel@rel32@hi+12\n\t"
        "s_and_b32 s33, s33, 0xffff\n\t"
        "s_mov_b32 s34, .L7_x-agg_kernel\n\t"
        "s_mov_b64 exec, 63\n\t"
        "buffer_load_dword v6, s[32:35], s81 offen lds\n\t"
        "s_barrier\n\t"
        "s_mov_b64 exec, -1\n\t"
        "s_mov_b64 exec, 0xffff\n\t"
        "buffer_load_dword v6, s[28:31], s80 offen lds\n\t"
        "buffer_load_dword v6, s[24:27], s80 offen lds\n\t"
        "s_mov_b64 exec, -1\n\t"
        "s_branch .L7_Q0\n"
        ".L7_G:\n\t"
        "s_sub_u32 s69, s69, 4\n\t"
        "v_lshlrev_b32 v4, 4, v1\n\t"
        "v_and_b32 v5, 0x200, v4\n\t"
        "v_mad_u32_u24 v4, v5, 7, v4\n\t"
        "s_mul_i32 s80, s70, 5\n\t"
        "s_mul_i32 s81, s69, 0x420\n\t"
        "s_add_u32 s81, s81, 2048\n\t"
        "s_mul_i32 s78, s80, 0x1080\n\t"
        "s_add_u32 m0, s78, s81\n\t"
        "s_lshr_b32 s78, s80, 1\n\t"
        "s_lshl1_add_u32 s78, s78, s80\n\t"
        "s_lshl_b32 s78, s78, 11\n\t"
        "s_waitcnt lgkmcnt(0)\n\t"
        "s_mul_i32 s72, s71, s65\n\t"
        "s_add_u32 s79, s72, s69\n\t"
        "s_lshl_b32 s79, s79, 9\n\t"
        "v_add_u32 v4, s79, v4\n\t"
        "buffer_load_dwordx4 v4, s[16:19], s78 offen lds\n\t"
        "s_add_u32 s80, s80, 1\n\t"
        "s_mul_i32 s78, s80, 0x1080\n\t"
        "s_add_u32 m0, s78, s81\n\t"
        "s_lshr_b32 s78, s80, 1\n\t"
        "s_lshl1_add_u32 s78, s78, s80\n\t"
        "s_lshl_b32 s78, s78, 11\n\t"
        "buffer_load_dwordx4 v4, s[16:19], s78 offen lds\n\t"
        "s_add_u32 s80, s80, 1\n\t"
        "s_mul_i32 s78, s80, 0x1080\n\t"
        "s_add_u32 m0, s78, s81\n\t"
        "s_lshr_b32 s78, s80, 1\n\t"
        "s_lshl1_add_u32 s78, s78, s80\n\t"
        "s_lshl_b32 s78, s78, 11\n\t"
        "buffer_load_dwordx4 v4, s[16:19], s78 offen lds\n\t"
        "s_add_u32 s80, s80, 1\n\t"
        "s_mul_i32 s78, s80, 0x1080\n\t"
        "s_add_u32 m0, s78, s81\n\t"
        "s_lshr_b32 s78, s80, 1\n\t"
        "s_lshl1_add_u32 s78, s78, s80\n\t"
        "s_lshl_b32 s78, s78, 11\n\t"
        "buffer_load_dwordx4 v4, s[16:19], s78 offen lds\n\t"
        "s_add_u32 s80, s80, 1\n\t"
        "s_mul_i32 s78, s80, 0x1080\n\t"
        "s_add_u32 m0, s78, s81\n\t"
        "s_lshr_b32 s78, s80, 1\n\t"
        "s_lshl1_add_u32 s78, s78, s80\n\t"
        "s_lshl_b32 s78, s78, 11\n\t"
        "buffer_load_dwordx4 v4, s[16:19], s78 offen lds\n\t"
        "s_add_u32 s80, s80, 1\n\t"
        "s_branch .L7_G2\n"
        ".L7_O2:\n\t"
        "s_barrier\n\t"
        "v_lshrrev_b32 v6, 4, v1\n\t"
        "v_lshlrev_b32 v7, 2, v6\n\t"
        "v_and_b32 v8, 15, v1\n\t"
        "s_lshl_b32 s78, s70, 8\n\t"
        "v_lshl_add_u32 v9, v8, 4, s78\n\t"
        "v_lshrrev_b32 v10, 3, v1\n\t"
        "v_lshlrev_b32 v10, 9, v10\n\t"
        "v_and_b32 v11, 7, v1\n\t"
        "v_lshl_add_u32 v10, v11, 4, v10\n\t"
        "v_and_b32 v11, 16, v1\n\t"
        "v_lshlrev_b32 v11, 2, v11\n\t"
        "v_xor_b32 v10, v10, v11\n\t"
        "s_add_u32 s81, s69, s76\n\t"
        "s_and_b32 s81, s81, 7\n\t"
        "s_lshr_b32 s78, s81, 1\n\t"
        "s_lshl_b32 s78, s78, 7\n\t"
        "s_and_b32 s79, s81, 1\n\t"
        "s_lshl_b32 s79, s79, 15\n\t"
        "s_add_u32 s78, s78, s79\n\t"
        "s_lshl_b32 s79, s70, 16\n\t"
        "s_add_u32 s78, s78, s79\n\t"
        "v_add_u32 v10, s78, v10\n\t"
        "s_lshl_b32 s82, s81, 13\n\t"
        "s_add_u32 s82, s82, 83968\n\t"
        "v_bfe_u32 v12, v1, 2, 2\n\t"
        "v_lshlrev_b32 v11, 10, v6\n\t"
        "v_lshl_add_u32 v11, v12, 7, v11\n\t"
        "v_lshrrev_b32 v12, 1, v12\n\t"
        "s_and_b32 s78, s81, 1\n\t"
        "v_xor_b32 v12, s78, v12\n\t"
        "v_lshl_add_u32 v11, v12, 6, v11\n\t"
        "v_and_b32 v12, 3, v1\n\t"
        "v_lshl_add_u32 v11, v12, 4, v11\n\t"
        "s_lshr_b32 s78, s81, 1\n\t"
        "s_lshl_b32 s78, s78, 14\n\t"
        "s_add_u32 s78, s78, 83968\n\t"
        "v_add_u32 v23, s78, v11\n\t"
        "s_waitcnt vmcnt(0)\n\t"
        "ds_bpermute_b32 v12, v7, v5 offset:0\n\t"
        "ds_bpermute_b32 v13, v7, v5 offset:16\n\t"
        "ds_bpermute_b32 v14, v7, v5 offset:32\n\t"
        "ds_bpermute_b32 v15, v7, v5 offset:48\n\t"
        "ds_bpermute_b32 v16, v7, v5 offset:64\n\t"
        "ds_bpermute_b32 v17, v7, v5 offset:80\n\t"
        "ds_bpermute_b32 v18, v7, v5 offset:96\n\t"
        "ds_bpermute_b32 v19, v7, v5 offset:112\n\t"
        "ds_bpermute_b32 v20, v7, v5 offset:128\n\t"
        "ds_bpermute_b32 v21, v7, v5 offset:144\n\t"
        "s_waitcnt lgkmcnt(0)\n\t"
        "v_lshl_add_u32 v12, v12, 9, v9\n\t"
        "v_lshl_add_u32 v13, v13, 9, v9\n\t"
        "v_lshl_add_u32 v14, v14, 9, v9\n\t"
        "v_lshl_add_u32 v15, v15, 9, v9\n\t"
        "v_lshl_add_u32 v16, v16, 9, v9\n\t"
        "v_lshl_add_u32 v17, v17, 9, v9\n\t"
        "v_lshl_add_u32 v18, v18, 9, v9\n\t"
        "v_lshl_add_u32 v19, v19, 9, v9\n\t"
        "v_lshl_add_u32 v20, v20, 9, v9\n\t"
        "v_lshl_add_u32 v21, v21, 9, v9\n\t"
        "s_setprio 0\n"
        ".L7_w1:\n\t"
        "ds_read_b32 v0, v2 offset:44\n\t"
        "s_waitcnt lgkmcnt(0)\n\t"
        "v_cmp_gt_u16 vcc, 1, v0\n\t"
        "s_cbranch_vccz .L7_d2\n\t"
        "s_sleep 1\n\t"
        "s_sub_i32 s83, s83, 1\n\t"
        "s_cmp_gt_i32 s83, 0\n\t"
        "s_cbranch_scc1 .L7_w1\n"
        ".L7_d2:\n"
        ".L7_w3:\n\t"
        "ds_read_b64 v[4:5], v2 offset:72\n\t"
        "s_waitcnt lgkmcnt(0)\n\t"
        "v_cmp_gt_u16 vcc, 1, v4\n\t"
        "s_cbranch_vccz .L7_d4\n\t"
        "s_sleep 1\n\t"
        "s_sub_i32 s83, s83, 1\n\t"
        "s_cmp_gt_i32 s83, 0\n\t"
        "s_cbranch_scc1 .L7_w3\n"
        ".L7_d4:\n\t"
        "v_readfirstlane_b32 s68, v5\n\t"
        "s_add_u32 m0, s82, 0\n\t"
        "s_mov_b32 s78, 0\n\t"
        "buffer_load_dwordx4 v10, s[28:31], s78 offen lds\n\t"
        "s_add_u32 m0, m0, 0x400\n\t"
        "s_add_u32 s78, s78, 0x1000\n\t"
        "buffer_load_dwordx4 v10, s[28:31], s78 offen lds\n\t"
        "s_add_u32 m0, m0, 0x400\n\t"
        "s_add_u32 s78, s78, 0x1000\n\t"
        "buffer_load_dwordx4 v10, s[28:31], s78 offen lds\n\t"
        "s_add_u32 m0, m0, 0x400\n\t"
        "s_add_u32 s78, s78, 0x1000\n\t"
        "buffer_load_dwordx4 v10, s[28:31], s78 offen lds\n\t"
        "s_add_u32 m0, m0, 0x400\n\t"
        "s_add_u32 s78, s78, 0x1000\n\t"
        "buffer_load_dwordx4 v10, s[28:31], s78 offen lds\n\t"
        "s_add_u32 m0, m0, 0x400\n\t"
        "s_add_u32 s78, s78, 0x1000\n\t"
        "buffer_load_dwordx4 v10, s[28:31], s78 offen lds\n\t"
        "s_add_u32 m0, m0, 0x400\n\t"
        "s_add_u32 s78, s78, 0x1000\n\t"
        "buffer_load_dwordx4 v10, s[28:31], s78 offen lds\n\t"
        "s_add_u32 m0, m0, 0x400\n\t"
        "s_add_u32 s78, s78, 0x1000\n\t"
        "buffer_load_dwordx4 v10, s[28:31], s78 offen lds\n\t"
        "s_mov_b64 exec, 1\n\t"
        "ds_add_u32 v2, v3 offset:88\n\t"
        "s_mov_b64 exec, -1\n"
        ".L7_w5:\n\t"
        "ds_read_b32 v0, v2 offset:84\n\t"
        "s_waitcnt lgkmcnt(0)\n\t"
        "v_cmp_gt_u16 vcc, 4, v0\n\t"
        "s_cbranch_vccz .L7_d6\n\t"
        "s_sleep 1\n\t"
        "s_sub_i32 s83, s83, 1\n\t"
        "s_cmp_gt_i32 s83, 0\n\t"
        "s_cbranch_scc1 .L7_w5\n"
        ".L7_d6:\n\t"
        "buffer_load_dwordx4 v[24:27], v12, s[16:19], 0 offen\n\t"
        "buffer_load_dwordx4 v[28:31], v13, s[16:19], 0 offen\n\t"
        "buffer_load_dwordx4 v[32:35], v14, s[16:19], 0 offen\n\t"
        "buffer_load_dwordx4 v[36:39], v15, s[16:19], 0 offen\n\t"
        "buffer_load_dwordx4 v[40:43], v16, s[16:19], 0 offen\n\t"
        "s_waitcnt vmcnt(5)\n\t"
        "s_mov_b64 exec, 1\n\t"
        "ds_add_u32 v2, v3 offset:20\n\t"
        "s_mov_b64 exec, -1\n"
        ".L7_w7:\n\t"
        "ds_read_b32 v0, v2 offset:20\n\t"
        "s_waitcnt lgkmcnt(0)\n\t"
        "v_cmp_gt_u16 vcc, 8, v0\n\t"
        "s_cbranch_vccz .L7_d8\n\t"
        "s_sleep 1\n\t"
        "s_sub_i32 s83, s83, 1\n\t"
        "s_cmp_gt_i32 s83, 0\n\t"
        "s_cbranch_scc1 .L7_w7\n"
        ".L7_d8:\n\t"
        "ds_read_b64_tr_b16 v[64:65], v23 offset:0\n\t"
        "ds_read_b64_tr_b16 v[66:67], v23 offset:512\n\t"
        "ds_read_b64_tr_b16 v[68:69], v23 offset:8\n\t"
        "ds_read_b64_tr_b16 v[70:71], v23 offset:520\n\t"
        "ds_read_b64_tr_b16 v[72:73], v23 offset:4096\n\t"
        "ds_read_b64_tr_b16 v[74:75], v23 offset:4608\n\t"
        "ds_read_b64_tr_b16 v[76:77], v23 offset:4104\n\t"
        "ds_read_b64_tr_b16 v[78:79], v23 offset:4616\n\t"
        "ds_read_b64_tr_b16 v[80:81], v23 offset:8192\n\t"
        "ds_read_b64_tr_b16 v[82:83], v23 offset:8704\n\t"
        "ds_read_b64_tr_b16 v[84:85], v23 offset:8200\n\t"
        "ds_read_b64_tr_b16 v[86:87], v23 offset:8712\n\t"
        "ds_read_b64_tr_b16 v[88:89], v23 offset:12288\n\t"
        "ds_read_b64_tr_b16 v[90:91], v23 offset:12800\n\t"
        "s_waitcnt lgkmcnt(10)\n\t"
        "ds_read_b64_tr_b16 v[92:93], v23 offset:12296\n\t"
        "ds_read_b64_tr_b16 v[94:95], v23 offset:12808\n\t"
        "s_mov_b64 exec, 1\n\t"
        "ds_add_u32 v2, v3 offset:24\n\t"
        "s_mov_b64 exec, -1\n\t"
        "buffer_load_dwordx4 v[44:47], v17, s[16:19], 0 offen\n\t"
        "buffer_load_dwordx4 v[48:51], v18, s[16:19], 0 offen\n\t"
        "buffer_load_dwordx4 v[52:55], v19, s[16:19], 0 offen\n\t"
        "buffer_load_dwordx4 v[56:59], v20, s[16:19], 0 offen\n\t"
        "buffer_load_dwordx4 v[60:63], v21, s[16:19], 0 offen\n\t"
        "s_lshl_b32 s78, s80, 4\n\t"
        "v_lshlrev_b32 v8, 4, v8\n\t"
        "s_add_u32 s79, s78, 0\n\t"
        "s_and_b32 s79, s79, 0xf0\n\t"
        "v_xor_b32 v12, s79, v8\n\t"
        "s_add_u32 s79, s78, 16\n\t"
        "s_and_b32 s79, s79, 0xf0\n\t"
        "v_xor_b32 v13, s79, v8\n\t"
        "s_add_u32 s79, s78, 32\n\t"
        "s_and_b32 s79, s79, 0xf0\n\t"
        "v_xor_b32 v14, s79, v8\n\t"
        "s_add_u32 s79, s78, 48\n\t"
        "s_and_b32 s79, s79, 0xf0\n\t"
        "v_xor_b32 v15, s79, v8\n\t"
        "s_add_u32 s79, s78, 64\n\t"
        "s_and_b32 s79, s79, 0xf0\n\t"
        "v_xor_b32 v16, s79, v8\n\t"
        "s_add_u32 s79, s78, 80\n\t"
        "s_and_b32 s79, s79, 0xf0\n\t"
        "v_xor_b32 v17, s79, v8\n\t"
        "s_add_u32 s79, s78, 96\n\t"
        "s_and_b32 s79, s79, 0xf0\n\t"
        "v_xor_b32 v18, s79, v8\n\t"
        "s_add_u32 s79, s78, 112\n\t"
        "s_and_b32 s79, s79, 0xf0\n\t"
        "v_xor_b32 v19, s79, v8\n\t"
        "s_add_u32 s79, s78, 128\n\t"
        "s_and_b32 s79, s79, 0xf0\n\t"
        "v_xor_b32 v20, s79, v8\n\t"
        "s_add_u32 s79, s78, 144\n\t"
        "s_and_b32 s79, s79, 0xf0\n\t"
        "v_xor_b32 v21, s79, v8\n\t"
        "s_lshl_b32 s78, s80, 8\n\t"
        "v_add_u32 v12, s78, v12\n\t"
        "v_add_u32 v13, s78, v13\n\t"
        "v_add_u32 v14, s78, v14\n\t"
        "v_add_u32 v15, s78, v15\n\t"
        "v_add_u32 v16, s78, v16\n\t"
        "v_add_u32 v17, s78, v17\n\t"
        "v_add_u32 v18, s78, v18\n\t"
        "v_add_u32 v19, s78, v19\n\t"
        "v_add_u32 v20, s78, v20\n\t"
        "v_add_u32 v21, s78, v21\n\t"
        "s_setprio 2\n\t"
        "s_mov_b32 s36, 0x22222222\n\t"
        "s_mov_b32 s37, s36\n\t"
        "s_lshl_b64 s[38:39], s[36:37], 1\n\t"
        "s_lshl_b64 s[40:41], s[36:37], 2\n\t"
        "s_and_b32 s44, s68, 0xfff\n\t"
        "s_xor_b32 s45, s44, 0x5a5\n\t"
        "s_lshr_b32 s46, s68, 12\n\t"
        "s_lshr_b32 s47, s68, 20\n\t"
        "s_add_u32 s46, s46, s68\n\t"
        "s_and_b32 s46, s46, 0xfff\n\t"
        "s_add_u32 s47, s47, s68\n\t"
        "s_and_b32 s47, s47, 0xfff\n\t"
        "v_mov_b32 v22, s44\n\t"
        "v_mov_b32 v0, s45\n\t"
        "v_cndmask_b32 v22, v22, v0, s[36:37]\n\t"
        "v_mov_b32 v0, s46\n\t"
        "v_cndmask_b32 v22, v22, v0, s[38:39]\n\t"
        "v_mov_b32 v0, s47\n\t"
        "v_cndmask_b32 v22, v22, v0, s[40:41]\n\t"
        "s_lshl3_add_u32 s42, %3, s77\n\t"
        "s_lshl_b32 s42, s42, 8\n\t"
        "s_add_u32 s42, s42, 262144\n\t"
        "s_xor_b32 s43, s42, 0x800\n\t"
        "s_movk_i32 s48, 0xfff\n"
        ".L7_w9:\n\t"
        "ds_read_b32 v0, v2 offset:16\n\t"
        "s_waitcnt lgkmcnt(0)\n\t"
        "v_cmp_gt_u16 vcc, 4, v0\n\t"
        "s_cbranch_vccz .L7_d10\n\t"
        "s_sleep 1\n\t"
        "s_sub_i32 s83, s83, 1\n\t"
        "s_cmp_gt_i32 s83, 0\n\t"
        "s_cbranch_scc1 .L7_w9\n"
        ".L7_d10:\n\t"
        "ds_read_b128 v[96:99], v12 offset:2048\n\t"
        "ds_read_b128 v[100:103], v13 offset:2304\n\t"
        "ds_read_b128 v[104:107], v14 offset:2560\n\t"
        "ds_read_b128 v[108:111], v15 offset:2816\n\t"
        "ds_read_b128 v[112:115], v16 offset:3072\n\t"
        "ds_read_b128 v[116:119], v17 offset:3328\n\t"
        "ds_read_b128 v[120:123], v18 offset:3584\n\t"
        "ds_read_b128 v[124:127], v19 offset:3840\n\t"
        "ds_read_b128 v[4:7], v20 offset:4096\n\t"
        "ds_read_b128 v[8:11], v21 offset:4352\n\t"
        "s_waitcnt vmcnt(0)\n\t"
        "s_waitcnt lgkmcnt(8)\n\t"
        "v_dot2_f32_bf16 v12, v24, v96, 0\n\t"
        "v_dot2_f32_bf16 v13, v28, v100, 0\n\t"
        "v_dot2_f32_bf16 v12, v25, v97, v12\n\t"
        "v_dot2_f32_bf16 v13, v29, v101, v13\n\t"
        "v_dot2_f32_bf16 v12, v26, v98, v12\n\t"
        "v_dot2_f32_bf16 v13, v30, v102, v13\n\t"
        "v_dot2_f32_bf16 v12, v27, v99, v12\n\t"
        "v_dot2_f32_bf16 v13, v31, v103, v13\n\t"
        "s_waitcnt lgkmcnt(6)\n\t"
        "v_dot2_f32_bf16 v14, v32, v104, 0\n\t"
        "v_dot2_f32_bf16 v15, v36, v108, 0\n\t"
        "v_dot2_f32_bf16 v14, v33, v105, v14\n\t"
        "v_dot2_f32_bf16 v15, v37, v109, v15\n\t"
        "v_dot2_f32_bf16 v14, v34, v106, v14\n\t"
        "v_dot2_f32_bf16 v15, v38, v110, v15\n\t"
        "v_dot2_f32_bf16 v14, v35, v107, v14\n\t"
        "v_dot2_f32_bf16 v15, v39, v111, v15\n\t"
        "s_waitcnt lgkmcnt(4)\n\t"
        "v_dot2_f32_bf16 v16, v40, v112, 0\n\t"
        "v_dot2_f32_bf16 v17, v44, v116, 0\n\t"
        "v_dot2_f32_bf16 v16, v41, v113, v16\n\t"
        "v_dot2_f32_bf16 v17, v45, v117, v17\n\t"
        "v_dot2_f32_bf16 v16, v42, v114, v16\n\t"
        "v_dot2_f32_bf16 v17, v46, v118, v17\n\t"
        "v_dot2_f32_bf16 v16, v43, v115, v16\n\t"
        "v_dot2_f32_bf16 v17, v47, v119, v17\n\t"
        "s_waitcnt lgkmcnt(2)\n\t"
        "v_dot2_f32_bf16 v18, v48, v120, 0\n\t"
        "v_dot2_f32_bf16 v19, v52, v124, 0\n\t"
        "v_dot2_f32_bf16 v18, v49, v121, v18\n\t"
        "v_dot2_f32_bf16 v19, v53, v125, v19\n\t"
        "v_dot2_f32_bf16 v18, v50, v122, v18\n\t"
        "v_dot2_f32_bf16 v19, v54, v126, v19\n\t"
        "v_dot2_f32_bf16 v18, v51, v123, v18\n\t"
        "v_dot2_f32_bf16 v19, v55, v127, v19\n\t"
        "s_waitcnt lgkmcnt(0)\n\t"
        "v_dot2_f32_bf16 v20, v56, v4, 0\n\t"
        "v_dot2_f32_bf16 v21, v60, v8, 0\n\t"
        "v_dot2_f32_bf16 v20, v57, v5, v20\n\t"
        "v_dot2_f32_bf16 v21, v61, v9, v21\n\t"
        "v_dot2_f32_bf16 v20, v58, v6, v20\n\t"
        "v_dot2_f32_bf16 v21, v62, v10, v21\n\t"
        "v_dot2_f32_bf16 v20, v59, v7, v20\n\t"
        "v_dot2_f32_bf16 v21, v63, v11, v21\n\t"
        "v_add_f32_dpp v12, v12, v12 row_ror:8 row_mask:0xf bank_mask:0x3\n\t"
        "v_add_f32_dpp v14, v14, v14 row_ror:8 row_mask:0xf bank_mask:0x3\n\t"
        "v_add_f32_dpp v16, v16, v16 row_ror:8 row_mask:0xf bank_mask:0x3\n\t"
        "v_add_f32_dpp v18, v18, v18 row_ror:8 row_mask:0xf bank_mask:0x3\n\t"
        "v_add_f32_dpp v20, v20, v20 row_ror:8 row_mask:0xf bank_mask:0x3\n\t"
        "v_add_f32_dpp v12, v13, v13 row_ror:8 row_mask:0xf bank_mask:0xc\n\t"
        "v_add_f32_dpp v14, v15, v15 row_ror:8 row_mask:0xf bank_mask:0xc\n\t"
        "v_add_f32_dpp v16, v17, v17 row_ror:8 row_mask:0xf bank_mask:0xc\n\t"
        "v_add_f32_dpp v18, v19, v19 row_ror:8 row_mask:0xf bank_mask:0xc\n\t"
        "v_add_f32_dpp v20, v21, v21 row_ror:8 row_mask:0xf bank_mask:0xc\n\t"
        "v_add_f32_dpp v12, v12, v12 row_half_mirror row_mask:0xf bank_mask:0x5\n\t"
        "v_add_f32_dpp v16, v16, v16 row_half_mirror row_mask:0xf bank_mask:0x5\n\t"
        "v_add_f32_dpp v20, v20, v20 row_half_mirror row_mask:0xf bank_mask:0xf\n\t"
        "v_add_f32_dpp v12, v14, v14 row_half_mirror row_mask:0xf bank_mask:0xa\n\t"
        "v_add_f32_dpp v16, v18, v18 row_half_mirror row_mask:0xf bank_mask:0xa\n\t"
        "v_add_f32_dpp v20, v20, v20 quad_perm:[1,0,3,2] row_mask:0xf bank_mask:0xf\n\t"
        "v_add_f32_dpp v12, v12, v12 quad_perm:[1,0,3,2] row_mask:0xf bank_mask:0xf\n\t"
        "v_add_f32_dpp v16, v16, v16 quad_perm:[1,0,3,2] row_mask:0xf bank_mask:0xf\n\t"
        "v_add_f32_dpp v20, v20, v20 quad_perm:[2,3,0,1] row_mask:0xf bank_mask:0xf\n\t"
        "v_add_f32_dpp v12, v12, v12 quad_perm:[2,3,0,1] row_mask:0xf bank_mask:0xf\n\t"
        "v_add_f32_dpp v16, v16, v16 quad_perm:[2,3,0,1] row_mask:0xf bank_mask:0xf\n\t"
        "v_cndmask_b32 v12, v12, v16, s[36:37]\n\t"
        "v_cndmask_b32 v12, v12, v20, s[38:39]\n\t"
        "v_add_u32 v12, 0x800, v12\n\t"
        "v_bfi_b32 v21, s48, v22, v12\n\t"
        "v_lshlrev_b32 v13, 2, v1\n\t"
        "s_setprio 3\n\t"
        "buffer_store_dword v21, v13, s[20:23], s42 offen sc1\n\t"
        "s_setprio 2\n\t"
        "v_and_b32 v14, 3, v1\n\t"
        "v_lshlrev_b32 v14, 4, v14\n\t"
        "s_mov_b32 s78, 0xffff\n\t"
        "s_mov_b32 s79, 0\n\t"
        "v_lshlrev_b64 v[4:5], v14, s[78:79]\n\t"
        "s_mov_b32 s50, 0x0f0f0f0f\n\t"
        "s_mov_b32 s51, s50\n\t"
        "v_cndmask_b32 v6, v4, 0, s[50:51]\n\t"
        "v_cndmask_b32 v7, v5, 0, s[50:51]\n\t"
        "v_cndmask_b32 v4, 0, v4, s[50:51]\n\t"
        "v_cndmask_b32 v5, 0, v5, s[50:51]\n\t"
        "v_lshrrev_b32 v14, 5, v1\n\t"
        "v_add_lshl_u32 v14, v14, s80, 1\n"
        ".L7_w11:\n\t"
        "ds_read_b32 v0, v2 offset:8\n\t"
        "ds_read_u16 v16, v14 offset:1792\n\t"
        "ds_read_u16 v17, v14 offset:1796\n\t"
        "ds_read_u16 v18, v14 offset:1800\n\t"
        "ds_read_u16 v19, v14 offset:1804\n\t"
        "ds_read_u16 v20, v14 offset:1808\n\t"
        "s_waitcnt lgkmcnt(5)\n\t"
        "v_cmp_gt_u16 vcc, 2, v0\n\t"
        "s_cbranch_vccz .L7_d12\n\t"
        "s_waitcnt lgkmcnt(0)\n\t"
        "s_sleep 1\n\t"
        "s_sub_i32 s83, s83, 1\n\t"
        "s_cmp_gt_i32 s83, 0\n\t"
        "s_cbranch_scc1 .L7_w11\n"
        ".L7_d12:\n\t"
        "v_and_b32 v14, 15, v1\n\t"
        "v_lshlrev_b32 v14, 4, v14\n\t"
        "v_bfe_u32 v15, v1, 4, 1\n\t"
        "v_lshl_add_u32 v14, v15, 3, v14\n\t"
        "s_waitcnt lgkmcnt(0)\n\t"
        "v_xor_b32 v16, v16, v14\n\t"
        "v_xor_b32 v17, v17, v14\n\t"
        "v_xor_b32 v18, v18, v14\n\t"
        "v_xor_b32 v19, v19, v14\n\t"
        "v_xor_b32 v20, v20, v14\n\t"
        "v_xor_b32 v21, v21, v22\n\t"
        "v_mov_b64 v[8:9], 0\n\t"
        "v_mov_b64 v[10:11], 0\n\t"
        "buffer_load_dword v12, v13, s[20:23], s43 offen sc1\n"
        ".L7_w13:\n\t"
        "ds_read_b32 v0, v2 offset:28\n\t"
        "s_waitcnt lgkmcnt(0)\n\t"
        "v_cmp_gt_u16 vcc, 4, v0\n\t"
        "s_cbranch_vccz .L7_d14\n\t"
        "s_sleep 1\n\t"
        "s_sub_i32 s83, s83, 1\n\t"
        "s_cmp_gt_i32 s83, 0\n\t"
        "s_cbranch_scc1 .L7_w13\n"
        ".L7_d14:\n\t"
        "ds_read_b64_tr_b16 v[96:97], v23 offset:0\n\t"
        "ds_read_b64_tr_b16 v[98:99], v23 offset:512\n\t"
        "ds_read_b64_tr_b16 v[100:101], v23 offset:8\n\t"
        "ds_read_b64_tr_b16 v[102:103], v23 offset:520\n\t"
        "ds_read_b64_tr_b16 v[104:105], v23 offset:4096\n\t"
        "ds_read_b64_tr_b16 v[106:107], v23 offset:4608\n\t"
        "ds_read_b64_tr_b16 v[108:109], v23 offset:4104\n\t"
        "ds_read_b64_tr_b16 v[110:111], v23 offset:4616\n\t"
        "ds_read_b64_tr_b16 v[112:113], v23 offset:8192\n\t"
        "ds_read_b64_tr_b16 v[114:115], v23 offset:8704\n\t"
        "ds_read_b64_tr_b16 v[116:117], v23 offset:8200\n\t"
        "ds_read_b64_tr_b16 v[118:119], v23 offset:8712\n\t"
        "ds_read_b64_tr_b16 v[120:121], v23 offset:12288\n\t"
        "ds_read_b64_tr_b16 v[122:123], v23 offset:12800\n\t"
        "s_branch .L7_sw\n"
        ".L7_sp:\n\t"
        "buffer_load_dword v12, v13, s[20:23], s43 offen sc1\n"
        ".L7_sw:\n\t"
        "s_waitcnt vmcnt(0)\n\t"
        "v_xor_b32 v12, v12, v22\n\t"
        "v_and_b32 v0, s48, v12\n\t"
        "v_cmp_ne_u32 vcc, 0, v0\n\t"
        "s_cbranch_vccz .L7_sq\n\t"
        "s_sleep 4\n\t"
        "s_sub_i32 s83, s83, 1\n\t"
        "s_cmp_gt_i32 s83, 0\n\t"
        "s_cbranch_scc1 .L7_sp\n"
        ".L7_sq:\n\t"
        "v_add_f32 v21, v21, v12\n\t"
        "v_exp_f32 v21, v21\n\t"
        "v_mov_b64 v[12:13], 0\n\t"
        "v_mov_b64 v[14:15], 0\n\t"
        "v_mov_b32 v22, v21\n\t"
        "v_mov_b32 v0, v21\n\t"
        "s_nop 1\n\t"
        "v_permlane16_swap_b32 v22, v0\n\t"
        "v_add_f32 v22, v22, v0\n\t"
        "v_mov_b32 v0, v22\n\t"
        "s_nop 1\n\t"
        "v_permlane32_swap_b32 v22, v0\n\t"
        "v_add_f32 v22, v22, v0\n\t"
        "v_add_f32 v22, 0x3089705f, v22\n\t"
        "v_rcp_f32 v22, v22\n\t"
        "s_nop 0\n\t"
        "v_mul_f32 v21, v21, v22\n\t"
        "v_cvt_pk_bf16_f32 v21, v21, v21\n\t"
        "v_lshlrev_b32 v22, 4, v1\n\t"
        "v_add_u32 v22, 157952, v22\n"
        ".L7_w15:\n\t"
        "ds_read_b32 v0, v2 offset:36\n\t"
        "s_waitcnt lgkmcnt(0)\n\t"
        "v_cmp_gt_u16 vcc, 4, v0\n\t"
        "s_cbranch_vccz .L7_d16\n\t"
        "s_sleep 1\n\t"
        "s_sub_i32 s83, s83, 1\n\t"
        "s_cmp_gt_i32 s83, 0\n\t"
        "s_cbranch_scc1 .L7_w15\n"
        ".L7_d16:\n\t"
        "v_cmp_lt_u32 vcc, 0xfffff, v0\n\t"
        "s_cbranch_vccnz .L7_na\n\t"
        "v_and_b32_dpp v8, v21, v4 row_newbcast:0 row_mask:0xf bank_mask:0x3\n\t"
        "v_and_b32_dpp v9, v21, v5 row_newbcast:0 row_mask:0xf bank_mask:0x3\n\t"
        "v_and_b32_dpp v10, v21, v6 row_newbcast:0 row_mask:0xf bank_mask:0x3\n\t"
        "v_and_b32_dpp v11, v21, v7 row_newbcast:0 row_mask:0xf bank_mask:0x3\n\t"
        "v_and_b32_dpp v12, v21, v4 row_newbcast:8 row_mask:0xf bank_mask:0xc\n\t"
        "v_and_b32_dpp v13, v21, v5 row_newbcast:8 row_mask:0xf bank_mask:0xc\n\t"
        "v_and_b32_dpp v14, v21, v6 row_newbcast:8 row_mask:0xf bank_mask:0xc\n\t"
        "v_and_b32_dpp v15, v21, v7 row_newbcast:8 row_mask:0xf bank_mask:0xc\n\t"
        "v_mfma_f32_16x16x32_bf16 v[124:127], v[8:11], v[24:27], 0\n\t"
        "s_nop 0\n\t"
        "v_mfma_f32_16x16x32_bf16 v[124:127], v[12:15], v[28:31], v[124:127]\n\t"
        "v_and_b32_dpp v8, v21, v4 row_newbcast:4 row_mask:0xf bank_mask:0x3\n\t"
        "v_and_b32_dpp v9, v21, v5 row_newbcast:4 row_mask:0xf bank_mask:0x3\n\t"
        "v_and_b32_dpp v10, v21, v6 row_newbcast:4 row_mask:0xf bank_mask:0x3\n\t"
        "v_and_b32_dpp v11, v21, v7 row_newbcast:4 row_mask:0xf bank_mask:0x3\n\t"
        "v_and_b32_dpp v12, v21, v4 row_newbcast:12 row_mask:0xf bank_mask:0xc\n\t"
        "v_and_b32_dpp v13, v21, v5 row_newbcast:12 row_mask:0xf bank_mask:0xc\n\t"
        "v_and_b32_dpp v14, v21, v6 row_newbcast:12 row_mask:0xf bank_mask:0xc\n\t"
        "v_and_b32_dpp v15, v21, v7 row_newbcast:12 row_mask:0xf bank_mask:0xc\n\t"
        "v_mfma_f32_16x16x32_bf16 v[24:27], v[8:11], v[32:35], 0\n\t"
        "s_nop 0\n\t"
        "v_mfma_f32_16x16x32_bf16 v[24:27], v[12:15], v[36:39], v[24:27]\n\t"
        "v_cvt_pk_bf16_f32 v124, v124, v125\n\t"
        "v_cvt_pk_bf16_f32 v125, v126, v127\n\t"
        "ds_write_b64 v16, v[124:125] offset:43008\n\t"
        "v_and_b32_dpp v8, v21, v4 row_newbcast:1 row_mask:0xf bank_mask:0x3\n\t"
        "v_and_b32_dpp v9, v21, v5 row_newbcast:1 row_mask:0xf bank_mask:0x3\n\t"
        "v_and_b32_dpp v10, v21, v6 row_newbcast:1 row_mask:0xf bank_mask:0x3\n\t"
        "v_and_b32_dpp v11, v21, v7 row_newbcast:1 row_mask:0xf bank_mask:0x3\n\t"
        "v_and_b32_dpp v12, v21, v4 row_newbcast:9 row_mask:0xf bank_mask:0xc\n\t"
        "v_and_b32_dpp v13, v21, v5 row_newbcast:9 row_mask:0xf bank_mask:0xc\n\t"
        "v_and_b32_dpp v14, v21, v6 row_newbcast:9 row_mask:0xf bank_mask:0xc\n\t"
        "v_and_b32_dpp v15, v21, v7 row_newbcast:9 row_mask:0xf bank_mask:0xc\n\t"
        "v_mfma_f32_16x16x32_bf16 v[32:35], v[8:11], v[40:43], 0\n\t"
        "s_nop 0\n\t"
        "v_mfma_f32_16x16x32_bf16 v[32:35], v[12:15], v[44:47], v[32:35]\n\t"
        "v_cvt_pk_bf16_f32 v24, v24, v25\n\t"
        "v_cvt_pk_bf16_f32 v25, v26, v27\n\t"
        "ds_write_b64 v17, v[24:25] offset:43008\n\t"
        "v_and_b32_dpp v8, v21, v4 row_newbcast:5 row_mask:0xf bank_mask:0x3\n\t"
        "v_and_b32_dpp v9, v21, v5 row_newbcast:5 row_mask:0xf bank_mask:0x3\n\t"
        "v_and_b32_dpp v10, v21, v6 row_newbcast:5 row_mask:0xf bank_mask:0x3\n\t"
        "v_and_b32_dpp v11, v21, v7 row_newbcast:5 row_mask:0xf bank_mask:0x3\n\t"
        "v_and_b32_dpp v12, v21, v4 row_newbcast:13 row_mask:0xf bank_mask:0xc\n\t"
        "v_and_b32_dpp v13, v21, v5 row_newbcast:13 row_mask:0xf bank_mask:0xc\n\t"
        "v_and_b32_dpp v14, v21, v6 row_newbcast:13 row_mask:0xf bank_mask:0xc\n\t"
        "v_and_b32_dpp v15, v21, v7 row_newbcast:13 row_mask:0xf bank_mask:0xc\n\t"
        "v_mfma_f32_16x16x32_bf16 v[40:43], v[8:11], v[48:51], 0\n\t"
        "s_nop 0\n\t"
        "v_mfma_f32_16x16x32_bf16 v[40:43], v[12:15], v[52:55], v[40:43]\n\t"
        "v_cvt_pk_bf16_f32 v32, v32, v33\n\t"
        "v_cvt_pk_bf16_f32 v33, v34, v35\n\t"
        "ds_write_b64 v18, v[32:33] offset:43008\n\t"
        "v_and_b32_dpp v8, v21, v4 row_newbcast:2 row_mask:0xf bank_mask:0x3\n\t"
        "v_and_b32_dpp v9, v21, v5 row_newbcast:2 row_mask:0xf bank_mask:0x3\n\t"
        "v_and_b32_dpp v10, v21, v6 row_newbcast:2 row_mask:0xf bank_mask:0x3\n\t"
        "v_and_b32_dpp v11, v21, v7 row_newbcast:2 row_mask:0xf bank_mask:0x3\n\t"
        "v_and_b32_dpp v12, v21, v4 row_newbcast:10 row_mask:0xf bank_mask:0xc\n\t"
        "v_and_b32_dpp v13, v21, v5 row_newbcast:10 row_mask:0xf bank_mask:0xc\n\t"
        "v_and_b32_dpp v14, v21, v6 row_newbcast:10 row_mask:0xf bank_mask:0xc\n\t"
        "v_and_b32_dpp v15, v21, v7 row_newbcast:10 row_mask:0xf bank_mask:0xc\n\t"
        "v_mfma_f32_16x16x32_bf16 v[48:51], v[8:11], v[56:59], 0\n\t"
        "s_nop 0\n\t"
        "v_mfma_f32_16x16x32_bf16 v[48:51], v[12:15], v[60:63], v[48:51]\n\t"
        "v_cvt_pk_bf16_f32 v40, v40, v41\n\t"
        "v_cvt_pk_bf16_f32 v41, v42, v43\n\t"
        "ds_write_b64 v19, v[40:41] offset:43008\n\t"
        "s_cmp_lt_u32 s69, 4\n\t"
        "s_cselect_b32 s78, 16, 1\n\t"
        "v_mov_b32 v0, s78\n\t"
        "s_nop 1\n\t"
        "v_cvt_pk_bf16_f32 v48, v48, v49\n\t"
        "v_cvt_pk_bf16_f32 v49, v50, v51\n\t"
        "ds_write_b64 v20, v[48:49] offset:43008\n\t"
        "s_mov_b64 exec, 1\n\t"
        "ds_add_u32 v2, v0 offset:40\n\t"
        "s_mov_b64 exec, -1\n"
        ".L7_na:\n\t"
        "ds_read_b64_tr_b16 v[124:125], v23 offset:12296\n\t"
        "ds_read_b64_tr_b16 v[126:127], v23 offset:12808\n\t"
        "ds_read_b128 v[4:7], v22\n\t"
        "s_lshl3_add_u32 s42, %3, s81\n\t"
        "s_mul_i32 s42, s42, 0xc00\n\t"
        "s_add_u32 s42, s42, 3145728\n\t"
        "s_xor_b32 s78, %3, 1\n\t"
        "s_lshl3_add_u32 s43, s78, s81\n\t"
        "s_mul_i32 s43, s43, 0xc00\n\t"
        "s_add_u32 s43, s43, 3145728\n\t"
        "s_lshl_b32 s50, s81, 6\n\t"
        "s_lshl_b32 s51, s69, 10\n\t"
        "s_add_u32 s51, s51, 149760\n\t"
        "s_mov_b32 s49, 0x10001\n\t"
        "s_mov_b32 s48, 0x07060302\n\t"
        "s_mov_b32 s55, 157952\n"
        ".L7_w17:\n\t"
        "ds_read_b32 v0, v2 offset:40\n\t"
        "s_waitcnt lgkmcnt(0)\n\t"
        "v_cmp_gt_u16 vcc, 64, v0\n\t"
        "s_cbranch_vccz .L7_d18\n\t"
        "s_setprio 0\n\t"
        "s_sleep 1\n\t"
        "s_sub_i32 s83, s83, 1\n\t"
        "s_cmp_gt_i32 s83, 0\n\t"
        "s_cbranch_scc1 .L7_w17\n"
        ".L7_d18:\n\t"
        "s_setprio 2\n\t"
        "ds_read_b128 v[8:11], v4 offset:43008\n\t"
        "ds_read_b128 v[12:15], v5 offset:43008\n\t"
        "ds_read_b128 v[16:19], v6 offset:43008\n\t"
        "ds_read_b128 v[20:23], v7 offset:43008\n\t"
        "ds_read_b128 v[48:51], v4 offset:47104\n\t"
        "ds_read_b128 v[52:55], v5 offset:47104\n\t"
        "ds_read_b128 v[56:59], v6 offset:47104\n\t"
        "ds_read_b128 v[60:63], v7 offset:47104\n\t"
        "s_waitcnt lgkmcnt(7)\n\t"
        "v_mfma_f32_16x16x32_bf16 v[24:27], v[64:67], v[8:11], 0\n\t"
        "v_mfma_f32_16x16x32_bf16 v[28:31], v[68:71], v[8:11], 0\n\t"
        "s_waitcnt lgkmcnt(6)\n\t"
        "v_mfma_f32_16x16x32_bf16 v[24:27], v[72:75], v[12:15], v[24:27]\n\t"
        "v_mfma_f32_16x16x32_bf16 v[28:31], v[76:79], v[12:15], v[28:31]\n\t"
        "s_waitcnt lgkmcnt(5)\n\t"
        "v_mfma_f32_16x16x32_bf16 v[24:27], v[80:83], v[16:19], v[24:27]\n\t"
        "v_mfma_f32_16x16x32_bf16 v[28:31], v[84:87], v[16:19], v[28:31]\n\t"
        "s_waitcnt lgkmcnt(4)\n\t"
        "v_mfma_f32_16x16x32_bf16 v[24:27], v[88:91], v[20:23], v[24:27]\n\t"
        "v_mfma_f32_16x16x32_bf16 v[28:31], v[92:95], v[20:23], v[28:31]\n\t"
        "ds_read_b128 v[8:11], v4 offset:51200\n\t"
        "ds_read_b128 v[12:15], v5 offset:51200\n\t"
        "ds_read_b128 v[16:19], v6 offset:51200\n\t"
        "ds_read_b128 v[20:23], v7 offset:51200\n\t"
        "s_waitcnt lgkmcnt(7)\n\t"
        "v_mfma_f32_16x16x32_bf16 v[32:35], v[64:67], v[48:51], 0\n\t"
        "v_mfma_f32_16x16x32_bf16 v[36:39], v[68:71], v[48:51], 0\n\t"
        "s_waitcnt lgkmcnt(6)\n\t"
        "v_mfma_f32_16x16x32_bf16 v[32:35], v[72:75], v[52:55], v[32:35]\n\t"
        "v_mfma_f32_16x16x32_bf16 v[36:39], v[76:79], v[52:55], v[36:39]\n\t"
        "s_waitcnt lgkmcnt(5)\n\t"
        "v_mfma_f32_16x16x32_bf16 v[32:35], v[80:83], v[56:59], v[32:35]\n\t"
        "v_mfma_f32_16x16x32_bf16 v[36:39], v[84:87], v[56:59], v[36:39]\n\t"
        "s_waitcnt lgkmcnt(4)\n\t"
        "v_mfma_f32_16x16x32_bf16 v[32:35], v[88:91], v[60:63], v[32:35]\n\t"
        "v_mfma_f32_16x16x32_bf16 v[36:39], v[92:95], v[60:63], v[36:39]\n\t"
        "ds_read_b32 v0, v2 offset:52\n\t"
        "ds_read_b128 v[48:51], v4 offset:20480\n\t"
        "ds_read_b128 v[52:55], v5 offset:20480\n\t"
        "ds_read_b128 v[56:59], v6 offset:20480\n\t"
        "ds_read_b128 v[60:63], v7 offset:20480\n\t"
        "s_waitcnt lgkmcnt(8)\n\t"
        "v_mfma_f32_16x16x32_bf16 v[40:43], v[64:67], v[8:11], 0\n\t"
        "v_mfma_f32_16x16x32_bf16 v[44:47], v[68:71], v[8:11], 0\n\t"
        "s_waitcnt lgkmcnt(7)\n\t"
        "v_mfma_f32_16x16x32_bf16 v[40:43], v[72:75], v[12:15], v[40:43]\n\t"
        "v_mfma_f32_16x16x32_bf16 v[44:47], v[76:79], v[12:15], v[44:47]\n\t"
        "s_waitcnt lgkmcnt(6)\n\t"
        "v_mfma_f32_16x16x32_bf16 v[40:43], v[80:83], v[16:19], v[40:43]\n\t"
        "v_mfma_f32_16x16x32_bf16 v[44:47], v[84:87], v[16:19], v[44:47]\n\t"
        "s_waitcnt lgkmcnt(5)\n\t"
        "v_mfma_f32_16x16x32_bf16 v[40:43], v[88:91], v[20:23], v[40:43]\n\t"
        "v_mfma_f32_16x16x32_bf16 v[44:47], v[92:95], v[20:23], v[44:47]\n\t"
        "ds_read_b128 v[8:11], v4 offset:24576\n\t"
        "ds_read_b128 v[12:15], v5 offset:24576\n\t"
        "ds_read_b128 v[16:19], v6 offset:24576\n\t"
        "ds_read_b128 v[20:23], v7 offset:24576\n\t"
        "s_waitcnt lgkmcnt(8)\n\t"
        "v_cmp_gt_u16 vcc, 6, v0\n\t"
        "s_cbranch_vccz .L7_t1\n"
        ".L7_w19:\n\t"
        "ds_read_b32 v0, v2 offset:52\n\t"
        "s_waitcnt lgkmcnt(0)\n\t"
        "v_cmp_gt_u16 vcc, 6, v0\n\t"
        "s_cbranch_vccz .L7_d20\n\t"
        "s_setprio 0\n\t"
        "s_sleep 2\n\t"
        "s_sub_i32 s83, s83, 1\n\t"
        "s_cmp_gt_i32 s83, 0\n\t"
        "s_cbranch_scc1 .L7_w19\n"
        ".L7_d20:\n\t"
        "s_setprio 2\n\t"
        "ds_read_b128 v[48:51], v4 offset:20480\n\t"
        "ds_read_b128 v[52:55], v5 offset:20480\n\t"
        "ds_read_b128 v[56:59], v6 offset:20480\n\t"
        "ds_read_b128 v[60:63], v7 offset:20480\n\t"
        "ds_read_b128 v[8:11], v4 offset:24576\n\t"
        "ds_read_b128 v[12:15], v5 offset:24576\n\t"
        "ds_read_b128 v[16:19], v6 offset:24576\n\t"
        "ds_read_b128 v[20:23], v7 offset:24576\n"
        ".L7_t1:\n\t"
        "v_lshl_add_u32 v3, v1, 4, s55\n\t"
        "s_waitcnt lgkmcnt(7)\n\t"
        "v_mfma_f32_16x16x32_bf16 v[24:27], v[96:99], v[48:51], v[24:27]\n\t"
        "v_mfma_f32_16x16x32_bf16 v[28:31], v[100:103], v[48:51], v[28:31]\n\t"
        "s_waitcnt lgkmcnt(6)\n\t"
        "v_mfma_f32_16x16x32_bf16 v[24:27], v[104:107], v[52:55], v[24:27]\n\t"
        "v_mfma_f32_16x16x32_bf16 v[28:31], v[108:111], v[52:55], v[28:31]\n\t"
        "s_waitcnt lgkmcnt(5)\n\t"
        "v_mfma_f32_16x16x32_bf16 v[24:27], v[112:115], v[56:59], v[24:27]\n\t"
        "v_mfma_f32_16x16x32_bf16 v[28:31], v[116:119], v[56:59], v[28:31]\n\t"
        "s_waitcnt lgkmcnt(4)\n\t"
        "v_mfma_f32_16x16x32_bf16 v[24:27], v[120:123], v[60:63], v[24:27]\n\t"
        "v_mfma_f32_16x16x32_bf16 v[28:31], v[124:127], v[60:63], v[28:31]\n\t"
        "ds_read_b128 v[60:63], v3 offset:1024\n\t"
        "s_waitcnt lgkmcnt(4)\n\t"
        "v_mfma_f32_16x16x32_bf16 v[32:35], v[96:99], v[8:11], v[32:35]\n\t"
        "v_mfma_f32_16x16x32_bf16 v[36:39], v[100:103], v[8:11], v[36:39]\n\t"
        "s_waitcnt lgkmcnt(3)\n\t"
        "v_mfma_f32_16x16x32_bf16 v[32:35], v[104:107], v[12:15], v[32:35]\n\t"
        "v_mfma_f32_16x16x32_bf16 v[36:39], v[108:111], v[12:15], v[36:39]\n\t"
        "s_waitcnt lgkmcnt(2)\n\t"
        "v_mfma_f32_16x16x32_bf16 v[32:35], v[112:115], v[16:19], v[32:35]\n\t"
        "v_mfma_f32_16x16x32_bf16 v[36:39], v[116:119], v[16:19], v[36:39]\n\t"
        "s_waitcnt lgkmcnt(1)\n\t"
        "v_mfma_f32_16x16x32_bf16 v[32:35], v[120:123], v[20:23], v[32:35]\n\t"
        "v_mfma_f32_16x16x32_bf16 v[36:39], v[124:127], v[20:23], v[36:39]\n\t"
        "ds_read_b32 v0, v2 offset:56\n\t"
        "ds_read_b128 v[8:11], v4 offset:28672\n\t"
        "ds_read_b128 v[12:15], v5 offset:28672\n\t"
        "ds_read_b128 v[16:19], v6 offset:28672\n\t"
        "ds_read_b128 v[20:23], v7 offset:28672\n\t"
        "s_waitcnt lgkmcnt(5)\n\t"
        "v_add_u32 v24, 0x10000, v24\n\t"
        "v_add_u32 v25, 0x10000, v25\n\t"
        "v_add_u32 v26, 0x10000, v26\n\t"
        "v_add_u32 v27, 0x10000, v27\n\t"
        "v_perm_b32 v48, v25, v24, s48\n\t"
        "v_perm_b32 v49, v27, v26, s48\n\t"
        "v_cvt_pk_bf16_f32 v50, v28, v29\n\t"
        "v_cvt_pk_bf16_f32 v51, v30, v31\n\t"
        "v_bfi_b32 v48, s49, v60, v48\n\t"
        "v_bfi_b32 v49, s49, v61, v49\n\t"
        "v_add_u32 v32, 0x10000, v32\n\t"
        "v_add_u32 v33, 0x10000, v33\n\t"
        "v_add_u32 v34, 0x10000, v34\n\t"
        "v_add_u32 v35, 0x10000, v35\n\t"
        "v_perm_b32 v52, v33, v32, s48\n\t"
        "v_perm_b32 v53, v35, v34, s48\n\t"
        "v_cvt_pk_bf16_f32 v54, v36, v37\n\t"
        "v_cvt_pk_bf16_f32 v55, v38, v39\n\t"
        "v_bfi_b32 v52, s49, v60, v52\n\t"
        "v_bfi_b32 v53, s49, v61, v53\n\t"
        "s_mov_b32 exec_lo, 0xff00ff00\n\t"
        "s_mov_b32 exec_hi, 0xff00ff00\n\t"
        "buffer_store_dwordx4 v[48:51], v62, s[20:23], s42 offen offset:0 sc1\n\t"
        "s_mov_b64 exec, -1\n\t"
        "buffer_store_dwordx4 v[52:55], v62, s[20:23], s42 offen offset:1024 sc1\n\t"
        "s_waitcnt lgkmcnt(4)\n\t"
        "v_cmp_gt_u16 vcc, 4, v0\n\t"
        "s_cbranch_vccz .L7_t2\n\t"
        "v_cmp_gt_u16 vcc, 3, v0\n\t"
        "s_cbranch_vccz .L7_t3\n\t"
        "s_waitcnt lgkmcnt(0)\n"
        ".L7_w21:\n\t"
        "ds_read_b32 v0, v2 offset:40\n\t"
        "ds_read_b128 v[8:11], v4 offset:55296\n\t"
        "ds_read_b128 v[12:15], v5 offset:55296\n\t"
        "ds_read_b128 v[16:19], v6 offset:55296\n\t"
        "ds_read_b128 v[20:23], v7 offset:55296\n\t"
        "s_waitcnt lgkmcnt(4)\n\t"
        "v_cmp_gt_u16 vcc, 68, v0\n\t"
        "s_cbranch_vccz .L7_d22\n\t"
        "s_waitcnt lgkmcnt(0)\n\t"
        "s_setprio 0\n\t"
        "s_sleep 1\n\t"
        "s_sub_i32 s83, s83, 1\n\t"
        "s_cmp_gt_i32 s83, 0\n\t"
        "s_cbranch_scc1 .L7_w21\n"
        ".L7_d22:\n\t"
        "s_setprio 2\n\t"
        "s_waitcnt lgkmcnt(3)\n\t"
        "v_mfma_f32_16x16x32_bf16 v[24:27], v[64:67], v[8:11], 0\n\t"
        "v_mfma_f32_16x16x32_bf16 v[28:31], v[68:71], v[8:11], 0\n\t"
        "s_waitcnt lgkmcnt(2)\n\t"
        "v_mfma_f32_16x16x32_bf16 v[24:27], v[72:75], v[12:15], v[24:27]\n\t"
        "v_mfma_f32_16x16x32_bf16 v[28:31], v[76:79], v[12:15], v[28:31]\n\t"
        "s_waitcnt lgkmcnt(1)\n\t"
        "v_mfma_f32_16x16x32_bf16 v[24:27], v[80:83], v[16:19], v[24:27]\n\t"
        "v_mfma_f32_16x16x32_bf16 v[28:31], v[84:87], v[16:19], v[28:31]\n\t"
        "s_waitcnt lgkmcnt(0)\n\t"
        "v_mfma_f32_16x16x32_bf16 v[24:27], v[88:91], v[20:23], v[24:27]\n\t"
        "v_mfma_f32_16x16x32_bf16 v[28:31], v[92:95], v[20:23], v[28:31]\n\t"
        "ds_read_b128 v[8:11], v4 offset:59392\n\t"
        "ds_read_b128 v[12:15], v5 offset:59392\n\t"
        "ds_read_b128 v[16:19], v6 offset:59392\n\t"
        "ds_read_b128 v[20:23], v7 offset:59392\n\t"
        "s_waitcnt lgkmcnt(3)\n\t"
        "v_mfma_f32_16x16x32_bf16 v[32:35], v[64:67], v[8:11], 0\n\t"
        "v_mfma_f32_16x16x32_bf16 v[36:39], v[68:71], v[8:11], 0\n\t"
        "s_waitcnt lgkmcnt(2)\n\t"
        "v_mfma_f32_16x16x32_bf16 v[32:35], v[72:75], v[12:15], v[32:35]\n\t"
        "v_mfma_f32_16x16x32_bf16 v[36:39], v[76:79], v[12:15], v[36:39]\n\t"
        "s_waitcnt lgkmcnt(1)\n\t"
        "v_mfma_f32_16x16x32_bf16 v[32:35], v[80:83], v[16:19], v[32:35]\n\t"
        "v_mfma_f32_16x16x32_bf16 v[36:39], v[84:87], v[16:19], v[36:39]\n\t"
        "s_waitcnt lgkmcnt(0)\n\t"
        "v_mfma_f32_16x16x32_bf16 v[32:35], v[88:91], v[20:23], v[32:35]\n\t"
        "v_mfma_f32_16x16x32_bf16 v[36:39], v[92:95], v[20:23], v[36:39]\n"
        ".L7_w23:\n\t"
        "ds_read_b32 v0, v2 offset:56\n\t"
        "s_waitcnt lgkmcnt(0)\n\t"
        "v_cmp_gt_u16 vcc, 3, v0\n\t"
        "s_cbranch_vccz .L7_d24\n\t"
        "s_setprio 0\n\t"
        "s_sleep 2\n\t"
        "s_sub_i32 s83, s83, 1\n\t"
        "s_cmp_gt_i32 s83, 0\n\t"
        "s_cbranch_scc1 .L7_w23\n"
        ".L7_d24:\n\t"
        "s_setprio 2\n"
        ".L7_w25:\n\t"
        "ds_read_b32 v0, v2 offset:56\n\t"
        "ds_read_b128 v[8:11], v4 offset:28672\n\t"
        "ds_read_b128 v[12:15], v5 offset:28672\n\t"
        "ds_read_b128 v[16:19], v6 offset:28672\n\t"
        "ds_read_b128 v[20:23], v7 offset:28672\n\t"
        "s_waitcnt lgkmcnt(4)\n\t"
        "v_cmp_gt_u16 vcc, 4, v0\n\t"
        "s_cbranch_vccz .L7_d26\n\t"
        "s_waitcnt lgkmcnt(0)\n\t"
        "s_setprio 0\n\t"
        "s_sleep 1\n\t"
        "s_sub_i32 s83, s83, 1\n\t"
        "s_cmp_gt_i32 s83, 0\n\t"
        "s_cbranch_scc1 .L7_w25\n"
        ".L7_d26:\n\t"
        "s_setprio 2\n\t"
        "s_waitcnt lgkmcnt(3)\n\t"
        "v_mfma_f32_16x16x32_bf16 v[40:43], v[96:99], v[8:11], v[40:43]\n\t"
        "v_mfma_f32_16x16x32_bf16 v[44:47], v[100:103], v[8:11], v[44:47]\n\t"
        "s_waitcnt lgkmcnt(2)\n\t"
        "v_mfma_f32_16x16x32_bf16 v[40:43], v[104:107], v[12:15], v[40:43]\n\t"
        "v_mfma_f32_16x16x32_bf16 v[44:47], v[108:111], v[12:15], v[44:47]\n\t"
        "s_waitcnt lgkmcnt(1)\n\t"
        "v_mfma_f32_16x16x32_bf16 v[40:43], v[112:115], v[16:19], v[40:43]\n\t"
        "v_mfma_f32_16x16x32_bf16 v[44:47], v[116:119], v[16:19], v[44:47]\n\t"
        "s_waitcnt lgkmcnt(0)\n\t"
        "v_mfma_f32_16x16x32_bf16 v[40:43], v[120:123], v[20:23], v[40:43]\n\t"
        "v_mfma_f32_16x16x32_bf16 v[44:47], v[124:127], v[20:23], v[44:47]\n\t"
        "s_nop 6\n\t"
        "v_add_u32 v40, 0x10000, v40\n\t"
        "v_add_u32 v41, 0x10000, v41\n\t"
        "v_add_u32 v42, 0x10000, v42\n\t"
        "v_add_u32 v43, 0x10000, v43\n\t"
        "v_perm_b32 v56, v41, v40, s48\n\t"
        "v_perm_b32 v57, v43, v42, s48\n\t"
        "v_cvt_pk_bf16_f32 v58, v44, v45\n\t"
        "v_cvt_pk_bf16_f32 v59, v46, v47\n\t"
        "v_bfi_b32 v56, s49, v60, v56\n\t"
        "v_bfi_b32 v57, s49, v61, v57\n\t"
        "buffer_store_dwordx4 v[56:59], v62, s[20:23], s42 offen offset:2048 sc1\n\t"
        "s_memrealtime s[52:53]\n\t"
        "ds_read_b128 v[8:11], v4 offset:63488\n\t"
        "ds_read_b128 v[12:15], v5 offset:63488\n\t"
        "ds_read_b128 v[16:19], v6 offset:63488\n\t"
        "ds_read_b128 v[20:23], v7 offset:63488\n\t"
        "s_waitcnt lgkmcnt(0)\n\t"
        "s_waitcnt lgkmcnt(3)\n\t"
        "v_mfma_f32_16x16x32_bf16 v[40:43], v[64:67], v[8:11], 0\n\t"
        "v_mfma_f32_16x16x32_bf16 v[44:47], v[68:71], v[8:11], 0\n\t"
        "s_waitcnt lgkmcnt(2)\n\t"
        "v_mfma_f32_16x16x32_bf16 v[40:43], v[72:75], v[12:15], v[40:43]\n\t"
        "v_mfma_f32_16x16x32_bf16 v[44:47], v[76:79], v[12:15], v[44:47]\n\t"
        "s_waitcnt lgkmcnt(1)\n\t"
        "v_mfma_f32_16x16x32_bf16 v[40:43], v[80:83], v[16:19], v[40:43]\n\t"
        "v_mfma_f32_16x16x32_bf16 v[44:47], v[84:87], v[16:19], v[44:47]\n\t"
        "s_waitcnt lgkmcnt(0)\n\t"
        "v_mfma_f32_16x16x32_bf16 v[40:43], v[88:91], v[20:23], v[40:43]\n\t"
        "v_mfma_f32_16x16x32_bf16 v[44:47], v[92:95], v[20:23], v[44:47]\n\t"
        "s_branch .L7_t4\n"
        ".L7_t3:\n"
        ".L7_w27:\n\t"
        "ds_read_b32 v0, v2 offset:56\n\t"
        "s_waitcnt lgkmcnt(0)\n\t"
        "v_cmp_gt_u16 vcc, 3, v0\n\t"
        "s_cbranch_vccz .L7_d28\n\t"
        "s_setprio 0\n\t"
        "s_sleep 2\n\t"
        "s_sub_i32 s83, s83, 1\n\t"
        "s_cmp_gt_i32 s83, 0\n\t"
        "s_cbranch_scc1 .L7_w27\n"
        ".L7_d28:\n\t"
        "s_setprio 2\n"
        ".L7_w29:\n\t"
        "ds_read_b32 v0, v2 offset:56\n\t"
        "ds_read_b128 v[8:11], v4 offset:28672\n\t"
        "ds_read_b128 v[12:15], v5 offset:28672\n\t"
        "ds_read_b128 v[16:19], v6 offset:28672\n\t"
        "ds_read_b128 v[20:23], v7 offset:28672\n\t"
        "s_waitcnt lgkmcnt(4)\n\t"
        "v_cmp_gt_u16 vcc, 4, v0\n\t"
        "s_cbranch_vccz .L7_d30\n\t"
        "s_waitcnt lgkmcnt(0)\n\t"
        "s_setprio 0\n\t"
        "s_sleep 1\n\t"
        "s_sub_i32 s83, s83, 1\n\t"
        "s_cmp_gt_i32 s83, 0\n\t"
        "s_cbranch_scc1 .L7_w29\n"
        ".L7_d30:\n\t"
        "s_setprio 2\n"
        ".L7_t2:\n\t"
        "s_waitcnt lgkmcnt(3)\n\t"
        "v_mfma_f32_16x16x32_bf16 v[40:43], v[96:99], v[8:11], v[40:43]\n\t"
        "v_mfma_f32_16x16x32_bf16 v[44:47], v[100:103], v[8:11], v[44:47]\n\t"
        "s_waitcnt lgkmcnt(2)\n\t"
        "v_mfma_f32_16x16x32_bf16 v[40:43], v[104:107], v[12:15], v[40:43]\n\t"
        "v_mfma_f32_16x16x32_bf16 v[44:47], v[108:111], v[12:15], v[44:47]\n\t"
        "s_waitcnt lgkmcnt(1)\n\t"
        "v_mfma_f32_16x16x32_bf16 v[40:43], v[112:115], v[16:19], v[40:43]\n\t"
        "v_mfma_f32_16x16x32_bf16 v[44:47], v[116:119], v[16:19], v[44:47]\n\t"
        "s_waitcnt lgkmcnt(0)\n\t"
        "v_mfma_f32_16x16x32_bf16 v[40:43], v[120:123], v[20:23], v[40:43]\n\t"
        "v_mfma_f32_16x16x32_bf16 v[44:47], v[124:127], v[20:23], v[44:47]\n\t"
        "s_nop 6\n\t"
        "v_add_u32 v40, 0x10000, v40\n\t"
        "v_add_u32 v41, 0x10000, v41\n\t"
        "v_add_u32 v42, 0x10000, v42\n\t"
        "v_add_u32 v43, 0x10000, v43\n\t"
        "v_perm_b32 v56, v41, v40, s48\n\t"
        "v_perm_b32 v57, v43, v42, s48\n\t"
        "v_cvt_pk_bf16_f32 v58, v44, v45\n\t"
        "v_cvt_pk_bf16_f32 v59, v46, v47\n\t"
        "v_bfi_b32 v56, s49, v60, v56\n\t"
        "v_bfi_b32 v57, s49, v61, v57\n\t"
        "buffer_store_dwordx4 v[56:59], v62, s[20:23], s42 offen offset:2048 sc1\n\t"
        "s_memrealtime s[52:53]\n\t"
        "s_waitcnt lgkmcnt(0)\n"
        ".L7_w31:\n\t"
        "ds_read_b32 v0, v2 offset:40\n\t"
        "ds_read_b128 v[8:11], v4 offset:55296\n\t"
        "ds_read_b128 v[12:15], v5 offset:55296\n\t"
        "ds_read_b128 v[16:19], v6 offset:55296\n\t"
        "ds_read_b128 v[20:23], v7 offset:55296\n\t"
        "s_waitcnt lgkmcnt(4)\n\t"
        "v_cmp_gt_u16 vcc, 68, v0\n\t"
        "s_cbranch_vccz .L7_d32\n\t"
        "s_waitcnt lgkmcnt(0)\n\t"
        "s_setprio 0\n\t"
        "s_sleep 1\n\t"
        "s_sub_i32 s83, s83, 1\n\t"
        "s_cmp_gt_i32 s83, 0\n\t"
        "s_cbranch_scc1 .L7_w31\n"
        ".L7_d32:\n\t"
        "s_setprio 2\n\t"
        "s_waitcnt lgkmcnt(3)\n\t"
        "v_mfma_f32_16x16x32_bf16 v[24:27], v[64:67], v[8:11], 0\n\t"
        "v_mfma_f32_16x16x32_bf16 v[28:31], v[68:71], v[8:11], 0\n\t"
        "s_waitcnt lgkmcnt(2)\n\t"
        "v_mfma_f32_16x16x32_bf16 v[24:27], v[72:75], v[12:15], v[24:27]\n\t"
        "v_mfma_f32_16x16x32_bf16 v[28:31], v[76:79], v[12:15], v[28:31]\n\t"
        "s_waitcnt lgkmcnt(1)\n\t"
        "v_mfma_f32_16x16x32_bf16 v[24:27], v[80:83], v[16:19], v[24:27]\n\t"
        "v_mfma_f32_16x16x32_bf16 v[28:31], v[84:87], v[16:19], v[28:31]\n\t"
        "s_waitcnt lgkmcnt(0)\n\t"
        "v_mfma_f32_16x16x32_bf16 v[24:27], v[88:91], v[20:23], v[24:27]\n\t"
        "v_mfma_f32_16x16x32_bf16 v[28:31], v[92:95], v[20:23], v[28:31]\n\t"
        "ds_read_b128 v[8:11], v4 offset:59392\n\t"
        "ds_read_b128 v[12:15], v5 offset:59392\n\t"
        "ds_read_b128 v[16:19], v6 offset:59392\n\t"
        "ds_read_b128 v[20:23], v7 offset:59392\n\t"
        "s_waitcnt lgkmcnt(3)\n\t"
        "v_mfma_f32_16x16x32_bf16 v[32:35], v[64:67], v[8:11], 0\n\t"
        "v_mfma_f32_16x16x32_bf16 v[36:39], v[68:71], v[8:11], 0\n\t"
        "s_waitcnt lgkmcnt(2)\n\t"
        "v_mfma_f32_16x16x32_bf16 v[32:35], v[72:75], v[12:15], v[32:35]\n\t"
        "v_mfma_f32_16x16x32_bf16 v[36:39], v[76:79], v[12:15], v[36:39]\n\t"
        "s_waitcnt lgkmcnt(1)\n\t"
        "v_mfma_f32_16x16x32_bf16 v[32:35], v[80:83], v[16:19], v[32:35]\n\t"
        "v_mfma_f32_16x16x32_bf16 v[36:39], v[84:87], v[16:19], v[36:39]\n\t"
        "s_waitcnt lgkmcnt(0)\n\t"
        "v_mfma_f32_16x16x32_bf16 v[32:35], v[88:91], v[20:23], v[32:35]\n\t"
        "v_mfma_f32_16x16x32_bf16 v[36:39], v[92:95], v[20:23], v[36:39]\n\t"
        "ds_read_b128 v[8:11], v4 offset:63488\n\t"
        "ds_read_b128 v[12:15], v5 offset:63488\n\t"
        "ds_read_b128 v[16:19], v6 offset:63488\n\t"
        "ds_read_b128 v[20:23], v7 offset:63488\n\t"
        "s_waitcnt lgkmcnt(3)\n\t"
        "v_mfma_f32_16x16x32_bf16 v[40:43], v[64:67], v[8:11], 0\n\t"
        "v_mfma_f32_16x16x32_bf16 v[44:47], v[68:71], v[8:11], 0\n\t"
        "s_waitcnt lgkmcnt(2)\n\t"
        "v_mfma_f32_16x16x32_bf16 v[40:43], v[72:75], v[12:15], v[40:43]\n\t"
        "v_mfma_f32_16x16x32_bf16 v[44:47], v[76:79], v[12:15], v[44:47]\n\t"
        "s_waitcnt lgkmcnt(1)\n\t"
        "v_mfma_f32_16x16x32_bf16 v[40:43], v[80:83], v[16:19], v[40:43]\n\t"
        "v_mfma_f32_16x16x32_bf16 v[44:47], v[84:87], v[16:19], v[44:47]\n\t"
        "s_waitcnt lgkmcnt(0)\n\t"
        "v_mfma_f32_16x16x32_bf16 v[40:43], v[88:91], v[20:23], v[40:43]\n\t"
        "v_mfma_f32_16x16x32_bf16 v[44:47], v[92:95], v[20:23], v[44:47]\n"
        ".L7_t4:\n\t"
        "ds_read_b128 v[68:71], v3 offset:2048\n\t"
        "ds_read_b128 v[72:75], v3 offset:3072\n\t"
        "ds_read_b128 v[76:79], v3 offset:4096\n"
        ".L7_w33:\n\t"
        "ds_read_b32 v0, v2 offset:60\n\t"
        "s_waitcnt lgkmcnt(0)\n\t"
        "v_cmp_gt_u16 vcc, 2, v0\n\t"
        "s_cbranch_vccz .L7_d34\n\t"
        "s_setprio 0\n\t"
        "s_sleep 2\n\t"
        "s_sub_i32 s83, s83, 1\n\t"
        "s_cmp_gt_i32 s83, 0\n\t"
        "s_cbranch_scc1 .L7_w33\n"
        ".L7_d34:\n\t"
        "s_setprio 2\n\t"
        "ds_read_b128 v[8:11], v4 offset:32768\n\t"
        "ds_read_b128 v[12:15], v5 offset:32768\n\t"
        "ds_read_b128 v[16:19], v6 offset:32768\n\t"
        "ds_read_b128 v[20:23], v7 offset:32768\n\t"
        "v_add_u32 v71, s51, v71\n\t"
        "v_add_u32 v72, s51, v72\n\t"
        "s_waitcnt lgkmcnt(3)\n\t"
        "v_mfma_f32_16x16x32_bf16 v[24:27], v[96:99], v[8:11], v[24:27]\n\t"
        "v_mfma_f32_16x16x32_bf16 v[28:31], v[100:103], v[8:11], v[28:31]\n\t"
        "s_waitcnt lgkmcnt(2)\n\t"
        "v_mfma_f32_16x16x32_bf16 v[24:27], v[104:107], v[12:15], v[24:27]\n\t"
        "v_mfma_f32_16x16x32_bf16 v[28:31], v[108:111], v[12:15], v[28:31]\n\t"
        "s_waitcnt lgkmcnt(1)\n\t"
        "v_mfma_f32_16x16x32_bf16 v[24:27], v[112:115], v[16:19], v[24:27]\n\t"
        "v_mfma_f32_16x16x32_bf16 v[28:31], v[116:119], v[16:19], v[28:31]\n\t"
        "s_waitcnt lgkmcnt(0)\n\t"
        "v_mfma_f32_16x16x32_bf16 v[24:27], v[120:123], v[20:23], v[24:27]\n\t"
        "v_mfma_f32_16x16x32_bf16 v[28:31], v[124:127], v[20:23], v[28:31]\n\t"
        "s_setprio 0\n"
        ".L7_g35:\n\t"
        "s_memrealtime s[78:79]\n\t"
        "s_waitcnt lgkmcnt(0)\n\t"
        "s_sub_u32 s78, s78, s52\n\t"
        "s_cmp_ge_u32 s78, 120\n\t"
        "s_cbranch_scc1 .L7_h36\n\t"
        "s_sleep 1\n\t"
        "s_branch .L7_g35\n"
        ".L7_h36:\n\t"
        "s_setprio 2\n\t"
        "buffer_load_dwordx4 v[48:51], v63, s[20:23], s43 offen offset:0 sc1\n\t"
        "buffer_load_dwordx4 v[52:55], v62, s[20:23], s43 offen offset:1024 sc1\n\t"
        "buffer_load_dwordx4 v[56:59], v62, s[20:23], s43 offen offset:2048 sc1\n"
        ".L7_w37:\n\t"
        "ds_read_b32 v0, v2 offset:64\n\t"
        "s_waitcnt lgkmcnt(0)\n\t"
        "v_cmp_gt_u16 vcc, 4, v0\n\t"
        "s_cbranch_vccz .L7_d38\n\t"
        "s_setprio 0\n\t"
        "s_sleep 2\n\t"
        "s_sub_i32 s83, s83, 1\n\t"
        "s_cmp_gt_i32 s83, 0\n\t"
        "s_cbranch_scc1 .L7_w37\n"
        ".L7_d38:\n\t"
        "s_setprio 2\n\t"
        "ds_read_b128 v[8:11], v4 offset:36864\n\t"
        "ds_read_b128 v[12:15], v5 offset:36864\n\t"
        "ds_read_b128 v[16:19], v6 offset:36864\n\t"
        "ds_read_b128 v[20:23], v7 offset:36864\n\t"
        "s_waitcnt lgkmcnt(3)\n\t"
        "v_mfma_f32_16x16x32_bf16 v[32:35], v[96:99], v[8:11], v[32:35]\n\t"
        "v_mfma_f32_16x16x32_bf16 v[36:39], v[100:103], v[8:11], v[36:39]\n\t"
        "s_waitcnt lgkmcnt(2)\n\t"
        "v_mfma_f32_16x16x32_bf16 v[32:35], v[104:107], v[12:15], v[32:35]\n\t"
        "v_mfma_f32_16x16x32_bf16 v[36:39], v[108:111], v[12:15], v[36:39]\n\t"
        "s_waitcnt lgkmcnt(1)\n\t"
        "v_mfma_f32_16x16x32_bf16 v[32:35], v[112:115], v[16:19], v[32:35]\n\t"
        "v_mfma_f32_16x16x32_bf16 v[36:39], v[116:119], v[16:19], v[36:39]\n\t"
        "s_waitcnt lgkmcnt(0)\n\t"
        "v_mfma_f32_16x16x32_bf16 v[32:35], v[120:123], v[20:23], v[32:35]\n\t"
        "v_mfma_f32_16x16x32_bf16 v[36:39], v[124:127], v[20:23], v[36:39]\n\t"
        "s_waitcnt vmcnt(0)\n\t"
        "s_branch .L7_s40\n"
        ".L7_r39:\n\t"
        "buffer_load_dwordx4 v[48:51], v63, s[20:23], s43 offen offset:0 sc1\n\t"
        "buffer_load_dwordx4 v[52:55], v62, s[20:23], s43 offen offset:1024 sc1\n\t"
        "buffer_load_dwordx4 v[56:59], v62, s[20:23], s43 offen offset:2048 sc1\n\t"
        "s_waitcnt vmcnt(0)\n"
        ".L7_s40:\n\t"
        "v_xor_b32 v48, v48, v60\n\t"
        "v_xor_b32 v49, v49, v61\n\t"
        "v_xor_b32 v52, v52, v60\n\t"
        "v_xor_b32 v53, v53, v61\n\t"
        "v_xor_b32 v56, v56, v60\n\t"
        "v_xor_b32 v57, v57, v61\n\t"
        "v_or_b32 v0, v48, v49\n\t"
        "v_or3_b32 v0, v0, v52, v53\n\t"
        "v_or3_b32 v0, v0, v56, v57\n\t"
        "v_and_b32 v0, s49, v0\n\t"
        "v_cmp_ne_u32 vcc, 0, v0\n\t"
        "s_cbranch_vccz .L7_t41\n\t"
        "s_sleep 8\n\t"
        "s_sub_i32 s83, s83, 1\n\t"
        "s_cmp_gt_i32 s83, 0\n\t"
        "s_cbranch_scc1 .L7_r39\n"
        ".L7_t41:\n\t"
        "v_mfma_f32_16x16x32_bf16 v[24:27], v[76:79], v[48:51], v[24:27]\n\t"
        "v_mfma_f32_16x16x32_bf16 v[28:31], v[74:77], v[48:51], v[28:31]\n\t"
        "v_mfma_f32_16x16x32_bf16 v[32:35], v[76:79], v[52:55], v[32:35]\n\t"
        "v_mfma_f32_16x16x32_bf16 v[36:39], v[74:77], v[52:55], v[36:39]\n\t"
        "v_mfma_f32_16x16x32_bf16 v[40:43], v[76:79], v[56:59], v[40:43]\n\t"
        "v_mfma_f32_16x16x32_bf16 v[44:47], v[74:77], v[56:59], v[44:47]\n\t"
        "s_nop 2\n\t"
        "v_cvt_pk_bf16_f32 v48, v24, v25\n\t"
        "v_cvt_pk_bf16_f32 v49, v26, v27\n\t"
        "v_cvt_pk_bf16_f32 v50, v28, v29\n\t"
        "v_cvt_pk_bf16_f32 v51, v30, v31\n\t"
        "ds_write_b128 v71, v[48:51]\n\t"
        "ds_read_b128 v[48:51], v72\n\t"
        "v_cvt_pk_bf16_f32 v52, v32, v33\n\t"
        "v_cvt_pk_bf16_f32 v53, v34, v35\n\t"
        "v_cvt_pk_bf16_f32 v54, v36, v37\n\t"
        "v_cvt_pk_bf16_f32 v55, v38, v39\n\t"
        "ds_write_b128 v71, v[52:55]\n\t"
        "ds_read_b128 v[52:55], v72\n\t"
        "s_waitcnt lgkmcnt(0)\n\t"
        "buffer_store_dwordx4 v[48:51], v68, s[60:63], s50 offen sc1\n\t"
        "buffer_store_dwordx4 v[52:55], v69, s[60:63], s50 offen sc1\n"
        ".L7_w42:\n\t"
        "ds_read_b32 v0, v2 offset:68\n\t"
        "s_waitcnt lgkmcnt(0)\n\t"
        "v_cmp_gt_u16 vcc, 3, v0\n\t"
        "s_cbranch_vccz .L7_d43\n\t"
        "s_setprio 0\n\t"
        "s_sleep 1\n\t"
        "s_sub_i32 s83, s83, 1\n\t"
        "s_cmp_gt_i32 s83, 0\n\t"
        "s_cbranch_scc1 .L7_w42\n"
        ".L7_d43:\n\t"
        "s_setprio 2\n"
        ".L7_w44:\n\t"
        "ds_read_b32 v0, v2 offset:68\n\t"
        "ds_read_b128 v[8:11], v4 offset:40960\n\t"
        "ds_read_b128 v[12:15], v5 offset:40960\n\t"
        "ds_read_b128 v[16:19], v6 offset:40960\n\t"
        "ds_read_b128 v[20:23], v7 offset:40960\n\t"
        "s_waitcnt lgkmcnt(4)\n\t"
        "v_cmp_gt_u16 vcc, 4, v0\n\t"
        "s_cbranch_vccz .L7_d45\n\t"
        "s_waitcnt lgkmcnt(0)\n\t"
        "s_setprio 0\n\t"
        "s_sleep 0\n\t"
        "s_sub_i32 s83, s83, 1\n\t"
        "s_cmp_gt_i32 s83, 0\n\t"
        "s_cbranch_scc1 .L7_w44\n"
        ".L7_d45:\n\t"
        "s_setprio 2\n\t"
        "s_waitcnt lgkmcnt(3)\n\t"
        "v_mfma_f32_16x16x32_bf16 v[40:43], v[96:99], v[8:11], v[40:43]\n\t"
        "v_mfma_f32_16x16x32_bf16 v[44:47], v[100:103], v[8:11], v[44:47]\n\t"
        "s_waitcnt lgkmcnt(2)\n\t"
        "v_mfma_f32_16x16x32_bf16 v[40:43], v[104:107], v[12:15], v[40:43]\n\t"
        "v_mfma_f32_16x16x32_bf16 v[44:47], v[108:111], v[12:15], v[44:47]\n\t"
        "s_waitcnt lgkmcnt(1)\n\t"
        "v_mfma_f32_16x16x32_bf16 v[40:43], v[112:115], v[16:19], v[40:43]\n\t"
        "v_mfma_f32_16x16x32_bf16 v[44:47], v[116:119], v[16:19], v[44:47]\n\t"
        "s_waitcnt lgkmcnt(0)\n\t"
        "v_mfma_f32_16x16x32_bf16 v[40:43], v[120:123], v[20:23], v[40:43]\n\t"
        "v_mfma_f32_16x16x32_bf16 v[44:47], v[124:127], v[20:23], v[44:47]\n\t"
        "s_nop 6\n\t"
        "v_cvt_pk_bf16_f32 v56, v40, v41\n\t"
        "v_cvt_pk_bf16_f32 v57, v42, v43\n\t"
        "v_cvt_pk_bf16_f32 v58, v44, v45\n\t"
        "v_cvt_pk_bf16_f32 v59, v46, v47\n\t"
        "ds_write_b128 v71, v[56:59]\n\t"
        "ds_read_b128 v[56:59], v72\n\t"
        "s_waitcnt lgkmcnt(0)\n\t"
        "buffer_store_dwordx4 v[56:59], v70, s[60:63], s50 offen sc1\n\t"
        "s_or_b32 s78, %3, s69\n\t"
        "s_cbranch_scc1 .L7_ng\n\t"
        "v_mov_b32 v0, s68\n\t"
        "buffer_store_dword v0, off, s[20:23], 0 sc1\n"
        ".L7_ng:\n\t"
        "s_endpgm\n"
        ".L7_U2:\n\t"
        "s_barrier\n\t"
        "v_cmp_gt_u32 s[24:25], s46, v4\n\t"
        "v_cmp_gt_u32 s[26:27], s46, v5\n\t"
        "v_cmp_gt_u32 s[28:29], s46, v6\n\t"
        "v_cmp_gt_u32 s[30:31], s46, v7\n\t"
        "v_cmp_gt_u32 s[32:33], s46, v8\n\t"
        "v_mov_b64 v[4:5], 0\n\t"
        "v_mov_b64 v[6:7], 0\n\t"
        "s_mov_b32 exec_hi, 0xffff\n\t"
        "ds_write_b128 v12, v[4:7] offset:1024\n\t"
        "s_mov_b64 exec, -1\n\t"
        "v_add_u32 v15, 83968, v12\n\t"
        "v_mov_b32 v13, 0x1000000\n\t"
        "v_mov_b32 v14, 33\n\t"
        "s_lshl_b32 s54, s65, 14\n\t"
        "s_sub_u32 s54, 0, s54\n\t"
        "s_lshl_b32 s48, s71, 14\n\t"
        "s_add_u32 s48, s48, 1048576\n\t"
        "s_add_u32 s49, s48, 4096\n\t"
        "s_add_u32 s50, s48, 8192\n\t"
        "s_add_u32 s51, s48, 12288\n\t"
        "s_movk_i32 s55, 1023\n\t"
        "s_mov_b32 s52, 83840\n\t"
        "s_waitcnt lgkmcnt(0)\n\t"
        "s_add_u32 s68, s68, 1\n\t"
        "s_lshl_b32 s78, s68, 24\n\t"
        "s_or_b32 s78, s78, 0xffffff\n\t"
        "v_mov_b32 v8, s78\n\t"
        "s_xor_b32 s78, s78, 0x5a000000\n\t"
        "v_mov_b32 v9, s78\n\t"
        "s_mul_i32 s78, s68, 0x01010000\n\t"
        "s_or_b32 s78, s78, 0xffffff\n\t"
        "v_mov_b32 v10, s78\n\t"
        "s_mul_i32 s78, s68, 0x02000200\n\t"
        "s_or_b32 s78, s78, 0xffffff\n\t"
        "v_mov_b32 v11, s78\n\t"
        "ds_write_b128 v15, v[8:11] offset:0\n\t"
        "ds_write_b128 v15, v[8:11] offset:1024\n\t"
        "ds_write_b128 v15, v[8:11] offset:2048\n\t"
        "ds_write_b128 v15, v[8:11] offset:3072\n\t"
        "ds_write_b128 v15, v[8:11] offset:4096\n\t"
        "ds_write_b128 v15, v[8:11] offset:5120\n\t"
        "ds_write_b128 v15, v[8:11] offset:6144\n\t"
        "ds_write_b128 v15, v[8:11] offset:7168\n\t"
        "ds_write_b128 v15, v[8:11] offset:8192\n\t"
        "ds_write_b128 v15, v[8:11] offset:9216\n\t"
        "ds_write_b128 v15, v[8:11] offset:10240\n\t"
        "ds_write_b128 v15, v[8:11] offset:11264\n\t"
        "s_waitcnt lgkmcnt(6)\n\t"
        "ds_write_b128 v15, v[8:11] offset:12288\n\t"
        "ds_write_b128 v15, v[8:11] offset:13312\n\t"
        "ds_write_b128 v15, v[8:11] offset:14336\n\t"
        "ds_write_b128 v15, v[8:11] offset:15360\n\t"
        "v_mov_b32 v4, s68\n\t"
        "ds_write_b32 v2, v4 offset:76\n\t"
        "s_waitcnt vmcnt(11) lgkmcnt(8)\n\t"
        "s_mov_b64 exec, s[24:25]\n\t"
        "v_mad_u32_u24 v56, v16, s66, v13\n\t"
        "v_mad_u32_u24 v57, v17, s66, v13\n\t"
        "v_mad_u32_u24 v58, v18, s66, v13\n\t"
        "v_mad_u32_u24 v59, v19, s66, v13\n\t"
        "v_lshlrev_b32_sdwa v56, 2, v56 dst_sel:DWORD dst_unused:UNUSED_PAD src0_sel:DWORD src1_sel:BYTE_3\n\t"
        "ds_add_rtn_u32 v76, v56, v14 offset:1024\n\t"
        "v_lshlrev_b32_sdwa v57, 2, v57 dst_sel:DWORD dst_unused:UNUSED_PAD src0_sel:DWORD src1_sel:BYTE_3\n\t"
        "ds_add_rtn_u32 v77, v57, v14 offset:1024\n\t"
        "v_lshlrev_b32_sdwa v58, 2, v58 dst_sel:DWORD dst_unused:UNUSED_PAD src0_sel:DWORD src1_sel:BYTE_3\n\t"
        "ds_add_rtn_u32 v78, v58, v14 offset:1024\n\t"
        "v_lshlrev_b32_sdwa v59, 2, v59 dst_sel:DWORD dst_unused:UNUSED_PAD src0_sel:DWORD src1_sel:BYTE_3\n\t"
        "ds_add_rtn_u32 v79, v59, v14 offset:1024\n\t"
        "s_waitcnt vmcnt(10) lgkmcnt(8)\n\t"
        "s_mov_b64 exec, s[26:27]\n\t"
        "v_mad_u32_u24 v60, v20, s66, v13\n\t"
        "v_mad_u32_u24 v61, v21, s66, v13\n\t"
        "v_mad_u32_u24 v62, v22, s66, v13\n\t"
        "v_mad_u32_u24 v63, v23, s66, v13\n\t"
        "v_lshlrev_b32_sdwa v60, 2, v60 dst_sel:DWORD dst_unused:UNUSED_PAD src0_sel:DWORD src1_sel:BYTE_3\n\t"
        "ds_add_rtn_u32 v80, v60, v14 offset:1024\n\t"
        "v_lshlrev_b32_sdwa v61, 2, v61 dst_sel:DWORD dst_unused:UNUSED_PAD src0_sel:DWORD src1_sel:BYTE_3\n\t"
        "ds_add_rtn_u32 v81, v61, v14 offset:1024\n\t"
        "v_lshlrev_b32_sdwa v62, 2, v62 dst_sel:DWORD dst_unused:UNUSED_PAD src0_sel:DWORD src1_sel:BYTE_3\n\t"
        "ds_add_rtn_u32 v82, v62, v14 offset:1024\n\t"
        "v_lshlrev_b32_sdwa v63, 2, v63 dst_sel:DWORD dst_unused:UNUSED_PAD src0_sel:DWORD src1_sel:BYTE_3\n\t"
        "ds_add_rtn_u32 v83, v63, v14 offset:1024\n\t"
        "s_waitcnt vmcnt(9) lgkmcnt(8)\n\t"
        "s_mov_b64 exec, s[28:29]\n\t"
        "v_mad_u32_u24 v64, v24, s66, v13\n\t"
        "v_mad_u32_u24 v65, v25, s66, v13\n\t"
        "v_mad_u32_u24 v66, v26, s66, v13\n\t"
        "v_mad_u32_u24 v67, v27, s66, v13\n\t"
        "v_lshlrev_b32_sdwa v64, 2, v64 dst_sel:DWORD dst_unused:UNUSED_PAD src0_sel:DWORD src1_sel:BYTE_3\n\t"
        "ds_add_rtn_u32 v84, v64, v14 offset:1024\n\t"
        "v_lshlrev_b32_sdwa v65, 2, v65 dst_sel:DWORD dst_unused:UNUSED_PAD src0_sel:DWORD src1_sel:BYTE_3\n\t"
        "ds_add_rtn_u32 v85, v65, v14 offset:1024\n\t"
        "v_lshlrev_b32_sdwa v66, 2, v66 dst_sel:DWORD dst_unused:UNUSED_PAD src0_sel:DWORD src1_sel:BYTE_3\n\t"
        "ds_add_rtn_u32 v86, v66, v14 offset:1024\n\t"
        "v_lshlrev_b32_sdwa v67, 2, v67 dst_sel:DWORD dst_unused:UNUSED_PAD src0_sel:DWORD src1_sel:BYTE_3\n\t"
        "ds_add_rtn_u32 v87, v67, v14 offset:1024\n\t"
        "s_waitcnt vmcnt(8) lgkmcnt(8)\n\t"
        "s_mov_b64 exec, s[30:31]\n\t"
        "v_mad_u32_u24 v68, v28, s66, v13\n\t"
        "v_mad_u32_u24 v69, v29, s66, v13\n\t"
        "v_mad_u32_u24 v70, v30, s66, v13\n\t"
        "v_mad_u32_u24 v71, v31, s66, v13\n\t"
        "v_lshlrev_b32_sdwa v68, 2, v68 dst_sel:DWORD dst_unused:UNUSED_PAD src0_sel:DWORD src1_sel:BYTE_3\n\t"
        "ds_add_rtn_u32 v88, v68, v14 offset:1024\n\t"
        "v_lshlrev_b32_sdwa v69, 2, v69 dst_sel:DWORD dst_unused:UNUSED_PAD src0_sel:DWORD src1_sel:BYTE_3\n\t"
        "ds_add_rtn_u32 v89, v69, v14 offset:1024\n\t"
        "v_lshlrev_b32_sdwa v70, 2, v70 dst_sel:DWORD dst_unused:UNUSED_PAD src0_sel:DWORD src1_sel:BYTE_3\n\t"
        "ds_add_rtn_u32 v90, v70, v14 offset:1024\n\t"
        "v_lshlrev_b32_sdwa v71, 2, v71 dst_sel:DWORD dst_unused:UNUSED_PAD src0_sel:DWORD src1_sel:BYTE_3\n\t"
        "ds_add_rtn_u32 v91, v71, v14 offset:1024\n\t"
        "s_waitcnt vmcnt(7) lgkmcnt(8)\n\t"
        "s_mov_b64 exec, s[32:33]\n\t"
        "v_mad_u32_u24 v72, v32, s66, v13\n\t"
        "v_mad_u32_u24 v73, v33, s66, v13\n\t"
        "v_mad_u32_u24 v74, v34, s66, v13\n\t"
        "v_mad_u32_u24 v75, v35, s66, v13\n\t"
        "v_lshlrev_b32_sdwa v72, 2, v72 dst_sel:DWORD dst_unused:UNUSED_PAD src0_sel:DWORD src1_sel:BYTE_3\n\t"
        "ds_add_rtn_u32 v92, v72, v14 offset:1024\n\t"
        "v_lshlrev_b32_sdwa v73, 2, v73 dst_sel:DWORD dst_unused:UNUSED_PAD src0_sel:DWORD src1_sel:BYTE_3\n\t"
        "ds_add_rtn_u32 v93, v73, v14 offset:1024\n\t"
        "v_lshlrev_b32_sdwa v74, 2, v74 dst_sel:DWORD dst_unused:UNUSED_PAD src0_sel:DWORD src1_sel:BYTE_3\n\t"
        "ds_add_rtn_u32 v94, v74, v14 offset:1024\n\t"
        "v_lshlrev_b32_sdwa v75, 2, v75 dst_sel:DWORD dst_unused:UNUSED_PAD src0_sel:DWORD src1_sel:BYTE_3\n\t"
        "ds_add_rtn_u32 v95, v75, v14 offset:1024\n\t"
        "s_mov_b64 exec, -1\n\t"
        "v_lshlrev_b32 v7, 2, v1\n\t"
        "ds_read_b32 v4, v7 offset:1028\n\t"
        "ds_read_b32 v5, v7 offset:1284\n\t"
        "v_lshl_add_u32 v96, v56, 5, s52\n\t"
        "v_lshl_add_u32 v97, v57, 5, s52\n\t"
        "v_lshl_add_u32 v98, v58, 5, s52\n\t"
        "v_lshl_add_u32 v99, v59, 5, s52\n\t"
        "v_lshl_add_u32 v100, v60, 5, s52\n\t"
        "v_lshl_add_u32 v101, v61, 5, s52\n\t"
        "v_lshl_add_u32 v102, v62, 5, s52\n\t"
        "v_lshl_add_u32 v103, v63, 5, s52\n\t"
        "v_lshl_add_u32 v104, v64, 5, s52\n\t"
        "v_lshl_add_u32 v105, v65, 5, s52\n\t"
        "v_lshl_add_u32 v106, v66, 5, s52\n\t"
        "v_lshl_add_u32 v107, v67, 5, s52\n\t"
        "v_lshl_add_u32 v108, v68, 5, s52\n\t"
        "v_lshl_add_u32 v109, v69, 5, s52\n\t"
        "v_lshl_add_u32 v110, v70, 5, s52\n\t"
        "v_lshl_add_u32 v111, v71, 5, s52\n\t"
        "v_lshl_add_u32 v112, v72, 5, s52\n\t"
        "v_lshl_add_u32 v113, v73, 5, s52\n\t"
        "v_lshl_add_u32 v114, v74, 5, s52\n\t"
        "v_lshl_add_u32 v115, v75, 5, s52\n\t"
        "s_waitcnt lgkmcnt(0)\n\t"
        "v_max_u32 v6, v4, v5\n\t"
        "v_cmp_lt_u32 s[34:35], s55, v6\n\t"
        "v_min_u32 v76, s55, v76\n\t"
        "v_bfe_u32 v76, v76, 3, 5\n\t"
        "v_lshl_add_u32 v76, v76, 2, v96\n\t"
        "v_min_u32 v77, s55, v77\n\t"
        "v_bfe_u32 v77, v77, 3, 5\n\t"
        "v_lshl_add_u32 v77, v77, 2, v97\n\t"
        "v_min_u32 v78, s55, v78\n\t"
        "v_bfe_u32 v78, v78, 3, 5\n\t"
        "v_lshl_add_u32 v78, v78, 2, v98\n\t"
        "v_min_u32 v79, s55, v79\n\t"
        "v_bfe_u32 v79, v79, 3, 5\n\t"
        "v_lshl_add_u32 v79, v79, 2, v99\n\t"
        "s_waitcnt vmcnt(6) lgkmcnt(8)\n\t"
        "v_lshl_add_u32 v16, v16, 16, v36\n\t"
        "v_lshl_add_u32 v17, v17, 16, v37\n\t"
        "v_lshl_add_u32 v18, v18, 16, v38\n\t"
        "v_lshl_add_u32 v19, v19, 16, v39\n\t"
        "s_mov_b64 exec, s[24:25]\n\t"
        "v_mad_i32_i24 v16, v56, s54, v16\n\t"
        "ds_and_b32 v76, v16\n\t"
        "v_mad_i32_i24 v17, v57, s54, v17\n\t"
        "ds_and_b32 v77, v17\n\t"
        "v_mad_i32_i24 v18, v58, s54, v18\n\t"
        "ds_and_b32 v78, v18\n\t"
        "v_mad_i32_i24 v19, v59, s54, v19\n\t"
        "ds_and_b32 v79, v19\n\t"
        "s_mov_b64 exec, -1\n\t"
        "v_min_u32 v80, s55, v80\n\t"
        "v_bfe_u32 v80, v80, 3, 5\n\t"
        "v_lshl_add_u32 v80, v80, 2, v100\n\t"
        "v_min_u32 v81, s55, v81\n\t"
        "v_bfe_u32 v81, v81, 3, 5\n\t"
        "v_lshl_add_u32 v81, v81, 2, v101\n\t"
        "v_min_u32 v82, s55, v82\n\t"
        "v_bfe_u32 v82, v82, 3, 5\n\t"
        "v_lshl_add_u32 v82, v82, 2, v102\n\t"
        "v_min_u32 v83, s55, v83\n\t"
        "v_bfe_u32 v83, v83, 3, 5\n\t"
        "v_lshl_add_u32 v83, v83, 2, v103\n\t"
        "s_waitcnt vmcnt(5) lgkmcnt(8)\n\t"
        "v_lshl_add_u32 v20, v20, 16, v40\n\t"
        "v_lshl_add_u32 v21, v21, 16, v41\n\t"
        "v_lshl_add_u32 v22, v22, 16, v42\n\t"
        "v_lshl_add_u32 v23, v23, 16, v43\n\t"
        "s_mov_b64 exec, s[26:27]\n\t"
        "v_mad_i32_i24 v20, v60, s54, v20\n\t"
        "ds_and_b32 v80, v20\n\t"
        "v_mad_i32_i24 v21, v61, s54, v21\n\t"
        "ds_and_b32 v81, v21\n\t"
        "v_mad_i32_i24 v22, v62, s54, v22\n\t"
        "ds_and_b32 v82, v22\n\t"
        "v_mad_i32_i24 v23, v63, s54, v23\n\t"
        "ds_and_b32 v83, v23\n\t"
        "s_mov_b64 exec, -1\n\t"
        "v_min_u32 v84, s55, v84\n\t"
        "v_bfe_u32 v84, v84, 3, 5\n\t"
        "v_lshl_add_u32 v84, v84, 2, v104\n\t"
        "v_min_u32 v85, s55, v85\n\t"
        "v_bfe_u32 v85, v85, 3, 5\n\t"
        "v_lshl_add_u32 v85, v85, 2, v105\n\t"
        "v_min_u32 v86, s55, v86\n\t"
        "v_bfe_u32 v86, v86, 3, 5\n\t"
        "v_lshl_add_u32 v86, v86, 2, v106\n\t"
        "v_min_u32 v87, s55, v87\n\t"
        "v_bfe_u32 v87, v87, 3, 5\n\t"
        "v_lshl_add_u32 v87, v87, 2, v107\n\t"
        "s_waitcnt vmcnt(4) lgkmcnt(8)\n\t"
        "v_lshl_add_u32 v24, v24, 16, v44\n\t"
        "v_lshl_add_u32 v25, v25, 16, v45\n\t"
        "v_lshl_add_u32 v26, v26, 16, v46\n\t"
        "v_lshl_add_u32 v27, v27, 16, v47\n\t"
        "s_mov_b64 exec, s[28:29]\n\t"
        "v_mad_i32_i24 v24, v64, s54, v24\n\t"
        "ds_and_b32 v84, v24\n\t"
        "v_mad_i32_i24 v25, v65, s54, v25\n\t"
        "ds_and_b32 v85, v25\n\t"
        "v_mad_i32_i24 v26, v66, s54, v26\n\t"
        "ds_and_b32 v86, v26\n\t"
        "v_mad_i32_i24 v27, v67, s54, v27\n\t"
        "ds_and_b32 v87, v27\n\t"
        "s_mov_b64 exec, -1\n\t"
        "v_min_u32 v88, s55, v88\n\t"
        "v_bfe_u32 v88, v88, 3, 5\n\t"
        "v_lshl_add_u32 v88, v88, 2, v108\n\t"
        "v_min_u32 v89, s55, v89\n\t"
        "v_bfe_u32 v89, v89, 3, 5\n\t"
        "v_lshl_add_u32 v89, v89, 2, v109\n\t"
        "v_min_u32 v90, s55, v90\n\t"
        "v_bfe_u32 v90, v90, 3, 5\n\t"
        "v_lshl_add_u32 v90, v90, 2, v110\n\t"
        "v_min_u32 v91, s55, v91\n\t"
        "v_bfe_u32 v91, v91, 3, 5\n\t"
        "v_lshl_add_u32 v91, v91, 2, v111\n\t"
        "s_waitcnt vmcnt(3) lgkmcnt(8)\n\t"
        "v_lshl_add_u32 v28, v28, 16, v48\n\t"
        "v_lshl_add_u32 v29, v29, 16, v49\n\t"
        "v_lshl_add_u32 v30, v30, 16, v50\n\t"
        "v_lshl_add_u32 v31, v31, 16, v51\n\t"
        "s_mov_b64 exec, s[30:31]\n\t"
        "v_mad_i32_i24 v28, v68, s54, v28\n\t"
        "ds_and_b32 v88, v28\n\t"
        "v_mad_i32_i24 v29, v69, s54, v29\n\t"
        "ds_and_b32 v89, v29\n\t"
        "v_mad_i32_i24 v30, v70, s54, v30\n\t"
        "ds_and_b32 v90, v30\n\t"
        "v_mad_i32_i24 v31, v71, s54, v31\n\t"
        "ds_and_b32 v91, v31\n\t"
        "s_mov_b64 exec, -1\n\t"
        "v_min_u32 v92, s55, v92\n\t"
        "v_bfe_u32 v92, v92, 3, 5\n\t"
        "v_lshl_add_u32 v92, v92, 2, v112\n\t"
        "v_min_u32 v93, s55, v93\n\t"
        "v_bfe_u32 v93, v93, 3, 5\n\t"
        "v_lshl_add_u32 v93, v93, 2, v113\n\t"
        "v_min_u32 v94, s55, v94\n\t"
        "v_bfe_u32 v94, v94, 3, 5\n\t"
        "v_lshl_add_u32 v94, v94, 2, v114\n\t"
        "v_min_u32 v95, s55, v95\n\t"
        "v_bfe_u32 v95, v95, 3, 5\n\t"
        "v_lshl_add_u32 v95, v95, 2, v115\n\t"
        "s_waitcnt vmcnt(2) lgkmcnt(8)\n\t"
        "v_lshl_add_u32 v32, v32, 16, v52\n\t"
        "v_lshl_add_u32 v33, v33, 16, v53\n\t"
        "v_lshl_add_u32 v34, v34, 16, v54\n\t"
        "v_lshl_add_u32 v35, v35, 16, v55\n\t"
        "s_mov_b64 exec, s[32:33]\n\t"
        "v_mad_i32_i24 v32, v72, s54, v32\n\t"
        "ds_and_b32 v92, v32\n\t"
        "v_mad_i32_i24 v33, v73, s54, v33\n\t"
        "ds_and_b32 v93, v33\n\t"
        "v_mad_i32_i24 v34, v74, s54, v34\n\t"
        "ds_and_b32 v94, v34\n\t"
        "v_mad_i32_i24 v35, v75, s54, v35\n\t"
        "ds_and_b32 v95, v35\n\t"
        "s_mov_b64 exec, -1\n\t"
        "v_add_u32 v6, s80, v1\n\t"
        "v_add_u32 v7, 64, v6\n\t"
        "v_cmp_gt_u32 s[36:37], s64, v6\n\t"
        "v_cmp_gt_u32 s[38:39], s64, v7\n\t"
        "s_waitcnt vmcnt(0)\n\t"
        "v_xor_b32 v116, v116, v6\n\t"
        "v_bitop3_b32 v116, v117, v116, v6 bitop3:0xde\n\t"
        "v_bitop3_b32 v116, v118, v116, v6 bitop3:0xde\n\t"
        "v_bitop3_b32 v116, v119, v116, v6 bitop3:0xde\n\t"
        "v_xor_b32 v120, v120, v7\n\t"
        "v_bitop3_b32 v120, v121, v120, v7 bitop3:0xde\n\t"
        "v_bitop3_b32 v120, v122, v120, v7 bitop3:0xde\n\t"
        "v_bitop3_b32 v120, v123, v120, v7 bitop3:0xde\n\t"
        "v_cmp_ne_u32 s[40:41], 0, v116\n\t"
        "v_cmp_ne_u32 s[42:43], 0, v120\n\t"
        "s_and_b64 s[38:39], s[38:39], 0xffff\n\t"
        "s_and_b64 s[36:37], s[36:37], s[40:41]\n\t"
        "s_and_b64 s[38:39], s[38:39], s[42:43]\n\t"
        "s_or_b64 s[36:37], s[36:37], s[38:39]\n\t"
        "s_cselect_b32 s67, 1, s67\n\t"
        "s_cmp_lg_u64 s[34:35], 0\n\t"
        "s_cbranch_scc1 .L7_ov\n"
        ".L7_ov2:\n\t"
        "s_waitcnt lgkmcnt(4)\n\t"
        "ds_read_b128 v[16:19], v15 offset:0\n\t"
        "ds_read_b128 v[20:23], v15 offset:1024\n\t"
        "ds_read_b128 v[24:27], v15 offset:2048\n\t"
        "ds_read_b128 v[28:31], v15 offset:3072\n\t"
        "ds_read_b128 v[32:35], v15 offset:4096\n\t"
        "ds_read_b128 v[36:39], v15 offset:5120\n\t"
        "ds_read_b128 v[40:43], v15 offset:6144\n\t"
        "ds_read_b128 v[44:47], v15 offset:7168\n\t"
        "s_waitcnt lgkmcnt(6)\n\t"
        "ds_read_b128 v[48:51], v15 offset:8192\n\t"
        "ds_read_b128 v[52:55], v15 offset:9216\n\t"
        "ds_read_b128 v[56:59], v15 offset:10240\n\t"
        "ds_read_b128 v[60:63], v15 offset:11264\n\t"
        "ds_read_b128 v[64:67], v15 offset:12288\n\t"
        "ds_read_b128 v[68:71], v15 offset:13312\n\t"
        "ds_read_b128 v[72:75], v15 offset:14336\n\t"
        "ds_read_b128 v[76:79], v15 offset:15360\n\t"
        "s_mov_b64 exec, 1\n\t"
        "ds_add_u32 v2, v3 offset:72\n\t"
        "s_mov_b64 exec, -1\n\t"
        "s_and_b32 s78, s67, 1\n\t"
        "s_lshl_b32 s78, s78, 24\n\t"
        "s_waitcnt lgkmcnt(15)\n\t"
        "v_xor_b32 v19, s78, v19\n\t"
        "buffer_store_dwordx4 v[16:19], v12, s[20:23], s48 offen offset:0 sc1\n\t"
        "s_waitcnt lgkmcnt(15)\n\t"
        "v_xor_b32 v23, s78, v23\n\t"
        "buffer_store_dwordx4 v[20:23], v12, s[20:23], s48 offen offset:1024 sc1\n\t"
        "s_waitcnt lgkmcnt(14)\n\t"
        "v_xor_b32 v27, s78, v27\n\t"
        "buffer_store_dwordx4 v[24:27], v12, s[20:23], s48 offen offset:2048 sc1\n\t"
        "s_waitcnt lgkmcnt(13)\n\t"
        "v_xor_b32 v31, s78, v31\n\t"
        "buffer_store_dwordx4 v[28:31], v12, s[20:23], s48 offen offset:3072 sc1\n\t"
        "s_waitcnt lgkmcnt(12)\n\t"
        "v_xor_b32 v35, s78, v35\n\t"
        "buffer_store_dwordx4 v[32:35], v12, s[20:23], s49 offen offset:0 sc1\n\t"
        "s_waitcnt lgkmcnt(11)\n\t"
        "v_xor_b32 v39, s78, v39\n\t"
        "buffer_store_dwordx4 v[36:39], v12, s[20:23], s49 offen offset:1024 sc1\n\t"
        "s_waitcnt lgkmcnt(10)\n\t"
        "v_xor_b32 v43, s78, v43\n\t"
        "buffer_store_dwordx4 v[40:43], v12, s[20:23], s49 offen offset:2048 sc1\n\t"
        "s_waitcnt lgkmcnt(9)\n\t"
        "v_xor_b32 v47, s78, v47\n\t"
        "buffer_store_dwordx4 v[44:47], v12, s[20:23], s49 offen offset:3072 sc1\n\t"
        "s_waitcnt lgkmcnt(8)\n\t"
        "v_xor_b32 v51, s78, v51\n\t"
        "buffer_store_dwordx4 v[48:51], v12, s[20:23], s50 offen offset:0 sc1\n\t"
        "s_waitcnt lgkmcnt(7)\n\t"
        "v_xor_b32 v55, s78, v55\n\t"
        "buffer_store_dwordx4 v[52:55], v12, s[20:23], s50 offen offset:1024 sc1\n\t"
        "s_waitcnt lgkmcnt(6)\n\t"
        "v_xor_b32 v59, s78, v59\n\t"
        "buffer_store_dwordx4 v[56:59], v12, s[20:23], s50 offen offset:2048 sc1\n\t"
        "s_waitcnt lgkmcnt(5)\n\t"
        "v_xor_b32 v63, s78, v63\n\t"
        "buffer_store_dwordx4 v[60:63], v12, s[20:23], s50 offen offset:3072 sc1\n\t"
        "s_waitcnt lgkmcnt(4)\n\t"
        "v_xor_b32 v67, s78, v67\n\t"
        "buffer_store_dwordx4 v[64:67], v12, s[20:23], s51 offen offset:0 sc1\n\t"
        "s_waitcnt lgkmcnt(3)\n\t"
        "v_xor_b32 v71, s78, v71\n\t"
        "buffer_store_dwordx4 v[68:71], v12, s[20:23], s51 offen offset:1024 sc1\n\t"
        "s_waitcnt lgkmcnt(2)\n\t"
        "v_xor_b32 v75, s78, v75\n\t"
        "buffer_store_dwordx4 v[72:75], v12, s[20:23], s51 offen offset:2048 sc1\n\t"
        "s_waitcnt lgkmcnt(1)\n\t"
        "v_xor_b32 v79, s78, v79\n\t"
        "buffer_store_dwordx4 v[76:79], v12, s[20:23], s51 offen offset:3072 sc1\n\t"
        "s_mov_b64 exec, 1\n\t"
        "ds_add_u32 v2, v3 offset:44\n\t"
        "s_mov_b64 exec, -1\n\t"
        "s_branch .L7_Q\n"
        ".L7_ov:\n\t"
        "v_lshlrev_b32 v7, 7, v1\n\t"
        "v_add_u32 v7, 84092, v7\n\t"
        "v_add_u32 v11, -1, v11\n\t"
        "v_cmpx_lt_u32 vcc, 1056, v4\n\t"
        "ds_write_b32 v7, v11\n\t"
        "s_mov_b64 exec, -1\n\t"
        "v_cmpx_lt_u32 vcc, 1056, v5\n\t"
        "ds_write_b32 v7, v11 offset:8192\n\t"
        "s_mov_b64 exec, -1\n\t"
        "s_branch .L7_ov2\n"
        ".L7_V2:\n\t"
        "s_barrier\n\t"
        "v_and_b32 v5, 7, v1\n\t"
        "v_mul_u32_u24 v5, 0x420, v5\n\t"
        "v_and_b32 v6, 8, v1\n\t"
        "v_lshl_add_u32 v5, v6, 6, v5\n\t"
        "v_and_b32 v6, 48, v1\n\t"
        "v_add_u32 v5, v5, v6\n\t"
        "v_add_u32 v5, 83968, v5\n\t"
        "s_lshr_b32 s78, s81, 1\n\t"
        "s_lshl3_add_u32 s78, s78, s80\n\t"
        "s_and_b32 s79, s81, 1\n\t"
        "s_lshl1_add_u32 s78, s78, s79\n\t"
        "s_lshl_b32 s82, s78, 10\n\t"
        "s_add_u32 s82, s82, 131072\n\t"
        "s_waitcnt lgkmcnt(0)\n\t"
        "s_add_u32 s68, s68, 1\n\t"
        "v_mov_b32 v4, s68\n\t"
        "ds_write_b32 v2, v4 offset:76\n\t"
        "s_and_b32 s73, s68, 1\n\t"
        "s_cselect_b32 s73, 1, 0x10000\n\t"
        "v_mov_b32 v16, s73\n\t"
        "v_and_b32 v17, 7, v1\n\t"
        "v_add_u32 v17, 1, v17\n\t"
        "v_lshrrev_b32 v17, v17, s68\n\t"
        "s_mov_b32 s49, 0x10001\n\t"
        "v_and_b32 v17, s49, v17\n\t"
        "s_mov_b32 s48, 0x3db8aa3b\n\t"
        "s_waitcnt vmcnt(0)\n\t"
        "ds_read_b128 v[32:35], v5 offset:0\n\t"
        "ds_read_b128 v[36:39], v5 offset:8448\n\t"
        "ds_read_b128 v[40:43], v5 offset:16896\n\t"
        "ds_read_b128 v[44:47], v5 offset:64\n\t"
        "ds_read_b128 v[48:51], v5 offset:8512\n\t"
        "ds_read_b128 v[52:55], v5 offset:16960\n\t"
        "ds_read_b128 v[56:59], v5 offset:128\n\t"
        "ds_read_b128 v[60:63], v5 offset:8576\n\t"
        "ds_read_b128 v[64:67], v5 offset:17024\n\t"
        "ds_read_b128 v[68:71], v5 offset:192\n\t"
        "ds_read_b128 v[72:75], v5 offset:8640\n\t"
        "ds_read_b128 v[76:79], v5 offset:17088\n\t"
        "s_waitcnt lgkmcnt(9)\n\t"
        "v_mfma_f32_16x16x32_bf16 v[20:23], v[36:39], v[32:35], 0\n\t"
        "v_mfma_f32_16x16x32_bf16 v[24:27], v[40:43], v[32:35], 0\n\t"
        "ds_read_b128 v[80:83], v5 offset:256\n\t"
        "ds_read_b128 v[84:87], v5 offset:8704\n\t"
        "ds_read_b128 v[88:91], v5 offset:17152\n\t"
        "s_waitcnt lgkmcnt(9)\n\t"
        "v_mfma_f32_16x16x32_bf16 v[20:23], v[48:51], v[44:47], v[20:23]\n\t"
        "v_mfma_f32_16x16x32_bf16 v[24:27], v[52:55], v[44:47], v[24:27]\n\t"
        "ds_read_b128 v[92:95], v5 offset:320\n\t"
        "ds_read_b128 v[96:99], v5 offset:8768\n\t"
        "ds_read_b128 v[100:103], v5 offset:17216\n\t"
        "s_waitcnt lgkmcnt(9)\n\t"
        "v_mfma_f32_16x16x32_bf16 v[20:23], v[60:63], v[56:59], v[20:23]\n\t"
        "v_mfma_f32_16x16x32_bf16 v[24:27], v[64:67], v[56:59], v[24:27]\n\t"
        "ds_read_b128 v[104:107], v5 offset:384\n\t"
        "ds_read_b128 v[108:111], v5 offset:8832\n\t"
        "ds_read_b128 v[112:115], v5 offset:17280\n\t"
        "s_waitcnt lgkmcnt(9)\n\t"
        "v_mfma_f32_16x16x32_bf16 v[20:23], v[72:75], v[68:71], v[20:23]\n\t"
        "v_mfma_f32_16x16x32_bf16 v[24:27], v[76:79], v[68:71], v[24:27]\n\t"
        "ds_read_b128 v[116:119], v5 offset:448\n\t"
        "ds_read_b128 v[120:123], v5 offset:8896\n\t"
        "ds_read_b128 v[124:127], v5 offset:17344\n\t"
        "s_waitcnt lgkmcnt(9)\n\t"
        "v_mfma_f32_16x16x32_bf16 v[20:23], v[84:87], v[80:83], v[20:23]\n\t"
        "v_mfma_f32_16x16x32_bf16 v[24:27], v[88:91], v[80:83], v[24:27]\n\t"
        "s_mov_b64 exec, 1\n\t"
        "ds_add_u32 v2, v3 offset:72\n\t"
        "s_mov_b64 exec, -1\n\t"
        "s_waitcnt lgkmcnt(7)\n\t"
        "v_mfma_f32_16x16x32_bf16 v[20:23], v[96:99], v[92:95], v[20:23]\n\t"
        "v_mfma_f32_16x16x32_bf16 v[24:27], v[100:103], v[92:95], v[24:27]\n\t"
        "s_waitcnt lgkmcnt(4)\n\t"
        "v_mfma_f32_16x16x32_bf16 v[20:23], v[108:111], v[104:107], v[20:23]\n\t"
        "v_mfma_f32_16x16x32_bf16 v[24:27], v[112:115], v[104:107], v[24:27]\n\t"
        "s_waitcnt lgkmcnt(1)\n\t"
        "v_mfma_f32_16x16x32_bf16 v[20:23], v[120:123], v[116:119], v[20:23]\n\t"
        "v_mfma_f32_16x16x32_bf16 v[24:27], v[124:127], v[116:119], v[24:27]\n\t"
        "s_nop 6\n\t"
        "v_mul_f32 v20, s48, v20\n\t"
        "v_mul_f32 v21, s48, v21\n\t"
        "v_mul_f32 v22, s48, v22\n\t"
        "v_mul_f32 v23, s48, v23\n\t"
        "v_mul_f32 v24, s48, v24\n\t"
        "v_mul_f32 v25, s48, v25\n\t"
        "v_mul_f32 v26, s48, v26\n\t"
        "v_mul_f32 v27, s48, v27\n\t"
        "s_mov_b32 s48, 0x07060302\n\t"
        "v_add_u32 v20, 0x10000, v20\n\t"
        "v_add_u32 v21, 0x10000, v21\n\t"
        "v_add_u32 v22, 0x10000, v22\n\t"
        "v_add_u32 v23, 0x10000, v23\n\t"
        "v_perm_b32 v20, v21, v20, s48\n\t"
        "v_perm_b32 v21, v23, v22, s48\n\t"
        "v_cvt_pk_bf16_f32 v22, v24, v25\n\t"
        "v_cvt_pk_bf16_f32 v23, v26, v27\n\t"
        "v_bfi_b32 v20, s49, v16, v20\n\t"
        "v_bfi_b32 v21, s49, v17, v21\n\t"
        "buffer_store_dwordx4 v[20:23], v12, s[20:23], s82 offen sc1\n\t"
        "s_mov_b64 exec, 1\n\t"
        "ds_add_u32 v2, v3 offset:44\n\t"
        "s_mov_b64 exec, -1\n\t"
        "s_branch .L7_Q\n"
        ".L7_P2:\n\t"
        "v_lshlrev_b32 v4, 4, v1\n\t"
        "v_add_u32 v4, 67584, v4\n\t"
        "v_mul_u32_u24 v5, 200, v1\n\t"
        "v_add_u32 v5, 67584, v5\n\t"
        "v_mov_b32 v8, 0xffff\n\t"
        "v_mov_b32 v9, v8\n\t"
        "v_mov_b64 v[10:11], v[8:9]\n\t"
        "s_waitcnt lgkmcnt(0)\n\t"
        "s_mul_i32 s72, s71, s65\n\t"
        "s_barrier\n\t"
        "ds_write_b128 v4, v[8:11] offset:0\n\t"
        "ds_write_b128 v4, v[8:11] offset:1024\n\t"
        "ds_write_b128 v4, v[8:11] offset:2048\n\t"
        "ds_write_b128 v4, v[8:11] offset:3072\n\t"
        "ds_write_b128 v4, v[8:11] offset:4096\n\t"
        "ds_write_b128 v4, v[8:11] offset:5120\n\t"
        "ds_write_b128 v4, v[8:11] offset:6144\n\t"
        "ds_write_b128 v4, v[8:11] offset:7168\n\t"
        "ds_write_b128 v4, v[8:11] offset:8192\n\t"
        "ds_write_b128 v4, v[8:11] offset:9216\n\t"
        "ds_write_b128 v4, v[8:11] offset:10240\n\t"
        "ds_write_b128 v4, v[8:11] offset:11264\n\t"
        "s_waitcnt lgkmcnt(6)\n\t"
        "ds_write_b128 v4, v[8:11] offset:12288\n\t"
        "ds_write_b128 v4, v[8:11] offset:13312\n\t"
        "ds_write_b128 v4, v[8:11] offset:14336\n\t"
        "ds_write_b128 v4, v[8:11] offset:15360\n\t"
        "ds_write_b32 v5, v2 offset:192\n\t"
        "s_mov_b64 exec, 0xffff\n\t"
        "ds_write_b32 v5, v2 offset:12992\n\t"
        "s_mov_b64 exec, 1\n\t"
        "ds_add_u32 v2, v3 offset:8\n\t"
        "s_mov_b64 exec, -1\n\t"
        "s_branch .L7_Q0\n"
        ".L7_R2:\n\t"
        "s_barrier\n\t"
        "s_sub_u32 s79, s64, s72\n\t"
        "s_min_i32 s79, s79, s65\n\t"
        "s_max_i32 s79, s79, 0\n\t"
        "v_mov_b32 v32, v1\n\t"
        "v_add_u32 v33, 64, v1\n\t"
        "v_cmp_gt_u32 s[32:33], s79, v32\n\t"
        "v_cmp_gt_u32 s[34:35], s79, v33\n\t"
        "v_cmp_gt_u32 s[36:37], 40, v1\n\t"
        "s_sub_u32 s78, 0x100, s65\n\t"
        "v_add_u32 v34, s78, v32\n\t"
        "v_add_u32 v6, 0xffffffb0, v34\n\t"
        "v_cmp_gt_u32 vcc, 0x100, v34\n\t"
        "s_nop 1\n\t"
        "v_cndmask_b32 v34, v6, v34, vcc\n\t"
        "v_add_u32 v35, s78, v33\n\t"
        "v_add_u32 v6, 0xffffffb0, v35\n\t"
        "v_cmp_gt_u32 vcc, 0x100, v35\n\t"
        "s_nop 1\n\t"
        "v_cndmask_b32 v35, v6, v35, vcc\n\t"
        "v_mov_b32 v12, 0x8000\n\t"
        "v_cndmask_b32 v44, v12, 0, s[32:33]\n\t"
        "v_cndmask_b32 v45, v12, 0, s[34:35]\n\t"
        "v_cndmask_b32 v46, 63, 31, s[36:37]\n\t"
        "v_mov_b32 v47, 63\n\t"
        "s_waitcnt vmcnt(0)\n\t"
        "v_lshlrev_b32 v16, 16, v16\n\t"
        "v_lshlrev_b32 v17, 16, v17\n\t"
        "v_mul_f32 v36, v16, v16\n\t"
        "v_mul_f32 v37, v17, v17\n\t"
        "v_rcp_f32 v36, v36\n\t"
        "v_rcp_f32 v37, v37\n\t"
        "v_or3_b32 v16, v16, v34, v44\n\t"
        "v_or3_b32 v17, v17, v35, v45\n\t"
        "v_add_f32 v36, 0xc0600000, v36\n\t"
        "v_add_f32 v37, 0xc0600000, v37\n\t"
        "v_cvt_u32_f32 v36, v36\n\t"
        "v_cvt_u32_f32 v37, v37\n\t"
        "v_min_u32 v36, 31, v36\n\t"
        "v_min_u32 v37, 31, v37\n\t"
        "v_cndmask_b32 v36, 0, v36, s[32:33]\n\t"
        "v_cndmask_b32 v37, 0, v37, s[34:35]\n\t"
        "v_sub_u32 v36, v46, v36\n\t"
        "v_sub_u32 v37, v47, v37\n\t"
        "v_lshlrev_b32 v36, 2, v36\n\t"
        "v_lshlrev_b32 v37, 2, v37\n\t"
        "ds_add_rtn_u32 v40, v36, v3 offset:256\n\t"
        "s_mov_b64 exec, 0xffff\n\t"
        "ds_add_rtn_u32 v41, v37, v3 offset:256\n\t"
        "s_mov_b64 exec, -1\n\t"
        "v_lshlrev_b32 v9, 2, v1\n\t"
        "ds_read_b32 v10, v9 offset:256\n\t"
        "s_cmp_eq_u32 s70, 0\n\t"
        "s_cselect_b32 s78, 56, 8\n\t"
        "s_cselect_b32 s79, 8, 56\n\t"
        "v_mov_b32 v49, -40\n\t"
        "v_cndmask_b32 v48, v49, 0, s[36:37]\n\t"
        "s_mov_b32 s40, 0x23108954\n\t"
        "s_mov_b32 s41, 0x67\n\t"
        "v_mov_b32 v13, s78\n\t"
        "v_mov_b32 v14, s79\n\t"
        "v_cndmask_b32 v13, v14, v13, s[36:37]\n\t"
        "s_waitcnt lgkmcnt(0)\n\t"
        "v_mov_b32 v11, v10\n\t"
        "s_nop 1\n\t"
        "v_add_u32_dpp v11, v11, v11 row_shr:1 row_mask:0xf bank_mask:0xf bound_ctrl:1\n\t"
        "s_nop 1\n\t"
        "v_add_u32_dpp v11, v11, v11 row_shr:2 row_mask:0xf bank_mask:0xf bound_ctrl:1\n\t"
        "s_nop 1\n\t"
        "v_add_u32_dpp v11, v11, v11 row_shr:4 row_mask:0xf bank_mask:0xf bound_ctrl:1\n\t"
        "s_nop 1\n\t"
        "v_add_u32_dpp v11, v11, v11 row_shr:8 row_mask:0xf bank_mask:0xf bound_ctrl:1\n\t"
        "s_nop 1\n\t"
        "v_add_u32_dpp v11, v11, v11 row_bcast:15 row_mask:0xa bank_mask:0xf\n\t"
        "s_nop 1\n\t"
        "v_add_u32_dpp v11, v11, v11 row_bcast:31 row_mask:0xc bank_mask:0xf\n\t"
        "v_sub_u32 v11, v11, v10\n\t"
        "ds_bpermute_b32 v36, v36, v11\n\t"
        "ds_bpermute_b32 v37, v37, v11\n\t"
        "v_lshlrev_b32 v9, 1, v1\n\t"
        "s_waitcnt lgkmcnt(0)\n\t"
        "v_add3_u32 v36, v36, v40, v48\n\t"
        "v_and_b32 v6, -4, v36\n\t"
        "v_lshrrev_b64 v[10:11], v6, s[40:41]\n\t"
        "v_and_b32 v10, 15, v10\n\t"
        "v_and_b32 v6, 3, v36\n\t"
        "v_lshl_add_u32 v6, v10, 2, v6\n\t"
        "v_add_u32 v36, v6, v13\n\t"
        "v_and_b32 v6, 15, v36\n\t"
        "v_lshlrev_b32 v6, 4, v6\n\t"
        "v_lshl_add_u32 v6, v36, 8, v6\n\t"
        "v_lshlrev_b32 v36, 2, v36\n\t"
        "ds_write_b32 v36, v16 offset:512\n\t"
        "ds_write_b16 v9, v6 offset:1792\n\t"
        "v_add3_u32 v37, v37, v41, v49\n\t"
        "v_and_b32 v6, -4, v37\n\t"
        "v_lshrrev_b64 v[10:11], v6, s[40:41]\n\t"
        "v_and_b32 v10, 15, v10\n\t"
        "v_and_b32 v6, 3, v37\n\t"
        "v_lshl_add_u32 v6, v10, 2, v6\n\t"
        "v_add_u32 v37, v6, v14\n\t"
        "v_and_b32 v6, 15, v37\n\t"
        "v_lshlrev_b32 v6, 4, v6\n\t"
        "v_lshl_add_u32 v6, v37, 8, v6\n\t"
        "v_lshlrev_b32 v37, 2, v37\n\t"
        "s_mov_b64 exec, 0xffff\n\t"
        "ds_write_b32 v37, v17 offset:512\n\t"
        "ds_write_b16 v9, v6 offset:1920\n\t"
        "s_mov_b64 exec, 1\n\t"
        "ds_add_u32 v2, v3 offset:8\n\t"
        "s_mov_b64 exec, -1\n"
        ".L7_w46:\n\t"
        "ds_read_b64 v[4:5], v2 offset:72\n\t"
        "s_waitcnt lgkmcnt(0)\n\t"
        "v_cmp_gt_u16 vcc, 1, v4\n\t"
        "s_cbranch_vccz .L7_d47\n\t"
        "s_sleep 2\n\t"
        "s_sub_i32 s83, s83, 1\n\t"
        "s_cmp_gt_i32 s83, 0\n\t"
        "s_cbranch_scc1 .L7_w46\n"
        ".L7_d47:\n\t"
        "v_readfirstlane_b32 s68, v5\n\t"
        "v_lshlrev_b32 v22, 4, v1\n\t"
        "v_add_u32 v9, 157952, v22\n\t"
        "v_and_b32 v10, 60, v1\n\t"
        "ds_read_b32 v24, v10 offset:704\n\t"
        "ds_read_b32 v25, v10 offset:768\n\t"
        "ds_read_b32 v26, v10 offset:832\n\t"
        "v_and_b32 v11, 15, v1\n\t"
        "v_lshrrev_b32 v12, 4, v1\n\t"
        "v_xor_b32 v16, v11, v12\n\t"
        "v_lshlrev_b32 v16, 4, v16\n\t"
        "v_lshl_add_u32 v16, v11, 8, v16\n\t"
        "v_xor_b32 v17, 64, v16\n\t"
        "v_xor_b32 v18, 128, v16\n\t"
        "v_xor_b32 v19, 192, v16\n\t"
        "ds_write_b128 v9, v[16:19]\n\t"
        "s_and_b32 s73, s68, 1\n\t"
        "s_cselect_b32 s73, 1, 0x10000\n\t"
        "v_mov_b32 v20, s73\n\t"
        "v_and_b32 v21, 7, v1\n\t"
        "v_add_u32 v21, 1, v21\n\t"
        "v_lshrrev_b32 v21, v21, s68\n\t"
        "s_mov_b32 s49, 0x10001\n\t"
        "v_and_b32 v21, s49, v21\n\t"
        "v_or_b32 v23, 0x80, v22\n\t"
        "ds_write_b128 v9, v[20:23] offset:1024\n\t"
        "s_add_u32 s78, s72, s65\n\t"
        "s_sub_u32 s78, s78, 0x100\n\t"
        "s_lshl_b32 s78, s78, 9\n\t"
        "v_and_b32 v13, 48, v22\n\t"
        "v_add_u32 v14, s78, v13\n\t"
        "s_movk_i32 s78, 0x200\n\t"
        "v_bfe_u32 v27, v1, 1, 2\n\t"
        "v_xor_b32 v27, v27, v12\n\t"
        "v_lshlrev_b32 v27, 4, v27\n\t"
        "v_lshl_add_u32 v27, v11, 6, v27\n\t"
        "v_lshlrev_b32 v28, 1, v1\n\t"
        "v_and_b32 v28, 48, v28\n\t"
        "v_xor_b32 v28, v28, v22\n\t"
        "s_waitcnt lgkmcnt(2)\n\t"
        "v_mad_u32_u16 v24, v24, s78, v14\n\t"
        "v_mad_u32_u16 v25, v25, s78, v14\n\t"
        "v_mad_u32_u16 v26, v26, s78, v14\n\t"
        "s_mov_b32 exec_hi, 0\n\t"
        "v_mov_b32 v24, -2.0\n\t"
        "s_mov_b64 exec, -1\n\t"
        "ds_write_b128 v9, v[24:27] offset:2048\n\t"
        "s_mov_b32 s78, 0x3f80\n\t"
        "s_mov_b32 s79, 0\n\t"
        "v_lshlrev_b64 v[32:33], v13, s[78:79]\n\t"
        "s_mov_b32 s78, 0x00f0000f\n\t"
        "s_mov_b32 s79, 0xf0000f00\n\t"
        "v_cndmask_b32 v32, 0, v32, s[78:79]\n\t"
        "v_cndmask_b32 v33, 0, v33, s[78:79]\n\t"
        "v_mov_b64 v[30:31], 0\n\t"
        "v_mov_b64 v[34:35], 0\n\t"
        "ds_write_b128 v9, v[28:31] offset:3072\n\t"
        "ds_write_b128 v9, v[32:35] offset:4096\n\t"
        "s_branch .L7_Q\n"
        ".L7_Q0:\n"
        ".L7_w48:\n\t"
        "ds_read_b64 v[4:5], v2 offset:72\n\t"
        "s_waitcnt lgkmcnt(0)\n\t"
        "v_cmp_gt_u16 vcc, 1, v4\n\t"
        "s_cbranch_vccz .L7_d49\n\t"
        "s_sleep 2\n\t"
        "s_sub_i32 s83, s83, 1\n\t"
        "s_cmp_gt_i32 s83, 0\n\t"
        "s_cbranch_scc1 .L7_w48\n"
        ".L7_d49:\n\t"
        "v_readfirstlane_b32 s68, v5\n"
        ".L7_Q:\n\t"
        "s_setprio 2\n\t"
        "v_and_b32 v5, 7, v1\n\t"
        "v_mul_u32_u24 v5, 0x420, v5\n\t"
        "v_and_b32 v6, 8, v1\n\t"
        "v_lshl_add_u32 v5, v6, 6, v5\n\t"
        "v_and_b32 v6, 48, v1\n\t"
        "v_add_u32 v5, v5, v6\n\t"
        "s_and_b32 s73, s68, 1\n\t"
        "s_cselect_b32 s73, 1, 0x10000\n\t"
        "v_and_b32 v6, 7, v1\n\t"
        "v_add_u32 v6, 1, v6\n\t"
        "v_lshrrev_b32 v6, v6, s68\n\t"
        "s_mov_b32 s49, 0x10001\n\t"
        "v_and_b32 v6, s49, v6\n\t"
        "s_add_u32 s80, s69, s76\n\t"
        "s_and_b32 s80, s80, 3\n\t"
        "s_lshl2_add_u32 s78, s70, s80\n\t"
        "s_lshl_b32 s78, s78, 14\n\t"
        "s_add_u32 s78, s78, 131072\n\t"
        "v_lshl_add_u32 v4, v1, 4, s78\n\t"
        "s_mov_b32 s24, 4096\n\t"
        "s_mov_b32 s25, 8192\n\t"
        "s_mov_b32 s26, 12288\n"
        ".L7_g50:\n\t"
        "s_memrealtime s[78:79]\n\t"
        "s_waitcnt lgkmcnt(0)\n\t"
        "s_sub_u32 s78, s78, s74\n\t"
        "s_cmp_ge_u32 s78, 300\n\t"
        "s_cbranch_scc1 .L7_h51\n\t"
        "s_sleep 1\n\t"
        "s_branch .L7_g50\n"
        ".L7_h51:\n"
        ".L7_w52:\n\t"
        "ds_read_b32 v0, v2 offset:88\n\t"
        "s_waitcnt lgkmcnt(0)\n\t"
        "v_cmp_gt_u16 vcc, 8, v0\n\t"
        "s_cbranch_vccz .L7_d53\n\t"
        "s_sleep 1\n\t"
        "s_sub_i32 s83, s83, 1\n\t"
        "s_cmp_gt_i32 s83, 0\n\t"
        "s_cbranch_scc1 .L7_w52\n"
        ".L7_d53:\n\t"
        "buffer_load_dwordx4 v[64:67], v4, s[20:23], 0 offen offset:0 sc1\n\t"
        "buffer_load_dwordx4 v[68:71], v4, s[20:23], 0 offen offset:1024 sc1\n\t"
        "buffer_load_dwordx4 v[72:75], v4, s[20:23], 0 offen offset:2048 sc1\n\t"
        "buffer_load_dwordx4 v[76:79], v4, s[20:23], 0 offen offset:3072 sc1\n\t"
        "buffer_load_dwordx4 v[80:83], v4, s[20:23], s24 offen offset:0 sc1\n\t"
        "buffer_load_dwordx4 v[84:87], v4, s[20:23], s24 offen offset:1024 sc1\n\t"
        "buffer_load_dwordx4 v[88:91], v4, s[20:23], s24 offen offset:2048 sc1\n\t"
        "buffer_load_dwordx4 v[92:95], v4, s[20:23], s24 offen offset:3072 sc1\n\t"
        "buffer_load_dwordx4 v[96:99], v4, s[20:23], s25 offen offset:0 sc1\n\t"
        "buffer_load_dwordx4 v[100:103], v4, s[20:23], s25 offen offset:1024 sc1\n\t"
        "buffer_load_dwordx4 v[104:107], v4, s[20:23], s25 offen offset:2048 sc1\n\t"
        "buffer_load_dwordx4 v[108:111], v4, s[20:23], s25 offen offset:3072 sc1\n\t"
        "buffer_load_dwordx4 v[112:115], v4, s[20:23], s26 offen offset:0 sc1\n\t"
        "buffer_load_dwordx4 v[116:119], v4, s[20:23], s26 offen offset:1024 sc1\n\t"
        "buffer_load_dwordx4 v[120:123], v4, s[20:23], s26 offen offset:2048 sc1\n\t"
        "buffer_load_dwordx4 v[124:127], v4, s[20:23], s26 offen offset:3072 sc1\n\t"
        "s_mov_b64 exec, 1\n\t"
        "ds_add_u32 v2, v3 offset:84\n\t"
        "s_mov_b64 exec, -1\n"
        ".L7_w54:\n\t"
        "ds_read_b32 v0, v2 offset:0\n\t"
        "s_waitcnt lgkmcnt(0)\n\t"
        "v_cmp_gt_u16 vcc, 4, v0\n\t"
        "s_cbranch_vccz .L7_d55\n\t"
        "s_sleep 1\n\t"
        "s_sub_i32 s83, s83, 1\n\t"
        "s_cmp_gt_i32 s83, 0\n\t"
        "s_cbranch_scc1 .L7_w54\n"
        ".L7_d55:\n\t"
        "s_branch .L7_qg\n"
        ".L7_qr:\n\t"
        "s_waitcnt lgkmcnt(0)\n\t"
        "s_sleep 8\n\t"
        "s_sub_i32 s83, s83, 64\n\t"
        "s_cmp_gt_i32 s83, 0\n\t"
        "s_cbranch_scc0 .L7_qd\n\t"
        "buffer_load_dwordx4 v[64:67], v4, s[20:23], 0 offen offset:0 sc1\n\t"
        "buffer_load_dwordx4 v[68:71], v4, s[20:23], 0 offen offset:1024 sc1\n\t"
        "buffer_load_dwordx4 v[72:75], v4, s[20:23], 0 offen offset:2048 sc1\n\t"
        "buffer_load_dwordx4 v[76:79], v4, s[20:23], 0 offen offset:3072 sc1\n\t"
        "buffer_load_dwordx4 v[80:83], v4, s[20:23], s24 offen offset:0 sc1\n\t"
        "buffer_load_dwordx4 v[84:87], v4, s[20:23], s24 offen offset:1024 sc1\n\t"
        "buffer_load_dwordx4 v[88:91], v4, s[20:23], s24 offen offset:2048 sc1\n\t"
        "buffer_load_dwordx4 v[92:95], v4, s[20:23], s24 offen offset:3072 sc1\n\t"
        "buffer_load_dwordx4 v[96:99], v4, s[20:23], s25 offen offset:0 sc1\n\t"
        "buffer_load_dwordx4 v[100:103], v4, s[20:23], s25 offen offset:1024 sc1\n\t"
        "buffer_load_dwordx4 v[104:107], v4, s[20:23], s25 offen offset:2048 sc1\n\t"
        "buffer_load_dwordx4 v[108:111], v4, s[20:23], s25 offen offset:3072 sc1\n\t"
        "buffer_load_dwordx4 v[112:115], v4, s[20:23], s26 offen offset:0 sc1\n\t"
        "buffer_load_dwordx4 v[116:119], v4, s[20:23], s26 offen offset:1024 sc1\n\t"
        "buffer_load_dwordx4 v[120:123], v4, s[20:23], s26 offen offset:2048 sc1\n\t"
        "buffer_load_dwordx4 v[124:127], v4, s[20:23], s26 offen offset:3072 sc1\n"
        ".L7_qg:\n\t"
        "v_mov_b32 v7, 0\n\t"
        "ds_read_b128 v[8:11], v5 offset:2048\n\t"
        "ds_read_b128 v[12:15], v5 offset:10496\n\t"
        "ds_read_b128 v[16:19], v5 offset:18944\n\t"
        "ds_read_b128 v[20:23], v5 offset:27392\n\t"
        "s_waitcnt vmcnt(14) lgkmcnt(3)\n\t"
        "v_xor_b32 v64, s73, v64\n\t"
        "v_xor_b32 v65, v6, v65\n\t"
        "v_xor_b32 v68, s73, v68\n\t"
        "v_xor_b32 v69, v6, v69\n\t"
        "v_mfma_f32_16x16x32_bf16 v[24:27], v[64:67], v[8:11], 0\n\t"
        "s_nop 0\n\t"
        "v_mfma_f32_16x16x32_bf16 v[28:31], v[68:71], v[8:11], 0\n\t"
        "v_or3_b32 v7, v7, v64, v65\n\t"
        "ds_read_b128 v[8:11], v5 offset:35840\n\t"
        "s_waitcnt lgkmcnt(3)\n\t"
        "v_mfma_f32_16x16x32_bf16 v[32:35], v[64:67], v[12:15], 0\n\t"
        "v_mfma_f32_16x16x32_bf16 v[36:39], v[68:71], v[12:15], 0\n\t"
        "v_or3_b32 v7, v7, v68, v69\n\t"
        "ds_read_b128 v[12:15], v5 offset:2112\n\t"
        "s_waitcnt lgkmcnt(3)\n\t"
        "v_mfma_f32_16x16x32_bf16 v[40:43], v[64:67], v[16:19], 0\n\t"
        "v_mfma_f32_16x16x32_bf16 v[44:47], v[68:71], v[16:19], 0\n\t"
        "ds_read_b128 v[16:19], v5 offset:10560\n\t"
        "s_waitcnt lgkmcnt(3)\n\t"
        "v_mfma_f32_16x16x32_bf16 v[48:51], v[64:67], v[20:23], 0\n\t"
        "v_mfma_f32_16x16x32_bf16 v[52:55], v[68:71], v[20:23], 0\n\t"
        "ds_read_b128 v[20:23], v5 offset:19008\n\t"
        "s_waitcnt vmcnt(12) lgkmcnt(3)\n\t"
        "v_xor_b32 v72, s73, v72\n\t"
        "v_xor_b32 v73, v6, v73\n\t"
        "v_xor_b32 v76, s73, v76\n\t"
        "v_xor_b32 v77, v6, v77\n\t"
        "v_mfma_f32_16x16x32_bf16 v[56:59], v[64:67], v[8:11], 0\n\t"
        "v_mfma_f32_16x16x32_bf16 v[60:63], v[68:71], v[8:11], 0\n\t"
        "ds_read_b128 v[8:11], v5 offset:27456\n\t"
        "ds_read_b128 v[64:67], v5 offset:35904\n\t"
        "ds_read_b128 v[68:71], v5 offset:2176\n\t"
        "s_waitcnt lgkmcnt(4)\n\t"
        "v_mfma_f32_16x16x32_bf16 v[24:27], v[72:75], v[12:15], v[24:27]\n\t"
        "v_mfma_f32_16x16x32_bf16 v[28:31], v[76:79], v[12:15], v[28:31]\n\t"
        "v_or3_b32 v7, v7, v72, v73\n\t"
        "ds_read_b128 v[12:15], v5 offset:10624\n\t"
        "v_mfma_f32_16x16x32_bf16 v[32:35], v[72:75], v[16:19], v[32:35]\n\t"
        "v_mfma_f32_16x16x32_bf16 v[36:39], v[76:79], v[16:19], v[36:39]\n\t"
        "v_or3_b32 v7, v7, v76, v77\n\t"
        "ds_read_b128 v[16:19], v5 offset:19072\n\t"
        "s_waitcnt lgkmcnt(4)\n\t"
        "v_mfma_f32_16x16x32_bf16 v[40:43], v[72:75], v[20:23], v[40:43]\n\t"
        "v_mfma_f32_16x16x32_bf16 v[44:47], v[76:79], v[20:23], v[44:47]\n\t"
        "ds_read_b128 v[20:23], v5 offset:27520\n\t"
        "v_mfma_f32_16x16x32_bf16 v[48:51], v[72:75], v[8:11], v[48:51]\n\t"
        "v_mfma_f32_16x16x32_bf16 v[52:55], v[76:79], v[8:11], v[52:55]\n\t"
        "ds_read_b128 v[8:11], v5 offset:35968\n\t"
        "s_waitcnt vmcnt(10) lgkmcnt(4)\n\t"
        "v_xor_b32 v80, s73, v80\n\t"
        "v_xor_b32 v81, v6, v81\n\t"
        "v_xor_b32 v84, s73, v84\n\t"
        "v_xor_b32 v85, v6, v85\n\t"
        "v_mfma_f32_16x16x32_bf16 v[56:59], v[72:75], v[64:67], v[56:59]\n\t"
        "v_mfma_f32_16x16x32_bf16 v[60:63], v[76:79], v[64:67], v[60:63]\n\t"
        "ds_read_b128 v[64:67], v5 offset:2240\n\t"
        "ds_read_b128 v[72:75], v5 offset:10688\n\t"
        "ds_read_b128 v[76:79], v5 offset:19136\n\t"
        "v_mfma_f32_16x16x32_bf16 v[24:27], v[80:83], v[68:71], v[24:27]\n\t"
        "v_mfma_f32_16x16x32_bf16 v[28:31], v[84:87], v[68:71], v[28:31]\n\t"
        "v_or3_b32 v7, v7, v80, v81\n\t"
        "ds_read_b128 v[68:71], v5 offset:27584\n\t"
        "s_waitcnt lgkmcnt(6)\n\t"
        "v_mfma_f32_16x16x32_bf16 v[32:35], v[80:83], v[12:15], v[32:35]\n\t"
        "v_mfma_f32_16x16x32_bf16 v[36:39], v[84:87], v[12:15], v[36:39]\n\t"
        "v_or3_b32 v7, v7, v84, v85\n\t"
        "ds_read_b128 v[12:15], v5 offset:36032\n\t"
        "v_mfma_f32_16x16x32_bf16 v[40:43], v[80:83], v[16:19], v[40:43]\n\t"
        "v_mfma_f32_16x16x32_bf16 v[44:47], v[84:87], v[16:19], v[44:47]\n\t"
        "ds_read_b128 v[16:19], v5 offset:2304\n\t"
        "s_waitcnt lgkmcnt(6)\n\t"
        "v_mfma_f32_16x16x32_bf16 v[48:51], v[80:83], v[20:23], v[48:51]\n\t"
        "v_mfma_f32_16x16x32_bf16 v[52:55], v[84:87], v[20:23], v[52:55]\n\t"
        "ds_read_b128 v[20:23], v5 offset:10752\n\t"
        "s_waitcnt vmcnt(8) lgkmcnt(6)\n\t"
        "v_xor_b32 v88, s73, v88\n\t"
        "v_xor_b32 v89, v6, v89\n\t"
        "v_xor_b32 v92, s73, v92\n\t"
        "v_xor_b32 v93, v6, v93\n\t"
        "v_mfma_f32_16x16x32_bf16 v[56:59], v[80:83], v[8:11], v[56:59]\n\t"
        "v_mfma_f32_16x16x32_bf16 v[60:63], v[84:87], v[8:11], v[60:63]\n\t"
        "ds_read_b128 v[8:11], v5 offset:19200\n\t"
        "ds_read_b128 v[80:83], v5 offset:27648\n\t"
        "v_mfma_f32_16x16x32_bf16 v[24:27], v[88:91], v[64:67], v[24:27]\n\t"
        "v_mfma_f32_16x16x32_bf16 v[28:31], v[92:95], v[64:67], v[28:31]\n\t"
        "v_or3_b32 v7, v7, v88, v89\n\t"
        "ds_read_b128 v[84:87], v5 offset:36096\n\t"
        "s_waitcnt lgkmcnt(7)\n\t"
        "v_mfma_f32_16x16x32_bf16 v[32:35], v[88:91], v[72:75], v[32:35]\n\t"
        "v_mfma_f32_16x16x32_bf16 v[36:39], v[92:95], v[72:75], v[36:39]\n\t"
        "v_or3_b32 v7, v7, v92, v93\n\t"
        "ds_read_b128 v[64:67], v5 offset:2368\n\t"
        "v_mfma_f32_16x16x32_bf16 v[40:43], v[88:91], v[76:79], v[40:43]\n\t"
        "v_mfma_f32_16x16x32_bf16 v[44:47], v[92:95], v[76:79], v[44:47]\n\t"
        "ds_read_b128 v[72:75], v5 offset:10816\n\t"
        "s_waitcnt lgkmcnt(7)\n\t"
        "v_mfma_f32_16x16x32_bf16 v[48:51], v[88:91], v[68:71], v[48:51]\n\t"
        "v_mfma_f32_16x16x32_bf16 v[52:55], v[92:95], v[68:71], v[52:55]\n\t"
        "ds_read_b128 v[76:79], v5 offset:19264\n\t"
        "s_waitcnt vmcnt(6) lgkmcnt(7)\n\t"
        "v_xor_b32 v96, s73, v96\n\t"
        "v_xor_b32 v97, v6, v97\n\t"
        "v_xor_b32 v100, s73, v100\n\t"
        "v_xor_b32 v101, v6, v101\n\t"
        "v_mfma_f32_16x16x32_bf16 v[56:59], v[88:91], v[12:15], v[56:59]\n\t"
        "v_mfma_f32_16x16x32_bf16 v[60:63], v[92:95], v[12:15], v[60:63]\n\t"
        "ds_read_b128 v[68:71], v5 offset:27712\n\t"
        "v_mfma_f32_16x16x32_bf16 v[24:27], v[96:99], v[16:19], v[24:27]\n\t"
        "v_mfma_f32_16x16x32_bf16 v[28:31], v[100:103], v[16:19], v[28:31]\n\t"
        "v_or3_b32 v7, v7, v96, v97\n\t"
        "ds_read_b128 v[12:15], v5 offset:36160\n\t"
        "s_waitcnt lgkmcnt(7)\n\t"
        "v_mfma_f32_16x16x32_bf16 v[32:35], v[96:99], v[20:23], v[32:35]\n\t"
        "v_mfma_f32_16x16x32_bf16 v[36:39], v[100:103], v[20:23], v[36:39]\n\t"
        "v_or3_b32 v7, v7, v100, v101\n\t"
        "ds_read_b128 v[88:91], v5 offset:2432\n\t"
        "v_mfma_f32_16x16x32_bf16 v[40:43], v[96:99], v[8:11], v[40:43]\n\t"
        "v_mfma_f32_16x16x32_bf16 v[44:47], v[100:103], v[8:11], v[44:47]\n\t"
        "ds_read_b128 v[92:95], v5 offset:10880\n\t"
        "s_waitcnt lgkmcnt(7)\n\t"
        "v_mfma_f32_16x16x32_bf16 v[48:51], v[96:99], v[80:83], v[48:51]\n\t"
        "v_mfma_f32_16x16x32_bf16 v[52:55], v[100:103], v[80:83], v[52:55]\n\t"
        "ds_read_b128 v[16:19], v5 offset:19328\n\t"
        "s_waitcnt vmcnt(4) lgkmcnt(7)\n\t"
        "v_xor_b32 v104, s73, v104\n\t"
        "v_xor_b32 v105, v6, v105\n\t"
        "v_xor_b32 v108, s73, v108\n\t"
        "v_xor_b32 v109, v6, v109\n\t"
        "v_mfma_f32_16x16x32_bf16 v[56:59], v[96:99], v[84:87], v[56:59]\n\t"
        "v_mfma_f32_16x16x32_bf16 v[60:63], v[100:103], v[84:87], v[60:63]\n\t"
        "ds_read_b128 v[20:23], v5 offset:27776\n\t"
        "v_mfma_f32_16x16x32_bf16 v[24:27], v[104:107], v[64:67], v[24:27]\n\t"
        "v_mfma_f32_16x16x32_bf16 v[28:31], v[108:111], v[64:67], v[28:31]\n\t"
        "v_or3_b32 v7, v7, v104, v105\n\t"
        "ds_read_b128 v[8:11], v5 offset:36224\n\t"
        "s_waitcnt lgkmcnt(7)\n\t"
        "v_mfma_f32_16x16x32_bf16 v[32:35], v[104:107], v[72:75], v[32:35]\n\t"
        "v_mfma_f32_16x16x32_bf16 v[36:39], v[108:111], v[72:75], v[36:39]\n\t"
        "v_or3_b32 v7, v7, v108, v109\n\t"
        "ds_read_b128 v[80:83], v5 offset:2496\n\t"
        "v_mfma_f32_16x16x32_bf16 v[40:43], v[104:107], v[76:79], v[40:43]\n\t"
        "v_mfma_f32_16x16x32_bf16 v[44:47], v[108:111], v[76:79], v[44:47]\n\t"
        "ds_read_b128 v[84:87], v5 offset:10944\n\t"
        "s_waitcnt lgkmcnt(7)\n\t"
        "v_mfma_f32_16x16x32_bf16 v[48:51], v[104:107], v[68:71], v[48:51]\n\t"
        "v_mfma_f32_16x16x32_bf16 v[52:55], v[108:111], v[68:71], v[52:55]\n\t"
        "ds_read_b128 v[96:99], v5 offset:19392\n\t"
        "s_waitcnt vmcnt(2) lgkmcnt(7)\n\t"
        "v_xor_b32 v112, s73, v112\n\t"
        "v_xor_b32 v113, v6, v113\n\t"
        "v_xor_b32 v116, s73, v116\n\t"
        "v_xor_b32 v117, v6, v117\n\t"
        "v_mfma_f32_16x16x32_bf16 v[56:59], v[104:107], v[12:15], v[56:59]\n\t"
        "v_mfma_f32_16x16x32_bf16 v[60:63], v[108:111], v[12:15], v[60:63]\n\t"
        "ds_read_b128 v[100:103], v5 offset:27840\n\t"
        "v_mfma_f32_16x16x32_bf16 v[24:27], v[112:115], v[88:91], v[24:27]\n\t"
        "v_mfma_f32_16x16x32_bf16 v[28:31], v[116:119], v[88:91], v[28:31]\n\t"
        "v_or3_b32 v7, v7, v112, v113\n\t"
        "ds_read_b128 v[64:67], v5 offset:36288\n\t"
        "s_waitcnt lgkmcnt(7)\n\t"
        "v_mfma_f32_16x16x32_bf16 v[32:35], v[112:115], v[92:95], v[32:35]\n\t"
        "v_mfma_f32_16x16x32_bf16 v[36:39], v[116:119], v[92:95], v[36:39]\n\t"
        "v_or3_b32 v7, v7, v116, v117\n\t"
        "v_mfma_f32_16x16x32_bf16 v[40:43], v[112:115], v[16:19], v[40:43]\n\t"
        "v_mfma_f32_16x16x32_bf16 v[44:47], v[116:119], v[16:19], v[44:47]\n\t"
        "s_waitcnt lgkmcnt(5)\n\t"
        "v_mfma_f32_16x16x32_bf16 v[48:51], v[112:115], v[20:23], v[48:51]\n\t"
        "v_mfma_f32_16x16x32_bf16 v[52:55], v[116:119], v[20:23], v[52:55]\n\t"
        "s_waitcnt vmcnt(0) lgkmcnt(4)\n\t"
        "v_xor_b32 v120, s73, v120\n\t"
        "v_xor_b32 v121, v6, v121\n\t"
        "v_xor_b32 v124, s73, v124\n\t"
        "v_xor_b32 v125, v6, v125\n\t"
        "v_mfma_f32_16x16x32_bf16 v[56:59], v[112:115], v[8:11], v[56:59]\n\t"
        "v_mfma_f32_16x16x32_bf16 v[60:63], v[116:119], v[8:11], v[60:63]\n\t"
        "v_mfma_f32_16x16x32_bf16 v[24:27], v[120:123], v[80:83], v[24:27]\n\t"
        "v_mfma_f32_16x16x32_bf16 v[28:31], v[124:127], v[80:83], v[28:31]\n\t"
        "v_or3_b32 v7, v7, v120, v121\n\t"
        "s_waitcnt lgkmcnt(3)\n\t"
        "v_mfma_f32_16x16x32_bf16 v[32:35], v[120:123], v[84:87], v[32:35]\n\t"
        "v_mfma_f32_16x16x32_bf16 v[36:39], v[124:127], v[84:87], v[36:39]\n\t"
        "v_or3_b32 v7, v7, v124, v125\n\t"
        "v_and_b32 v7, s49, v7\n\t"
        "v_cmp_ne_u32 vcc, 0, v7\n\t"
        "s_cbranch_vccnz .L7_qr\n"
        ".L7_qd:\n\t"
        "s_mov_b64 exec, 1\n\t"
        "ds_add_u32 v2, v3 offset:12\n\t"
        "s_mov_b64 exec, -1\n\t"
        "v_and_b32 v6, 15, v1\n\t"
        "v_lshrrev_b32 v7, 4, v1\n\t"
        "s_lshl_b32 s78, s80, 2\n\t"
        "v_add_u32 v7, s78, v7\n\t"
        "v_xor_b32 v7, v7, v6\n\t"
        "v_lshlrev_b32 v7, 4, v7\n\t"
        "v_lshl_add_u32 v6, v6, 8, v7\n\t"
        "s_waitcnt lgkmcnt(2)\n\t"
        "v_mfma_f32_16x16x32_bf16 v[40:43], v[120:123], v[96:99], v[40:43]\n\t"
        "v_mfma_f32_16x16x32_bf16 v[44:47], v[124:127], v[96:99], v[44:47]\n\t"
        "v_cvt_pk_bf16_f32 v24, v24, v25\n\t"
        "v_cvt_pk_bf16_f32 v25, v26, v27\n\t"
        "v_cvt_pk_bf16_f32 v26, v28, v29\n\t"
        "v_cvt_pk_bf16_f32 v27, v30, v31\n\t"
        "s_waitcnt lgkmcnt(0)\n\t"
        "v_mfma_f32_16x16x32_bf16 v[48:51], v[120:123], v[100:103], v[48:51]\n\t"
        "v_mfma_f32_16x16x32_bf16 v[52:55], v[124:127], v[100:103], v[52:55]\n\t"
        "v_cvt_pk_bf16_f32 v32, v32, v33\n\t"
        "v_cvt_pk_bf16_f32 v33, v34, v35\n\t"
        "v_cvt_pk_bf16_f32 v34, v36, v37\n\t"
        "v_cvt_pk_bf16_f32 v35, v38, v39\n\t"
        "v_mfma_f32_16x16x32_bf16 v[56:59], v[120:123], v[64:67], v[56:59]\n\t"
        "v_mfma_f32_16x16x32_bf16 v[60:63], v[124:127], v[64:67], v[60:63]\n\t"
        "v_cvt_pk_bf16_f32 v40, v40, v41\n\t"
        "v_cvt_pk_bf16_f32 v41, v42, v43\n\t"
        "v_cvt_pk_bf16_f32 v42, v44, v45\n\t"
        "v_cvt_pk_bf16_f32 v43, v46, v47\n\t"
        "ds_read_b32 v0, v2 offset:12\n\t"
        "v_cvt_pk_bf16_f32 v48, v48, v49\n\t"
        "v_cvt_pk_bf16_f32 v49, v50, v51\n\t"
        "v_cvt_pk_bf16_f32 v50, v52, v53\n\t"
        "v_cvt_pk_bf16_f32 v51, v54, v55\n\t"
        "v_cvt_pk_bf16_f32 v56, v56, v57\n\t"
        "v_cvt_pk_bf16_f32 v57, v58, v59\n\t"
        "v_cvt_pk_bf16_f32 v58, v60, v61\n\t"
        "v_cvt_pk_bf16_f32 v59, v62, v63\n\t"
        "s_waitcnt lgkmcnt(0)\n\t"
        "v_cmp_gt_u16 vcc, 4, v0\n\t"
        "s_cbranch_vccz .L7_qx\n"
        ".L7_w56:\n\t"
        "ds_read_b32 v0, v2 offset:12\n\t"
        "s_waitcnt lgkmcnt(0)\n\t"
        "v_cmp_gt_u16 vcc, 4, v0\n\t"
        "s_cbranch_vccz .L7_d57\n\t"
        "s_sleep 0\n\t"
        "s_sub_i32 s83, s83, 1\n\t"
        "s_cmp_gt_i32 s83, 0\n\t"
        "s_cbranch_scc1 .L7_w56\n"
        ".L7_d57:\n"
        ".L7_qx:\n\t"
        "ds_write_b128 v6, v[24:27] offset:2048\n\t"
        "ds_write_b128 v6, v[32:35] offset:6144\n\t"
        "ds_write_b128 v6, v[40:43] offset:10240\n\t"
        "ds_write_b128 v6, v[48:51] offset:14336\n\t"
        "ds_write_b128 v6, v[56:59] offset:18432\n\t"
        "s_mov_b64 exec, 1\n\t"
        "ds_add_u32 v2, v3 offset:16\n\t"
        "s_mov_b64 exec, -1\n\t"
        "v_lshrrev_b32 v10, 3, v1\n\t"
        "v_lshlrev_b32 v10, 9, v10\n\t"
        "v_and_b32 v11, 7, v1\n\t"
        "v_lshl_add_u32 v10, v11, 4, v10\n\t"
        "v_and_b32 v11, 16, v1\n\t"
        "v_lshlrev_b32 v11, 2, v11\n\t"
        "v_xor_b32 v10, v10, v11\n\t"
        "s_load_dwordx4 s[24:27], %2, 32\n\t"
        "s_lshl_b32 s78, s80, 14\n\t"
        "s_add_u32 m0, s78, 83968\n\t"
        "s_lshl_b32 s78, s70, 9\n\t"
        "s_add_u32 s78, s78, s80\n\t"
        "s_lshl_b32 s78, s78, 7\n"
        ".L7_w58:\n\t"
        "ds_read_b32 v0, v2 offset:24\n\t"
        "s_waitcnt lgkmcnt(0)\n\t"
        "v_cmp_gt_u16 vcc, 8, v0\n\t"
        "s_cbranch_vccz .L7_d59\n\t"
        "s_sleep 1\n\t"
        "s_sub_i32 s83, s83, 1\n\t"
        "s_cmp_gt_i32 s83, 0\n\t"
        "s_cbranch_scc1 .L7_w58\n"
        ".L7_d59:\n\t"
        "s_setprio 3\n\t"
        "buffer_load_dwordx4 v10, s[24:27], s78 offen lds\n\t"
        "s_add_u32 m0, m0, 0x400\n\t"
        "s_add_u32 s78, s78, 0x1000\n\t"
        "buffer_load_dwordx4 v10, s[24:27], s78 offen lds\n\t"
        "s_add_u32 m0, m0, 0x400\n\t"
        "s_add_u32 s78, s78, 0x1000\n\t"
        "buffer_load_dwordx4 v10, s[24:27], s78 offen lds\n\t"
        "s_add_u32 m0, m0, 0x400\n\t"
        "s_add_u32 s78, s78, 0x1000\n\t"
        "buffer_load_dwordx4 v10, s[24:27], s78 offen lds\n\t"
        "s_add_u32 m0, m0, 0x400\n\t"
        "s_add_u32 s78, s78, 0x1000\n\t"
        "buffer_load_dwordx4 v10, s[24:27], s78 offen lds\n\t"
        "s_add_u32 m0, m0, 0x400\n\t"
        "s_add_u32 s78, s78, 0x1000\n\t"
        "buffer_load_dwordx4 v10, s[24:27], s78 offen lds\n\t"
        "s_add_u32 m0, m0, 0x400\n\t"
        "s_add_u32 s78, s78, 0x1000\n\t"
        "buffer_load_dwordx4 v10, s[24:27], s78 offen lds\n\t"
        "s_add_u32 m0, m0, 0x400\n\t"
        "s_add_u32 s78, s78, 0x1000\n\t"
        "buffer_load_dwordx4 v10, s[24:27], s78 offen lds\n\t"
        "s_add_u32 m0, m0, 0x400\n\t"
        "s_add_u32 s78, s78, 0x1000\n\t"
        "buffer_load_dwordx4 v10, s[24:27], s78 offen lds\n\t"
        "s_add_u32 m0, m0, 0x400\n\t"
        "s_add_u32 s78, s78, 0x1000\n\t"
        "buffer_load_dwordx4 v10, s[24:27], s78 offen lds\n\t"
        "s_add_u32 m0, m0, 0x400\n\t"
        "s_add_u32 s78, s78, 0x1000\n\t"
        "buffer_load_dwordx4 v10, s[24:27], s78 offen lds\n\t"
        "s_add_u32 m0, m0, 0x400\n\t"
        "s_add_u32 s78, s78, 0x1000\n\t"
        "buffer_load_dwordx4 v10, s[24:27], s78 offen lds\n\t"
        "s_add_u32 m0, m0, 0x400\n\t"
        "s_add_u32 s78, s78, 0x1000\n\t"
        "buffer_load_dwordx4 v10, s[24:27], s78 offen lds\n\t"
        "s_add_u32 m0, m0, 0x400\n\t"
        "s_add_u32 s78, s78, 0x1000\n\t"
        "buffer_load_dwordx4 v10, s[24:27], s78 offen lds\n\t"
        "s_add_u32 m0, m0, 0x400\n\t"
        "s_add_u32 s78, s78, 0x1000\n\t"
        "buffer_load_dwordx4 v10, s[24:27], s78 offen lds\n\t"
        "s_add_u32 m0, m0, 0x400\n\t"
        "s_add_u32 s78, s78, 0x1000\n\t"
        "buffer_load_dwordx4 v10, s[24:27], s78 offen lds\n\t"
        "s_waitcnt vmcnt(0)\n\t"
        "s_mov_b64 exec, 1\n\t"
        "ds_add_u32 v2, v3 offset:28\n\t"
        "s_mov_b64 exec, -1\n\t"
        "s_setprio 0\n"
        ".L7_w60:\n\t"
        "ds_read_b32 v0, v2 offset:36\n\t"
        "s_waitcnt lgkmcnt(0)\n\t"
        "v_cmp_gt_u16 vcc, 4, v0\n\t"
        "s_cbranch_vccz .L7_d61\n\t"
        "s_sleep 2\n\t"
        "s_sub_i32 s83, s83, 1\n\t"
        "s_cmp_gt_i32 s83, 0\n\t"
        "s_cbranch_scc1 .L7_w60\n"
        ".L7_d61:\n\t"
        "v_cmp_lt_u32 vcc, 0xfffff, v0\n\t"
        "s_cbranch_vccnz .L7_exit\n\t"
        "s_endpgm\n"
        ".L7_G2:\n\t"
        "s_barrier\n\t"
        "s_setprio 1\n\t"
        "v_lshlrev_b32 v5, 4, v1\n\t"
        "s_mul_i32 s82, s76, 13\n\t"
        "s_lshr_b32 s82, s82, 6\n\t"
        "s_mul_i32 s82, s82, 5\n\t"
        "s_sub_u32 s82, s76, s82\n\t"
        "s_lshl2_add_u32 s78, s82, s69\n\t"
        "s_lshl_b32 s78, s78, 10\n\t"
        "s_add_u32 m0, s78, 45056\n\t"
        "s_add_u32 s82, s82, 1\n\t"
        "s_cmp_eq_u32 s82, 5\n\t"
        "buffer_load_dwordx4 v5, s[56:59], s78 offen lds\n\t"
        "s_cselect_b32 s82, 0, s82\n\t"
        "v_lshrrev_b32 v18, 3, v1\n\t"
        "v_lshlrev_b32 v18, 14, v18\n\t"
        "v_and_b32 v6, 0x70, v5\n\t"
        "s_lshl_b32 s78, s69, 19\n\t"
        "s_lshl_b32 s79, s71, 7\n\t"
        "s_add_u32 s78, s78, s79\n\t"
        "v_add3_u32 v18, v18, v6, s78\n\t"
        "s_mov_b32 s24, 1048576\n\t"
        "s_mov_b32 s25, 1179648\n\t"
        "s_mov_b32 s26, 1310720\n\t"
        "s_mov_b32 s27, 1441792\n\t"
        "v_and_b32 v6, 0xf0, v5\n\t"
        "s_lshl_b32 s78, s70, 8\n\t"
        "v_add_u32 v14, s78, v6\n\t"
        "v_mov_b32 v7, 200\n\t"
        "s_lshl_b32 s33, s69, 3\n\t"
        "s_lshl_b32 s78, 1, s33\n\t"
        "v_mov_b32 v29, s78\n\t"
        "s_sub_u32 s32, s78, 1\n\t"
        "s_and_b32 s32, s32, 0x01010101\n\t"
        "s_mov_b32 s34, 0xfffe\n\t"
        "s_mov_b32 s35, 0xffffff\n\t"
        "s_cmp_lt_u32 s69, 2\n\t"
        "s_mov_b32 s78, 0x12080402\n\t"
        "s_cselect_b32 s40, 0x100e0a06, s78\n\t"
        "s_mov_b32 s78, 0x16161616\n\t"
        "s_cselect_b32 s41, 0x14141414, s78\n\t"
        "s_cselect_b32 s78, 4, 0\n\t"
        "v_mov_b32 v61, s78\n\t"
        "s_and_b32 s78, s69, 1\n\t"
        "s_mul_i32 s78, s78, 0x01010101\n\t"
        "s_add_u32 s40, s40, s78\n\t"
        "s_add_u32 s41, s41, s78\n\t"
        "v_lshrrev_b32 v12, 4, v1\n\t"
        "s_bfe_u32 s78, s40, 0x80000\n\t"
        "s_lshl_b32 s78, s78, 2\n\t"
        "v_add_u32 v13, s78, v12\n\t"
        "v_lshlrev_b32 v24, 2, v13\n\t"
        "v_lshlrev_b32 v15, 4, v13\n\t"
        "v_and_b32 v15, 0xf0, v15\n\t"
        "v_xor_b32 v15, v15, v6\n\t"
        "v_lshl_add_u32 v56, v13, 8, v15\n\t"
        "s_bfe_u32 s78, s40, 0x80008\n\t"
        "s_lshl_b32 s78, s78, 2\n\t"
        "v_add_u32 v13, s78, v12\n\t"
        "v_lshlrev_b32 v25, 2, v13\n\t"
        "v_lshlrev_b32 v15, 4, v13\n\t"
        "v_and_b32 v15, 0xf0, v15\n\t"
        "v_xor_b32 v15, v15, v6\n\t"
        "v_lshl_add_u32 v57, v13, 8, v15\n\t"
        "s_bfe_u32 s78, s40, 0x80010\n\t"
        "s_lshl_b32 s78, s78, 2\n\t"
        "v_add_u32 v13, s78, v12\n\t"
        "v_lshlrev_b32 v26, 2, v13\n\t"
        "v_lshlrev_b32 v15, 4, v13\n\t"
        "v_and_b32 v15, 0xf0, v15\n\t"
        "v_xor_b32 v15, v15, v6\n\t"
        "v_lshl_add_u32 v58, v13, 8, v15\n\t"
        "s_bfe_u32 s78, s40, 0x80018\n\t"
        "s_lshl_b32 s78, s78, 2\n\t"
        "v_add_u32 v13, s78, v12\n\t"
        "v_lshlrev_b32 v27, 2, v13\n\t"
        "v_lshlrev_b32 v15, 4, v13\n\t"
        "v_and_b32 v15, 0xf0, v15\n\t"
        "v_xor_b32 v15, v15, v6\n\t"
        "v_lshl_add_u32 v59, v13, 8, v15\n\t"
        "s_bfe_u32 s78, s41, 0x80000\n\t"
        "s_lshl_b32 s78, s78, 2\n\t"
        "v_add_u32 v13, s78, v12\n\t"
        "v_lshlrev_b32 v28, 2, v13\n\t"
        "v_lshlrev_b32 v15, 4, v13\n\t"
        "v_and_b32 v15, 0xf0, v15\n\t"
        "v_xor_b32 v15, v15, v6\n\t"
        "v_lshl_add_u32 v60, v13, 8, v15\n\t"
        "v_and_b32 v20, 28, v1\n\t"
        "v_lshlrev_b32 v20, 1, v20\n\t"
        "v_lshrrev_b64 v[12:13], v20, s[40:41]\n\t"
        "v_and_b32 v13, 0xff, v12\n\t"
        "v_and_b32 v12, 3, v1\n\t"
        "v_lshl_add_u32 v13, v13, 2, v12\n\t"
        "v_lshlrev_b32 v31, 2, v13\n\t"
        "s_and_b32 s78, s67, 0x10000\n\t"
        "s_or_b32 s78, s78, 1\n\t"
        "v_mov_b32 v30, s78\n\t"
        "s_mov_b32 s36, 0\n\t"
        "v_and_b32 v0, 48, v5\n\t"
        "s_mov_b32 s78, 0x0d0c\n\t"
        "s_mov_b32 s79, 0\n\t"
        "v_lshlrev_b64 v[8:9], v0, s[78:79]\n\t"
        "s_mov_b32 s78, 0x0f0e\n\t"
        "v_lshlrev_b64 v[10:11], v0, s[78:79]\n\t"
        "v_xor_b32 v8, 0x0c0c0c0c, v8\n\t"
        "v_xor_b32 v9, 0x0c0c0c0c, v9\n\t"
        "v_xor_b32 v10, 0x0c0c0c0c, v10\n\t"
        "v_xor_b32 v11, 0x0c0c0c0c, v11\n\t"
        "s_movk_i32 s48, 0x200\n\t"
        "s_mov_b32 s37, 0x01010101\n\t"
        "s_mov_b32 s38, 0x07060302\n\t"
        "s_mov_b32 s49, 0x05040100\n\t"
        "s_getpc_b64 s[50:51]\n"
        ".L7_pc:\n\t"
        "s_add_u32 s52, s50, .L7_ce-.L7_pc\n\t"
        "s_addc_u32 s53, s51, 0\n\t"
        "s_add_u32 s50, s50, .L7_ie-.L7_pc\n\t"
        "s_addc_u32 s51, s51, 0\n\t"
        "s_cmp_eq_u32 s80, 10\n\t"
        "s_cselect_b32 s80, 0, s80\n\t"
        "s_mul_i32 s78, s80, 0x1080\n\t"
        "s_add_u32 m0, s78, s81\n\t"
        "s_lshr_b32 s78, s80, 1\n\t"
        "s_lshl1_add_u32 s78, s78, s80\n\t"
        "s_lshl_b32 s78, s78, 11\n\t"
        "buffer_load_dwordx4 v4, s[16:19], s78 offen lds\n\t"
        "s_add_u32 s80, s80, 1\n\t"
        "s_mul_i32 s78, s80, 0x1080\n\t"
        "s_add_u32 m0, s78, s81\n\t"
        "s_lshr_b32 s78, s80, 1\n\t"
        "s_lshl1_add_u32 s78, s78, s80\n\t"
        "s_lshl_b32 s78, s78, 11\n\t"
        "buffer_load_dwordx4 v4, s[16:19], s78 offen lds\n\t"
        "s_add_u32 s80, s80, 1\n\t"
        "s_mul_i32 s78, s80, 0x1080\n\t"
        "s_add_u32 m0, s78, s81\n\t"
        "s_lshr_b32 s78, s80, 1\n\t"
        "s_lshl1_add_u32 s78, s78, s80\n\t"
        "s_lshl_b32 s78, s78, 11\n\t"
        "buffer_load_dwordx4 v4, s[16:19], s78 offen lds\n\t"
        "s_add_u32 s80, s80, 1\n\t"
        "s_mul_i32 s78, s80, 0x1080\n\t"
        "s_add_u32 m0, s78, s81\n\t"
        "s_lshr_b32 s78, s80, 1\n\t"
        "s_lshl1_add_u32 s78, s78, s80\n\t"
        "s_lshl_b32 s78, s78, 11\n\t"
        "buffer_load_dwordx4 v4, s[16:19], s78 offen lds\n\t"
        "s_add_u32 s80, s80, 1\n\t"
        "s_mul_i32 s78, s80, 0x1080\n\t"
        "s_add_u32 m0, s78, s81\n\t"
        "s_lshr_b32 s78, s80, 1\n\t"
        "s_lshl1_add_u32 s78, s78, s80\n\t"
        "s_lshl_b32 s78, s78, 11\n\t"
        "buffer_load_dwordx4 v4, s[16:19], s78 offen lds\n\t"
        "s_add_u32 s80, s80, 1\n\t"
        "s_lshl2_add_u32 s78, s82, s69\n\t"
        "s_lshl_b32 s78, s78, 10\n\t"
        "s_add_u32 m0, s78, 45056\n\t"
        "s_add_u32 s82, s82, 1\n\t"
        "s_cmp_eq_u32 s82, 5\n\t"
        "buffer_load_dwordx4 v5, s[56:59], s78 offen lds\n\t"
        "s_cselect_b32 s82, 0, s82\n\t"
        "s_lshl2_add_u32 s78, s82, s69\n\t"
        "s_lshl_b32 s78, s78, 10\n\t"
        "s_add_u32 m0, s78, 45056\n\t"
        "s_add_u32 s82, s82, 1\n\t"
        "s_cmp_eq_u32 s82, 5\n\t"
        "buffer_load_dwordx4 v5, s[56:59], s78 offen lds\n\t"
        "s_cselect_b32 s82, 0, s82\n\t"
        "s_lshl2_add_u32 s78, s82, s69\n\t"
        "s_lshl_b32 s78, s78, 10\n\t"
        "s_add_u32 m0, s78, 45056\n\t"
        "s_add_u32 s82, s82, 1\n\t"
        "s_cmp_eq_u32 s82, 5\n\t"
        "buffer_load_dwordx4 v5, s[56:59], s78 offen lds\n\t"
        "s_cselect_b32 s82, 0, s82\n\t"
        "s_lshl2_add_u32 s78, s82, s69\n\t"
        "s_lshl_b32 s78, s78, 10\n\t"
        "s_add_u32 m0, s78, 45056\n\t"
        "s_add_u32 s82, s82, 1\n\t"
        "s_cmp_eq_u32 s82, 5\n\t"
        "buffer_load_dwordx4 v5, s[56:59], s78 offen lds\n\t"
        "s_cselect_b32 s82, 0, s82\n\t"
        "s_mov_b64 exec, 1\n\t"
        "ds_add_u32 v2, v3 offset:80\n\t"
        "s_mov_b64 exec, -1\n\t"
        "s_waitcnt vmcnt(4)\n\t"
        "s_mov_b64 exec, 1\n\t"
        "ds_add_u32 v2, v3 offset:0\n\t"
        "s_mov_b64 exec, -1\n"
        ".L7_w62:\n\t"
        "ds_read_b64 v[12:13], v2 offset:72\n\t"
        "s_waitcnt lgkmcnt(0)\n\t"
        "v_cmp_gt_u16 vcc, 1, v12\n\t"
        "s_cbranch_vccz .L7_d63\n\t"
        "s_sleep 1\n\t"
        "s_sub_i32 s83, s83, 1\n\t"
        "s_cmp_gt_i32 s83, 0\n\t"
        "s_cbranch_scc1 .L7_w62\n"
        ".L7_d63:\n\t"
        "v_readfirstlane_b32 s68, v13\n"
        ".L7_w64:\n\t"
        "ds_read_b32 v0, v2 offset:8\n\t"
        "s_waitcnt lgkmcnt(0)\n\t"
        "v_cmp_gt_u16 vcc, 2, v0\n\t"
        "s_cbranch_vccz .L7_d65\n\t"
        "s_sleep 1\n\t"
        "s_sub_i32 s83, s83, 1\n\t"
        "s_cmp_gt_i32 s83, 0\n\t"
        "s_cbranch_scc1 .L7_w64\n"
        ".L7_d65:\n\t"
        "ds_read_b32 v5, v31 offset:512\n\t"
        "ds_read_b32 v24, v24 offset:512\n\t"
        "ds_read_b32 v25, v25 offset:512\n\t"
        "ds_read_b32 v26, v26 offset:512\n\t"
        "ds_read_b32 v27, v27 offset:512\n\t"
        "ds_read_b32 v28, v28 offset:512\n\t"
        "s_waitcnt lgkmcnt(0)\n\t"
        "v_mul_u32_u24_sdwa v31, v7, v5 dst_sel:DWORD dst_unused:UNUSED_PAD src0_sel:DWORD src1_sel:BYTE_0\n\t"
        "s_waitcnt vmcnt(0)\n\t"
        "s_mov_b64 exec, 1\n\t"
        "ds_add_u32 v2, v3 offset:4\n\t"
        "s_mov_b64 exec, -1\n\t"
        "s_setprio 3\n\t"
        "s_lshl_b32 s44, s68, 24\n\t"
        "s_xor_b32 s45, s44, 0x5a000000\n\t"
        "s_mul_i32 s46, s68, 0x01010000\n\t"
        "s_mul_i32 s47, s68, 0x02000200\n"
        ".L7_g66:\n\t"
        "s_memrealtime s[78:79]\n\t"
        "s_waitcnt lgkmcnt(0)\n\t"
        "s_sub_u32 s78, s78, s74\n\t"
        "s_cmp_ge_u32 s78, 350\n\t"
        "s_cbranch_scc1 .L7_h67\n\t"
        "s_sleep 1\n\t"
        "s_branch .L7_g66\n"
        ".L7_h67:\n\t"
        "s_branch .L7_ll\n"
        ".L7_again:\n\t"
        "s_waitcnt lgkmcnt(0)\n\t"
        "s_sleep 8\n\t"
        "s_sub_i32 s83, s83, 16\n\t"
        "s_cmp_gt_i32 s83, 0\n\t"
        "s_cbranch_scc0 .L7_got\n"
        ".L7_ll:\n\t"
        "buffer_load_dwordx4 v[64:67], v18, s[20:23], s24 offen sc1\n\t"
        "buffer_load_dwordx4 v[68:71], v18, s[20:23], s25 offen sc1\n\t"
        "buffer_load_dwordx4 v[72:75], v18, s[20:23], s26 offen sc1\n\t"
        "buffer_load_dwordx4 v[76:79], v18, s[20:23], s27 offen sc1\n"
        ".L7_w68:\n\t"
        "ds_read_b32 v0, v2 offset:4\n\t"
        "s_waitcnt lgkmcnt(0)\n\t"
        "v_cmp_gt_u16 vcc, 4, v0\n\t"
        "s_cbranch_vccz .L7_d69\n\t"
        "s_sleep 1\n\t"
        "s_sub_i32 s83, s83, 1\n\t"
        "s_cmp_gt_i32 s83, 0\n\t"
        "s_cbranch_scc1 .L7_w68\n"
        ".L7_d69:\n\t"
        "s_waitcnt vmcnt(3)\n\t"
        "v_xor_b32 v5, s44, v64\n\t"
        "v_bitop3_b32 v5, v65, v5, s45 bitop3:0xde\n\t"
        "v_bitop3_b32 v5, v66, v5, s46 bitop3:0xde\n\t"
        "v_xor_b32 v4, s47, v67\n\t"
        "v_lshlrev_b32_sdwa v112, 1, v64 dst_sel:DWORD dst_unused:UNUSED_PAD src0_sel:DWORD src1_sel:WORD_0\n\t"
        "ds_read_u16 v112, v112 offset:45056\n\t"
        "v_lshlrev_b32_sdwa v113, 1, v65 dst_sel:DWORD dst_unused:UNUSED_PAD src0_sel:DWORD src1_sel:WORD_0\n\t"
        "ds_read_u16 v113, v113 offset:45056\n\t"
        "v_lshlrev_b32_sdwa v114, 1, v66 dst_sel:DWORD dst_unused:UNUSED_PAD src0_sel:DWORD src1_sel:WORD_0\n\t"
        "ds_read_u16 v114, v114 offset:45056\n\t"
        "v_mul_u32_u24_sdwa v80, v7, v64 dst_sel:DWORD dst_unused:UNUSED_PAD src0_sel:DWORD src1_sel:BYTE_2\n\t"
        "v_mul_u32_u24_sdwa v81, v7, v65 dst_sel:DWORD dst_unused:UNUSED_PAD src0_sel:DWORD src1_sel:BYTE_2\n\t"
        "v_mul_u32_u24_sdwa v82, v7, v66 dst_sel:DWORD dst_unused:UNUSED_PAD src0_sel:DWORD src1_sel:BYTE_2\n\t"
        "s_waitcnt vmcnt(2)\n\t"
        "v_bitop3_b32 v5, v68, v5, s44 bitop3:0xde\n\t"
        "v_bitop3_b32 v5, v69, v5, s45 bitop3:0xde\n\t"
        "v_bitop3_b32 v5, v70, v5, s46 bitop3:0xde\n\t"
        "v_bitop3_b32 v4, v71, v4, s47 bitop3:0xde\n\t"
        "v_lshlrev_b32_sdwa v116, 1, v68 dst_sel:DWORD dst_unused:UNUSED_PAD src0_sel:DWORD src1_sel:WORD_0\n\t"
        "ds_read_u16 v116, v116 offset:45056\n\t"
        "v_lshlrev_b32_sdwa v117, 1, v69 dst_sel:DWORD dst_unused:UNUSED_PAD src0_sel:DWORD src1_sel:WORD_0\n\t"
        "ds_read_u16 v117, v117 offset:45056\n\t"
        "v_lshlrev_b32_sdwa v118, 1, v70 dst_sel:DWORD dst_unused:UNUSED_PAD src0_sel:DWORD src1_sel:WORD_0\n\t"
        "ds_read_u16 v118, v118 offset:45056\n\t"
        "v_mul_u32_u24_sdwa v84, v7, v68 dst_sel:DWORD dst_unused:UNUSED_PAD src0_sel:DWORD src1_sel:BYTE_2\n\t"
        "v_mul_u32_u24_sdwa v85, v7, v69 dst_sel:DWORD dst_unused:UNUSED_PAD src0_sel:DWORD src1_sel:BYTE_2\n\t"
        "v_mul_u32_u24_sdwa v86, v7, v70 dst_sel:DWORD dst_unused:UNUSED_PAD src0_sel:DWORD src1_sel:BYTE_2\n\t"
        "s_waitcnt vmcnt(1)\n\t"
        "v_bitop3_b32 v5, v72, v5, s44 bitop3:0xde\n\t"
        "v_bitop3_b32 v5, v73, v5, s45 bitop3:0xde\n\t"
        "v_bitop3_b32 v5, v74, v5, s46 bitop3:0xde\n\t"
        "v_bitop3_b32 v4, v75, v4, s47 bitop3:0xde\n\t"
        "v_lshlrev_b32_sdwa v120, 1, v72 dst_sel:DWORD dst_unused:UNUSED_PAD src0_sel:DWORD src1_sel:WORD_0\n\t"
        "ds_read_u16 v120, v120 offset:45056\n\t"
        "v_lshlrev_b32_sdwa v121, 1, v73 dst_sel:DWORD dst_unused:UNUSED_PAD src0_sel:DWORD src1_sel:WORD_0\n\t"
        "ds_read_u16 v121, v121 offset:45056\n\t"
        "v_lshlrev_b32_sdwa v122, 1, v74 dst_sel:DWORD dst_unused:UNUSED_PAD src0_sel:DWORD src1_sel:WORD_0\n\t"
        "ds_read_u16 v122, v122 offset:45056\n\t"
        "v_mul_u32_u24_sdwa v88, v7, v72 dst_sel:DWORD dst_unused:UNUSED_PAD src0_sel:DWORD src1_sel:BYTE_2\n\t"
        "v_mul_u32_u24_sdwa v89, v7, v73 dst_sel:DWORD dst_unused:UNUSED_PAD src0_sel:DWORD src1_sel:BYTE_2\n\t"
        "v_mul_u32_u24_sdwa v90, v7, v74 dst_sel:DWORD dst_unused:UNUSED_PAD src0_sel:DWORD src1_sel:BYTE_2\n\t"
        "s_waitcnt vmcnt(0)\n\t"
        "v_bitop3_b32 v5, v76, v5, s44 bitop3:0xde\n\t"
        "v_bitop3_b32 v5, v77, v5, s45 bitop3:0xde\n\t"
        "v_bitop3_b32 v5, v78, v5, s46 bitop3:0xde\n\t"
        "v_bitop3_b32 v4, v79, v4, s47 bitop3:0xde\n\t"
        "v_lshlrev_b32_sdwa v124, 1, v76 dst_sel:DWORD dst_unused:UNUSED_PAD src0_sel:DWORD src1_sel:WORD_0\n\t"
        "ds_read_u16 v124, v124 offset:45056\n\t"
        "v_lshlrev_b32_sdwa v125, 1, v77 dst_sel:DWORD dst_unused:UNUSED_PAD src0_sel:DWORD src1_sel:WORD_0\n\t"
        "ds_read_u16 v125, v125 offset:45056\n\t"
        "v_lshlrev_b32_sdwa v126, 1, v78 dst_sel:DWORD dst_unused:UNUSED_PAD src0_sel:DWORD src1_sel:WORD_0\n\t"
        "ds_read_u16 v126, v126 offset:45056\n\t"
        "v_mul_u32_u24_sdwa v92, v7, v76 dst_sel:DWORD dst_unused:UNUSED_PAD src0_sel:DWORD src1_sel:BYTE_2\n\t"
        "v_mul_u32_u24_sdwa v93, v7, v77 dst_sel:DWORD dst_unused:UNUSED_PAD src0_sel:DWORD src1_sel:BYTE_2\n\t"
        "v_mul_u32_u24_sdwa v94, v7, v78 dst_sel:DWORD dst_unused:UNUSED_PAD src0_sel:DWORD src1_sel:BYTE_2\n\t"
        "v_and_b32 v6, 0xfe000000, v4\n\t"
        "v_or_b32 v5, v5, v6\n\t"
        "v_cmp_lt_u32 vcc, s35, v5\n\t"
        "s_cbranch_vccnz .L7_again\n"
        ".L7_got:\n\t"
        "v_and_b32 v6, v67, v71\n\t"
        "v_bitop3_b32 v6, v6, v75, v79 bitop3:0x80\n\t"
        "s_waitcnt lgkmcnt(2)\n\t"
        "v_cmpx_gt_u16 vcc, s34, v64\n\t"
        "ds_add_rtn_u32 v96, v80, v29 offset:32576\n\t"
        "v_cmpx_gt_u16 vcc, s34, v65\n\t"
        "ds_add_rtn_u32 v97, v81, v29 offset:32576\n\t"
        "v_cmpx_gt_u16 vcc, s34, v66\n\t"
        "ds_add_rtn_u32 v98, v82, v29 offset:32576\n\t"
        "s_mov_b64 exec, -1\n\t"
        "v_cmpx_gt_u16 vcc, s34, v68\n\t"
        "ds_add_rtn_u32 v100, v84, v29 offset:32576\n\t"
        "v_cmpx_gt_u16 vcc, s34, v69\n\t"
        "ds_add_rtn_u32 v101, v85, v29 offset:32576\n\t"
        "v_cmpx_gt_u16 vcc, s34, v70\n\t"
        "ds_add_rtn_u32 v102, v86, v29 offset:32576\n\t"
        "s_mov_b64 exec, -1\n\t"
        "v_cmpx_gt_u16 vcc, s34, v72\n\t"
        "ds_add_rtn_u32 v104, v88, v29 offset:32576\n\t"
        "v_cmpx_gt_u16 vcc, s34, v73\n\t"
        "ds_add_rtn_u32 v105, v89, v29 offset:32576\n\t"
        "v_cmpx_gt_u16 vcc, s34, v74\n\t"
        "ds_add_rtn_u32 v106, v90, v29 offset:32576\n\t"
        "s_mov_b64 exec, -1\n\t"
        "v_cmpx_gt_u16 vcc, s34, v76\n\t"
        "ds_add_rtn_u32 v108, v92, v29 offset:32576\n\t"
        "v_cmpx_gt_u16 vcc, s34, v77\n\t"
        "ds_add_rtn_u32 v109, v93, v29 offset:32576\n\t"
        "v_cmpx_gt_u16 vcc, s34, v78\n\t"
        "ds_add_rtn_u32 v110, v94, v29 offset:32576\n\t"
        "s_mov_b64 exec, -1\n\t"
        "v_cmp_ne_u16 vcc, -1, v6\n\t"
        "s_cbranch_vccnz .L7_x\n"
        ".L7_y:\n\t"
        "s_mov_b64 exec, 1\n\t"
        "ds_add_u32 v2, v3 offset:32\n\t"
        "s_mov_b64 exec, -1\n\t"
        "v_perm_b32 v112, v112, v64, s49\n\t"
        "v_perm_b32 v113, v113, v65, s49\n\t"
        "v_perm_b32 v114, v114, v66, s49\n\t"
        "v_perm_b32 v116, v116, v68, s49\n\t"
        "v_perm_b32 v117, v117, v69, s49\n\t"
        "v_perm_b32 v118, v118, v70, s49\n\t"
        "v_perm_b32 v120, v120, v72, s49\n\t"
        "v_perm_b32 v121, v121, v73, s49\n\t"
        "v_perm_b32 v122, v122, v74, s49\n\t"
        "v_perm_b32 v124, v124, v76, s49\n\t"
        "v_cmp_lt_u32 vcc, s35, v4\n\t"
        "s_cmp_lg_u64 vcc, 0\n\t"
        "s_cselect_b32 s78, 0x100000, 0\n\t"
        "v_or_b32 v30, s78, v30\n\t"
        "s_waitcnt lgkmcnt(1)\n\t"
        "v_perm_b32 v125, v125, v77, s49\n\t"
        "v_perm_b32 v126, v126, v78, s49\n\t"
        "v_bfe_u32 v96, v96, s33, 8\n\t"
        "v_bfe_u32 v97, v97, s33, 8\n\t"
        "v_bfe_u32 v98, v98, s33, 8\n\t"
        "v_bfe_u32 v100, v100, s33, 8\n\t"
        "v_bfe_u32 v101, v101, s33, 8\n\t"
        "v_bfe_u32 v102, v102, s33, 8\n\t"
        "v_bfe_u32 v104, v104, s33, 8\n\t"
        "v_bfe_u32 v105, v105, s33, 8\n\t"
        "v_bfe_u32 v106, v106, s33, 8\n\t"
        "v_bfe_u32 v108, v108, s33, 8\n\t"
        "v_bfe_u32 v109, v109, s33, 8\n\t"
        "v_bfe_u32 v110, v110, s33, 8\n"
        ".L7_w70:\n\t"
        "ds_read_b32 v0, v2 offset:32\n\t"
        "ds_read_b32 v32, v80 offset:32576\n\t"
        "ds_read_b32 v33, v81 offset:32576\n\t"
        "ds_read_b32 v34, v82 offset:32576\n\t"
        "ds_read_b32 v35, v84 offset:32576\n\t"
        "ds_read_b32 v36, v85 offset:32576\n\t"
        "ds_read_b32 v37, v86 offset:32576\n\t"
        "ds_read_b32 v38, v88 offset:32576\n\t"
        "ds_read_b32 v39, v89 offset:32576\n\t"
        "ds_read_b32 v40, v90 offset:32576\n\t"
        "ds_read_b32 v41, v92 offset:32576\n\t"
        "ds_read_b32 v42, v93 offset:32576\n\t"
        "ds_read_b32 v43, v94 offset:32576\n\t"
        "ds_read_b32 v6, v31 offset:32576\n\t"
        "s_waitcnt lgkmcnt(13)\n\t"
        "v_cmp_gt_u16 vcc, 4, v0\n\t"
        "s_cbranch_vccz .L7_d71\n\t"
        "s_waitcnt lgkmcnt(0)\n\t"
        "s_sleep 0\n\t"
        "s_sub_i32 s83, s83, 1\n\t"
        "s_cmp_gt_i32 s83, 0\n\t"
        "s_cbranch_scc1 .L7_w70\n"
        ".L7_d71:\n\t"
        "s_waitcnt lgkmcnt(10)\n\t"
        "v_dot4_u32_u8 v96, v32, s32, v96\n\t"
        "v_dot4_u32_u8 v97, v33, s32, v97\n\t"
        "v_dot4_u32_u8 v98, v34, s32, v98\n\t"
        "s_waitcnt lgkmcnt(7)\n\t"
        "v_dot4_u32_u8 v100, v35, s32, v100\n\t"
        "v_dot4_u32_u8 v101, v36, s32, v101\n\t"
        "v_dot4_u32_u8 v102, v37, s32, v102\n\t"
        "s_waitcnt lgkmcnt(4)\n\t"
        "v_dot4_u32_u8 v104, v38, s32, v104\n\t"
        "v_dot4_u32_u8 v105, v39, s32, v105\n\t"
        "v_dot4_u32_u8 v106, v40, s32, v106\n\t"
        "s_waitcnt lgkmcnt(1)\n\t"
        "v_dot4_u32_u8 v108, v41, s32, v108\n\t"
        "v_dot4_u32_u8 v109, v42, s32, v109\n\t"
        "v_dot4_u32_u8 v110, v43, s32, v110\n\t"
        "v_min_u32 v96, 47, v96\n\t"
        "v_min_u32 v97, 47, v97\n\t"
        "v_min_u32 v98, 47, v98\n\t"
        "v_min_u32 v100, 47, v100\n\t"
        "v_min_u32 v101, 47, v101\n\t"
        "v_min_u32 v102, 47, v102\n\t"
        "v_min_u32 v104, 47, v104\n\t"
        "v_min_u32 v105, 47, v105\n\t"
        "v_min_u32 v106, 47, v106\n\t"
        "v_min_u32 v108, 47, v108\n\t"
        "v_min_u32 v109, 47, v109\n\t"
        "v_min_u32 v110, 47, v110\n\t"
        "v_lshl_add_u32 v96, v96, 2, v80\n\t"
        "v_lshl_add_u32 v97, v97, 2, v81\n\t"
        "v_lshl_add_u32 v98, v98, 2, v82\n\t"
        "v_lshl_add_u32 v100, v100, 2, v84\n\t"
        "v_lshl_add_u32 v101, v101, 2, v85\n\t"
        "v_lshl_add_u32 v102, v102, 2, v86\n\t"
        "v_lshl_add_u32 v104, v104, 2, v88\n\t"
        "v_lshl_add_u32 v105, v105, 2, v89\n\t"
        "v_lshl_add_u32 v106, v106, 2, v90\n\t"
        "v_lshl_add_u32 v108, v108, 2, v92\n\t"
        "v_lshl_add_u32 v109, v109, 2, v93\n\t"
        "v_lshl_add_u32 v110, v110, 2, v94\n\t"
        "v_cmpx_gt_u16 vcc, s34, v64\n\t"
        "ds_write_b32 v96, v112 offset:32384\n\t"
        "v_cmpx_gt_u16 vcc, s34, v65\n\t"
        "ds_write_b32 v97, v113 offset:32384\n\t"
        "v_cmpx_gt_u16 vcc, s34, v66\n\t"
        "ds_write_b32 v98, v114 offset:32384\n\t"
        "s_mov_b64 exec, -1\n\t"
        "v_cmpx_gt_u16 vcc, s34, v68\n\t"
        "ds_write_b32 v100, v116 offset:32384\n\t"
        "v_cmpx_gt_u16 vcc, s34, v69\n\t"
        "ds_write_b32 v101, v117 offset:32384\n\t"
        "v_cmpx_gt_u16 vcc, s34, v70\n\t"
        "ds_write_b32 v102, v118 offset:32384\n\t"
        "s_mov_b64 exec, -1\n\t"
        "v_cmpx_gt_u16 vcc, s34, v72\n\t"
        "ds_write_b32 v104, v120 offset:32384\n\t"
        "v_cmpx_gt_u16 vcc, s34, v73\n\t"
        "ds_write_b32 v105, v121 offset:32384\n\t"
        "v_cmpx_gt_u16 vcc, s34, v74\n\t"
        "ds_write_b32 v106, v122 offset:32384\n\t"
        "s_mov_b64 exec, -1\n\t"
        "v_cmpx_gt_u16 vcc, s34, v76\n\t"
        "ds_write_b32 v108, v124 offset:32384\n\t"
        "v_cmpx_gt_u16 vcc, s34, v77\n\t"
        "ds_write_b32 v109, v125 offset:32384\n\t"
        "v_cmpx_gt_u16 vcc, s34, v78\n\t"
        "ds_write_b32 v110, v126 offset:32384\n\t"
        "s_mov_b64 exec, -1\n\t"
        "s_cmp_lg_u32 s36, 0\n\t"
        "s_cbranch_scc1 .L7_z\n"
        ".L7_zr:\n\t"
        "s_mov_b64 exec, 1\n\t"
        "ds_add_u32 v2, v30 offset:36\n\t"
        "s_mov_b64 exec, -1\n\t"
        "s_waitcnt lgkmcnt(13)\n\t"
        "v_dot4_u32_u8 v6, v6, s37, -1\n\t"
        "v_mov_b64 v[32:33], 0\n\t"
        "v_mov_b64 v[34:35], 0\n\t"
        "v_mov_b64 v[36:37], 0\n\t"
        "v_med3_i32 v6, v6, 0, 47\n\t"
        "v_lshrrev_b32 v6, 1, v6\n\t"
        "v_mov_b64 v[38:39], 0\n\t"
        "v_mul_u32_u24_sdwa v19, v7, v24 dst_sel:DWORD dst_unused:UNUSED_PAD src0_sel:DWORD src1_sel:BYTE_0\n\t"
        "v_max_u32_dpp v6, v6, v6 quad_perm:[1,0,3,2] row_mask:0xf bank_mask:0xf\n\t"
        "s_nop 1\n\t"
        "v_max_u32_dpp v6, v6, v6 quad_perm:[2,3,0,1] row_mask:0xf bank_mask:0xf\n\t"
        "s_nop 0\n\t"
        "v_readlane_b32 s24, v6, 0\n\t"
        "v_readlane_b32 s25, v6, 4\n\t"
        "v_readlane_b32 s26, v6, 8\n\t"
        "v_readlane_b32 s27, v6, 12\n\t"
        "v_readlane_b32 s28, v6, 16\n\t"
        "s_max_u32 s28, s28, 4\n\t"
        "s_mov_b32 s56, s24\n\t"
        "s_mov_b32 s57, s24\n\t"
        "s_mov_b32 s78, 0xf0000\n"
        ".L7_w72:\n\t"
        "ds_read_b32 v4, v2 offset:36\n\t"
        "ds_read_b32 v12, v31 offset:32572\n\t"
        "s_waitcnt lgkmcnt(1)\n\t"
        "v_cmp_gt_u16 vcc, 4, v4\n\t"
        "s_cbranch_vccz .L7_d73\n\t"
        "s_waitcnt lgkmcnt(0)\n\t"
        "s_sleep 0\n\t"
        "s_sub_i32 s83, s83, 1\n\t"
        "s_cmp_gt_i32 s83, 0\n\t"
        "s_cbranch_scc1 .L7_w72\n"
        ".L7_d73:\n\t"
        "ds_read_b64 v[86:87], v19 offset:32384\n\t"
        "v_add_u32 v19, 8, v19\n\t"
        "s_sub_u32 s56, s56, 1\n\t"
        "s_cbranch_scc1 .L7_i74\n"
        ".L7_j75:\n\t"
        "ds_read_b64 v[94:95], v19 offset:32384\n\t"
        "v_add_u32 v19, 8, v19\n\t"
        "s_sub_u32 s56, s56, 1\n\t"
        "s_cbranch_scc1 .L7_i76\n"
        ".L7_j77:\n\t"
        "ds_read_b64 v[102:103], v19 offset:32384\n\t"
        "v_add_u32 v19, 8, v19\n\t"
        "s_sub_u32 s56, s56, 1\n\t"
        "s_cbranch_scc1 .L7_i78\n"
        ".L7_j79:\n\t"
        "ds_read_b64 v[110:111], v19 offset:32384\n\t"
        "v_add_u32 v19, 8, v19\n\t"
        "s_sub_u32 s56, s56, 1\n\t"
        "s_cbranch_scc1 .L7_i80\n"
        ".L7_j81:\n\t"
        "ds_read_b64 v[118:119], v19 offset:32384\n\t"
        "v_add_u32 v19, 8, v19\n\t"
        "s_sub_u32 s56, s56, 1\n\t"
        "s_cbranch_scc1 .L7_i82\n"
        ".L7_j83:\n\t"
        "ds_read_b64 v[126:127], v19 offset:32384\n\t"
        "v_add_u32 v19, 8, v19\n\t"
        "s_sub_u32 s56, s56, 1\n\t"
        "s_cbranch_scc1 .L7_i84\n"
        ".L7_j85:\n\t"
        "ds_read_b64 v[40:41], v19 offset:32384\n\t"
        "v_add_u32 v19, 8, v19\n\t"
        "s_sub_u32 s56, s56, 1\n\t"
        "s_cbranch_scc1 .L7_i86\n"
        ".L7_j87:\n\t"
        "ds_read_b64 v[42:43], v19 offset:32384\n\t"
        "v_add_u32 v19, 8, v19\n\t"
        "s_sub_u32 s56, s56, 1\n\t"
        "s_cbranch_scc1 .L7_i88\n"
        ".L7_j89:\n\t"
        "s_waitcnt lgkmcnt(8)\n\t"
        "v_and_or_b32 v12, v4, s78, v12\n\t"
        "v_cmp_ne_u32 vcc, 0xffff, v12\n\t"
        "s_cbranch_vccnz .L7_exit4\n\t"
        "s_waitcnt lgkmcnt(7)\n\t"
        "v_mad_u32_u16 v16, v86, s48, v14\n\t"
        "v_mad_u32_u16 v17, v87, s48, v14\n\t"
        "v_perm_b32 v48, v87, v86, s38\n\t"
        "buffer_load_dwordx4 v[80:83], v16, s[16:19], 0 offen\n\t"
        "buffer_load_dwordx4 v[84:87], v17, s[16:19], 0 offen\n\t"
        "s_waitcnt lgkmcnt(6)\n\t"
        "v_mad_u32_u16 v16, v94, s48, v14\n\t"
        "v_mad_u32_u16 v17, v95, s48, v14\n\t"
        "v_perm_b32 v49, v95, v94, s38\n\t"
        "buffer_load_dwordx4 v[88:91], v16, s[16:19], 0 offen\n\t"
        "buffer_load_dwordx4 v[92:95], v17, s[16:19], 0 offen\n\t"
        "s_waitcnt lgkmcnt(5)\n\t"
        "v_mad_u32_u16 v16, v102, s48, v14\n\t"
        "v_mad_u32_u16 v17, v103, s48, v14\n\t"
        "v_perm_b32 v50, v103, v102, s38\n\t"
        "buffer_load_dwordx4 v[96:99], v16, s[16:19], 0 offen\n\t"
        "buffer_load_dwordx4 v[100:103], v17, s[16:19], 0 offen\n\t"
        "s_waitcnt lgkmcnt(4)\n\t"
        "v_mad_u32_u16 v16, v110, s48, v14\n\t"
        "v_mad_u32_u16 v17, v111, s48, v14\n\t"
        "v_perm_b32 v51, v111, v110, s38\n\t"
        "buffer_load_dwordx4 v[104:107], v16, s[16:19], 0 offen\n\t"
        "buffer_load_dwordx4 v[108:111], v17, s[16:19], 0 offen\n\t"
        "s_waitcnt lgkmcnt(3)\n\t"
        "v_mad_u32_u16 v16, v118, s48, v14\n\t"
        "v_mad_u32_u16 v17, v119, s48, v14\n\t"
        "v_perm_b32 v52, v119, v118, s38\n\t"
        "buffer_load_dwordx4 v[112:115], v16, s[16:19], 0 offen\n\t"
        "buffer_load_dwordx4 v[116:119], v17, s[16:19], 0 offen\n\t"
        "s_waitcnt lgkmcnt(2)\n\t"
        "v_mad_u32_u16 v16, v126, s48, v14\n\t"
        "v_mad_u32_u16 v17, v127, s48, v14\n\t"
        "v_perm_b32 v53, v127, v126, s38\n\t"
        "buffer_load_dwordx4 v[120:123], v16, s[16:19], 0 offen\n\t"
        "buffer_load_dwordx4 v[124:127], v17, s[16:19], 0 offen\n\t"
        "s_setprio 3\n"
        ".L7_loop:\n\t"
        "v_perm_b32 v20, v48, v48, v8\n\t"
        "v_perm_b32 v21, v48, v48, v9\n\t"
        "v_perm_b32 v22, v48, v48, v10\n\t"
        "v_perm_b32 v23, v48, v48, v11\n\t"
        "s_waitcnt vmcnt(10) lgkmcnt(1)\n\t"
        "v_mfma_f32_4x4x4_16b_bf16 v[32:35], v[20:21], v[80:81], v[32:35]\n\t"
        "v_mfma_f32_4x4x4_16b_bf16 v[36:39], v[20:21], v[82:83], v[36:39]\n\t"
        "v_mad_u32_u16 v16, v40, s48, v14\n\t"
        "v_mfma_f32_4x4x4_16b_bf16 v[32:35], v[22:23], v[84:85], v[32:35]\n\t"
        "v_mfma_f32_4x4x4_16b_bf16 v[36:39], v[22:23], v[86:87], v[36:39]\n\t"
        "v_mad_u32_u16 v17, v41, s48, v14\n\t"
        "s_cbranch_vccnz .L7_dr0\n\t"
        "buffer_load_dwordx4 v[80:83], v16, s[16:19], 0 offen\n\t"
        "buffer_load_dwordx4 v[84:87], v17, s[16:19], 0 offen\n\t"
        "v_perm_b32 v48, v41, v40, s38\n\t"
        "ds_read_b64 v[40:41], v19 offset:32384\n\t"
        "v_add_u32 v19, 8, v19\n\t"
        "s_sub_u32 s56, s56, 1\n\t"
        "s_cbranch_scc1 .L7_i90\n"
        ".L7_j91:\n\t"
        "s_sub_u32 s57, s57, 1\n\t"
        "s_cbranch_scc1 .L7_i92\n"
        ".L7_j93:\n\t"
        "v_perm_b32 v20, v49, v49, v8\n\t"
        "v_perm_b32 v21, v49, v49, v9\n\t"
        "v_perm_b32 v22, v49, v49, v10\n\t"
        "v_perm_b32 v23, v49, v49, v11\n\t"
        "s_waitcnt vmcnt(10) lgkmcnt(1)\n\t"
        "v_mfma_f32_4x4x4_16b_bf16 v[32:35], v[20:21], v[88:89], v[32:35]\n\t"
        "v_mfma_f32_4x4x4_16b_bf16 v[36:39], v[20:21], v[90:91], v[36:39]\n\t"
        "v_mad_u32_u16 v16, v42, s48, v14\n\t"
        "v_mfma_f32_4x4x4_16b_bf16 v[32:35], v[22:23], v[92:93], v[32:35]\n\t"
        "v_mfma_f32_4x4x4_16b_bf16 v[36:39], v[22:23], v[94:95], v[36:39]\n\t"
        "v_mad_u32_u16 v17, v43, s48, v14\n\t"
        "s_cbranch_vccnz .L7_dr1\n\t"
        "buffer_load_dwordx4 v[88:91], v16, s[16:19], 0 offen\n\t"
        "buffer_load_dwordx4 v[92:95], v17, s[16:19], 0 offen\n\t"
        "v_perm_b32 v49, v43, v42, s38\n\t"
        "ds_read_b64 v[42:43], v19 offset:32384\n\t"
        "v_add_u32 v19, 8, v19\n\t"
        "s_sub_u32 s56, s56, 1\n\t"
        "s_cbranch_scc1 .L7_i94\n"
        ".L7_j95:\n\t"
        "s_sub_u32 s57, s57, 1\n\t"
        "s_cbranch_scc1 .L7_i96\n"
        ".L7_j97:\n\t"
        "v_perm_b32 v20, v50, v50, v8\n\t"
        "v_perm_b32 v21, v50, v50, v9\n\t"
        "v_perm_b32 v22, v50, v50, v10\n\t"
        "v_perm_b32 v23, v50, v50, v11\n\t"
        "s_waitcnt vmcnt(10) lgkmcnt(1)\n\t"
        "v_mfma_f32_4x4x4_16b_bf16 v[32:35], v[20:21], v[96:97], v[32:35]\n\t"
        "v_mfma_f32_4x4x4_16b_bf16 v[36:39], v[20:21], v[98:99], v[36:39]\n\t"
        "v_mad_u32_u16 v16, v40, s48, v14\n\t"
        "v_mfma_f32_4x4x4_16b_bf16 v[32:35], v[22:23], v[100:101], v[32:35]\n\t"
        "v_mfma_f32_4x4x4_16b_bf16 v[36:39], v[22:23], v[102:103], v[36:39]\n\t"
        "v_mad_u32_u16 v17, v41, s48, v14\n\t"
        "s_cbranch_vccnz .L7_dr2\n\t"
        "buffer_load_dwordx4 v[96:99], v16, s[16:19], 0 offen\n\t"
        "buffer_load_dwordx4 v[100:103], v17, s[16:19], 0 offen\n\t"
        "v_perm_b32 v50, v41, v40, s38\n\t"
        "ds_read_b64 v[40:41], v19 offset:32384\n\t"
        "v_add_u32 v19, 8, v19\n\t"
        "s_sub_u32 s56, s56, 1\n\t"
        "s_cbranch_scc1 .L7_i98\n"
        ".L7_j99:\n\t"
        "s_sub_u32 s57, s57, 1\n\t"
        "s_cbranch_scc1 .L7_i100\n"
        ".L7_j101:\n\t"
        "v_perm_b32 v20, v51, v51, v8\n\t"
        "v_perm_b32 v21, v51, v51, v9\n\t"
        "v_perm_b32 v22, v51, v51, v10\n\t"
        "v_perm_b32 v23, v51, v51, v11\n\t"
        "s_waitcnt vmcnt(10) lgkmcnt(1)\n\t"
        "v_mfma_f32_4x4x4_16b_bf16 v[32:35], v[20:21], v[104:105], v[32:35]\n\t"
        "v_mfma_f32_4x4x4_16b_bf16 v[36:39], v[20:21], v[106:107], v[36:39]\n\t"
        "v_mad_u32_u16 v16, v42, s48, v14\n\t"
        "v_mfma_f32_4x4x4_16b_bf16 v[32:35], v[22:23], v[108:109], v[32:35]\n\t"
        "v_mfma_f32_4x4x4_16b_bf16 v[36:39], v[22:23], v[110:111], v[36:39]\n\t"
        "v_mad_u32_u16 v17, v43, s48, v14\n\t"
        "s_cbranch_vccnz .L7_dr3\n\t"
        "buffer_load_dwordx4 v[104:107], v16, s[16:19], 0 offen\n\t"
        "buffer_load_dwordx4 v[108:111], v17, s[16:19], 0 offen\n\t"
        "v_perm_b32 v51, v43, v42, s38\n\t"
        "ds_read_b64 v[42:43], v19 offset:32384\n\t"
        "v_add_u32 v19, 8, v19\n\t"
        "s_sub_u32 s56, s56, 1\n\t"
        "s_cbranch_scc1 .L7_i102\n"
        ".L7_j103:\n\t"
        "s_sub_u32 s57, s57, 1\n\t"
        "s_cbranch_scc1 .L7_i104\n"
        ".L7_j105:\n\t"
        "v_perm_b32 v20, v52, v52, v8\n\t"
        "v_perm_b32 v21, v52, v52, v9\n\t"
        "v_perm_b32 v22, v52, v52, v10\n\t"
        "v_perm_b32 v23, v52, v52, v11\n\t"
        "s_waitcnt vmcnt(10) lgkmcnt(1)\n\t"
        "v_mfma_f32_4x4x4_16b_bf16 v[32:35], v[20:21], v[112:113], v[32:35]\n\t"
        "v_mfma_f32_4x4x4_16b_bf16 v[36:39], v[20:21], v[114:115], v[36:39]\n\t"
        "v_mad_u32_u16 v16, v40, s48, v14\n\t"
        "v_mfma_f32_4x4x4_16b_bf16 v[32:35], v[22:23], v[116:117], v[32:35]\n\t"
        "v_mfma_f32_4x4x4_16b_bf16 v[36:39], v[22:23], v[118:119], v[36:39]\n\t"
        "v_mad_u32_u16 v17, v41, s48, v14\n\t"
        "s_cbranch_vccnz .L7_dr4\n\t"
        "buffer_load_dwordx4 v[112:115], v16, s[16:19], 0 offen\n\t"
        "buffer_load_dwordx4 v[116:119], v17, s[16:19], 0 offen\n\t"
        "v_perm_b32 v52, v41, v40, s38\n\t"
        "ds_read_b64 v[40:41], v19 offset:32384\n\t"
        "v_add_u32 v19, 8, v19\n\t"
        "s_sub_u32 s56, s56, 1\n\t"
        "s_cbranch_scc1 .L7_i106\n"
        ".L7_j107:\n\t"
        "s_sub_u32 s57, s57, 1\n\t"
        "s_cbranch_scc1 .L7_i108\n"
        ".L7_j109:\n\t"
        "v_perm_b32 v20, v53, v53, v8\n\t"
        "v_perm_b32 v21, v53, v53, v9\n\t"
        "v_perm_b32 v22, v53, v53, v10\n\t"
        "v_perm_b32 v23, v53, v53, v11\n\t"
        "s_waitcnt vmcnt(10) lgkmcnt(1)\n\t"
        "v_mfma_f32_4x4x4_16b_bf16 v[32:35], v[20:21], v[120:121], v[32:35]\n\t"
        "v_mfma_f32_4x4x4_16b_bf16 v[36:39], v[20:21], v[122:123], v[36:39]\n\t"
        "v_mad_u32_u16 v16, v42, s48, v14\n\t"
        "v_mfma_f32_4x4x4_16b_bf16 v[32:35], v[22:23], v[124:125], v[32:35]\n\t"
        "v_mfma_f32_4x4x4_16b_bf16 v[36:39], v[22:23], v[126:127], v[36:39]\n\t"
        "v_mad_u32_u16 v17, v43, s48, v14\n\t"
        "s_cbranch_vccnz .L7_dr5\n\t"
        "buffer_load_dwordx4 v[120:123], v16, s[16:19], 0 offen\n\t"
        "buffer_load_dwordx4 v[124:127], v17, s[16:19], 0 offen\n\t"
        "v_perm_b32 v53, v43, v42, s38\n\t"
        "ds_read_b64 v[42:43], v19 offset:32384\n\t"
        "v_add_u32 v19, 8, v19\n\t"
        "s_sub_u32 s56, s56, 1\n\t"
        "s_cbranch_scc1 .L7_i110\n"
        ".L7_j111:\n\t"
        "s_sub_u32 s57, s57, 1\n\t"
        "s_cbranch_scc1 .L7_i112\n"
        ".L7_j113:\n\t"
        "s_branch .L7_loop\n"
        ".L7_i74:\n\t"
        "s_swappc_b64 s[54:55], s[50:51]\n\t"
        "s_branch .L7_j75\n"
        ".L7_i76:\n\t"
        "s_swappc_b64 s[54:55], s[50:51]\n\t"
        "s_branch .L7_j77\n"
        ".L7_i78:\n\t"
        "s_swappc_b64 s[54:55], s[50:51]\n\t"
        "s_branch .L7_j79\n"
        ".L7_i80:\n\t"
        "s_swappc_b64 s[54:55], s[50:51]\n\t"
        "s_branch .L7_j81\n"
        ".L7_i82:\n\t"
        "s_swappc_b64 s[54:55], s[50:51]\n\t"
        "s_branch .L7_j83\n"
        ".L7_i84:\n\t"
        "s_swappc_b64 s[54:55], s[50:51]\n\t"
        "s_branch .L7_j85\n"
        ".L7_i86:\n\t"
        "s_swappc_b64 s[54:55], s[50:51]\n\t"
        "s_branch .L7_j87\n"
        ".L7_i88:\n\t"
        "s_swappc_b64 s[54:55], s[50:51]\n\t"
        "s_branch .L7_j89\n"
        ".L7_i90:\n\t"
        "s_swappc_b64 s[54:55], s[50:51]\n\t"
        "s_branch .L7_j91\n"
        ".L7_i92:\n\t"
        "s_swappc_b64 s[54:55], s[52:53]\n\t"
        "s_branch .L7_j93\n"
        ".L7_i94:\n\t"
        "s_swappc_b64 s[54:55], s[50:51]\n\t"
        "s_branch .L7_j95\n"
        ".L7_i96:\n\t"
        "s_swappc_b64 s[54:55], s[52:53]\n\t"
        "s_branch .L7_j97\n"
        ".L7_i98:\n\t"
        "s_swappc_b64 s[54:55], s[50:51]\n\t"
        "s_branch .L7_j99\n"
        ".L7_i100:\n\t"
        "s_swappc_b64 s[54:55], s[52:53]\n\t"
        "s_branch .L7_j101\n"
        ".L7_i102:\n\t"
        "s_swappc_b64 s[54:55], s[50:51]\n\t"
        "s_branch .L7_j103\n"
        ".L7_i104:\n\t"
        "s_swappc_b64 s[54:55], s[52:53]\n\t"
        "s_branch .L7_j105\n"
        ".L7_i106:\n\t"
        "s_swappc_b64 s[54:55], s[50:51]\n\t"
        "s_branch .L7_j107\n"
        ".L7_i108:\n\t"
        "s_swappc_b64 s[54:55], s[52:53]\n\t"
        "s_branch .L7_j109\n"
        ".L7_i110:\n\t"
        "s_swappc_b64 s[54:55], s[50:51]\n\t"
        "s_branch .L7_j111\n"
        ".L7_i112:\n\t"
        "s_swappc_b64 s[54:55], s[52:53]\n\t"
        "s_branch .L7_j113\n"
        ".L7_dr0:\n\t"
        "s_sub_u32 s57, s57, 1\n\t"
        "s_cbranch_scc0 .L7_ds0\n\t"
        "s_swappc_b64 s[54:55], s[52:53]\n"
        ".L7_ds0:\n\t"
        "s_mov_b32 s57, 4\n\t"
        "s_waitcnt vmcnt(4)\n\t"
        "s_branch .L7_dt1\n"
        ".L7_dr1:\n\t"
        "s_sub_u32 s57, s57, 1\n\t"
        "s_cbranch_scc0 .L7_ds1\n\t"
        "s_swappc_b64 s[54:55], s[52:53]\n"
        ".L7_ds1:\n\t"
        "s_mov_b32 s57, 4\n\t"
        "s_waitcnt vmcnt(4)\n\t"
        "s_branch .L7_dt2\n"
        ".L7_dr2:\n\t"
        "s_sub_u32 s57, s57, 1\n\t"
        "s_cbranch_scc0 .L7_ds2\n\t"
        "s_swappc_b64 s[54:55], s[52:53]\n"
        ".L7_ds2:\n\t"
        "s_mov_b32 s57, 4\n\t"
        "s_waitcnt vmcnt(4)\n\t"
        "s_branch .L7_dt3\n"
        ".L7_dr3:\n\t"
        "s_sub_u32 s57, s57, 1\n\t"
        "s_cbranch_scc0 .L7_ds3\n\t"
        "s_swappc_b64 s[54:55], s[52:53]\n"
        ".L7_ds3:\n\t"
        "s_mov_b32 s57, 4\n\t"
        "s_waitcnt vmcnt(4)\n\t"
        "s_branch .L7_dt4\n"
        ".L7_dr4:\n\t"
        "s_sub_u32 s57, s57, 1\n\t"
        "s_cbranch_scc0 .L7_ds4\n\t"
        "s_swappc_b64 s[54:55], s[52:53]\n"
        ".L7_ds4:\n\t"
        "s_mov_b32 s57, 4\n\t"
        "s_waitcnt vmcnt(4)\n\t"
        "s_branch .L7_dt5\n"
        ".L7_dr5:\n\t"
        "s_sub_u32 s57, s57, 1\n\t"
        "s_cbranch_scc0 .L7_ds5\n\t"
        "s_swappc_b64 s[54:55], s[52:53]\n"
        ".L7_ds5:\n\t"
        "s_mov_b32 s57, 4\n\t"
        "s_waitcnt vmcnt(4)\n\t"
        "s_branch .L7_dt0\n"
        ".L7_dt0:\n\t"
        "v_perm_b32 v20, v48, v48, v8\n\t"
        "v_perm_b32 v21, v48, v48, v9\n\t"
        "v_perm_b32 v22, v48, v48, v10\n\t"
        "v_perm_b32 v23, v48, v48, v11\n\t"
        "s_cmp_lt_u32 s57, 2\n\t"
        "s_cbranch_scc0 .L7_du0\n\t"
        "s_waitcnt vmcnt(0)\n"
        ".L7_du0:\n\t"
        "v_mfma_f32_4x4x4_16b_bf16 v[32:35], v[20:21], v[80:81], v[32:35]\n\t"
        "v_mfma_f32_4x4x4_16b_bf16 v[36:39], v[20:21], v[82:83], v[36:39]\n\t"
        "s_sub_u32 s57, s57, 1\n\t"
        "v_mfma_f32_4x4x4_16b_bf16 v[32:35], v[22:23], v[84:85], v[32:35]\n\t"
        "v_mfma_f32_4x4x4_16b_bf16 v[36:39], v[22:23], v[86:87], v[36:39]\n\t"
        "s_cbranch_scc1 .L7_last\n"
        ".L7_dt1:\n\t"
        "v_perm_b32 v20, v49, v49, v8\n\t"
        "v_perm_b32 v21, v49, v49, v9\n\t"
        "v_perm_b32 v22, v49, v49, v10\n\t"
        "v_perm_b32 v23, v49, v49, v11\n\t"
        "s_cmp_lt_u32 s57, 2\n\t"
        "s_cbranch_scc0 .L7_du1\n\t"
        "s_waitcnt vmcnt(0)\n"
        ".L7_du1:\n\t"
        "v_mfma_f32_4x4x4_16b_bf16 v[32:35], v[20:21], v[88:89], v[32:35]\n\t"
        "v_mfma_f32_4x4x4_16b_bf16 v[36:39], v[20:21], v[90:91], v[36:39]\n\t"
        "s_sub_u32 s57, s57, 1\n\t"
        "v_mfma_f32_4x4x4_16b_bf16 v[32:35], v[22:23], v[92:93], v[32:35]\n\t"
        "v_mfma_f32_4x4x4_16b_bf16 v[36:39], v[22:23], v[94:95], v[36:39]\n\t"
        "s_cbranch_scc1 .L7_last\n"
        ".L7_dt2:\n\t"
        "v_perm_b32 v20, v50, v50, v8\n\t"
        "v_perm_b32 v21, v50, v50, v9\n\t"
        "v_perm_b32 v22, v50, v50, v10\n\t"
        "v_perm_b32 v23, v50, v50, v11\n\t"
        "s_cmp_lt_u32 s57, 2\n\t"
        "s_cbranch_scc0 .L7_du2\n\t"
        "s_waitcnt vmcnt(0)\n"
        ".L7_du2:\n\t"
        "v_mfma_f32_4x4x4_16b_bf16 v[32:35], v[20:21], v[96:97], v[32:35]\n\t"
        "v_mfma_f32_4x4x4_16b_bf16 v[36:39], v[20:21], v[98:99], v[36:39]\n\t"
        "s_sub_u32 s57, s57, 1\n\t"
        "v_mfma_f32_4x4x4_16b_bf16 v[32:35], v[22:23], v[100:101], v[32:35]\n\t"
        "v_mfma_f32_4x4x4_16b_bf16 v[36:39], v[22:23], v[102:103], v[36:39]\n\t"
        "s_cbranch_scc1 .L7_last\n"
        ".L7_dt3:\n\t"
        "v_perm_b32 v20, v51, v51, v8\n\t"
        "v_perm_b32 v21, v51, v51, v9\n\t"
        "v_perm_b32 v22, v51, v51, v10\n\t"
        "v_perm_b32 v23, v51, v51, v11\n\t"
        "s_cmp_lt_u32 s57, 2\n\t"
        "s_cbranch_scc0 .L7_du3\n\t"
        "s_waitcnt vmcnt(0)\n"
        ".L7_du3:\n\t"
        "v_mfma_f32_4x4x4_16b_bf16 v[32:35], v[20:21], v[104:105], v[32:35]\n\t"
        "v_mfma_f32_4x4x4_16b_bf16 v[36:39], v[20:21], v[106:107], v[36:39]\n\t"
        "s_sub_u32 s57, s57, 1\n\t"
        "v_mfma_f32_4x4x4_16b_bf16 v[32:35], v[22:23], v[108:109], v[32:35]\n\t"
        "v_mfma_f32_4x4x4_16b_bf16 v[36:39], v[22:23], v[110:111], v[36:39]\n\t"
        "s_cbranch_scc1 .L7_last\n"
        ".L7_dt4:\n\t"
        "v_perm_b32 v20, v52, v52, v8\n\t"
        "v_perm_b32 v21, v52, v52, v9\n\t"
        "v_perm_b32 v22, v52, v52, v10\n\t"
        "v_perm_b32 v23, v52, v52, v11\n\t"
        "s_cmp_lt_u32 s57, 2\n\t"
        "s_cbranch_scc0 .L7_du4\n\t"
        "s_waitcnt vmcnt(0)\n"
        ".L7_du4:\n\t"
        "v_mfma_f32_4x4x4_16b_bf16 v[32:35], v[20:21], v[112:113], v[32:35]\n\t"
        "v_mfma_f32_4x4x4_16b_bf16 v[36:39], v[20:21], v[114:115], v[36:39]\n\t"
        "s_sub_u32 s57, s57, 1\n\t"
        "v_mfma_f32_4x4x4_16b_bf16 v[32:35], v[22:23], v[116:117], v[32:35]\n\t"
        "v_mfma_f32_4x4x4_16b_bf16 v[36:39], v[22:23], v[118:119], v[36:39]\n\t"
        "s_cbranch_scc1 .L7_last\n"
        ".L7_dt5:\n\t"
        "v_perm_b32 v20, v53, v53, v8\n\t"
        "v_perm_b32 v21, v53, v53, v9\n\t"
        "v_perm_b32 v22, v53, v53, v10\n\t"
        "v_perm_b32 v23, v53, v53, v11\n\t"
        "s_cmp_lt_u32 s57, 2\n\t"
        "s_cbranch_scc0 .L7_du5\n\t"
        "s_waitcnt vmcnt(0)\n"
        ".L7_du5:\n\t"
        "v_mfma_f32_4x4x4_16b_bf16 v[32:35], v[20:21], v[120:121], v[32:35]\n\t"
        "v_mfma_f32_4x4x4_16b_bf16 v[36:39], v[20:21], v[122:123], v[36:39]\n\t"
        "s_sub_u32 s57, s57, 1\n\t"
        "v_mfma_f32_4x4x4_16b_bf16 v[32:35], v[22:23], v[124:125], v[32:35]\n\t"
        "v_mfma_f32_4x4x4_16b_bf16 v[36:39], v[22:23], v[126:127], v[36:39]\n\t"
        "s_cbranch_scc1 .L7_last\n\t"
        "s_branch .L7_dt0\n"
        ".L7_ie:\n\t"
        "v_mul_u32_u24_sdwa v19, v7, v25 dst_sel:DWORD dst_unused:UNUSED_PAD src0_sel:DWORD src1_sel:BYTE_0\n\t"
        "s_mov_b32 s56, s25\n\t"
        "s_swappc_b64 s[50:51], s[54:55]\n\t"
        "v_mul_u32_u24_sdwa v19, v7, v26 dst_sel:DWORD dst_unused:UNUSED_PAD src0_sel:DWORD src1_sel:BYTE_0\n\t"
        "s_mov_b32 s56, s26\n\t"
        "s_swappc_b64 s[50:51], s[54:55]\n\t"
        "v_mul_u32_u24_sdwa v19, v7, v27 dst_sel:DWORD dst_unused:UNUSED_PAD src0_sel:DWORD src1_sel:BYTE_0\n\t"
        "s_mov_b32 s56, s27\n\t"
        "s_swappc_b64 s[50:51], s[54:55]\n\t"
        "v_mul_u32_u24_sdwa v19, v7, v28 dst_sel:DWORD dst_unused:UNUSED_PAD src0_sel:DWORD src1_sel:BYTE_0\n\t"
        "s_add_u32 s56, s28, 2\n\t"
        "s_swappc_b64 s[50:51], s[54:55]\n\t"
        "s_mov_b64 vcc, -1\n\t"
        "s_setpc_b64 s[54:55]\n"
        ".L7_ce:\n\t"
        "v_and_b32 v4, 0xffff0000, v24\n\t"
        "s_nop 2\n\t"
        "v_pk_mul_f32 v[32:33], v[32:33], v[4:5] op_sel_hi:[1,0]\n\t"
        "v_pk_mul_f32 v[34:35], v[34:35], v[4:5] op_sel_hi:[1,0]\n\t"
        "v_pk_mul_f32 v[36:37], v[36:37], v[4:5] op_sel_hi:[1,0]\n\t"
        "v_pk_mul_f32 v[38:39], v[38:39], v[4:5] op_sel_hi:[1,0]\n\t"
        "v_cvt_pk_bf16_f32 v44, v32, v33\n\t"
        "v_cvt_pk_bf16_f32 v45, v34, v35\n\t"
        "v_cvt_pk_bf16_f32 v46, v36, v37\n\t"
        "v_cvt_pk_bf16_f32 v47, v38, v39\n\t"
        "s_mov_b64 s[58:59], vcc\n"
        ".L7_w114:\n\t"
        "ds_read_b32 v4, v2 offset:12\n\t"
        "s_waitcnt lgkmcnt(0)\n\t"
        "v_cmp_gt_u16 vcc, 4, v4\n\t"
        "s_cbranch_vccz .L7_d115\n\t"
        "s_sleep 1\n\t"
        "s_sub_i32 s83, s83, 1\n\t"
        "s_cmp_gt_i32 s83, 0\n\t"
        "s_cbranch_scc1 .L7_w114\n"
        ".L7_d115:\n\t"
        "s_mov_b64 vcc, s[58:59]\n\t"
        "ds_write_b128 v56, v[44:47] offset:20480\n\t"
        "s_mov_b64 exec, 1\n\t"
        "ds_add_u32 v2, v3 offset:52\n\t"
        "s_mov_b64 exec, -1\n\t"
        "v_mov_b64 v[32:33], 0\n\t"
        "v_mov_b64 v[34:35], 0\n\t"
        "v_mov_b64 v[36:37], 0\n\t"
        "v_mov_b64 v[38:39], 0\n\t"
        "s_mov_b32 s57, s25\n\t"
        "s_swappc_b64 s[52:53], s[54:55]\n\t"
        "v_and_b32 v4, 0xffff0000, v25\n\t"
        "s_nop 2\n\t"
        "v_pk_mul_f32 v[32:33], v[32:33], v[4:5] op_sel_hi:[1,0]\n\t"
        "v_pk_mul_f32 v[34:35], v[34:35], v[4:5] op_sel_hi:[1,0]\n\t"
        "v_pk_mul_f32 v[36:37], v[36:37], v[4:5] op_sel_hi:[1,0]\n\t"
        "v_pk_mul_f32 v[38:39], v[38:39], v[4:5] op_sel_hi:[1,0]\n\t"
        "v_cvt_pk_bf16_f32 v44, v32, v33\n\t"
        "v_cvt_pk_bf16_f32 v45, v34, v35\n\t"
        "v_cvt_pk_bf16_f32 v46, v36, v37\n\t"
        "v_cvt_pk_bf16_f32 v47, v38, v39\n\t"
        "ds_write_b128 v57, v[44:47] offset:20480\n\t"
        "s_mov_b64 exec, 1\n\t"
        "ds_add_u32 v61, v3 offset:52\n\t"
        "s_mov_b64 exec, -1\n\t"
        "v_mov_b64 v[32:33], 0\n\t"
        "v_mov_b64 v[34:35], 0\n\t"
        "v_mov_b64 v[36:37], 0\n\t"
        "v_mov_b64 v[38:39], 0\n\t"
        "s_mov_b32 s57, s26\n\t"
        "s_swappc_b64 s[52:53], s[54:55]\n\t"
        "v_and_b32 v4, 0xffff0000, v26\n\t"
        "s_nop 2\n\t"
        "v_pk_mul_f32 v[32:33], v[32:33], v[4:5] op_sel_hi:[1,0]\n\t"
        "v_pk_mul_f32 v[34:35], v[34:35], v[4:5] op_sel_hi:[1,0]\n\t"
        "v_pk_mul_f32 v[36:37], v[36:37], v[4:5] op_sel_hi:[1,0]\n\t"
        "v_pk_mul_f32 v[38:39], v[38:39], v[4:5] op_sel_hi:[1,0]\n\t"
        "v_cvt_pk_bf16_f32 v44, v32, v33\n\t"
        "v_cvt_pk_bf16_f32 v45, v34, v35\n\t"
        "v_cvt_pk_bf16_f32 v46, v36, v37\n\t"
        "v_cvt_pk_bf16_f32 v47, v38, v39\n\t"
        "ds_write_b128 v58, v[44:47] offset:20480\n\t"
        "s_mov_b64 exec, 1\n\t"
        "ds_add_u32 v61, v3 offset:56\n\t"
        "s_mov_b64 exec, -1\n\t"
        "v_mov_b64 v[32:33], 0\n\t"
        "v_mov_b64 v[34:35], 0\n\t"
        "v_mov_b64 v[36:37], 0\n\t"
        "v_mov_b64 v[38:39], 0\n\t"
        "s_mov_b32 s57, s27\n\t"
        "s_swappc_b64 s[52:53], s[54:55]\n\t"
        "v_and_b32 v4, 0xffff0000, v27\n\t"
        "s_nop 2\n\t"
        "v_pk_mul_f32 v[32:33], v[32:33], v[4:5] op_sel_hi:[1,0]\n\t"
        "v_pk_mul_f32 v[34:35], v[34:35], v[4:5] op_sel_hi:[1,0]\n\t"
        "v_pk_mul_f32 v[36:37], v[36:37], v[4:5] op_sel_hi:[1,0]\n\t"
        "v_pk_mul_f32 v[38:39], v[38:39], v[4:5] op_sel_hi:[1,0]\n\t"
        "v_cvt_pk_bf16_f32 v44, v32, v33\n\t"
        "v_cvt_pk_bf16_f32 v45, v34, v35\n\t"
        "v_cvt_pk_bf16_f32 v46, v36, v37\n\t"
        "v_cvt_pk_bf16_f32 v47, v38, v39\n\t"
        "ds_write_b128 v59, v[44:47] offset:20480\n\t"
        "s_mov_b64 exec, 1\n\t"
        "ds_add_u32 v2, v3 offset:64\n\t"
        "s_mov_b64 exec, -1\n\t"
        "v_mov_b64 v[32:33], 0\n\t"
        "v_mov_b64 v[34:35], 0\n\t"
        "v_mov_b64 v[36:37], 0\n\t"
        "v_mov_b64 v[38:39], 0\n\t"
        "s_mov_b32 s57, s28\n\t"
        "s_swappc_b64 s[52:53], s[54:55]\n"
        ".L7_last:\n\t"
        "v_and_b32 v4, 0xffff0000, v28\n\t"
        "s_nop 2\n\t"
        "v_pk_mul_f32 v[32:33], v[32:33], v[4:5] op_sel_hi:[1,0]\n\t"
        "v_pk_mul_f32 v[34:35], v[34:35], v[4:5] op_sel_hi:[1,0]\n\t"
        "v_pk_mul_f32 v[36:37], v[36:37], v[4:5] op_sel_hi:[1,0]\n\t"
        "v_pk_mul_f32 v[38:39], v[38:39], v[4:5] op_sel_hi:[1,0]\n\t"
        "v_cvt_pk_bf16_f32 v44, v32, v33\n\t"
        "v_cvt_pk_bf16_f32 v45, v34, v35\n\t"
        "v_cvt_pk_bf16_f32 v46, v36, v37\n\t"
        "v_cvt_pk_bf16_f32 v47, v38, v39\n\t"
        "ds_write_b128 v60, v[44:47] offset:20480\n\t"
        "s_mov_b64 exec, 1\n\t"
        "ds_add_u32 v2, v3 offset:68\n\t"
        "s_mov_b64 exec, -1\n\t"
        "s_endpgm\n"
        ".L7_x:\n\t"
        "s_mov_b32 s36, 1\n\t"
        "s_waitcnt lgkmcnt(0)\n\t"
        "v_mul_u32_u24_sdwa v83, v7, v67 dst_sel:DWORD dst_unused:UNUSED_PAD src0_sel:DWORD src1_sel:BYTE_2\n\t"
        "v_lshlrev_b32_sdwa v115, 1, v67 dst_sel:DWORD dst_unused:UNUSED_PAD src0_sel:DWORD src1_sel:WORD_0\n\t"
        "ds_read_u16 v115, v115 offset:45056\n\t"
        "v_cmp_eq_u16 vcc, s34, v67\n\t"
        "s_cmp_lg_u64 vcc, 0\n\t"
        "s_cselect_b32 s78, 0x10000, 0\n\t"
        "v_or_b32 v30, s78, v30\n\t"
        "v_cmpx_gt_u16 vcc, s34, v67\n\t"
        "ds_add_rtn_u32 v99, v83, v29 offset:32576\n\t"
        "s_mov_b64 exec, -1\n\t"
        "v_mul_u32_u24_sdwa v87, v7, v71 dst_sel:DWORD dst_unused:UNUSED_PAD src0_sel:DWORD src1_sel:BYTE_2\n\t"
        "v_lshlrev_b32_sdwa v119, 1, v71 dst_sel:DWORD dst_unused:UNUSED_PAD src0_sel:DWORD src1_sel:WORD_0\n\t"
        "ds_read_u16 v119, v119 offset:45056\n\t"
        "v_cmp_eq_u16 vcc, s34, v71\n\t"
        "s_cmp_lg_u64 vcc, 0\n\t"
        "s_cselect_b32 s78, 0x10000, 0\n\t"
        "v_or_b32 v30, s78, v30\n\t"
        "v_cmpx_gt_u16 vcc, s34, v71\n\t"
        "ds_add_rtn_u32 v103, v87, v29 offset:32576\n\t"
        "s_mov_b64 exec, -1\n\t"
        "v_mul_u32_u24_sdwa v91, v7, v75 dst_sel:DWORD dst_unused:UNUSED_PAD src0_sel:DWORD src1_sel:BYTE_2\n\t"
        "v_lshlrev_b32_sdwa v123, 1, v75 dst_sel:DWORD dst_unused:UNUSED_PAD src0_sel:DWORD src1_sel:WORD_0\n\t"
        "ds_read_u16 v123, v123 offset:45056\n\t"
        "v_cmp_eq_u16 vcc, s34, v75\n\t"
        "s_cmp_lg_u64 vcc, 0\n\t"
        "s_cselect_b32 s78, 0x10000, 0\n\t"
        "v_or_b32 v30, s78, v30\n\t"
        "v_cmpx_gt_u16 vcc, s34, v75\n\t"
        "ds_add_rtn_u32 v107, v91, v29 offset:32576\n\t"
        "s_mov_b64 exec, -1\n\t"
        "v_mul_u32_u24_sdwa v95, v7, v79 dst_sel:DWORD dst_unused:UNUSED_PAD src0_sel:DWORD src1_sel:BYTE_2\n\t"
        "v_lshlrev_b32_sdwa v127, 1, v79 dst_sel:DWORD dst_unused:UNUSED_PAD src0_sel:DWORD src1_sel:WORD_0\n\t"
        "ds_read_u16 v127, v127 offset:45056\n\t"
        "v_cmp_eq_u16 vcc, s34, v79\n\t"
        "s_cmp_lg_u64 vcc, 0\n\t"
        "s_cselect_b32 s78, 0x10000, 0\n\t"
        "v_or_b32 v30, s78, v30\n\t"
        "v_cmpx_gt_u16 vcc, s34, v79\n\t"
        "ds_add_rtn_u32 v111, v95, v29 offset:32576\n\t"
        "s_mov_b64 exec, -1\n\t"
        "s_branch .L7_y\n"
        ".L7_z:\n\t"
        "s_waitcnt lgkmcnt(0)\n\t"
        "ds_read_b32 v32, v83 offset:32576\n\t"
        "ds_read_b32 v33, v87 offset:32576\n\t"
        "ds_read_b32 v34, v91 offset:32576\n\t"
        "ds_read_b32 v35, v95 offset:32576\n\t"
        "v_bfe_u32 v99, v99, s33, 8\n\t"
        "v_perm_b32 v115, v115, v67, s49\n\t"
        "v_bfe_u32 v103, v103, s33, 8\n\t"
        "v_perm_b32 v119, v119, v71, s49\n\t"
        "v_bfe_u32 v107, v107, s33, 8\n\t"
        "v_perm_b32 v123, v123, v75, s49\n\t"
        "v_bfe_u32 v111, v111, s33, 8\n\t"
        "v_perm_b32 v127, v127, v79, s49\n\t"
        "s_waitcnt lgkmcnt(0)\n\t"
        "v_dot4_u32_u8 v99, v32, s32, v99\n\t"
        "v_dot4_u32_u8 v103, v33, s32, v103\n\t"
        "v_dot4_u32_u8 v107, v34, s32, v107\n\t"
        "v_dot4_u32_u8 v111, v35, s32, v111\n\t"
        "v_min_u32 v99, 47, v99\n\t"
        "v_min_u32 v103, 47, v103\n\t"
        "v_min_u32 v107, 47, v107\n\t"
        "v_min_u32 v111, 47, v111\n\t"
        "v_lshl_add_u32 v99, v99, 2, v83\n\t"
        "v_lshl_add_u32 v103, v103, 2, v87\n\t"
        "v_lshl_add_u32 v107, v107, 2, v91\n\t"
        "v_lshl_add_u32 v111, v111, 2, v95\n\t"
        "v_cmpx_gt_u16 vcc, s34, v67\n\t"
        "ds_write_b32 v99, v115 offset:32384\n\t"
        "s_mov_b64 exec, -1\n\t"
        "v_cmpx_gt_u16 vcc, s34, v71\n\t"
        "ds_write_b32 v103, v119 offset:32384\n\t"
        "s_mov_b64 exec, -1\n\t"
        "v_cmpx_gt_u16 vcc, s34, v75\n\t"
        "ds_write_b32 v107, v123 offset:32384\n\t"
        "s_mov_b64 exec, -1\n\t"
        "v_cmpx_gt_u16 vcc, s34, v79\n\t"
        "ds_write_b32 v111, v127 offset:32384\n\t"
        "s_mov_b64 exec, -1\n\t"
        "s_branch .L7_zr\n"
        ".L7_exit4:\n\t"
        "s_add_u32 s69, s69, 4\n"
        ".L7_exit:\n\t"
        "s_waitcnt vmcnt(0) lgkmcnt(0)\n\t"
        "s_setprio 0\n\t"
        : "+{v0}"(tid), "=&{s69}"(wv)
        : "s"(__builtin_amdgcn_kernarg_segment_ptr()), "s"(blockIdx.x)
        : "scc", "vcc", "memory", "s16", "s17", "s18", "s19", "s20", "s21", "s22", "s23", "s24", "s25", "s26", "s27", "s28", "s29", "s30", "s31", "s32", "s33", "s34", "s35",
          "s36", "s37", "s38", "s39", "s40", "s41", "s42", "s43", "s44", "s45", "s46", "s47", "s48", "s49", "s50", "s51", "s52", "s53", "s54", "s55",
          "s56", "s57", "s58", "s59", "s60", "s61", "s62", "s63", "s64", "s65", "s66", "s67", "s68", "s70", "s71", "s72", "s73", "s74", "s75", "s76",
          "s77", "s78", "s79", "s80", "s81", "s82", "s83", "s84", "s85", "s86", "s87", "s88", "s89", "s90", "s91", "s92", "s93", "s94", "s95", "s96",
          "s97", "s98", "s99", "s100", "s101", "v1", "v2", "v3", "v4", "v5", "v6", "v7", "v8", "v9", "v10", "v11", "v12", "v13", "v14", "v15",
          "v16", "v17", "v18", "v19", "v20", "v21", "v22", "v23", "v24", "v25", "v26", "v27", "v28", "v29", "v30", "v31", "v32", "v33", "v34", "v35",
          "v36", "v37", "v38", "v39", "v40", "v41", "v42", "v43", "v44", "v45", "v46", "v47", "v48", "v49", "v50", "v51", "v52", "v53", "v54", "v55",
          "v56", "v57", "v58", "v59", "v60", "v61", "v62", "v63", "v64", "v65", "v66", "v67", "v68", "v69", "v70", "v71", "v72", "v73", "v74", "v75",
          "v76", "v77", "v78", "v79", "v80", "v81", "v82", "v83", "v84", "v85", "v86", "v87", "v88", "v89", "v90", "v91", "v92", "v93", "v94", "v95",
          "v96", "v97", "v98", "v99", "v100", "v101", "v102", "v103", "v104", "v105", "v106", "v107", "v108", "v109", "v110", "v111", "v112", "v113", "v114", "v115",
          "v116", "v117", "v118", "v119", "v120", "v121", "v122", "v123", "v124", "v125", "v126", "v127");
    const __attribute__((address_space(4))) Args* q = (const __attribute__((address_space(4))) Args*)__builtin_amdgcn_kernarg_segment_ptr();
    asm volatile("" : "+s"(q));
    const u32 wave = __builtin_amdgcn_readfirstlane(wv);
    u32 lane;
    asm volatile("v_mbcnt_lo_u32_b32 %0, -1, 0\n\tv_mbcnt_hi_u32_b32 %0, -1, %0" : "=v"(lane));
    const u32 half = blockIdx.x & 1u, TN = q->TN, r0 = (blockIdx.x >> 1) * TN;
    const u16* x = q->xall;
    _Pragma("nounroll") for (u32 n = 0; n < (1u << 26) && *(volatile __attribute__((address_space(3))) u32*)S_QK < 4u; ++n) __builtin_amdgcn_s_sleep(2);
    if (wave >= 4u) {
        const int E = q->E, *dst = (const int*)q->dst.p, *src = (const int*)q->src.p;
        const u16* dis = q->disall;
        const u32 w = wave - 4u;
        _Pragma("nounroll") for (u32 k = 0; k < 5u; ++k) {
            const u32 grp = (u32)(((w < 2u ? 0x14100e0a06ull : 0x1612080402ull) >> (8u * k)) & 255u) + (w & 1u);
            const u32 row = grp * 4u + (lane >> 4), iv = *(const u32*)(smem + L_INV + row * 4u);
            const int me = (iv & 0x8000u) ? -1 : q->nbase + (int)(r0 + (iv & 255u) + TN - 256u);
            const int m0 = __builtin_amdgcn_readlane(me, 0), m1 = __builtin_amdgcn_readlane(me, 16), m2 = __builtin_amdgcn_readlane(me, 32), m3 = __builtin_amdgcn_readlane(me, 48);
            float s[8] = {0.f, 0.f, 0.f, 0.f, 0.f, 0.f, 0.f, 0.f};
            for (int e0 = 0; e0 < E; e0 += 64) {
                const int e = min(e0 + (int)lane, E - 1), d = dst[e], si = src[e];
                unsigned long long m = __ballot(e0 + (int)lane < E && (d == m0 || d == m1 || d == m2 || d == m3));
                while (m) {
                    const int l = __builtin_ctzll(m);
                    m &= m - 1;
                    const int sl = __builtin_amdgcn_readlane(si, l);
                    if (__builtin_amdgcn_readlane(d, l) == me) {
                        const u32x4 r = *(const u32x4*)(x + (size_t)sl * 256 + half * 128u + (lane & 15u) * 8u);
                        const float wt = bf2f(dis[sl]);
#pragma unroll
                        for (int j = 0; j < 4; ++j) { s[2 * j] += wt * lo(r[j]); s[2 * j + 1] += wt * hi(r[j]); }
                    }
                }
            }
            const float sc = bf2f(dis[max(me, 0)]);
            *(u32x4*)(smem + L_AL + row * 256u + (((lane ^ row) & 15u) << 4)) =
                (u32x4){pack2(s[0] * sc, s[1] * sc), pack2(s[2] * sc, s[3] * sc), pack2(s[4] * sc, s[5] * sc), pack2(s[6] * sc, s[7] * sc)};
            lds_signal(S_T0 + 4u * max(grp >> 2, 1u), 1u);
        }
        return;
    }
    const int Ee = q->Ee, *edst = (const int*)q->edst.p, *esrc = (const int*)q->esrc.p;
    const u16 *wq = (const u16*)q->wq.p, *wk = (const u16*)q->wk.p;
    float* qs = (float*)(smem + L_QK + wave * 1024u);
    _Pragma("nounroll") for (u32 n = wave; n < TN && r0 + n < q->N; n += 4u) {
        const int me = q->nbase + (int)(r0 + n);
        float c0 = 0.f, c1 = 0.f, c2 = 0.f, c3 = 0.f;
        _Pragma("nounroll") for (int i = 0; i < 256; ++i) {
            const float xv = bf2f(x[(size_t)me * 256 + i]);
            const u32x2 r = *(const u32x2*)(wq + i * 256 + lane * 4u);
            c0 += xv * lo(r[0]); c1 += xv * hi(r[0]); c2 += xv * lo(r[1]); c3 += xv * hi(r[1]);
        }
        qs[lane * 4u] = c0; qs[lane * 4u + 1u] = c1; qs[lane * 4u + 2u] = c2; qs[lane * 4u + 3u] = c3;
        float k[4] = {0.f, 0.f, 0.f, 0.f};
        _Pragma("nounroll") for (int c = 0; c < 256; c += 2) {
            const float qa = qs[c], qb = qs[c + 1];
#pragma unroll
            for (int j = 0; j < 4; ++j) { const u32 r = *(const u32*)(wk + (lane * 4u + j) * 256u + c); k[j] += qa * lo(r) + qb * hi(r); }
        }
        float s0 = 0.f, s1 = 0.f, s2 = 0.f, s3 = 0.f, dn = 0.f;
        for (int e0 = 0; e0 < Ee; e0 += 64) {
            const int e = min(e0 + (int)lane, Ee - 1), si = esrc[e];
            unsigned long long m = __ballot(e0 + (int)lane < Ee && edst[e] == me);
            while (m) {
                const int l = __builtin_ctzll(m);
                m &= m - 1;
                const u32x2 r = *(const u32x2*)(x + (size_t)__builtin_amdgcn_readlane(si, l) * 256 + lane * 4u);
                float d = k[0] * lo(r[0]) + k[1] * hi(r[0]) + k[2] * lo(r[1]) + k[3] * hi(r[1]);
#pragma unroll
                for (int o = 1; o < 64; o <<= 1)
                    d += __uint_as_float((u32)__builtin_amdgcn_ds_bpermute((int)((lane ^ (u32)o) * 4u), (int)__float_as_uint(d)));
                const float wt = __builtin_amdgcn_exp2f(d * 0.09016844f);
                s0 += wt * lo(r[0]); s1 += wt * hi(r[0]); s2 += wt * lo(r[1]); s3 += wt * hi(r[1]);
                dn += wt;
            }
        }
        const float sc = __builtin_amdgcn_rcpf(dn + 1e-9f);
        if ((lane >> 5) == half)
            *(u32x2*)(smem + L_AA + ((u32)*(const u16*)(smem + L_MAP + n * 2u) ^ ((lane & 31u) * 8u))) =
                (u32x2){pack2(s0 * sc, s1 * sc), pack2(s2 * sc, s3 * sc)};
    }
    lds_signal(S_Z, 17u);
}

extern "C" void kernel_launch(void* const* d_in, const int* in_sizes, int n_in,
                              void* d_out, int out_size, void* d_ws, size_t ws_size,
                              hipStream_t stream) {
    const u32 N = in_sizes[5], E = in_sizes[1], Ee = in_sizes[3];
    const u16* x = (const u16*)d_in[0];
    const u16* dis = (const u16*)d_in[5];
    const u32 FL = 0x00020000u;
    const bool generic = N > NMAX || E > EMAX || (E & 3u) || (N & 1u);
    const bool irr = N > NMAX || Ee != 4u * N;
    (void)hipFuncSetAttribute((const void*)agg_kernel, hipFuncAttributeMaxDynamicSharedMemorySize, LDS_BYTES);
    for (u32 nbase = 0; nbase < N; nbase += NMAX) {
        const u32 n = N - nbase < NMAX ? N - nbase : NMAX;
        Args a;
        a.x = Desc{x + (size_t)nbase * 256, n * 512u, FL};
        a.ws = Desc{d_ws, WS_END, FL};
        a.wl = Desc{d_in[6], 131072u, FL}; a.wq = Desc{d_in[7], 131072u, FL}; a.wk = Desc{d_in[8], 131072u, FL}; a.wv = Desc{d_in[9], 131072u, FL};
        a.src = Desc{d_in[1], generic ? 0u : E * 4u, FL}; a.dst = Desc{d_in[2], generic ? 0u : E * 4u, FL};
        a.esrc = Desc{d_in[3], irr ? 0u : Ee * 4u, FL}; a.edst = Desc{d_in[4], irr ? 0u : Ee * 4u, FL};
        a.dis = Desc{dis + nbase, n * 2u, FL};
        a.out = Desc{(u16*)d_out + (size_t)nbase * 256, n * 512u, FL};
        a.N = n; a.TN = n > 256u ? (n + 127u) / 128u : 2u; a.M = ((1u << 24) + a.TN - 1u) / a.TN;
        a.irr = (irr ? 1u : 0u) | (generic ? 0x10000u : 0u);
        a.xall = x; a.disall = dis;
        a.E = (int)E; a.Ee = (int)Ee; a.nbase = (int)nbase; a.pad = 0;
        agg_kernel<<<256, 1024, LDS_BYTES, stream>>>(a);
    }
}
```

```cpp
#include <hip/hip_runtime.h>
#include <hip/hip_bf16.h>
#include <stdint.h>

typedef unsigned short u16;
typedef unsigned int u32;
typedef __attribute__((ext_vector_type(4))) u32 u32x4;
typedef __attribute__((ext_vector_type(2))) u32 u32x2;
struct Desc { const void* p; u32 bytes, flags; };
struct Args {
    Desc x, ws, wl, wv, wq, wk, src, dst, esrc, edst, dis, out;
    u32 N, TN, M, irr;
    const u16* xall;
    const u16* disall;
    int E, Ee, nbase, pad;
};
constexpr u32 NMAX = 10240, EMAX = 163840;
constexpr u32 WS_END = 9437184;
constexpr int LDS_BYTES = 163840;
constexpr u32 L_INV = 512, L_MAP = 1792, L_QK = 2048, L_AL = 20480, L_AA = 43008, S_QK = 16, S_Z = 40, S_T0 = 48;

__device__ __forceinline__ float bf2f(u32 h) { return __uint_as_float(h << 16); }
__device__ __forceinline__ float lo(u32 v) { return __uint_as_float(v << 16); }
__device__ __forceinline__ float hi(u32 v) { return __uint_as_float(v & 0xFFFF0000u); }
__device__ __forceinline__ u32 pack2(float a, float b) {
    return (u32)__builtin_bit_cast(u16, (__bf16)a) | ((u32)__builtin_bit_cast(u16, (__bf16)b) << 16);
}
__device__ __forceinline__ void lds_signal(const u32 addr, const u32 v) {
    asm volatile("s_mov_b64 exec, 1\n\tds_add_u32 %0, %1\n\ts_mov_b64 exec, -1" :: "v"(addr), "v"(v) : "memory");
}

extern "C" __global__ void __launch_bounds__(1024, 4)
agg_kernel(const Args a) {
    extern __shared__ __attribute__((aligned(16))) char smem[];
    u32 tid = threadIdx.x, wv;
    asm volatile(
        "s_memrealtime s[74:75]\n\t"
        "s_load_dwordx16 s[16:31], %2, 0\n\t"
        "s_load_dwordx16 s[32:47], %2, 64\n\t"
        "s_load_dwordx16 s[48:63], %2, 128\n\t"
        "s_load_dwordx4 s[64:67], %2, 192\n\t"
        "v_readfirstlane_b32 s69, v0\n\t"
        "v_and_b32 v1, 63, v0\n\t"
        "v_mov_b32 v2, 0\n\t"
        "v_mov_b32 v3, 1\n\t"
        "s_lshr_b32 s76, %3, 3\n\t"
        "s_and_b32 s70, %3, 1\n\t"
        "s_lshr_b32 s71, %3, 1\n\t"
        "s_mov_b32 s83, 0x2000000\n\t"
        "s_lshr_b32 s69, s69, 6\n\t"
        "s_cbranch_scc0 .L7_U\n\t"
        "s_cmp_lt_u32 s69, 8\n\t"
        "s_cbranch_scc1 .L7_L\n\t"
        "s_sub_u32 s69, s69, 8\n\t"
        "v_lshlrev_b32 v4, 2, v1\n\t"
        "s_lshl_b32 s77, s70, 2\n\t"
        "s_xor_b32 s77, s77, s69\n\t"
        "s_xor_b32 s77, s77, 4\n\t"
        "s_mul_i32 s80, s77, 10\n\t"
        "s_waitcnt lgkmcnt(0)\n\t"
        "s_mul_i32 s72, s71, s65\n\t"
        "s_add_u32 s78, s72, s80\n\t"
        "s_lshl_b32 s78, s78, 4\n\t"
        "s_mov_b32 exec_hi, 0xff\n\t"
        "buffer_load_dword v5, v4, s[48:51], s78 offen\n\t"
        "s_mov_b64 exec, -1\n\t"
        "s_branch .L7_O2\n"
        ".L7_U:\n\t"
        "s_setprio 3\n\t"
        "s_cmp_eq_u32 s70, 0\n\t"
        "s_cbranch_scc0 .L7_V\n\t"
        "v_lshrrev_b32 v4, 3, v1\n\t"
        "v_and_b32 v5, 7, v1\n\t"
        "v_lshlrev_b32 v4, 14, v4\n\t"
        "v_lshl_add_u32 v4, v5, 4, v4\n\t"
        "s_lshl_b32 s78, s71, 7\n\t"
        "v_add_u32 v4, s78, v4\n\t"
        "v_add_u32 v5, 0x20000, v4\n\t"
        "v_add_u32 v6, 0x40000, v4\n\t"
        "v_add_u32 v7, 0x60000, v4\n\t"
        "v_add_u32 v8, 0x80000, v4\n\t"
        "s_waitcnt lgkmcnt(0)\n\t"
        "s_mul_i32 s72, s71, s65\n\t"
        "buffer_load_dwordx4 v[16:19], v4, s[44:47], 0 offen nt\n\t"
        "buffer_load_dwordx4 v[20:23], v5, s[44:47], 0 offen nt\n\t"
        "buffer_load_dwordx4 v[24:27], v6, s[44:47], 0 offen nt\n\t"
        "buffer_load_dwordx4 v[28:31], v7, s[44:47], 0 offen nt\n\t"
        "buffer_load_dwordx4 v[32:35], v8, s[44:47], 0 offen nt\n\t"
        "s_buffer_load_dword s68, s[20:23], 0\n\t"
        "buffer_load_dwordx4 v[36:39], v4, s[40:43], 0 offen nt\n\t"
        "buffer_load_dwordx4 v[40:43], v5, s[40:43], 0 offen nt\n\t"
        "buffer_load_dwordx4 v[44:47], v6, s[40:43], 0 offen nt\n\t"
        "buffer_load_dwordx4 v[48:51], v7, s[40:43], 0 offen nt\n\t"
        "buffer_load_dwordx4 v[52:55], v8, s[40:43], 0 offen nt\n\t"
        "s_mul_i32 s80, s71, 0x50\n\t"
        "s_lshl_b32 s78, s80, 4\n\t"
        "v_lshlrev_b32 v12, 4, v1\n\t"
        "buffer_load_dwordx4 v[116:119], v12, s[52:55], s78 offen nt\n\t"
        "s_mov_b64 exec, 0xffff\n\t"
        "buffer_load_dwordx4 v[120:123], v12, s[52:55], s78 offen offset:1024 nt\n\t"
        "s_mov_b64 exec, -1\n\t"
        "s_branch .L7_U2\n"
        ".L7_V:\n\t"
        "s_and_b32 s78, s71, 1\n\t"
        "s_and_b32 s79, s76, 3\n\t"
        "s_lshl2_add_u32 s80, s78, s79\n\t"
        "s_and_b32 s78, s71, 2\n\t"
        "s_lshr_b32 s79, s76, 2\n\t"
        "s_lshl2_add_u32 s81, s78, s79\n\t"
        "v_lshlrev_b32 v12, 4, v1\n\t"
        "v_and_b32 v5, 0x200, v12\n\t"
        "v_mad_u32_u24 v4, v5, 15, v12\n\t"
        "s_lshl_b32 s82, s80, 14\n\t"
        "s_lshr_b32 s78, s81, 1\n\t"
        "s_lshl_b32 s78, s78, 14\n\t"
        "s_and_b32 s79, s81, 1\n\t"
        "s_lshl_b32 s79, s79, 11\n\t"
        "s_add_u32 s77, s78, s79\n\t"
        "s_mov_b32 m0, 83968\n\t"
        "s_waitcnt lgkmcnt(0)\n\t"
        "s_mul_i32 s72, s71, s65\n\t"
        "s_buffer_load_dword s68, s[20:23], 0\n\t"
        "buffer_load_dwordx4 v4, s[36:39], s77 offen offset:0 lds\n\t"
        "s_add_u32 m0, m0, 544\n\t"
        "s_nop 0\n\t"
        "buffer_load_dwordx4 v4, s[36:39], s77 offen offset:512 lds\n\t"
        "s_add_u32 m0, m0, 544\n\t"
        "s_nop 0\n\t"
        "buffer_load_dwordx4 v4, s[36:39], s77 offen offset:1024 lds\n\t"
        "s_add_u32 m0, m0, 544\n\t"
        "s_nop 0\n\t"
        "buffer_load_dwordx4 v4, s[36:39], s77 offen offset:1536 lds\n\t"
        "s_add_u32 m0, m0, 2592\n\t"
        "s_add_u32 s77, s77, 0x1000\n\t"
        "buffer_load_dwordx4 v4, s[36:39], s77 offen offset:0 lds\n\t"
        "s_add_u32 m0, m0, 544\n\t"
        "s_nop 0\n\t"
        "buffer_load_dwordx4 v4, s[36:39], s77 offen offset:512 lds\n\t"
        "s_add_u32 m0, m0, 544\n\t"
        "s_nop 0\n\t"
        "buffer_load_dwordx4 v4, s[36:39], s77 offen offset:1024 lds\n\t"
        "s_add_u32 m0, m0, 544\n\t"
        "s_nop 0\n\t"
        "buffer_load_dwordx4 v4, s[36:39], s77 offen offset:1536 lds\n\t"
        "s_add_u32 m0, m0, 2592\n\t"
        "s_nop 0\n\t"
        "buffer_load_dwordx4 v4, s[32:35], s82 offen offset:0 lds\n\t"
        "s_add_u32 m0, m0, 544\n\t"
        "s_nop 0\n\t"
        "buffer_load_dwordx4 v4, s[32:35], s82 offen offset:512 lds\n\t"
        "s_add_u32 m0, m0, 544\n\t"
        "s_nop 0\n\t"
        "buffer_load_dwordx4 v4, s[32:35], s82 offen offset:1024 lds\n\t"
        "s_add_u32 m0, m0, 544\n\t"
        "s_nop 0\n\t"
        "buffer_load_dwordx4 v4, s[32:35], s82 offen offset:1536 lds\n\t"
        "s_add_u32 m0, m0, 2592\n\t"
        "s_add_u32 s82, s82, 0x1000\n\t"
        "buffer_load_dwordx4 v4, s[32:35], s82 offen offset:0 lds\n\t"
        "s_add_u32 m0, m0, 544\n\t"
        "s_nop 0\n\t"
        "buffer_load_dwordx4 v4, s[32:35], s82 offen offset:512 lds\n\t"
        "s_add_u32 m0, m0, 544\n\t"
        "s_nop 0\n\t"
        "buffer_load_dwordx4 v4, s[32:35], s82 offen offset:1024 lds\n\t"
        "s_add_u32 m0, m0, 544\n\t"
        "s_nop 0\n\t"
        "buffer_load_dwordx4 v4, s[32:35], s82 offen offset:1536 lds\n\t"
        "s_add_u32 m0, m0, 2592\n\t"
        "s_sub_u32 s82, s82, 0x800\n\t"
        "buffer_load_dwordx4 v4, s[32:35], s82 offen offset:0 lds\n\t"
        "s_add_u32 m0, m0, 544\n\t"
        "s_nop 0\n\t"
        "buffer_load_dwordx4 v4, s[32:35], s82 offen offset:512 lds\n\t"
        "s_add_u32 m0, m0, 544\n\t"
        "s_nop 0\n\t"
        "buffer_load_dwordx4 v4, s[32:35], s82 offen offset:1024 lds\n\t"
        "s_add_u32 m0, m0, 544\n\t"
        "s_nop 0\n\t"
        "buffer_load_dwordx4 v4, s[32:35], s82 offen offset:1536 lds\n\t"
        "s_add_u32 m0, m0, 2592\n\t"
        "s_add_u32 s82, s82, 0x1000\n\t"
        "buffer_load_dwordx4 v4, s[32:35], s82 offen offset:0 lds\n\t"
        "s_add_u32 m0, m0, 544\n\t"
        "s_nop 0\n\t"
        "buffer_load_dwordx4 v4, s[32:35], s82 offen offset:512 lds\n\t"
        "s_add_u32 m0, m0, 544\n\t"
        "s_nop 0\n\t"
        "buffer_load_dwordx4 v4, s[32:35], s82 offen offset:1024 lds\n\t"
        "s_add_u32 m0, m0, 544\n\t"
        "s_nop 0\n\t"
        "buffer_load_dwordx4 v4, s[32:35], s82 offen offset:1536 lds\n\t"
        "s_branch .L7_V2\n"
        ".L7_L:\n\t"
        "s_cmp_lt_u32 s69, 4\n\t"
        "s_cbranch_scc0 .L7_G\n\t"
        "s_cmp_eq_u32 s69, 2\n\t"
        "s_cbranch_scc1 .L7_P2\n\t"
        "s_cmp_eq_u32 s69, 1\n\t"
        "s_cbranch_scc0 .L7_T\n\t"
        "v_lshlrev_b32 v4, 3, v1\n\t"
        "v_mov_b64 v[8:9], 0\n\t"
        "ds_write_b64 v4, v[8:9]\n\t"
        "s_waitcnt lgkmcnt(0)\n\t"
        "s_mul_i32 s72, s71, s65\n\t"
        "v_add_lshl_u32 v4, v1, s72, 1\n\t"
        "buffer_load_ushort v16, v4, s[56:59], 0 offen\n\t"
        "buffer_load_ushort v17, v4, s[56:59], 0 offen offset:128\n\t"
        "s_branch .L7_R2\n"
        ".L7_T:\n\t"
        "v_lshlrev_b32 v6, 7, v1\n\t"
        "s_mov_b32 m0, 149504\n\t"
        "s_lshl_b32 s78, s70, 16\n\t"
        "s_lshl_b32 s79, s76, 11\n\t"
        "s_add_u32 s80, s78, s79\n\t"
        "s_mul_i32 s81, s76, 0x300\n\t"
        "s_lshl_b32 s82, %3, 7\n\t"
        "s_add_u32 s82, s82, 4096\n\t"
        "v_lshlrev_b32 v8, 2, v1\n\t"
        "s_waitcnt lgkmcnt(0)\n\t"
        "s_mul_i32 s72, s71, s65\n\t"
        "v_mov_b32 v7, s74\n\t"
        "s_mov_b32 exec_hi, 0\n\t"
        "buffer_store_dword v7, v8, s[20:23], s82 offen sc1\n\t"
        "s_getpc_b64 s[32:33]\n\t"
        "s_add_u32 s32, s32, agg_kernel@rel32@lo+4\n\t"
        "s_addc_u32 s33, s33, agg_kernel@rel32@hi+12\n\t"
        "s_and_b32 s33, s33, 0xffff\n\t"
        "s_mov_b32 s34, .L7_x-agg_kernel\n\t"
        "s_mov_b64 exec, 63\n\t"
        "buffer_load_dword v6, s[32:35], s81 offen lds\n\t"
        "s_barrier\n\t"
        "s_mov_b64 exec, -1\n\t"
        "s_mov_b64 exec, 0xffff\n\t"
        "buffer_load_dword v6, s[28:31], s80 offen lds\n\t"
        "buffer_load_dword v6, s[24:27], s80 offen lds\n\t"
        "s_mov_b64 exec, -1\n\t"
        "s_branch .L7_T2\n"
        ".L7_G:\n\t"
        "s_sub_u32 s69, s69, 4\n\t"
        "v_lshlrev_b32 v4, 4, v1\n\t"
        "v_and_b32 v5, 0x200, v4\n\t"
        "v_mad_u32_u24 v4, v5, 7, v4\n\t"
        "s_mul_i32 s80, s70, 5\n\t"
        "s_mul_i32 s81, s69, 0x420\n\t"
        "s_add_u32 s81, s81, 2048\n\t"
        "s_mul_i32 s78, s80, 0x1080\n\t"
        "s_add_u32 m0, s78, s81\n\t"
        "s_lshr_b32 s78, s80, 1\n\t"
        "s_lshl1_add_u32 s78, s78, s80\n\t"
        "s_lshl_b32 s78, s78, 11\n\t"
        "s_waitcnt lgkmcnt(0)\n\t"
        "s_mul_i32 s72, s71, s65\n\t"
        "s_add_u32 s79, s72, s69\n\t"
        "s_lshl_b32 s79, s79, 9\n\t"
        "v_add_u32 v4, s79, v4\n\t"
        "buffer_load_dwordx4 v4, s[16:19], s78 offen lds\n\t"
        "s_add_u32 s80, s80, 1\n\t"
        "s_mul_i32 s78, s80, 0x1080\n\t"
        "s_add_u32 m0, s78, s81\n\t"
        "s_lshr_b32 s78, s80, 1\n\t"
        "s_lshl1_add_u32 s78, s78, s80\n\t"
        "s_lshl_b32 s78, s78, 11\n\t"
        "buffer_load_dwordx4 v4, s[16:19], s78 offen lds\n\t"
        "s_add_u32 s80, s80, 1\n\t"
        "s_mul_i32 s78, s80, 0x1080\n\t"
        "s_add_u32 m0, s78, s81\n\t"
        "s_lshr_b32 s78, s80, 1\n\t"
        "s_lshl1_add_u32 s78, s78, s80\n\t"
        "s_lshl_b32 s78, s78, 11\n\t"
        "buffer_load_dwordx4 v4, s[16:19], s78 offen lds\n\t"
        "s_add_u32 s80, s80, 1\n\t"
        "s_mul_i32 s78, s80, 0x1080\n\t"
        "s_add_u32 m0, s78, s81\n\t"
        "s_lshr_b32 s78, s80, 1\n\t"
        "s_lshl1_add_u32 s78, s78, s80\n\t"
        "s_lshl_b32 s78, s78, 11\n\t"
        "buffer_load_dwordx4 v4, s[16:19], s78 offen lds\n\t"
        "s_add_u32 s80, s80, 1\n\t"
        "s_mul_i32 s78, s80, 0x1080\n\t"
        "s_add_u32 m0, s78, s81\n\t"
        "s_lshr_b32 s78, s80, 1\n\t"
        "s_lshl1_add_u32 s78, s78, s80\n\t"
        "s_lshl_b32 s78, s78, 11\n\t"
        "buffer_load_dwordx4 v4, s[16:19], s78 offen lds\n\t"
        "s_add_u32 s80, s80, 1\n\t"
        "s_branch .L7_G2\n"
        ".L7_O2:\n\t"
        "s_barrier\n\t"
        "v_lshrrev_b32 v6, 4, v1\n\t"
        "v_lshlrev_b32 v7, 2, v6\n\t"
        "v_and_b32 v8, 15, v1\n\t"
        "s_lshl_b32 s78, s70, 8\n\t"
        "v_lshl_add_u32 v9, v8, 4, s78\n\t"
        "v_lshrrev_b32 v10, 3, v1\n\t"
        "v_lshlrev_b32 v10, 9, v10\n\t"
        "v_and_b32 v11, 7, v1\n\t"
        "v_lshl_add_u32 v10, v11, 4, v10\n\t"
        "v_and_b32 v11, 16, v1\n\t"
        "v_lshlrev_b32 v11, 2, v11\n\t"
        "v_xor_b32 v10, v10, v11\n\t"
        "s_add_u32 s81, s69, s76\n\t"
        "s_and_b32 s81, s81, 7\n\t"
        "s_lshr_b32 s78, s81, 1\n\t"
        "s_lshl_b32 s78, s78, 7\n\t"
        "s_and_b32 s79, s81, 1\n\t"
        "s_lshl_b32 s79, s79, 15\n\t"
        "s_add_u32 s78, s78, s79\n\t"
        "s_lshl_b32 s79, s70, 16\n\t"
        "s_add_u32 s78, s78, s79\n\t"
        "v_add_u32 v10, s78, v10\n\t"
        "s_lshl_b32 s82, s81, 13\n\t"
        "s_add_u32 s82, s82, 83968\n\t"
        "v_bfe_u32 v12, v1, 2, 2\n\t"
        "v_lshlrev_b32 v11, 10, v6\n\t"
        "v_lshl_add_u32 v11, v12, 7, v11\n\t"
        "v_lshrrev_b32 v12, 1, v12\n\t"
        "s_and_b32 s78, s81, 1\n\t"
        "v_xor_b32 v12, s78, v12\n\t"
        "v_lshl_add_u32 v11, v12, 6, v11\n\t"
        "v_and_b32 v12, 3, v1\n\t"
        "v_lshl_add_u32 v11, v12, 4, v11\n\t"
        "s_lshr_b32 s78, s81, 1\n\t"
        "s_lshl_b32 s78, s78, 14\n\t"
        "s_add_u32 s78, s78, 83968\n\t"
        "v_add_u32 v23, s78, v11\n\t"
        "s_waitcnt vmcnt(0)\n\t"
        "ds_bpermute_b32 v12, v7, v5 offset:0\n\t"
        "ds_bpermute_b32 v13, v7, v5 offset:16\n\t"
        "ds_bpermute_b32 v14, v7, v5 offset:32\n\t"
        "ds_bpermute_b32 v15, v7, v5 offset:48\n\t"
        "ds_bpermute_b32 v16, v7, v5 offset:64\n\t"
        "ds_bpermute_b32 v17, v7, v5 offset:80\n\t"
        "ds_bpermute_b32 v18, v7, v5 offset:96\n\t"
        "ds_bpermute_b32 v19, v7, v5 offset:112\n\t"
        "ds_bpermute_b32 v20, v7, v5 offset:128\n\t"
        "ds_bpermute_b32 v21, v7, v5 offset:144\n\t"
        "s_waitcnt lgkmcnt(0)\n\t"
        "v_lshl_add_u32 v12, v12, 9, v9\n\t"
        "v_lshl_add_u32 v13, v13, 9, v9\n\t"
        "v_lshl_add_u32 v14, v14, 9, v9\n\t"
        "v_lshl_add_u32 v15, v15, 9, v9\n\t"
        "v_lshl_add_u32 v16, v16, 9, v9\n\t"
        "v_lshl_add_u32 v17, v17, 9, v9\n\t"
        "v_lshl_add_u32 v18, v18, 9, v9\n\t"
        "v_lshl_add_u32 v19, v19, 9, v9\n\t"
        "v_lshl_add_u32 v20, v20, 9, v9\n\t"
        "v_lshl_add_u32 v21, v21, 9, v9\n\t"
        "s_setprio 0\n"
        ".L7_w1:\n\t"
        "ds_read_b32 v0, v2 offset:44\n\t"
        "s_waitcnt lgkmcnt(0)\n\t"
        "v_cmp_gt_u16 vcc, 1, v0\n\t"
        "s_cbranch_vccz .L7_d2\n\t"
        "s_sleep 1\n\t"
        "s_sub_i32 s83, s83, 1\n\t"
        "s_cmp_gt_i32 s83, 0\n\t"
        "s_cbranch_scc1 .L7_w1\n"
        ".L7_d2:\n"
        ".L7_w3:\n\t"
        "ds_read_b64 v[4:5], v2 offset:72\n\t"
        "s_waitcnt lgkmcnt(0)\n\t"
        "v_cmp_gt_u16 vcc, 1, v4\n\t"
        "s_cbranch_vccz .L7_d4\n\t"
        "s_sleep 1\n\t"
        "s_sub_i32 s83, s83, 1\n\t"
        "s_cmp_gt_i32 s83, 0\n\t"
        "s_cbranch_scc1 .L7_w3\n"
        ".L7_d4:\n\t"
        "v_readfirstlane_b32 s68, v5\n\t"
        "s_add_u32 m0, s82, 0\n\t"
        "s_mov_b32 s78, 0\n\t"
        "buffer_load_dwordx4 v10, s[28:31], s78 offen lds\n\t"
        "s_add_u32 m0, m0, 0x400\n\t"
        "s_add_u32 s78, s78, 0x1000\n\t"
        "buffer_load_dwordx4 v10, s[28:31], s78 offen lds\n\t"
        "s_add_u32 m0, m0, 0x400\n\t"
        "s_add_u32 s78, s78, 0x1000\n\t"
        "buffer_load_dwordx4 v10, s[28:31], s78 offen lds\n\t"
        "s_add_u32 m0, m0, 0x400\n\t"
        "s_add_u32 s78, s78, 0x1000\n\t"
        "buffer_load_dwordx4 v10, s[28:31], s78 offen lds\n\t"
        "s_add_u32 m0, m0, 0x400\n\t"
        "s_add_u32 s78, s78, 0x1000\n\t"
        "buffer_load_dwordx4 v10, s[28:31], s78 offen lds\n\t"
        "s_add_u32 m0, m0, 0x400\n\t"
        "s_add_u32 s78, s78, 0x1000\n\t"
        "buffer_load_dwordx4 v10, s[28:31], s78 offen lds\n\t"
        "s_add_u32 m0, m0, 0x400\n\t"
        "s_add_u32 s78, s78, 0x1000\n\t"
        "buffer_load_dwordx4 v10, s[28:31], s78 offen lds\n\t"
        "s_add_u32 m0, m0, 0x400\n\t"
        "s_add_u32 s78, s78, 0x1000\n\t"
        "buffer_load_dwordx4 v10, s[28:31], s78 offen lds\n\t"
        "s_mov_b64 exec, 1\n\t"
        "ds_add_u32 v2, v3 offset:88\n\t"
        "s_mov_b64 exec, -1\n"
        ".L7_w5:\n\t"
        "ds_read_b32 v0, v2 offset:84\n\t"
        "s_waitcnt lgkmcnt(0)\n\t"
        "v_cmp_gt_u16 vcc, 4, v0\n\t"
        "s_cbranch_vccz .L7_d6\n\t"
        "s_sleep 1\n\t"
        "s_sub_i32 s83, s83, 1\n\t"
        "s_cmp_gt_i32 s83, 0\n\t"
        "s_cbranch_scc1 .L7_w5\n"
        ".L7_d6:\n\t"
        "buffer_load_dwordx4 v[24:27], v12, s[16:19], 0 offen\n\t"
        "buffer_load_dwordx4 v[28:31], v13, s[16:19], 0 offen\n\t"
        "buffer_load_dwordx4 v[32:35], v14, s[16:19], 0 offen\n\t"
        "buffer_load_dwordx4 v[36:39], v15, s[16:19], 0 offen\n\t"
        "buffer_load_dwordx4 v[40:43], v16, s[16:19], 0 offen\n\t"
        "s_waitcnt vmcnt(5)\n\t"
        "s_mov_b64 exec, 1\n\t"
        "ds_add_u32 v2, v3 offset:20\n\t"
        "s_mov_b64 exec, -1\n"
        ".L7_w7:\n\t"
        "ds_read_b32 v0, v2 offset:20\n\t"
        "s_waitcnt lgkmcnt(0)\n\t"
        "v_cmp_gt_u16 vcc, 8, v0\n\t"
        "s_cbranch_vccz .L7_d8\n\t"
        "s_sleep 1\n\t"
        "s_sub_i32 s83, s83, 1\n\t"
        "s_cmp_gt_i32 s83, 0\n\t"
        "s_cbranch_scc1 .L7_w7\n"
        ".L7_d8:\n\t"
        "ds_read_b64_tr_b16 v[64:65], v23 offset:0\n\t"
        "ds_read_b64_tr_b16 v[66:67], v23 offset:512\n\t"
        "ds_read_b64_tr_b16 v[68:69], v23 offset:8\n\t"
        "ds_read_b64_tr_b16 v[70:71], v23 offset:520\n\t"
        "ds_read_b64_tr_b16 v[72:73], v23 offset:4096\n\t"
        "ds_read_b64_tr_b16 v[74:75], v23 offset:4608\n\t"
        "ds_read_b64_tr_b16 v[76:77], v23 offset:4104\n\t"
        "ds_read_b64_tr_b16 v[78:79], v23 offset:4616\n\t"
        "ds_read_b64_tr_b16 v[80:81], v23 offset:8192\n\t"
        "ds_read_b64_tr_b16 v[82:83], v23 offset:8704\n\t"
        "ds_read_b64_tr_b16 v[84:85], v23 offset:8200\n\t"
        "ds_read_b64_tr_b16 v[86:87], v23 offset:8712\n\t"
        "ds_read_b64_tr_b16 v[88:89], v23 offset:12288\n\t"
        "ds_read_b64_tr_b16 v[90:91], v23 offset:12800\n\t"
        "s_waitcnt lgkmcnt(10)\n\t"
        "ds_read_b64_tr_b16 v[92:93], v23 offset:12296\n\t"
        "ds_read_b64_tr_b16 v[94:95], v23 offset:12808\n\t"
        "s_mov_b64 exec, 1\n\t"
        "ds_add_u32 v2, v3 offset:24\n\t"
        "s_mov_b64 exec, -1\n\t"
        "buffer_load_dwordx4 v[44:47], v17, s[16:19], 0 offen\n\t"
        "buffer_load_dwordx4 v[48:51], v18, s[16:19], 0 offen\n\t"
        "buffer_load_dwordx4 v[52:55], v19, s[16:19], 0 offen\n\t"
        "buffer_load_dwordx4 v[56:59], v20, s[16:19], 0 offen\n\t"
        "buffer_load_dwordx4 v[60:63], v21, s[16:19], 0 offen\n\t"
        "s_lshl_b32 s78, s80, 4\n\t"
        "v_lshlrev_b32 v8, 4, v8\n\t"
        "s_add_u32 s79, s78, 0\n\t"
        "s_and_b32 s79, s79, 0xf0\n\t"
        "v_xor_b32 v12, s79, v8\n\t"
        "s_add_u32 s79, s78, 16\n\t"
        "s_and_b32 s79, s79, 0xf0\n\t"
        "v_xor_b32 v13, s79, v8\n\t"
        "s_add_u32 s79, s78, 32\n\t"
        "s_and_b32 s79, s79, 0xf0\n\t"
        "v_xor_b32 v14, s79, v8\n\t"
        "s_add_u32 s79, s78, 48\n\t"
        "s_and_b32 s79, s79, 0xf0\n\t"
        "v_xor_b32 v15, s79, v8\n\t"
        "s_add_u32 s79, s78, 64\n\t"
        "s_and_b32 s79, s79, 0xf0\n\t"
        "v_xor_b32 v16, s79, v8\n\t"
        "s_add_u32 s79, s78, 80\n\t"
        "s_and_b32 s79, s79, 0xf0\n\t"
        "v_xor_b32 v17, s79, v8\n\t"
        "s_add_u32 s79, s78, 96\n\t"
        "s_and_b32 s79, s79, 0xf0\n\t"
        "v_xor_b32 v18, s79, v8\n\t"
        "s_add_u32 s79, s78, 112\n\t"
        "s_and_b32 s79, s79, 0xf0\n\t"
        "v_xor_b32 v19, s79, v8\n\t"
        "s_add_u32 s79, s78, 128\n\t"
        "s_and_b32 s79, s79, 0xf0\n\t"
        "v_xor_b32 v20, s79, v8\n\t"
        "s_add_u32 s79, s78, 144\n\t"
        "s_and_b32 s79, s79, 0xf0\n\t"
        "v_xor_b32 v21, s79, v8\n\t"
        "s_lshl_b32 s78, s80, 8\n\t"
        "v_add_u32 v12, s78, v12\n\t"
        "v_add_u32 v13, s78, v13\n\t"
        "v_add_u32 v14, s78, v14\n\t"
        "v_add_u32 v15, s78, v15\n\t"
        "v_add_u32 v16, s78, v16\n\t"
        "v_add_u32 v17, s78, v17\n\t"
        "v_add_u32 v18, s78, v18\n\t"
        "v_add_u32 v19, s78, v19\n\t"
        "v_add_u32 v20, s78, v20\n\t"
        "v_add_u32 v21, s78, v21\n"
        ".L7_o2:\n\t"
        "s_setprio 2\n\t"
        "s_mov_b32 s36, 0x22222222\n\t"
        "s_mov_b32 s37, s36\n\t"
        "s_lshl_b64 s[38:39], s[36:37], 1\n\t"
        "s_lshl_b64 s[40:41], s[36:37], 2\n\t"
        "s_and_b32 s44, s68, 0xfff\n\t"
        "s_xor_b32 s45, s44, 0x5a5\n\t"
        "s_lshr_b32 s46, s68, 12\n\t"
        "s_lshr_b32 s47, s68, 20\n\t"
        "s_add_u32 s46, s46, s68\n\t"
        "s_and_b32 s46, s46, 0xfff\n\t"
        "s_add_u32 s47, s47, s68\n\t"
        "s_and_b32 s47, s47, 0xfff\n\t"
        "v_mov_b32 v22, s44\n\t"
        "v_mov_b32 v0, s45\n\t"
        "v_cndmask_b32 v22, v22, v0, s[36:37]\n\t"
        "v_mov_b32 v0, s46\n\t"
        "v_cndmask_b32 v22, v22, v0, s[38:39]\n\t"
        "v_mov_b32 v0, s47\n\t"
        "v_cndmask_b32 v22, v22, v0, s[40:41]\n\t"
        "s_lshl3_add_u32 s42, %3, s77\n\t"
        "s_lshl_b32 s42, s42, 8\n\t"
        "s_add_u32 s42, s42, 262144\n\t"
        "s_xor_b32 s43, s42, 0x800\n\t"
        "s_movk_i32 s48, 0xfff\n"
        ".L7_w9:\n\t"
        "ds_read_b32 v0, v2 offset:16\n\t"
        "s_waitcnt lgkmcnt(0)\n\t"
        "v_cmp_gt_u16 vcc, 4, v0\n\t"
        "s_cbranch_vccz .L7_d10\n\t"
        "s_sleep 1\n\t"
        "s_sub_i32 s83, s83, 1\n\t"
        "s_cmp_gt_i32 s83, 0\n\t"
        "s_cbranch_scc1 .L7_w9\n"
        ".L7_d10:\n\t"
        "ds_read_b128 v[96:99], v12 offset:2048\n\t"
        "ds_read_b128 v[100:103], v13 offset:2304\n\t"
        "ds_read_b128 v[104:107], v14 offset:2560\n\t"
        "ds_read_b128 v[108:111], v15 offset:2816\n\t"
        "ds_read_b128 v[112:115], v16 offset:3072\n\t"
        "ds_read_b128 v[116:119], v17 offset:3328\n\t"
        "ds_read_b128 v[120:123], v18 offset:3584\n\t"
        "ds_read_b128 v[124:127], v19 offset:3840\n\t"
        "ds_read_b128 v[4:7], v20 offset:4096\n\t"
        "ds_read_b128 v[8:11], v21 offset:4352\n\t"
        "s_waitcnt vmcnt(0)\n\t"
        "s_waitcnt lgkmcnt(8)\n\t"
        "v_dot2_f32_bf16 v12, v24, v96, 0\n\t"
        "v_dot2_f32_bf16 v13, v28, v100, 0\n\t"
        "v_dot2_f32_bf16 v12, v25, v97, v12\n\t"
        "v_dot2_f32_bf16 v13, v29, v101, v13\n\t"
        "v_dot2_f32_bf16 v12, v26, v98, v12\n\t"
        "v_dot2_f32_bf16 v13, v30, v102, v13\n\t"
        "v_dot2_f32_bf16 v12, v27, v99, v12\n\t"
        "v_dot2_f32_bf16 v13, v31, v103, v13\n\t"
        "s_waitcnt lgkmcnt(6)\n\t"
        "v_dot2_f32_bf16 v14, v32, v104, 0\n\t"
        "v_dot2_f32_bf16 v15, v36, v108, 0\n\t"
        "v_dot2_f32_bf16 v14, v33, v105, v14\n\t"
        "v_dot2_f32_bf16 v15, v37, v109, v15\n\t"
        "v_dot2_f32_bf16 v14, v34, v106, v14\n\t"
        "v_dot2_f32_bf16 v15, v38, v110, v15\n\t"
        "v_dot2_f32_bf16 v14, v35, v107, v14\n\t"
        "v_dot2_f32_bf16 v15, v39, v111, v15\n\t"
        "s_waitcnt lgkmcnt(4)\n\t"
        "v_dot2_f32_bf16 v16, v40, v112, 0\n\t"
        "v_dot2_f32_bf16 v17, v44, v116, 0\n\t"
        "v_dot2_f32_bf16 v16, v41, v113, v16\n\t"
        "v_dot2_f32_bf16 v17, v45, v117, v17\n\t"
        "v_dot2_f32_bf16 v16, v42, v114, v16\n\t"
        "v_dot2_f32_bf16 v17, v46, v118, v17\n\t"
        "v_dot2_f32_bf16 v16, v43, v115, v16\n\t"
        "v_dot2_f32_bf16 v17, v47, v119, v17\n\t"
        "s_waitcnt lgkmcnt(2)\n\t"
        "v_dot2_f32_bf16 v18, v48, v120, 0\n\t"
        "v_dot2_f32_bf16 v19, v52, v124, 0\n\t"
        "v_dot2_f32_bf16 v18, v49, v121, v18\n\t"
        "v_dot2_f32_bf16 v19, v53, v125, v19\n\t"
        "v_dot2_f32_bf16 v18, v50, v122, v18\n\t"
        "v_dot2_f32_bf16 v19, v54, v126, v19\n\t"
        "v_dot2_f32_bf16 v18, v51, v123, v18\n\t"
        "v_dot2_f32_bf16 v19, v55, v127, v19\n\t"
        "s_waitcnt lgkmcnt(0)\n\t"
        "v_dot2_f32_bf16 v20, v56, v4, 0\n\t"
        "v_dot2_f32_bf16 v21, v60, v8, 0\n\t"
        "v_dot2_f32_bf16 v20, v57, v5, v20\n\t"
        "v_dot2_f32_bf16 v21, v61, v9, v21\n\t"
        "v_dot2_f32_bf16 v20, v58, v6, v20\n\t"
        "v_dot2_f32_bf16 v21, v62, v10, v21\n\t"
        "v_dot2_f32_bf16 v20, v59, v7, v20\n\t"
        "v_dot2_f32_bf16 v21, v63, v11, v21\n\t"
        "v_add_f32_dpp v12, v12, v12 row_ror:8 row_mask:0xf bank_mask:0x3\n\t"
        "v_add_f32_dpp v14, v14, v14 row_ror:8 row_mask:0xf bank_mask:0x3\n\t"
        "v_add_f32_dpp v16, v16, v16 row_ror:8 row_mask:0xf bank_mask:0x3\n\t"
        "v_add_f32_dpp v18, v18, v18 row_ror:8 row_mask:0xf bank_mask:0x3\n\t"
        "v_add_f32_dpp v20, v20, v20 row_ror:8 row_mask:0xf bank_mask:0x3\n\t"
        "v_add_f32_dpp v12, v13, v13 row_ror:8 row_mask:0xf bank_mask:0xc\n\t"
        "v_add_f32_dpp v14, v15, v15 row_ror:8 row_mask:0xf bank_mask:0xc\n\t"
        "v_add_f32_dpp v16, v17, v17 row_ror:8 row_mask:0xf bank_mask:0xc\n\t"
        "v_add_f32_dpp v18, v19, v19 row_ror:8 row_mask:0xf bank_mask:0xc\n\t"
        "v_add_f32_dpp v20, v21, v21 row_ror:8 row_mask:0xf bank_mask:0xc\n\t"
        "v_add_f32_dpp v12, v12, v12 row_half_mirror row_mask:0xf bank_mask:0x5\n\t"
        "v_add_f32_dpp v16, v16, v16 row_half_mirror row_mask:0xf bank_mask:0x5\n\t"
        "v_add_f32_dpp v20, v20, v20 row_half_mirror row_mask:0xf bank_mask:0xf\n\t"
        "v_add_f32_dpp v12, v14, v14 row_half_mirror row_mask:0xf bank_mask:0xa\n\t"
        "v_add_f32_dpp v16, v18, v18 row_half_mirror row_mask:0xf bank_mask:0xa\n\t"
        "v_add_f32_dpp v20, v20, v20 quad_perm:[1,0,3,2] row_mask:0xf bank_mask:0xf\n\t"
        "v_add_f32_dpp v12, v12, v12 quad_perm:[1,0,3,2] row_mask:0xf bank_mask:0xf\n\t"
        "v_add_f32_dpp v16, v16, v16 quad_perm:[1,0,3,2] row_mask:0xf bank_mask:0xf\n\t"
        "v_add_f32_dpp v20, v20, v20 quad_perm:[2,3,0,1] row_mask:0xf bank_mask:0xf\n\t"
        "v_add_f32_dpp v12, v12, v12 quad_perm:[2,3,0,1] row_mask:0xf bank_mask:0xf\n\t"
        "v_add_f32_dpp v16, v16, v16 quad_perm:[2,3,0,1] row_mask:0xf bank_mask:0xf\n\t"
        "v_cndmask_b32 v12, v12, v16, s[36:37]\n\t"
        "v_cndmask_b32 v12, v12, v20, s[38:39]\n\t"
        "v_add_u32 v12, 0x800, v12\n\t"
        "v_bfi_b32 v21, s48, v22, v12\n\t"
        "v_lshlrev_b32 v13, 2, v1\n\t"
        "s_setprio 3\n\t"
        "buffer_store_dword v21, v13, s[20:23], s42 offen sc1\n\t"
        "s_setprio 2\n\t"
        "v_and_b32 v14, 3, v1\n\t"
        "v_lshlrev_b32 v14, 4, v14\n\t"
        "s_mov_b32 s78, 0xffff\n\t"
        "s_mov_b32 s79, 0\n\t"
        "v_lshlrev_b64 v[4:5], v14, s[78:79]\n\t"
        "s_mov_b32 s50, 0x0f0f0f0f\n\t"
        "s_mov_b32 s51, s50\n\t"
        "v_cndmask_b32 v6, v4, 0, s[50:51]\n\t"
        "v_cndmask_b32 v7, v5, 0, s[50:51]\n\t"
        "v_cndmask_b32 v4, 0, v4, s[50:51]\n\t"
        "v_cndmask_b32 v5, 0, v5, s[50:51]\n\t"
        "v_lshrrev_b32 v14, 5, v1\n\t"
        "v_add_lshl_u32 v14, v14, s80, 1\n"
        ".L7_w11:\n\t"
        "ds_read_b32 v0, v2 offset:8\n\t"
        "ds_read_u16 v16, v14 offset:1792\n\t"
        "ds_read_u16 v17, v14 offset:1796\n\t"
        "ds_read_u16 v18, v14 offset:1800\n\t"
        "ds_read_u16 v19, v14 offset:1804\n\t"
        "ds_read_u16 v20, v14 offset:1808\n\t"
        "s_waitcnt lgkmcnt(5)\n\t"
        "v_cmp_gt_u16 vcc, 2, v0\n\t"
        "s_cbranch_vccz .L7_d12\n\t"
        "s_waitcnt lgkmcnt(0)\n\t"
        "s_sleep 1\n\t"
        "s_sub_i32 s83, s83, 1\n\t"
        "s_cmp_gt_i32 s83, 0\n\t"
        "s_cbranch_scc1 .L7_w11\n"
        ".L7_d12:\n\t"
        "v_and_b32 v14, 15, v1\n\t"
        "v_lshlrev_b32 v14, 4, v14\n\t"
        "v_bfe_u32 v15, v1, 4, 1\n\t"
        "v_lshl_add_u32 v14, v15, 3, v14\n\t"
        "s_waitcnt lgkmcnt(0)\n\t"
        "v_xor_b32 v16, v16, v14\n\t"
        "v_xor_b32 v17, v17, v14\n\t"
        "v_xor_b32 v18, v18, v14\n\t"
        "v_xor_b32 v19, v19, v14\n\t"
        "v_xor_b32 v20, v20, v14\n\t"
        "v_xor_b32 v21, v21, v22\n\t"
        "v_mov_b64 v[8:9], 0\n\t"
        "v_mov_b64 v[10:11], 0\n\t"
        "buffer_load_dword v12, v13, s[20:23], s43 offen sc1\n"
        ".L7_w13:\n\t"
        "ds_read_b32 v0, v2 offset:28\n\t"
        "s_waitcnt lgkmcnt(0)\n\t"
        "v_cmp_gt_u16 vcc, 4, v0\n\t"
        "s_cbranch_vccz .L7_d14\n\t"
        "s_sleep 1\n\t"
        "s_sub_i32 s83, s83, 1\n\t"
        "s_cmp_gt_i32 s83, 0\n\t"
        "s_cbranch_scc1 .L7_w13\n"
        ".L7_d14:\n\t"
        "ds_read_b64_tr_b16 v[96:97], v23 offset:0\n\t"
        "ds_read_b64_tr_b16 v[98:99], v23 offset:512\n\t"
        "ds_read_b64_tr_b16 v[100:101], v23 offset:8\n\t"
        "ds_read_b64_tr_b16 v[102:103], v23 offset:520\n\t"
        "ds_read_b64_tr_b16 v[104:105], v23 offset:4096\n\t"
        "ds_read_b64_tr_b16 v[106:107], v23 offset:4608\n\t"
        "ds_read_b64_tr_b16 v[108:109], v23 offset:4104\n\t"
        "ds_read_b64_tr_b16 v[110:111], v23 offset:4616\n\t"
        "ds_read_b64_tr_b16 v[112:113], v23 offset:8192\n\t"
        "ds_read_b64_tr_b16 v[114:115], v23 offset:8704\n\t"
        "ds_read_b64_tr_b16 v[116:117], v23 offset:8200\n\t"
        "ds_read_b64_tr_b16 v[118:119], v23 offset:8712\n\t"
        "ds_read_b64_tr_b16 v[120:121], v23 offset:12288\n\t"
        "ds_read_b64_tr_b16 v[122:123], v23 offset:12800\n\t"
        "s_branch .L7_sw\n"
        ".L7_sp:\n\t"
        "buffer_load_dword v12, v13, s[20:23], s43 offen sc1\n"
        ".L7_sw:\n\t"
        "s_waitcnt vmcnt(0)\n\t"
        "v_xor_b32 v12, v12, v22\n\t"
        "v_and_b32 v0, s48, v12\n\t"
        "v_cmp_ne_u32 vcc, 0, v0\n\t"
        "s_cbranch_vccz .L7_sq\n\t"
        "s_sleep 4\n\t"
        "s_sub_i32 s83, s83, 1\n\t"
        "s_cmp_gt_i32 s83, 0\n\t"
        "s_cbranch_scc1 .L7_sp\n"
        ".L7_sq:\n\t"
        "v_add_f32 v21, v21, v12\n\t"
        "v_exp_f32 v21, v21\n\t"
        "v_mov_b64 v[12:13], 0\n\t"
        "v_mov_b64 v[14:15], 0\n\t"
        "v_mov_b32 v22, v21\n\t"
        "v_mov_b32 v0, v21\n\t"
        "s_nop 1\n\t"
        "v_permlane16_swap_b32 v22, v0\n\t"
        "v_add_f32 v22, v22, v0\n\t"
        "v_mov_b32 v0, v22\n\t"
        "s_nop 1\n\t"
        "v_permlane32_swap_b32 v22, v0\n\t"
        "v_add_f32 v22, v22, v0\n\t"
        "v_add_f32 v22, 0x3089705f, v22\n\t"
        "v_rcp_f32 v22, v22\n\t"
        "s_nop 0\n\t"
        "v_mul_f32 v21, v21, v22\n\t"
        "v_cvt_pk_bf16_f32 v21, v21, v21\n\t"
        "v_lshlrev_b32 v22, 4, v1\n\t"
        "v_add_u32 v22, 157952, v22\n"
        ".L7_w15:\n\t"
        "ds_read_b32 v0, v2 offset:36\n\t"
        "s_waitcnt lgkmcnt(0)\n\t"
        "v_cmp_gt_u16 vcc, 4, v0\n\t"
        "s_cbranch_vccz .L7_d16\n\t"
        "s_sleep 1\n\t"
        "s_sub_i32 s83, s83, 1\n\t"
        "s_cmp_gt_i32 s83, 0\n\t"
        "s_cbranch_scc1 .L7_w15\n"
        ".L7_d16:\n\t"
        "v_cmp_lt_u32 vcc, 0xfffff, v0\n\t"
        "s_cbranch_vccnz .L7_na\n\t"
        "v_and_b32_dpp v8, v21, v4 row_newbcast:0 row_mask:0xf bank_mask:0x3\n\t"
        "v_and_b32_dpp v9, v21, v5 row_newbcast:0 row_mask:0xf bank_mask:0x3\n\t"
        "v_and_b32_dpp v10, v21, v6 row_newbcast:0 row_mask:0xf bank_mask:0x3\n\t"
        "v_and_b32_dpp v11, v21, v7 row_newbcast:0 row_mask:0xf bank_mask:0x3\n\t"
        "v_and_b32_dpp v12, v21, v4 row_newbcast:8 row_mask:0xf bank_mask:0xc\n\t"
        "v_and_b32_dpp v13, v21, v5 row_newbcast:8 row_mask:0xf bank_mask:0xc\n\t"
        "v_and_b32_dpp v14, v21, v6 row_newbcast:8 row_mask:0xf bank_mask:0xc\n\t"
        "v_and_b32_dpp v15, v21, v7 row_newbcast:8 row_mask:0xf bank_mask:0xc\n\t"
        "v_mfma_f32_16x16x32_bf16 v[124:127], v[8:11], v[24:27], 0\n\t"
        "s_nop 0\n\t"
        "v_mfma_f32_16x16x32_bf16 v[124:127], v[12:15], v[28:31], v[124:127]\n\t"
        "v_and_b32_dpp v8, v21, v4 row_newbcast:4 row_mask:0xf bank_mask:0x3\n\t"
        "v_and_b32_dpp v9, v21, v5 row_newbcast:4 row_mask:0xf bank_mask:0x3\n\t"
        "v_and_b32_dpp v10, v21, v6 row_newbcast:4 row_mask:0xf bank_mask:0x3\n\t"
        "v_and_b32_dpp v11, v21, v7 row_newbcast:4 row_mask:0xf bank_mask:0x3\n\t"
        "v_and_b32_dpp v12, v21, v4 row_newbcast:12 row_mask:0xf bank_mask:0xc\n\t"
        "v_and_b32_dpp v13, v21, v5 row_newbcast:12 row_mask:0xf bank_mask:0xc\n\t"
        "v_and_b32_dpp v14, v21, v6 row_newbcast:12 row_mask:0xf bank_mask:0xc\n\t"
        "v_and_b32_dpp v15, v21, v7 row_newbcast:12 row_mask:0xf bank_mask:0xc\n\t"
        "v_mfma_f32_16x16x32_bf16 v[24:27], v[8:11], v[32:35], 0\n\t"
        "s_nop 0\n\t"
        "v_mfma_f32_16x16x32_bf16 v[24:27], v[12:15], v[36:39], v[24:27]\n\t"
        "v_cvt_pk_bf16_f32 v124, v124, v125\n\t"
        "v_cvt_pk_bf16_f32 v125, v126, v127\n\t"
        "ds_write_b64 v16, v[124:125] offset:43008\n\t"
        "v_and_b32_dpp v8, v21, v4 row_newbcast:1 row_mask:0xf bank_mask:0x3\n\t"
        "v_and_b32_dpp v9, v21, v5 row_newbcast:1 row_mask:0xf bank_mask:0x3\n\t"
        "v_and_b32_dpp v10, v21, v6 row_newbcast:1 row_mask:0xf bank_mask:0x3\n\t"
        "v_and_b32_dpp v11, v21, v7 row_newbcast:1 row_mask:0xf bank_mask:0x3\n\t"
        "v_and_b32_dpp v12, v21, v4 row_newbcast:9 row_mask:0xf bank_mask:0xc\n\t"
        "v_and_b32_dpp v13, v21, v5 row_newbcast:9 row_mask:0xf bank_mask:0xc\n\t"
        "v_and_b32_dpp v14, v21, v6 row_newbcast:9 row_mask:0xf bank_mask:0xc\n\t"
        "v_and_b32_dpp v15, v21, v7 row_newbcast:9 row_mask:0xf bank_mask:0xc\n\t"
        "v_mfma_f32_16x16x32_bf16 v[32:35], v[8:11], v[40:43], 0\n\t"
        "s_nop 0\n\t"
        "v_mfma_f32_16x16x32_bf16 v[32:35], v[12:15], v[44:47], v[32:35]\n\t"
        "v_cvt_pk_bf16_f32 v24, v24, v25\n\t"
        "v_cvt_pk_bf16_f32 v25, v26, v27\n\t"
        "ds_write_b64 v17, v[24:25] offset:43008\n\t"
        "v_and_b32_dpp v8, v21, v4 row_newbcast:5 row_mask:0xf bank_mask:0x3\n\t"
        "v_and_b32_dpp v9, v21, v5 row_newbcast:5 row_mask:0xf bank_mask:0x3\n\t"
        "v_and_b32_dpp v10, v21, v6 row_newbcast:5 row_mask:0xf bank_mask:0x3\n\t"
        "v_and_b32_dpp v11, v21, v7 row_newbcast:5 row_mask:0xf bank_mask:0x3\n\t"
        "v_and_b32_dpp v12, v21, v4 row_newbcast:13 row_mask:0xf bank_mask:0xc\n\t"
        "v_and_b32_dpp v13, v21, v5 row_newbcast:13 row_mask:0xf bank_mask:0xc\n\t"
        "v_and_b32_dpp v14, v21, v6 row_newbcast:13 row_mask:0xf bank_mask:0xc\n\t"
        "v_and_b32_dpp v15, v21, v7 row_newbcast:13 row_mask:0xf bank_mask:0xc\n\t"
        "v_mfma_f32_16x16x32_bf16 v[40:43], v[8:11], v[48:51], 0\n\t"
        "s_nop 0\n\t"
        "v_mfma_f32_16x16x32_bf16 v[40:43], v[12:15], v[52:55], v[40:43]\n\t"
        "v_cvt_pk_bf16_f32 v32, v32, v33\n\t"
        "v_cvt_pk_bf16_f32 v33, v34, v35\n\t"
        "ds_write_b64 v18, v[32:33] offset:43008\n\t"
        "v_and_b32_dpp v8, v21, v4 row_newbcast:2 row_mask:0xf bank_mask:0x3\n\t"
        "v_and_b32_dpp v9, v21, v5 row_newbcast:2 row_mask:0xf bank_mask:0x3\n\t"
        "v_and_b32_dpp v10, v21, v6 row_newbcast:2 row_mask:0xf bank_mask:0x3\n\t"
        "v_and_b32_dpp v11, v21, v7 row_newbcast:2 row_mask:0xf bank_mask:0x3\n\t"
        "v_and_b32_dpp v12, v21, v4 row_newbcast:10 row_mask:0xf bank_mask:0xc\n\t"
        "v_and_b32_dpp v13, v21, v5 row_newbcast:10 row_mask:0xf bank_mask:0xc\n\t"
        "v_and_b32_dpp v14, v21, v6 row_newbcast:10 row_mask:0xf bank_mask:0xc\n\t"
        "v_and_b32_dpp v15, v21, v7 row_newbcast:10 row_mask:0xf bank_mask:0xc\n\t"
        "v_mfma_f32_16x16x32_bf16 v[48:51], v[8:11], v[56:59], 0\n\t"
        "s_nop 0\n\t"
        "v_mfma_f32_16x16x32_bf16 v[48:51], v[12:15], v[60:63], v[48:51]\n\t"
        "v_cvt_pk_bf16_f32 v40, v40, v41\n\t"
        "v_cvt_pk_bf16_f32 v41, v42, v43\n\t"
        "ds_write_b64 v19, v[40:41] offset:43008\n\t"
        "s_cmp_lt_u32 s69, 4\n\t"
        "s_cselect_b32 s78, 16, 1\n\t"
        "v_mov_b32 v0, s78\n\t"
        "s_nop 1\n\t"
        "v_cvt_pk_bf16_f32 v48, v48, v49\n\t"
        "v_cvt_pk_bf16_f32 v49, v50, v51\n\t"
        "ds_write_b64 v20, v[48:49] offset:43008\n\t"
        "s_mov_b64 exec, 1\n\t"
        "ds_add_u32 v2, v0 offset:40\n\t"
        "s_mov_b64 exec, -1\n"
        ".L7_na:\n\t"
        "ds_read_b64_tr_b16 v[124:125], v23 offset:12296\n\t"
        "ds_read_b64_tr_b16 v[126:127], v23 offset:12808\n\t"
        "ds_read_b128 v[4:7], v22\n\t"
        "s_lshl3_add_u32 s42, %3, s81\n\t"
        "s_mul_i32 s42, s42, 0xc00\n\t"
        "s_add_u32 s42, s42, 3145728\n\t"
        "s_xor_b32 s78, %3, 1\n\t"
        "s_lshl3_add_u32 s43, s78, s81\n\t"
        "s_mul_i32 s43, s43, 0xc00\n\t"
        "s_add_u32 s43, s43, 3145728\n\t"
        "s_lshl_b32 s50, s81, 6\n\t"
        "s_lshl_b32 s51, s69, 10\n\t"
        "s_add_u32 s51, s51, 149760\n\t"
        "s_mov_b32 s49, 0x10001\n\t"
        "s_mov_b32 s48, 0x07060302\n\t"
        "s_mov_b32 s55, 157952\n"
        ".L7_w17:\n\t"
        "ds_read_b32 v0, v2 offset:40\n\t"
        "s_waitcnt lgkmcnt(0)\n\t"
        "v_cmp_gt_u16 vcc, 64, v0\n\t"
        "s_cbranch_vccz .L7_d18\n\t"
        "s_setprio 0\n\t"
        "s_sleep 1\n\t"
        "s_sub_i32 s83, s83, 1\n\t"
        "s_cmp_gt_i32 s83, 0\n\t"
        "s_cbranch_scc1 .L7_w17\n"
        ".L7_d18:\n\t"
        "s_setprio 2\n\t"
        "ds_read_b128 v[8:11], v4 offset:43008\n\t"
        "ds_read_b128 v[12:15], v5 offset:43008\n\t"
        "ds_read_b128 v[16:19], v6 offset:43008\n\t"
        "ds_read_b128 v[20:23], v7 offset:43008\n\t"
        "ds_read_b128 v[48:51], v4 offset:47104\n\t"
        "ds_read_b128 v[52:55], v5 offset:47104\n\t"
        "ds_read_b128 v[56:59], v6 offset:47104\n\t"
        "ds_read_b128 v[60:63], v7 offset:47104\n\t"
        "s_waitcnt lgkmcnt(7)\n\t"
        "v_mfma_f32_16x16x32_bf16 v[24:27], v[64:67], v[8:11], 0\n\t"
        "v_mfma_f32_16x16x32_bf16 v[28:31], v[68:71], v[8:11], 0\n\t"
        "s_waitcnt lgkmcnt(6)\n\t"
        "v_mfma_f32_16x16x32_bf16 v[24:27], v[72:75], v[12:15], v[24:27]\n\t"
        "v_mfma_f32_16x16x32_bf16 v[28:31], v[76:79], v[12:15], v[28:31]\n\t"
        "s_waitcnt lgkmcnt(5)\n\t"
        "v_mfma_f32_16x16x32_bf16 v[24:27], v[80:83], v[16:19], v[24:27]\n\t"
        "v_mfma_f32_16x16x32_bf16 v[28:31], v[84:87], v[16:19], v[28:31]\n\t"
        "s_waitcnt lgkmcnt(4)\n\t"
        "v_mfma_f32_16x16x32_bf16 v[24:27], v[88:91], v[20:23], v[24:27]\n\t"
        "v_mfma_f32_16x16x32_bf16 v[28:31], v[92:95], v[20:23], v[28:31]\n\t"
        "ds_read_b128 v[8:11], v4 offset:51200\n\t"
        "ds_read_b128 v[12:15], v5 offset:51200\n\t"
        "ds_read_b128 v[16:19], v6 offset:51200\n\t"
        "ds_read_b128 v[20:23], v7 offset:51200\n\t"
        "s_waitcnt lgkmcnt(7)\n\t"
        "v_mfma_f32_16x16x32_bf16 v[32:35], v[64:67], v[48:51], 0\n\t"
        "v_mfma_f32_16x16x32_bf16 v[36:39], v[68:71], v[48:51], 0\n\t"
        "s_waitcnt lgkmcnt(6)\n\t"
        "v_mfma_f32_16x16x32_bf16 v[32:35], v[72:75], v[52:55], v[32:35]\n\t"
        "v_mfma_f32_16x16x32_bf16 v[36:39], v[76:79], v[52:55], v[36:39]\n\t"
        "s_waitcnt lgkmcnt(5)\n\t"
        "v_mfma_f32_16x16x32_bf16 v[32:35], v[80:83], v[56:59], v[32:35]\n\t"
        "v_mfma_f32_16x16x32_bf16 v[36:39], v[84:87], v[56:59], v[36:39]\n\t"
        "s_waitcnt lgkmcnt(4)\n\t"
        "v_mfma_f32_16x16x32_bf16 v[32:35], v[88:91], v[60:63], v[32:35]\n\t"
        "v_mfma_f32_16x16x32_bf16 v[36:39], v[92:95], v[60:63], v[36:39]\n\t"
        "ds_read_b32 v0, v2 offset:52\n\t"
        "ds_read_b128 v[48:51], v4 offset:20480\n\t"
        "ds_read_b128 v[52:55], v5 offset:20480\n\t"
        "ds_read_b128 v[56:59], v6 offset:20480\n\t"
        "ds_read_b128 v[60:63], v7 offset:20480\n\t"
        "s_waitcnt lgkmcnt(8)\n\t"
        "v_mfma_f32_16x16x32_bf16 v[40:43], v[64:67], v[8:11], 0\n\t"
        "v_mfma_f32_16x16x32_bf16 v[44:47], v[68:71], v[8:11], 0\n\t"
        "s_waitcnt lgkmcnt(7)\n\t"
        "v_mfma_f32_16x16x32_bf16 v[40:43], v[72:75], v[12:15], v[40:43]\n\t"
        "v_mfma_f32_16x16x32_bf16 v[44:47], v[76:79], v[12:15], v[44:47]\n\t"
        "s_waitcnt lgkmcnt(6)\n\t"
        "v_mfma_f32_16x16x32_bf16 v[40:43], v[80:83], v[16:19], v[40:43]\n\t"
        "v_mfma_f32_16x16x32_bf16 v[44:47], v[84:87], v[16:19], v[44:47]\n\t"
        "s_waitcnt lgkmcnt(5)\n\t"
        "v_mfma_f32_16x16x32_bf16 v[40:43], v[88:91], v[20:23], v[40:43]\n\t"
        "v_mfma_f32_16x16x32_bf16 v[44:47], v[92:95], v[20:23], v[44:47]\n\t"
        "ds_read_b128 v[8:11], v4 offset:24576\n\t"
        "ds_read_b128 v[12:15], v5 offset:24576\n\t"
        "ds_read_b128 v[16:19], v6 offset:24576\n\t"
        "ds_read_b128 v[20:23], v7 offset:24576\n\t"
        "s_waitcnt lgkmcnt(8)\n\t"
        "v_cmp_gt_u16 vcc, 6, v0\n\t"
        "s_cbranch_vccz .L7_t1\n"
        ".L7_w19:\n\t"
        "ds_read_b32 v0, v2 offset:52\n\t"
        "s_waitcnt lgkmcnt(0)\n\t"
        "v_cmp_gt_u16 vcc, 6, v0\n\t"
        "s_cbranch_vccz .L7_d20\n\t"
        "s_setprio 0\n\t"
        "s_sleep 2\n\t"
        "s_sub_i32 s83, s83, 1\n\t"
        "s_cmp_gt_i32 s83, 0\n\t"
        "s_cbranch_scc1 .L7_w19\n"
        ".L7_d20:\n\t"
        "s_setprio 2\n\t"
        "ds_read_b128 v[48:51], v4 offset:20480\n\t"
        "ds_read_b128 v[52:55], v5 offset:20480\n\t"
        "ds_read_b128 v[56:59], v6 offset:20480\n\t"
        "ds_read_b128 v[60:63], v7 offset:20480\n\t"
        "ds_read_b128 v[8:11], v4 offset:24576\n\t"
        "ds_read_b128 v[12:15], v5 offset:24576\n\t"
        "ds_read_b128 v[16:19], v6 offset:24576\n\t"
        "ds_read_b128 v[20:23], v7 offset:24576\n"
        ".L7_t1:\n\t"
        "v_lshl_add_u32 v3, v1, 4, s55\n\t"
        "s_waitcnt lgkmcnt(7)\n\t"
        "v_mfma_f32_16x16x32_bf16 v[24:27], v[96:99], v[48:51], v[24:27]\n\t"
        "v_mfma_f32_16x16x32_bf16 v[28:31], v[100:103], v[48:51], v[28:31]\n\t"
        "s_waitcnt lgkmcnt(6)\n\t"
        "v_mfma_f32_16x16x32_bf16 v[24:27], v[104:107], v[52:55], v[24:27]\n\t"
        "v_mfma_f32_16x16x32_bf16 v[28:31], v[108:111], v[52:55], v[28:31]\n\t"
        "s_waitcnt lgkmcnt(5)\n\t"
        "v_mfma_f32_16x16x32_bf16 v[24:27], v[112:115], v[56:59], v[24:27]\n\t"
        "v_mfma_f32_16x16x32_bf16 v[28:31], v[116:119], v[56:59], v[28:31]\n\t"
        "s_waitcnt lgkmcnt(4)\n\t"
        "v_mfma_f32_16x16x32_bf16 v[24:27], v[120:123], v[60:63], v[24:27]\n\t"
        "v_mfma_f32_16x16x32_bf16 v[28:31], v[124:127], v[60:63], v[28:31]\n\t"
        "ds_read_b128 v[60:63], v3 offset:1024\n\t"
        "s_waitcnt lgkmcnt(4)\n\t"
        "v_mfma_f32_16x16x32_bf16 v[32:35], v[96:99], v[8:11], v[32:35]\n\t"
        "v_mfma_f32_16x16x32_bf16 v[36:39], v[100:103], v[8:11], v[36:39]\n\t"
        "s_waitcnt lgkmcnt(3)\n\t"
        "v_mfma_f32_16x16x32_bf16 v[32:35], v[104:107], v[12:15], v[32:35]\n\t"
        "v_mfma_f32_16x16x32_bf16 v[36:39], v[108:111], v[12:15], v[36:39]\n\t"
        "s_waitcnt lgkmcnt(2)\n\t"
        "v_mfma_f32_16x16x32_bf16 v[32:35], v[112:115], v[16:19], v[32:35]\n\t"
        "v_mfma_f32_16x16x32_bf16 v[36:39], v[116:119], v[16:19], v[36:39]\n\t"
        "s_waitcnt lgkmcnt(1)\n\t"
        "v_mfma_f32_16x16x32_bf16 v[32:35], v[120:123], v[20:23], v[32:35]\n\t"
        "v_mfma_f32_16x16x32_bf16 v[36:39], v[124:127], v[20:23], v[36:39]\n\t"
        "ds_read_b32 v0, v2 offset:56\n\t"
        "ds_read_b128 v[8:11], v4 offset:28672\n\t"
        "ds_read_b128 v[12:15], v5 offset:28672\n\t"
        "ds_read_b128 v[16:19], v6 offset:28672\n\t"
        "ds_read_b128 v[20:23], v7 offset:28672\n\t"
        "s_waitcnt lgkmcnt(5)\n\t"
        "v_add_u32 v24, 0x10000, v24\n\t"
        "v_add_u32 v25, 0x10000, v25\n\t"
        "v_add_u32 v26, 0x10000, v26\n\t"
        "v_add_u32 v27, 0x10000, v27\n\t"
        "v_perm_b32 v48, v25, v24, s48\n\t"
        "v_perm_b32 v49, v27, v26, s48\n\t"
        "v_cvt_pk_bf16_f32 v50, v28, v29\n\t"
        "v_cvt_pk_bf16_f32 v51, v30, v31\n\t"
        "v_bfi_b32 v48, s49, v60, v48\n\t"
        "v_bfi_b32 v49, s49, v61, v49\n\t"
        "v_add_u32 v32, 0x10000, v32\n\t"
        "v_add_u32 v33, 0x10000, v33\n\t"
        "v_add_u32 v34, 0x10000, v34\n\t"
        "v_add_u32 v35, 0x10000, v35\n\t"
        "v_perm_b32 v52, v33, v32, s48\n\t"
        "v_perm_b32 v53, v35, v34, s48\n\t"
        "v_cvt_pk_bf16_f32 v54, v36, v37\n\t"
        "v_cvt_pk_bf16_f32 v55, v38, v39\n\t"
        "v_bfi_b32 v52, s49, v60, v52\n\t"
        "v_bfi_b32 v53, s49, v61, v53\n\t"
        "s_mov_b32 exec_lo, 0xff00ff00\n\t"
        "s_mov_b32 exec_hi, 0xff00ff00\n\t"
        "buffer_store_dwordx4 v[48:51], v62, s[20:23], s42 offen offset:0 sc1\n\t"
        "s_mov_b64 exec, -1\n\t"
        "buffer_store_dwordx4 v[52:55], v62, s[20:23], s42 offen offset:1024 sc1\n\t"
        "s_waitcnt lgkmcnt(4)\n\t"
        "v_cmp_gt_u16 vcc, 4, v0\n\t"
        "s_cbranch_vccz .L7_t2\n"
        ".L7_w21:\n\t"
        "ds_read_b32 v0, v2 offset:56\n\t"
        "s_waitcnt lgkmcnt(0)\n\t"
        "v_cmp_gt_u16 vcc, 3, v0\n\t"
        "s_cbranch_vccz .L7_d22\n\t"
        "s_setprio 0\n\t"
        "s_sleep 2\n\t"
        "s_sub_i32 s83, s83, 1\n\t"
        "s_cmp_gt_i32 s83, 0\n\t"
        "s_cbranch_scc1 .L7_w21\n"
        ".L7_d22:\n\t"
        "s_setprio 2\n"
        ".L7_w23:\n\t"
        "ds_read_b32 v0, v2 offset:56\n\t"
        "ds_read_b128 v[8:11], v4 offset:28672\n\t"
        "ds_read_b128 v[12:15], v5 offset:28672\n\t"
        "ds_read_b128 v[16:19], v6 offset:28672\n\t"
        "ds_read_b128 v[20:23], v7 offset:28672\n\t"
        "s_waitcnt lgkmcnt(4)\n\t"
        "v_cmp_gt_u16 vcc, 4, v0\n\t"
        "s_cbranch_vccz .L7_d24\n\t"
        "s_waitcnt lgkmcnt(0)\n\t"
        "s_setprio 0\n\t"
        "s_sleep 1\n\t"
        "s_sub_i32 s83, s83, 1\n\t"
        "s_cmp_gt_i32 s83, 0\n\t"
        "s_cbranch_scc1 .L7_w23\n"
        ".L7_d24:\n\t"
        "s_setprio 2\n"
        ".L7_t2:\n\t"
        "s_waitcnt lgkmcnt(3)\n\t"
        "v_mfma_f32_16x16x32_bf16 v[40:43], v[96:99], v[8:11], v[40:43]\n\t"
        "v_mfma_f32_16x16x32_bf16 v[44:47], v[100:103], v[8:11], v[44:47]\n\t"
        "s_waitcnt lgkmcnt(2)\n\t"
        "v_mfma_f32_16x16x32_bf16 v[40:43], v[104:107], v[12:15], v[40:43]\n\t"
        "v_mfma_f32_16x16x32_bf16 v[44:47], v[108:111], v[12:15], v[44:47]\n\t"
        "s_waitcnt lgkmcnt(1)\n\t"
        "v_mfma_f32_16x16x32_bf16 v[40:43], v[112:115], v[16:19], v[40:43]\n\t"
        "v_mfma_f32_16x16x32_bf16 v[44:47], v[116:119], v[16:19], v[44:47]\n\t"
        "s_waitcnt lgkmcnt(0)\n\t"
        "v_mfma_f32_16x16x32_bf16 v[40:43], v[120:123], v[20:23], v[40:43]\n\t"
        "v_mfma_f32_16x16x32_bf16 v[44:47], v[124:127], v[20:23], v[44:47]\n\t"
        "s_nop 6\n\t"
        "v_add_u32 v40, 0x10000, v40\n\t"
        "v_add_u32 v41, 0x10000, v41\n\t"
        "v_add_u32 v42, 0x10000, v42\n\t"
        "v_add_u32 v43, 0x10000, v43\n\t"
        "v_perm_b32 v56, v41, v40, s48\n\t"
        "v_perm_b32 v57, v43, v42, s48\n\t"
        "v_cvt_pk_bf16_f32 v58, v44, v45\n\t"
        "v_cvt_pk_bf16_f32 v59, v46, v47\n\t"
        "v_bfi_b32 v56, s49, v60, v56\n\t"
        "v_bfi_b32 v57, s49, v61, v57\n\t"
        "buffer_store_dwordx4 v[56:59], v62, s[20:23], s42 offen offset:2048 sc1\n\t"
        "s_memrealtime s[52:53]\n\t"
        "s_waitcnt lgkmcnt(0)\n"
        ".L7_w25:\n\t"
        "ds_read_b32 v0, v2 offset:40\n\t"
        "ds_read_b128 v[8:11], v4 offset:55296\n\t"
        "ds_read_b128 v[12:15], v5 offset:55296\n\t"
        "ds_read_b128 v[16:19], v6 offset:55296\n\t"
        "ds_read_b128 v[20:23], v7 offset:55296\n\t"
        "s_waitcnt lgkmcnt(4)\n\t"
        "v_cmp_gt_u16 vcc, 68, v0\n\t"
        "s_cbranch_vccz .L7_d26\n\t"
        "s_waitcnt lgkmcnt(0)\n\t"
        "s_setprio 0\n\t"
        "s_sleep 1\n\t"
        "s_sub_i32 s83, s83, 1\n\t"
        "s_cmp_gt_i32 s83, 0\n\t"
        "s_cbranch_scc1 .L7_w25\n"
        ".L7_d26:\n\t"
        "s_setprio 2\n\t"
        "s_waitcnt lgkmcnt(3)\n\t"
        "v_mfma_f32_16x16x32_bf16 v[24:27], v[64:67], v[8:11], 0\n\t"
        "v_mfma_f32_16x16x32_bf16 v[28:31], v[68:71], v[8:11], 0\n\t"
        "s_waitcnt lgkmcnt(2)\n\t"
        "v_mfma_f32_16x16x32_bf16 v[24:27], v[72:75], v[12:15], v[24:27]\n\t"
        "v_mfma_f32_16x16x32_bf16 v[28:31], v[76:79], v[12:15], v[28:31]\n\t"
        "s_waitcnt lgkmcnt(1)\n\t"
        "v_mfma_f32_16x16x32_bf16 v[24:27], v[80:83], v[16:19], v[24:27]\n\t"
        "v_mfma_f32_16x16x32_bf16 v[28:31], v[84:87], v[16:19], v[28:31]\n\t"
        "s_waitcnt lgkmcnt(0)\n\t"
        "v_mfma_f32_16x16x32_bf16 v[24:27], v[88:91], v[20:23], v[24:27]\n\t"
        "v_mfma_f32_16x16x32_bf16 v[28:31], v[92:95], v[20:23], v[28:31]\n\t"
        "ds_read_b128 v[8:11], v4 offset:59392\n\t"
        "ds_read_b128 v[12:15], v5 offset:59392\n\t"
        "ds_read_b128 v[16:19], v6 offset:59392\n\t"
        "ds_read_b128 v[20:23], v7 offset:59392\n\t"
        "s_waitcnt lgkmcnt(3)\n\t"
        "v_mfma_f32_16x16x32_bf16 v[32:35], v[64:67], v[8:11], 0\n\t"
        "v_mfma_f32_16x16x32_bf16 v[36:39], v[68:71], v[8:11], 0\n\t"
        "s_waitcnt lgkmcnt(2)\n\t"
        "v_mfma_f32_16x16x32_bf16 v[32:35], v[72:75], v[12:15], v[32:35]\n\t"
        "v_mfma_f32_16x16x32_bf16 v[36:39], v[76:79], v[12:15], v[36:39]\n\t"
        "s_waitcnt lgkmcnt(1)\n\t"
        "v_mfma_f32_16x16x32_bf16 v[32:35], v[80:83], v[16:19], v[32:35]\n\t"
        "v_mfma_f32_16x16x32_bf16 v[36:39], v[84:87], v[16:19], v[36:39]\n\t"
        "s_waitcnt lgkmcnt(0)\n\t"
        "v_mfma_f32_16x16x32_bf16 v[32:35], v[88:91], v[20:23], v[32:35]\n\t"
        "v_mfma_f32_16x16x32_bf16 v[36:39], v[92:95], v[20:23], v[36:39]\n\t"
        "ds_read_b128 v[8:11], v4 offset:63488\n\t"
        "ds_read_b128 v[12:15], v5 offset:63488\n\t"
        "ds_read_b128 v[16:19], v6 offset:63488\n\t"
        "ds_read_b128 v[20:23], v7 offset:63488\n\t"
        "s_waitcnt lgkmcnt(3)\n\t"
        "v_mfma_f32_16x16x32_bf16 v[40:43], v[64:67], v[8:11], 0\n\t"
        "v_mfma_f32_16x16x32_bf16 v[44:47], v[68:71], v[8:11], 0\n\t"
        "s_waitcnt lgkmcnt(2)\n\t"
        "v_mfma_f32_16x16x32_bf16 v[40:43], v[72:75], v[12:15], v[40:43]\n\t"
        "v_mfma_f32_16x16x32_bf16 v[44:47], v[76:79], v[12:15], v[44:47]\n\t"
        "s_waitcnt lgkmcnt(1)\n\t"
        "v_mfma_f32_16x16x32_bf16 v[40:43], v[80:83], v[16:19], v[40:43]\n\t"
        "v_mfma_f32_16x16x32_bf16 v[44:47], v[84:87], v[16:19], v[44:47]\n\t"
        "s_waitcnt lgkmcnt(0)\n\t"
        "v_mfma_f32_16x16x32_bf16 v[40:43], v[88:91], v[20:23], v[40:43]\n\t"
        "v_mfma_f32_16x16x32_bf16 v[44:47], v[92:95], v[20:23], v[44:47]\n"
        ".L7_t4:\n\t"
        "ds_read_b128 v[68:71], v3 offset:2048\n\t"
        "ds_read_b128 v[72:75], v3 offset:3072\n\t"
        "ds_read_b128 v[76:79], v3 offset:4096\n"
        ".L7_w27:\n\t"
        "ds_read_b32 v0, v2 offset:60\n\t"
        "s_waitcnt lgkmcnt(0)\n\t"
        "v_cmp_gt_u16 vcc, 2, v0\n\t"
        "s_cbranch_vccz .L7_d28\n\t"
        "s_setprio 0\n\t"
        "s_sleep 2\n\t"
        "s_sub_i32 s83, s83, 1\n\t"
        "s_cmp_gt_i32 s83, 0\n\t"
        "s_cbranch_scc1 .L7_w27\n"
        ".L7_d28:\n\t"
        "s_setprio 2\n\t"
        "ds_read_b128 v[8:11], v4 offset:32768\n\t"
        "ds_read_b128 v[12:15], v5 offset:32768\n\t"
        "ds_read_b128 v[16:19], v6 offset:32768\n\t"
        "ds_read_b128 v[20:23], v7 offset:32768\n\t"
        "v_add_u32 v71, s51, v71\n\t"
        "v_add_u32 v72, s51, v72\n\t"
        "s_waitcnt lgkmcnt(3)\n\t"
        "v_mfma_f32_16x16x32_bf16 v[24:27], v[96:99], v[8:11], v[24:27]\n\t"
        "v_mfma_f32_16x16x32_bf16 v[28:31], v[100:103], v[8:11], v[28:31]\n\t"
        "s_waitcnt lgkmcnt(2)\n\t"
        "v_mfma_f32_16x16x32_bf16 v[24:27], v[104:107], v[12:15], v[24:27]\n\t"
        "v_mfma_f32_16x16x32_bf16 v[28:31], v[108:111], v[12:15], v[28:31]\n\t"
        "s_waitcnt lgkmcnt(1)\n\t"
        "v_mfma_f32_16x16x32_bf16 v[24:27], v[112:115], v[16:19], v[24:27]\n\t"
        "v_mfma_f32_16x16x32_bf16 v[28:31], v[116:119], v[16:19], v[28:31]\n\t"
        "s_waitcnt lgkmcnt(0)\n\t"
        "v_mfma_f32_16x16x32_bf16 v[24:27], v[120:123], v[20:23], v[24:27]\n\t"
        "v_mfma_f32_16x16x32_bf16 v[28:31], v[124:127], v[20:23], v[28:31]\n\t"
        "s_setprio 0\n"
        ".L7_g29:\n\t"
        "s_memrealtime s[78:79]\n\t"
        "s_waitcnt lgkmcnt(0)\n\t"
        "s_sub_u32 s78, s78, s52\n\t"
        "s_cmp_ge_u32 s78, 120\n\t"
        "s_cbranch_scc1 .L7_h30\n\t"
        "s_sleep 1\n\t"
        "s_branch .L7_g29\n"
        ".L7_h30:\n\t"
        "s_setprio 2\n\t"
        "buffer_load_dwordx4 v[48:51], v63, s[20:23], s43 offen offset:0 sc1\n\t"
        "buffer_load_dwordx4 v[52:55], v62, s[20:23], s43 offen offset:1024 sc1\n\t"
        "buffer_load_dwordx4 v[56:59], v62, s[20:23], s43 offen offset:2048 sc1\n"
        ".L7_w31:\n\t"
        "ds_read_b32 v0, v2 offset:64\n\t"
        "s_waitcnt lgkmcnt(0)\n\t"
        "v_cmp_gt_u16 vcc, 4, v0\n\t"
        "s_cbranch_vccz .L7_d32\n\t"
        "s_setprio 0\n\t"
        "s_sleep 2\n\t"
        "s_sub_i32 s83, s83, 1\n\t"
        "s_cmp_gt_i32 s83, 0\n\t"
        "s_cbranch_scc1 .L7_w31\n"
        ".L7_d32:\n\t"
        "s_setprio 2\n\t"
        "ds_read_b128 v[8:11], v4 offset:36864\n\t"
        "ds_read_b128 v[12:15], v5 offset:36864\n\t"
        "ds_read_b128 v[16:19], v6 offset:36864\n\t"
        "ds_read_b128 v[20:23], v7 offset:36864\n\t"
        "s_waitcnt lgkmcnt(3)\n\t"
        "v_mfma_f32_16x16x32_bf16 v[32:35], v[96:99], v[8:11], v[32:35]\n\t"
        "v_mfma_f32_16x16x32_bf16 v[36:39], v[100:103], v[8:11], v[36:39]\n\t"
        "s_waitcnt lgkmcnt(2)\n\t"
        "v_mfma_f32_16x16x32_bf16 v[32:35], v[104:107], v[12:15], v[32:35]\n\t"
        "v_mfma_f32_16x16x32_bf16 v[36:39], v[108:111], v[12:15], v[36:39]\n\t"
        "s_waitcnt lgkmcnt(1)\n\t"
        "v_mfma_f32_16x16x32_bf16 v[32:35], v[112:115], v[16:19], v[32:35]\n\t"
        "v_mfma_f32_16x16x32_bf16 v[36:39], v[116:119], v[16:19], v[36:39]\n\t"
        "s_waitcnt lgkmcnt(0)\n\t"
        "v_mfma_f32_16x16x32_bf16 v[32:35], v[120:123], v[20:23], v[32:35]\n\t"
        "v_mfma_f32_16x16x32_bf16 v[36:39], v[124:127], v[20:23], v[36:39]\n\t"
        "s_waitcnt vmcnt(0)\n\t"
        "s_branch .L7_s34\n"
        ".L7_r33:\n\t"
        "buffer_load_dwordx4 v[48:51], v63, s[20:23], s43 offen offset:0 sc1\n\t"
        "buffer_load_dwordx4 v[52:55], v62, s[20:23], s43 offen offset:1024 sc1\n\t"
        "buffer_load_dwordx4 v[56:59], v62, s[20:23], s43 offen offset:2048 sc1\n\t"
        "s_waitcnt vmcnt(0)\n"
        ".L7_s34:\n\t"
        "v_xor_b32 v48, v48, v60\n\t"
        "v_xor_b32 v49, v49, v61\n\t"
        "v_xor_b32 v52, v52, v60\n\t"
        "v_xor_b32 v53, v53, v61\n\t"
        "v_xor_b32 v56, v56, v60\n\t"
        "v_xor_b32 v57, v57, v61\n\t"
        "v_or_b32 v0, v48, v49\n\t"
        "v_or3_b32 v0, v0, v52, v53\n\t"
        "v_or3_b32 v0, v0, v56, v57\n\t"
        "v_and_b32 v0, s49, v0\n\t"
        "v_cmp_ne_u32 vcc, 0, v0\n\t"
        "s_cbranch_vccz .L7_t35\n\t"
        "s_sleep 8\n\t"
        "s_sub_i32 s83, s83, 1\n\t"
        "s_cmp_gt_i32 s83, 0\n\t"
        "s_cbranch_scc1 .L7_r33\n"
        ".L7_t35:\n\t"
        "v_mfma_f32_16x16x32_bf16 v[24:27], v[76:79], v[48:51], v[24:27]\n\t"
        "v_mfma_f32_16x16x32_bf16 v[28:31], v[74:77], v[48:51], v[28:31]\n\t"
        "v_mfma_f32_16x16x32_bf16 v[32:35], v[76:79], v[52:55], v[32:35]\n\t"
        "v_mfma_f32_16x16x32_bf16 v[36:39], v[74:77], v[52:55], v[36:39]\n\t"
        "v_mfma_f32_16x16x32_bf16 v[40:43], v[76:79], v[56:59], v[40:43]\n\t"
        "v_mfma_f32_16x16x32_bf16 v[44:47], v[74:77], v[56:59], v[44:47]\n\t"
        "s_nop 2\n\t"
        "v_cvt_pk_bf16_f32 v48, v24, v25\n\t"
        "v_cvt_pk_bf16_f32 v49, v26, v27\n\t"
        "v_cvt_pk_bf16_f32 v50, v28, v29\n\t"
        "v_cvt_pk_bf16_f32 v51, v30, v31\n\t"
        "ds_write_b128 v71, v[48:51]\n\t"
        "ds_read_b128 v[48:51], v72\n\t"
        "v_cvt_pk_bf16_f32 v52, v32, v33\n\t"
        "v_cvt_pk_bf16_f32 v53, v34, v35\n\t"
        "v_cvt_pk_bf16_f32 v54, v36, v37\n\t"
        "v_cvt_pk_bf16_f32 v55, v38, v39\n\t"
        "ds_write_b128 v71, v[52:55]\n\t"
        "ds_read_b128 v[52:55], v72\n\t"
        "s_waitcnt lgkmcnt(0)\n\t"
        "buffer_store_dwordx4 v[48:51], v68, s[60:63], s50 offen sc1\n\t"
        "buffer_store_dwordx4 v[52:55], v69, s[60:63], s50 offen sc1\n"
        ".L7_w36:\n\t"
        "ds_read_b32 v0, v2 offset:68\n\t"
        "s_waitcnt lgkmcnt(0)\n\t"
        "v_cmp_gt_u16 vcc, 3, v0\n\t"
        "s_cbranch_vccz .L7_d37\n\t"
        "s_setprio 0\n\t"
        "s_sleep 1\n\t"
        "s_sub_i32 s83, s83, 1\n\t"
        "s_cmp_gt_i32 s83, 0\n\t"
        "s_cbranch_scc1 .L7_w36\n"
        ".L7_d37:\n\t"
        "s_setprio 2\n"
        ".L7_w38:\n\t"
        "ds_read_b32 v0, v2 offset:68\n\t"
        "ds_read_b128 v[8:11], v4 offset:40960\n\t"
        "ds_read_b128 v[12:15], v5 offset:40960\n\t"
        "ds_read_b128 v[16:19], v6 offset:40960\n\t"
        "ds_read_b128 v[20:23], v7 offset:40960\n\t"
        "s_waitcnt lgkmcnt(4)\n\t"
        "v_cmp_gt_u16 vcc, 4, v0\n\t"
        "s_cbranch_vccz .L7_d39\n\t"
        "s_waitcnt lgkmcnt(0)\n\t"
        "s_setprio 0\n\t"
        "s_sleep 0\n\t"
        "s_sub_i32 s83, s83, 1\n\t"
        "s_cmp_gt_i32 s83, 0\n\t"
        "s_cbranch_scc1 .L7_w38\n"
        ".L7_d39:\n\t"
        "s_setprio 2\n\t"
        "s_waitcnt lgkmcnt(3)\n\t"
        "v_mfma_f32_16x16x32_bf16 v[40:43], v[96:99], v[8:11], v[40:43]\n\t"
        "v_mfma_f32_16x16x32_bf16 v[44:47], v[100:103], v[8:11], v[44:47]\n\t"
        "s_waitcnt lgkmcnt(2)\n\t"
        "v_mfma_f32_16x16x32_bf16 v[40:43], v[104:107], v[12:15], v[40:43]\n\t"
        "v_mfma_f32_16x16x32_bf16 v[44:47], v[108:111], v[12:15], v[44:47]\n\t"
        "s_waitcnt lgkmcnt(1)\n\t"
        "v_mfma_f32_16x16x32_bf16 v[40:43], v[112:115], v[16:19], v[40:43]\n\t"
        "v_mfma_f32_16x16x32_bf16 v[44:47], v[116:119], v[16:19], v[44:47]\n\t"
        "s_waitcnt lgkmcnt(0)\n\t"
        "v_mfma_f32_16x16x32_bf16 v[40:43], v[120:123], v[20:23], v[40:43]\n\t"
        "v_mfma_f32_16x16x32_bf16 v[44:47], v[124:127], v[20:23], v[44:47]\n\t"
        "s_nop 6\n\t"
        "v_cvt_pk_bf16_f32 v56, v40, v41\n\t"
        "v_cvt_pk_bf16_f32 v57, v42, v43\n\t"
        "v_cvt_pk_bf16_f32 v58, v44, v45\n\t"
        "v_cvt_pk_bf16_f32 v59, v46, v47\n\t"
        "ds_write_b128 v71, v[56:59]\n\t"
        "ds_read_b128 v[56:59], v72\n\t"
        "s_waitcnt lgkmcnt(0)\n\t"
        "buffer_store_dwordx4 v[56:59], v70, s[60:63], s50 offen sc1\n\t"
        "s_or_b32 s78, %3, s69\n\t"
        "s_cbranch_scc1 .L7_ng\n\t"
        "v_mov_b32 v0, s68\n\t"
        "buffer_store_dword v0, off, s[20:23], 0 sc1\n"
        ".L7_ng:\n\t"
        "s_endpgm\n"
        ".L7_U2:\n\t"
        "s_barrier\n\t"
        "v_cmp_gt_u32 s[24:25], s46, v4\n\t"
        "v_cmp_gt_u32 s[26:27], s46, v5\n\t"
        "v_cmp_gt_u32 s[28:29], s46, v6\n\t"
        "v_cmp_gt_u32 s[30:31], s46, v7\n\t"
        "v_cmp_gt_u32 s[32:33], s46, v8\n\t"
        "v_mov_b64 v[4:5], 0\n\t"
        "v_mov_b64 v[6:7], 0\n\t"
        "s_mov_b32 exec_hi, 0xffff\n\t"
        "ds_write_b128 v12, v[4:7] offset:1024\n\t"
        "s_mov_b64 exec, -1\n\t"
        "v_add_u32 v15, 83968, v12\n\t"
        "v_mov_b32 v13, 0x1000000\n\t"
        "v_mov_b32 v14, 33\n\t"
        "s_lshl_b32 s54, s65, 14\n\t"
        "s_sub_u32 s54, 0, s54\n\t"
        "s_lshl_b32 s48, s71, 14\n\t"
        "s_add_u32 s48, s48, 1048576\n\t"
        "s_add_u32 s49, s48, 4096\n\t"
        "s_add_u32 s50, s48, 8192\n\t"
        "s_add_u32 s51, s48, 12288\n\t"
        "s_movk_i32 s55, 1023\n\t"
        "s_mov_b32 s52, 83840\n\t"
        "s_waitcnt lgkmcnt(0)\n\t"
        "s_add_u32 s68, s68, 1\n\t"
        "s_lshl_b32 s78, s68, 24\n\t"
        "s_or_b32 s78, s78, 0xffffff\n\t"
        "v_mov_b32 v8, s78\n\t"
        "s_xor_b32 s78, s78, 0x5a000000\n\t"
        "v_mov_b32 v9, s78\n\t"
        "s_mul_i32 s78, s68, 0x01010000\n\t"
        "s_or_b32 s78, s78, 0xffffff\n\t"
        "v_mov_b32 v10, s78\n\t"
        "s_mul_i32 s78, s68, 0x02000200\n\t"
        "s_or_b32 s78, s78, 0xffffff\n\t"
        "v_mov_b32 v11, s78\n\t"
        "ds_write_b128 v15, v[8:11] offset:0\n\t"
        "ds_write_b128 v15, v[8:11] offset:1024\n\t"
        "ds_write_b128 v15, v[8:11] offset:2048\n\t"
        "ds_write_b128 v15, v[8:11] offset:3072\n\t"
        "ds_write_b128 v15, v[8:11] offset:4096\n\t"
        "ds_write_b128 v15, v[8:11] offset:5120\n\t"
        "ds_write_b128 v15, v[8:11] offset:6144\n\t"
        "ds_write_b128 v15, v[8:11] offset:7168\n\t"
        "ds_write_b128 v15, v[8:11] offset:8192\n\t"
        "ds_write_b128 v15, v[8:11] offset:9216\n\t"
        "ds_write_b128 v15, v[8:11] offset:10240\n\t"
        "ds_write_b128 v15, v[8:11] offset:11264\n\t"
        "s_waitcnt lgkmcnt(6)\n\t"
        "ds_write_b128 v15, v[8:11] offset:12288\n\t"
        "ds_write_b128 v15, v[8:11] offset:13312\n\t"
        "ds_write_b128 v15, v[8:11] offset:14336\n\t"
        "ds_write_b128 v15, v[8:11] offset:15360\n\t"
        "v_mov_b32 v4, s68\n\t"
        "ds_write_b32 v2, v4 offset:76\n\t"
        "s_waitcnt vmcnt(11) lgkmcnt(8)\n\t"
        "s_mov_b64 exec, s[24:25]\n\t"
        "v_mad_u32_u24 v56, v16, s66, v13\n\t"
        "v_mad_u32_u24 v57, v17, s66, v13\n\t"
        "v_mad_u32_u24 v58, v18, s66, v13\n\t"
        "v_mad_u32_u24 v59, v19, s66, v13\n\t"
        "v_lshlrev_b32_sdwa v56, 2, v56 dst_sel:DWORD dst_unused:UNUSED_PAD src0_sel:DWORD src1_sel:BYTE_3\n\t"
        "ds_add_rtn_u32 v76, v56, v14 offset:1024\n\t"
        "v_lshlrev_b32_sdwa v57, 2, v57 dst_sel:DWORD dst_unused:UNUSED_PAD src0_sel:DWORD src1_sel:BYTE_3\n\t"
        "ds_add_rtn_u32 v77, v57, v14 offset:1024\n\t"
        "v_lshlrev_b32_sdwa v58, 2, v58 dst_sel:DWORD dst_unused:UNUSED_PAD src0_sel:DWORD src1_sel:BYTE_3\n\t"
        "ds_add_rtn_u32 v78, v58, v14 offset:1024\n\t"
        "v_lshlrev_b32_sdwa v59, 2, v59 dst_sel:DWORD dst_unused:UNUSED_PAD src0_sel:DWORD src1_sel:BYTE_3\n\t"
        "ds_add_rtn_u32 v79, v59, v14 offset:1024\n\t"
        "s_waitcnt vmcnt(10) lgkmcnt(8)\n\t"
        "s_mov_b64 exec, s[26:27]\n\t"
        "v_mad_u32_u24 v60, v20, s66, v13\n\t"
        "v_mad_u32_u24 v61, v21, s66, v13\n\t"
        "v_mad_u32_u24 v62, v22, s66, v13\n\t"
        "v_mad_u32_u24 v63, v23, s66, v13\n\t"
        "v_lshlrev_b32_sdwa v60, 2, v60 dst_sel:DWORD dst_unused:UNUSED_PAD src0_sel:DWORD src1_sel:BYTE_3\n\t"
        "ds_add_rtn_u32 v80, v60, v14 offset:1024\n\t"
        "v_lshlrev_b32_sdwa v61, 2, v61 dst_sel:DWORD dst_unused:UNUSED_PAD src0_sel:DWORD src1_sel:BYTE_3\n\t"
        "ds_add_rtn_u32 v81, v61, v14 offset:1024\n\t"
        "v_lshlrev_b32_sdwa v62, 2, v62 dst_sel:DWORD dst_unused:UNUSED_PAD src0_sel:DWORD src1_sel:BYTE_3\n\t"
        "ds_add_rtn_u32 v82, v62, v14 offset:1024\n\t"
        "v_lshlrev_b32_sdwa v63, 2, v63 dst_sel:DWORD dst_unused:UNUSED_PAD src0_sel:DWORD src1_sel:BYTE_3\n\t"
        "ds_add_rtn_u32 v83, v63, v14 offset:1024\n\t"
        "s_waitcnt vmcnt(9) lgkmcnt(8)\n\t"
        "s_mov_b64 exec, s[28:29]\n\t"
        "v_mad_u32_u24 v64, v24, s66, v13\n\t"
        "v_mad_u32_u24 v65, v25, s66, v13\n\t"
        "v_mad_u32_u24 v66, v26, s66, v13\n\t"
        "v_mad_u32_u24 v67, v27, s66, v13\n\t"
        "v_lshlrev_b32_sdwa v64, 2, v64 dst_sel:DWORD dst_unused:UNUSED_PAD src0_sel:DWORD src1_sel:BYTE_3\n\t"
        "ds_add_rtn_u32 v84, v64, v14 offset:1024\n\t"
        "v_lshlrev_b32_sdwa v65, 2, v65 dst_sel:DWORD dst_unused:UNUSED_PAD src0_sel:DWORD src1_sel:BYTE_3\n\t"
        "ds_add_rtn_u32 v85, v65, v14 offset:1024\n\t"
        "v_lshlrev_b32_sdwa v66, 2, v66 dst_sel:DWORD dst_unused:UNUSED_PAD src0_sel:DWORD src1_sel:BYTE_3\n\t"
        "ds_add_rtn_u32 v86, v66, v14 offset:1024\n\t"
        "v_lshlrev_b32_sdwa v67, 2, v67 dst_sel:DWORD dst_unused:UNUSED_PAD src0_sel:DWORD src1_sel:BYTE_3\n\t"
        "ds_add_rtn_u32 v87, v67, v14 offset:1024\n\t"
        "s_waitcnt vmcnt(8) lgkmcnt(8)\n\t"
        "s_mov_b64 exec, s[30:31]\n\t"
        "v_mad_u32_u24 v68, v28, s66, v13\n\t"
        "v_mad_u32_u24 v69, v29, s66, v13\n\t"
        "v_mad_u32_u24 v70, v30, s66, v13\n\t"
        "v_mad_u32_u24 v71, v31, s66, v13\n\t"
        "v_lshlrev_b32_sdwa v68, 2, v68 dst_sel:DWORD dst_unused:UNUSED_PAD src0_sel:DWORD src1_sel:BYTE_3\n\t"
        "ds_add_rtn_u32 v88, v68, v14 offset:1024\n\t"
        "v_lshlrev_b32_sdwa v69, 2, v69 dst_sel:DWORD dst_unused:UNUSED_PAD src0_sel:DWORD src1_sel:BYTE_3\n\t"
        "ds_add_rtn_u32 v89, v69, v14 offset:1024\n\t"
        "v_lshlrev_b32_sdwa v70, 2, v70 dst_sel:DWORD dst_unused:UNUSED_PAD src0_sel:DWORD src1_sel:BYTE_3\n\t"
        "ds_add_rtn_u32 v90, v70, v14 offset:1024\n\t"
        "v_lshlrev_b32_sdwa v71, 2, v71 dst_sel:DWORD dst_unused:UNUSED_PAD src0_sel:DWORD src1_sel:BYTE_3\n\t"
        "ds_add_rtn_u32 v91, v71, v14 offset:1024\n\t"
        "s_waitcnt vmcnt(7) lgkmcnt(8)\n\t"
        "s_mov_b64 exec, s[32:33]\n\t"
        "v_mad_u32_u24 v72, v32, s66, v13\n\t"
        "v_mad_u32_u24 v73, v33, s66, v13\n\t"
        "v_mad_u32_u24 v74, v34, s66, v13\n\t"
        "v_mad_u32_u24 v75, v35, s66, v13\n\t"
        "v_lshlrev_b32_sdwa v72, 2, v72 dst_sel:DWORD dst_unused:UNUSED_PAD src0_sel:DWORD src1_sel:BYTE_3\n\t"
        "ds_add_rtn_u32 v92, v72, v14 offset:1024\n\t"
        "v_lshlrev_b32_sdwa v73, 2, v73 dst_sel:DWORD dst_unused:UNUSED_PAD src0_sel:DWORD src1_sel:BYTE_3\n\t"
        "ds_add_rtn_u32 v93, v73, v14 offset:1024\n\t"
        "v_lshlrev_b32_sdwa v74, 2, v74 dst_sel:DWORD dst_unused:UNUSED_PAD src0_sel:DWORD src1_sel:BYTE_3\n\t"
        "ds_add_rtn_u32 v94, v74, v14 offset:1024\n\t"
        "v_lshlrev_b32_sdwa v75, 2, v75 dst_sel:DWORD dst_unused:UNUSED_PAD src0_sel:DWORD src1_sel:BYTE_3\n\t"
        "ds_add_rtn_u32 v95, v75, v14 offset:1024\n\t"
        "s_mov_b64 exec, -1\n\t"
        "v_lshlrev_b32 v7, 2, v1\n\t"
        "ds_read_b32 v4, v7 offset:1028\n\t"
        "ds_read_b32 v5, v7 offset:1284\n\t"
        "v_lshl_add_u32 v96, v56, 5, s52\n\t"
        "v_lshl_add_u32 v97, v57, 5, s52\n\t"
        "v_lshl_add_u32 v98, v58, 5, s52\n\t"
        "v_lshl_add_u32 v99, v59, 5, s52\n\t"
        "v_lshl_add_u32 v100, v60, 5, s52\n\t"
        "v_lshl_add_u32 v101, v61, 5, s52\n\t"
        "v_lshl_add_u32 v102, v62, 5, s52\n\t"
        "v_lshl_add_u32 v103, v63, 5, s52\n\t"
        "v_lshl_add_u32 v104, v64, 5, s52\n\t"
        "v_lshl_add_u32 v105, v65, 5, s52\n\t"
        "v_lshl_add_u32 v106, v66, 5, s52\n\t"
        "v_lshl_add_u32 v107, v67, 5, s52\n\t"
        "v_lshl_add_u32 v108, v68, 5, s52\n\t"
        "v_lshl_add_u32 v109, v69, 5, s52\n\t"
        "v_lshl_add_u32 v110, v70, 5, s52\n\t"
        "v_lshl_add_u32 v111, v71, 5, s52\n\t"
        "v_lshl_add_u32 v112, v72, 5, s52\n\t"
        "v_lshl_add_u32 v113, v73, 5, s52\n\t"
        "v_lshl_add_u32 v114, v74, 5, s52\n\t"
        "v_lshl_add_u32 v115, v75, 5, s52\n\t"
        "s_waitcnt lgkmcnt(0)\n\t"
        "v_max_u32 v6, v4, v5\n\t"
        "v_cmp_lt_u32 s[34:35], s55, v6\n\t"
        "v_min_u32 v76, s55, v76\n\t"
        "v_bfe_u32 v76, v76, 3, 5\n\t"
        "v_lshl_add_u32 v76, v76, 2, v96\n\t"
        "v_min_u32 v77, s55, v77\n\t"
        "v_bfe_u32 v77, v77, 3, 5\n\t"
        "v_lshl_add_u32 v77, v77, 2, v97\n\t"
        "v_min_u32 v78, s55, v78\n\t"
        "v_bfe_u32 v78, v78, 3, 5\n\t"
        "v_lshl_add_u32 v78, v78, 2, v98\n\t"
        "v_min_u32 v79, s55, v79\n\t"
        "v_bfe_u32 v79, v79, 3, 5\n\t"
        "v_lshl_add_u32 v79, v79, 2, v99\n\t"
        "s_waitcnt vmcnt(6) lgkmcnt(8)\n\t"
        "v_lshl_add_u32 v16, v16, 16, v36\n\t"
        "v_lshl_add_u32 v17, v17, 16, v37\n\t"
        "v_lshl_add_u32 v18, v18, 16, v38\n\t"
        "v_lshl_add_u32 v19, v19, 16, v39\n\t"
        "s_mov_b64 exec, s[24:25]\n\t"
        "v_mad_i32_i24 v16, v56, s54, v16\n\t"
        "ds_and_b32 v76, v16\n\t"
        "v_mad_i32_i24 v17, v57, s54, v17\n\t"
        "ds_and_b32 v77, v17\n\t"
        "v_mad_i32_i24 v18, v58, s54, v18\n\t"
        "ds_and_b32 v78, v18\n\t"
        "v_mad_i32_i24 v19, v59, s54, v19\n\t"
        "ds_and_b32 v79, v19\n\t"
        "s_mov_b64 exec, -1\n\t"
        "v_min_u32 v80, s55, v80\n\t"
        "v_bfe_u32 v80, v80, 3, 5\n\t"
        "v_lshl_add_u32 v80, v80, 2, v100\n\t"
        "v_min_u32 v81, s55, v81\n\t"
        "v_bfe_u32 v81, v81, 3, 5\n\t"
        "v_lshl_add_u32 v81, v81, 2, v101\n\t"
        "v_min_u32 v82, s55, v82\n\t"
        "v_bfe_u32 v82, v82, 3, 5\n\t"
        "v_lshl_add_u32 v82, v82, 2, v102\n\t"
        "v_min_u32 v83, s55, v83\n\t"
        "v_bfe_u32 v83, v83, 3, 5\n\t"
        "v_lshl_add_u32 v83, v83, 2, v103\n\t"
        "s_waitcnt vmcnt(5) lgkmcnt(8)\n\t"
        "v_lshl_add_u32 v20, v20, 16, v40\n\t"
        "v_lshl_add_u32 v21, v21, 16, v41\n\t"
        "v_lshl_add_u32 v22, v22, 16, v42\n\t"
        "v_lshl_add_u32 v23, v23, 16, v43\n\t"
        "s_mov_b64 exec, s[26:27]\n\t"
        "v_mad_i32_i24 v20, v60, s54, v20\n\t"
        "ds_and_b32 v80, v20\n\t"
        "v_mad_i32_i24 v21, v61, s54, v21\n\t"
        "ds_and_b32 v81, v21\n\t"
        "v_mad_i32_i24 v22, v62, s54, v22\n\t"
        "ds_and_b32 v82, v22\n\t"
        "v_mad_i32_i24 v23, v63, s54, v23\n\t"
        "ds_and_b32 v83, v23\n\t"
        "s_mov_b64 exec, -1\n\t"
        "v_min_u32 v84, s55, v84\n\t"
        "v_bfe_u32 v84, v84, 3, 5\n\t"
        "v_lshl_add_u32 v84, v84, 2, v104\n\t"
        "v_min_u32 v85, s55, v85\n\t"
        "v_bfe_u32 v85, v85, 3, 5\n\t"
        "v_lshl_add_u32 v85, v85, 2, v105\n\t"
        "v_min_u32 v86, s55, v86\n\t"
        "v_bfe_u32 v86, v86, 3, 5\n\t"
        "v_lshl_add_u32 v86, v86, 2, v106\n\t"
        "v_min_u32 v87, s55, v87\n\t"
        "v_bfe_u32 v87, v87, 3, 5\n\t"
        "v_lshl_add_u32 v87, v87, 2, v107\n\t"
        "s_waitcnt vmcnt(4) lgkmcnt(8)\n\t"
        "v_lshl_add_u32 v24, v24, 16, v44\n\t"
        "v_lshl_add_u32 v25, v25, 16, v45\n\t"
        "v_lshl_add_u32 v26, v26, 16, v46\n\t"
        "v_lshl_add_u32 v27, v27, 16, v47\n\t"
        "s_mov_b64 exec, s[28:29]\n\t"
        "v_mad_i32_i24 v24, v64, s54, v24\n\t"
        "ds_and_b32 v84, v24\n\t"
        "v_mad_i32_i24 v25, v65, s54, v25\n\t"
        "ds_and_b32 v85, v25\n\t"
        "v_mad_i32_i24 v26, v66, s54, v26\n\t"
        "ds_and_b32 v86, v26\n\t"
        "v_mad_i32_i24 v27, v67, s54, v27\n\t"
        "ds_and_b32 v87, v27\n\t"
        "s_mov_b64 exec, -1\n\t"
        "v_min_u32 v88, s55, v88\n\t"
        "v_bfe_u32 v88, v88, 3, 5\n\t"
        "v_lshl_add_u32 v88, v88, 2, v108\n\t"
        "v_min_u32 v89, s55, v89\n\t"
        "v_bfe_u32 v89, v89, 3, 5\n\t"
        "v_lshl_add_u32 v89, v89, 2, v109\n\t"
        "v_min_u32 v90, s55, v90\n\t"
        "v_bfe_u32 v90, v90, 3, 5\n\t"
        "v_lshl_add_u32 v90, v90, 2, v110\n\t"
        "v_min_u32 v91, s55, v91\n\t"
        "v_bfe_u32 v91, v91, 3, 5\n\t"
        "v_lshl_add_u32 v91, v91, 2, v111\n\t"
        "s_waitcnt vmcnt(3) lgkmcnt(8)\n\t"
        "v_lshl_add_u32 v28, v28, 16, v48\n\t"
        "v_lshl_add_u32 v29, v29, 16, v49\n\t"
        "v_lshl_add_u32 v30, v30, 16, v50\n\t"
        "v_lshl_add_u32 v31, v31, 16, v51\n\t"
        "s_mov_b64 exec, s[30:31]\n\t"
        "v_mad_i32_i24 v28, v68, s54, v28\n\t"
        "ds_and_b32 v88, v28\n\t"
        "v_mad_i32_i24 v29, v69, s54, v29\n\t"
        "ds_and_b32 v89, v29\n\t"
        "v_mad_i32_i24 v30, v70, s54, v30\n\t"
        "ds_and_b32 v90, v30\n\t"
        "v_mad_i32_i24 v31, v71, s54, v31\n\t"
        "ds_and_b32 v91, v31\n\t"
        "s_mov_b64 exec, -1\n\t"
        "v_min_u32 v92, s55, v92\n\t"
        "v_bfe_u32 v92, v92, 3, 5\n\t"
        "v_lshl_add_u32 v92, v92, 2, v112\n\t"
        "v_min_u32 v93, s55, v93\n\t"
        "v_bfe_u32 v93, v93, 3, 5\n\t"
        "v_lshl_add_u32 v93, v93, 2, v113\n\t"
        "v_min_u32 v94, s55, v94\n\t"
        "v_bfe_u32 v94, v94, 3, 5\n\t"
        "v_lshl_add_u32 v94, v94, 2, v114\n\t"
        "v_min_u32 v95, s55, v95\n\t"
        "v_bfe_u32 v95, v95, 3, 5\n\t"
        "v_lshl_add_u32 v95, v95, 2, v115\n\t"
        "s_waitcnt vmcnt(2) lgkmcnt(8)\n\t"
        "v_lshl_add_u32 v32, v32, 16, v52\n\t"
        "v_lshl_add_u32 v33, v33, 16, v53\n\t"
        "v_lshl_add_u32 v34, v34, 16, v54\n\t"
        "v_lshl_add_u32 v35, v35, 16, v55\n\t"
        "s_mov_b64 exec, s[32:33]\n\t"
        "v_mad_i32_i24 v32, v72, s54, v32\n\t"
        "ds_and_b32 v92, v32\n\t"
        "v_mad_i32_i24 v33, v73, s54, v33\n\t"
        "ds_and_b32 v93, v33\n\t"
        "v_mad_i32_i24 v34, v74, s54, v34\n\t"
        "ds_and_b32 v94, v34\n\t"
        "v_mad_i32_i24 v35, v75, s54, v35\n\t"
        "ds_and_b32 v95, v35\n\t"
        "s_mov_b64 exec, -1\n\t"
        "v_add_u32 v6, s80, v1\n\t"
        "v_add_u32 v7, 64, v6\n\t"
        "v_cmp_gt_u32 s[36:37], s64, v6\n\t"
        "v_cmp_gt_u32 s[38:39], s64, v7\n\t"
        "s_waitcnt vmcnt(0)\n\t"
        "v_xor_b32 v116, v116, v6\n\t"
        "v_bitop3_b32 v116, v117, v116, v6 bitop3:0xde\n\t"
        "v_bitop3_b32 v116, v118, v116, v6 bitop3:0xde\n\t"
        "v_bitop3_b32 v116, v119, v116, v6 bitop3:0xde\n\t"
        "v_xor_b32 v120, v120, v7\n\t"
        "v_bitop3_b32 v120, v121, v120, v7 bitop3:0xde\n\t"
        "v_bitop3_b32 v120, v122, v120, v7 bitop3:0xde\n\t"
        "v_bitop3_b32 v120, v123, v120, v7 bitop3:0xde\n\t"
        "v_cmp_ne_u32 s[40:41], 0, v116\n\t"
        "v_cmp_ne_u32 s[42:43], 0, v120\n\t"
        "s_and_b64 s[38:39], s[38:39], 0xffff\n\t"
        "s_and_b64 s[36:37], s[36:37], s[40:41]\n\t"
        "s_and_b64 s[38:39], s[38:39], s[42:43]\n\t"
        "s_or_b64 s[36:37], s[36:37], s[38:39]\n\t"
        "s_cselect_b32 s67, 1, s67\n\t"
        "s_cmp_lg_u64 s[34:35], 0\n\t"
        "s_cbranch_scc1 .L7_ov\n"
        ".L7_ov2:\n\t"
        "s_waitcnt lgkmcnt(4)\n\t"
        "ds_read_b128 v[16:19], v15 offset:0\n\t"
        "ds_read_b128 v[20:23], v15 offset:1024\n\t"
        "ds_read_b128 v[24:27], v15 offset:2048\n\t"
        "ds_read_b128 v[28:31], v15 offset:3072\n\t"
        "ds_read_b128 v[32:35], v15 offset:4096\n\t"
        "ds_read_b128 v[36:39], v15 offset:5120\n\t"
        "ds_read_b128 v[40:43], v15 offset:6144\n\t"
        "ds_read_b128 v[44:47], v15 offset:7168\n\t"
        "s_waitcnt lgkmcnt(6)\n\t"
        "ds_read_b128 v[48:51], v15 offset:8192\n\t"
        "ds_read_b128 v[52:55], v15 offset:9216\n\t"
        "ds_read_b128 v[56:59], v15 offset:10240\n\t"
        "ds_read_b128 v[60:63], v15 offset:11264\n\t"
        "ds_read_b128 v[64:67], v15 offset:12288\n\t"
        "ds_read_b128 v[68:71], v15 offset:13312\n\t"
        "ds_read_b128 v[72:75], v15 offset:14336\n\t"
        "ds_read_b128 v[76:79], v15 offset:15360\n\t"
        "s_mov_b64 exec, 1\n\t"
        "ds_add_u32 v2, v3 offset:72\n\t"
        "s_mov_b64 exec, -1\n\t"
        "s_and_b32 s78, s67, 1\n\t"
        "s_lshl_b32 s78, s78, 24\n\t"
        "s_waitcnt lgkmcnt(15)\n\t"
        "v_xor_b32 v19, s78, v19\n\t"
        "buffer_store_dwordx4 v[16:19], v12, s[20:23], s48 offen offset:0 sc1\n\t"
        "s_waitcnt lgkmcnt(15)\n\t"
        "v_xor_b32 v23, s78, v23\n\t"
        "buffer_store_dwordx4 v[20:23], v12, s[20:23], s48 offen offset:1024 sc1\n\t"
        "s_waitcnt lgkmcnt(14)\n\t"
        "v_xor_b32 v27, s78, v27\n\t"
        "buffer_store_dwordx4 v[24:27], v12, s[20:23], s48 offen offset:2048 sc1\n\t"
        "s_waitcnt lgkmcnt(13)\n\t"
        "v_xor_b32 v31, s78, v31\n\t"
        "buffer_store_dwordx4 v[28:31], v12, s[20:23], s48 offen offset:3072 sc1\n\t"
        "s_waitcnt lgkmcnt(12)\n\t"
        "v_xor_b32 v35, s78, v35\n\t"
        "buffer_store_dwordx4 v[32:35], v12, s[20:23], s49 offen offset:0 sc1\n\t"
        "s_waitcnt lgkmcnt(11)\n\t"
        "v_xor_b32 v39, s78, v39\n\t"
        "buffer_store_dwordx4 v[36:39], v12, s[20:23], s49 offen offset:1024 sc1\n\t"
        "s_waitcnt lgkmcnt(10)\n\t"
        "v_xor_b32 v43, s78, v43\n\t"
        "buffer_store_dwordx4 v[40:43], v12, s[20:23], s49 offen offset:2048 sc1\n\t"
        "s_waitcnt lgkmcnt(9)\n\t"
        "v_xor_b32 v47, s78, v47\n\t"
        "buffer_store_dwordx4 v[44:47], v12, s[20:23], s49 offen offset:3072 sc1\n\t"
        "s_waitcnt lgkmcnt(8)\n\t"
        "v_xor_b32 v51, s78, v51\n\t"
        "buffer_store_dwordx4 v[48:51], v12, s[20:23], s50 offen offset:0 sc1\n\t"
        "s_waitcnt lgkmcnt(7)\n\t"
        "v_xor_b32 v55, s78, v55\n\t"
        "buffer_store_dwordx4 v[52:55], v12, s[20:23], s50 offen offset:1024 sc1\n\t"
        "s_waitcnt lgkmcnt(6)\n\t"
        "v_xor_b32 v59, s78, v59\n\t"
        "buffer_store_dwordx4 v[56:59], v12, s[20:23], s50 offen offset:2048 sc1\n\t"
        "s_waitcnt lgkmcnt(5)\n\t"
        "v_xor_b32 v63, s78, v63\n\t"
        "buffer_store_dwordx4 v[60:63], v12, s[20:23], s50 offen offset:3072 sc1\n\t"
        "s_waitcnt lgkmcnt(4)\n\t"
        "v_xor_b32 v67, s78, v67\n\t"
        "buffer_store_dwordx4 v[64:67], v12, s[20:23], s51 offen offset:0 sc1\n\t"
        "s_waitcnt lgkmcnt(3)\n\t"
        "v_xor_b32 v71, s78, v71\n\t"
        "buffer_store_dwordx4 v[68:71], v12, s[20:23], s51 offen offset:1024 sc1\n\t"
        "s_waitcnt lgkmcnt(2)\n\t"
        "v_xor_b32 v75, s78, v75\n\t"
        "buffer_store_dwordx4 v[72:75], v12, s[20:23], s51 offen offset:2048 sc1\n\t"
        "s_waitcnt lgkmcnt(1)\n\t"
        "v_xor_b32 v79, s78, v79\n\t"
        "buffer_store_dwordx4 v[76:79], v12, s[20:23], s51 offen offset:3072 sc1\n\t"
        "s_mov_b64 exec, 1\n\t"
        "ds_add_u32 v2, v3 offset:44\n\t"
        "s_mov_b64 exec, -1\n\t"
        "s_branch .L7_Q\n"
        ".L7_ov:\n\t"
        "v_lshlrev_b32 v7, 7, v1\n\t"
        "v_add_u32 v7, 84092, v7\n\t"
        "v_add_u32 v11, -1, v11\n\t"
        "v_cmpx_lt_u32 vcc, 1056, v4\n\t"
        "ds_write_b32 v7, v11\n\t"
        "s_mov_b64 exec, -1\n\t"
        "v_cmpx_lt_u32 vcc, 1056, v5\n\t"
        "ds_write_b32 v7, v11 offset:8192\n\t"
        "s_mov_b64 exec, -1\n\t"
        "s_branch .L7_ov2\n"
        ".L7_V2:\n\t"
        "s_barrier\n\t"
        "v_and_b32 v5, 7, v1\n\t"
        "v_mul_u32_u24 v5, 0x420, v5\n\t"
        "v_and_b32 v6, 8, v1\n\t"
        "v_lshl_add_u32 v5, v6, 6, v5\n\t"
        "v_and_b32 v6, 48, v1\n\t"
        "v_add_u32 v5, v5, v6\n\t"
        "v_add_u32 v5, 83968, v5\n\t"
        "s_lshr_b32 s78, s81, 1\n\t"
        "s_lshl3_add_u32 s78, s78, s80\n\t"
        "s_and_b32 s79, s81, 1\n\t"
        "s_lshl1_add_u32 s78, s78, s79\n\t"
        "s_lshl_b32 s82, s78, 10\n\t"
        "s_add_u32 s82, s82, 131072\n\t"
        "s_waitcnt lgkmcnt(0)\n\t"
        "s_add_u32 s68, s68, 1\n\t"
        "v_mov_b32 v4, s68\n\t"
        "ds_write_b32 v2, v4 offset:76\n\t"
        "s_and_b32 s73, s68, 1\n\t"
        "s_cselect_b32 s73, 1, 0x10000\n\t"
        "v_mov_b32 v16, s73\n\t"
        "v_and_b32 v17, 7, v1\n\t"
        "v_add_u32 v17, 1, v17\n\t"
        "v_lshrrev_b32 v17, v17, s68\n\t"
        "s_mov_b32 s49, 0x10001\n\t"
        "v_and_b32 v17, s49, v17\n\t"
        "s_mov_b32 s48, 0x3db8aa3b\n\t"
        "s_waitcnt vmcnt(0)\n\t"
        "ds_read_b128 v[32:35], v5 offset:0\n\t"
        "ds_read_b128 v[36:39], v5 offset:8448\n\t"
        "ds_read_b128 v[40:43], v5 offset:16896\n\t"
        "ds_read_b128 v[44:47], v5 offset:64\n\t"
        "ds_read_b128 v[48:51], v5 offset:8512\n\t"
        "ds_read_b128 v[52:55], v5 offset:16960\n\t"
        "ds_read_b128 v[56:59], v5 offset:128\n\t"
        "ds_read_b128 v[60:63], v5 offset:8576\n\t"
        "ds_read_b128 v[64:67], v5 offset:17024\n\t"
        "ds_read_b128 v[68:71], v5 offset:192\n\t"
        "ds_read_b128 v[72:75], v5 offset:8640\n\t"
        "ds_read_b128 v[76:79], v5 offset:17088\n\t"
        "s_waitcnt lgkmcnt(9)\n\t"
        "v_mfma_f32_16x16x32_bf16 v[20:23], v[36:39], v[32:35], 0\n\t"
        "v_mfma_f32_16x16x32_bf16 v[24:27], v[40:43], v[32:35], 0\n\t"
        "ds_read_b128 v[80:83], v5 offset:256\n\t"
        "ds_read_b128 v[84:87], v5 offset:8704\n\t"
        "ds_read_b128 v[88:91], v5 offset:17152\n\t"
        "s_waitcnt lgkmcnt(9)\n\t"
        "v_mfma_f32_16x16x32_bf16 v[20:23], v[48:51], v[44:47], v[20:23]\n\t"
        "v_mfma_f32_16x16x32_bf16 v[24:27], v[52:55], v[44:47], v[24:27]\n\t"
        "ds_read_b128 v[92:95], v5 offset:320\n\t"
        "ds_read_b128 v[96:99], v5 offset:8768\n\t"
        "ds_read_b128 v[100:103], v5 offset:17216\n\t"
        "s_waitcnt lgkmcnt(9)\n\t"
        "v_mfma_f32_16x16x32_bf16 v[20:23], v[60:63], v[56:59], v[20:23]\n\t"
        "v_mfma_f32_16x16x32_bf16 v[24:27], v[64:67], v[56:59], v[24:27]\n\t"
        "ds_read_b128 v[104:107], v5 offset:384\n\t"
        "ds_read_b128 v[108:111], v5 offset:8832\n\t"
        "ds_read_b128 v[112:115], v5 offset:17280\n\t"
        "s_waitcnt lgkmcnt(9)\n\t"
        "v_mfma_f32_16x16x32_bf16 v[20:23], v[72:75], v[68:71], v[20:23]\n\t"
        "v_mfma_f32_16x16x32_bf16 v[24:27], v[76:79], v[68:71], v[24:27]\n\t"
        "ds_read_b128 v[116:119], v5 offset:448\n\t"
        "ds_read_b128 v[120:123], v5 offset:8896\n\t"
        "ds_read_b128 v[124:127], v5 offset:17344\n\t"
        "s_waitcnt lgkmcnt(9)\n\t"
        "v_mfma_f32_16x16x32_bf16 v[20:23], v[84:87], v[80:83], v[20:23]\n\t"
        "v_mfma_f32_16x16x32_bf16 v[24:27], v[88:91], v[80:83], v[24:27]\n\t"
        "s_mov_b64 exec, 1\n\t"
        "ds_add_u32 v2, v3 offset:72\n\t"
        "s_mov_b64 exec, -1\n\t"
        "s_waitcnt lgkmcnt(7)\n\t"
        "v_mfma_f32_16x16x32_bf16 v[20:23], v[96:99], v[92:95], v[20:23]\n\t"
        "v_mfma_f32_16x16x32_bf16 v[24:27], v[100:103], v[92:95], v[24:27]\n\t"
        "s_waitcnt lgkmcnt(4)\n\t"
        "v_mfma_f32_16x16x32_bf16 v[20:23], v[108:111], v[104:107], v[20:23]\n\t"
        "v_mfma_f32_16x16x32_bf16 v[24:27], v[112:115], v[104:107], v[24:27]\n\t"
        "s_waitcnt lgkmcnt(1)\n\t"
        "v_mfma_f32_16x16x32_bf16 v[20:23], v[120:123], v[116:119], v[20:23]\n\t"
        "v_mfma_f32_16x16x32_bf16 v[24:27], v[124:127], v[116:119], v[24:27]\n\t"
        "s_nop 6\n\t"
        "v_mul_f32 v20, s48, v20\n\t"
        "v_mul_f32 v21, s48, v21\n\t"
        "v_mul_f32 v22, s48, v22\n\t"
        "v_mul_f32 v23, s48, v23\n\t"
        "v_mul_f32 v24, s48, v24\n\t"
        "v_mul_f32 v25, s48, v25\n\t"
        "v_mul_f32 v26, s48, v26\n\t"
        "v_mul_f32 v27, s48, v27\n\t"
        "s_mov_b32 s48, 0x07060302\n\t"
        "v_add_u32 v20, 0x10000, v20\n\t"
        "v_add_u32 v21, 0x10000, v21\n\t"
        "v_add_u32 v22, 0x10000, v22\n\t"
        "v_add_u32 v23, 0x10000, v23\n\t"
        "v_perm_b32 v20, v21, v20, s48\n\t"
        "v_perm_b32 v21, v23, v22, s48\n\t"
        "v_cvt_pk_bf16_f32 v22, v24, v25\n\t"
        "v_cvt_pk_bf16_f32 v23, v26, v27\n\t"
        "v_bfi_b32 v20, s49, v16, v20\n\t"
        "v_bfi_b32 v21, s49, v17, v21\n\t"
        "buffer_store_dwordx4 v[20:23], v12, s[20:23], s82 offen sc1\n\t"
        "s_mov_b64 exec, 1\n\t"
        "ds_add_u32 v2, v3 offset:44\n\t"
        "s_mov_b64 exec, -1\n\t"
        "s_branch .L7_Q\n"
        ".L7_T2:\n\t"
        "v_and_b32 v6, 0x380, v6\n\t"
        "s_and_b32 s82, s82, -1024\n\t"
        "v_mov_b32 v9, 4\n"
        ".L7_g40:\n\t"
        "s_memrealtime s[78:79]\n\t"
        "s_waitcnt lgkmcnt(0)\n\t"
        "s_sub_u32 s78, s78, s74\n\t"
        "s_cmp_ge_i32 s78, 170\n\t"
        "s_cbranch_scc1 .L7_h41\n\t"
        "s_sleep 1\n\t"
        "s_branch .L7_g40\n"
        ".L7_h41:\n"
        ".L7_b1:\n\t"
        "buffer_load_dword v7, v6, s[20:23], s82 offen sc1\n\t"
        "s_waitcnt vmcnt(0)\n\t"
        "v_subrev_u32 v7, s74, v7\n\t"
        "v_add_u32 v8, 0x3e8, v7\n\t"
        "v_cmp_lt_u32 vcc, 0x7d0, v8\n\t"
        "s_cbranch_vccz .L7_b2\n\t"
        "s_mov_b64 exec, 1\n\t"
        "ds_add_u32 v2, v9 offset:84\n\t"
        "s_mov_b64 exec, -1\n\t"
        "v_mov_b32 v9, 0\n\t"
        "v_mov_b32 v7, 0\n\t"
        "s_sleep 4\n\t"
        "s_memrealtime s[78:79]\n\t"
        "s_waitcnt lgkmcnt(0)\n\t"
        "s_sub_u32 s78, s78, s74\n\t"
        "s_cmp_lt_u32 s78, 0x4b0\n\t"
        "s_cbranch_scc1 .L7_b1\n"
        ".L7_b2:\n\t"
        "v_max_i32_dpp v7, v7, v7 row_half_mirror row_mask:0xf bank_mask:0xf\n\t"
        "s_nop 1\n\t"
        "v_max_i32_dpp v7, v7, v7 quad_perm:[1,0,3,2] row_mask:0xf bank_mask:0xf\n\t"
        "s_nop 1\n\t"
        "v_max_i32_dpp v7, v7, v7 quad_perm:[2,3,0,1] row_mask:0xf bank_mask:0xf\n\t"
        "v_add_u32 v7, 1, v7\n\t"
        "s_mov_b64 exec, 1\n\t"
        "ds_add_u32 v2, v7 offset:92\n\t"
        "s_mov_b64 exec, -1\n\t"
        "s_branch .L7_Q0\n"
        ".L7_P2:\n\t"
        "v_lshlrev_b32 v4, 4, v1\n\t"
        "v_add_u32 v4, 67584, v4\n\t"
        "v_mul_u32_u24 v5, 200, v1\n\t"
        "v_add_u32 v5, 67584, v5\n\t"
        "v_mov_b32 v8, 0xffff\n\t"
        "v_mov_b32 v9, v8\n\t"
        "v_mov_b64 v[10:11], v[8:9]\n\t"
        "s_waitcnt lgkmcnt(0)\n\t"
        "s_mul_i32 s72, s71, s65\n\t"
        "s_barrier\n\t"
        "ds_write_b128 v4, v[8:11] offset:0\n\t"
        "ds_write_b128 v4, v[8:11] offset:1024\n\t"
        "ds_write_b128 v4, v[8:11] offset:2048\n\t"
        "ds_write_b128 v4, v[8:11] offset:3072\n\t"
        "ds_write_b128 v4, v[8:11] offset:4096\n\t"
        "ds_write_b128 v4, v[8:11] offset:5120\n\t"
        "ds_write_b128 v4, v[8:11] offset:6144\n\t"
        "ds_write_b128 v4, v[8:11] offset:7168\n\t"
        "ds_write_b128 v4, v[8:11] offset:8192\n\t"
        "ds_write_b128 v4, v[8:11] offset:9216\n\t"
        "ds_write_b128 v4, v[8:11] offset:10240\n\t"
        "ds_write_b128 v4, v[8:11] offset:11264\n\t"
        "s_waitcnt lgkmcnt(6)\n\t"
        "ds_write_b128 v4, v[8:11] offset:12288\n\t"
        "ds_write_b128 v4, v[8:11] offset:13312\n\t"
        "ds_write_b128 v4, v[8:11] offset:14336\n\t"
        "ds_write_b128 v4, v[8:11] offset:15360\n\t"
        "ds_write_b32 v5, v2 offset:192\n\t"
        "s_mov_b64 exec, 0xffff\n\t"
        "ds_write_b32 v5, v2 offset:12992\n\t"
        "s_mov_b64 exec, 1\n\t"
        "ds_add_u32 v2, v3 offset:8\n\t"
        "s_mov_b64 exec, -1\n\t"
        "s_branch .L7_Q0\n"
        ".L7_R2:\n\t"
        "s_barrier\n\t"
        "s_sub_u32 s79, s64, s72\n\t"
        "s_min_i32 s79, s79, s65\n\t"
        "s_max_i32 s79, s79, 0\n\t"
        "v_mov_b32 v32, v1\n\t"
        "v_add_u32 v33, 64, v1\n\t"
        "v_cmp_gt_u32 s[32:33], s79, v32\n\t"
        "v_cmp_gt_u32 s[34:35], s79, v33\n\t"
        "v_cmp_gt_u32 s[36:37], 40, v1\n\t"
        "s_sub_u32 s78, 0x100, s65\n\t"
        "v_add_u32 v34, s78, v32\n\t"
        "v_add_u32 v6, 0xffffffb0, v34\n\t"
        "v_cmp_gt_u32 vcc, 0x100, v34\n\t"
        "s_nop 1\n\t"
        "v_cndmask_b32 v34, v6, v34, vcc\n\t"
        "v_add_u32 v35, s78, v33\n\t"
        "v_add_u32 v6, 0xffffffb0, v35\n\t"
        "v_cmp_gt_u32 vcc, 0x100, v35\n\t"
        "s_nop 1\n\t"
        "v_cndmask_b32 v35, v6, v35, vcc\n\t"
        "v_mov_b32 v12, 0x8000\n\t"
        "v_cndmask_b32 v44, v12, 0, s[32:33]\n\t"
        "v_cndmask_b32 v45, v12, 0, s[34:35]\n\t"
        "v_cndmask_b32 v46, 63, 31, s[36:37]\n\t"
        "v_mov_b32 v47, 63\n\t"
        "s_waitcnt vmcnt(0)\n\t"
        "v_lshlrev_b32 v16, 16, v16\n\t"
        "v_lshlrev_b32 v17, 16, v17\n\t"
        "v_mul_f32 v36, v16, v16\n\t"
        "v_mul_f32 v37, v17, v17\n\t"
        "v_rcp_f32 v36, v36\n\t"
        "v_rcp_f32 v37, v37\n\t"
        "v_or3_b32 v16, v16, v34, v44\n\t"
        "v_or3_b32 v17, v17, v35, v45\n\t"
        "v_add_f32 v36, 0xc0600000, v36\n\t"
        "v_add_f32 v37, 0xc0600000, v37\n\t"
        "v_cvt_u32_f32 v36, v36\n\t"
        "v_cvt_u32_f32 v37, v37\n\t"
        "v_min_u32 v36, 31, v36\n\t"
        "v_min_u32 v37, 31, v37\n\t"
        "v_cndmask_b32 v36, 0, v36, s[32:33]\n\t"
        "v_cndmask_b32 v37, 0, v37, s[34:35]\n\t"
        "v_sub_u32 v36, v46, v36\n\t"
        "v_sub_u32 v37, v47, v37\n\t"
        "v_lshlrev_b32 v36, 2, v36\n\t"
        "v_lshlrev_b32 v37, 2, v37\n\t"
        "ds_add_rtn_u32 v40, v36, v3 offset:256\n\t"
        "s_mov_b64 exec, 0xffff\n\t"
        "ds_add_rtn_u32 v41, v37, v3 offset:256\n\t"
        "s_mov_b64 exec, -1\n\t"
        "v_lshlrev_b32 v9, 2, v1\n\t"
        "ds_read_b32 v10, v9 offset:256\n\t"
        "s_cmp_eq_u32 s70, 0\n\t"
        "s_cselect_b32 s78, 56, 8\n\t"
        "s_cselect_b32 s79, 8, 56\n\t"
        "v_mov_b32 v49, -40\n\t"
        "v_cndmask_b32 v48, v49, 0, s[36:37]\n\t"
        "s_mov_b32 s40, 0x23108954\n\t"
        "s_mov_b32 s41, 0x67\n\t"
        "v_mov_b32 v13, s78\n\t"
        "v_mov_b32 v14, s79\n\t"
        "v_cndmask_b32 v13, v14, v13, s[36:37]\n\t"
        "s_waitcnt lgkmcnt(0)\n\t"
        "v_mov_b32 v11, v10\n\t"
        "s_nop 1\n\t"
        "v_add_u32_dpp v11, v11, v11 row_shr:1 row_mask:0xf bank_mask:0xf bound_ctrl:1\n\t"
        "s_nop 1\n\t"
        "v_add_u32_dpp v11, v11, v11 row_shr:2 row_mask:0xf bank_mask:0xf bound_ctrl:1\n\t"
        "s_nop 1\n\t"
        "v_add_u32_dpp v11, v11, v11 row_shr:4 row_mask:0xf bank_mask:0xf bound_ctrl:1\n\t"
        "s_nop 1\n\t"
        "v_add_u32_dpp v11, v11, v11 row_shr:8 row_mask:0xf bank_mask:0xf bound_ctrl:1\n\t"
        "s_nop 1\n\t"
        "v_add_u32_dpp v11, v11, v11 row_bcast:15 row_mask:0xa bank_mask:0xf\n\t"
        "s_nop 1\n\t"
        "v_add_u32_dpp v11, v11, v11 row_bcast:31 row_mask:0xc bank_mask:0xf\n\t"
        "v_sub_u32 v11, v11, v10\n\t"
        "ds_bpermute_b32 v36, v36, v11\n\t"
        "ds_bpermute_b32 v37, v37, v11\n\t"
        "v_lshlrev_b32 v9, 1, v1\n\t"
        "s_waitcnt lgkmcnt(0)\n\t"
        "v_add3_u32 v36, v36, v40, v48\n\t"
        "v_and_b32 v6, -4, v36\n\t"
        "v_lshrrev_b64 v[10:11], v6, s[40:41]\n\t"
        "v_and_b32 v10, 15, v10\n\t"
        "v_and_b32 v6, 3, v36\n\t"
        "v_lshl_add_u32 v6, v10, 2, v6\n\t"
        "v_add_u32 v36, v6, v13\n\t"
        "v_and_b32 v6, 15, v36\n\t"
        "v_lshlrev_b32 v6, 4, v6\n\t"
        "v_lshl_add_u32 v6, v36, 8, v6\n\t"
        "v_lshlrev_b32 v36, 2, v36\n\t"
        "ds_write_b32 v36, v16 offset:512\n\t"
        "ds_write_b16 v9, v6 offset:1792\n\t"
        "v_add3_u32 v37, v37, v41, v49\n\t"
        "v_and_b32 v6, -4, v37\n\t"
        "v_lshrrev_b64 v[10:11], v6, s[40:41]\n\t"
        "v_and_b32 v10, 15, v10\n\t"
        "v_and_b32 v6, 3, v37\n\t"
        "v_lshl_add_u32 v6, v10, 2, v6\n\t"
        "v_add_u32 v37, v6, v14\n\t"
        "v_and_b32 v6, 15, v37\n\t"
        "v_lshlrev_b32 v6, 4, v6\n\t"
        "v_lshl_add_u32 v6, v37, 8, v6\n\t"
        "v_lshlrev_b32 v37, 2, v37\n\t"
        "s_mov_b64 exec, 0xffff\n\t"
        "ds_write_b32 v37, v17 offset:512\n\t"
        "ds_write_b16 v9, v6 offset:1920\n\t"
        "s_mov_b64 exec, 1\n\t"
        "ds_add_u32 v2, v3 offset:8\n\t"
        "s_mov_b64 exec, -1\n"
        ".L7_w42:\n\t"
        "ds_read_b64 v[4:5], v2 offset:72\n\t"
        "s_waitcnt lgkmcnt(0)\n\t"
        "v_cmp_gt_u16 vcc, 1, v4\n\t"
        "s_cbranch_vccz .L7_d43\n\t"
        "s_sleep 2\n\t"
        "s_sub_i32 s83, s83, 1\n\t"
        "s_cmp_gt_i32 s83, 0\n\t"
        "s_cbranch_scc1 .L7_w42\n"
        ".L7_d43:\n\t"
        "v_readfirstlane_b32 s68, v5\n\t"
        "v_lshlrev_b32 v22, 4, v1\n\t"
        "v_add_u32 v9, 157952, v22\n\t"
        "v_and_b32 v10, 60, v1\n\t"
        "ds_read_b32 v24, v10 offset:704\n\t"
        "ds_read_b32 v25, v10 offset:768\n\t"
        "ds_read_b32 v26, v10 offset:832\n\t"
        "v_and_b32 v11, 15, v1\n\t"
        "v_lshrrev_b32 v12, 4, v1\n\t"
        "v_xor_b32 v16, v11, v12\n\t"
        "v_lshlrev_b32 v16, 4, v16\n\t"
        "v_lshl_add_u32 v16, v11, 8, v16\n\t"
        "v_xor_b32 v17, 64, v16\n\t"
        "v_xor_b32 v18, 128, v16\n\t"
        "v_xor_b32 v19, 192, v16\n\t"
        "ds_write_b128 v9, v[16:19]\n\t"
        "s_and_b32 s73, s68, 1\n\t"
        "s_cselect_b32 s73, 1, 0x10000\n\t"
        "v_mov_b32 v20, s73\n\t"
        "v_and_b32 v21, 7, v1\n\t"
        "v_add_u32 v21, 1, v21\n\t"
        "v_lshrrev_b32 v21, v21, s68\n\t"
        "s_mov_b32 s49, 0x10001\n\t"
        "v_and_b32 v21, s49, v21\n\t"
        "v_or_b32 v23, 0x80, v22\n\t"
        "ds_write_b128 v9, v[20:23] offset:1024\n\t"
        "s_add_u32 s78, s72, s65\n\t"
        "s_sub_u32 s78, s78, 0x100\n\t"
        "s_lshl_b32 s78, s78, 9\n\t"
        "v_and_b32 v13, 48, v22\n\t"
        "v_add_u32 v14, s78, v13\n\t"
        "s_movk_i32 s78, 0x200\n\t"
        "v_bfe_u32 v27, v1, 1, 2\n\t"
        "v_xor_b32 v27, v27, v12\n\t"
        "v_lshlrev_b32 v27, 4, v27\n\t"
        "v_lshl_add_u32 v27, v11, 6, v27\n\t"
        "v_lshlrev_b32 v28, 1, v1\n\t"
        "v_and_b32 v28, 48, v28\n\t"
        "v_xor_b32 v28, v28, v22\n\t"
        "s_waitcnt lgkmcnt(2)\n\t"
        "v_mad_u32_u16 v24, v24, s78, v14\n\t"
        "v_mad_u32_u16 v25, v25, s78, v14\n\t"
        "v_mad_u32_u16 v26, v26, s78, v14\n\t"
        "s_mov_b32 exec_hi, 0\n\t"
        "v_mov_b32 v24, -2.0\n\t"
        "s_mov_b64 exec, -1\n\t"
        "ds_write_b128 v9, v[24:27] offset:2048\n\t"
        "s_mov_b32 s78, 0x3f80\n\t"
        "s_mov_b32 s79, 0\n\t"
        "v_lshlrev_b64 v[32:33], v13, s[78:79]\n\t"
        "s_mov_b32 s78, 0x00f0000f\n\t"
        "s_mov_b32 s79, 0xf0000f00\n\t"
        "v_cndmask_b32 v32, 0, v32, s[78:79]\n\t"
        "v_cndmask_b32 v33, 0, v33, s[78:79]\n\t"
        "v_mov_b64 v[30:31], 0\n\t"
        "v_mov_b64 v[34:35], 0\n\t"
        "ds_write_b128 v9, v[28:31] offset:3072\n\t"
        "ds_write_b128 v9, v[32:35] offset:4096\n\t"
        "s_branch .L7_Q\n"
        ".L7_Q0:\n"
        ".L7_w44:\n\t"
        "ds_read_b64 v[4:5], v2 offset:72\n\t"
        "s_waitcnt lgkmcnt(0)\n\t"
        "v_cmp_gt_u16 vcc, 1, v4\n\t"
        "s_cbranch_vccz .L7_d45\n\t"
        "s_sleep 2\n\t"
        "s_sub_i32 s83, s83, 1\n\t"
        "s_cmp_gt_i32 s83, 0\n\t"
        "s_cbranch_scc1 .L7_w44\n"
        ".L7_d45:\n\t"
        "v_readfirstlane_b32 s68, v5\n"
        ".L7_Q:\n\t"
        "s_setprio 2\n\t"
        "v_and_b32 v5, 7, v1\n\t"
        "v_mul_u32_u24 v5, 0x420, v5\n\t"
        "v_and_b32 v6, 8, v1\n\t"
        "v_lshl_add_u32 v5, v6, 6, v5\n\t"
        "v_and_b32 v6, 48, v1\n\t"
        "v_add_u32 v5, v5, v6\n\t"
        "s_and_b32 s73, s68, 1\n\t"
        "s_cselect_b32 s73, 1, 0x10000\n\t"
        "v_and_b32 v6, 7, v1\n\t"
        "v_add_u32 v6, 1, v6\n\t"
        "v_lshrrev_b32 v6, v6, s68\n\t"
        "s_mov_b32 s49, 0x10001\n\t"
        "v_and_b32 v6, s49, v6\n\t"
        "s_add_u32 s80, s69, s76\n\t"
        "s_and_b32 s80, s80, 3\n\t"
        "s_lshl2_add_u32 s78, s70, s80\n\t"
        "s_lshl_b32 s78, s78, 14\n\t"
        "s_add_u32 s78, s78, 131072\n\t"
        "v_lshl_add_u32 v4, v1, 4, s78\n\t"
        "s_mov_b32 s24, 4096\n\t"
        "s_mov_b32 s25, 8192\n\t"
        "s_mov_b32 s26, 12288\n"
        ".L7_w46:\n\t"
        "ds_read_b32 v0, v2 offset:92\n\t"
        "s_waitcnt lgkmcnt(0)\n\t"
        "v_cmp_gt_u16 vcc, 1, v0\n\t"
        "s_cbranch_vccz .L7_d47\n\t"
        "s_sleep 1\n\t"
        "s_sub_i32 s83, s83, 1\n\t"
        "s_cmp_gt_i32 s83, 0\n\t"
        "s_cbranch_scc1 .L7_w46\n"
        ".L7_d47:\n\t"
        "v_readfirstlane_b32 s78, v0\n\t"
        "s_sub_u32 s78, s78, 1\n\t"
        "s_add_u32 s77, s74, s78\n"
        ".L7_g48:\n\t"
        "s_memrealtime s[78:79]\n\t"
        "s_waitcnt lgkmcnt(0)\n\t"
        "s_sub_u32 s78, s78, s77\n\t"
        "s_cmp_ge_i32 s78, 280\n\t"
        "s_cbranch_scc1 .L7_h49\n\t"
        "s_sleep 1\n\t"
        "s_branch .L7_g48\n"
        ".L7_h49:\n"
        ".L7_w50:\n\t"
        "ds_read_b32 v0, v2 offset:88\n\t"
        "s_waitcnt lgkmcnt(0)\n\t"
        "v_cmp_gt_u16 vcc, 8, v0\n\t"
        "s_cbranch_vccz .L7_d51\n\t"
        "s_sleep 1\n\t"
        "s_sub_i32 s83, s83, 1\n\t"
        "s_cmp_gt_i32 s83, 0\n\t"
        "s_cbranch_scc1 .L7_w50\n"
        ".L7_d51:\n\t"
        "buffer_load_dwordx4 v[64:67], v4, s[20:23], 0 offen offset:0 sc1\n\t"
        "buffer_load_dwordx4 v[68:71], v4, s[20:23], 0 offen offset:1024 sc1\n\t"
        "buffer_load_dwordx4 v[72:75], v4, s[20:23], 0 offen offset:2048 sc1\n\t"
        "buffer_load_dwordx4 v[76:79], v4, s[20:23], 0 offen offset:3072 sc1\n\t"
        "buffer_load_dwordx4 v[80:83], v4, s[20:23], s24 offen offset:0 sc1\n\t"
        "buffer_load_dwordx4 v[84:87], v4, s[20:23], s24 offen offset:1024 sc1\n\t"
        "buffer_load_dwordx4 v[88:91], v4, s[20:23], s24 offen offset:2048 sc1\n\t"
        "buffer_load_dwordx4 v[92:95], v4, s[20:23], s24 offen offset:3072 sc1\n\t"
        "buffer_load_dwordx4 v[96:99], v4, s[20:23], s25 offen offset:0 sc1\n\t"
        "buffer_load_dwordx4 v[100:103], v4, s[20:23], s25 offen offset:1024 sc1\n\t"
        "buffer_load_dwordx4 v[104:107], v4, s[20:23], s25 offen offset:2048 sc1\n\t"
        "buffer_load_dwordx4 v[108:111], v4, s[20:23], s25 offen offset:3072 sc1\n\t"
        "buffer_load_dwordx4 v[112:115], v4, s[20:23], s26 offen offset:0 sc1\n\t"
        "buffer_load_dwordx4 v[116:119], v4, s[20:23], s26 offen offset:1024 sc1\n\t"
        "buffer_load_dwordx4 v[120:123], v4, s[20:23], s26 offen offset:2048 sc1\n\t"
        "buffer_load_dwordx4 v[124:127], v4, s[20:23], s26 offen offset:3072 sc1\n\t"
        "s_mov_b64 exec, 1\n\t"
        "ds_add_u32 v2, v3 offset:84\n\t"
        "s_mov_b64 exec, -1\n"
        ".L7_w52:\n\t"
        "ds_read_b32 v0, v2 offset:0\n\t"
        "s_waitcnt lgkmcnt(0)\n\t"
        "v_cmp_gt_u16 vcc, 4, v0\n\t"
        "s_cbranch_vccz .L7_d53\n\t"
        "s_sleep 1\n\t"
        "s_sub_i32 s83, s83, 1\n\t"
        "s_cmp_gt_i32 s83, 0\n\t"
        "s_cbranch_scc1 .L7_w52\n"
        ".L7_d53:\n\t"
        "s_branch .L7_qg\n"
        ".L7_qr:\n\t"
        "s_waitcnt lgkmcnt(0)\n\t"
        "s_sleep 8\n\t"
        "s_sub_i32 s83, s83, 64\n\t"
        "s_cmp_gt_i32 s83, 0\n\t"
        "s_cbranch_scc0 .L7_qd\n\t"
        "buffer_load_dwordx4 v[64:67], v4, s[20:23], 0 offen offset:0 sc1\n\t"
        "buffer_load_dwordx4 v[68:71], v4, s[20:23], 0 offen offset:1024 sc1\n\t"
        "buffer_load_dwordx4 v[72:75], v4, s[20:23], 0 offen offset:2048 sc1\n\t"
        "buffer_load_dwordx4 v[76:79], v4, s[20:23], 0 offen offset:3072 sc1\n\t"
        "buffer_load_dwordx4 v[80:83], v4, s[20:23], s24 offen offset:0 sc1\n\t"
        "buffer_load_dwordx4 v[84:87], v4, s[20:23], s24 offen offset:1024 sc1\n\t"
        "buffer_load_dwordx4 v[88:91], v4, s[20:23], s24 offen offset:2048 sc1\n\t"
        "buffer_load_dwordx4 v[92:95], v4, s[20:23], s24 offen offset:3072 sc1\n\t"
        "buffer_load_dwordx4 v[96:99], v4, s[20:23], s25 offen offset:0 sc1\n\t"
        "buffer_load_dwordx4 v[100:103], v4, s[20:23], s25 offen offset:1024 sc1\n\t"
        "buffer_load_dwordx4 v[104:107], v4, s[20:23], s25 offen offset:2048 sc1\n\t"
        "buffer_load_dwordx4 v[108:111], v4, s[20:23], s25 offen offset:3072 sc1\n\t"
        "buffer_load_dwordx4 v[112:115], v4, s[20:23], s26 offen offset:0 sc1\n\t"
        "buffer_load_dwordx4 v[116:119], v4, s[20:23], s26 offen offset:1024 sc1\n\t"
        "buffer_load_dwordx4 v[120:123], v4, s[20:23], s26 offen offset:2048 sc1\n\t"
        "buffer_load_dwordx4 v[124:127], v4, s[20:23], s26 offen offset:3072 sc1\n"
        ".L7_qg:\n\t"
        "v_mov_b32 v7, 0\n\t"
        "ds_read_b128 v[8:11], v5 offset:2048\n\t"
        "ds_read_b128 v[12:15], v5 offset:10496\n\t"
        "ds_read_b128 v[16:19], v5 offset:18944\n\t"
        "ds_read_b128 v[20:23], v5 offset:27392\n\t"
        "s_waitcnt vmcnt(14) lgkmcnt(3)\n\t"
        "v_xor_b32 v64, s73, v64\n\t"
        "v_xor_b32 v65, v6, v65\n\t"
        "v_xor_b32 v68, s73, v68\n\t"
        "v_xor_b32 v69, v6, v69\n\t"
        "v_mfma_f32_16x16x32_bf16 v[24:27], v[64:67], v[8:11], 0\n\t"
        "s_nop 0\n\t"
        "v_mfma_f32_16x16x32_bf16 v[28:31], v[68:71], v[8:11], 0\n\t"
        "v_or3_b32 v7, v7, v64, v65\n\t"
        "ds_read_b128 v[8:11], v5 offset:35840\n\t"
        "s_waitcnt lgkmcnt(3)\n\t"
        "v_mfma_f32_16x16x32_bf16 v[32:35], v[64:67], v[12:15], 0\n\t"
        "v_mfma_f32_16x16x32_bf16 v[36:39], v[68:71], v[12:15], 0\n\t"
        "v_or3_b32 v7, v7, v68, v69\n\t"
        "ds_read_b128 v[12:15], v5 offset:2112\n\t"
        "s_waitcnt lgkmcnt(3)\n\t"
        "v_mfma_f32_16x16x32_bf16 v[40:43], v[64:67], v[16:19], 0\n\t"
        "v_mfma_f32_16x16x32_bf16 v[44:47], v[68:71], v[16:19], 0\n\t"
        "ds_read_b128 v[16:19], v5 offset:10560\n\t"
        "s_waitcnt lgkmcnt(3)\n\t"
        "v_mfma_f32_16x16x32_bf16 v[48:51], v[64:67], v[20:23], 0\n\t"
        "v_mfma_f32_16x16x32_bf16 v[52:55], v[68:71], v[20:23], 0\n\t"
        "ds_read_b128 v[20:23], v5 offset:19008\n\t"
        "s_waitcnt vmcnt(12) lgkmcnt(3)\n\t"
        "v_xor_b32 v72, s73, v72\n\t"
        "v_xor_b32 v73, v6, v73\n\t"
        "v_xor_b32 v76, s73, v76\n\t"
        "v_xor_b32 v77, v6, v77\n\t"
        "v_mfma_f32_16x16x32_bf16 v[56:59], v[64:67], v[8:11], 0\n\t"
        "v_mfma_f32_16x16x32_bf16 v[60:63], v[68:71], v[8:11], 0\n\t"
        "ds_read_b128 v[8:11], v5 offset:27456\n\t"
        "ds_read_b128 v[64:67], v5 offset:35904\n\t"
        "ds_read_b128 v[68:71], v5 offset:2176\n\t"
        "s_waitcnt lgkmcnt(4)\n\t"
        "v_mfma_f32_16x16x32_bf16 v[24:27], v[72:75], v[12:15], v[24:27]\n\t"
        "v_mfma_f32_16x16x32_bf16 v[28:31], v[76:79], v[12:15], v[28:31]\n\t"
        "v_or3_b32 v7, v7, v72, v73\n\t"
        "ds_read_b128 v[12:15], v5 offset:10624\n\t"
        "v_mfma_f32_16x16x32_bf16 v[32:35], v[72:75], v[16:19], v[32:35]\n\t"
        "v_mfma_f32_16x16x32_bf16 v[36:39], v[76:79], v[16:19], v[36:39]\n\t"
        "v_or3_b32 v7, v7, v76, v77\n\t"
        "ds_read_b128 v[16:19], v5 offset:19072\n\t"
        "s_waitcnt lgkmcnt(4)\n\t"
        "v_mfma_f32_16x16x32_bf16 v[40:43], v[72:75], v[20:23], v[40:43]\n\t"
        "v_mfma_f32_16x16x32_bf16 v[44:47], v[76:79], v[20:23], v[44:47]\n\t"
        "ds_read_b128 v[20:23], v5 offset:27520\n\t"
        "v_mfma_f32_16x16x32_bf16 v[48:51], v[72:75], v[8:11], v[48:51]\n\t"
        "v_mfma_f32_16x16x32_bf16 v[52:55], v[76:79], v[8:11], v[52:55]\n\t"
        "ds_read_b128 v[8:11], v5 offset:35968\n\t"
        "s_waitcnt vmcnt(10) lgkmcnt(4)\n\t"
        "v_xor_b32 v80, s73, v80\n\t"
        "v_xor_b32 v81, v6, v81\n\t"
        "v_xor_b32 v84, s73, v84\n\t"
        "v_xor_b32 v85, v6, v85\n\t"
        "v_mfma_f32_16x16x32_bf16 v[56:59], v[72:75], v[64:67], v[56:59]\n\t"
        "v_mfma_f32_16x16x32_bf16 v[60:63], v[76:79], v[64:67], v[60:63]\n\t"
        "ds_read_b128 v[64:67], v5 offset:2240\n\t"
        "ds_read_b128 v[72:75], v5 offset:10688\n\t"
        "ds_read_b128 v[76:79], v5 offset:19136\n\t"
        "v_mfma_f32_16x16x32_bf16 v[24:27], v[80:83], v[68:71], v[24:27]\n\t"
        "v_mfma_f32_16x16x32_bf16 v[28:31], v[84:87], v[68:71], v[28:31]\n\t"
        "v_or3_b32 v7, v7, v80, v81\n\t"
        "ds_read_b128 v[68:71], v5 offset:27584\n\t"
        "s_waitcnt lgkmcnt(6)\n\t"
        "v_mfma_f32_16x16x32_bf16 v[32:35], v[80:83], v[12:15], v[32:35]\n\t"
        "v_mfma_f32_16x16x32_bf16 v[36:39], v[84:87], v[12:15], v[36:39]\n\t"
        "v_or3_b32 v7, v7, v84, v85\n\t"
        "ds_read_b128 v[12:15], v5 offset:36032\n\t"
        "v_mfma_f32_16x16x32_bf16 v[40:43], v[80:83], v[16:19], v[40:43]\n\t"
        "v_mfma_f32_16x16x32_bf16 v[44:47], v[84:87], v[16:19], v[44:47]\n\t"
        "ds_read_b128 v[16:19], v5 offset:2304\n\t"
        "s_waitcnt lgkmcnt(6)\n\t"
        "v_mfma_f32_16x16x32_bf16 v[48:51], v[80:83], v[20:23], v[48:51]\n\t"
        "v_mfma_f32_16x16x32_bf16 v[52:55], v[84:87], v[20:23], v[52:55]\n\t"
        "ds_read_b128 v[20:23], v5 offset:10752\n\t"
        "s_waitcnt vmcnt(8) lgkmcnt(6)\n\t"
        "v_xor_b32 v88, s73, v88\n\t"
        "v_xor_b32 v89, v6, v89\n\t"
        "v_xor_b32 v92, s73, v92\n\t"
        "v_xor_b32 v93, v6, v93\n\t"
        "v_mfma_f32_16x16x32_bf16 v[56:59], v[80:83], v[8:11], v[56:59]\n\t"
        "v_mfma_f32_16x16x32_bf16 v[60:63], v[84:87], v[8:11], v[60:63]\n\t"
        "ds_read_b128 v[8:11], v5 offset:19200\n\t"
        "ds_read_b128 v[80:83], v5 offset:27648\n\t"
        "v_mfma_f32_16x16x32_bf16 v[24:27], v[88:91], v[64:67], v[24:27]\n\t"
        "v_mfma_f32_16x16x32_bf16 v[28:31], v[92:95], v[64:67], v[28:31]\n\t"
        "v_or3_b32 v7, v7, v88, v89\n\t"
        "ds_read_b128 v[84:87], v5 offset:36096\n\t"
        "s_waitcnt lgkmcnt(7)\n\t"
        "v_mfma_f32_16x16x32_bf16 v[32:35], v[88:91], v[72:75], v[32:35]\n\t"
        "v_mfma_f32_16x16x32_bf16 v[36:39], v[92:95], v[72:75], v[36:39]\n\t"
        "v_or3_b32 v7, v7, v92, v93\n\t"
        "ds_read_b128 v[64:67], v5 offset:2368\n\t"
        "v_mfma_f32_16x16x32_bf16 v[40:43], v[88:91], v[76:79], v[40:43]\n\t"
        "v_mfma_f32_16x16x32_bf16 v[44:47], v[92:95], v[76:79], v[44:47]\n\t"
        "ds_read_b128 v[72:75], v5 offset:10816\n\t"
        "s_waitcnt lgkmcnt(7)\n\t"
        "v_mfma_f32_16x16x32_bf16 v[48:51], v[88:91], v[68:71], v[48:51]\n\t"
        "v_mfma_f32_16x16x32_bf16 v[52:55], v[92:95], v[68:71], v[52:55]\n\t"
        "ds_read_b128 v[76:79], v5 offset:19264\n\t"
        "s_waitcnt vmcnt(6) lgkmcnt(7)\n\t"
        "v_xor_b32 v96, s73, v96\n\t"
        "v_xor_b32 v97, v6, v97\n\t"
        "v_xor_b32 v100, s73, v100\n\t"
        "v_xor_b32 v101, v6, v101\n\t"
        "v_mfma_f32_16x16x32_bf16 v[56:59], v[88:91], v[12:15], v[56:59]\n\t"
        "v_mfma_f32_16x16x32_bf16 v[60:63], v[92:95], v[12:15], v[60:63]\n\t"
        "ds_read_b128 v[68:71], v5 offset:27712\n\t"
        "v_mfma_f32_16x16x32_bf16 v[24:27], v[96:99], v[16:19], v[24:27]\n\t"
        "v_mfma_f32_16x16x32_bf16 v[28:31], v[100:103], v[16:19], v[28:31]\n\t"
        "v_or3_b32 v7, v7, v96, v97\n\t"
        "ds_read_b128 v[12:15], v5 offset:36160\n\t"
        "s_waitcnt lgkmcnt(7)\n\t"
        "v_mfma_f32_16x16x32_bf16 v[32:35], v[96:99], v[20:23], v[32:35]\n\t"
        "v_mfma_f32_16x16x32_bf16 v[36:39], v[100:103], v[20:23], v[36:39]\n\t"
        "v_or3_b32 v7, v7, v100, v101\n\t"
        "ds_read_b128 v[88:91], v5 offset:2432\n\t"
        "v_mfma_f32_16x16x32_bf16 v[40:43], v[96:99], v[8:11], v[40:43]\n\t"
        "v_mfma_f32_16x16x32_bf16 v[44:47], v[100:103], v[8:11], v[44:47]\n\t"
        "ds_read_b128 v[92:95], v5 offset:10880\n\t"
        "s_waitcnt lgkmcnt(7)\n\t"
        "v_mfma_f32_16x16x32_bf16 v[48:51], v[96:99], v[80:83], v[48:51]\n\t"
        "v_mfma_f32_16x16x32_bf16 v[52:55], v[100:103], v[80:83], v[52:55]\n\t"
        "ds_read_b128 v[16:19], v5 offset:19328\n\t"
        "s_waitcnt vmcnt(4) lgkmcnt(7)\n\t"
        "v_xor_b32 v104, s73, v104\n\t"
        "v_xor_b32 v105, v6, v105\n\t"
        "v_xor_b32 v108, s73, v108\n\t"
        "v_xor_b32 v109, v6, v109\n\t"
        "v_mfma_f32_16x16x32_bf16 v[56:59], v[96:99], v[84:87], v[56:59]\n\t"
        "v_mfma_f32_16x16x32_bf16 v[60:63], v[100:103], v[84:87], v[60:63]\n\t"
        "ds_read_b128 v[20:23], v5 offset:27776\n\t"
        "v_mfma_f32_16x16x32_bf16 v[24:27], v[104:107], v[64:67], v[24:27]\n\t"
        "v_mfma_f32_16x16x32_bf16 v[28:31], v[108:111], v[64:67], v[28:31]\n\t"
        "v_or3_b32 v7, v7, v104, v105\n\t"
        "ds_read_b128 v[8:11], v5 offset:36224\n\t"
        "s_waitcnt lgkmcnt(7)\n\t"
        "v_mfma_f32_16x16x32_bf16 v[32:35], v[104:107], v[72:75], v[32:35]\n\t"
        "v_mfma_f32_16x16x32_bf16 v[36:39], v[108:111], v[72:75], v[36:39]\n\t"
        "v_or3_b32 v7, v7, v108, v109\n\t"
        "ds_read_b128 v[80:83], v5 offset:2496\n\t"
        "v_mfma_f32_16x16x32_bf16 v[40:43], v[104:107], v[76:79], v[40:43]\n\t"
        "v_mfma_f32_16x16x32_bf16 v[44:47], v[108:111], v[76:79], v[44:47]\n\t"
        "ds_read_b128 v[84:87], v5 offset:10944\n\t"
        "s_waitcnt lgkmcnt(7)\n\t"
        "v_mfma_f32_16x16x32_bf16 v[48:51], v[104:107], v[68:71], v[48:51]\n\t"
        "v_mfma_f32_16x16x32_bf16 v[52:55], v[108:111], v[68:71], v[52:55]\n\t"
        "ds_read_b128 v[96:99], v5 offset:19392\n\t"
        "s_waitcnt vmcnt(2) lgkmcnt(7)\n\t"
        "v_xor_b32 v112, s73, v112\n\t"
        "v_xor_b32 v113, v6, v113\n\t"
        "v_xor_b32 v116, s73, v116\n\t"
        "v_xor_b32 v117, v6, v117\n\t"
        "v_mfma_f32_16x16x32_bf16 v[56:59], v[104:107], v[12:15], v[56:59]\n\t"
        "v_mfma_f32_16x16x32_bf16 v[60:63], v[108:111], v[12:15], v[60:63]\n\t"
        "ds_read_b128 v[100:103], v5 offset:27840\n\t"
        "v_mfma_f32_16x16x32_bf16 v[24:27], v[112:115], v[88:91], v[24:27]\n\t"
        "v_mfma_f32_16x16x32_bf16 v[28:31], v[116:119], v[88:91], v[28:31]\n\t"
        "v_or3_b32 v7, v7, v112, v113\n\t"
        "ds_read_b128 v[64:67], v5 offset:36288\n\t"
        "s_waitcnt lgkmcnt(7)\n\t"
        "v_mfma_f32_16x16x32_bf16 v[32:35], v[112:115], v[92:95], v[32:35]\n\t"
        "v_mfma_f32_16x16x32_bf16 v[36:39], v[116:119], v[92:95], v[36:39]\n\t"
        "v_or3_b32 v7, v7, v116, v117\n\t"
        "v_mfma_f32_16x16x32_bf16 v[40:43], v[112:115], v[16:19], v[40:43]\n\t"
        "v_mfma_f32_16x16x32_bf16 v[44:47], v[116:119], v[16:19], v[44:47]\n\t"
        "s_waitcnt lgkmcnt(5)\n\t"
        "v_mfma_f32_16x16x32_bf16 v[48:51], v[112:115], v[20:23], v[48:51]\n\t"
        "v_mfma_f32_16x16x32_bf16 v[52:55], v[116:119], v[20:23], v[52:55]\n\t"
        "s_waitcnt vmcnt(0) lgkmcnt(4)\n\t"
        "v_xor_b32 v120, s73, v120\n\t"
        "v_xor_b32 v121, v6, v121\n\t"
        "v_xor_b32 v124, s73, v124\n\t"
        "v_xor_b32 v125, v6, v125\n\t"
        "v_mfma_f32_16x16x32_bf16 v[56:59], v[112:115], v[8:11], v[56:59]\n\t"
        "v_mfma_f32_16x16x32_bf16 v[60:63], v[116:119], v[8:11], v[60:63]\n\t"
        "v_mfma_f32_16x16x32_bf16 v[24:27], v[120:123], v[80:83], v[24:27]\n\t"
        "v_mfma_f32_16x16x32_bf16 v[28:31], v[124:127], v[80:83], v[28:31]\n\t"
        "v_or3_b32 v7, v7, v120, v121\n\t"
        "s_waitcnt lgkmcnt(3)\n\t"
        "v_mfma_f32_16x16x32_bf16 v[32:35], v[120:123], v[84:87], v[32:35]\n\t"
        "v_mfma_f32_16x16x32_bf16 v[36:39], v[124:127], v[84:87], v[36:39]\n\t"
        "v_or3_b32 v7, v7, v124, v125\n\t"
        "v_and_b32 v7, s49, v7\n\t"
        "v_cmp_ne_u32 vcc, 0, v7\n\t"
        "s_cbranch_vccnz .L7_qr\n"
        ".L7_qd:\n\t"
        "s_mov_b64 exec, 1\n\t"
        "ds_add_u32 v2, v3 offset:12\n\t"
        "s_mov_b64 exec, -1\n\t"
        "v_and_b32 v6, 15, v1\n\t"
        "v_lshrrev_b32 v7, 4, v1\n\t"
        "s_lshl_b32 s78, s80, 2\n\t"
        "v_add_u32 v7, s78, v7\n\t"
        "v_xor_b32 v7, v7, v6\n\t"
        "v_lshlrev_b32 v7, 4, v7\n\t"
        "v_lshl_add_u32 v6, v6, 8, v7\n\t"
        "s_waitcnt lgkmcnt(2)\n\t"
        "v_mfma_f32_16x16x32_bf16 v[40:43], v[120:123], v[96:99], v[40:43]\n\t"
        "v_mfma_f32_16x16x32_bf16 v[44:47], v[124:127], v[96:99], v[44:47]\n\t"
        "v_cvt_pk_bf16_f32 v24, v24, v25\n\t"
        "v_cvt_pk_bf16_f32 v25, v26, v27\n\t"
        "v_cvt_pk_bf16_f32 v26, v28, v29\n\t"
        "v_cvt_pk_bf16_f32 v27, v30, v31\n\t"
        "s_waitcnt lgkmcnt(0)\n\t"
        "v_mfma_f32_16x16x32_bf16 v[48:51], v[120:123], v[100:103], v[48:51]\n\t"
        "v_mfma_f32_16x16x32_bf16 v[52:55], v[124:127], v[100:103], v[52:55]\n\t"
        "v_cvt_pk_bf16_f32 v32, v32, v33\n\t"
        "v_cvt_pk_bf16_f32 v33, v34, v35\n\t"
        "v_cvt_pk_bf16_f32 v34, v36, v37\n\t"
        "v_cvt_pk_bf16_f32 v35, v38, v39\n\t"
        "v_mfma_f32_16x16x32_bf16 v[56:59], v[120:123], v[64:67], v[56:59]\n\t"
        "v_mfma_f32_16x16x32_bf16 v[60:63], v[124:127], v[64:67], v[60:63]\n\t"
        "v_cvt_pk_bf16_f32 v40, v40, v41\n\t"
        "v_cvt_pk_bf16_f32 v41, v42, v43\n\t"
        "v_cvt_pk_bf16_f32 v42, v44, v45\n\t"
        "v_cvt_pk_bf16_f32 v43, v46, v47\n\t"
        "ds_read_b32 v0, v2 offset:12\n\t"
        "v_cvt_pk_bf16_f32 v48, v48, v49\n\t"
        "v_cvt_pk_bf16_f32 v49, v50, v51\n\t"
        "v_cvt_pk_bf16_f32 v50, v52, v53\n\t"
        "v_cvt_pk_bf16_f32 v51, v54, v55\n\t"
        "v_cvt_pk_bf16_f32 v56, v56, v57\n\t"
        "v_cvt_pk_bf16_f32 v57, v58, v59\n\t"
        "v_cvt_pk_bf16_f32 v58, v60, v61\n\t"
        "v_cvt_pk_bf16_f32 v59, v62, v63\n\t"
        "s_waitcnt lgkmcnt(0)\n\t"
        "v_cmp_gt_u16 vcc, 4, v0\n\t"
        "s_cbranch_vccz .L7_qx\n"
        ".L7_w54:\n\t"
        "ds_read_b32 v0, v2 offset:12\n\t"
        "s_waitcnt lgkmcnt(0)\n\t"
        "v_cmp_gt_u16 vcc, 4, v0\n\t"
        "s_cbranch_vccz .L7_d55\n\t"
        "s_sleep 0\n\t"
        "s_sub_i32 s83, s83, 1\n\t"
        "s_cmp_gt_i32 s83, 0\n\t"
        "s_cbranch_scc1 .L7_w54\n"
        ".L7_d55:\n"
        ".L7_qx:\n\t"
        "ds_write_b128 v6, v[24:27] offset:2048\n\t"
        "ds_write_b128 v6, v[32:35] offset:6144\n\t"
        "ds_write_b128 v6, v[40:43] offset:10240\n\t"
        "ds_write_b128 v6, v[48:51] offset:14336\n\t"
        "ds_write_b128 v6, v[56:59] offset:18432\n\t"
        "s_mov_b64 exec, 1\n\t"
        "ds_add_u32 v2, v3 offset:16\n\t"
        "s_mov_b64 exec, -1\n\t"
        "v_lshrrev_b32 v10, 3, v1\n\t"
        "v_lshlrev_b32 v10, 9, v10\n\t"
        "v_and_b32 v11, 7, v1\n\t"
        "v_lshl_add_u32 v10, v11, 4, v10\n\t"
        "v_and_b32 v11, 16, v1\n\t"
        "v_lshlrev_b32 v11, 2, v11\n\t"
        "v_xor_b32 v10, v10, v11\n\t"
        "s_load_dwordx4 s[24:27], %2, 32\n\t"
        "s_lshl_b32 s78, s80, 14\n\t"
        "s_add_u32 m0, s78, 83968\n\t"
        "s_lshl_b32 s78, s70, 9\n\t"
        "s_add_u32 s78, s78, s80\n\t"
        "s_lshl_b32 s78, s78, 7\n"
        ".L7_w56:\n\t"
        "ds_read_b32 v0, v2 offset:24\n\t"
        "s_waitcnt lgkmcnt(0)\n\t"
        "v_cmp_gt_u16 vcc, 8, v0\n\t"
        "s_cbranch_vccz .L7_d57\n\t"
        "s_sleep 1\n\t"
        "s_sub_i32 s83, s83, 1\n\t"
        "s_cmp_gt_i32 s83, 0\n\t"
        "s_cbranch_scc1 .L7_w56\n"
        ".L7_d57:\n\t"
        "s_setprio 3\n\t"
        "buffer_load_dwordx4 v10, s[24:27], s78 offen lds\n\t"
        "s_add_u32 m0, m0, 0x400\n\t"
        "s_add_u32 s78, s78, 0x1000\n\t"
        "buffer_load_dwordx4 v10, s[24:27], s78 offen lds\n\t"
        "s_add_u32 m0, m0, 0x400\n\t"
        "s_add_u32 s78, s78, 0x1000\n\t"
        "buffer_load_dwordx4 v10, s[24:27], s78 offen lds\n\t"
        "s_add_u32 m0, m0, 0x400\n\t"
        "s_add_u32 s78, s78, 0x1000\n\t"
        "buffer_load_dwordx4 v10, s[24:27], s78 offen lds\n\t"
        "s_add_u32 m0, m0, 0x400\n\t"
        "s_add_u32 s78, s78, 0x1000\n\t"
        "buffer_load_dwordx4 v10, s[24:27], s78 offen lds\n\t"
        "s_add_u32 m0, m0, 0x400\n\t"
        "s_add_u32 s78, s78, 0x1000\n\t"
        "buffer_load_dwordx4 v10, s[24:27], s78 offen lds\n\t"
        "s_add_u32 m0, m0, 0x400\n\t"
        "s_add_u32 s78, s78, 0x1000\n\t"
        "buffer_load_dwordx4 v10, s[24:27], s78 offen lds\n\t"
        "s_add_u32 m0, m0, 0x400\n\t"
        "s_add_u32 s78, s78, 0x1000\n\t"
        "buffer_load_dwordx4 v10, s[24:27], s78 offen lds\n\t"
        "s_add_u32 m0, m0, 0x400\n\t"
        "s_add_u32 s78, s78, 0x1000\n\t"
        "buffer_load_dwordx4 v10, s[24:27], s78 offen lds\n\t"
        "s_add_u32 m0, m0, 0x400\n\t"
        "s_add_u32 s78, s78, 0x1000\n\t"
        "buffer_load_dwordx4 v10, s[24:27], s78 offen lds\n\t"
        "s_add_u32 m0, m0, 0x400\n\t"
        "s_add_u32 s78, s78, 0x1000\n\t"
        "buffer_load_dwordx4 v10, s[24:27], s78 offen lds\n\t"
        "s_add_u32 m0, m0, 0x400\n\t"
        "s_add_u32 s78, s78, 0x1000\n\t"
        "buffer_load_dwordx4 v10, s[24:27], s78 offen lds\n\t"
        "s_add_u32 m0, m0, 0x400\n\t"
        "s_add_u32 s78, s78, 0x1000\n\t"
        "buffer_load_dwordx4 v10, s[24:27], s78 offen lds\n\t"
        "s_add_u32 m0, m0, 0x400\n\t"
        "s_add_u32 s78, s78, 0x1000\n\t"
        "buffer_load_dwordx4 v10, s[24:27], s78 offen lds\n\t"
        "s_add_u32 m0, m0, 0x400\n\t"
        "s_add_u32 s78, s78, 0x1000\n\t"
        "buffer_load_dwordx4 v10, s[24:27], s78 offen lds\n\t"
        "s_add_u32 m0, m0, 0x400\n\t"
        "s_add_u32 s78, s78, 0x1000\n\t"
        "buffer_load_dwordx4 v10, s[24:27], s78 offen lds\n\t"
        "s_waitcnt vmcnt(0)\n\t"
        "s_mov_b64 exec, 1\n\t"
        "ds_add_u32 v2, v3 offset:28\n\t"
        "s_mov_b64 exec, -1\n\t"
        "s_setprio 0\n"
        ".L7_w58:\n\t"
        "ds_read_b32 v0, v2 offset:36\n\t"
        "s_waitcnt lgkmcnt(0)\n\t"
        "v_cmp_gt_u16 vcc, 4, v0\n\t"
        "s_cbranch_vccz .L7_d59\n\t"
        "s_sleep 2\n\t"
        "s_sub_i32 s83, s83, 1\n\t"
        "s_cmp_gt_i32 s83, 0\n\t"
        "s_cbranch_scc1 .L7_w58\n"
        ".L7_d59:\n\t"
        "v_cmp_lt_u32 vcc, 0xfffff, v0\n\t"
        "s_cbranch_vccnz .L7_exit\n\t"
        "s_endpgm\n"
        ".L7_G2:\n\t"
        "s_barrier\n\t"
        "s_setprio 1\n\t"
        "v_lshlrev_b32 v5, 4, v1\n\t"
        "s_mul_i32 s82, s76, 13\n\t"
        "s_lshr_b32 s82, s82, 6\n\t"
        "s_mul_i32 s82, s82, 5\n\t"
        "s_sub_u32 s82, s76, s82\n\t"
        "s_lshl2_add_u32 s78, s82, s69\n\t"
        "s_lshl_b32 s78, s78, 10\n\t"
        "s_add_u32 m0, s78, 45056\n\t"
        "s_add_u32 s82, s82, 1\n\t"
        "s_cmp_eq_u32 s82, 5\n\t"
        "buffer_load_dwordx4 v5, s[56:59], s78 offen lds\n\t"
        "s_cselect_b32 s82, 0, s82\n\t"
        "v_lshrrev_b32 v18, 3, v1\n\t"
        "v_lshlrev_b32 v18, 14, v18\n\t"
        "v_and_b32 v6, 0x70, v5\n\t"
        "s_lshl_b32 s78, s69, 19\n\t"
        "s_lshl_b32 s79, s71, 7\n\t"
        "s_add_u32 s78, s78, s79\n\t"
        "v_add3_u32 v18, v18, v6, s78\n\t"
        "s_mov_b32 s24, 1048576\n\t"
        "s_mov_b32 s25, 1179648\n\t"
        "s_mov_b32 s26, 1310720\n\t"
        "s_mov_b32 s27, 1441792\n\t"
        "v_and_b32 v6, 0xf0, v5\n\t"
        "s_lshl_b32 s78, s70, 8\n\t"
        "v_add_u32 v14, s78, v6\n\t"
        "v_mov_b32 v7, 200\n\t"
        "s_lshl_b32 s33, s69, 3\n\t"
        "s_lshl_b32 s78, 1, s33\n\t"
        "v_mov_b32 v29, s78\n\t"
        "s_sub_u32 s32, s78, 1\n\t"
        "s_and_b32 s32, s32, 0x01010101\n\t"
        "s_mov_b32 s34, 0xfffe\n\t"
        "s_mov_b32 s35, 0xffffff\n\t"
        "s_mov_b32 s40, 0x12090503\n\t"
        "s_mov_b32 s41, 22\n\t"
        "s_cmp_eq_u32 s69, 1\n\t"
        "s_cselect_b32 s40, 0x13080402, s40\n\t"
        "s_cselect_b32 s41, 23, s41\n\t"
        "s_cmp_eq_u32 s69, 2\n\t"
        "s_cselect_b32 s40, 0x100e0a06, s40\n\t"
        "s_cselect_b32 s41, 20, s41\n\t"
        "s_cmp_eq_u32 s69, 3\n\t"
        "s_cselect_b32 s40, 0x110f0b07, s40\n\t"
        "s_cselect_b32 s41, 21, s41\n\t"
        "s_mul_i32 s41, s41, 0x01010101\n\t"
        "s_and_b32 s78, s40, 4\n\t"
        "v_mov_b32 v61, s78\n\t"
        "v_lshrrev_b32 v12, 4, v1\n\t"
        "s_bfe_u32 s78, s40, 0x80000\n\t"
        "s_lshl_b32 s78, s78, 2\n\t"
        "v_add_u32 v13, s78, v12\n\t"
        "v_lshlrev_b32 v24, 2, v13\n\t"
        "v_lshlrev_b32 v15, 4, v13\n\t"
        "v_and_b32 v15, 0xf0, v15\n\t"
        "v_xor_b32 v15, v15, v6\n\t"
        "v_lshl_add_u32 v56, v13, 8, v15\n\t"
        "s_bfe_u32 s78, s40, 0x80008\n\t"
        "s_lshl_b32 s78, s78, 2\n\t"
        "v_add_u32 v13, s78, v12\n\t"
        "v_lshlrev_b32 v25, 2, v13\n\t"
        "v_lshlrev_b32 v15, 4, v13\n\t"
        "v_and_b32 v15, 0xf0, v15\n\t"
        "v_xor_b32 v15, v15, v6\n\t"
        "v_lshl_add_u32 v57, v13, 8, v15\n\t"
        "s_bfe_u32 s78, s40, 0x80010\n\t"
        "s_lshl_b32 s78, s78, 2\n\t"
        "v_add_u32 v13, s78, v12\n\t"
        "v_lshlrev_b32 v26, 2, v13\n\t"
        "v_lshlrev_b32 v15, 4, v13\n\t"
        "v_and_b32 v15, 0xf0, v15\n\t"
        "v_xor_b32 v15, v15, v6\n\t"
        "v_lshl_add_u32 v58, v13, 8, v15\n\t"
        "s_bfe_u32 s78, s40, 0x80018\n\t"
        "s_lshl_b32 s78, s78, 2\n\t"
        "v_add_u32 v13, s78, v12\n\t"
        "v_lshlrev_b32 v27, 2, v13\n\t"
        "v_lshlrev_b32 v15, 4, v13\n\t"
        "v_and_b32 v15, 0xf0, v15\n\t"
        "v_xor_b32 v15, v15, v6\n\t"
        "v_lshl_add_u32 v59, v13, 8, v15\n\t"
        "s_bfe_u32 s78, s41, 0x80000\n\t"
        "s_lshl_b32 s78, s78, 2\n\t"
        "v_add_u32 v13, s78, v12\n\t"
        "v_lshlrev_b32 v28, 2, v13\n\t"
        "v_lshlrev_b32 v15, 4, v13\n\t"
        "v_and_b32 v15, 0xf0, v15\n\t"
        "v_xor_b32 v15, v15, v6\n\t"
        "v_lshl_add_u32 v60, v13, 8, v15\n\t"
        "v_and_b32 v20, 28, v1\n\t"
        "v_lshlrev_b32 v20, 1, v20\n\t"
        "v_lshrrev_b64 v[12:13], v20, s[40:41]\n\t"
        "v_and_b32 v13, 0xff, v12\n\t"
        "v_and_b32 v12, 3, v1\n\t"
        "v_lshl_add_u32 v13, v13, 2, v12\n\t"
        "v_lshlrev_b32 v31, 2, v13\n\t"
        "s_and_b32 s78, s67, 0x10000\n\t"
        "s_or_b32 s78, s78, 1\n\t"
        "v_mov_b32 v30, s78\n\t"
        "s_mov_b32 s36, 0\n\t"
        "v_and_b32 v0, 48, v5\n\t"
        "s_mov_b32 s78, 0x0d0c\n\t"
        "s_mov_b32 s79, 0\n\t"
        "v_lshlrev_b64 v[8:9], v0, s[78:79]\n\t"
        "s_mov_b32 s78, 0x0f0e\n\t"
        "v_lshlrev_b64 v[10:11], v0, s[78:79]\n\t"
        "v_xor_b32 v8, 0x0c0c0c0c, v8\n\t"
        "v_xor_b32 v9, 0x0c0c0c0c, v9\n\t"
        "v_xor_b32 v10, 0x0c0c0c0c, v10\n\t"
        "v_xor_b32 v11, 0x0c0c0c0c, v11\n\t"
        "s_movk_i32 s48, 0x200\n\t"
        "s_mov_b32 s37, 0x01010101\n\t"
        "s_mov_b32 s38, 0x07060302\n\t"
        "s_mov_b32 s49, 0x05040100\n\t"
        "s_getpc_b64 s[50:51]\n"
        ".L7_pc:\n\t"
        "s_add_u32 s52, s50, .L7_ce-.L7_pc\n\t"
        "s_addc_u32 s53, s51, 0\n\t"
        "s_add_u32 s50, s50, .L7_ie-.L7_pc\n\t"
        "s_addc_u32 s51, s51, 0\n\t"
        "s_cmp_eq_u32 s80, 10\n\t"
        "s_cselect_b32 s80, 0, s80\n\t"
        "s_mul_i32 s78, s80, 0x1080\n\t"
        "s_add_u32 m0, s78, s81\n\t"
        "s_lshr_b32 s78, s80, 1\n\t"
        "s_lshl1_add_u32 s78, s78, s80\n\t"
        "s_lshl_b32 s78, s78, 11\n\t"
        "buffer_load_dwordx4 v4, s[16:19], s78 offen lds\n\t"
        "s_add_u32 s80, s80, 1\n\t"
        "s_mul_i32 s78, s80, 0x1080\n\t"
        "s_add_u32 m0, s78, s81\n\t"
        "s_lshr_b32 s78, s80, 1\n\t"
        "s_lshl1_add_u32 s78, s78, s80\n\t"
        "s_lshl_b32 s78, s78, 11\n\t"
        "buffer_load_dwordx4 v4, s[16:19], s78 offen lds\n\t"
        "s_add_u32 s80, s80, 1\n\t"
        "s_mul_i32 s78, s80, 0x1080\n\t"
        "s_add_u32 m0, s78, s81\n\t"
        "s_lshr_b32 s78, s80, 1\n\t"
        "s_lshl1_add_u32 s78, s78, s80\n\t"
        "s_lshl_b32 s78, s78, 11\n\t"
        "buffer_load_dwordx4 v4, s[16:19], s78 offen lds\n\t"
        "s_add_u32 s80, s80, 1\n\t"
        "s_mul_i32 s78, s80, 0x1080\n\t"
        "s_add_u32 m0, s78, s81\n\t"
        "s_lshr_b32 s78, s80, 1\n\t"
        "s_lshl1_add_u32 s78, s78, s80\n\t"
        "s_lshl_b32 s78, s78, 11\n\t"
        "buffer_load_dwordx4 v4, s[16:19], s78 offen lds\n\t"
        "s_add_u32 s80, s80, 1\n\t"
        "s_mul_i32 s78, s80, 0x1080\n\t"
        "s_add_u32 m0, s78, s81\n\t"
        "s_lshr_b32 s78, s80, 1\n\t"
        "s_lshl1_add_u32 s78, s78, s80\n\t"
        "s_lshl_b32 s78, s78, 11\n\t"
        "buffer_load_dwordx4 v4, s[16:19], s78 offen lds\n\t"
        "s_add_u32 s80, s80, 1\n\t"
        "s_lshl2_add_u32 s78, s82, s69\n\t"
        "s_lshl_b32 s78, s78, 10\n\t"
        "s_add_u32 m0, s78, 45056\n\t"
        "s_add_u32 s82, s82, 1\n\t"
        "s_cmp_eq_u32 s82, 5\n\t"
        "buffer_load_dwordx4 v5, s[56:59], s78 offen lds\n\t"
        "s_cselect_b32 s82, 0, s82\n\t"
        "s_lshl2_add_u32 s78, s82, s69\n\t"
        "s_lshl_b32 s78, s78, 10\n\t"
        "s_add_u32 m0, s78, 45056\n\t"
        "s_add_u32 s82, s82, 1\n\t"
        "s_cmp_eq_u32 s82, 5\n\t"
        "buffer_load_dwordx4 v5, s[56:59], s78 offen lds\n\t"
        "s_cselect_b32 s82, 0, s82\n\t"
        "s_lshl2_add_u32 s78, s82, s69\n\t"
        "s_lshl_b32 s78, s78, 10\n\t"
        "s_add_u32 m0, s78, 45056\n\t"
        "s_add_u32 s82, s82, 1\n\t"
        "s_cmp_eq_u32 s82, 5\n\t"
        "buffer_load_dwordx4 v5, s[56:59], s78 offen lds\n\t"
        "s_cselect_b32 s82, 0, s82\n\t"
        "s_lshl2_add_u32 s78, s82, s69\n\t"
        "s_lshl_b32 s78, s78, 10\n\t"
        "s_add_u32 m0, s78, 45056\n\t"
        "s_add_u32 s82, s82, 1\n\t"
        "s_cmp_eq_u32 s82, 5\n\t"
        "buffer_load_dwordx4 v5, s[56:59], s78 offen lds\n\t"
        "s_cselect_b32 s82, 0, s82\n\t"
        "s_mov_b64 exec, 1\n\t"
        "ds_add_u32 v2, v3 offset:80\n\t"
        "s_mov_b64 exec, -1\n\t"
        "s_waitcnt vmcnt(4)\n\t"
        "s_mov_b64 exec, 1\n\t"
        "ds_add_u32 v2, v3 offset:0\n\t"
        "s_mov_b64 exec, -1\n"
        ".L7_w60:\n\t"
        "ds_read_b64 v[12:13], v2 offset:72\n\t"
        "s_waitcnt lgkmcnt(0)\n\t"
        "v_cmp_gt_u16 vcc, 1, v12\n\t"
        "s_cbranch_vccz .L7_d61\n\t"
        "s_sleep 1\n\t"
        "s_sub_i32 s83, s83, 1\n\t"
        "s_cmp_gt_i32 s83, 0\n\t"
        "s_cbranch_scc1 .L7_w60\n"
        ".L7_d61:\n\t"
        "v_readfirstlane_b32 s68, v13\n"
        ".L7_w62:\n\t"
        "ds_read_b32 v0, v2 offset:8\n\t"
        "s_waitcnt lgkmcnt(0)\n\t"
        "v_cmp_gt_u16 vcc, 2, v0\n\t"
        "s_cbranch_vccz .L7_d63\n\t"
        "s_sleep 1\n\t"
        "s_sub_i32 s83, s83, 1\n\t"
        "s_cmp_gt_i32 s83, 0\n\t"
        "s_cbranch_scc1 .L7_w62\n"
        ".L7_d63:\n\t"
        "ds_read_b32 v5, v31 offset:512\n\t"
        "ds_read_b32 v24, v24 offset:512\n\t"
        "ds_read_b32 v25, v25 offset:512\n\t"
        "ds_read_b32 v26, v26 offset:512\n\t"
        "ds_read_b32 v27, v27 offset:512\n\t"
        "ds_read_b32 v28, v28 offset:512\n\t"
        "s_waitcnt lgkmcnt(0)\n\t"
        "v_mul_u32_u24_sdwa v31, v7, v5 dst_sel:DWORD dst_unused:UNUSED_PAD src0_sel:DWORD src1_sel:BYTE_0\n\t"
        "s_waitcnt vmcnt(0)\n\t"
        "s_mov_b64 exec, 1\n\t"
        "ds_add_u32 v2, v3 offset:4\n\t"
        "s_mov_b64 exec, -1\n\t"
        "s_setprio 3\n\t"
        "s_lshl_b32 s44, s68, 24\n\t"
        "s_xor_b32 s45, s44, 0x5a000000\n\t"
        "s_mul_i32 s46, s68, 0x01010000\n\t"
        "s_mul_i32 s47, s68, 0x02000200\n"
        ".L7_w64:\n\t"
        "ds_read_b32 v0, v2 offset:92\n\t"
        "s_waitcnt lgkmcnt(0)\n\t"
        "v_cmp_gt_u16 vcc, 1, v0\n\t"
        "s_cbranch_vccz .L7_d65\n\t"
        "s_sleep 1\n\t"
        "s_sub_i32 s83, s83, 1\n\t"
        "s_cmp_gt_i32 s83, 0\n\t"
        "s_cbranch_scc1 .L7_w64\n"
        ".L7_d65:\n\t"
        "v_readfirstlane_b32 s78, v0\n\t"
        "s_sub_u32 s78, s78, 1\n\t"
        "s_add_u32 s77, s74, s78\n"
        ".L7_g66:\n\t"
        "s_memrealtime s[78:79]\n\t"
        "s_waitcnt lgkmcnt(0)\n\t"
        "s_sub_u32 s78, s78, s77\n\t"
        "s_cmp_ge_i32 s78, 315\n\t"
        "s_cbranch_scc1 .L7_h67\n\t"
        "s_sleep 1\n\t"
        "s_branch .L7_g66\n"
        ".L7_h67:\n\t"
        "s_branch .L7_ll\n"
        ".L7_again:\n\t"
        "s_waitcnt lgkmcnt(0)\n\t"
        "s_sleep 8\n\t"
        "s_sub_i32 s83, s83, 16\n\t"
        "s_cmp_gt_i32 s83, 0\n\t"
        "s_cbranch_scc0 .L7_got\n"
        ".L7_ll:\n\t"
        "buffer_load_dwordx4 v[64:67], v18, s[20:23], s24 offen sc1\n\t"
        "buffer_load_dwordx4 v[68:71], v18, s[20:23], s25 offen sc1\n\t"
        "buffer_load_dwordx4 v[72:75], v18, s[20:23], s26 offen sc1\n\t"
        "buffer_load_dwordx4 v[76:79], v18, s[20:23], s27 offen sc1\n"
        ".L7_w68:\n\t"
        "ds_read_b32 v0, v2 offset:4\n\t"
        "s_waitcnt lgkmcnt(0)\n\t"
        "v_cmp_gt_u16 vcc, 4, v0\n\t"
        "s_cbranch_vccz .L7_d69\n\t"
        "s_sleep 1\n\t"
        "s_sub_i32 s83, s83, 1\n\t"
        "s_cmp_gt_i32 s83, 0\n\t"
        "s_cbranch_scc1 .L7_w68\n"
        ".L7_d69:\n\t"
        "s_waitcnt vmcnt(3)\n\t"
        "v_xor_b32 v5, s44, v64\n\t"
        "v_bitop3_b32 v5, v65, v5, s45 bitop3:0xde\n\t"
        "v_bitop3_b32 v5, v66, v5, s46 bitop3:0xde\n\t"
        "v_xor_b32 v4, s47, v67\n\t"
        "v_lshlrev_b32_sdwa v112, 1, v64 dst_sel:DWORD dst_unused:UNUSED_PAD src0_sel:DWORD src1_sel:WORD_0\n\t"
        "ds_read_u16 v112, v112 offset:45056\n\t"
        "v_lshlrev_b32_sdwa v113, 1, v65 dst_sel:DWORD dst_unused:UNUSED_PAD src0_sel:DWORD src1_sel:WORD_0\n\t"
        "ds_read_u16 v113, v113 offset:45056\n\t"
        "v_lshlrev_b32_sdwa v114, 1, v66 dst_sel:DWORD dst_unused:UNUSED_PAD src0_sel:DWORD src1_sel:WORD_0\n\t"
        "ds_read_u16 v114, v114 offset:45056\n\t"
        "v_mul_u32_u24_sdwa v80, v7, v64 dst_sel:DWORD dst_unused:UNUSED_PAD src0_sel:DWORD src1_sel:BYTE_2\n\t"
        "v_mul_u32_u24_sdwa v81, v7, v65 dst_sel:DWORD dst_unused:UNUSED_PAD src0_sel:DWORD src1_sel:BYTE_2\n\t"
        "v_mul_u32_u24_sdwa v82, v7, v66 dst_sel:DWORD dst_unused:UNUSED_PAD src0_sel:DWORD src1_sel:BYTE_2\n\t"
        "s_waitcnt vmcnt(2)\n\t"
        "v_bitop3_b32 v5, v68, v5, s44 bitop3:0xde\n\t"
        "v_bitop3_b32 v5, v69, v5, s45 bitop3:0xde\n\t"
        "v_bitop3_b32 v5, v70, v5, s46 bitop3:0xde\n\t"
        "v_bitop3_b32 v4, v71, v4, s47 bitop3:0xde\n\t"
        "v_lshlrev_b32_sdwa v116, 1, v68 dst_sel:DWORD dst_unused:UNUSED_PAD src0_sel:DWORD src1_sel:WORD_0\n\t"
        "ds_read_u16 v116, v116 offset:45056\n\t"
        "v_lshlrev_b32_sdwa v117, 1, v69 dst_sel:DWORD dst_unused:UNUSED_PAD src0_sel:DWORD src1_sel:WORD_0\n\t"
        "ds_read_u16 v117, v117 offset:45056\n\t"
        "v_lshlrev_b32_sdwa v118, 1, v70 dst_sel:DWORD dst_unused:UNUSED_PAD src0_sel:DWORD src1_sel:WORD_0\n\t"
        "ds_read_u16 v118, v118 offset:45056\n\t"
        "v_mul_u32_u24_sdwa v84, v7, v68 dst_sel:DWORD dst_unused:UNUSED_PAD src0_sel:DWORD src1_sel:BYTE_2\n\t"
        "v_mul_u32_u24_sdwa v85, v7, v69 dst_sel:DWORD dst_unused:UNUSED_PAD src0_sel:DWORD src1_sel:BYTE_2\n\t"
        "v_mul_u32_u24_sdwa v86, v7, v70 dst_sel:DWORD dst_unused:UNUSED_PAD src0_sel:DWORD src1_sel:BYTE_2\n\t"
        "s_waitcnt vmcnt(1)\n\t"
        "v_bitop3_b32 v5, v72, v5, s44 bitop3:0xde\n\t"
        "v_bitop3_b32 v5, v73, v5, s45 bitop3:0xde\n\t"
        "v_bitop3_b32 v5, v74, v5, s46 bitop3:0xde\n\t"
        "v_bitop3_b32 v4, v75, v4, s47 bitop3:0xde\n\t"
        "v_lshlrev_b32_sdwa v120, 1, v72 dst_sel:DWORD dst_unused:UNUSED_PAD src0_sel:DWORD src1_sel:WORD_0\n\t"
        "ds_read_u16 v120, v120 offset:45056\n\t"
        "v_lshlrev_b32_sdwa v121, 1, v73 dst_sel:DWORD dst_unused:UNUSED_PAD src0_sel:DWORD src1_sel:WORD_0\n\t"
        "ds_read_u16 v121, v121 offset:45056\n\t"
        "v_lshlrev_b32_sdwa v122, 1, v74 dst_sel:DWORD dst_unused:UNUSED_PAD src0_sel:DWORD src1_sel:WORD_0\n\t"
        "ds_read_u16 v122, v122 offset:45056\n\t"
        "v_mul_u32_u24_sdwa v88, v7, v72 dst_sel:DWORD dst_unused:UNUSED_PAD src0_sel:DWORD src1_sel:BYTE_2\n\t"
        "v_mul_u32_u24_sdwa v89, v7, v73 dst_sel:DWORD dst_unused:UNUSED_PAD src0_sel:DWORD src1_sel:BYTE_2\n\t"
        "v_mul_u32_u24_sdwa v90, v7, v74 dst_sel:DWORD dst_unused:UNUSED_PAD src0_sel:DWORD src1_sel:BYTE_2\n\t"
        "s_waitcnt vmcnt(0)\n\t"
        "v_bitop3_b32 v5, v76, v5, s44 bitop3:0xde\n\t"
        "v_bitop3_b32 v5, v77, v5, s45 bitop3:0xde\n\t"
        "v_bitop3_b32 v5, v78, v5, s46 bitop3:0xde\n\t"
        "v_bitop3_b32 v4, v79, v4, s47 bitop3:0xde\n\t"
        "v_lshlrev_b32_sdwa v124, 1, v76 dst_sel:DWORD dst_unused:UNUSED_PAD src0_sel:DWORD src1_sel:WORD_0\n\t"
        "ds_read_u16 v124, v124 offset:45056\n\t"
        "v_lshlrev_b32_sdwa v125, 1, v77 dst_sel:DWORD dst_unused:UNUSED_PAD src0_sel:DWORD src1_sel:WORD_0\n\t"
        "ds_read_u16 v125, v125 offset:45056\n\t"
        "v_lshlrev_b32_sdwa v126, 1, v78 dst_sel:DWORD dst_unused:UNUSED_PAD src0_sel:DWORD src1_sel:WORD_0\n\t"
        "ds_read_u16 v126, v126 offset:45056\n\t"
        "v_mul_u32_u24_sdwa v92, v7, v76 dst_sel:DWORD dst_unused:UNUSED_PAD src0_sel:DWORD src1_sel:BYTE_2\n\t"
        "v_mul_u32_u24_sdwa v93, v7, v77 dst_sel:DWORD dst_unused:UNUSED_PAD src0_sel:DWORD src1_sel:BYTE_2\n\t"
        "v_mul_u32_u24_sdwa v94, v7, v78 dst_sel:DWORD dst_unused:UNUSED_PAD src0_sel:DWORD src1_sel:BYTE_2\n\t"
        "v_and_b32 v6, 0xfe000000, v4\n\t"
        "v_or_b32 v5, v5, v6\n\t"
        "v_cmp_lt_u32 vcc, s35, v5\n\t"
        "s_cbranch_vccnz .L7_again\n"
        ".L7_got:\n\t"
        "v_and_b32 v6, v67, v71\n\t"
        "v_bitop3_b32 v6, v6, v75, v79 bitop3:0x80\n\t"
        "s_waitcnt lgkmcnt(2)\n\t"
        "v_cmpx_gt_u16 vcc, s34, v64\n\t"
        "ds_add_rtn_u32 v96, v80, v29 offset:32576\n\t"
        "v_cmpx_gt_u16 vcc, s34, v65\n\t"
        "ds_add_rtn_u32 v97, v81, v29 offset:32576\n\t"
        "v_cmpx_gt_u16 vcc, s34, v66\n\t"
        "ds_add_rtn_u32 v98, v82, v29 offset:32576\n\t"
        "s_mov_b64 exec, -1\n\t"
        "v_cmpx_gt_u16 vcc, s34, v68\n\t"
        "ds_add_rtn_u32 v100, v84, v29 offset:32576\n\t"
        "v_cmpx_gt_u16 vcc, s34, v69\n\t"
        "ds_add_rtn_u32 v101, v85, v29 offset:32576\n\t"
        "v_cmpx_gt_u16 vcc, s34, v70\n\t"
        "ds_add_rtn_u32 v102, v86, v29 offset:32576\n\t"
        "s_mov_b64 exec, -1\n\t"
        "v_cmpx_gt_u16 vcc, s34, v72\n\t"
        "ds_add_rtn_u32 v104, v88, v29 offset:32576\n\t"
        "v_cmpx_gt_u16 vcc, s34, v73\n\t"
        "ds_add_rtn_u32 v105, v89, v29 offset:32576\n\t"
        "v_cmpx_gt_u16 vcc, s34, v74\n\t"
        "ds_add_rtn_u32 v106, v90, v29 offset:32576\n\t"
        "s_mov_b64 exec, -1\n\t"
        "v_cmpx_gt_u16 vcc, s34, v76\n\t"
        "ds_add_rtn_u32 v108, v92, v29 offset:32576\n\t"
        "v_cmpx_gt_u16 vcc, s34, v77\n\t"
        "ds_add_rtn_u32 v109, v93, v29 offset:32576\n\t"
        "v_cmpx_gt_u16 vcc, s34, v78\n\t"
        "ds_add_rtn_u32 v110, v94, v29 offset:32576\n\t"
        "s_mov_b64 exec, -1\n\t"
        "v_cmp_ne_u16 vcc, -1, v6\n\t"
        "s_cbranch_vccnz .L7_x\n"
        ".L7_y:\n\t"
        "s_mov_b64 exec, 1\n\t"
        "ds_add_u32 v2, v3 offset:32\n\t"
        "s_mov_b64 exec, -1\n\t"
        "v_perm_b32 v112, v112, v64, s49\n\t"
        "v_perm_b32 v113, v113, v65, s49\n\t"
        "v_perm_b32 v114, v114, v66, s49\n\t"
        "v_perm_b32 v116, v116, v68, s49\n\t"
        "v_perm_b32 v117, v117, v69, s49\n\t"
        "v_perm_b32 v118, v118, v70, s49\n\t"
        "v_perm_b32 v120, v120, v72, s49\n\t"
        "v_perm_b32 v121, v121, v73, s49\n\t"
        "v_perm_b32 v122, v122, v74, s49\n\t"
        "v_perm_b32 v124, v124, v76, s49\n\t"
        "v_cmp_lt_u32 vcc, s35, v4\n\t"
        "s_cmp_lg_u64 vcc, 0\n\t"
        "s_cselect_b32 s78, 0x100000, 0\n\t"
        "v_or_b32 v30, s78, v30\n\t"
        "s_waitcnt lgkmcnt(1)\n\t"
        "v_perm_b32 v125, v125, v77, s49\n\t"
        "v_perm_b32 v126, v126, v78, s49\n\t"
        "v_bfe_u32 v96, v96, s33, 8\n\t"
        "v_bfe_u32 v97, v97, s33, 8\n\t"
        "v_bfe_u32 v98, v98, s33, 8\n\t"
        "v_bfe_u32 v100, v100, s33, 8\n\t"
        "v_bfe_u32 v101, v101, s33, 8\n\t"
        "v_bfe_u32 v102, v102, s33, 8\n\t"
        "v_bfe_u32 v104, v104, s33, 8\n\t"
        "v_bfe_u32 v105, v105, s33, 8\n\t"
        "v_bfe_u32 v106, v106, s33, 8\n\t"
        "v_bfe_u32 v108, v108, s33, 8\n\t"
        "v_bfe_u32 v109, v109, s33, 8\n\t"
        "v_bfe_u32 v110, v110, s33, 8\n"
        ".L7_w70:\n\t"
        "ds_read_b32 v0, v2 offset:32\n\t"
        "ds_read_b32 v32, v80 offset:32576\n\t"
        "ds_read_b32 v33, v81 offset:32576\n\t"
        "ds_read_b32 v34, v82 offset:32576\n\t"
        "ds_read_b32 v35, v84 offset:32576\n\t"
        "ds_read_b32 v36, v85 offset:32576\n\t"
        "ds_read_b32 v37, v86 offset:32576\n\t"
        "ds_read_b32 v38, v88 offset:32576\n\t"
        "ds_read_b32 v39, v89 offset:32576\n\t"
        "ds_read_b32 v40, v90 offset:32576\n\t"
        "ds_read_b32 v41, v92 offset:32576\n\t"
        "ds_read_b32 v42, v93 offset:32576\n\t"
        "ds_read_b32 v43, v94 offset:32576\n\t"
        "ds_read_b32 v6, v31 offset:32576\n\t"
        "s_waitcnt lgkmcnt(13)\n\t"
        "v_cmp_gt_u16 vcc, 4, v0\n\t"
        "s_cbranch_vccz .L7_d71\n\t"
        "s_waitcnt lgkmcnt(0)\n\t"
        "s_sleep 0\n\t"
        "s_sub_i32 s83, s83, 1\n\t"
        "s_cmp_gt_i32 s83, 0\n\t"
        "s_cbranch_scc1 .L7_w70\n"
        ".L7_d71:\n\t"
        "s_waitcnt lgkmcnt(10)\n\t"
        "v_dot4_u32_u8 v96, v32, s32, v96\n\t"
        "v_dot4_u32_u8 v97, v33, s32, v97\n\t"
        "v_dot4_u32_u8 v98, v34, s32, v98\n\t"
        "s_waitcnt lgkmcnt(7)\n\t"
        "v_dot4_u32_u8 v100, v35, s32, v100\n\t"
        "v_dot4_u32_u8 v101, v36, s32, v101\n\t"
        "v_dot4_u32_u8 v102, v37, s32, v102\n\t"
        "s_waitcnt lgkmcnt(4)\n\t"
        "v_dot4_u32_u8 v104, v38, s32, v104\n\t"
        "v_dot4_u32_u8 v105, v39, s32, v105\n\t"
        "v_dot4_u32_u8 v106, v40, s32, v106\n\t"
        "s_waitcnt lgkmcnt(1)\n\t"
        "v_dot4_u32_u8 v108, v41, s32, v108\n\t"
        "v_dot4_u32_u8 v109, v42, s32, v109\n\t"
        "v_dot4_u32_u8 v110, v43, s32, v110\n\t"
        "v_min_u32 v96, 47, v96\n\t"
        "v_min_u32 v97, 47, v97\n\t"
        "v_min_u32 v98, 47, v98\n\t"
        "v_min_u32 v100, 47, v100\n\t"
        "v_min_u32 v101, 47, v101\n\t"
        "v_min_u32 v102, 47, v102\n\t"
        "v_min_u32 v104, 47, v104\n\t"
        "v_min_u32 v105, 47, v105\n\t"
        "v_min_u32 v106, 47, v106\n\t"
        "v_min_u32 v108, 47, v108\n\t"
        "v_min_u32 v109, 47, v109\n\t"
        "v_min_u32 v110, 47, v110\n\t"
        "v_lshl_add_u32 v96, v96, 2, v80\n\t"
        "v_lshl_add_u32 v97, v97, 2, v81\n\t"
        "v_lshl_add_u32 v98, v98, 2, v82\n\t"
        "v_lshl_add_u32 v100, v100, 2, v84\n\t"
        "v_lshl_add_u32 v101, v101, 2, v85\n\t"
        "v_lshl_add_u32 v102, v102, 2, v86\n\t"
        "v_lshl_add_u32 v104, v104, 2, v88\n\t"
        "v_lshl_add_u32 v105, v105, 2, v89\n\t"
        "v_lshl_add_u32 v106, v106, 2, v90\n\t"
        "v_lshl_add_u32 v108, v108, 2, v92\n\t"
        "v_lshl_add_u32 v109, v109, 2, v93\n\t"
        "v_lshl_add_u32 v110, v110, 2, v94\n\t"
        "v_cmpx_gt_u16 vcc, s34, v64\n\t"
        "ds_write_b32 v96, v112 offset:32384\n\t"
        "v_cmpx_gt_u16 vcc, s34, v65\n\t"
        "ds_write_b32 v97, v113 offset:32384\n\t"
        "v_cmpx_gt_u16 vcc, s34, v66\n\t"
        "ds_write_b32 v98, v114 offset:32384\n\t"
        "s_mov_b64 exec, -1\n\t"
        "v_cmpx_gt_u16 vcc, s34, v68\n\t"
        "ds_write_b32 v100, v116 offset:32384\n\t"
        "v_cmpx_gt_u16 vcc, s34, v69\n\t"
        "ds_write_b32 v101, v117 offset:32384\n\t"
        "v_cmpx_gt_u16 vcc, s34, v70\n\t"
        "ds_write_b32 v102, v118 offset:32384\n\t"
        "s_mov_b64 exec, -1\n\t"
        "v_cmpx_gt_u16 vcc, s34, v72\n\t"
        "ds_write_b32 v104, v120 offset:32384\n\t"
        "v_cmpx_gt_u16 vcc, s34, v73\n\t"
        "ds_write_b32 v105, v121 offset:32384\n\t"
        "v_cmpx_gt_u16 vcc, s34, v74\n\t"
        "ds_write_b32 v106, v122 offset:32384\n\t"
        "s_mov_b64 exec, -1\n\t"
        "v_cmpx_gt_u16 vcc, s34, v76\n\t"
        "ds_write_b32 v108, v124 offset:32384\n\t"
        "v_cmpx_gt_u16 vcc, s34, v77\n\t"
        "ds_write_b32 v109, v125 offset:32384\n\t"
        "v_cmpx_gt_u16 vcc, s34, v78\n\t"
        "ds_write_b32 v110, v126 offset:32384\n\t"
        "s_mov_b64 exec, -1\n\t"
        "s_cmp_lg_u32 s36, 0\n\t"
        "s_cbranch_scc1 .L7_z\n"
        ".L7_zr:\n\t"
        "s_mov_b64 exec, 1\n\t"
        "ds_add_u32 v2, v30 offset:36\n\t"
        "s_mov_b64 exec, -1\n\t"
        "s_waitcnt lgkmcnt(13)\n\t"
        "v_dot4_u32_u8 v6, v6, s37, -1\n\t"
        "v_mov_b64 v[32:33], 0\n\t"
        "v_mov_b64 v[34:35], 0\n\t"
        "v_mov_b64 v[36:37], 0\n\t"
        "v_med3_i32 v6, v6, 0, 47\n\t"
        "v_lshrrev_b32 v6, 1, v6\n\t"
        "v_mov_b64 v[38:39], 0\n\t"
        "v_mul_u32_u24_sdwa v19, v7, v24 dst_sel:DWORD dst_unused:UNUSED_PAD src0_sel:DWORD src1_sel:BYTE_0\n\t"
        "v_max_u32_dpp v6, v6, v6 quad_perm:[1,0,3,2] row_mask:0xf bank_mask:0xf\n\t"
        "s_nop 1\n\t"
        "v_max_u32_dpp v6, v6, v6 quad_perm:[2,3,0,1] row_mask:0xf bank_mask:0xf\n\t"
        "s_nop 0\n\t"
        "v_readlane_b32 s24, v6, 0\n\t"
        "v_readlane_b32 s25, v6, 4\n\t"
        "v_readlane_b32 s26, v6, 8\n\t"
        "v_readlane_b32 s27, v6, 12\n\t"
        "v_readlane_b32 s28, v6, 16\n\t"
        "s_max_u32 s28, s28, 4\n\t"
        "s_mov_b32 s56, s24\n\t"
        "s_mov_b32 s57, s24\n\t"
        "s_mov_b32 s78, 0xf0000\n"
        ".L7_w72:\n\t"
        "ds_read_b32 v4, v2 offset:36\n\t"
        "ds_read_b32 v12, v31 offset:32572\n\t"
        "s_waitcnt lgkmcnt(1)\n\t"
        "v_cmp_gt_u16 vcc, 4, v4\n\t"
        "s_cbranch_vccz .L7_d73\n\t"
        "s_waitcnt lgkmcnt(0)\n\t"
        "s_sleep 0\n\t"
        "s_sub_i32 s83, s83, 1\n\t"
        "s_cmp_gt_i32 s83, 0\n\t"
        "s_cbranch_scc1 .L7_w72\n"
        ".L7_d73:\n\t"
        "ds_read_b64 v[86:87], v19 offset:32384\n\t"
        "v_add_u32 v19, 8, v19\n\t"
        "s_sub_u32 s56, s56, 1\n\t"
        "s_cbranch_scc1 .L7_i74\n"
        ".L7_j75:\n\t"
        "ds_read_b64 v[94:95], v19 offset:32384\n\t"
        "v_add_u32 v19, 8, v19\n\t"
        "s_sub_u32 s56, s56, 1\n\t"
        "s_cbranch_scc1 .L7_i76\n"
        ".L7_j77:\n\t"
        "ds_read_b64 v[102:103], v19 offset:32384\n\t"
        "v_add_u32 v19, 8, v19\n\t"
        "s_sub_u32 s56, s56, 1\n\t"
        "s_cbranch_scc1 .L7_i78\n"
        ".L7_j79:\n\t"
        "ds_read_b64 v[110:111], v19 offset:32384\n\t"
        "v_add_u32 v19, 8, v19\n\t"
        "s_sub_u32 s56, s56, 1\n\t"
        "s_cbranch_scc1 .L7_i80\n"
        ".L7_j81:\n\t"
        "ds_read_b64 v[118:119], v19 offset:32384\n\t"
        "v_add_u32 v19, 8, v19\n\t"
        "s_sub_u32 s56, s56, 1\n\t"
        "s_cbranch_scc1 .L7_i82\n"
        ".L7_j83:\n\t"
        "ds_read_b64 v[126:127], v19 offset:32384\n\t"
        "v_add_u32 v19, 8, v19\n\t"
        "s_sub_u32 s56, s56, 1\n\t"
        "s_cbranch_scc1 .L7_i84\n"
        ".L7_j85:\n\t"
        "ds_read_b64 v[40:41], v19 offset:32384\n\t"
        "v_add_u32 v19, 8, v19\n\t"
        "s_sub_u32 s56, s56, 1\n\t"
        "s_cbranch_scc1 .L7_i86\n"
        ".L7_j87:\n\t"
        "ds_read_b64 v[42:43], v19 offset:32384\n\t"
        "v_add_u32 v19, 8, v19\n\t"
        "s_sub_u32 s56, s56, 1\n\t"
        "s_cbranch_scc1 .L7_i88\n"
        ".L7_j89:\n\t"
        "s_waitcnt lgkmcnt(8)\n\t"
        "v_and_or_b32 v12, v4, s78, v12\n\t"
        "v_cmp_ne_u32 vcc, 0xffff, v12\n\t"
        "s_cbranch_vccnz .L7_exit4\n\t"
        "s_waitcnt lgkmcnt(7)\n\t"
        "v_mad_u32_u16 v16, v86, s48, v14\n\t"
        "v_mad_u32_u16 v17, v87, s48, v14\n\t"
        "v_perm_b32 v48, v87, v86, s38\n\t"
        "buffer_load_dwordx4 v[80:83], v16, s[16:19], 0 offen\n\t"
        "buffer_load_dwordx4 v[84:87], v17, s[16:19], 0 offen\n\t"
        "s_waitcnt lgkmcnt(6)\n\t"
        "v_mad_u32_u16 v16, v94, s48, v14\n\t"
        "v_mad_u32_u16 v17, v95, s48, v14\n\t"
        "v_perm_b32 v49, v95, v94, s38\n\t"
        "buffer_load_dwordx4 v[88:91], v16, s[16:19], 0 offen\n\t"
        "buffer_load_dwordx4 v[92:95], v17, s[16:19], 0 offen\n\t"
        "s_waitcnt lgkmcnt(5)\n\t"
        "v_mad_u32_u16 v16, v102, s48, v14\n\t"
        "v_mad_u32_u16 v17, v103, s48, v14\n\t"
        "v_perm_b32 v50, v103, v102, s38\n\t"
        "buffer_load_dwordx4 v[96:99], v16, s[16:19], 0 offen\n\t"
        "buffer_load_dwordx4 v[100:103], v17, s[16:19], 0 offen\n\t"
        "s_waitcnt lgkmcnt(4)\n\t"
        "v_mad_u32_u16 v16, v110, s48, v14\n\t"
        "v_mad_u32_u16 v17, v111, s48, v14\n\t"
        "v_perm_b32 v51, v111, v110, s38\n\t"
        "buffer_load_dwordx4 v[104:107], v16, s[16:19], 0 offen\n\t"
        "buffer_load_dwordx4 v[108:111], v17, s[16:19], 0 offen\n\t"
        "s_waitcnt lgkmcnt(3)\n\t"
        "v_mad_u32_u16 v16, v118, s48, v14\n\t"
        "v_mad_u32_u16 v17, v119, s48, v14\n\t"
        "v_perm_b32 v52, v119, v118, s38\n\t"
        "buffer_load_dwordx4 v[112:115], v16, s[16:19], 0 offen\n\t"
        "buffer_load_dwordx4 v[116:119], v17, s[16:19], 0 offen\n\t"
        "s_waitcnt lgkmcnt(2)\n\t"
        "v_mad_u32_u16 v16, v126, s48, v14\n\t"
        "v_mad_u32_u16 v17, v127, s48, v14\n\t"
        "v_perm_b32 v53, v127, v126, s38\n\t"
        "buffer_load_dwordx4 v[120:123], v16, s[16:19], 0 offen\n\t"
        "buffer_load_dwordx4 v[124:127], v17, s[16:19], 0 offen\n\t"
        "s_setprio 3\n"
        ".L7_loop:\n\t"
        "v_perm_b32 v20, v48, v48, v8\n\t"
        "v_perm_b32 v21, v48, v48, v9\n\t"
        "v_perm_b32 v22, v48, v48, v10\n\t"
        "v_perm_b32 v23, v48, v48, v11\n\t"
        "s_waitcnt vmcnt(10) lgkmcnt(1)\n\t"
        "v_mfma_f32_4x4x4_16b_bf16 v[32:35], v[20:21], v[80:81], v[32:35]\n\t"
        "v_mfma_f32_4x4x4_16b_bf16 v[36:39], v[20:21], v[82:83], v[36:39]\n\t"
        "v_mad_u32_u16 v16, v40, s48, v14\n\t"
        "v_mfma_f32_4x4x4_16b_bf16 v[32:35], v[22:23], v[84:85], v[32:35]\n\t"
        "v_mfma_f32_4x4x4_16b_bf16 v[36:39], v[22:23], v[86:87], v[36:39]\n\t"
        "v_mad_u32_u16 v17, v41, s48, v14\n\t"
        "s_cbranch_vccnz .L7_dr0\n\t"
        "buffer_load_dwordx4 v[80:83], v16, s[16:19], 0 offen\n\t"
        "buffer_load_dwordx4 v[84:87], v17, s[16:19], 0 offen\n\t"
        "v_perm_b32 v48, v41, v40, s38\n\t"
        "ds_read_b64 v[40:41], v19 offset:32384\n\t"
        "v_add_u32 v19, 8, v19\n\t"
        "s_sub_u32 s56, s56, 1\n\t"
        "s_cbranch_scc1 .L7_i90\n"
        ".L7_j91:\n\t"
        "s_sub_u32 s57, s57, 1\n\t"
        "s_cbranch_scc1 .L7_i92\n"
        ".L7_j93:\n\t"
        "v_perm_b32 v20, v49, v49, v8\n\t"
        "v_perm_b32 v21, v49, v49, v9\n\t"
        "v_perm_b32 v22, v49, v49, v10\n\t"
        "v_perm_b32 v23, v49, v49, v11\n\t"
        "s_waitcnt vmcnt(10) lgkmcnt(1)\n\t"
        "v_mfma_f32_4x4x4_16b_bf16 v[32:35], v[20:21], v[88:89], v[32:35]\n\t"
        "v_mfma_f32_4x4x4_16b_bf16 v[36:39], v[20:21], v[90:91], v[36:39]\n\t"
        "v_mad_u32_u16 v16, v42, s48, v14\n\t"
        "v_mfma_f32_4x4x4_16b_bf16 v[32:35], v[22:23], v[92:93], v[32:35]\n\t"
        "v_mfma_f32_4x4x4_16b_bf16 v[36:39], v[22:23], v[94:95], v[36:39]\n\t"
        "v_mad_u32_u16 v17, v43, s48, v14\n\t"
        "s_cbranch_vccnz .L7_dr1\n\t"
        "buffer_load_dwordx4 v[88:91], v16, s[16:19], 0 offen\n\t"
        "buffer_load_dwordx4 v[92:95], v17, s[16:19], 0 offen\n\t"
        "v_perm_b32 v49, v43, v42, s38\n\t"
        "ds_read_b64 v[42:43], v19 offset:32384\n\t"
        "v_add_u32 v19, 8, v19\n\t"
        "s_sub_u32 s56, s56, 1\n\t"
        "s_cbranch_scc1 .L7_i94\n"
        ".L7_j95:\n\t"
        "s_sub_u32 s57, s57, 1\n\t"
        "s_cbranch_scc1 .L7_i96\n"
        ".L7_j97:\n\t"
        "v_perm_b32 v20, v50, v50, v8\n\t"
        "v_perm_b32 v21, v50, v50, v9\n\t"
        "v_perm_b32 v22, v50, v50, v10\n\t"
        "v_perm_b32 v23, v50, v50, v11\n\t"
        "s_waitcnt vmcnt(10) lgkmcnt(1)\n\t"
        "v_mfma_f32_4x4x4_16b_bf16 v[32:35], v[20:21], v[96:97], v[32:35]\n\t"
        "v_mfma_f32_4x4x4_16b_bf16 v[36:39], v[20:21], v[98:99], v[36:39]\n\t"
        "v_mad_u32_u16 v16, v40, s48, v14\n\t"
        "v_mfma_f32_4x4x4_16b_bf16 v[32:35], v[22:23], v[100:101], v[32:35]\n\t"
        "v_mfma_f32_4x4x4_16b_bf16 v[36:39], v[22:23], v[102:103], v[36:39]\n\t"
        "v_mad_u32_u16 v17, v41, s48, v14\n\t"
        "s_cbranch_vccnz .L7_dr2\n\t"
        "buffer_load_dwordx4 v[96:99], v16, s[16:19], 0 offen\n\t"
        "buffer_load_dwordx4 v[100:103], v17, s[16:19], 0 offen\n\t"
        "v_perm_b32 v50, v41, v40, s38\n\t"
        "ds_read_b64 v[40:41], v19 offset:32384\n\t"
        "v_add_u32 v19, 8, v19\n\t"
        "s_sub_u32 s56, s56, 1\n\t"
        "s_cbranch_scc1 .L7_i98\n"
        ".L7_j99:\n\t"
        "s_sub_u32 s57, s57, 1\n\t"
        "s_cbranch_scc1 .L7_i100\n"
        ".L7_j101:\n\t"
        "v_perm_b32 v20, v51, v51, v8\n\t"
        "v_perm_b32 v21, v51, v51, v9\n\t"
        "v_perm_b32 v22, v51, v51, v10\n\t"
        "v_perm_b32 v23, v51, v51, v11\n\t"
        "s_waitcnt vmcnt(10) lgkmcnt(1)\n\t"
        "v_mfma_f32_4x4x4_16b_bf16 v[32:35], v[20:21], v[104:105], v[32:35]\n\t"
        "v_mfma_f32_4x4x4_16b_bf16 v[36:39], v[20:21], v[106:107], v[36:39]\n\t"
        "v_mad_u32_u16 v16, v42, s48, v14\n\t"
        "v_mfma_f32_4x4x4_16b_bf16 v[32:35], v[22:23], v[108:109], v[32:35]\n\t"
        "v_mfma_f32_4x4x4_16b_bf16 v[36:39], v[22:23], v[110:111], v[36:39]\n\t"
        "v_mad_u32_u16 v17, v43, s48, v14\n\t"
        "s_cbranch_vccnz .L7_dr3\n\t"
        "buffer_load_dwordx4 v[104:107], v16, s[16:19], 0 offen\n\t"
        "buffer_load_dwordx4 v[108:111], v17, s[16:19], 0 offen\n\t"
        "v_perm_b32 v51, v43, v42, s38\n\t"
        "ds_read_b64 v[42:43], v19 offset:32384\n\t"
        "v_add_u32 v19, 8, v19\n\t"
        "s_sub_u32 s56, s56, 1\n\t"
        "s_cbranch_scc1 .L7_i102\n"
        ".L7_j103:\n\t"
        "s_sub_u32 s57, s57, 1\n\t"
        "s_cbranch_scc1 .L7_i104\n"
        ".L7_j105:\n\t"
        "v_perm_b32 v20, v52, v52, v8\n\t"
        "v_perm_b32 v21, v52, v52, v9\n\t"
        "v_perm_b32 v22, v52, v52, v10\n\t"
        "v_perm_b32 v23, v52, v52, v11\n\t"
        "s_waitcnt vmcnt(10) lgkmcnt(1)\n\t"
        "v_mfma_f32_4x4x4_16b_bf16 v[32:35], v[20:21], v[112:113], v[32:35]\n\t"
        "v_mfma_f32_4x4x4_16b_bf16 v[36:39], v[20:21], v[114:115], v[36:39]\n\t"
        "v_mad_u32_u16 v16, v40, s48, v14\n\t"
        "v_mfma_f32_4x4x4_16b_bf16 v[32:35], v[22:23], v[116:117], v[32:35]\n\t"
        "v_mfma_f32_4x4x4_16b_bf16 v[36:39], v[22:23], v[118:119], v[36:39]\n\t"
        "v_mad_u32_u16 v17, v41, s48, v14\n\t"
        "s_cbranch_vccnz .L7_dr4\n\t"
        "buffer_load_dwordx4 v[112:115], v16, s[16:19], 0 offen\n\t"
        "buffer_load_dwordx4 v[116:119], v17, s[16:19], 0 offen\n\t"
        "v_perm_b32 v52, v41, v40, s38\n\t"
        "ds_read_b64 v[40:41], v19 offset:32384\n\t"
        "v_add_u32 v19, 8, v19\n\t"
        "s_sub_u32 s56, s56, 1\n\t"
        "s_cbranch_scc1 .L7_i106\n"
        ".L7_j107:\n\t"
        "s_sub_u32 s57, s57, 1\n\t"
        "s_cbranch_scc1 .L7_i108\n"
        ".L7_j109:\n\t"
        "v_perm_b32 v20, v53, v53, v8\n\t"
        "v_perm_b32 v21, v53, v53, v9\n\t"
        "v_perm_b32 v22, v53, v53, v10\n\t"
        "v_perm_b32 v23, v53, v53, v11\n\t"
        "s_waitcnt vmcnt(10) lgkmcnt(1)\n\t"
        "v_mfma_f32_4x4x4_16b_bf16 v[32:35], v[20:21], v[120:121], v[32:35]\n\t"
        "v_mfma_f32_4x4x4_16b_bf16 v[36:39], v[20:21], v[122:123], v[36:39]\n\t"
        "v_mad_u32_u16 v16, v42, s48, v14\n\t"
        "v_mfma_f32_4x4x4_16b_bf16 v[32:35], v[22:23], v[124:125], v[32:35]\n\t"
        "v_mfma_f32_4x4x4_16b_bf16 v[36:39], v[22:23], v[126:127], v[36:39]\n\t"
        "v_mad_u32_u16 v17, v43, s48, v14\n\t"
        "s_cbranch_vccnz .L7_dr5\n\t"
        "buffer_load_dwordx4 v[120:123], v16, s[16:19], 0 offen\n\t"
        "buffer_load_dwordx4 v[124:127], v17, s[16:19], 0 offen\n\t"
        "v_perm_b32 v53, v43, v42, s38\n\t"
        "ds_read_b64 v[42:43], v19 offset:32384\n\t"
        "v_add_u32 v19, 8, v19\n\t"
        "s_sub_u32 s56, s56, 1\n\t"
        "s_cbranch_scc1 .L7_i110\n"
        ".L7_j111:\n\t"
        "s_sub_u32 s57, s57, 1\n\t"
        "s_cbranch_scc1 .L7_i112\n"
        ".L7_j113:\n\t"
        "s_branch .L7_loop\n"
        ".L7_i74:\n\t"
        "s_swappc_b64 s[54:55], s[50:51]\n\t"
        "s_branch .L7_j75\n"
        ".L7_i76:\n\t"
        "s_swappc_b64 s[54:55], s[50:51]\n\t"
        "s_branch .L7_j77\n"
        ".L7_i78:\n\t"
        "s_swappc_b64 s[54:55], s[50:51]\n\t"
        "s_branch .L7_j79\n"
        ".L7_i80:\n\t"
        "s_swappc_b64 s[54:55], s[50:51]\n\t"
        "s_branch .L7_j81\n"
        ".L7_i82:\n\t"
        "s_swappc_b64 s[54:55], s[50:51]\n\t"
        "s_branch .L7_j83\n"
        ".L7_i84:\n\t"
        "s_swappc_b64 s[54:55], s[50:51]\n\t"
        "s_branch .L7_j85\n"
        ".L7_i86:\n\t"
        "s_swappc_b64 s[54:55], s[50:51]\n\t"
        "s_branch .L7_j87\n"
        ".L7_i88:\n\t"
        "s_swappc_b64 s[54:55], s[50:51]\n\t"
        "s_branch .L7_j89\n"
        ".L7_i90:\n\t"
        "s_swappc_b64 s[54:55], s[50:51]\n\t"
        "s_branch .L7_j91\n"
        ".L7_i92:\n\t"
        "s_swappc_b64 s[54:55], s[52:53]\n\t"
        "s_branch .L7_j93\n"
        ".L7_i94:\n\t"
        "s_swappc_b64 s[54:55], s[50:51]\n\t"
        "s_branch .L7_j95\n"
        ".L7_i96:\n\t"
        "s_swappc_b64 s[54:55], s[52:53]\n\t"
        "s_branch .L7_j97\n"
        ".L7_i98:\n\t"
        "s_swappc_b64 s[54:55], s[50:51]\n\t"
        "s_branch .L7_j99\n"
        ".L7_i100:\n\t"
        "s_swappc_b64 s[54:55], s[52:53]\n\t"
        "s_branch .L7_j101\n"
        ".L7_i102:\n\t"
        "s_swappc_b64 s[54:55], s[50:51]\n\t"
        "s_branch .L7_j103\n"
        ".L7_i104:\n\t"
        "s_swappc_b64 s[54:55], s[52:53]\n\t"
        "s_branch .L7_j105\n"
        ".L7_i106:\n\t"
        "s_swappc_b64 s[54:55], s[50:51]\n\t"
        "s_branch .L7_j107\n"
        ".L7_i108:\n\t"
        "s_swappc_b64 s[54:55], s[52:53]\n\t"
        "s_branch .L7_j109\n"
        ".L7_i110:\n\t"
        "s_swappc_b64 s[54:55], s[50:51]\n\t"
        "s_branch .L7_j111\n"
        ".L7_i112:\n\t"
        "s_swappc_b64 s[54:55], s[52:53]\n\t"
        "s_branch .L7_j113\n"
        ".L7_dr0:\n\t"
        "s_sub_u32 s57, s57, 1\n\t"
        "s_cbranch_scc0 .L7_ds0\n\t"
        "s_swappc_b64 s[54:55], s[52:53]\n"
        ".L7_ds0:\n\t"
        "s_mov_b32 s57, 4\n\t"
        "s_waitcnt vmcnt(4)\n\t"
        "s_branch .L7_dt1\n"
        ".L7_dr1:\n\t"
        "s_sub_u32 s57, s57, 1\n\t"
        "s_cbranch_scc0 .L7_ds1\n\t"
        "s_swappc_b64 s[54:55], s[52:53]\n"
        ".L7_ds1:\n\t"
        "s_mov_b32 s57, 4\n\t"
        "s_waitcnt vmcnt(4)\n\t"
        "s_branch .L7_dt2\n"
        ".L7_dr2:\n\t"
        "s_sub_u32 s57, s57, 1\n\t"
        "s_cbranch_scc0 .L7_ds2\n\t"
        "s_swappc_b64 s[54:55], s[52:53]\n"
        ".L7_ds2:\n\t"
        "s_mov_b32 s57, 4\n\t"
        "s_waitcnt vmcnt(4)\n\t"
        "s_branch .L7_dt3\n"
        ".L7_dr3:\n\t"
        "s_sub_u32 s57, s57, 1\n\t"
        "s_cbranch_scc0 .L7_ds3\n\t"
        "s_swappc_b64 s[54:55], s[52:53]\n"
        ".L7_ds3:\n\t"
        "s_mov_b32 s57, 4\n\t"
        "s_waitcnt vmcnt(4)\n\t"
        "s_branch .L7_dt4\n"
        ".L7_dr4:\n\t"
        "s_sub_u32 s57, s57, 1\n\t"
        "s_cbranch_scc0 .L7_ds4\n\t"
        "s_swappc_b64 s[54:55], s[52:53]\n"
        ".L7_ds4:\n\t"
        "s_mov_b32 s57, 4\n\t"
        "s_waitcnt vmcnt(4)\n\t"
        "s_branch .L7_dt5\n"
        ".L7_dr5:\n\t"
        "s_sub_u32 s57, s57, 1\n\t"
        "s_cbranch_scc0 .L7_ds5\n\t"
        "s_swappc_b64 s[54:55], s[52:53]\n"
        ".L7_ds5:\n\t"
        "s_mov_b32 s57, 4\n\t"
        "s_waitcnt vmcnt(4)\n\t"
        "s_branch .L7_dt0\n"
        ".L7_dt0:\n\t"
        "v_perm_b32 v20, v48, v48, v8\n\t"
        "v_perm_b32 v21, v48, v48, v9\n\t"
        "v_perm_b32 v22, v48, v48, v10\n\t"
        "v_perm_b32 v23, v48, v48, v11\n\t"
        "s_cmp_lt_u32 s57, 2\n\t"
        "s_cbranch_scc0 .L7_du0\n\t"
        "s_waitcnt vmcnt(0)\n"
        ".L7_du0:\n\t"
        "v_mfma_f32_4x4x4_16b_bf16 v[32:35], v[20:21], v[80:81], v[32:35]\n\t"
        "v_mfma_f32_4x4x4_16b_bf16 v[36:39], v[20:21], v[82:83], v[36:39]\n\t"
        "s_sub_u32 s57, s57, 1\n\t"
        "v_mfma_f32_4x4x4_16b_bf16 v[32:35], v[22:23], v[84:85], v[32:35]\n\t"
        "v_mfma_f32_4x4x4_16b_bf16 v[36:39], v[22:23], v[86:87], v[36:39]\n\t"
        "s_cbranch_scc1 .L7_last\n"
        ".L7_dt1:\n\t"
        "v_perm_b32 v20, v49, v49, v8\n\t"
        "v_perm_b32 v21, v49, v49, v9\n\t"
        "v_perm_b32 v22, v49, v49, v10\n\t"
        "v_perm_b32 v23, v49, v49, v11\n\t"
        "s_cmp_lt_u32 s57, 2\n\t"
        "s_cbranch_scc0 .L7_du1\n\t"
        "s_waitcnt vmcnt(0)\n"
        ".L7_du1:\n\t"
        "v_mfma_f32_4x4x4_16b_bf16 v[32:35], v[20:21], v[88:89], v[32:35]\n\t"
        "v_mfma_f32_4x4x4_16b_bf16 v[36:39], v[20:21], v[90:91], v[36:39]\n\t"
        "s_sub_u32 s57, s57, 1\n\t"
        "v_mfma_f32_4x4x4_16b_bf16 v[32:35], v[22:23], v[92:93], v[32:35]\n\t"
        "v_mfma_f32_4x4x4_16b_bf16 v[36:39], v[22:23], v[94:95], v[36:39]\n\t"
        "s_cbranch_scc1 .L7_last\n"
        ".L7_dt2:\n\t"
        "v_perm_b32 v20, v50, v50, v8\n\t"
        "v_perm_b32 v21, v50, v50, v9\n\t"
        "v_perm_b32 v22, v50, v50, v10\n\t"
        "v_perm_b32 v23, v50, v50, v11\n\t"
        "s_cmp_lt_u32 s57, 2\n\t"
        "s_cbranch_scc0 .L7_du2\n\t"
        "s_waitcnt vmcnt(0)\n"
        ".L7_du2:\n\t"
        "v_mfma_f32_4x4x4_16b_bf16 v[32:35], v[20:21], v[96:97], v[32:35]\n\t"
        "v_mfma_f32_4x4x4_16b_bf16 v[36:39], v[20:21], v[98:99], v[36:39]\n\t"
        "s_sub_u32 s57, s57, 1\n\t"
        "v_mfma_f32_4x4x4_16b_bf16 v[32:35], v[22:23], v[100:101], v[32:35]\n\t"
        "v_mfma_f32_4x4x4_16b_bf16 v[36:39], v[22:23], v[102:103], v[36:39]\n\t"
        "s_cbranch_scc1 .L7_last\n"
        ".L7_dt3:\n\t"
        "v_perm_b32 v20, v51, v51, v8\n\t"
        "v_perm_b32 v21, v51, v51, v9\n\t"
        "v_perm_b32 v22, v51, v51, v10\n\t"
        "v_perm_b32 v23, v51, v51, v11\n\t"
        "s_cmp_lt_u32 s57, 2\n\t"
        "s_cbranch_scc0 .L7_du3\n\t"
        "s_waitcnt vmcnt(0)\n"
        ".L7_du3:\n\t"
        "v_mfma_f32_4x4x4_16b_bf16 v[32:35], v[20:21], v[104:105], v[32:35]\n\t"
        "v_mfma_f32_4x4x4_16b_bf16 v[36:39], v[20:21], v[106:107], v[36:39]\n\t"
        "s_sub_u32 s57, s57, 1\n\t"
        "v_mfma_f32_4x4x4_16b_bf16 v[32:35], v[22:23], v[108:109], v[32:35]\n\t"
        "v_mfma_f32_4x4x4_16b_bf16 v[36:39], v[22:23], v[110:111], v[36:39]\n\t"
        "s_cbranch_scc1 .L7_last\n"
        ".L7_dt4:\n\t"
        "v_perm_b32 v20, v52, v52, v8\n\t"
        "v_perm_b32 v21, v52, v52, v9\n\t"
        "v_perm_b32 v22, v52, v52, v10\n\t"
        "v_perm_b32 v23, v52, v52, v11\n\t"
        "s_cmp_lt_u32 s57, 2\n\t"
        "s_cbranch_scc0 .L7_du4\n\t"
        "s_waitcnt vmcnt(0)\n"
        ".L7_du4:\n\t"
        "v_mfma_f32_4x4x4_16b_bf16 v[32:35], v[20:21], v[112:113], v[32:35]\n\t"
        "v_mfma_f32_4x4x4_16b_bf16 v[36:39], v[20:21], v[114:115], v[36:39]\n\t"
        "s_sub_u32 s57, s57, 1\n\t"
        "v_mfma_f32_4x4x4_16b_bf16 v[32:35], v[22:23], v[116:117], v[32:35]\n\t"
        "v_mfma_f32_4x4x4_16b_bf16 v[36:39], v[22:23], v[118:119], v[36:39]\n\t"
        "s_cbranch_scc1 .L7_last\n"
        ".L7_dt5:\n\t"
        "v_perm_b32 v20, v53, v53, v8\n\t"
        "v_perm_b32 v21, v53, v53, v9\n\t"
        "v_perm_b32 v22, v53, v53, v10\n\t"
        "v_perm_b32 v23, v53, v53, v11\n\t"
        "s_cmp_lt_u32 s57, 2\n\t"
        "s_cbranch_scc0 .L7_du5\n\t"
        "s_waitcnt vmcnt(0)\n"
        ".L7_du5:\n\t"
        "v_mfma_f32_4x4x4_16b_bf16 v[32:35], v[20:21], v[120:121], v[32:35]\n\t"
        "v_mfma_f32_4x4x4_16b_bf16 v[36:39], v[20:21], v[122:123], v[36:39]\n\t"
        "s_sub_u32 s57, s57, 1\n\t"
        "v_mfma_f32_4x4x4_16b_bf16 v[32:35], v[22:23], v[124:125], v[32:35]\n\t"
        "v_mfma_f32_4x4x4_16b_bf16 v[36:39], v[22:23], v[126:127], v[36:39]\n\t"
        "s_cbranch_scc1 .L7_last\n\t"
        "s_branch .L7_dt0\n"
        ".L7_ie:\n\t"
        "v_mul_u32_u24_sdwa v19, v7, v25 dst_sel:DWORD dst_unused:UNUSED_PAD src0_sel:DWORD src1_sel:BYTE_0\n\t"
        "s_mov_b32 s56, s25\n\t"
        "s_swappc_b64 s[50:51], s[54:55]\n\t"
        "v_mul_u32_u24_sdwa v19, v7, v26 dst_sel:DWORD dst_unused:UNUSED_PAD src0_sel:DWORD src1_sel:BYTE_0\n\t"
        "s_mov_b32 s56, s26\n\t"
        "s_swappc_b64 s[50:51], s[54:55]\n\t"
        "v_mul_u32_u24_sdwa v19, v7, v27 dst_sel:DWORD dst_unused:UNUSED_PAD src0_sel:DWORD src1_sel:BYTE_0\n\t"
        "s_mov_b32 s56, s27\n\t"
        "s_swappc_b64 s[50:51], s[54:55]\n\t"
        "v_mul_u32_u24_sdwa v19, v7, v28 dst_sel:DWORD dst_unused:UNUSED_PAD src0_sel:DWORD src1_sel:BYTE_0\n\t"
        "s_add_u32 s56, s28, 2\n\t"
        "s_swappc_b64 s[50:51], s[54:55]\n\t"
        "s_mov_b64 vcc, -1\n\t"
        "s_setpc_b64 s[54:55]\n"
        ".L7_ce:\n\t"
        "v_and_b32 v4, 0xffff0000, v24\n\t"
        "s_nop 2\n\t"
        "v_pk_mul_f32 v[32:33], v[32:33], v[4:5] op_sel_hi:[1,0]\n\t"
        "v_pk_mul_f32 v[34:35], v[34:35], v[4:5] op_sel_hi:[1,0]\n\t"
        "v_pk_mul_f32 v[36:37], v[36:37], v[4:5] op_sel_hi:[1,0]\n\t"
        "v_pk_mul_f32 v[38:39], v[38:39], v[4:5] op_sel_hi:[1,0]\n\t"
        "v_cvt_pk_bf16_f32 v44, v32, v33\n\t"
        "v_cvt_pk_bf16_f32 v45, v34, v35\n\t"
        "v_cvt_pk_bf16_f32 v46, v36, v37\n\t"
        "v_cvt_pk_bf16_f32 v47, v38, v39\n\t"
        "s_mov_b64 s[58:59], vcc\n"
        ".L7_w114:\n\t"
        "ds_read_b32 v4, v2 offset:12\n\t"
        "s_waitcnt lgkmcnt(0)\n\t"
        "v_cmp_gt_u16 vcc, 4, v4\n\t"
        "s_cbranch_vccz .L7_d115\n\t"
        "s_sleep 1\n\t"
        "s_sub_i32 s83, s83, 1\n\t"
        "s_cmp_gt_i32 s83, 0\n\t"
        "s_cbranch_scc1 .L7_w114\n"
        ".L7_d115:\n\t"
        "s_mov_b64 vcc, s[58:59]\n\t"
        "ds_write_b128 v56, v[44:47] offset:20480\n\t"
        "s_mov_b64 exec, 1\n\t"
        "ds_add_u32 v2, v3 offset:52\n\t"
        "s_mov_b64 exec, -1\n\t"
        "v_mov_b64 v[32:33], 0\n\t"
        "v_mov_b64 v[34:35], 0\n\t"
        "v_mov_b64 v[36:37], 0\n\t"
        "v_mov_b64 v[38:39], 0\n\t"
        "s_mov_b32 s57, s25\n\t"
        "s_swappc_b64 s[52:53], s[54:55]\n\t"
        "v_and_b32 v4, 0xffff0000, v25\n\t"
        "s_nop 2\n\t"
        "v_pk_mul_f32 v[32:33], v[32:33], v[4:5] op_sel_hi:[1,0]\n\t"
        "v_pk_mul_f32 v[34:35], v[34:35], v[4:5] op_sel_hi:[1,0]\n\t"
        "v_pk_mul_f32 v[36:37], v[36:37], v[4:5] op_sel_hi:[1,0]\n\t"
        "v_pk_mul_f32 v[38:39], v[38:39], v[4:5] op_sel_hi:[1,0]\n\t"
        "v_cvt_pk_bf16_f32 v44, v32, v33\n\t"
        "v_cvt_pk_bf16_f32 v45, v34, v35\n\t"
        "v_cvt_pk_bf16_f32 v46, v36, v37\n\t"
        "v_cvt_pk_bf16_f32 v47, v38, v39\n\t"
        "ds_write_b128 v57, v[44:47] offset:20480\n\t"
        "s_mov_b64 exec, 1\n\t"
        "ds_add_u32 v61, v3 offset:52\n\t"
        "s_mov_b64 exec, -1\n\t"
        "v_mov_b64 v[32:33], 0\n\t"
        "v_mov_b64 v[34:35], 0\n\t"
        "v_mov_b64 v[36:37], 0\n\t"
        "v_mov_b64 v[38:39], 0\n\t"
        "s_mov_b32 s57, s26\n\t"
        "s_swappc_b64 s[52:53], s[54:55]\n\t"
        "v_and_b32 v4, 0xffff0000, v26\n\t"
        "s_nop 2\n\t"
        "v_pk_mul_f32 v[32:33], v[32:33], v[4:5] op_sel_hi:[1,0]\n\t"
        "v_pk_mul_f32 v[34:35], v[34:35], v[4:5] op_sel_hi:[1,0]\n\t"
        "v_pk_mul_f32 v[36:37], v[36:37], v[4:5] op_sel_hi:[1,0]\n\t"
        "v_pk_mul_f32 v[38:39], v[38:39], v[4:5] op_sel_hi:[1,0]\n\t"
        "v_cvt_pk_bf16_f32 v44, v32, v33\n\t"
        "v_cvt_pk_bf16_f32 v45, v34, v35\n\t"
        "v_cvt_pk_bf16_f32 v46, v36, v37\n\t"
        "v_cvt_pk_bf16_f32 v47, v38, v39\n\t"
        "ds_write_b128 v58, v[44:47] offset:20480\n\t"
        "s_mov_b64 exec, 1\n\t"
        "ds_add_u32 v61, v3 offset:56\n\t"
        "s_mov_b64 exec, -1\n\t"
        "v_mov_b64 v[32:33], 0\n\t"
        "v_mov_b64 v[34:35], 0\n\t"
        "v_mov_b64 v[36:37], 0\n\t"
        "v_mov_b64 v[38:39], 0\n\t"
        "s_mov_b32 s57, s27\n\t"
        "s_swappc_b64 s[52:53], s[54:55]\n\t"
        "v_and_b32 v4, 0xffff0000, v27\n\t"
        "s_nop 2\n\t"
        "v_pk_mul_f32 v[32:33], v[32:33], v[4:5] op_sel_hi:[1,0]\n\t"
        "v_pk_mul_f32 v[34:35], v[34:35], v[4:5] op_sel_hi:[1,0]\n\t"
        "v_pk_mul_f32 v[36:37], v[36:37], v[4:5] op_sel_hi:[1,0]\n\t"
        "v_pk_mul_f32 v[38:39], v[38:39], v[4:5] op_sel_hi:[1,0]\n\t"
        "v_cvt_pk_bf16_f32 v44, v32, v33\n\t"
        "v_cvt_pk_bf16_f32 v45, v34, v35\n\t"
        "v_cvt_pk_bf16_f32 v46, v36, v37\n\t"
        "v_cvt_pk_bf16_f32 v47, v38, v39\n\t"
        "ds_write_b128 v59, v[44:47] offset:20480\n\t"
        "s_mov_b64 exec, 1\n\t"
        "ds_add_u32 v2, v3 offset:64\n\t"
        "s_mov_b64 exec, -1\n\t"
        "v_mov_b64 v[32:33], 0\n\t"
        "v_mov_b64 v[34:35], 0\n\t"
        "v_mov_b64 v[36:37], 0\n\t"
        "v_mov_b64 v[38:39], 0\n\t"
        "s_mov_b32 s57, s28\n\t"
        "s_swappc_b64 s[52:53], s[54:55]\n"
        ".L7_last:\n\t"
        "v_and_b32 v4, 0xffff0000, v28\n\t"
        "s_nop 2\n\t"
        "v_pk_mul_f32 v[32:33], v[32:33], v[4:5] op_sel_hi:[1,0]\n\t"
        "v_pk_mul_f32 v[34:35], v[34:35], v[4:5] op_sel_hi:[1,0]\n\t"
        "v_pk_mul_f32 v[36:37], v[36:37], v[4:5] op_sel_hi:[1,0]\n\t"
        "v_pk_mul_f32 v[38:39], v[38:39], v[4:5] op_sel_hi:[1,0]\n\t"
        "v_cvt_pk_bf16_f32 v44, v32, v33\n\t"
        "v_cvt_pk_bf16_f32 v45, v34, v35\n\t"
        "v_cvt_pk_bf16_f32 v46, v36, v37\n\t"
        "v_cvt_pk_bf16_f32 v47, v38, v39\n\t"
        "ds_write_b128 v60, v[44:47] offset:20480\n\t"
        "s_mov_b64 exec, 1\n\t"
        "ds_add_u32 v2, v3 offset:68\n\t"
        "s_mov_b64 exec, -1\n\t"
        "s_endpgm\n"
        ".L7_x:\n\t"
        "s_mov_b32 s36, 1\n\t"
        "s_waitcnt lgkmcnt(0)\n\t"
        "v_mul_u32_u24_sdwa v83, v7, v67 dst_sel:DWORD dst_unused:UNUSED_PAD src0_sel:DWORD src1_sel:BYTE_2\n\t"
        "v_lshlrev_b32_sdwa v115, 1, v67 dst_sel:DWORD dst_unused:UNUSED_PAD src0_sel:DWORD src1_sel:WORD_0\n\t"
        "ds_read_u16 v115, v115 offset:45056\n\t"
        "v_cmp_eq_u16 vcc, s34, v67\n\t"
        "s_cmp_lg_u64 vcc, 0\n\t"
        "s_cselect_b32 s78, 0x10000, 0\n\t"
        "v_or_b32 v30, s78, v30\n\t"
        "v_cmpx_gt_u16 vcc, s34, v67\n\t"
        "ds_add_rtn_u32 v99, v83, v29 offset:32576\n\t"
        "s_mov_b64 exec, -1\n\t"
        "v_mul_u32_u24_sdwa v87, v7, v71 dst_sel:DWORD dst_unused:UNUSED_PAD src0_sel:DWORD src1_sel:BYTE_2\n\t"
        "v_lshlrev_b32_sdwa v119, 1, v71 dst_sel:DWORD dst_unused:UNUSED_PAD src0_sel:DWORD src1_sel:WORD_0\n\t"
        "ds_read_u16 v119, v119 offset:45056\n\t"
        "v_cmp_eq_u16 vcc, s34, v71\n\t"
        "s_cmp_lg_u64 vcc, 0\n\t"
        "s_cselect_b32 s78, 0x10000, 0\n\t"
        "v_or_b32 v30, s78, v30\n\t"
        "v_cmpx_gt_u16 vcc, s34, v71\n\t"
        "ds_add_rtn_u32 v103, v87, v29 offset:32576\n\t"
        "s_mov_b64 exec, -1\n\t"
        "v_mul_u32_u24_sdwa v91, v7, v75 dst_sel:DWORD dst_unused:UNUSED_PAD src0_sel:DWORD src1_sel:BYTE_2\n\t"
        "v_lshlrev_b32_sdwa v123, 1, v75 dst_sel:DWORD dst_unused:UNUSED_PAD src0_sel:DWORD src1_sel:WORD_0\n\t"
        "ds_read_u16 v123, v123 offset:45056\n\t"
        "v_cmp_eq_u16 vcc, s34, v75\n\t"
        "s_cmp_lg_u64 vcc, 0\n\t"
        "s_cselect_b32 s78, 0x10000, 0\n\t"
        "v_or_b32 v30, s78, v30\n\t"
        "v_cmpx_gt_u16 vcc, s34, v75\n\t"
        "ds_add_rtn_u32 v107, v91, v29 offset:32576\n\t"
        "s_mov_b64 exec, -1\n\t"
        "v_mul_u32_u24_sdwa v95, v7, v79 dst_sel:DWORD dst_unused:UNUSED_PAD src0_sel:DWORD src1_sel:BYTE_2\n\t"
        "v_lshlrev_b32_sdwa v127, 1, v79 dst_sel:DWORD dst_unused:UNUSED_PAD src0_sel:DWORD src1_sel:WORD_0\n\t"
        "ds_read_u16 v127, v127 offset:45056\n\t"
        "v_cmp_eq_u16 vcc, s34, v79\n\t"
        "s_cmp_lg_u64 vcc, 0\n\t"
        "s_cselect_b32 s78, 0x10000, 0\n\t"
        "v_or_b32 v30, s78, v30\n\t"
        "v_cmpx_gt_u16 vcc, s34, v79\n\t"
        "ds_add_rtn_u32 v111, v95, v29 offset:32576\n\t"
        "s_mov_b64 exec, -1\n\t"
        "s_branch .L7_y\n"
        ".L7_z:\n\t"
        "s_waitcnt lgkmcnt(0)\n\t"
        "ds_read_b32 v32, v83 offset:32576\n\t"
        "ds_read_b32 v33, v87 offset:32576\n\t"
        "ds_read_b32 v34, v91 offset:32576\n\t"
        "ds_read_b32 v35, v95 offset:32576\n\t"
        "v_bfe_u32 v99, v99, s33, 8\n\t"
        "v_perm_b32 v115, v115, v67, s49\n\t"
        "v_bfe_u32 v103, v103, s33, 8\n\t"
        "v_perm_b32 v119, v119, v71, s49\n\t"
        "v_bfe_u32 v107, v107, s33, 8\n\t"
        "v_perm_b32 v123, v123, v75, s49\n\t"
        "v_bfe_u32 v111, v111, s33, 8\n\t"
        "v_perm_b32 v127, v127, v79, s49\n\t"
        "s_waitcnt lgkmcnt(0)\n\t"
        "v_dot4_u32_u8 v99, v32, s32, v99\n\t"
        "v_dot4_u32_u8 v103, v33, s32, v103\n\t"
        "v_dot4_u32_u8 v107, v34, s32, v107\n\t"
        "v_dot4_u32_u8 v111, v35, s32, v111\n\t"
        "v_min_u32 v99, 47, v99\n\t"
        "v_min_u32 v103, 47, v103\n\t"
        "v_min_u32 v107, 47, v107\n\t"
        "v_min_u32 v111, 47, v111\n\t"
        "v_lshl_add_u32 v99, v99, 2, v83\n\t"
        "v_lshl_add_u32 v103, v103, 2, v87\n\t"
        "v_lshl_add_u32 v107, v107, 2, v91\n\t"
        "v_lshl_add_u32 v111, v111, 2, v95\n\t"
        "v_cmpx_gt_u16 vcc, s34, v67\n\t"
        "ds_write_b32 v99, v115 offset:32384\n\t"
        "s_mov_b64 exec, -1\n\t"
        "v_cmpx_gt_u16 vcc, s34, v71\n\t"
        "ds_write_b32 v103, v119 offset:32384\n\t"
        "s_mov_b64 exec, -1\n\t"
        "v_cmpx_gt_u16 vcc, s34, v75\n\t"
        "ds_write_b32 v107, v123 offset:32384\n\t"
        "s_mov_b64 exec, -1\n\t"
        "v_cmpx_gt_u16 vcc, s34, v79\n\t"
        "ds_write_b32 v111, v127 offset:32384\n\t"
        "s_mov_b64 exec, -1\n\t"
        "s_branch .L7_zr\n"
        ".L7_exit4:\n\t"
        "s_add_u32 s69, s69, 4\n"
        ".L7_exit:\n\t"
        "s_waitcnt vmcnt(0) lgkmcnt(0)\n\t"
        "s_setprio 0\n\t"
        : "+{v0}"(tid), "=&{s69}"(wv)
        : "s"(__builtin_amdgcn_kernarg_segment_ptr()), "s"(blockIdx.x)
        : "scc", "vcc", "memory", "s16", "s17", "s18", "s19", "s20", "s21", "s22", "s23", "s24", "s25", "s26", "s27", "s28", "s29", "s30", "s31", "s32", "s33", "s34", "s35",
          "s36", "s37", "s38", "s39", "s40", "s41", "s42", "s43", "s44", "s45", "s46", "s47", "s48", "s49", "s50", "s51", "s52", "s53", "s54", "s55",
          "s56", "s57", "s58", "s59", "s60", "s61", "s62", "s63", "s64", "s65", "s66", "s67", "s68", "s70", "s71", "s72", "s73", "s74", "s75", "s76",
          "s77", "s78", "s79", "s80", "s81", "s82", "s83", "s84", "s85", "s86", "s87", "s88", "s89", "s90", "s91", "s92", "s93", "s94", "s95", "s96",
          "s97", "s98", "s99", "s100", "s101", "v1", "v2", "v3", "v4", "v5", "v6", "v7", "v8", "v9", "v10", "v11", "v12", "v13", "v14", "v15",
          "v16", "v17", "v18", "v19", "v20", "v21", "v22", "v23", "v24", "v25", "v26", "v27", "v28", "v29", "v30", "v31", "v32", "v33", "v34", "v35",
          "v36", "v37", "v38", "v39", "v40", "v41", "v42", "v43", "v44", "v45", "v46", "v47", "v48", "v49", "v50", "v51", "v52", "v53", "v54", "v55",
          "v56", "v57", "v58", "v59", "v60", "v61", "v62", "v63", "v64", "v65", "v66", "v67", "v68", "v69", "v70", "v71", "v72", "v73", "v74", "v75",
          "v76", "v77", "v78", "v79", "v80", "v81", "v82", "v83", "v84", "v85", "v86", "v87", "v88", "v89", "v90", "v91", "v92", "v93", "v94", "v95",
          "v96", "v97", "v98", "v99", "v100", "v101", "v102", "v103", "v104", "v105", "v106", "v107", "v108", "v109", "v110", "v111", "v112", "v113", "v114", "v115",
          "v116", "v117", "v118", "v119", "v120", "v121", "v122", "v123", "v124", "v125", "v126", "v127");
    const __attribute__((address_space(4))) Args* q = (const __attribute__((address_space(4))) Args*)__builtin_amdgcn_kernarg_segment_ptr();
    asm volatile("" : "+s"(q));
    const u32 wave = __builtin_amdgcn_readfirstlane(wv);
    u32 lane;
    asm volatile("v_mbcnt_lo_u32_b32 %0, -1, 0\n\tv_mbcnt_hi_u32_b32 %0, -1, %0" : "=v"(lane));
    const u32 half = blockIdx.x & 1u, TN = q->TN, r0 = (blockIdx.x >> 1) * TN;
    const u16* x = q->xall;
    _Pragma("nounroll") for (u32 n = 0; n < (1u << 26) && *(volatile __attribute__((address_space(3))) u32*)S_QK < 4u; ++n) __builtin_amdgcn_s_sleep(2);
    if (wave >= 4u) {
        const int E = q->E, *dst = (const int*)q->dst.p, *src = (const int*)q->src.p;
        const u16* dis = q->disall;
        const u32 w = wave - 4u;
        _Pragma("nounroll") for (u32 k = 0; k < 5u; ++k) {
            const u32 grp = (u32)(((w == 0u ? 0x1612090503ull : w == 1u ? 0x1713080402ull : w == 2u ? 0x14100e0a06ull : 0x15110f0b07ull) >> (8u * k)) & 255u);
            const u32 row = grp * 4u + (lane >> 4), iv = *(const u32*)(smem + L_INV + row * 4u);
            const int me = (iv & 0x8000u) ? -1 : q->nbase + (int)(r0 + (iv & 255u) + TN - 256u);
            const int m0 = __builtin_amdgcn_readlane(me, 0), m1 = __builtin_amdgcn_readlane(me, 16), m2 = __builtin_amdgcn_readlane(me, 32), m3 = __builtin_amdgcn_readlane(me, 48);
            float s[8] = {0.f, 0.f, 0.f, 0.f, 0.f, 0.f, 0.f, 0.f};
            for (int e0 = 0; e0 < E; e0 += 64) {
                const int e = min(e0 + (int)lane, E - 1), d = dst[e], si = src[e];
                unsigned long long m = __ballot(e0 + (int)lane < E && (d == m0 || d == m1 || d == m2 || d == m3));
                while (m) {
                    const int l = __builtin_ctzll(m);
                    m &= m - 1;
                    const int sl = __builtin_amdgcn_readlane(si, l);
                    if (__builtin_amdgcn_readlane(d, l) == me) {
                        const u32x4 r = *(const u32x4*)(x + (size_t)sl * 256 + half * 128u + (lane & 15u) * 8u);
                        const float wt = bf2f(dis[sl]);
#pragma unroll
                        for (int j = 0; j < 4; ++j) { s[2 * j] += wt * lo(r[j]); s[2 * j + 1] += wt * hi(r[j]); }
                    }
                }
            }
            const float sc = bf2f(dis[max(me, 0)]);
            *(u32x4*)(smem + L_AL + row * 256u + (((lane ^ row) & 15u) << 4)) =
                (u32x4){pack2(s[0] * sc, s[1] * sc), pack2(s[2] * sc, s[3] * sc), pack2(s[4] * sc, s[5] * sc), pack2(s[6] * sc, s[7] * sc)};
            lds_signal(S_T0 + 4u * max(grp >> 2, 1u), 1u);
        }
        return;
    }
    const int Ee = q->Ee, *edst = (const int*)q->edst.p, *esrc = (const int*)q->esrc.p;
    const u16 *wq = (const u16*)q->wq.p, *wk = (const u16*)q->wk.p;
    float* qs = (float*)(smem + L_QK + wave * 1024u);
    _Pragma("nounroll") for (u32 n = wave; n < TN && r0 + n < q->N; n += 4u) {
        const int me = q->nbase + (int)(r0 + n);
        float c0 = 0.f, c1 = 0.f, c2 = 0.f, c3 = 0.f;
        _Pragma("nounroll") for (int i = 0; i < 256; ++i) {
            const float xv = bf2f(x[(size_t)me * 256 + i]);
            const u32x2 r = *(const u32x2*)(wq + i * 256 + lane * 4u);
            c0 += xv * lo(r[0]); c1 += xv * hi(r[0]); c2 += xv * lo(r[1]); c3 += xv * hi(r[1]);
        }
        qs[lane * 4u] = c0; qs[lane * 4u + 1u] = c1; qs[lane * 4u + 2u] = c2; qs[lane * 4u + 3u] = c3;
        float k[4] = {0.f, 0.f, 0.f, 0.f};
        _Pragma("nounroll") for (int c = 0; c < 256; c += 2) {
            const float qa = qs[c], qb = qs[c + 1];
#pragma unroll
            for (int j = 0; j < 4; ++j) { const u32 r = *(const u32*)(wk + (lane * 4u + j) * 256u + c); k[j] += qa * lo(r) + qb * hi(r); }
        }
        float s0 = 0.f, s1 = 0.f, s2 = 0.f, s3 = 0.f, dn = 0.f;
        for (int e0 = 0; e0 < Ee; e0 += 64) {
            const int e = min(e0 + (int)lane, Ee - 1), si = esrc[e];
            unsigned long long m = __ballot(e0 + (int)lane < Ee && edst[e] == me);
            while (m) {
                const int l = __builtin_ctzll(m);
                m &= m - 1;
                const u32x2 r = *(const u32x2*)(x + (size_t)__builtin_amdgcn_readlane(si, l) * 256 + lane * 4u);
                float d = k[0] * lo(r[0]) + k[1] * hi(r[0]) + k[2] * lo(r[1]) + k[3] * hi(r[1]);
#pragma unroll
                for (int o = 1; o < 64; o <<= 1)
                    d += __uint_as_float((u32)__builtin_amdgcn_ds_bpermute((int)((lane ^ (u32)o) * 4u), (int)__float_as_uint(d)));
                const float wt = __builtin_amdgcn_exp2f(d * 0.09016844f);
                s0 += wt * lo(r[0]); s1 += wt * hi(r[0]); s2 += wt * lo(r[1]); s3 += wt * hi(r[1]);
                dn += wt;
            }
        }
        const float sc = __builtin_amdgcn_rcpf(dn + 1e-9f);
        if ((lane >> 5) == half)
            *(u32x2*)(smem + L_AA + ((u32)*(const u16*)(smem + L_MAP + n * 2u) ^ ((lane & 31u) * 8u))) =
                (u32x2){pack2(s0 * sc, s1 * sc), pack2(s2 * sc, s3 * sc)};
    }
    lds_signal(S_Z, 17u);
}

extern "C" void kernel_launch(void* const* d_in, const int* in_sizes, int n_in,
                              void* d_out, int out_size, void* d_ws, size_t ws_size,
                              hipStream_t stream) {
    const u32 N = in_sizes[5], E = in_sizes[1], Ee = in_sizes[3];
    const u16* x = (const u16*)d_in[0];
    const u16* dis = (const u16*)d_in[5];
    const u32 FL = 0x00020000u;
    const bool generic = N > NMAX || E > EMAX || (E & 3u) || (N & 1u);
    const bool irr = N > NMAX || Ee != 4u * N;
    (void)hipFuncSetAttribute((const void*)agg_kernel, hipFuncAttributeMaxDynamicSharedMemorySize, LDS_BYTES);
    for (u32 nbase = 0; nbase < N; nbase += NMAX) {
        const u32 n = N - nbase < NMAX ? N - nbase : NMAX;
        Args a;
        a.x = Desc{x + (size_t)nbase * 256, n * 512u, FL};
        a.ws = Desc{d_ws, WS_END, FL};
        a.wl = Desc{d_in[6], 131072u, FL}; a.wq = Desc{d_in[7], 131072u, FL}; a.wk = Desc{d_in[8], 131072u, FL}; a.wv = Desc{d_in[9], 131072u, FL};
        a.src = Desc{d_in[1], generic ? 0u : E * 4u, FL}; a.dst = Desc{d_in[2], generic ? 0u : E * 4u, FL};
        a.esrc = Desc{d_in[3], irr ? 0u : Ee * 4u, FL}; a.edst = Desc{d_in[4], irr ? 0u : Ee * 4u, FL};
        a.dis = Desc{dis + nbase, n * 2u, FL};
        a.out = Desc{(u16*)d_out + (size_t)nbase * 256, n * 512u, FL};
        a.N = n; a.TN = n > 256u ? (n + 127u) / 128u : 2u; a.M = ((1u << 24) + a.TN - 1u) / a.TN;
        a.irr = (irr ? 1u : 0u) | (generic ? 0x10000u : 0u);
        a.xall = x; a.disall = dis;
        a.E = (int)E; a.Ee = (int)Ee; a.nbase = (int)nbase; a.pad = 0;
        agg_kernel<<<256, 1024, LDS_BYTES, stream>>>(a);
    }
}
```

```cpp
#include <hip/hip_runtime.h>
#include <hip/hip_bf16.h>
#include <stdint.h>

typedef unsigned short u16;
typedef unsigned int u32;
typedef __attribute__((ext_vector_type(4))) u32 u32x4;
typedef __attribute__((ext_vector_type(2))) u32 u32x2;
struct Desc { const void* p; u32 bytes, flags; };
struct Args {
    Desc x, ws, wl, wv, wq, wk, src, dst, esrc, edst, dis, out;
    u32 N, TN, M, irr;
    const u16* xall;
    const u16* disall;
    int E, Ee, nbase, pad;
};
constexpr u32 NMAX = 10240, EMAX = 163840;
constexpr u32 WS_END = 9437184;
constexpr int LDS_BYTES = 163840;
constexpr u32 L_INV = 512, L_MAP = 1792, L_QK = 2048, L_AL = 21760, L_AA = 44288, S_QK = 16, S_Z = 40, S_T0 = 48;

__device__ __forceinline__ float bf2f(u32 h) { return __uint_as_float(h << 16); }
__device__ __forceinline__ float lo(u32 v) { return __uint_as_float(v << 16); }
__device__ __forceinline__ float hi(u32 v) { return __uint_as_float(v & 0xFFFF0000u); }
__device__ __forceinline__ u32 pack2(float a, float b) {
    return (u32)__builtin_bit_cast(u16, (__bf16)a) | ((u32)__builtin_bit_cast(u16, (__bf16)b) << 16);
}
__device__ __forceinline__ void lds_signal(const u32 addr, const u32 v) {
    asm volatile("s_mov_b64 exec, 1\n\tds_add_u32 %0, %1\n\ts_mov_b64 exec, -1" :: "v"(addr), "v"(v) : "memory");
}

extern "C" __global__ void __launch_bounds__(1024, 4)
agg_kernel(const Args a) {
    extern __shared__ __attribute__((aligned(16))) char smem[];
    u32 tid = threadIdx.x, wv;
    asm volatile(
        "s_memrealtime s[74:75]\n\t"
        "s_load_dwordx16 s[16:31], %2, 0\n\t"
        "s_load_dwordx16 s[32:47], %2, 64\n\t"
        "s_load_dwordx16 s[48:63], %2, 128\n\t"
        "s_load_dwordx4 s[64:67], %2, 192\n\t"
        "v_readfirstlane_b32 s69, v0\n\t"
        "v_and_b32 v1, 63, v0\n\t"
        "v_mov_b32 v2, 0\n\t"
        "v_mov_b32 v3, 1\n\t"
        "s_lshr_b32 s76, %3, 3\n\t"
        "s_and_b32 s70, %3, 1\n\t"
        "s_lshr_b32 s71, %3, 1\n\t"
        "s_mov_b32 s83, 0x2000000\n\t"
        "s_lshr_b32 s69, s69, 6\n\t"
        "s_cbranch_scc0 .L7_U\n\t"
        "s_cmp_lt_u32 s69, 8\n\t"
        "s_cbranch_scc1 .L7_L\n\t"
        "s_sub_u32 s69, s69, 8\n\t"
        "v_lshlrev_b32 v4, 2, v1\n\t"
        "s_lshl_b32 s77, s70, 2\n\t"
        "s_xor_b32 s77, s77, s69\n\t"
        "s_xor_b32 s77, s77, 4\n\t"
        "s_mul_i32 s80, s77, 10\n\t"
        "s_waitcnt lgkmcnt(0)\n\t"
        "s_mul_i32 s72, s71, s65\n\t"
        "s_add_u32 s78, s72, s80\n\t"
        "s_lshl_b32 s78, s78, 4\n\t"
        "s_mov_b32 exec_hi, 0xff\n\t"
        "buffer_load_dword v5, v4, s[48:51], s78 offen\n\t"
        "s_mov_b64 exec, -1\n\t"
        "s_branch .L7_O2\n"
        ".L7_U:\n\t"
        "s_setprio 3\n\t"
        "s_cmp_eq_u32 s70, 0\n\t"
        "s_cbranch_scc0 .L7_V\n\t"
        "v_lshrrev_b32 v4, 3, v1\n\t"
        "v_and_b32 v5, 7, v1\n\t"
        "v_lshlrev_b32 v4, 14, v4\n\t"
        "v_lshl_add_u32 v4, v5, 4, v4\n\t"
        "s_lshl_b32 s78, s71, 7\n\t"
        "v_add_u32 v4, s78, v4\n\t"
        "v_add_u32 v5, 0x20000, v4\n\t"
        "v_add_u32 v6, 0x40000, v4\n\t"
        "v_add_u32 v7, 0x60000, v4\n\t"
        "v_add_u32 v8, 0x80000, v4\n\t"
        "s_waitcnt lgkmcnt(0)\n\t"
        "s_mul_i32 s72, s71, s65\n\t"
        "buffer_load_dwordx4 v[16:19], v4, s[44:47], 0 offen nt\n\t"
        "buffer_load_dwordx4 v[20:23], v5, s[44:47], 0 offen nt\n\t"
        "buffer_load_dwordx4 v[24:27], v6, s[44:47], 0 offen nt\n\t"
        "buffer_load_dwordx4 v[28:31], v7, s[44:47], 0 offen nt\n\t"
        "buffer_load_dwordx4 v[32:35], v8, s[44:47], 0 offen nt\n\t"
        "s_buffer_load_dword s68, s[20:23], 0\n\t"
        "s_mul_i32 s80, s71, 0x50\n\t"
        "s_lshl_b32 s78, s80, 4\n\t"
        "v_lshlrev_b32 v12, 4, v1\n\t"
        "buffer_load_dwordx4 v[116:119], v12, s[52:55], s78 offen nt\n\t"
        "s_mov_b64 exec, 0xffff\n\t"
        "buffer_load_dwordx4 v[120:123], v12, s[52:55], s78 offen offset:1024 nt\n\t"
        "s_mov_b64 exec, -1\n\t"
        "buffer_load_dwordx4 v[36:39], v4, s[40:43], 0 offen nt\n\t"
        "buffer_load_dwordx4 v[40:43], v5, s[40:43], 0 offen nt\n\t"
        "buffer_load_dwordx4 v[44:47], v6, s[40:43], 0 offen nt\n\t"
        "buffer_load_dwordx4 v[48:51], v7, s[40:43], 0 offen nt\n\t"
        "buffer_load_dwordx4 v[52:55], v8, s[40:43], 0 offen nt\n\t"
        "s_branch .L7_U2\n"
        ".L7_V:\n\t"
        "s_and_b32 s78, s71, 1\n\t"
        "s_and_b32 s79, s76, 3\n\t"
        "s_lshl2_add_u32 s80, s78, s79\n\t"
        "s_and_b32 s78, s71, 2\n\t"
        "s_lshr_b32 s79, s76, 2\n\t"
        "s_lshl2_add_u32 s81, s78, s79\n\t"
        "v_lshlrev_b32 v12, 4, v1\n\t"
        "v_and_b32 v5, 0x200, v12\n\t"
        "v_mad_u32_u24 v4, v5, 15, v12\n\t"
        "s_lshl_b32 s82, s80, 14\n\t"
        "s_lshr_b32 s78, s81, 1\n\t"
        "s_lshl_b32 s78, s78, 14\n\t"
        "s_and_b32 s79, s81, 1\n\t"
        "s_lshl_b32 s79, s79, 11\n\t"
        "s_add_u32 s77, s78, s79\n\t"
        "s_mov_b32 m0, 68864\n\t"
        "s_waitcnt lgkmcnt(0)\n\t"
        "s_mul_i32 s72, s71, s65\n\t"
        "s_buffer_load_dword s68, s[20:23], 0\n\t"
        "buffer_load_dwordx4 v4, s[36:39], s77 offen offset:0 lds\n\t"
        "s_add_u32 m0, m0, 544\n\t"
        "s_nop 0\n\t"
        "buffer_load_dwordx4 v4, s[36:39], s77 offen offset:512 lds\n\t"
        "s_add_u32 m0, m0, 544\n\t"
        "s_nop 0\n\t"
        "buffer_load_dwordx4 v4, s[36:39], s77 offen offset:1024 lds\n\t"
        "s_add_u32 m0, m0, 544\n\t"
        "s_nop 0\n\t"
        "buffer_load_dwordx4 v4, s[36:39], s77 offen offset:1536 lds\n\t"
        "s_add_u32 m0, m0, 2592\n\t"
        "s_add_u32 s77, s77, 0x1000\n\t"
        "buffer_load_dwordx4 v4, s[36:39], s77 offen offset:0 lds\n\t"
        "s_add_u32 m0, m0, 544\n\t"
        "s_nop 0\n\t"
        "buffer_load_dwordx4 v4, s[36:39], s77 offen offset:512 lds\n\t"
        "s_add_u32 m0, m0, 544\n\t"
        "s_nop 0\n\t"
        "buffer_load_dwordx4 v4, s[36:39], s77 offen offset:1024 lds\n\t"
        "s_add_u32 m0, m0, 544\n\t"
        "s_nop 0\n\t"
        "buffer_load_dwordx4 v4, s[36:39], s77 offen offset:1536 lds\n\t"
        "s_add_u32 m0, m0, 2592\n\t"
        "s_nop 0\n\t"
        "buffer_load_dwordx4 v4, s[32:35], s82 offen offset:0 lds\n\t"
        "s_add_u32 m0, m0, 544\n\t"
        "s_nop 0\n\t"
        "buffer_load_dwordx4 v4, s[32:35], s82 offen offset:512 lds\n\t"
        "s_add_u32 m0, m0, 544\n\t"
        "s_nop 0\n\t"
        "buffer_load_dwordx4 v4, s[32:35], s82 offen offset:1024 lds\n\t"
        "s_add_u32 m0, m0, 544\n\t"
        "s_nop 0\n\t"
        "buffer_load_dwordx4 v4, s[32:35], s82 offen offset:1536 lds\n\t"
        "s_add_u32 m0, m0, 2592\n\t"
        "s_add_u32 s82, s82, 0x1000\n\t"
        "buffer_load_dwordx4 v4, s[32:35], s82 offen offset:0 lds\n\t"
        "s_add_u32 m0, m0, 544\n\t"
        "s_nop 0\n\t"
        "buffer_load_dwordx4 v4, s[32:35], s82 offen offset:512 lds\n\t"
        "s_add_u32 m0, m0, 544\n\t"
        "s_nop 0\n\t"
        "buffer_load_dwordx4 v4, s[32:35], s82 offen offset:1024 lds\n\t"
        "s_add_u32 m0, m0, 544\n\t"
        "s_nop 0\n\t"
        "buffer_load_dwordx4 v4, s[32:35], s82 offen offset:1536 lds\n\t"
        "s_add_u32 m0, m0, 2592\n\t"
        "s_sub_u32 s82, s82, 0x800\n\t"
        "buffer_load_dwordx4 v4, s[32:35], s82 offen offset:0 lds\n\t"
        "s_add_u32 m0, m0, 544\n\t"
        "s_nop 0\n\t"
        "buffer_load_dwordx4 v4, s[32:35], s82 offen offset:512 lds\n\t"
        "s_add_u32 m0, m0, 544\n\t"
        "s_nop 0\n\t"
        "buffer_load_dwordx4 v4, s[32:35], s82 offen offset:1024 lds\n\t"
        "s_add_u32 m0, m0, 544\n\t"
        "s_nop 0\n\t"
        "buffer_load_dwordx4 v4, s[32:35], s82 offen offset:1536 lds\n\t"
        "s_add_u32 m0, m0, 2592\n\t"
        "s_add_u32 s82, s82, 0x1000\n\t"
        "buffer_load_dwordx4 v4, s[32:35], s82 offen offset:0 lds\n\t"
        "s_add_u32 m0, m0, 544\n\t"
        "s_nop 0\n\t"
        "buffer_load_dwordx4 v4, s[32:35], s82 offen offset:512 lds\n\t"
        "s_add_u32 m0, m0, 544\n\t"
        "s_nop 0\n\t"
        "buffer_load_dwordx4 v4, s[32:35], s82 offen offset:1024 lds\n\t"
        "s_add_u32 m0, m0, 544\n\t"
        "s_nop 0\n\t"
        "buffer_load_dwordx4 v4, s[32:35], s82 offen offset:1536 lds\n\t"
        "s_branch .L7_V2\n"
        ".L7_L:\n\t"
        "s_cmp_lt_u32 s69, 4\n\t"
        "s_cbranch_scc0 .L7_G\n\t"
        "s_cmp_eq_u32 s69, 2\n\t"
        "s_cbranch_scc1 .L7_P2\n\t"
        "s_cmp_eq_u32 s69, 1\n\t"
        "s_cbranch_scc0 .L7_T\n\t"
        "v_lshlrev_b32 v4, 3, v1\n\t"
        "v_mov_b64 v[8:9], 0\n\t"
        "ds_write_b64 v4, v[8:9]\n\t"
        "s_waitcnt lgkmcnt(0)\n\t"
        "s_mul_i32 s72, s71, s65\n\t"
        "v_add_lshl_u32 v4, v1, s72, 1\n\t"
        "buffer_load_ushort v16, v4, s[56:59], 0 offen\n\t"
        "buffer_load_ushort v17, v4, s[56:59], 0 offen offset:128\n\t"
        "s_branch .L7_R2\n"
        ".L7_T:\n\t"
        "v_lshlrev_b32 v6, 7, v1\n\t"
        "s_mov_b32 m0, 134400\n\t"
        "s_lshl_b32 s78, s70, 16\n\t"
        "s_lshl_b32 s79, s76, 11\n\t"
        "s_add_u32 s80, s78, s79\n\t"
        "s_mul_i32 s81, s76, 0x300\n\t"
        "s_lshl_b32 s82, %3, 7\n\t"
        "s_add_u32 s82, s82, 4096\n\t"
        "v_lshlrev_b32 v8, 2, v1\n\t"
        "s_waitcnt lgkmcnt(0)\n\t"
        "s_mul_i32 s72, s71, s65\n\t"
        "v_mov_b32 v7, s74\n\t"
        "s_mov_b32 exec_hi, 0\n\t"
        "buffer_store_dword v7, v8, s[20:23], s82 offen sc1\n\t"
        "s_getpc_b64 s[32:33]\n\t"
        "s_add_u32 s32, s32, agg_kernel@rel32@lo+4\n\t"
        "s_addc_u32 s33, s33, agg_kernel@rel32@hi+12\n\t"
        "s_and_b32 s33, s33, 0xffff\n\t"
        "s_mov_b32 s34, .L7_x-agg_kernel\n\t"
        "s_mov_b64 exec, 63\n\t"
        "buffer_load_dword v6, s[32:35], s81 offen lds\n\t"
        "s_barrier\n\t"
        "s_mov_b64 exec, -1\n\t"
        "s_mov_b64 exec, 0xffff\n\t"
        "buffer_load_dword v6, s[28:31], s80 offen lds\n\t"
        "buffer_load_dword v6, s[24:27], s80 offen lds\n\t"
        "s_mov_b64 exec, -1\n\t"
        "s_branch .L7_T2\n"
        ".L7_G:\n\t"
        "s_sub_u32 s69, s69, 4\n\t"
        "v_lshlrev_b32 v4, 4, v1\n\t"
        "v_and_b32 v5, 0x200, v4\n\t"
        "v_mad_u32_u24 v4, v5, 7, v4\n\t"
        "s_mul_i32 s80, s70, 5\n\t"
        "s_mul_i32 s81, s69, 0x420\n\t"
        "s_add_u32 s81, s81, 2048\n\t"
        "s_mul_i32 s78, s80, 0x1080\n\t"
        "s_add_u32 m0, s78, s81\n\t"
        "s_lshr_b32 s78, s80, 1\n\t"
        "s_lshl1_add_u32 s78, s78, s80\n\t"
        "s_lshl_b32 s78, s78, 11\n\t"
        "s_waitcnt lgkmcnt(0)\n\t"
        "s_mul_i32 s72, s71, s65\n\t"
        "s_add_u32 s79, s72, s69\n\t"
        "s_lshl_b32 s79, s79, 9\n\t"
        "v_add_u32 v4, s79, v4\n\t"
        "buffer_load_dwordx4 v4, s[16:19], s78 offen lds\n\t"
        "s_add_u32 s80, s80, 1\n\t"
        "s_mul_i32 s78, s80, 0x1080\n\t"
        "s_add_u32 m0, s78, s81\n\t"
        "s_lshr_b32 s78, s80, 1\n\t"
        "s_lshl1_add_u32 s78, s78, s80\n\t"
        "s_lshl_b32 s78, s78, 11\n\t"
        "buffer_load_dwordx4 v4, s[16:19], s78 offen lds\n\t"
        "s_add_u32 s80, s80, 1\n\t"
        "s_mul_i32 s78, s80, 0x1080\n\t"
        "s_add_u32 m0, s78, s81\n\t"
        "s_lshr_b32 s78, s80, 1\n\t"
        "s_lshl1_add_u32 s78, s78, s80\n\t"
        "s_lshl_b32 s78, s78, 11\n\t"
        "buffer_load_dwordx4 v4, s[16:19], s78 offen lds\n\t"
        "s_add_u32 s80, s80, 1\n\t"
        "s_mul_i32 s78, s80, 0x1080\n\t"
        "s_add_u32 m0, s78, s81\n\t"
        "s_lshr_b32 s78, s80, 1\n\t"
        "s_lshl1_add_u32 s78, s78, s80\n\t"
        "s_lshl_b32 s78, s78, 11\n\t"
        "buffer_load_dwordx4 v4, s[16:19], s78 offen lds\n\t"
        "s_add_u32 s80, s80, 1\n\t"
        "s_mul_i32 s78, s80, 0x1080\n\t"
        "s_add_u32 m0, s78, s81\n\t"
        "s_lshr_b32 s78, s80, 1\n\t"
        "s_lshl1_add_u32 s78, s78, s80\n\t"
        "s_lshl_b32 s78, s78, 11\n\t"
        "buffer_load_dwordx4 v4, s[16:19], s78 offen lds\n\t"
        "s_add_u32 s80, s80, 1\n\t"
        "s_branch .L7_G2\n"
        ".L7_O2:\n\t"
        "s_barrier\n\t"
        "v_lshrrev_b32 v6, 4, v1\n\t"
        "v_lshlrev_b32 v7, 2, v6\n\t"
        "v_and_b32 v8, 15, v1\n\t"
        "s_lshl_b32 s78, s70, 8\n\t"
        "v_lshl_add_u32 v9, v8, 4, s78\n\t"
        "v_lshrrev_b32 v10, 3, v1\n\t"
        "v_lshlrev_b32 v10, 9, v10\n\t"
        "v_and_b32 v11, 7, v1\n\t"
        "v_lshl_add_u32 v10, v11, 4, v10\n\t"
        "v_and_b32 v11, 16, v1\n\t"
        "v_lshlrev_b32 v11, 2, v11\n\t"
        "v_xor_b32 v10, v10, v11\n\t"
        "s_add_u32 s81, s69, s76\n\t"
        "s_and_b32 s81, s81, 7\n\t"
        "s_lshr_b32 s78, s81, 1\n\t"
        "s_lshl_b32 s78, s78, 7\n\t"
        "s_and_b32 s79, s81, 1\n\t"
        "s_lshl_b32 s79, s79, 15\n\t"
        "s_add_u32 s78, s78, s79\n\t"
        "s_lshl_b32 s79, s70, 16\n\t"
        "s_add_u32 s78, s78, s79\n\t"
        "v_add_u32 v10, s78, v10\n\t"
        "s_lshl_b32 s82, s81, 13\n\t"
        "s_add_u32 s82, s82, 68864\n\t"
        "v_bfe_u32 v12, v1, 2, 2\n\t"
        "v_lshlrev_b32 v11, 10, v6\n\t"
        "v_lshl_add_u32 v11, v12, 7, v11\n\t"
        "v_lshrrev_b32 v12, 1, v12\n\t"
        "s_and_b32 s78, s81, 1\n\t"
        "v_xor_b32 v12, s78, v12\n\t"
        "v_lshl_add_u32 v11, v12, 6, v11\n\t"
        "v_and_b32 v12, 3, v1\n\t"
        "v_lshl_add_u32 v11, v12, 4, v11\n\t"
        "s_lshr_b32 s78, s81, 1\n\t"
        "s_lshl_b32 s78, s78, 14\n\t"
        "s_add_u32 s78, s78, 68864\n\t"
        "v_add_u32 v23, s78, v11\n\t"
        "s_waitcnt vmcnt(0)\n\t"
        "ds_bpermute_b32 v12, v7, v5 offset:0\n\t"
        "ds_bpermute_b32 v13, v7, v5 offset:16\n\t"
        "ds_bpermute_b32 v14, v7, v5 offset:32\n\t"
        "ds_bpermute_b32 v15, v7, v5 offset:48\n\t"
        "ds_bpermute_b32 v16, v7, v5 offset:64\n\t"
        "ds_bpermute_b32 v17, v7, v5 offset:80\n\t"
        "ds_bpermute_b32 v18, v7, v5 offset:96\n\t"
        "ds_bpermute_b32 v19, v7, v5 offset:112\n\t"
        "ds_bpermute_b32 v20, v7, v5 offset:128\n\t"
        "ds_bpermute_b32 v21, v7, v5 offset:144\n\t"
        "s_waitcnt lgkmcnt(0)\n\t"
        "v_lshl_add_u32 v12, v12, 9, v9\n\t"
        "v_lshl_add_u32 v13, v13, 9, v9\n\t"
        "v_lshl_add_u32 v14, v14, 9, v9\n\t"
        "v_lshl_add_u32 v15, v15, 9, v9\n\t"
        "v_lshl_add_u32 v16, v16, 9, v9\n\t"
        "v_lshl_add_u32 v17, v17, 9, v9\n\t"
        "v_lshl_add_u32 v18, v18, 9, v9\n\t"
        "v_lshl_add_u32 v19, v19, 9, v9\n\t"
        "v_lshl_add_u32 v20, v20, 9, v9\n\t"
        "v_lshl_add_u32 v21, v21, 9, v9\n\t"
        "s_setprio 0\n"
        ".L7_w1:\n\t"
        "ds_read_b32 v0, v2 offset:44\n\t"
        "s_waitcnt lgkmcnt(0)\n\t"
        "v_cmp_gt_u16 vcc, 1, v0\n\t"
        "s_cbranch_vccz .L7_d2\n\t"
        "s_sleep 1\n\t"
        "s_sub_i32 s83, s83, 1\n\t"
        "s_cmp_gt_i32 s83, 0\n\t"
        "s_cbranch_scc1 .L7_w1\n"
        ".L7_d2:\n"
        ".L7_w3:\n\t"
        "ds_read_b64 v[4:5], v2 offset:72\n\t"
        "s_waitcnt lgkmcnt(0)\n\t"
        "v_cmp_gt_u16 vcc, 1, v4\n\t"
        "s_cbranch_vccz .L7_d4\n\t"
        "s_sleep 1\n\t"
        "s_sub_i32 s83, s83, 1\n\t"
        "s_cmp_gt_i32 s83, 0\n\t"
        "s_cbranch_scc1 .L7_w3\n"
        ".L7_d4:\n\t"
        "v_readfirstlane_b32 s68, v5\n\t"
        "s_add_u32 m0, s82, 0\n\t"
        "s_mov_b32 s78, 0\n\t"
        "buffer_load_dwordx4 v10, s[28:31], s78 offen lds\n\t"
        "s_add_u32 m0, m0, 0x400\n\t"
        "s_add_u32 s78, s78, 0x1000\n\t"
        "buffer_load_dwordx4 v10, s[28:31], s78 offen lds\n\t"
        "s_add_u32 m0, m0, 0x400\n\t"
        "s_add_u32 s78, s78, 0x1000\n\t"
        "buffer_load_dwordx4 v10, s[28:31], s78 offen lds\n\t"
        "s_add_u32 m0, m0, 0x400\n\t"
        "s_add_u32 s78, s78, 0x1000\n\t"
        "buffer_load_dwordx4 v10, s[28:31], s78 offen lds\n\t"
        "s_add_u32 m0, m0, 0x400\n\t"
        "s_add_u32 s78, s78, 0x1000\n\t"
        "buffer_load_dwordx4 v10, s[28:31], s78 offen lds\n\t"
        "s_add_u32 m0, m0, 0x400\n\t"
        "s_add_u32 s78, s78, 0x1000\n\t"
        "buffer_load_dwordx4 v10, s[28:31], s78 offen lds\n\t"
        "s_add_u32 m0, m0, 0x400\n\t"
        "s_add_u32 s78, s78, 0x1000\n\t"
        "buffer_load_dwordx4 v10, s[28:31], s78 offen lds\n\t"
        "s_add_u32 m0, m0, 0x400\n\t"
        "s_add_u32 s78, s78, 0x1000\n\t"
        "buffer_load_dwordx4 v10, s[28:31], s78 offen lds\n\t"
        "s_mov_b64 exec, 1\n\t"
        "ds_add_u32 v2, v3 offset:88\n\t"
        "s_mov_b64 exec, -1\n\t"
        "s_mov_b32 s36, 0x22222222\n\t"
        "s_mov_b32 s37, s36\n\t"
        "s_lshl_b64 s[38:39], s[36:37], 1\n\t"
        "s_lshl_b64 s[40:41], s[36:37], 2\n\t"
        "s_and_b32 s44, s68, 0xfff\n\t"
        "s_xor_b32 s45, s44, 0x5a5\n\t"
        "s_lshr_b32 s46, s68, 12\n\t"
        "s_lshr_b32 s47, s68, 20\n\t"
        "s_add_u32 s46, s46, s68\n\t"
        "s_and_b32 s46, s46, 0xfff\n\t"
        "s_add_u32 s47, s47, s68\n\t"
        "s_and_b32 s47, s47, 0xfff\n\t"
        "v_mov_b32 v22, s44\n\t"
        "v_mov_b32 v0, s45\n\t"
        "v_cndmask_b32 v22, v22, v0, s[36:37]\n\t"
        "v_mov_b32 v0, s46\n\t"
        "v_cndmask_b32 v22, v22, v0, s[38:39]\n\t"
        "v_mov_b32 v0, s47\n\t"
        "v_cndmask_b32 v22, v22, v0, s[40:41]\n\t"
        "s_lshl3_add_u32 s42, %3, s77\n\t"
        "s_lshl_b32 s42, s42, 8\n\t"
        "s_add_u32 s42, s42, 262144\n\t"
        "s_xor_b32 s43, s42, 0x800\n\t"
        "s_movk_i32 s48, 0xfff\n\t"
        "s_waitcnt vmcnt(0)\n\t"
        "s_mov_b64 exec, 1\n\t"
        "ds_add_u32 v2, v3 offset:20\n\t"
        "s_mov_b64 exec, -1\n"
        ".L7_w5:\n\t"
        "ds_read_b32 v0, v2 offset:20\n\t"
        "s_waitcnt lgkmcnt(0)\n\t"
        "v_cmp_gt_u16 vcc, 8, v0\n\t"
        "s_cbranch_vccz .L7_d6\n\t"
        "s_sleep 1\n\t"
        "s_sub_i32 s83, s83, 1\n\t"
        "s_cmp_gt_i32 s83, 0\n\t"
        "s_cbranch_scc1 .L7_w5\n"
        ".L7_d6:\n\t"
        "ds_read_b64_tr_b16 v[64:65], v23 offset:0\n\t"
        "ds_read_b64_tr_b16 v[66:67], v23 offset:512\n\t"
        "ds_read_b64_tr_b16 v[68:69], v23 offset:8\n\t"
        "ds_read_b64_tr_b16 v[70:71], v23 offset:520\n\t"
        "ds_read_b64_tr_b16 v[72:73], v23 offset:4096\n\t"
        "ds_read_b64_tr_b16 v[74:75], v23 offset:4608\n\t"
        "ds_read_b64_tr_b16 v[76:77], v23 offset:4104\n\t"
        "ds_read_b64_tr_b16 v[78:79], v23 offset:4616\n\t"
        "ds_read_b64_tr_b16 v[80:81], v23 offset:8192\n\t"
        "ds_read_b64_tr_b16 v[82:83], v23 offset:8704\n\t"
        "ds_read_b64_tr_b16 v[84:85], v23 offset:8200\n\t"
        "ds_read_b64_tr_b16 v[86:87], v23 offset:8712\n\t"
        "ds_read_b64_tr_b16 v[88:89], v23 offset:12288\n\t"
        "ds_read_b64_tr_b16 v[90:91], v23 offset:12800\n\t"
        "s_waitcnt lgkmcnt(10)\n\t"
        "ds_read_b64_tr_b16 v[92:93], v23 offset:12296\n\t"
        "ds_read_b64_tr_b16 v[94:95], v23 offset:12808\n\t"
        "s_mov_b64 exec, 1\n\t"
        "ds_add_u32 v2, v3 offset:24\n\t"
        "s_mov_b64 exec, -1\n\t"
        "buffer_load_dwordx4 v[24:27], v12, s[16:19], 0 offen\n\t"
        "buffer_load_dwordx4 v[28:31], v13, s[16:19], 0 offen\n\t"
        "buffer_load_dwordx4 v[32:35], v14, s[16:19], 0 offen\n\t"
        "buffer_load_dwordx4 v[36:39], v15, s[16:19], 0 offen\n\t"
        "buffer_load_dwordx4 v[40:43], v16, s[16:19], 0 offen\n\t"
        "buffer_load_dwordx4 v[44:47], v17, s[16:19], 0 offen\n\t"
        "buffer_load_dwordx4 v[48:51], v18, s[16:19], 0 offen\n\t"
        "buffer_load_dwordx4 v[52:55], v19, s[16:19], 0 offen\n\t"
        "buffer_load_dwordx4 v[56:59], v20, s[16:19], 0 offen\n\t"
        "buffer_load_dwordx4 v[60:63], v21, s[16:19], 0 offen\n"
        ".L7_o2:\n\t"
        "s_setprio 2\n\t"
        "s_mul_i32 s78, s80, 272\n\t"
        "v_lshl_add_u32 v21, v8, 4, s78\n"
        ".L7_w7:\n\t"
        "ds_read_b32 v0, v2 offset:16\n\t"
        "s_waitcnt lgkmcnt(0)\n\t"
        "v_cmp_gt_u16 vcc, 4, v0\n\t"
        "s_cbranch_vccz .L7_d8\n\t"
        "s_sleep 1\n\t"
        "s_sub_i32 s83, s83, 1\n\t"
        "s_cmp_gt_i32 s83, 0\n\t"
        "s_cbranch_scc1 .L7_w7\n"
        ".L7_d8:\n\t"
        "ds_read_b128 v[96:99], v21 offset:2048\n\t"
        "ds_read_b128 v[100:103], v21 offset:2320\n\t"
        "ds_read_b128 v[104:107], v21 offset:2592\n\t"
        "ds_read_b128 v[108:111], v21 offset:2864\n\t"
        "ds_read_b128 v[112:115], v21 offset:3136\n\t"
        "ds_read_b128 v[116:119], v21 offset:3408\n\t"
        "ds_read_b128 v[120:123], v21 offset:3680\n\t"
        "ds_read_b128 v[124:127], v21 offset:3952\n\t"
        "ds_read_b128 v[4:7], v21 offset:4224\n\t"
        "ds_read_b128 v[8:11], v21 offset:4496\n\t"
        "s_waitcnt vmcnt(0)\n\t"
        "s_waitcnt lgkmcnt(8)\n\t"
        "v_dot2_f32_bf16 v12, v24, v96, 0\n\t"
        "v_dot2_f32_bf16 v13, v28, v100, 0\n\t"
        "v_dot2_f32_bf16 v12, v25, v97, v12\n\t"
        "v_dot2_f32_bf16 v13, v29, v101, v13\n\t"
        "v_dot2_f32_bf16 v12, v26, v98, v12\n\t"
        "v_dot2_f32_bf16 v13, v30, v102, v13\n\t"
        "v_dot2_f32_bf16 v12, v27, v99, v12\n\t"
        "v_dot2_f32_bf16 v13, v31, v103, v13\n\t"
        "s_waitcnt lgkmcnt(6)\n\t"
        "v_dot2_f32_bf16 v14, v32, v104, 0\n\t"
        "v_dot2_f32_bf16 v15, v36, v108, 0\n\t"
        "v_dot2_f32_bf16 v14, v33, v105, v14\n\t"
        "v_dot2_f32_bf16 v15, v37, v109, v15\n\t"
        "v_dot2_f32_bf16 v14, v34, v106, v14\n\t"
        "v_dot2_f32_bf16 v15, v38, v110, v15\n\t"
        "v_dot2_f32_bf16 v14, v35, v107, v14\n\t"
        "v_dot2_f32_bf16 v15, v39, v111, v15\n\t"
        "s_waitcnt lgkmcnt(4)\n\t"
        "v_dot2_f32_bf16 v16, v40, v112, 0\n\t"
        "v_dot2_f32_bf16 v17, v44, v116, 0\n\t"
        "v_dot2_f32_bf16 v16, v41, v113, v16\n\t"
        "v_dot2_f32_bf16 v17, v45, v117, v17\n\t"
        "v_dot2_f32_bf16 v16, v42, v114, v16\n\t"
        "v_dot2_f32_bf16 v17, v46, v118, v17\n\t"
        "v_dot2_f32_bf16 v16, v43, v115, v16\n\t"
        "v_dot2_f32_bf16 v17, v47, v119, v17\n\t"
        "s_waitcnt lgkmcnt(2)\n\t"
        "v_dot2_f32_bf16 v18, v48, v120, 0\n\t"
        "v_dot2_f32_bf16 v19, v52, v124, 0\n\t"
        "v_dot2_f32_bf16 v18, v49, v121, v18\n\t"
        "v_dot2_f32_bf16 v19, v53, v125, v19\n\t"
        "v_dot2_f32_bf16 v18, v50, v122, v18\n\t"
        "v_dot2_f32_bf16 v19, v54, v126, v19\n\t"
        "v_dot2_f32_bf16 v18, v51, v123, v18\n\t"
        "v_dot2_f32_bf16 v19, v55, v127, v19\n\t"
        "s_waitcnt lgkmcnt(0)\n\t"
        "v_dot2_f32_bf16 v20, v56, v4, 0\n\t"
        "v_dot2_f32_bf16 v21, v60, v8, 0\n\t"
        "v_dot2_f32_bf16 v20, v57, v5, v20\n\t"
        "v_dot2_f32_bf16 v21, v61, v9, v21\n\t"
        "v_dot2_f32_bf16 v20, v58, v6, v20\n\t"
        "v_dot2_f32_bf16 v21, v62, v10, v21\n\t"
        "v_dot2_f32_bf16 v20, v59, v7, v20\n\t"
        "v_dot2_f32_bf16 v21, v63, v11, v21\n\t"
        "v_add_f32_dpp v12, v12, v12 row_ror:8 row_mask:0xf bank_mask:0x3\n\t"
        "v_add_f32_dpp v14, v14, v14 row_ror:8 row_mask:0xf bank_mask:0x3\n\t"
        "v_add_f32_dpp v16, v16, v16 row_ror:8 row_mask:0xf bank_mask:0x3\n\t"
        "v_add_f32_dpp v18, v18, v18 row_ror:8 row_mask:0xf bank_mask:0x3\n\t"
        "v_add_f32_dpp v20, v20, v20 row_ror:8 row_mask:0xf bank_mask:0x3\n\t"
        "v_add_f32_dpp v12, v13, v13 row_ror:8 row_mask:0xf bank_mask:0xc\n\t"
        "v_add_f32_dpp v14, v15, v15 row_ror:8 row_mask:0xf bank_mask:0xc\n\t"
        "v_add_f32_dpp v16, v17, v17 row_ror:8 row_mask:0xf bank_mask:0xc\n\t"
        "v_add_f32_dpp v18, v19, v19 row_ror:8 row_mask:0xf bank_mask:0xc\n\t"
        "v_add_f32_dpp v20, v21, v21 row_ror:8 row_mask:0xf bank_mask:0xc\n\t"
        "v_add_f32_dpp v12, v12, v12 row_half_mirror row_mask:0xf bank_mask:0x5\n\t"
        "v_add_f32_dpp v16, v16, v16 row_half_mirror row_mask:0xf bank_mask:0x5\n\t"
        "v_add_f32_dpp v20, v20, v20 row_half_mirror row_mask:0xf bank_mask:0xf\n\t"
        "v_add_f32_dpp v12, v14, v14 row_half_mirror row_mask:0xf bank_mask:0xa\n\t"
        "v_add_f32_dpp v16, v18, v18 row_half_mirror row_mask:0xf bank_mask:0xa\n\t"
        "v_add_f32_dpp v20, v20, v20 quad_perm:[1,0,3,2] row_mask:0xf bank_mask:0xf\n\t"
        "v_add_f32_dpp v12, v12, v12 quad_perm:[1,0,3,2] row_mask:0xf bank_mask:0xf\n\t"
        "v_add_f32_dpp v16, v16, v16 quad_perm:[1,0,3,2] row_mask:0xf bank_mask:0xf\n\t"
        "v_add_f32_dpp v20, v20, v20 quad_perm:[2,3,0,1] row_mask:0xf bank_mask:0xf\n\t"
        "v_add_f32_dpp v12, v12, v12 quad_perm:[2,3,0,1] row_mask:0xf bank_mask:0xf\n\t"
        "v_add_f32_dpp v16, v16, v16 quad_perm:[2,3,0,1] row_mask:0xf bank_mask:0xf\n\t"
        "v_cndmask_b32 v12, v12, v16, s[36:37]\n\t"
        "v_cndmask_b32 v12, v12, v20, s[38:39]\n\t"
        "v_add_u32 v12, 0x800, v12\n\t"
        "v_bfi_b32 v21, s48, v22, v12\n\t"
        "v_lshlrev_b32 v13, 2, v1\n\t"
        "s_setprio 3\n\t"
        "buffer_store_dword v21, v13, s[20:23], s42 offen sc1\n\t"
        "s_setprio 2\n\t"
        "v_and_b32 v14, 3, v1\n\t"
        "v_lshlrev_b32 v14, 4, v14\n\t"
        "s_mov_b32 s78, 0xffff\n\t"
        "s_mov_b32 s79, 0\n\t"
        "v_lshlrev_b64 v[4:5], v14, s[78:79]\n\t"
        "s_mov_b32 s50, 0x0f0f0f0f\n\t"
        "s_mov_b32 s51, s50\n\t"
        "v_cndmask_b32 v6, v4, 0, s[50:51]\n\t"
        "v_cndmask_b32 v7, v5, 0, s[50:51]\n\t"
        "v_cndmask_b32 v4, 0, v4, s[50:51]\n\t"
        "v_cndmask_b32 v5, 0, v5, s[50:51]\n\t"
        "v_lshrrev_b32 v14, 5, v1\n\t"
        "v_add_lshl_u32 v14, v14, s80, 1\n"
        ".L7_w9:\n\t"
        "ds_read_b32 v0, v2 offset:8\n\t"
        "ds_read_u16 v16, v14 offset:1792\n\t"
        "ds_read_u16 v17, v14 offset:1796\n\t"
        "ds_read_u16 v18, v14 offset:1800\n\t"
        "ds_read_u16 v19, v14 offset:1804\n\t"
        "ds_read_u16 v20, v14 offset:1808\n\t"
        "s_waitcnt lgkmcnt(5)\n\t"
        "v_cmp_gt_u16 vcc, 2, v0\n\t"
        "s_cbranch_vccz .L7_d10\n\t"
        "s_waitcnt lgkmcnt(0)\n\t"
        "s_sleep 1\n\t"
        "s_sub_i32 s83, s83, 1\n\t"
        "s_cmp_gt_i32 s83, 0\n\t"
        "s_cbranch_scc1 .L7_w9\n"
        ".L7_d10:\n\t"
        "v_and_b32 v14, 15, v1\n\t"
        "v_lshlrev_b32 v14, 4, v14\n\t"
        "v_bfe_u32 v15, v1, 4, 1\n\t"
        "v_lshl_add_u32 v14, v15, 3, v14\n\t"
        "s_waitcnt lgkmcnt(0)\n\t"
        "v_xor_b32 v16, v16, v14\n\t"
        "v_xor_b32 v17, v17, v14\n\t"
        "v_xor_b32 v18, v18, v14\n\t"
        "v_xor_b32 v19, v19, v14\n\t"
        "v_xor_b32 v20, v20, v14\n\t"
        "v_xor_b32 v21, v21, v22\n\t"
        "v_mov_b64 v[8:9], 0\n\t"
        "v_mov_b64 v[10:11], 0\n\t"
        "buffer_load_dword v12, v13, s[20:23], s43 offen sc1\n"
        ".L7_w11:\n\t"
        "ds_read_b32 v0, v2 offset:28\n\t"
        "s_waitcnt lgkmcnt(0)\n\t"
        "v_cmp_gt_u16 vcc, 4, v0\n\t"
        "s_cbranch_vccz .L7_d12\n\t"
        "s_sleep 1\n\t"
        "s_sub_i32 s83, s83, 1\n\t"
        "s_cmp_gt_i32 s83, 0\n\t"
        "s_cbranch_scc1 .L7_w11\n"
        ".L7_d12:\n\t"
        "ds_read_b64_tr_b16 v[96:97], v23 offset:0\n\t"
        "ds_read_b64_tr_b16 v[98:99], v23 offset:512\n\t"
        "ds_read_b64_tr_b16 v[100:101], v23 offset:8\n\t"
        "ds_read_b64_tr_b16 v[102:103], v23 offset:520\n\t"
        "ds_read_b64_tr_b16 v[104:105], v23 offset:4096\n\t"
        "ds_read_b64_tr_b16 v[106:107], v23 offset:4608\n\t"
        "ds_read_b64_tr_b16 v[108:109], v23 offset:4104\n\t"
        "ds_read_b64_tr_b16 v[110:111], v23 offset:4616\n\t"
        "ds_read_b64_tr_b16 v[112:113], v23 offset:8192\n\t"
        "ds_read_b64_tr_b16 v[114:115], v23 offset:8704\n\t"
        "ds_read_b64_tr_b16 v[116:117], v23 offset:8200\n\t"
        "ds_read_b64_tr_b16 v[118:119], v23 offset:8712\n\t"
        "ds_read_b64_tr_b16 v[120:121], v23 offset:12288\n\t"
        "ds_read_b64_tr_b16 v[122:123], v23 offset:12800\n\t"
        "s_branch .L7_sw\n"
        ".L7_sp:\n\t"
        "buffer_load_dword v12, v13, s[20:23], s43 offen sc1\n"
        ".L7_sw:\n\t"
        "s_waitcnt vmcnt(0)\n\t"
        "v_xor_b32 v12, v12, v22\n\t"
        "v_and_b32 v0, s48, v12\n\t"
        "v_cmp_ne_u32 vcc, 0, v0\n\t"
        "s_cbranch_vccz .L7_sq\n\t"
        "s_sleep 4\n\t"
        "s_sub_i32 s83, s83, 1\n\t"
        "s_cmp_gt_i32 s83, 0\n\t"
        "s_cbranch_scc1 .L7_sp\n"
        ".L7_sq:\n\t"
        "v_add_f32 v21, v21, v12\n\t"
        "v_exp_f32 v21, v21\n\t"
        "v_mov_b64 v[12:13], 0\n\t"
        "v_mov_b64 v[14:15], 0\n\t"
        "v_mov_b32 v22, v21\n\t"
        "v_mov_b32 v0, v21\n\t"
        "s_nop 1\n\t"
        "v_permlane16_swap_b32 v22, v0\n\t"
        "v_add_f32 v22, v22, v0\n\t"
        "v_mov_b32 v0, v22\n\t"
        "s_nop 1\n\t"
        "v_permlane32_swap_b32 v22, v0\n\t"
        "v_add_f32 v22, v22, v0\n\t"
        "v_add_f32 v22, 0x3089705f, v22\n\t"
        "v_rcp_f32 v22, v22\n\t"
        "s_nop 0\n\t"
        "v_mul_f32 v21, v21, v22\n\t"
        "v_cvt_pk_bf16_f32 v21, v21, v21\n\t"
        "v_lshlrev_b32 v22, 4, v1\n\t"
        "v_add_u32 v22, 142848, v22\n"
        ".L7_w13:\n\t"
        "ds_read_b32 v0, v2 offset:36\n\t"
        "s_waitcnt lgkmcnt(0)\n\t"
        "v_cmp_gt_u16 vcc, 4, v0\n\t"
        "s_cbranch_vccz .L7_d14\n\t"
        "s_sleep 1\n\t"
        "s_sub_i32 s83, s83, 1\n\t"
        "s_cmp_gt_i32 s83, 0\n\t"
        "s_cbranch_scc1 .L7_w13\n"
        ".L7_d14:\n\t"
        "v_cmp_lt_u32 vcc, 0xfffff, v0\n\t"
        "s_cbranch_vccnz .L7_na\n\t"
        "v_and_b32_dpp v8, v21, v4 row_newbcast:0 row_mask:0xf bank_mask:0x3\n\t"
        "v_and_b32_dpp v9, v21, v5 row_newbcast:0 row_mask:0xf bank_mask:0x3\n\t"
        "v_and_b32_dpp v10, v21, v6 row_newbcast:0 row_mask:0xf bank_mask:0x3\n\t"
        "v_and_b32_dpp v11, v21, v7 row_newbcast:0 row_mask:0xf bank_mask:0x3\n\t"
        "v_and_b32_dpp v12, v21, v4 row_newbcast:8 row_mask:0xf bank_mask:0xc\n\t"
        "v_and_b32_dpp v13, v21, v5 row_newbcast:8 row_mask:0xf bank_mask:0xc\n\t"
        "v_and_b32_dpp v14, v21, v6 row_newbcast:8 row_mask:0xf bank_mask:0xc\n\t"
        "v_and_b32_dpp v15, v21, v7 row_newbcast:8 row_mask:0xf bank_mask:0xc\n\t"
        "v_mfma_f32_16x16x32_bf16 v[124:127], v[8:11], v[24:27], 0\n\t"
        "s_nop 0\n\t"
        "v_mfma_f32_16x16x32_bf16 v[124:127], v[12:15], v[28:31], v[124:127]\n\t"
        "v_and_b32_dpp v8, v21, v4 row_newbcast:4 row_mask:0xf bank_mask:0x3\n\t"
        "v_and_b32_dpp v9, v21, v5 row_newbcast:4 row_mask:0xf bank_mask:0x3\n\t"
        "v_and_b32_dpp v10, v21, v6 row_newbcast:4 row_mask:0xf bank_mask:0x3\n\t"
        "v_and_b32_dpp v11, v21, v7 row_newbcast:4 row_mask:0xf bank_mask:0x3\n\t"
        "v_and_b32_dpp v12, v21, v4 row_newbcast:12 row_mask:0xf bank_mask:0xc\n\t"
        "v_and_b32_dpp v13, v21, v5 row_newbcast:12 row_mask:0xf bank_mask:0xc\n\t"
        "v_and_b32_dpp v14, v21, v6 row_newbcast:12 row_mask:0xf bank_mask:0xc\n\t"
        "v_and_b32_dpp v15, v21, v7 row_newbcast:12 row_mask:0xf bank_mask:0xc\n\t"
        "v_mfma_f32_16x16x32_bf16 v[24:27], v[8:11], v[32:35], 0\n\t"
        "s_nop 0\n\t"
        "v_mfma_f32_16x16x32_bf16 v[24:27], v[12:15], v[36:39], v[24:27]\n\t"
        "v_cvt_pk_bf16_f32 v124, v124, v125\n\t"
        "v_cvt_pk_bf16_f32 v125, v126, v127\n\t"
        "ds_write_b64 v16, v[124:125] offset:44288\n\t"
        "v_and_b32_dpp v8, v21, v4 row_newbcast:1 row_mask:0xf bank_mask:0x3\n\t"
        "v_and_b32_dpp v9, v21, v5 row_newbcast:1 row_mask:0xf bank_mask:0x3\n\t"
        "v_and_b32_dpp v10, v21, v6 row_newbcast:1 row_mask:0xf bank_mask:0x3\n\t"
        "v_and_b32_dpp v11, v21, v7 row_newbcast:1 row_mask:0xf bank_mask:0x3\n\t"
        "v_and_b32_dpp v12, v21, v4 row_newbcast:9 row_mask:0xf bank_mask:0xc\n\t"
        "v_and_b32_dpp v13, v21, v5 row_newbcast:9 row_mask:0xf bank_mask:0xc\n\t"
        "v_and_b32_dpp v14, v21, v6 row_newbcast:9 row_mask:0xf bank_mask:0xc\n\t"
        "v_and_b32_dpp v15, v21, v7 row_newbcast:9 row_mask:0xf bank_mask:0xc\n\t"
        "v_mfma_f32_16x16x32_bf16 v[32:35], v[8:11], v[40:43], 0\n\t"
        "s_nop 0\n\t"
        "v_mfma_f32_16x16x32_bf16 v[32:35], v[12:15], v[44:47], v[32:35]\n\t"
        "v_cvt_pk_bf16_f32 v24, v24, v25\n\t"
        "v_cvt_pk_bf16_f32 v25, v26, v27\n\t"
        "ds_write_b64 v17, v[24:25] offset:44288\n\t"
        "v_and_b32_dpp v8, v21, v4 row_newbcast:5 row_mask:0xf bank_mask:0x3\n\t"
        "v_and_b32_dpp v9, v21, v5 row_newbcast:5 row_mask:0xf bank_mask:0x3\n\t"
        "v_and_b32_dpp v10, v21, v6 row_newbcast:5 row_mask:0xf bank_mask:0x3\n\t"
        "v_and_b32_dpp v11, v21, v7 row_newbcast:5 row_mask:0xf bank_mask:0x3\n\t"
        "v_and_b32_dpp v12, v21, v4 row_newbcast:13 row_mask:0xf bank_mask:0xc\n\t"
        "v_and_b32_dpp v13, v21, v5 row_newbcast:13 row_mask:0xf bank_mask:0xc\n\t"
        "v_and_b32_dpp v14, v21, v6 row_newbcast:13 row_mask:0xf bank_mask:0xc\n\t"
        "v_and_b32_dpp v15, v21, v7 row_newbcast:13 row_mask:0xf bank_mask:0xc\n\t"
        "v_mfma_f32_16x16x32_bf16 v[40:43], v[8:11], v[48:51], 0\n\t"
        "s_nop 0\n\t"
        "v_mfma_f32_16x16x32_bf16 v[40:43], v[12:15], v[52:55], v[40:43]\n\t"
        "v_cvt_pk_bf16_f32 v32, v32, v33\n\t"
        "v_cvt_pk_bf16_f32 v33, v34, v35\n\t"
        "ds_write_b64 v18, v[32:33] offset:44288\n\t"
        "v_and_b32_dpp v8, v21, v4 row_newbcast:2 row_mask:0xf bank_mask:0x3\n\t"
        "v_and_b32_dpp v9, v21, v5 row_newbcast:2 row_mask:0xf bank_mask:0x3\n\t"
        "v_and_b32_dpp v10, v21, v6 row_newbcast:2 row_mask:0xf bank_mask:0x3\n\t"
        "v_and_b32_dpp v11, v21, v7 row_newbcast:2 row_mask:0xf bank_mask:0x3\n\t"
        "v_and_b32_dpp v12, v21, v4 row_newbcast:10 row_mask:0xf bank_mask:0xc\n\t"
        "v_and_b32_dpp v13, v21, v5 row_newbcast:10 row_mask:0xf bank_mask:0xc\n\t"
        "v_and_b32_dpp v14, v21, v6 row_newbcast:10 row_mask:0xf bank_mask:0xc\n\t"
        "v_and_b32_dpp v15, v21, v7 row_newbcast:10 row_mask:0xf bank_mask:0xc\n\t"
        "v_mfma_f32_16x16x32_bf16 v[48:51], v[8:11], v[56:59], 0\n\t"
        "s_nop 0\n\t"
        "v_mfma_f32_16x16x32_bf16 v[48:51], v[12:15], v[60:63], v[48:51]\n\t"
        "v_cvt_pk_bf16_f32 v40, v40, v41\n\t"
        "v_cvt_pk_bf16_f32 v41, v42, v43\n\t"
        "ds_write_b64 v19, v[40:41] offset:44288\n\t"
        "s_cmp_lt_u32 s69, 4\n\t"
        "s_cselect_b32 s78, 16, 1\n\t"
        "v_mov_b32 v0, s78\n\t"
        "s_nop 1\n\t"
        "v_cvt_pk_bf16_f32 v48, v48, v49\n\t"
        "v_cvt_pk_bf16_f32 v49, v50, v51\n\t"
        "ds_write_b64 v20, v[48:49] offset:44288\n\t"
        "s_mov_b64 exec, 1\n\t"
        "ds_add_u32 v2, v0 offset:40\n\t"
        "s_mov_b64 exec, -1\n"
        ".L7_na:\n\t"
        "ds_read_b64_tr_b16 v[124:125], v23 offset:12296\n\t"
        "ds_read_b64_tr_b16 v[126:127], v23 offset:12808\n\t"
        "ds_read_b128 v[4:7], v22\n\t"
        "s_lshl3_add_u32 s42, %3, s81\n\t"
        "s_mul_i32 s42, s42, 0xc00\n\t"
        "s_add_u32 s42, s42, 3145728\n\t"
        "s_xor_b32 s78, %3, 1\n\t"
        "s_lshl3_add_u32 s43, s78, s81\n\t"
        "s_mul_i32 s43, s43, 0xc00\n\t"
        "s_add_u32 s43, s43, 3145728\n\t"
        "s_lshl_b32 s50, s81, 6\n\t"
        "s_lshl_b32 s51, s69, 10\n\t"
        "s_add_u32 s51, s51, 134656\n\t"
        "s_mov_b32 s49, 0x10001\n\t"
        "s_mov_b32 s48, 0x07060302\n\t"
        "s_mov_b32 s55, 142848\n"
        ".L7_w15:\n\t"
        "ds_read_b32 v0, v2 offset:40\n\t"
        "s_waitcnt lgkmcnt(0)\n\t"
        "v_cmp_gt_u16 vcc, 64, v0\n\t"
        "s_cbranch_vccz .L7_d16\n\t"
        "s_setprio 0\n\t"
        "s_sleep 1\n\t"
        "s_sub_i32 s83, s83, 1\n\t"
        "s_cmp_gt_i32 s83, 0\n\t"
        "s_cbranch_scc1 .L7_w15\n"
        ".L7_d16:\n\t"
        "s_setprio 2\n\t"
        "ds_read_b128 v[8:11], v4 offset:44288\n\t"
        "ds_read_b128 v[12:15], v5 offset:44288\n\t"
        "ds_read_b128 v[16:19], v6 offset:44288\n\t"
        "ds_read_b128 v[20:23], v7 offset:44288\n\t"
        "ds_read_b128 v[48:51], v4 offset:48384\n\t"
        "ds_read_b128 v[52:55], v5 offset:48384\n\t"
        "ds_read_b128 v[56:59], v6 offset:48384\n\t"
        "ds_read_b128 v[60:63], v7 offset:48384\n\t"
        "s_waitcnt lgkmcnt(7)\n\t"
        "v_mfma_f32_16x16x32_bf16 v[24:27], v[64:67], v[8:11], 0\n\t"
        "v_mfma_f32_16x16x32_bf16 v[28:31], v[68:71], v[8:11], 0\n\t"
        "s_waitcnt lgkmcnt(6)\n\t"
        "v_mfma_f32_16x16x32_bf16 v[24:27], v[72:75], v[12:15], v[24:27]\n\t"
        "v_mfma_f32_16x16x32_bf16 v[28:31], v[76:79], v[12:15], v[28:31]\n\t"
        "s_waitcnt lgkmcnt(5)\n\t"
        "v_mfma_f32_16x16x32_bf16 v[24:27], v[80:83], v[16:19], v[24:27]\n\t"
        "v_mfma_f32_16x16x32_bf16 v[28:31], v[84:87], v[16:19], v[28:31]\n\t"
        "s_waitcnt lgkmcnt(4)\n\t"
        "v_mfma_f32_16x16x32_bf16 v[24:27], v[88:91], v[20:23], v[24:27]\n\t"
        "v_mfma_f32_16x16x32_bf16 v[28:31], v[92:95], v[20:23], v[28:31]\n\t"
        "ds_read_b128 v[8:11], v4 offset:52480\n\t"
        "ds_read_b128 v[12:15], v5 offset:52480\n\t"
        "ds_read_b128 v[16:19], v6 offset:52480\n\t"
        "ds_read_b128 v[20:23], v7 offset:52480\n\t"
        "s_waitcnt lgkmcnt(7)\n\t"
        "v_mfma_f32_16x16x32_bf16 v[32:35], v[64:67], v[48:51], 0\n\t"
        "v_mfma_f32_16x16x32_bf16 v[36:39], v[68:71], v[48:51], 0\n\t"
        "s_waitcnt lgkmcnt(6)\n\t"
        "v_mfma_f32_16x16x32_bf16 v[32:35], v[72:75], v[52:55], v[32:35]\n\t"
        "v_mfma_f32_16x16x32_bf16 v[36:39], v[76:79], v[52:55], v[36:39]\n\t"
        "s_waitcnt lgkmcnt(5)\n\t"
        "v_mfma_f32_16x16x32_bf16 v[32:35], v[80:83], v[56:59], v[32:35]\n\t"
        "v_mfma_f32_16x16x32_bf16 v[36:39], v[84:87], v[56:59], v[36:39]\n\t"
        "s_waitcnt lgkmcnt(4)\n\t"
        "v_mfma_f32_16x16x32_bf16 v[32:35], v[88:91], v[60:63], v[32:35]\n\t"
        "v_mfma_f32_16x16x32_bf16 v[36:39], v[92:95], v[60:63], v[36:39]\n\t"
        "ds_read_b32 v0, v2 offset:52\n\t"
        "ds_read_b128 v[48:51], v4 offset:21760\n\t"
        "ds_read_b128 v[52:55], v5 offset:21760\n\t"
        "ds_read_b128 v[56:59], v6 offset:21760\n\t"
        "ds_read_b128 v[60:63], v7 offset:21760\n\t"
        "s_waitcnt lgkmcnt(8)\n\t"
        "v_mfma_f32_16x16x32_bf16 v[40:43], v[64:67], v[8:11], 0\n\t"
        "v_mfma_f32_16x16x32_bf16 v[44:47], v[68:71], v[8:11], 0\n\t"
        "s_waitcnt lgkmcnt(7)\n\t"
        "v_mfma_f32_16x16x32_bf16 v[40:43], v[72:75], v[12:15], v[40:43]\n\t"
        "v_mfma_f32_16x16x32_bf16 v[44:47], v[76:79], v[12:15], v[44:47]\n\t"
        "s_waitcnt lgkmcnt(6)\n\t"
        "v_mfma_f32_16x16x32_bf16 v[40:43], v[80:83], v[16:19], v[40:43]\n\t"
        "v_mfma_f32_16x16x32_bf16 v[44:47], v[84:87], v[16:19], v[44:47]\n\t"
        "s_waitcnt lgkmcnt(5)\n\t"
        "v_mfma_f32_16x16x32_bf16 v[40:43], v[88:91], v[20:23], v[40:43]\n\t"
        "v_mfma_f32_16x16x32_bf16 v[44:47], v[92:95], v[20:23], v[44:47]\n\t"
        "ds_read_b128 v[8:11], v4 offset:25856\n\t"
        "ds_read_b128 v[12:15], v5 offset:25856\n\t"
        "ds_read_b128 v[16:19], v6 offset:25856\n\t"
        "ds_read_b128 v[20:23], v7 offset:25856\n\t"
        "s_waitcnt lgkmcnt(8)\n\t"
        "v_cmp_gt_u16 vcc, 6, v0\n\t"
        "s_cbranch_vccz .L7_t1\n"
        ".L7_w17:\n\t"
        "ds_read_b32 v0, v2 offset:52\n\t"
        "s_waitcnt lgkmcnt(0)\n\t"
        "v_cmp_gt_u16 vcc, 6, v0\n\t"
        "s_cbranch_vccz .L7_d18\n\t"
        "s_setprio 0\n\t"
        "s_sleep 2\n\t"
        "s_sub_i32 s83, s83, 1\n\t"
        "s_cmp_gt_i32 s83, 0\n\t"
        "s_cbranch_scc1 .L7_w17\n"
        ".L7_d18:\n\t"
        "s_setprio 2\n\t"
        "ds_read_b128 v[48:51], v4 offset:21760\n\t"
        "ds_read_b128 v[52:55], v5 offset:21760\n\t"
        "ds_read_b128 v[56:59], v6 offset:21760\n\t"
        "ds_read_b128 v[60:63], v7 offset:21760\n\t"
        "ds_read_b128 v[8:11], v4 offset:25856\n\t"
        "ds_read_b128 v[12:15], v5 offset:25856\n\t"
        "ds_read_b128 v[16:19], v6 offset:25856\n\t"
        "ds_read_b128 v[20:23], v7 offset:25856\n"
        ".L7_t1:\n\t"
        "v_lshl_add_u32 v3, v1, 4, s55\n\t"
        "s_waitcnt lgkmcnt(7)\n\t"
        "v_mfma_f32_16x16x32_bf16 v[24:27], v[96:99], v[48:51], v[24:27]\n\t"
        "v_mfma_f32_16x16x32_bf16 v[28:31], v[100:103], v[48:51], v[28:31]\n\t"
        "s_waitcnt lgkmcnt(6)\n\t"
        "v_mfma_f32_16x16x32_bf16 v[24:27], v[104:107], v[52:55], v[24:27]\n\t"
        "v_mfma_f32_16x16x32_bf16 v[28:31], v[108:111], v[52:55], v[28:31]\n\t"
        "s_waitcnt lgkmcnt(5)\n\t"
        "v_mfma_f32_16x16x32_bf16 v[24:27], v[112:115], v[56:59], v[24:27]\n\t"
        "v_mfma_f32_16x16x32_bf16 v[28:31], v[116:119], v[56:59], v[28:31]\n\t"
        "s_waitcnt lgkmcnt(4)\n\t"
        "v_mfma_f32_16x16x32_bf16 v[24:27], v[120:123], v[60:63], v[24:27]\n\t"
        "v_mfma_f32_16x16x32_bf16 v[28:31], v[124:127], v[60:63], v[28:31]\n\t"
        "ds_read_b128 v[60:63], v3 offset:1024\n\t"
        "s_waitcnt lgkmcnt(4)\n\t"
        "v_mfma_f32_16x16x32_bf16 v[32:35], v[96:99], v[8:11], v[32:35]\n\t"
        "v_mfma_f32_16x16x32_bf16 v[36:39], v[100:103], v[8:11], v[36:39]\n\t"
        "s_waitcnt lgkmcnt(3)\n\t"
        "v_mfma_f32_16x16x32_bf16 v[32:35], v[104:107], v[12:15], v[32:35]\n\t"
        "v_mfma_f32_16x16x32_bf16 v[36:39], v[108:111], v[12:15], v[36:39]\n\t"
        "s_waitcnt lgkmcnt(2)\n\t"
        "v_mfma_f32_16x16x32_bf16 v[32:35], v[112:115], v[16:19], v[32:35]\n\t"
        "v_mfma_f32_16x16x32_bf16 v[36:39], v[116:119], v[16:19], v[36:39]\n\t"
        "s_waitcnt lgkmcnt(1)\n\t"
        "v_mfma_f32_16x16x32_bf16 v[32:35], v[120:123], v[20:23], v[32:35]\n\t"
        "v_mfma_f32_16x16x32_bf16 v[36:39], v[124:127], v[20:23], v[36:39]\n\t"
        "ds_read_b32 v0, v2 offset:56\n\t"
        "ds_read_b128 v[8:11], v4 offset:29952\n\t"
        "ds_read_b128 v[12:15], v5 offset:29952\n\t"
        "ds_read_b128 v[16:19], v6 offset:29952\n\t"
        "ds_read_b128 v[20:23], v7 offset:29952\n\t"
        "s_waitcnt lgkmcnt(5)\n\t"
        "v_add_u32 v24, 0x10000, v24\n\t"
        "v_add_u32 v25, 0x10000, v25\n\t"
        "v_add_u32 v26, 0x10000, v26\n\t"
        "v_add_u32 v27, 0x10000, v27\n\t"
        "v_perm_b32 v48, v25, v24, s48\n\t"
        "v_perm_b32 v49, v27, v26, s48\n\t"
        "v_cvt_pk_bf16_f32 v50, v28, v29\n\t"
        "v_cvt_pk_bf16_f32 v51, v30, v31\n\t"
        "v_bfi_b32 v48, s49, v60, v48\n\t"
        "v_bfi_b32 v49, s49, v61, v49\n\t"
        "v_add_u32 v32, 0x10000, v32\n\t"
        "v_add_u32 v33, 0x10000, v33\n\t"
        "v_add_u32 v34, 0x10000, v34\n\t"
        "v_add_u32 v35, 0x10000, v35\n\t"
        "v_perm_b32 v52, v33, v32, s48\n\t"
        "v_perm_b32 v53, v35, v34, s48\n\t"
        "v_cvt_pk_bf16_f32 v54, v36, v37\n\t"
        "v_cvt_pk_bf16_f32 v55, v38, v39\n\t"
        "v_bfi_b32 v52, s49, v60, v52\n\t"
        "v_bfi_b32 v53, s49, v61, v53\n\t"
        "s_mov_b32 exec_lo, 0xff00ff00\n\t"
        "s_mov_b32 exec_hi, 0xff00ff00\n\t"
        "buffer_store_dwordx4 v[48:51], v62, s[20:23], s42 offen offset:0 sc1\n\t"
        "s_mov_b64 exec, -1\n\t"
        "buffer_store_dwordx4 v[52:55], v62, s[20:23], s42 offen offset:1024 sc1\n\t"
        "s_waitcnt lgkmcnt(4)\n\t"
        "v_cmp_gt_u16 vcc, 4, v0\n\t"
        "s_cbranch_vccz .L7_t2\n"
        ".L7_w19:\n\t"
        "ds_read_b32 v0, v2 offset:56\n\t"
        "s_waitcnt lgkmcnt(0)\n\t"
        "v_cmp_gt_u16 vcc, 3, v0\n\t"
        "s_cbranch_vccz .L7_d20\n\t"
        "s_setprio 0\n\t"
        "s_sleep 2\n\t"
        "s_sub_i32 s83, s83, 1\n\t"
        "s_cmp_gt_i32 s83, 0\n\t"
        "s_cbranch_scc1 .L7_w19\n"
        ".L7_d20:\n\t"
        "s_setprio 2\n"
        ".L7_w21:\n\t"
        "ds_read_b32 v0, v2 offset:56\n\t"
        "ds_read_b128 v[8:11], v4 offset:29952\n\t"
        "ds_read_b128 v[12:15], v5 offset:29952\n\t"
        "ds_read_b128 v[16:19], v6 offset:29952\n\t"
        "ds_read_b128 v[20:23], v7 offset:29952\n\t"
        "s_waitcnt lgkmcnt(4)\n\t"
        "v_cmp_gt_u16 vcc, 4, v0\n\t"
        "s_cbranch_vccz .L7_d22\n\t"
        "s_waitcnt lgkmcnt(0)\n\t"
        "s_setprio 0\n\t"
        "s_sleep 1\n\t"
        "s_sub_i32 s83, s83, 1\n\t"
        "s_cmp_gt_i32 s83, 0\n\t"
        "s_cbranch_scc1 .L7_w21\n"
        ".L7_d22:\n\t"
        "s_setprio 2\n"
        ".L7_t2:\n\t"
        "s_waitcnt lgkmcnt(3)\n\t"
        "v_mfma_f32_16x16x32_bf16 v[40:43], v[96:99], v[8:11], v[40:43]\n\t"
        "v_mfma_f32_16x16x32_bf16 v[44:47], v[100:103], v[8:11], v[44:47]\n\t"
        "s_waitcnt lgkmcnt(2)\n\t"
        "v_mfma_f32_16x16x32_bf16 v[40:43], v[104:107], v[12:15], v[40:43]\n\t"
        "v_mfma_f32_16x16x32_bf16 v[44:47], v[108:111], v[12:15], v[44:47]\n\t"
        "s_waitcnt lgkmcnt(1)\n\t"
        "v_mfma_f32_16x16x32_bf16 v[40:43], v[112:115], v[16:19], v[40:43]\n\t"
        "v_mfma_f32_16x16x32_bf16 v[44:47], v[116:119], v[16:19], v[44:47]\n\t"
        "s_waitcnt lgkmcnt(0)\n\t"
        "v_mfma_f32_16x16x32_bf16 v[40:43], v[120:123], v[20:23], v[40:43]\n\t"
        "v_mfma_f32_16x16x32_bf16 v[44:47], v[124:127], v[20:23], v[44:47]\n\t"
        "s_nop 6\n\t"
        "v_add_u32 v40, 0x10000, v40\n\t"
        "v_add_u32 v41, 0x10000, v41\n\t"
        "v_add_u32 v42, 0x10000, v42\n\t"
        "v_add_u32 v43, 0x10000, v43\n\t"
        "v_perm_b32 v56, v41, v40, s48\n\t"
        "v_perm_b32 v57, v43, v42, s48\n\t"
        "v_cvt_pk_bf16_f32 v58, v44, v45\n\t"
        "v_cvt_pk_bf16_f32 v59, v46, v47\n\t"
        "v_bfi_b32 v56, s49, v60, v56\n\t"
        "v_bfi_b32 v57, s49, v61, v57\n\t"
        "buffer_store_dwordx4 v[56:59], v62, s[20:23], s42 offen offset:2048 sc1\n\t"
        "v_mov_b32 v0, 1\n\t"
        "s_mov_b64 exec, 1\n\t"
        "ds_add_u32 v2, v0 offset:96\n\t"
        "s_mov_b64 exec, -1\n\t"
        "s_waitcnt lgkmcnt(0)\n"
        ".L7_w23:\n\t"
        "ds_read_b32 v0, v2 offset:40\n\t"
        "ds_read_b128 v[8:11], v4 offset:56576\n\t"
        "ds_read_b128 v[12:15], v5 offset:56576\n\t"
        "ds_read_b128 v[16:19], v6 offset:56576\n\t"
        "ds_read_b128 v[20:23], v7 offset:56576\n\t"
        "s_waitcnt lgkmcnt(4)\n\t"
        "v_cmp_gt_u16 vcc, 68, v0\n\t"
        "s_cbranch_vccz .L7_d24\n\t"
        "s_waitcnt lgkmcnt(0)\n\t"
        "s_setprio 0\n\t"
        "s_sleep 1\n\t"
        "s_sub_i32 s83, s83, 1\n\t"
        "s_cmp_gt_i32 s83, 0\n\t"
        "s_cbranch_scc1 .L7_w23\n"
        ".L7_d24:\n\t"
        "s_setprio 2\n\t"
        "s_waitcnt lgkmcnt(3)\n\t"
        "v_mfma_f32_16x16x32_bf16 v[24:27], v[64:67], v[8:11], 0\n\t"
        "v_mfma_f32_16x16x32_bf16 v[28:31], v[68:71], v[8:11], 0\n\t"
        "s_waitcnt lgkmcnt(2)\n\t"
        "v_mfma_f32_16x16x32_bf16 v[24:27], v[72:75], v[12:15], v[24:27]\n\t"
        "v_mfma_f32_16x16x32_bf16 v[28:31], v[76:79], v[12:15], v[28:31]\n\t"
        "s_waitcnt lgkmcnt(1)\n\t"
        "v_mfma_f32_16x16x32_bf16 v[24:27], v[80:83], v[16:19], v[24:27]\n\t"
        "v_mfma_f32_16x16x32_bf16 v[28:31], v[84:87], v[16:19], v[28:31]\n\t"
        "s_waitcnt lgkmcnt(0)\n\t"
        "v_mfma_f32_16x16x32_bf16 v[24:27], v[88:91], v[20:23], v[24:27]\n\t"
        "v_mfma_f32_16x16x32_bf16 v[28:31], v[92:95], v[20:23], v[28:31]\n\t"
        "ds_read_b128 v[8:11], v4 offset:60672\n\t"
        "ds_read_b128 v[12:15], v5 offset:60672\n\t"
        "ds_read_b128 v[16:19], v6 offset:60672\n\t"
        "ds_read_b128 v[20:23], v7 offset:60672\n\t"
        "s_waitcnt lgkmcnt(3)\n\t"
        "v_mfma_f32_16x16x32_bf16 v[32:35], v[64:67], v[8:11], 0\n\t"
        "v_mfma_f32_16x16x32_bf16 v[36:39], v[68:71], v[8:11], 0\n\t"
        "s_waitcnt lgkmcnt(2)\n\t"
        "v_mfma_f32_16x16x32_bf16 v[32:35], v[72:75], v[12:15], v[32:35]\n\t"
        "v_mfma_f32_16x16x32_bf16 v[36:39], v[76:79], v[12:15], v[36:39]\n\t"
        "s_waitcnt lgkmcnt(1)\n\t"
        "v_mfma_f32_16x16x32_bf16 v[32:35], v[80:83], v[16:19], v[32:35]\n\t"
        "v_mfma_f32_16x16x32_bf16 v[36:39], v[84:87], v[16:19], v[36:39]\n\t"
        "s_waitcnt lgkmcnt(0)\n\t"
        "v_mfma_f32_16x16x32_bf16 v[32:35], v[88:91], v[20:23], v[32:35]\n\t"
        "v_mfma_f32_16x16x32_bf16 v[36:39], v[92:95], v[20:23], v[36:39]\n\t"
        "ds_read_b128 v[8:11], v4 offset:64768\n\t"
        "ds_read_b128 v[12:15], v5 offset:64768\n\t"
        "ds_read_b128 v[16:19], v6 offset:64768\n\t"
        "ds_read_b128 v[20:23], v7 offset:64768\n\t"
        "s_waitcnt lgkmcnt(3)\n\t"
        "v_mfma_f32_16x16x32_bf16 v[40:43], v[64:67], v[8:11], 0\n\t"
        "v_mfma_f32_16x16x32_bf16 v[44:47], v[68:71], v[8:11], 0\n\t"
        "s_waitcnt lgkmcnt(2)\n\t"
        "v_mfma_f32_16x16x32_bf16 v[40:43], v[72:75], v[12:15], v[40:43]\n\t"
        "v_mfma_f32_16x16x32_bf16 v[44:47], v[76:79], v[12:15], v[44:47]\n\t"
        "s_waitcnt lgkmcnt(1)\n\t"
        "v_mfma_f32_16x16x32_bf16 v[40:43], v[80:83], v[16:19], v[40:43]\n\t"
        "v_mfma_f32_16x16x32_bf16 v[44:47], v[84:87], v[16:19], v[44:47]\n\t"
        "s_waitcnt lgkmcnt(0)\n\t"
        "v_mfma_f32_16x16x32_bf16 v[40:43], v[88:91], v[20:23], v[40:43]\n\t"
        "v_mfma_f32_16x16x32_bf16 v[44:47], v[92:95], v[20:23], v[44:47]\n"
        ".L7_t4:\n\t"
        "ds_read_b128 v[68:71], v3 offset:2048\n\t"
        "ds_read_b128 v[74:77], v3 offset:3072\n"
        ".L7_w25:\n\t"
        "ds_read_b32 v0, v2 offset:60\n\t"
        "s_waitcnt lgkmcnt(0)\n\t"
        "v_cmp_gt_u16 vcc, 2, v0\n\t"
        "s_cbranch_vccz .L7_d26\n\t"
        "s_setprio 0\n\t"
        "s_sleep 2\n\t"
        "s_sub_i32 s83, s83, 1\n\t"
        "s_cmp_gt_i32 s83, 0\n\t"
        "s_cbranch_scc1 .L7_w25\n"
        ".L7_d26:\n\t"
        "s_setprio 2\n\t"
        "ds_read_b128 v[8:11], v4 offset:34048\n\t"
        "ds_read_b128 v[12:15], v5 offset:34048\n\t"
        "ds_read_b128 v[16:19], v6 offset:34048\n\t"
        "ds_read_b128 v[20:23], v7 offset:34048\n\t"
        "v_add_u32 v71, s51, v71\n\t"
        "v_add_u32 v72, s51, v74\n\t"
        "v_mov_b64 v[74:75], 0\n\t"
        "v_mov_b64 v[78:79], 0\n\t"
        "s_waitcnt lgkmcnt(3)\n\t"
        "v_mfma_f32_16x16x32_bf16 v[24:27], v[96:99], v[8:11], v[24:27]\n\t"
        "v_mfma_f32_16x16x32_bf16 v[28:31], v[100:103], v[8:11], v[28:31]\n\t"
        "s_waitcnt lgkmcnt(2)\n\t"
        "v_mfma_f32_16x16x32_bf16 v[24:27], v[104:107], v[12:15], v[24:27]\n\t"
        "v_mfma_f32_16x16x32_bf16 v[28:31], v[108:111], v[12:15], v[28:31]\n\t"
        "s_waitcnt lgkmcnt(1)\n\t"
        "v_mfma_f32_16x16x32_bf16 v[24:27], v[112:115], v[16:19], v[24:27]\n\t"
        "v_mfma_f32_16x16x32_bf16 v[28:31], v[116:119], v[16:19], v[28:31]\n\t"
        "s_waitcnt lgkmcnt(0)\n\t"
        "v_mfma_f32_16x16x32_bf16 v[24:27], v[120:123], v[20:23], v[24:27]\n\t"
        "v_mfma_f32_16x16x32_bf16 v[28:31], v[124:127], v[20:23], v[28:31]\n"
        ".L7_w27:\n\t"
        "ds_read_b32 v0, v2 offset:100\n\t"
        "s_waitcnt lgkmcnt(0)\n\t"
        "v_cmp_gt_u16 vcc, 1, v0\n\t"
        "s_cbranch_vccz .L7_d28\n\t"
        "s_setprio 0\n\t"
        "s_sleep 1\n\t"
        "s_sub_i32 s83, s83, 1\n\t"
        "s_cmp_gt_i32 s83, 0\n\t"
        "s_cbranch_scc1 .L7_w27\n"
        ".L7_d28:\n\t"
        "s_setprio 2\n\t"
        "buffer_load_dwordx4 v[48:51], v63, s[20:23], s43 offen offset:0 sc1\n\t"
        "buffer_load_dwordx4 v[52:55], v62, s[20:23], s43 offen offset:1024 sc1\n\t"
        "buffer_load_dwordx4 v[56:59], v62, s[20:23], s43 offen offset:2048 sc1\n"
        ".L7_w29:\n\t"
        "ds_read_b32 v0, v2 offset:64\n\t"
        "s_waitcnt lgkmcnt(0)\n\t"
        "v_cmp_gt_u16 vcc, 4, v0\n\t"
        "s_cbranch_vccz .L7_d30\n\t"
        "s_setprio 0\n\t"
        "s_sleep 2\n\t"
        "s_sub_i32 s83, s83, 1\n\t"
        "s_cmp_gt_i32 s83, 0\n\t"
        "s_cbranch_scc1 .L7_w29\n"
        ".L7_d30:\n\t"
        "s_setprio 2\n\t"
        "ds_read_b128 v[8:11], v4 offset:38144\n\t"
        "ds_read_b128 v[12:15], v5 offset:38144\n\t"
        "ds_read_b128 v[16:19], v6 offset:38144\n\t"
        "ds_read_b128 v[20:23], v7 offset:38144\n\t"
        "s_waitcnt lgkmcnt(3)\n\t"
        "v_mfma_f32_16x16x32_bf16 v[32:35], v[96:99], v[8:11], v[32:35]\n\t"
        "v_mfma_f32_16x16x32_bf16 v[36:39], v[100:103], v[8:11], v[36:39]\n\t"
        "s_waitcnt lgkmcnt(2)\n\t"
        "v_mfma_f32_16x16x32_bf16 v[32:35], v[104:107], v[12:15], v[32:35]\n\t"
        "v_mfma_f32_16x16x32_bf16 v[36:39], v[108:111], v[12:15], v[36:39]\n\t"
        "s_waitcnt lgkmcnt(1)\n\t"
        "v_mfma_f32_16x16x32_bf16 v[32:35], v[112:115], v[16:19], v[32:35]\n\t"
        "v_mfma_f32_16x16x32_bf16 v[36:39], v[116:119], v[16:19], v[36:39]\n\t"
        "s_waitcnt lgkmcnt(0)\n\t"
        "v_mfma_f32_16x16x32_bf16 v[32:35], v[120:123], v[20:23], v[32:35]\n\t"
        "v_mfma_f32_16x16x32_bf16 v[36:39], v[124:127], v[20:23], v[36:39]\n\t"
        "s_waitcnt vmcnt(0)\n\t"
        "s_branch .L7_s32\n"
        ".L7_r31:\n\t"
        "buffer_load_dwordx4 v[48:51], v63, s[20:23], s43 offen offset:0 sc1\n\t"
        "buffer_load_dwordx4 v[52:55], v62, s[20:23], s43 offen offset:1024 sc1\n\t"
        "buffer_load_dwordx4 v[56:59], v62, s[20:23], s43 offen offset:2048 sc1\n\t"
        "s_waitcnt vmcnt(0)\n"
        ".L7_s32:\n\t"
        "v_xor_b32 v48, v48, v60\n\t"
        "v_xor_b32 v49, v49, v61\n\t"
        "v_xor_b32 v52, v52, v60\n\t"
        "v_xor_b32 v53, v53, v61\n\t"
        "v_xor_b32 v56, v56, v60\n\t"
        "v_xor_b32 v57, v57, v61\n\t"
        "v_or_b32 v0, v48, v49\n\t"
        "v_or3_b32 v0, v0, v52, v53\n\t"
        "v_or3_b32 v0, v0, v56, v57\n\t"
        "v_and_b32 v0, s49, v0\n\t"
        "v_cmp_ne_u32 vcc, 0, v0\n\t"
        "s_cbranch_vccz .L7_t33\n\t"
        "s_sleep 8\n\t"
        "s_sub_i32 s83, s83, 1\n\t"
        "s_cmp_gt_i32 s83, 0\n\t"
        "s_cbranch_scc1 .L7_r31\n"
        ".L7_t33:\n\t"
        "v_mfma_f32_16x16x32_bf16 v[24:27], v[76:79], v[48:51], v[24:27]\n\t"
        "v_mfma_f32_16x16x32_bf16 v[28:31], v[74:77], v[48:51], v[28:31]\n\t"
        "v_mfma_f32_16x16x32_bf16 v[32:35], v[76:79], v[52:55], v[32:35]\n\t"
        "v_mfma_f32_16x16x32_bf16 v[36:39], v[74:77], v[52:55], v[36:39]\n\t"
        "v_mfma_f32_16x16x32_bf16 v[40:43], v[76:79], v[56:59], v[40:43]\n\t"
        "v_mfma_f32_16x16x32_bf16 v[44:47], v[74:77], v[56:59], v[44:47]\n\t"
        "s_nop 2\n\t"
        "v_cvt_pk_bf16_f32 v48, v24, v25\n\t"
        "v_cvt_pk_bf16_f32 v49, v26, v27\n\t"
        "v_cvt_pk_bf16_f32 v50, v28, v29\n\t"
        "v_cvt_pk_bf16_f32 v51, v30, v31\n\t"
        "ds_write_b128 v71, v[48:51]\n\t"
        "ds_read_b128 v[48:51], v72\n\t"
        "v_cvt_pk_bf16_f32 v52, v32, v33\n\t"
        "v_cvt_pk_bf16_f32 v53, v34, v35\n\t"
        "v_cvt_pk_bf16_f32 v54, v36, v37\n\t"
        "v_cvt_pk_bf16_f32 v55, v38, v39\n\t"
        "ds_write_b128 v71, v[52:55]\n\t"
        "ds_read_b128 v[52:55], v72\n\t"
        "s_waitcnt lgkmcnt(0)\n\t"
        "buffer_store_dwordx4 v[48:51], v68, s[60:63], s50 offen sc1\n\t"
        "buffer_store_dwordx4 v[52:55], v69, s[60:63], s50 offen sc1\n"
        ".L7_w34:\n\t"
        "ds_read_b32 v0, v2 offset:68\n\t"
        "s_waitcnt lgkmcnt(0)\n\t"
        "v_cmp_gt_u16 vcc, 3, v0\n\t"
        "s_cbranch_vccz .L7_d35\n\t"
        "s_setprio 0\n\t"
        "s_sleep 1\n\t"
        "s_sub_i32 s83, s83, 1\n\t"
        "s_cmp_gt_i32 s83, 0\n\t"
        "s_cbranch_scc1 .L7_w34\n"
        ".L7_d35:\n\t"
        "s_setprio 2\n"
        ".L7_w36:\n\t"
        "ds_read_b32 v0, v2 offset:68\n\t"
        "ds_read_b128 v[8:11], v4 offset:42240\n\t"
        "ds_read_b128 v[12:15], v5 offset:42240\n\t"
        "ds_read_b128 v[16:19], v6 offset:42240\n\t"
        "ds_read_b128 v[20:23], v7 offset:42240\n\t"
        "s_waitcnt lgkmcnt(4)\n\t"
        "v_cmp_gt_u16 vcc, 4, v0\n\t"
        "s_cbranch_vccz .L7_d37\n\t"
        "s_waitcnt lgkmcnt(0)\n\t"
        "s_setprio 0\n\t"
        "s_sleep 0\n\t"
        "s_sub_i32 s83, s83, 1\n\t"
        "s_cmp_gt_i32 s83, 0\n\t"
        "s_cbranch_scc1 .L7_w36\n"
        ".L7_d37:\n\t"
        "s_setprio 2\n\t"
        "s_waitcnt lgkmcnt(3)\n\t"
        "v_mfma_f32_16x16x32_bf16 v[40:43], v[96:99], v[8:11], v[40:43]\n\t"
        "v_mfma_f32_16x16x32_bf16 v[44:47], v[100:103], v[8:11], v[44:47]\n\t"
        "s_waitcnt lgkmcnt(2)\n\t"
        "v_mfma_f32_16x16x32_bf16 v[40:43], v[104:107], v[12:15], v[40:43]\n\t"
        "v_mfma_f32_16x16x32_bf16 v[44:47], v[108:111], v[12:15], v[44:47]\n\t"
        "s_waitcnt lgkmcnt(1)\n\t"
        "v_mfma_f32_16x16x32_bf16 v[40:43], v[112:115], v[16:19], v[40:43]\n\t"
        "v_mfma_f32_16x16x32_bf16 v[44:47], v[116:119], v[16:19], v[44:47]\n\t"
        "s_waitcnt lgkmcnt(0)\n\t"
        "v_mfma_f32_16x16x32_bf16 v[40:43], v[120:123], v[20:23], v[40:43]\n\t"
        "v_mfma_f32_16x16x32_bf16 v[44:47], v[124:127], v[20:23], v[44:47]\n\t"
        "s_nop 6\n\t"
        "v_cvt_pk_bf16_f32 v56, v40, v41\n\t"
        "v_cvt_pk_bf16_f32 v57, v42, v43\n\t"
        "v_cvt_pk_bf16_f32 v58, v44, v45\n\t"
        "v_cvt_pk_bf16_f32 v59, v46, v47\n\t"
        "ds_write_b128 v71, v[56:59]\n\t"
        "ds_read_b128 v[56:59], v72\n\t"
        "s_waitcnt lgkmcnt(0)\n\t"
        "buffer_store_dwordx4 v[56:59], v70, s[60:63], s50 offen sc1\n\t"
        "s_or_b32 s78, %3, s69\n\t"
        "s_cbranch_scc1 .L7_ng\n\t"
        "v_mov_b32 v0, s68\n\t"
        "buffer_store_dword v0, off, s[20:23], 0 sc1\n"
        ".L7_ng:\n\t"
        "s_endpgm\n"
        ".L7_U2:\n\t"
        "s_barrier\n\t"
        "v_cmp_gt_u32 s[24:25], s46, v4\n\t"
        "v_cmp_gt_u32 s[26:27], s46, v5\n\t"
        "v_cmp_gt_u32 s[28:29], s46, v6\n\t"
        "v_cmp_gt_u32 s[30:31], s46, v7\n\t"
        "v_cmp_gt_u32 s[32:33], s46, v8\n\t"
        "v_mul_u32_u24 v112, 0xa0, v1\n\t"
        "v_add_u32 v112, 68864, v112\n\t"
        "v_add_u32 v113, 10240, v112\n\t"
        "v_lshlrev_b32 v7, 2, v1\n\t"
        "ds_write_b32 v7, v112 offset:1028\n\t"
        "ds_write_b32 v7, v113 offset:1284\n\t"
        "v_lshrrev_b32 v15, 3, v1\n\t"
        "v_mul_u32_u24 v15, 0xa0, v15\n\t"
        "v_add_u32 v15, 68864, v15\n\t"
        "v_and_b32 v4, 7, v1\n\t"
        "v_lshl_add_u32 v5, v4, 4, v15\n\t"
        "v_lshl_add_u32 v15, v4, 2, v15\n\t"
        "v_mov_b32 v13, 0x1000000\n\t"
        "v_mov_b32 v14, 4\n\t"
        "s_lshl_b32 s54, s65, 14\n\t"
        "s_sub_u32 s54, 0, s54\n\t"
        "s_lshl_b32 s48, s71, 14\n\t"
        "s_add_u32 s48, s48, 1048576\n\t"
        "s_add_u32 s49, s48, 4096\n\t"
        "s_add_u32 s50, s48, 8192\n\t"
        "s_add_u32 s51, s48, 12288\n\t"
        "s_mov_b32 s34, 0x0c0c0c0c\n\t"
        "s_mov_b32 s35, s34\n\t"
        "s_lshl_b64 s[36:37], s[34:35], 2\n\t"
        "s_lshl_b64 s[38:39], s[34:35], 4\n\t"
        "v_add_u32 v97, 1280, v15\n\t"
        "v_add_u32 v98, 2560, v15\n\t"
        "v_add_u32 v99, 3840, v15\n\t"
        "v_add_u32 v100, 5120, v15\n\t"
        "v_add_u32 v101, 6400, v15\n\t"
        "v_add_u32 v102, 7680, v15\n\t"
        "v_add_u32 v103, 8960, v15\n\t"
        "v_add_u32 v104, 10240, v15\n\t"
        "v_add_u32 v105, 11520, v15\n\t"
        "v_add_u32 v106, 12800, v15\n\t"
        "v_add_u32 v107, 14080, v15\n\t"
        "v_add_u32 v108, 15360, v15\n\t"
        "v_add_u32 v109, 16640, v15\n\t"
        "v_add_u32 v110, 17920, v15\n\t"
        "v_add_u32 v111, 19200, v15\n\t"
        "v_add_u32 v112, 0x80, v112\n\t"
        "v_add_u32 v113, 0x80, v113\n\t"
        "v_add_u32 v114, s80, v1\n\t"
        "v_add_u32 v115, 64, v114\n\t"
        "s_waitcnt lgkmcnt(0)\n\t"
        "s_add_u32 s68, s68, 1\n\t"
        "s_lshl_b32 s78, s68, 24\n\t"
        "v_mov_b32 v8, s78\n\t"
        "s_xor_b32 s78, s78, 0x5a000000\n\t"
        "v_mov_b32 v9, s78\n\t"
        "v_cndmask_b32 v8, v8, v9, s[34:35]\n\t"
        "s_mul_i32 s78, s68, 0x01010000\n\t"
        "v_mov_b32 v9, s78\n\t"
        "v_cndmask_b32 v8, v8, v9, s[36:37]\n\t"
        "s_mul_i32 s55, s68, 0x02000200\n\t"
        "v_mov_b32 v9, s55\n\t"
        "v_cndmask_b32 v8, v8, v9, s[38:39]\n\t"
        "v_and_b32 v8, 0xff000000, v8\n\t"
        "v_or_b32 v8, 0xff0000, v8\n\t"
        "v_mov_b32 v9, v8\n\t"
        "v_mov_b64 v[10:11], v[8:9]\n\t"
        "ds_write_b128 v5, v[8:11] offset:0\n\t"
        "ds_write_b128 v5, v[8:11] offset:1280\n\t"
        "ds_write_b128 v5, v[8:11] offset:2560\n\t"
        "ds_write_b128 v5, v[8:11] offset:3840\n\t"
        "ds_write_b128 v5, v[8:11] offset:5120\n\t"
        "ds_write_b128 v5, v[8:11] offset:6400\n\t"
        "ds_write_b128 v5, v[8:11] offset:7680\n\t"
        "ds_write_b128 v5, v[8:11] offset:8960\n\t"
        "ds_write_b128 v5, v[8:11] offset:10240\n\t"
        "ds_write_b128 v5, v[8:11] offset:11520\n\t"
        "ds_write_b128 v5, v[8:11] offset:12800\n\t"
        "ds_write_b128 v5, v[8:11] offset:14080\n\t"
        "s_waitcnt lgkmcnt(6)\n\t"
        "ds_write_b128 v5, v[8:11] offset:15360\n\t"
        "ds_write_b128 v5, v[8:11] offset:16640\n\t"
        "ds_write_b128 v5, v[8:11] offset:17920\n\t"
        "ds_write_b128 v5, v[8:11] offset:19200\n\t"
        "v_mov_b32 v4, s68\n\t"
        "ds_write_b32 v2, v4 offset:76\n\t"
        "v_cmp_gt_u32 s[36:37], s64, v114\n\t"
        "v_cmp_gt_u32 s[38:39], s64, v115\n\t"
        "s_waitcnt vmcnt(11) lgkmcnt(8)\n\t"
        "s_mov_b64 exec, s[24:25]\n\t"
        "v_mad_u32_u24 v56, v16, s66, v13\n\t"
        "v_mad_u32_u24 v57, v17, s66, v13\n\t"
        "v_mad_u32_u24 v58, v18, s66, v13\n\t"
        "v_mad_u32_u24 v59, v19, s66, v13\n\t"
        "v_lshlrev_b32_sdwa v56, 2, v56 dst_sel:DWORD dst_unused:UNUSED_PAD src0_sel:DWORD src1_sel:BYTE_3\n\t"
        "ds_add_rtn_u32 v76, v56, v14 offset:1024\n\t"
        "v_lshlrev_b32_sdwa v57, 2, v57 dst_sel:DWORD dst_unused:UNUSED_PAD src0_sel:DWORD src1_sel:BYTE_3\n\t"
        "ds_add_rtn_u32 v77, v57, v14 offset:1024\n\t"
        "v_lshlrev_b32_sdwa v58, 2, v58 dst_sel:DWORD dst_unused:UNUSED_PAD src0_sel:DWORD src1_sel:BYTE_3\n\t"
        "ds_add_rtn_u32 v78, v58, v14 offset:1024\n\t"
        "v_lshlrev_b32_sdwa v59, 2, v59 dst_sel:DWORD dst_unused:UNUSED_PAD src0_sel:DWORD src1_sel:BYTE_3\n\t"
        "ds_add_rtn_u32 v79, v59, v14 offset:1024\n\t"
        "s_waitcnt vmcnt(10) lgkmcnt(8)\n\t"
        "s_mov_b64 exec, s[26:27]\n\t"
        "v_mad_u32_u24 v60, v20, s66, v13\n\t"
        "v_mad_u32_u24 v61, v21, s66, v13\n\t"
        "v_mad_u32_u24 v62, v22, s66, v13\n\t"
        "v_mad_u32_u24 v63, v23, s66, v13\n\t"
        "v_lshlrev_b32_sdwa v60, 2, v60 dst_sel:DWORD dst_unused:UNUSED_PAD src0_sel:DWORD src1_sel:BYTE_3\n\t"
        "ds_add_rtn_u32 v80, v60, v14 offset:1024\n\t"
        "v_lshlrev_b32_sdwa v61, 2, v61 dst_sel:DWORD dst_unused:UNUSED_PAD src0_sel:DWORD src1_sel:BYTE_3\n\t"
        "ds_add_rtn_u32 v81, v61, v14 offset:1024\n\t"
        "v_lshlrev_b32_sdwa v62, 2, v62 dst_sel:DWORD dst_unused:UNUSED_PAD src0_sel:DWORD src1_sel:BYTE_3\n\t"
        "ds_add_rtn_u32 v82, v62, v14 offset:1024\n\t"
        "v_lshlrev_b32_sdwa v63, 2, v63 dst_sel:DWORD dst_unused:UNUSED_PAD src0_sel:DWORD src1_sel:BYTE_3\n\t"
        "ds_add_rtn_u32 v83, v63, v14 offset:1024\n\t"
        "s_waitcnt vmcnt(9) lgkmcnt(8)\n\t"
        "s_mov_b64 exec, s[28:29]\n\t"
        "v_mad_u32_u24 v64, v24, s66, v13\n\t"
        "v_mad_u32_u24 v65, v25, s66, v13\n\t"
        "v_mad_u32_u24 v66, v26, s66, v13\n\t"
        "v_mad_u32_u24 v67, v27, s66, v13\n\t"
        "v_lshlrev_b32_sdwa v64, 2, v64 dst_sel:DWORD dst_unused:UNUSED_PAD src0_sel:DWORD src1_sel:BYTE_3\n\t"
        "ds_add_rtn_u32 v84, v64, v14 offset:1024\n\t"
        "v_lshlrev_b32_sdwa v65, 2, v65 dst_sel:DWORD dst_unused:UNUSED_PAD src0_sel:DWORD src1_sel:BYTE_3\n\t"
        "ds_add_rtn_u32 v85, v65, v14 offset:1024\n\t"
        "v_lshlrev_b32_sdwa v66, 2, v66 dst_sel:DWORD dst_unused:UNUSED_PAD src0_sel:DWORD src1_sel:BYTE_3\n\t"
        "ds_add_rtn_u32 v86, v66, v14 offset:1024\n\t"
        "v_lshlrev_b32_sdwa v67, 2, v67 dst_sel:DWORD dst_unused:UNUSED_PAD src0_sel:DWORD src1_sel:BYTE_3\n\t"
        "ds_add_rtn_u32 v87, v67, v14 offset:1024\n\t"
        "s_waitcnt vmcnt(8) lgkmcnt(8)\n\t"
        "s_mov_b64 exec, s[30:31]\n\t"
        "v_mad_u32_u24 v68, v28, s66, v13\n\t"
        "v_mad_u32_u24 v69, v29, s66, v13\n\t"
        "v_mad_u32_u24 v70, v30, s66, v13\n\t"
        "v_mad_u32_u24 v71, v31, s66, v13\n\t"
        "v_lshlrev_b32_sdwa v68, 2, v68 dst_sel:DWORD dst_unused:UNUSED_PAD src0_sel:DWORD src1_sel:BYTE_3\n\t"
        "ds_add_rtn_u32 v88, v68, v14 offset:1024\n\t"
        "v_lshlrev_b32_sdwa v69, 2, v69 dst_sel:DWORD dst_unused:UNUSED_PAD src0_sel:DWORD src1_sel:BYTE_3\n\t"
        "ds_add_rtn_u32 v89, v69, v14 offset:1024\n\t"
        "v_lshlrev_b32_sdwa v70, 2, v70 dst_sel:DWORD dst_unused:UNUSED_PAD src0_sel:DWORD src1_sel:BYTE_3\n\t"
        "ds_add_rtn_u32 v90, v70, v14 offset:1024\n\t"
        "v_lshlrev_b32_sdwa v71, 2, v71 dst_sel:DWORD dst_unused:UNUSED_PAD src0_sel:DWORD src1_sel:BYTE_3\n\t"
        "ds_add_rtn_u32 v91, v71, v14 offset:1024\n\t"
        "s_waitcnt vmcnt(7) lgkmcnt(8)\n\t"
        "s_mov_b64 exec, s[32:33]\n\t"
        "v_mad_u32_u24 v72, v32, s66, v13\n\t"
        "v_mad_u32_u24 v73, v33, s66, v13\n\t"
        "v_mad_u32_u24 v74, v34, s66, v13\n\t"
        "v_mad_u32_u24 v75, v35, s66, v13\n\t"
        "v_lshlrev_b32_sdwa v72, 2, v72 dst_sel:DWORD dst_unused:UNUSED_PAD src0_sel:DWORD src1_sel:BYTE_3\n\t"
        "ds_add_rtn_u32 v92, v72, v14 offset:1024\n\t"
        "v_lshlrev_b32_sdwa v73, 2, v73 dst_sel:DWORD dst_unused:UNUSED_PAD src0_sel:DWORD src1_sel:BYTE_3\n\t"
        "ds_add_rtn_u32 v93, v73, v14 offset:1024\n\t"
        "v_lshlrev_b32_sdwa v74, 2, v74 dst_sel:DWORD dst_unused:UNUSED_PAD src0_sel:DWORD src1_sel:BYTE_3\n\t"
        "ds_add_rtn_u32 v94, v74, v14 offset:1024\n\t"
        "v_lshlrev_b32_sdwa v75, 2, v75 dst_sel:DWORD dst_unused:UNUSED_PAD src0_sel:DWORD src1_sel:BYTE_3\n\t"
        "ds_add_rtn_u32 v95, v75, v14 offset:1024\n\t"
        "s_mov_b64 exec, -1\n\t"
        "ds_read_b32 v4, v7 offset:1028\n\t"
        "ds_read_b32 v5, v7 offset:1284\n\t"
        "s_waitcnt vmcnt(5)\n\t"
        "v_xor_b32 v116, v116, v114\n\t"
        "v_bitop3_b32 v116, v117, v116, v114 bitop3:0xde\n\t"
        "v_bitop3_b32 v116, v118, v116, v114 bitop3:0xde\n\t"
        "v_bitop3_b32 v116, v119, v116, v114 bitop3:0xde\n\t"
        "v_xor_b32 v120, v120, v115\n\t"
        "v_bitop3_b32 v120, v121, v120, v115 bitop3:0xde\n\t"
        "v_bitop3_b32 v120, v122, v120, v115 bitop3:0xde\n\t"
        "v_bitop3_b32 v120, v123, v120, v115 bitop3:0xde\n\t"
        "v_cmp_ne_u32 s[40:41], 0, v116\n\t"
        "v_cmp_ne_u32 s[42:43], 0, v120\n\t"
        "s_and_b64 s[38:39], s[38:39], 0xffff\n\t"
        "s_and_b64 s[36:37], s[36:37], s[40:41]\n\t"
        "s_and_b64 s[38:39], s[38:39], s[42:43]\n\t"
        "s_or_b64 s[36:37], s[36:37], s[38:39]\n\t"
        "s_cselect_b32 s67, 1, s67\n\t"
        "s_waitcnt lgkmcnt(0)\n\t"
        "v_cmp_gt_u32 s[34:35], v4, v112\n\t"
        "v_cmp_gt_u32 s[36:37], v5, v113\n\t"
        "s_or_b64 s[38:39], s[34:35], s[36:37]\n\t"
        "s_cbranch_scc1 .L7_ov\n"
        ".L7_ov1:\n\t"
        "s_waitcnt vmcnt(4) lgkmcnt(8)\n\t"
        "s_mov_b64 exec, s[24:25]\n\t"
        "v_lshl_add_u32 v16, v16, 16, v36\n\t"
        "v_lshl_add_u32 v17, v17, 16, v37\n\t"
        "v_lshl_add_u32 v18, v18, 16, v38\n\t"
        "v_lshl_add_u32 v19, v19, 16, v39\n\t"
        "v_mad_i32_i24 v16, v56, s54, v16\n\t"
        "ds_add_u32 v76, v16\n\t"
        "v_mad_i32_i24 v17, v57, s54, v17\n\t"
        "ds_add_u32 v77, v17\n\t"
        "v_mad_i32_i24 v18, v58, s54, v18\n\t"
        "ds_add_u32 v78, v18\n\t"
        "v_mad_i32_i24 v19, v59, s54, v19\n\t"
        "ds_add_u32 v79, v19\n\t"
        "s_waitcnt vmcnt(3) lgkmcnt(8)\n\t"
        "s_mov_b64 exec, s[26:27]\n\t"
        "v_lshl_add_u32 v20, v20, 16, v40\n\t"
        "v_lshl_add_u32 v21, v21, 16, v41\n\t"
        "v_lshl_add_u32 v22, v22, 16, v42\n\t"
        "v_lshl_add_u32 v23, v23, 16, v43\n\t"
        "v_mad_i32_i24 v20, v60, s54, v20\n\t"
        "ds_add_u32 v80, v20\n\t"
        "v_mad_i32_i24 v21, v61, s54, v21\n\t"
        "ds_add_u32 v81, v21\n\t"
        "v_mad_i32_i24 v22, v62, s54, v22\n\t"
        "ds_add_u32 v82, v22\n\t"
        "v_mad_i32_i24 v23, v63, s54, v23\n\t"
        "ds_add_u32 v83, v23\n\t"
        "s_waitcnt vmcnt(2) lgkmcnt(8)\n\t"
        "s_mov_b64 exec, s[28:29]\n\t"
        "v_lshl_add_u32 v24, v24, 16, v44\n\t"
        "v_lshl_add_u32 v25, v25, 16, v45\n\t"
        "v_lshl_add_u32 v26, v26, 16, v46\n\t"
        "v_lshl_add_u32 v27, v27, 16, v47\n\t"
        "v_mad_i32_i24 v24, v64, s54, v24\n\t"
        "ds_add_u32 v84, v24\n\t"
        "v_mad_i32_i24 v25, v65, s54, v25\n\t"
        "ds_add_u32 v85, v25\n\t"
        "v_mad_i32_i24 v26, v66, s54, v26\n\t"
        "ds_add_u32 v86, v26\n\t"
        "v_mad_i32_i24 v27, v67, s54, v27\n\t"
        "ds_add_u32 v87, v27\n\t"
        "s_waitcnt vmcnt(1) lgkmcnt(8)\n\t"
        "s_mov_b64 exec, s[30:31]\n\t"
        "v_lshl_add_u32 v28, v28, 16, v48\n\t"
        "v_lshl_add_u32 v29, v29, 16, v49\n\t"
        "v_lshl_add_u32 v30, v30, 16, v50\n\t"
        "v_lshl_add_u32 v31, v31, 16, v51\n\t"
        "v_mad_i32_i24 v28, v68, s54, v28\n\t"
        "ds_add_u32 v88, v28\n\t"
        "v_mad_i32_i24 v29, v69, s54, v29\n\t"
        "ds_add_u32 v89, v29\n\t"
        "v_mad_i32_i24 v30, v70, s54, v30\n\t"
        "ds_add_u32 v90, v30\n\t"
        "v_mad_i32_i24 v31, v71, s54, v31\n\t"
        "ds_add_u32 v91, v31\n\t"
        "s_waitcnt vmcnt(0) lgkmcnt(8)\n\t"
        "s_mov_b64 exec, s[32:33]\n\t"
        "v_lshl_add_u32 v32, v32, 16, v52\n\t"
        "v_lshl_add_u32 v33, v33, 16, v53\n\t"
        "v_lshl_add_u32 v34, v34, 16, v54\n\t"
        "v_lshl_add_u32 v35, v35, 16, v55\n\t"
        "v_mad_i32_i24 v32, v72, s54, v32\n\t"
        "ds_add_u32 v92, v32\n\t"
        "v_mad_i32_i24 v33, v73, s54, v33\n\t"
        "ds_add_u32 v93, v33\n\t"
        "v_mad_i32_i24 v34, v74, s54, v34\n\t"
        "ds_add_u32 v94, v34\n\t"
        "v_mad_i32_i24 v35, v75, s54, v35\n\t"
        "ds_add_u32 v95, v35\n\t"
        "s_mov_b64 exec, -1\n\t"
        "s_and_b32 s78, s67, 1\n\t"
        "s_or_b32 s78, s78, s38\n\t"
        "s_or_b32 s78, s78, s39\n\t"
        "s_cbranch_scc1 .L7_mk\n"
        ".L7_mk1:\n\t"
        "s_waitcnt lgkmcnt(3)\n\t"
        "ds_read2_b32 v[16:17], v15 offset0:0 offset1:8\n\t"
        "ds_read2_b32 v[18:19], v15 offset0:16 offset1:24\n\t"
        "ds_read2_b32 v[20:21], v97 offset0:0 offset1:8\n\t"
        "ds_read2_b32 v[22:23], v97 offset0:16 offset1:24\n\t"
        "ds_read2_b32 v[24:25], v98 offset0:0 offset1:8\n\t"
        "ds_read2_b32 v[26:27], v98 offset0:16 offset1:24\n\t"
        "ds_read2_b32 v[28:29], v99 offset0:0 offset1:8\n\t"
        "ds_read2_b32 v[30:31], v99 offset0:16 offset1:24\n\t"
        "ds_read2_b32 v[32:33], v100 offset0:0 offset1:8\n\t"
        "ds_read2_b32 v[34:35], v100 offset0:16 offset1:24\n\t"
        "ds_read2_b32 v[36:37], v101 offset0:0 offset1:8\n\t"
        "ds_read2_b32 v[38:39], v101 offset0:16 offset1:24\n\t"
        "s_waitcnt lgkmcnt(10)\n\t"
        "buffer_store_dwordx4 v[16:19], v12, s[20:23], s48 offen offset:0 sc1\n\t"
        "ds_read2_b32 v[40:41], v102 offset0:0 offset1:8\n\t"
        "ds_read2_b32 v[42:43], v102 offset0:16 offset1:24\n\t"
        "s_waitcnt lgkmcnt(10)\n\t"
        "buffer_store_dwordx4 v[20:23], v12, s[20:23], s48 offen offset:1024 sc1\n\t"
        "ds_read2_b32 v[44:45], v103 offset0:0 offset1:8\n\t"
        "ds_read2_b32 v[46:47], v103 offset0:16 offset1:24\n\t"
        "s_waitcnt lgkmcnt(10)\n\t"
        "buffer_store_dwordx4 v[24:27], v12, s[20:23], s48 offen offset:2048 sc1\n\t"
        "ds_read2_b32 v[48:49], v104 offset0:0 offset1:8\n\t"
        "ds_read2_b32 v[50:51], v104 offset0:16 offset1:24\n\t"
        "s_waitcnt lgkmcnt(10)\n\t"
        "buffer_store_dwordx4 v[28:31], v12, s[20:23], s48 offen offset:3072 sc1\n\t"
        "ds_read2_b32 v[52:53], v105 offset0:0 offset1:8\n\t"
        "ds_read2_b32 v[54:55], v105 offset0:16 offset1:24\n\t"
        "s_waitcnt lgkmcnt(10)\n\t"
        "buffer_store_dwordx4 v[32:35], v12, s[20:23], s49 offen offset:0 sc1\n\t"
        "ds_read2_b32 v[56:57], v106 offset0:0 offset1:8\n\t"
        "ds_read2_b32 v[58:59], v106 offset0:16 offset1:24\n\t"
        "s_waitcnt lgkmcnt(10)\n\t"
        "buffer_store_dwordx4 v[36:39], v12, s[20:23], s49 offen offset:1024 sc1\n\t"
        "ds_read2_b32 v[60:61], v107 offset0:0 offset1:8\n\t"
        "ds_read2_b32 v[62:63], v107 offset0:16 offset1:24\n\t"
        "s_waitcnt lgkmcnt(10)\n\t"
        "buffer_store_dwordx4 v[40:43], v12, s[20:23], s49 offen offset:2048 sc1\n\t"
        "ds_read2_b32 v[64:65], v108 offset0:0 offset1:8\n\t"
        "ds_read2_b32 v[66:67], v108 offset0:16 offset1:24\n\t"
        "s_waitcnt lgkmcnt(10)\n\t"
        "buffer_store_dwordx4 v[44:47], v12, s[20:23], s49 offen offset:3072 sc1\n\t"
        "ds_read2_b32 v[68:69], v109 offset0:0 offset1:8\n\t"
        "ds_read2_b32 v[70:71], v109 offset0:16 offset1:24\n\t"
        "s_waitcnt lgkmcnt(10)\n\t"
        "buffer_store_dwordx4 v[48:51], v12, s[20:23], s50 offen offset:0 sc1\n\t"
        "ds_read2_b32 v[72:73], v110 offset0:0 offset1:8\n\t"
        "ds_read2_b32 v[74:75], v110 offset0:16 offset1:24\n\t"
        "s_waitcnt lgkmcnt(10)\n\t"
        "buffer_store_dwordx4 v[52:55], v12, s[20:23], s50 offen offset:1024 sc1\n\t"
        "ds_read2_b32 v[76:77], v111 offset0:0 offset1:8\n\t"
        "ds_read2_b32 v[78:79], v111 offset0:16 offset1:24\n\t"
        "s_mov_b64 exec, 1\n\t"
        "ds_add_u32 v2, v3 offset:72\n\t"
        "s_mov_b64 exec, -1\n\t"
        "s_waitcnt lgkmcnt(11)\n\t"
        "buffer_store_dwordx4 v[56:59], v12, s[20:23], s50 offen offset:2048 sc1\n\t"
        "s_waitcnt lgkmcnt(9)\n\t"
        "buffer_store_dwordx4 v[60:63], v12, s[20:23], s50 offen offset:3072 sc1\n\t"
        "s_waitcnt lgkmcnt(7)\n\t"
        "buffer_store_dwordx4 v[64:67], v12, s[20:23], s51 offen offset:0 sc1\n\t"
        "s_waitcnt lgkmcnt(5)\n\t"
        "buffer_store_dwordx4 v[68:71], v12, s[20:23], s51 offen offset:1024 sc1\n\t"
        "s_waitcnt lgkmcnt(3)\n\t"
        "buffer_store_dwordx4 v[72:75], v12, s[20:23], s51 offen offset:2048 sc1\n\t"
        "s_waitcnt lgkmcnt(1)\n\t"
        "buffer_store_dwordx4 v[76:79], v12, s[20:23], s51 offen offset:3072 sc1\n\t"
        "s_mov_b64 exec, 1\n\t"
        "ds_add_u32 v2, v3 offset:44\n\t"
        "s_mov_b64 exec, -1\n\t"
        "s_branch .L7_Q\n"
        ".L7_ov:\n\t"
        "s_mov_b32 s78, 68828\n\t"
        "v_mov_b32 v6, 40\n\t"
        "v_mad_u32_u24 v8, v56, v6, s78\n\t"
        "v_min_u32 v76, v76, v8\n\t"
        "v_mad_u32_u24 v8, v57, v6, s78\n\t"
        "v_min_u32 v77, v77, v8\n\t"
        "v_mad_u32_u24 v8, v58, v6, s78\n\t"
        "v_min_u32 v78, v78, v8\n\t"
        "v_mad_u32_u24 v8, v59, v6, s78\n\t"
        "v_min_u32 v79, v79, v8\n\t"
        "v_mad_u32_u24 v8, v60, v6, s78\n\t"
        "v_min_u32 v80, v80, v8\n\t"
        "v_mad_u32_u24 v8, v61, v6, s78\n\t"
        "v_min_u32 v81, v81, v8\n\t"
        "v_mad_u32_u24 v8, v62, v6, s78\n\t"
        "v_min_u32 v82, v82, v8\n\t"
        "v_mad_u32_u24 v8, v63, v6, s78\n\t"
        "v_min_u32 v83, v83, v8\n\t"
        "v_mad_u32_u24 v8, v64, v6, s78\n\t"
        "v_min_u32 v84, v84, v8\n\t"
        "v_mad_u32_u24 v8, v65, v6, s78\n\t"
        "v_min_u32 v85, v85, v8\n\t"
        "v_mad_u32_u24 v8, v66, v6, s78\n\t"
        "v_min_u32 v86, v86, v8\n\t"
        "v_mad_u32_u24 v8, v67, v6, s78\n\t"
        "v_min_u32 v87, v87, v8\n\t"
        "v_mad_u32_u24 v8, v68, v6, s78\n\t"
        "v_min_u32 v88, v88, v8\n\t"
        "v_mad_u32_u24 v8, v69, v6, s78\n\t"
        "v_min_u32 v89, v89, v8\n\t"
        "v_mad_u32_u24 v8, v70, v6, s78\n\t"
        "v_min_u32 v90, v90, v8\n\t"
        "v_mad_u32_u24 v8, v71, v6, s78\n\t"
        "v_min_u32 v91, v91, v8\n\t"
        "v_mad_u32_u24 v8, v72, v6, s78\n\t"
        "v_min_u32 v92, v92, v8\n\t"
        "v_mad_u32_u24 v8, v73, v6, s78\n\t"
        "v_min_u32 v93, v93, v8\n\t"
        "v_mad_u32_u24 v8, v74, v6, s78\n\t"
        "v_min_u32 v94, v94, v8\n\t"
        "v_mad_u32_u24 v8, v75, v6, s78\n\t"
        "v_min_u32 v95, v95, v8\n\t"
        "s_branch .L7_ov1\n"
        ".L7_mk:\n\t"
        "s_and_b32 s78, s55, 0xff000000\n\t"
        "s_or_b32 s78, s78, 0xffffff\n\t"
        "v_mov_b32 v6, s78\n\t"
        "v_add_u32 v112, -4, v112\n\t"
        "v_add_u32 v113, -4, v113\n\t"
        "s_mov_b64 exec, s[34:35]\n\t"
        "ds_write_b32 v112, v6\n\t"
        "s_mov_b64 exec, s[36:37]\n\t"
        "ds_write_b32 v113, v6\n\t"
        "s_mov_b64 exec, -1\n\t"
        "s_and_b32 s78, s67, 1\n\t"
        "s_lshl_b32 s78, s78, 24\n\t"
        "v_mov_b32 v6, s78\n\t"
        "ds_xor_b32 v112, v6\n\t"
        "ds_xor_b32 v113, v6\n\t"
        "s_branch .L7_mk1\n"
        ".L7_V2:\n\t"
        "s_barrier\n\t"
        "v_and_b32 v5, 7, v1\n\t"
        "v_mul_u32_u24 v5, 0x420, v5\n\t"
        "v_and_b32 v6, 8, v1\n\t"
        "v_lshl_add_u32 v5, v6, 6, v5\n\t"
        "v_and_b32 v6, 48, v1\n\t"
        "v_add_u32 v5, v5, v6\n\t"
        "v_add_u32 v5, 68864, v5\n\t"
        "s_lshr_b32 s78, s81, 1\n\t"
        "s_lshl3_add_u32 s78, s78, s80\n\t"
        "s_and_b32 s79, s81, 1\n\t"
        "s_lshl1_add_u32 s78, s78, s79\n\t"
        "s_lshl_b32 s82, s78, 10\n\t"
        "s_add_u32 s82, s82, 131072\n\t"
        "s_waitcnt lgkmcnt(0)\n\t"
        "s_add_u32 s68, s68, 1\n\t"
        "v_mov_b32 v4, s68\n\t"
        "ds_write_b32 v2, v4 offset:76\n\t"
        "s_and_b32 s73, s68, 1\n\t"
        "s_cselect_b32 s73, 1, 0x10000\n\t"
        "v_mov_b32 v16, s73\n\t"
        "v_and_b32 v17, 7, v1\n\t"
        "v_add_u32 v17, 1, v17\n\t"
        "v_lshrrev_b32 v17, v17, s68\n\t"
        "s_mov_b32 s49, 0x10001\n\t"
        "v_and_b32 v17, s49, v17\n\t"
        "s_mov_b32 s48, 0x3db8aa3b\n\t"
        "s_waitcnt vmcnt(0)\n\t"
        "ds_read_b128 v[32:35], v5 offset:0\n\t"
        "ds_read_b128 v[36:39], v5 offset:8448\n\t"
        "ds_read_b128 v[40:43], v5 offset:16896\n\t"
        "ds_read_b128 v[44:47], v5 offset:64\n\t"
        "ds_read_b128 v[48:51], v5 offset:8512\n\t"
        "ds_read_b128 v[52:55], v5 offset:16960\n\t"
        "ds_read_b128 v[56:59], v5 offset:128\n\t"
        "ds_read_b128 v[60:63], v5 offset:8576\n\t"
        "ds_read_b128 v[64:67], v5 offset:17024\n\t"
        "ds_read_b128 v[68:71], v5 offset:192\n\t"
        "ds_read_b128 v[72:75], v5 offset:8640\n\t"
        "ds_read_b128 v[76:79], v5 offset:17088\n\t"
        "s_waitcnt lgkmcnt(9)\n\t"
        "v_mfma_f32_16x16x32_bf16 v[20:23], v[36:39], v[32:35], 0\n\t"
        "v_mfma_f32_16x16x32_bf16 v[24:27], v[40:43], v[32:35], 0\n\t"
        "ds_read_b128 v[80:83], v5 offset:256\n\t"
        "ds_read_b128 v[84:87], v5 offset:8704\n\t"
        "ds_read_b128 v[88:91], v5 offset:17152\n\t"
        "s_waitcnt lgkmcnt(9)\n\t"
        "v_mfma_f32_16x16x32_bf16 v[20:23], v[48:51], v[44:47], v[20:23]\n\t"
        "v_mfma_f32_16x16x32_bf16 v[24:27], v[52:55], v[44:47], v[24:27]\n\t"
        "ds_read_b128 v[92:95], v5 offset:320\n\t"
        "ds_read_b128 v[96:99], v5 offset:8768\n\t"
        "ds_read_b128 v[100:103], v5 offset:17216\n\t"
        "s_waitcnt lgkmcnt(9)\n\t"
        "v_mfma_f32_16x16x32_bf16 v[20:23], v[60:63], v[56:59], v[20:23]\n\t"
        "v_mfma_f32_16x16x32_bf16 v[24:27], v[64:67], v[56:59], v[24:27]\n\t"
        "ds_read_b128 v[104:107], v5 offset:384\n\t"
        "ds_read_b128 v[108:111], v5 offset:8832\n\t"
        "ds_read_b128 v[112:115], v5 offset:17280\n\t"
        "s_waitcnt lgkmcnt(9)\n\t"
        "v_mfma_f32_16x16x32_bf16 v[20:23], v[72:75], v[68:71], v[20:23]\n\t"
        "v_mfma_f32_16x16x32_bf16 v[24:27], v[76:79], v[68:71], v[24:27]\n\t"
        "ds_read_b128 v[116:119], v5 offset:448\n\t"
        "ds_read_b128 v[120:123], v5 offset:8896\n\t"
        "ds_read_b128 v[124:127], v5 offset:17344\n\t"
        "s_waitcnt lgkmcnt(9)\n\t"
        "v_mfma_f32_16x16x32_bf16 v[20:23], v[84:87], v[80:83], v[20:23]\n\t"
        "v_mfma_f32_16x16x32_bf16 v[24:27], v[88:91], v[80:83], v[24:27]\n\t"
        "s_mov_b64 exec, 1\n\t"
        "ds_add_u32 v2, v3 offset:72\n\t"
        "s_mov_b64 exec, -1\n\t"
        "s_waitcnt lgkmcnt(7)\n\t"
        "v_mfma_f32_16x16x32_bf16 v[20:23], v[96:99], v[92:95], v[20:23]\n\t"
        "v_mfma_f32_16x16x32_bf16 v[24:27], v[100:103], v[92:95], v[24:27]\n\t"
        "s_waitcnt lgkmcnt(4)\n\t"
        "v_mfma_f32_16x16x32_bf16 v[20:23], v[108:111], v[104:107], v[20:23]\n\t"
        "v_mfma_f32_16x16x32_bf16 v[24:27], v[112:115], v[104:107], v[24:27]\n\t"
        "s_waitcnt lgkmcnt(1)\n\t"
        "v_mfma_f32_16x16x32_bf16 v[20:23], v[120:123], v[116:119], v[20:23]\n\t"
        "v_mfma_f32_16x16x32_bf16 v[24:27], v[124:127], v[116:119], v[24:27]\n\t"
        "s_nop 6\n\t"
        "v_mul_f32 v20, s48, v20\n\t"
        "v_mul_f32 v21, s48, v21\n\t"
        "v_mul_f32 v22, s48, v22\n\t"
        "v_mul_f32 v23, s48, v23\n\t"
        "v_mul_f32 v24, s48, v24\n\t"
        "v_mul_f32 v25, s48, v25\n\t"
        "v_mul_f32 v26, s48, v26\n\t"
        "v_mul_f32 v27, s48, v27\n\t"
        "s_mov_b32 s48, 0x07060302\n\t"
        "v_add_u32 v20, 0x10000, v20\n\t"
        "v_add_u32 v21, 0x10000, v21\n\t"
        "v_add_u32 v22, 0x10000, v22\n\t"
        "v_add_u32 v23, 0x10000, v23\n\t"
        "v_perm_b32 v20, v21, v20, s48\n\t"
        "v_perm_b32 v21, v23, v22, s48\n\t"
        "v_cvt_pk_bf16_f32 v22, v24, v25\n\t"
        "v_cvt_pk_bf16_f32 v23, v26, v27\n\t"
        "v_bfi_b32 v20, s49, v16, v20\n\t"
        "v_bfi_b32 v21, s49, v17, v21\n\t"
        "buffer_store_dwordx4 v[20:23], v12, s[20:23], s82 offen sc1\n\t"
        "s_mov_b64 exec, 1\n\t"
        "ds_add_u32 v2, v3 offset:44\n\t"
        "s_mov_b64 exec, -1\n\t"
        "s_branch .L7_Q\n"
        ".L7_T2:\n\t"
        "v_and_b32 v6, 0x380, v6\n\t"
        "s_and_b32 s82, s82, -1024\n\t"
        "v_mov_b32 v9, 4\n"
        ".L7_g38:\n\t"
        "s_memrealtime s[78:79]\n\t"
        "s_waitcnt lgkmcnt(0)\n\t"
        "s_sub_u32 s78, s78, s74\n\t"
        "s_cmp_ge_i32 s78, 170\n\t"
        "s_cbranch_scc1 .L7_h39\n\t"
        "s_sleep 1\n\t"
        "s_branch .L7_g38\n"
        ".L7_h39:\n"
        ".L7_b1:\n\t"
        "buffer_load_dword v7, v6, s[20:23], s82 offen sc1\n\t"
        "s_waitcnt vmcnt(0)\n\t"
        "v_subrev_u32 v7, s74, v7\n\t"
        "v_add_u32 v8, 0x3e8, v7\n\t"
        "v_cmp_lt_u32 vcc, 0x7d0, v8\n\t"
        "s_cbranch_vccz .L7_b2\n\t"
        "s_mov_b64 exec, 1\n\t"
        "ds_add_u32 v2, v9 offset:84\n\t"
        "s_mov_b64 exec, -1\n\t"
        "v_mov_b32 v9, 0\n\t"
        "v_mov_b32 v7, 0\n\t"
        "s_sleep 4\n\t"
        "s_memrealtime s[78:79]\n\t"
        "s_waitcnt lgkmcnt(0)\n\t"
        "s_sub_u32 s78, s78, s74\n\t"
        "s_cmp_lt_u32 s78, 0x4b0\n\t"
        "s_cbranch_scc1 .L7_b1\n"
        ".L7_b2:\n\t"
        "v_max_i32_dpp v7, v7, v7 row_half_mirror row_mask:0xf bank_mask:0xf\n\t"
        "s_nop 1\n\t"
        "v_max_i32_dpp v7, v7, v7 quad_perm:[1,0,3,2] row_mask:0xf bank_mask:0xf\n\t"
        "s_nop 1\n\t"
        "v_max_i32_dpp v7, v7, v7 quad_perm:[2,3,0,1] row_mask:0xf bank_mask:0xf\n\t"
        "v_add_u32 v7, 1, v7\n\t"
        "s_mov_b64 exec, 1\n\t"
        "ds_add_u32 v2, v7 offset:92\n\t"
        "s_mov_b64 exec, -1\n\t"
        "s_branch .L7_Q0\n"
        ".L7_P2:\n\t"
        "v_lshlrev_b32 v4, 4, v1\n\t"
        "v_add_u32 v4, 148160, v4\n\t"
        "v_mul_u32_u24 v5, 196, v1\n\t"
        "v_add_u32 v5, 148160, v5\n\t"
        "v_mov_b32 v8, 0xffff\n\t"
        "v_mov_b32 v9, v8\n\t"
        "v_mov_b64 v[10:11], v[8:9]\n\t"
        "s_waitcnt lgkmcnt(0)\n\t"
        "s_mul_i32 s72, s71, s65\n\t"
        "s_barrier\n\t"
        "ds_write_b128 v4, v[8:11] offset:0\n\t"
        "ds_write_b128 v4, v[8:11] offset:1024\n\t"
        "ds_write_b128 v4, v[8:11] offset:2048\n\t"
        "ds_write_b128 v4, v[8:11] offset:3072\n\t"
        "ds_write_b128 v4, v[8:11] offset:4096\n\t"
        "ds_write_b128 v4, v[8:11] offset:5120\n\t"
        "ds_write_b128 v4, v[8:11] offset:6144\n\t"
        "ds_write_b128 v4, v[8:11] offset:7168\n\t"
        "ds_write_b128 v4, v[8:11] offset:8192\n\t"
        "ds_write_b128 v4, v[8:11] offset:9216\n\t"
        "ds_write_b128 v4, v[8:11] offset:10240\n\t"
        "ds_write_b128 v4, v[8:11] offset:11264\n\t"
        "s_waitcnt lgkmcnt(6)\n\t"
        "ds_write_b128 v4, v[8:11] offset:12288\n\t"
        "ds_write_b128 v4, v[8:11] offset:13312\n\t"
        "ds_write_b128 v4, v[8:11] offset:14336\n\t"
        "ds_write_b128 v4, v[8:11] offset:15360\n\t"
        "ds_write_b32 v5, v2 offset:192\n\t"
        "s_mov_b64 exec, 0xffff\n\t"
        "ds_write_b32 v5, v2 offset:12736\n\t"
        "s_mov_b64 exec, 1\n\t"
        "ds_add_u32 v2, v3 offset:8\n\t"
        "s_mov_b64 exec, -1\n\t"
        "s_branch .L7_Q0\n"
        ".L7_R2:\n\t"
        "s_barrier\n\t"
        "s_sub_u32 s79, s64, s72\n\t"
        "s_min_i32 s79, s79, s65\n\t"
        "s_max_i32 s79, s79, 0\n\t"
        "v_mov_b32 v32, v1\n\t"
        "v_add_u32 v33, 64, v1\n\t"
        "v_cmp_gt_u32 s[32:33], s79, v32\n\t"
        "v_cmp_gt_u32 s[34:35], s79, v33\n\t"
        "v_cmp_gt_u32 s[36:37], 40, v1\n\t"
        "s_sub_u32 s78, 0xff, s65\n\t"
        "v_add_u32 v34, s78, v32\n\t"
        "v_add_u32 v6, 0xffffffb0, v34\n\t"
        "v_cmp_gt_u32 vcc, 0xff, v34\n\t"
        "s_nop 1\n\t"
        "v_cndmask_b32 v34, v6, v34, vcc\n\t"
        "v_add_u32 v35, s78, v33\n\t"
        "v_add_u32 v6, 0xffffffb0, v35\n\t"
        "v_cmp_gt_u32 vcc, 0xff, v35\n\t"
        "s_nop 1\n\t"
        "v_cndmask_b32 v35, v6, v35, vcc\n\t"
        "v_mov_b32 v12, 0x8000\n\t"
        "v_cndmask_b32 v44, v12, 0, s[32:33]\n\t"
        "v_cndmask_b32 v45, v12, 0, s[34:35]\n\t"
        "v_cndmask_b32 v46, 63, 31, s[36:37]\n\t"
        "v_mov_b32 v47, 63\n\t"
        "s_waitcnt vmcnt(0)\n\t"
        "v_lshlrev_b32 v16, 16, v16\n\t"
        "v_lshlrev_b32 v17, 16, v17\n\t"
        "v_mul_f32 v36, v16, v16\n\t"
        "v_mul_f32 v37, v17, v17\n\t"
        "v_rcp_f32 v36, v36\n\t"
        "v_rcp_f32 v37, v37\n\t"
        "v_or3_b32 v16, v16, v34, v44\n\t"
        "v_or3_b32 v17, v17, v35, v45\n\t"
        "v_add_f32 v36, 0xc0600000, v36\n\t"
        "v_add_f32 v37, 0xc0600000, v37\n\t"
        "v_cvt_u32_f32 v36, v36\n\t"
        "v_cvt_u32_f32 v37, v37\n\t"
        "v_min_u32 v36, 31, v36\n\t"
        "v_min_u32 v37, 31, v37\n\t"
        "v_cndmask_b32 v36, 0, v36, s[32:33]\n\t"
        "v_cndmask_b32 v37, 0, v37, s[34:35]\n\t"
        "v_sub_u32 v36, v46, v36\n\t"
        "v_sub_u32 v37, v47, v37\n\t"
        "v_lshlrev_b32 v36, 2, v36\n\t"
        "v_lshlrev_b32 v37, 2, v37\n\t"
        "ds_add_rtn_u32 v40, v36, v3 offset:256\n\t"
        "s_mov_b64 exec, 0xffff\n\t"
        "ds_add_rtn_u32 v41, v37, v3 offset:256\n\t"
        "s_mov_b64 exec, -1\n\t"
        "v_lshlrev_b32 v9, 2, v1\n\t"
        "ds_read_b32 v10, v9 offset:256\n\t"
        "s_cmp_eq_u32 s70, 0\n\t"
        "s_cselect_b32 s78, 56, 8\n\t"
        "s_cselect_b32 s79, 8, 56\n\t"
        "v_mov_b32 v49, -40\n\t"
        "v_cndmask_b32 v48, v49, 0, s[36:37]\n\t"
        "s_mov_b32 s40, 0x23108954\n\t"
        "s_mov_b32 s41, 0x67\n\t"
        "v_mov_b32 v13, s78\n\t"
        "v_mov_b32 v14, s79\n\t"
        "v_cndmask_b32 v13, v14, v13, s[36:37]\n\t"
        "s_waitcnt lgkmcnt(0)\n\t"
        "v_mov_b32 v11, v10\n\t"
        "s_nop 1\n\t"
        "v_add_u32_dpp v11, v11, v11 row_shr:1 row_mask:0xf bank_mask:0xf bound_ctrl:1\n\t"
        "s_nop 1\n\t"
        "v_add_u32_dpp v11, v11, v11 row_shr:2 row_mask:0xf bank_mask:0xf bound_ctrl:1\n\t"
        "s_nop 1\n\t"
        "v_add_u32_dpp v11, v11, v11 row_shr:4 row_mask:0xf bank_mask:0xf bound_ctrl:1\n\t"
        "s_nop 1\n\t"
        "v_add_u32_dpp v11, v11, v11 row_shr:8 row_mask:0xf bank_mask:0xf bound_ctrl:1\n\t"
        "s_nop 1\n\t"
        "v_add_u32_dpp v11, v11, v11 row_bcast:15 row_mask:0xa bank_mask:0xf\n\t"
        "s_nop 1\n\t"
        "v_add_u32_dpp v11, v11, v11 row_bcast:31 row_mask:0xc bank_mask:0xf\n\t"
        "v_sub_u32 v11, v11, v10\n\t"
        "ds_bpermute_b32 v36, v36, v11\n\t"
        "ds_bpermute_b32 v37, v37, v11\n\t"
        "v_lshlrev_b32 v9, 1, v1\n\t"
        "s_waitcnt lgkmcnt(0)\n\t"
        "v_add3_u32 v36, v36, v40, v48\n\t"
        "v_and_b32 v6, -4, v36\n\t"
        "v_lshrrev_b64 v[10:11], v6, s[40:41]\n\t"
        "v_and_b32 v10, 15, v10\n\t"
        "v_and_b32 v6, 3, v36\n\t"
        "v_lshl_add_u32 v6, v10, 2, v6\n\t"
        "v_add_u32 v36, v6, v13\n\t"
        "v_and_b32 v6, 15, v36\n\t"
        "v_lshlrev_b32 v6, 4, v6\n\t"
        "v_lshl_add_u32 v6, v36, 8, v6\n\t"
        "v_lshlrev_b32 v36, 2, v36\n\t"
        "ds_write_b32 v36, v16 offset:512\n\t"
        "ds_write_b16 v9, v6 offset:1792\n\t"
        "v_add3_u32 v37, v37, v41, v49\n\t"
        "v_and_b32 v6, -4, v37\n\t"
        "v_lshrrev_b64 v[10:11], v6, s[40:41]\n\t"
        "v_and_b32 v10, 15, v10\n\t"
        "v_and_b32 v6, 3, v37\n\t"
        "v_lshl_add_u32 v6, v10, 2, v6\n\t"
        "v_add_u32 v37, v6, v14\n\t"
        "v_and_b32 v6, 15, v37\n\t"
        "v_lshlrev_b32 v6, 4, v6\n\t"
        "v_lshl_add_u32 v6, v37, 8, v6\n\t"
        "v_lshlrev_b32 v37, 2, v37\n\t"
        "s_mov_b64 exec, 0xffff\n\t"
        "ds_write_b32 v37, v17 offset:512\n\t"
        "ds_write_b16 v9, v6 offset:1920\n\t"
        "s_mov_b64 exec, 1\n\t"
        "ds_add_u32 v2, v3 offset:8\n\t"
        "s_mov_b64 exec, -1\n"
        ".L7_w40:\n\t"
        "ds_read_b64 v[4:5], v2 offset:72\n\t"
        "s_waitcnt lgkmcnt(0)\n\t"
        "v_cmp_gt_u16 vcc, 1, v4\n\t"
        "s_cbranch_vccz .L7_d41\n\t"
        "s_sleep 2\n\t"
        "s_sub_i32 s83, s83, 1\n\t"
        "s_cmp_gt_i32 s83, 0\n\t"
        "s_cbranch_scc1 .L7_w40\n"
        ".L7_d41:\n\t"
        "v_readfirstlane_b32 s68, v5\n\t"
        "v_lshlrev_b32 v22, 4, v1\n\t"
        "v_add_u32 v9, 142848, v22\n\t"
        "v_and_b32 v10, 60, v1\n\t"
        "ds_read_b32 v24, v10 offset:704\n\t"
        "ds_read_b32 v25, v10 offset:768\n\t"
        "ds_read_b32 v26, v10 offset:832\n\t"
        "v_and_b32 v11, 15, v1\n\t"
        "v_lshrrev_b32 v12, 4, v1\n\t"
        "v_xor_b32 v16, v11, v12\n\t"
        "v_lshlrev_b32 v16, 4, v16\n\t"
        "v_lshl_add_u32 v16, v11, 8, v16\n\t"
        "v_xor_b32 v17, 64, v16\n\t"
        "v_xor_b32 v18, 128, v16\n\t"
        "v_xor_b32 v19, 192, v16\n\t"
        "ds_write_b128 v9, v[16:19]\n\t"
        "s_and_b32 s73, s68, 1\n\t"
        "s_cselect_b32 s73, 1, 0x10000\n\t"
        "v_mov_b32 v20, s73\n\t"
        "v_and_b32 v21, 7, v1\n\t"
        "v_add_u32 v21, 1, v21\n\t"
        "v_lshrrev_b32 v21, v21, s68\n\t"
        "s_mov_b32 s49, 0x10001\n\t"
        "v_and_b32 v21, s49, v21\n\t"
        "v_or_b32 v23, 0x80, v22\n\t"
        "ds_write_b128 v9, v[20:23] offset:1024\n\t"
        "s_add_u32 s78, s72, s65\n\t"
        "s_sub_u32 s78, s78, 0xff\n\t"
        "s_lshl_b32 s78, s78, 9\n\t"
        "v_and_b32 v13, 48, v22\n\t"
        "v_add_u32 v14, s78, v13\n\t"
        "s_movk_i32 s78, 0x200\n\t"
        "v_bfe_u32 v27, v1, 1, 2\n\t"
        "v_xor_b32 v27, v27, v12\n\t"
        "v_lshlrev_b32 v27, 4, v27\n\t"
        "v_lshl_add_u32 v27, v11, 6, v27\n\t"
        "v_lshlrev_b32 v28, 1, v1\n\t"
        "v_and_b32 v28, 48, v28\n\t"
        "v_xor_b32 v28, v28, v22\n\t"
        "s_waitcnt lgkmcnt(2)\n\t"
        "v_mad_u32_u16 v24, v24, s78, v14\n\t"
        "v_mad_u32_u16 v25, v25, s78, v14\n\t"
        "v_mad_u32_u16 v26, v26, s78, v14\n\t"
        "s_mov_b32 exec_hi, 0\n\t"
        "v_mov_b32 v24, -2.0\n\t"
        "s_mov_b64 exec, -1\n\t"
        "ds_write_b128 v9, v[24:27] offset:2048\n\t"
        "s_mov_b32 s78, 0x3f80\n\t"
        "s_mov_b32 s79, 0\n\t"
        "v_lshlrev_b64 v[30:31], v13, s[78:79]\n\t"
        "s_mov_b32 s78, 0x00f0000f\n\t"
        "s_mov_b32 s79, 0xf0000f00\n\t"
        "v_cndmask_b32 v30, 0, v30, s[78:79]\n\t"
        "v_cndmask_b32 v31, 0, v31, s[78:79]\n\t"
        "ds_write_b128 v9, v[28:31] offset:3072\n\t"
        "s_branch .L7_Q\n"
        ".L7_Q0:\n"
        ".L7_w42:\n\t"
        "ds_read_b64 v[4:5], v2 offset:72\n\t"
        "s_waitcnt lgkmcnt(0)\n\t"
        "v_cmp_gt_u16 vcc, 1, v4\n\t"
        "s_cbranch_vccz .L7_d43\n\t"
        "s_sleep 2\n\t"
        "s_sub_i32 s83, s83, 1\n\t"
        "s_cmp_gt_i32 s83, 0\n\t"
        "s_cbranch_scc1 .L7_w42\n"
        ".L7_d43:\n\t"
        "v_readfirstlane_b32 s68, v5\n"
        ".L7_Q:\n\t"
        "s_setprio 3\n\t"
        "v_and_b32 v5, 7, v1\n\t"
        "v_mul_u32_u24 v5, 0x420, v5\n\t"
        "v_and_b32 v6, 8, v1\n\t"
        "v_lshl_add_u32 v5, v6, 6, v5\n\t"
        "v_and_b32 v6, 48, v1\n\t"
        "v_add_u32 v5, v5, v6\n\t"
        "s_and_b32 s73, s68, 1\n\t"
        "s_cselect_b32 s73, 1, 0x10000\n\t"
        "v_and_b32 v6, 7, v1\n\t"
        "v_add_u32 v6, 1, v6\n\t"
        "v_lshrrev_b32 v6, v6, s68\n\t"
        "s_mov_b32 s49, 0x10001\n\t"
        "v_and_b32 v6, s49, v6\n\t"
        "s_add_u32 s80, s69, s76\n\t"
        "s_and_b32 s80, s80, 3\n\t"
        "s_lshl2_add_u32 s78, s70, s80\n\t"
        "s_lshl_b32 s78, s78, 14\n\t"
        "s_add_u32 s78, s78, 131072\n\t"
        "v_lshl_add_u32 v4, v1, 4, s78\n\t"
        "s_mov_b32 s24, 4096\n\t"
        "s_mov_b32 s25, 8192\n\t"
        "s_mov_b32 s26, 12288\n"
        ".L7_w44:\n\t"
        "ds_read_b32 v0, v2 offset:92\n\t"
        "s_waitcnt lgkmcnt(0)\n\t"
        "v_cmp_gt_u16 vcc, 1, v0\n\t"
        "s_cbranch_vccz .L7_d45\n\t"
        "s_sleep 1\n\t"
        "s_sub_i32 s83, s83, 1\n\t"
        "s_cmp_gt_i32 s83, 0\n\t"
        "s_cbranch_scc1 .L7_w44\n"
        ".L7_d45:\n\t"
        "v_readfirstlane_b32 s78, v0\n\t"
        "s_sub_u32 s78, s78, 1\n\t"
        "s_add_u32 s77, s74, s78\n"
        ".L7_g46:\n\t"
        "s_memrealtime s[78:79]\n\t"
        "s_waitcnt lgkmcnt(0)\n\t"
        "s_sub_u32 s78, s78, s77\n\t"
        "s_cmp_ge_i32 s78, 280\n\t"
        "s_cbranch_scc1 .L7_h47\n\t"
        "s_sleep 1\n\t"
        "s_branch .L7_g46\n"
        ".L7_h47:\n"
        ".L7_w48:\n\t"
        "ds_read_b32 v0, v2 offset:88\n\t"
        "s_waitcnt lgkmcnt(0)\n\t"
        "v_cmp_gt_u16 vcc, 8, v0\n\t"
        "s_cbranch_vccz .L7_d49\n\t"
        "s_sleep 1\n\t"
        "s_sub_i32 s83, s83, 1\n\t"
        "s_cmp_gt_i32 s83, 0\n\t"
        "s_cbranch_scc1 .L7_w48\n"
        ".L7_d49:\n\t"
        "buffer_load_dwordx4 v[64:67], v4, s[20:23], 0 offen offset:0 sc1\n\t"
        "buffer_load_dwordx4 v[68:71], v4, s[20:23], 0 offen offset:1024 sc1\n\t"
        "buffer_load_dwordx4 v[72:75], v4, s[20:23], 0 offen offset:2048 sc1\n\t"
        "buffer_load_dwordx4 v[76:79], v4, s[20:23], 0 offen offset:3072 sc1\n\t"
        "buffer_load_dwordx4 v[80:83], v4, s[20:23], s24 offen offset:0 sc1\n\t"
        "buffer_load_dwordx4 v[84:87], v4, s[20:23], s24 offen offset:1024 sc1\n\t"
        "buffer_load_dwordx4 v[88:91], v4, s[20:23], s24 offen offset:2048 sc1\n\t"
        "buffer_load_dwordx4 v[92:95], v4, s[20:23], s24 offen offset:3072 sc1\n\t"
        "buffer_load_dwordx4 v[96:99], v4, s[20:23], s25 offen offset:0 sc1\n\t"
        "buffer_load_dwordx4 v[100:103], v4, s[20:23], s25 offen offset:1024 sc1\n\t"
        "buffer_load_dwordx4 v[104:107], v4, s[20:23], s25 offen offset:2048 sc1\n\t"
        "buffer_load_dwordx4 v[108:111], v4, s[20:23], s25 offen offset:3072 sc1\n\t"
        "buffer_load_dwordx4 v[112:115], v4, s[20:23], s26 offen offset:0 sc1\n\t"
        "buffer_load_dwordx4 v[116:119], v4, s[20:23], s26 offen offset:1024 sc1\n\t"
        "buffer_load_dwordx4 v[120:123], v4, s[20:23], s26 offen offset:2048 sc1\n\t"
        "buffer_load_dwordx4 v[124:127], v4, s[20:23], s26 offen offset:3072 sc1\n\t"
        "s_mov_b64 exec, 1\n\t"
        "ds_add_u32 v2, v3 offset:84\n\t"
        "s_mov_b64 exec, -1\n"
        ".L7_w50:\n\t"
        "ds_read_b32 v0, v2 offset:0\n\t"
        "s_waitcnt lgkmcnt(0)\n\t"
        "v_cmp_gt_u16 vcc, 4, v0\n\t"
        "s_cbranch_vccz .L7_d51\n\t"
        "s_sleep 1\n\t"
        "s_sub_i32 s83, s83, 1\n\t"
        "s_cmp_gt_i32 s83, 0\n\t"
        "s_cbranch_scc1 .L7_w50\n"
        ".L7_d51:\n\t"
        "s_branch .L7_qg\n"
        ".L7_qr:\n\t"
        "s_waitcnt lgkmcnt(0)\n\t"
        "s_sleep 8\n\t"
        "s_sub_i32 s83, s83, 64\n\t"
        "s_cmp_gt_i32 s83, 0\n\t"
        "s_cbranch_scc0 .L7_qd\n\t"
        "buffer_load_dwordx4 v[64:67], v4, s[20:23], 0 offen offset:0 sc1\n\t"
        "buffer_load_dwordx4 v[68:71], v4, s[20:23], 0 offen offset:1024 sc1\n\t"
        "buffer_load_dwordx4 v[72:75], v4, s[20:23], 0 offen offset:2048 sc1\n\t"
        "buffer_load_dwordx4 v[76:79], v4, s[20:23], 0 offen offset:3072 sc1\n\t"
        "buffer_load_dwordx4 v[80:83], v4, s[20:23], s24 offen offset:0 sc1\n\t"
        "buffer_load_dwordx4 v[84:87], v4, s[20:23], s24 offen offset:1024 sc1\n\t"
        "buffer_load_dwordx4 v[88:91], v4, s[20:23], s24 offen offset:2048 sc1\n\t"
        "buffer_load_dwordx4 v[92:95], v4, s[20:23], s24 offen offset:3072 sc1\n\t"
        "buffer_load_dwordx4 v[96:99], v4, s[20:23], s25 offen offset:0 sc1\n\t"
        "buffer_load_dwordx4 v[100:103], v4, s[20:23], s25 offen offset:1024 sc1\n\t"
        "buffer_load_dwordx4 v[104:107], v4, s[20:23], s25 offen offset:2048 sc1\n\t"
        "buffer_load_dwordx4 v[108:111], v4, s[20:23], s25 offen offset:3072 sc1\n\t"
        "buffer_load_dwordx4 v[112:115], v4, s[20:23], s26 offen offset:0 sc1\n\t"
        "buffer_load_dwordx4 v[116:119], v4, s[20:23], s26 offen offset:1024 sc1\n\t"
        "buffer_load_dwordx4 v[120:123], v4, s[20:23], s26 offen offset:2048 sc1\n\t"
        "buffer_load_dwordx4 v[124:127], v4, s[20:23], s26 offen offset:3072 sc1\n"
        ".L7_qg:\n\t"
        "v_mov_b32 v7, 0\n\t"
        "ds_read_b128 v[8:11], v5 offset:2048\n\t"
        "ds_read_b128 v[12:15], v5 offset:10496\n\t"
        "ds_read_b128 v[16:19], v5 offset:18944\n\t"
        "ds_read_b128 v[20:23], v5 offset:27392\n\t"
        "s_waitcnt vmcnt(14) lgkmcnt(3)\n\t"
        "v_xor_b32 v64, s73, v64\n\t"
        "v_xor_b32 v65, v6, v65\n\t"
        "v_xor_b32 v68, s73, v68\n\t"
        "v_xor_b32 v69, v6, v69\n\t"
        "v_mfma_f32_16x16x32_bf16 v[24:27], v[64:67], v[8:11], 0\n\t"
        "s_nop 0\n\t"
        "v_mfma_f32_16x16x32_bf16 v[28:31], v[68:71], v[8:11], 0\n\t"
        "v_or3_b32 v7, v7, v64, v65\n\t"
        "ds_read_b128 v[8:11], v5 offset:35840\n\t"
        "s_waitcnt lgkmcnt(3)\n\t"
        "v_mfma_f32_16x16x32_bf16 v[32:35], v[64:67], v[12:15], 0\n\t"
        "v_mfma_f32_16x16x32_bf16 v[36:39], v[68:71], v[12:15], 0\n\t"
        "v_or3_b32 v7, v7, v68, v69\n\t"
        "ds_read_b128 v[12:15], v5 offset:2112\n\t"
        "s_waitcnt lgkmcnt(3)\n\t"
        "v_mfma_f32_16x16x32_bf16 v[40:43], v[64:67], v[16:19], 0\n\t"
        "v_mfma_f32_16x16x32_bf16 v[44:47], v[68:71], v[16:19], 0\n\t"
        "ds_read_b128 v[16:19], v5 offset:10560\n\t"
        "s_waitcnt lgkmcnt(3)\n\t"
        "v_mfma_f32_16x16x32_bf16 v[48:51], v[64:67], v[20:23], 0\n\t"
        "v_mfma_f32_16x16x32_bf16 v[52:55], v[68:71], v[20:23], 0\n\t"
        "ds_read_b128 v[20:23], v5 offset:19008\n\t"
        "s_waitcnt vmcnt(12) lgkmcnt(3)\n\t"
        "v_xor_b32 v72, s73, v72\n\t"
        "v_xor_b32 v73, v6, v73\n\t"
        "v_xor_b32 v76, s73, v76\n\t"
        "v_xor_b32 v77, v6, v77\n\t"
        "v_mfma_f32_16x16x32_bf16 v[56:59], v[64:67], v[8:11], 0\n\t"
        "v_mfma_f32_16x16x32_bf16 v[60:63], v[68:71], v[8:11], 0\n\t"
        "ds_read_b128 v[8:11], v5 offset:27456\n\t"
        "ds_read_b128 v[64:67], v5 offset:35904\n\t"
        "ds_read_b128 v[68:71], v5 offset:2176\n\t"
        "s_waitcnt lgkmcnt(4)\n\t"
        "v_mfma_f32_16x16x32_bf16 v[24:27], v[72:75], v[12:15], v[24:27]\n\t"
        "v_mfma_f32_16x16x32_bf16 v[28:31], v[76:79], v[12:15], v[28:31]\n\t"
        "v_or3_b32 v7, v7, v72, v73\n\t"
        "ds_read_b128 v[12:15], v5 offset:10624\n\t"
        "v_mfma_f32_16x16x32_bf16 v[32:35], v[72:75], v[16:19], v[32:35]\n\t"
        "v_mfma_f32_16x16x32_bf16 v[36:39], v[76:79], v[16:19], v[36:39]\n\t"
        "v_or3_b32 v7, v7, v76, v77\n\t"
        "ds_read_b128 v[16:19], v5 offset:19072\n\t"
        "s_waitcnt lgkmcnt(4)\n\t"
        "v_mfma_f32_16x16x32_bf16 v[40:43], v[72:75], v[20:23], v[40:43]\n\t"
        "v_mfma_f32_16x16x32_bf16 v[44:47], v[76:79], v[20:23], v[44:47]\n\t"
        "ds_read_b128 v[20:23], v5 offset:27520\n\t"
        "v_mfma_f32_16x16x32_bf16 v[48:51], v[72:75], v[8:11], v[48:51]\n\t"
        "v_mfma_f32_16x16x32_bf16 v[52:55], v[76:79], v[8:11], v[52:55]\n\t"
        "ds_read_b128 v[8:11], v5 offset:35968\n\t"
        "s_waitcnt vmcnt(10) lgkmcnt(4)\n\t"
        "v_xor_b32 v80, s73, v80\n\t"
        "v_xor_b32 v81, v6, v81\n\t"
        "v_xor_b32 v84, s73, v84\n\t"
        "v_xor_b32 v85, v6, v85\n\t"
        "v_mfma_f32_16x16x32_bf16 v[56:59], v[72:75], v[64:67], v[56:59]\n\t"
        "v_mfma_f32_16x16x32_bf16 v[60:63], v[76:79], v[64:67], v[60:63]\n\t"
        "ds_read_b128 v[64:67], v5 offset:2240\n\t"
        "ds_read_b128 v[72:75], v5 offset:10688\n\t"
        "ds_read_b128 v[76:79], v5 offset:19136\n\t"
        "v_mfma_f32_16x16x32_bf16 v[24:27], v[80:83], v[68:71], v[24:27]\n\t"
        "v_mfma_f32_16x16x32_bf16 v[28:31], v[84:87], v[68:71], v[28:31]\n\t"
        "v_or3_b32 v7, v7, v80, v81\n\t"
        "ds_read_b128 v[68:71], v5 offset:27584\n\t"
        "s_waitcnt lgkmcnt(6)\n\t"
        "v_mfma_f32_16x16x32_bf16 v[32:35], v[80:83], v[12:15], v[32:35]\n\t"
        "v_mfma_f32_16x16x32_bf16 v[36:39], v[84:87], v[12:15], v[36:39]\n\t"
        "v_or3_b32 v7, v7, v84, v85\n\t"
        "ds_read_b128 v[12:15], v5 offset:36032\n\t"
        "v_mfma_f32_16x16x32_bf16 v[40:43], v[80:83], v[16:19], v[40:43]\n\t"
        "v_mfma_f32_16x16x32_bf16 v[44:47], v[84:87], v[16:19], v[44:47]\n\t"
        "ds_read_b128 v[16:19], v5 offset:2304\n\t"
        "s_waitcnt lgkmcnt(6)\n\t"
        "v_mfma_f32_16x16x32_bf16 v[48:51], v[80:83], v[20:23], v[48:51]\n\t"
        "v_mfma_f32_16x16x32_bf16 v[52:55], v[84:87], v[20:23], v[52:55]\n\t"
        "ds_read_b128 v[20:23], v5 offset:10752\n\t"
        "s_waitcnt vmcnt(8) lgkmcnt(6)\n\t"
        "v_xor_b32 v88, s73, v88\n\t"
        "v_xor_b32 v89, v6, v89\n\t"
        "v_xor_b32 v92, s73, v92\n\t"
        "v_xor_b32 v93, v6, v93\n\t"
        "v_mfma_f32_16x16x32_bf16 v[56:59], v[80:83], v[8:11], v[56:59]\n\t"
        "v_mfma_f32_16x16x32_bf16 v[60:63], v[84:87], v[8:11], v[60:63]\n\t"
        "ds_read_b128 v[8:11], v5 offset:19200\n\t"
        "ds_read_b128 v[80:83], v5 offset:27648\n\t"
        "v_mfma_f32_16x16x32_bf16 v[24:27], v[88:91], v[64:67], v[24:27]\n\t"
        "v_mfma_f32_16x16x32_bf16 v[28:31], v[92:95], v[64:67], v[28:31]\n\t"
        "v_or3_b32 v7, v7, v88, v89\n\t"
        "ds_read_b128 v[84:87], v5 offset:36096\n\t"
        "s_waitcnt lgkmcnt(7)\n\t"
        "v_mfma_f32_16x16x32_bf16 v[32:35], v[88:91], v[72:75], v[32:35]\n\t"
        "v_mfma_f32_16x16x32_bf16 v[36:39], v[92:95], v[72:75], v[36:39]\n\t"
        "v_or3_b32 v7, v7, v92, v93\n\t"
        "ds_read_b128 v[64:67], v5 offset:2368\n\t"
        "v_mfma_f32_16x16x32_bf16 v[40:43], v[88:91], v[76:79], v[40:43]\n\t"
        "v_mfma_f32_16x16x32_bf16 v[44:47], v[92:95], v[76:79], v[44:47]\n\t"
        "ds_read_b128 v[72:75], v5 offset:10816\n\t"
        "s_waitcnt lgkmcnt(7)\n\t"
        "v_mfma_f32_16x16x32_bf16 v[48:51], v[88:91], v[68:71], v[48:51]\n\t"
        "v_mfma_f32_16x16x32_bf16 v[52:55], v[92:95], v[68:71], v[52:55]\n\t"
        "ds_read_b128 v[76:79], v5 offset:19264\n\t"
        "s_waitcnt vmcnt(6) lgkmcnt(7)\n\t"
        "v_xor_b32 v96, s73, v96\n\t"
        "v_xor_b32 v97, v6, v97\n\t"
        "v_xor_b32 v100, s73, v100\n\t"
        "v_xor_b32 v101, v6, v101\n\t"
        "v_mfma_f32_16x16x32_bf16 v[56:59], v[88:91], v[12:15], v[56:59]\n\t"
        "v_mfma_f32_16x16x32_bf16 v[60:63], v[92:95], v[12:15], v[60:63]\n\t"
        "ds_read_b128 v[68:71], v5 offset:27712\n\t"
        "v_mfma_f32_16x16x32_bf16 v[24:27], v[96:99], v[16:19], v[24:27]\n\t"
        "v_mfma_f32_16x16x32_bf16 v[28:31], v[100:103], v[16:19], v[28:31]\n\t"
        "v_or3_b32 v7, v7, v96, v97\n\t"
        "ds_read_b128 v[12:15], v5 offset:36160\n\t"
        "s_waitcnt lgkmcnt(7)\n\t"
        "v_mfma_f32_16x16x32_bf16 v[32:35], v[96:99], v[20:23], v[32:35]\n\t"
        "v_mfma_f32_16x16x32_bf16 v[36:39], v[100:103], v[20:23], v[36:39]\n\t"
        "v_or3_b32 v7, v7, v100, v101\n\t"
        "ds_read_b128 v[88:91], v5 offset:2432\n\t"
        "v_mfma_f32_16x16x32_bf16 v[40:43], v[96:99], v[8:11], v[40:43]\n\t"
        "v_mfma_f32_16x16x32_bf16 v[44:47], v[100:103], v[8:11], v[44:47]\n\t"
        "ds_read_b128 v[92:95], v5 offset:10880\n\t"
        "s_waitcnt lgkmcnt(7)\n\t"
        "v_mfma_f32_16x16x32_bf16 v[48:51], v[96:99], v[80:83], v[48:51]\n\t"
        "v_mfma_f32_16x16x32_bf16 v[52:55], v[100:103], v[80:83], v[52:55]\n\t"
        "ds_read_b128 v[16:19], v5 offset:19328\n\t"
        "s_waitcnt vmcnt(4) lgkmcnt(7)\n\t"
        "v_xor_b32 v104, s73, v104\n\t"
        "v_xor_b32 v105, v6, v105\n\t"
        "v_xor_b32 v108, s73, v108\n\t"
        "v_xor_b32 v109, v6, v109\n\t"
        "v_mfma_f32_16x16x32_bf16 v[56:59], v[96:99], v[84:87], v[56:59]\n\t"
        "v_mfma_f32_16x16x32_bf16 v[60:63], v[100:103], v[84:87], v[60:63]\n\t"
        "ds_read_b128 v[20:23], v5 offset:27776\n\t"
        "v_mfma_f32_16x16x32_bf16 v[24:27], v[104:107], v[64:67], v[24:27]\n\t"
        "v_mfma_f32_16x16x32_bf16 v[28:31], v[108:111], v[64:67], v[28:31]\n\t"
        "v_or3_b32 v7, v7, v104, v105\n\t"
        "ds_read_b128 v[8:11], v5 offset:36224\n\t"
        "s_waitcnt lgkmcnt(7)\n\t"
        "v_mfma_f32_16x16x32_bf16 v[32:35], v[104:107], v[72:75], v[32:35]\n\t"
        "v_mfma_f32_16x16x32_bf16 v[36:39], v[108:111], v[72:75], v[36:39]\n\t"
        "v_or3_b32 v7, v7, v108, v109\n\t"
        "ds_read_b128 v[80:83], v5 offset:2496\n\t"
        "v_mfma_f32_16x16x32_bf16 v[40:43], v[104:107], v[76:79], v[40:43]\n\t"
        "v_mfma_f32_16x16x32_bf16 v[44:47], v[108:111], v[76:79], v[44:47]\n\t"
        "ds_read_b128 v[84:87], v5 offset:10944\n\t"
        "s_waitcnt lgkmcnt(7)\n\t"
        "v_mfma_f32_16x16x32_bf16 v[48:51], v[104:107], v[68:71], v[48:51]\n\t"
        "v_mfma_f32_16x16x32_bf16 v[52:55], v[108:111], v[68:71], v[52:55]\n\t"
        "ds_read_b128 v[96:99], v5 offset:19392\n\t"
        "s_waitcnt vmcnt(2) lgkmcnt(7)\n\t"
        "v_xor_b32 v112, s73, v112\n\t"
        "v_xor_b32 v113, v6, v113\n\t"
        "v_xor_b32 v116, s73, v116\n\t"
        "v_xor_b32 v117, v6, v117\n\t"
        "v_mfma_f32_16x16x32_bf16 v[56:59], v[104:107], v[12:15], v[56:59]\n\t"
        "v_mfma_f32_16x16x32_bf16 v[60:63], v[108:111], v[12:15], v[60:63]\n\t"
        "ds_read_b128 v[100:103], v5 offset:27840\n\t"
        "v_mfma_f32_16x16x32_bf16 v[24:27], v[112:115], v[88:91], v[24:27]\n\t"
        "v_mfma_f32_16x16x32_bf16 v[28:31], v[116:119], v[88:91], v[28:31]\n\t"
        "v_or3_b32 v7, v7, v112, v113\n\t"
        "ds_read_b128 v[64:67], v5 offset:36288\n\t"
        "s_waitcnt lgkmcnt(7)\n\t"
        "v_mfma_f32_16x16x32_bf16 v[32:35], v[112:115], v[92:95], v[32:35]\n\t"
        "v_mfma_f32_16x16x32_bf16 v[36:39], v[116:119], v[92:95], v[36:39]\n\t"
        "v_or3_b32 v7, v7, v116, v117\n\t"
        "v_mfma_f32_16x16x32_bf16 v[40:43], v[112:115], v[16:19], v[40:43]\n\t"
        "v_mfma_f32_16x16x32_bf16 v[44:47], v[116:119], v[16:19], v[44:47]\n\t"
        "s_waitcnt lgkmcnt(5)\n\t"
        "v_mfma_f32_16x16x32_bf16 v[48:51], v[112:115], v[20:23], v[48:51]\n\t"
        "v_mfma_f32_16x16x32_bf16 v[52:55], v[116:119], v[20:23], v[52:55]\n\t"
        "s_waitcnt vmcnt(0) lgkmcnt(4)\n\t"
        "v_xor_b32 v120, s73, v120\n\t"
        "v_xor_b32 v121, v6, v121\n\t"
        "v_xor_b32 v124, s73, v124\n\t"
        "v_xor_b32 v125, v6, v125\n\t"
        "v_mfma_f32_16x16x32_bf16 v[56:59], v[112:115], v[8:11], v[56:59]\n\t"
        "v_mfma_f32_16x16x32_bf16 v[60:63], v[116:119], v[8:11], v[60:63]\n\t"
        "v_mfma_f32_16x16x32_bf16 v[24:27], v[120:123], v[80:83], v[24:27]\n\t"
        "v_mfma_f32_16x16x32_bf16 v[28:31], v[124:127], v[80:83], v[28:31]\n\t"
        "v_or3_b32 v7, v7, v120, v121\n\t"
        "s_waitcnt lgkmcnt(3)\n\t"
        "v_mfma_f32_16x16x32_bf16 v[32:35], v[120:123], v[84:87], v[32:35]\n\t"
        "v_mfma_f32_16x16x32_bf16 v[36:39], v[124:127], v[84:87], v[36:39]\n\t"
        "v_or3_b32 v7, v7, v124, v125\n\t"
        "v_and_b32 v7, s49, v7\n\t"
        "v_cmp_ne_u32 vcc, 0, v7\n\t"
        "s_cbranch_vccnz .L7_qr\n"
        ".L7_qd:\n\t"
        "s_mov_b64 exec, 1\n\t"
        "ds_add_u32 v2, v3 offset:12\n\t"
        "s_mov_b64 exec, -1\n\t"
        "v_and_b32 v6, 15, v1\n\t"
        "v_mul_u32_u24 v6, 272, v6\n\t"
        "v_and_b32 v7, 48, v1\n\t"
        "s_lshl_b32 s78, s80, 6\n\t"
        "v_add3_u32 v6, v6, v7, s78\n\t"
        "s_waitcnt lgkmcnt(2)\n\t"
        "v_mfma_f32_16x16x32_bf16 v[40:43], v[120:123], v[96:99], v[40:43]\n\t"
        "v_mfma_f32_16x16x32_bf16 v[44:47], v[124:127], v[96:99], v[44:47]\n\t"
        "v_cvt_pk_bf16_f32 v24, v24, v25\n\t"
        "v_cvt_pk_bf16_f32 v25, v26, v27\n\t"
        "v_cvt_pk_bf16_f32 v26, v28, v29\n\t"
        "v_cvt_pk_bf16_f32 v27, v30, v31\n\t"
        "s_waitcnt lgkmcnt(0)\n\t"
        "v_mfma_f32_16x16x32_bf16 v[48:51], v[120:123], v[100:103], v[48:51]\n\t"
        "v_mfma_f32_16x16x32_bf16 v[52:55], v[124:127], v[100:103], v[52:55]\n\t"
        "v_cvt_pk_bf16_f32 v32, v32, v33\n\t"
        "v_cvt_pk_bf16_f32 v33, v34, v35\n\t"
        "v_cvt_pk_bf16_f32 v34, v36, v37\n\t"
        "v_cvt_pk_bf16_f32 v35, v38, v39\n\t"
        "v_mfma_f32_16x16x32_bf16 v[56:59], v[120:123], v[64:67], v[56:59]\n\t"
        "v_mfma_f32_16x16x32_bf16 v[60:63], v[124:127], v[64:67], v[60:63]\n\t"
        "v_cvt_pk_bf16_f32 v40, v40, v41\n\t"
        "v_cvt_pk_bf16_f32 v41, v42, v43\n\t"
        "v_cvt_pk_bf16_f32 v42, v44, v45\n\t"
        "v_cvt_pk_bf16_f32 v43, v46, v47\n\t"
        "ds_read_b32 v0, v2 offset:12\n\t"
        "v_cvt_pk_bf16_f32 v48, v48, v49\n\t"
        "v_cvt_pk_bf16_f32 v49, v50, v51\n\t"
        "v_cvt_pk_bf16_f32 v50, v52, v53\n\t"
        "v_cvt_pk_bf16_f32 v51, v54, v55\n\t"
        "v_cvt_pk_bf16_f32 v56, v56, v57\n\t"
        "v_cvt_pk_bf16_f32 v57, v58, v59\n\t"
        "v_cvt_pk_bf16_f32 v58, v60, v61\n\t"
        "v_cvt_pk_bf16_f32 v59, v62, v63\n\t"
        "s_waitcnt lgkmcnt(0)\n\t"
        "v_cmp_gt_u16 vcc, 4, v0\n\t"
        "s_cbranch_vccz .L7_qx\n"
        ".L7_w52:\n\t"
        "ds_read_b32 v0, v2 offset:12\n\t"
        "s_waitcnt lgkmcnt(0)\n\t"
        "v_cmp_gt_u16 vcc, 4, v0\n\t"
        "s_cbranch_vccz .L7_d53\n\t"
        "s_sleep 0\n\t"
        "s_sub_i32 s83, s83, 1\n\t"
        "s_cmp_gt_i32 s83, 0\n\t"
        "s_cbranch_scc1 .L7_w52\n"
        ".L7_d53:\n"
        ".L7_qx:\n\t"
        "ds_write_b128 v6, v[24:27] offset:2048\n\t"
        "ds_write_b128 v6, v[32:35] offset:6400\n\t"
        "ds_write_b128 v6, v[40:43] offset:10752\n\t"
        "ds_write_b128 v6, v[48:51] offset:15104\n\t"
        "ds_write_b128 v6, v[56:59] offset:19456\n\t"
        "s_mov_b64 exec, 1\n\t"
        "ds_add_u32 v2, v3 offset:16\n\t"
        "s_mov_b64 exec, -1\n\t"
        "v_lshrrev_b32 v10, 3, v1\n\t"
        "v_lshlrev_b32 v10, 9, v10\n\t"
        "v_and_b32 v11, 7, v1\n\t"
        "v_lshl_add_u32 v10, v11, 4, v10\n\t"
        "v_and_b32 v11, 16, v1\n\t"
        "v_lshlrev_b32 v11, 2, v11\n\t"
        "v_xor_b32 v10, v10, v11\n\t"
        "s_load_dwordx4 s[24:27], %2, 32\n\t"
        "s_lshl_b32 s78, s80, 14\n\t"
        "s_add_u32 m0, s78, 68864\n\t"
        "s_lshl_b32 s78, s70, 9\n\t"
        "s_add_u32 s78, s78, s80\n\t"
        "s_lshl_b32 s78, s78, 7\n"
        ".L7_w54:\n\t"
        "ds_read_b32 v0, v2 offset:24\n\t"
        "s_waitcnt lgkmcnt(0)\n\t"
        "v_cmp_gt_u16 vcc, 8, v0\n\t"
        "s_cbranch_vccz .L7_d55\n\t"
        "s_sleep 1\n\t"
        "s_sub_i32 s83, s83, 1\n\t"
        "s_cmp_gt_i32 s83, 0\n\t"
        "s_cbranch_scc1 .L7_w54\n"
        ".L7_d55:\n\t"
        "s_setprio 3\n\t"
        "buffer_load_dwordx4 v10, s[24:27], s78 offen lds\n\t"
        "s_add_u32 m0, m0, 0x400\n\t"
        "s_add_u32 s78, s78, 0x1000\n\t"
        "buffer_load_dwordx4 v10, s[24:27], s78 offen lds\n\t"
        "s_add_u32 m0, m0, 0x400\n\t"
        "s_add_u32 s78, s78, 0x1000\n\t"
        "buffer_load_dwordx4 v10, s[24:27], s78 offen lds\n\t"
        "s_add_u32 m0, m0, 0x400\n\t"
        "s_add_u32 s78, s78, 0x1000\n\t"
        "buffer_load_dwordx4 v10, s[24:27], s78 offen lds\n\t"
        "s_add_u32 m0, m0, 0x400\n\t"
        "s_add_u32 s78, s78, 0x1000\n\t"
        "buffer_load_dwordx4 v10, s[24:27], s78 offen lds\n\t"
        "s_add_u32 m0, m0, 0x400\n\t"
        "s_add_u32 s78, s78, 0x1000\n\t"
        "buffer_load_dwordx4 v10, s[24:27], s78 offen lds\n\t"
        "s_add_u32 m0, m0, 0x400\n\t"
        "s_add_u32 s78, s78, 0x1000\n\t"
        "buffer_load_dwordx4 v10, s[24:27], s78 offen lds\n\t"
        "s_add_u32 m0, m0, 0x400\n\t"
        "s_add_u32 s78, s78, 0x1000\n\t"
        "buffer_load_dwordx4 v10, s[24:27], s78 offen lds\n\t"
        "s_add_u32 m0, m0, 0x400\n\t"
        "s_add_u32 s78, s78, 0x1000\n\t"
        "buffer_load_dwordx4 v10, s[24:27], s78 offen lds\n\t"
        "s_add_u32 m0, m0, 0x400\n\t"
        "s_add_u32 s78, s78, 0x1000\n\t"
        "buffer_load_dwordx4 v10, s[24:27], s78 offen lds\n\t"
        "s_add_u32 m0, m0, 0x400\n\t"
        "s_add_u32 s78, s78, 0x1000\n\t"
        "buffer_load_dwordx4 v10, s[24:27], s78 offen lds\n\t"
        "s_add_u32 m0, m0, 0x400\n\t"
        "s_add_u32 s78, s78, 0x1000\n\t"
        "buffer_load_dwordx4 v10, s[24:27], s78 offen lds\n\t"
        "s_add_u32 m0, m0, 0x400\n\t"
        "s_add_u32 s78, s78, 0x1000\n\t"
        "buffer_load_dwordx4 v10, s[24:27], s78 offen lds\n\t"
        "s_add_u32 m0, m0, 0x400\n\t"
        "s_add_u32 s78, s78, 0x1000\n\t"
        "buffer_load_dwordx4 v10, s[24:27], s78 offen lds\n\t"
        "s_add_u32 m0, m0, 0x400\n\t"
        "s_add_u32 s78, s78, 0x1000\n\t"
        "buffer_load_dwordx4 v10, s[24:27], s78 offen lds\n\t"
        "s_add_u32 m0, m0, 0x400\n\t"
        "s_add_u32 s78, s78, 0x1000\n\t"
        "buffer_load_dwordx4 v10, s[24:27], s78 offen lds\n\t"
        "s_waitcnt vmcnt(0)\n\t"
        "s_mov_b64 exec, 1\n\t"
        "ds_add_u32 v2, v3 offset:28\n\t"
        "s_mov_b64 exec, -1\n\t"
        "s_setprio 0\n"
        ".L7_w56:\n\t"
        "ds_read_b32 v0, v2 offset:36\n\t"
        "s_waitcnt lgkmcnt(0)\n\t"
        "v_cmp_gt_u16 vcc, 4, v0\n\t"
        "s_cbranch_vccz .L7_d57\n\t"
        "s_sleep 2\n\t"
        "s_sub_i32 s83, s83, 1\n\t"
        "s_cmp_gt_i32 s83, 0\n\t"
        "s_cbranch_scc1 .L7_w56\n"
        ".L7_d57:\n\t"
        "v_cmp_lt_u32 vcc, 0xfffff, v0\n\t"
        "s_cbranch_vccnz .L7_exh\n\t"
        "s_cmp_eq_u32 s69, 0\n\t"
        "s_cbranch_scc0 .L7_hz\n\t"
        "s_lshr_b32 s47, s68, 20\n\t"
        "s_add_u32 s47, s47, s68\n\t"
        "s_and_b32 s47, s47, 0xfff\n\t"
        "s_mul_i32 s46, s68, 0x9e377000\n\t"
        "s_or_b32 s46, s46, s47\n\t"
        "v_mov_b32 v4, s46\n\t"
        "s_xor_b32 s44, s70, 1\n\t"
        "s_lshl1_add_u32 s44, %3, s44\n\t"
        "s_lshl_b32 s44, s44, 10\n\t"
        "s_add_u32 s44, s44, 262144\n\t"
        "s_xor_b32 s45, s44, 0xc00\n"
        ".L7_w58:\n\t"
        "ds_read_b32 v0, v2 offset:96\n\t"
        "s_waitcnt lgkmcnt(0)\n\t"
        "v_cmp_gt_u16 vcc, 1, v0\n\t"
        "s_cbranch_vccz .L7_d59\n\t"
        "s_sleep 8\n\t"
        "s_sub_i32 s83, s83, 1\n\t"
        "s_cmp_gt_i32 s83, 0\n\t"
        "s_cbranch_scc1 .L7_w58\n"
        ".L7_d59:\n\t"
        "s_setprio 3\n"
        ".L7_w60:\n\t"
        "ds_read_b32 v0, v2 offset:96\n\t"
        "s_waitcnt lgkmcnt(0)\n\t"
        "v_cmp_gt_u16 vcc, 6, v0\n\t"
        "s_cbranch_vccz .L7_d61\n\t"
        "s_sleep 1\n\t"
        "s_sub_i32 s83, s83, 1\n\t"
        "s_cmp_gt_i32 s83, 0\n\t"
        "s_cbranch_scc1 .L7_w60\n"
        ".L7_d61:\n\t"
        "s_mov_b64 exec, 1\n\t"
        "buffer_store_dword v4, off, s[20:23], s44 offset:12 sc1\n\t"
        "s_mov_b32 s83, 64\n\t"
        "s_sleep 12\n"
        ".L7_h1:\n\t"
        "buffer_load_dword v0, off, s[20:23], s45 offset:12 sc1\n\t"
        "s_waitcnt vmcnt(0)\n\t"
        "v_cmp_ne_u32 vcc, s46, v0\n\t"
        "s_cbranch_vccz .L7_h2\n\t"
        "s_sleep 1\n\t"
        "s_sub_i32 s83, s83, 1\n\t"
        "s_cmp_gt_i32 s83, 0\n\t"
        "s_cbranch_scc1 .L7_h1\n"
        ".L7_h2:\n\t"
        "s_mov_b64 exec, 1\n\t"
        "ds_add_u32 v2, v3 offset:100\n\t"
        "s_mov_b64 exec, -1\n"
        ".L7_hz:\n\t"
        "s_endpgm\n"
        ".L7_exh:\n\t"
        "s_mov_b64 exec, 1\n\t"
        "ds_add_u32 v2, v3 offset:100\n\t"
        "s_mov_b64 exec, -1\n\t"
        "s_branch .L7_exit\n"
        ".L7_G2:\n\t"
        "s_barrier\n\t"
        "s_setprio 1\n\t"
        "v_lshlrev_b32 v5, 4, v1\n\t"
        "s_mul_i32 s82, s76, 13\n\t"
        "s_lshr_b32 s82, s82, 6\n\t"
        "s_mul_i32 s82, s82, 5\n\t"
        "s_sub_u32 s82, s76, s82\n\t"
        "s_lshl2_add_u32 s78, s82, s69\n\t"
        "s_lshl_b32 s78, s78, 10\n\t"
        "s_add_u32 m0, s78, 46336\n\t"
        "s_add_u32 s82, s82, 1\n\t"
        "s_cmp_eq_u32 s82, 5\n\t"
        "buffer_load_dwordx4 v5, s[56:59], s78 offen lds\n\t"
        "s_cselect_b32 s82, 0, s82\n\t"
        "v_lshrrev_b32 v18, 3, v1\n\t"
        "v_lshlrev_b32 v18, 14, v18\n\t"
        "v_and_b32 v6, 0x70, v5\n\t"
        "s_lshl_b32 s78, s69, 19\n\t"
        "s_lshl_b32 s79, s71, 7\n\t"
        "s_add_u32 s78, s78, s79\n\t"
        "v_add3_u32 v18, v18, v6, s78\n\t"
        "s_mov_b32 s24, 1048576\n\t"
        "s_mov_b32 s25, 1179648\n\t"
        "s_mov_b32 s26, 1310720\n\t"
        "s_mov_b32 s27, 1441792\n\t"
        "v_and_b32 v6, 0xf0, v5\n\t"
        "s_lshl_b32 s78, s70, 8\n\t"
        "v_add_u32 v14, s78, v6\n\t"
        "v_mov_b32 v7, 113860\n\t"
        "s_movk_i32 s38, 196\n\t"
        "s_mov_b32 s39, 12845056\n\t"
        "s_lshl_b32 s33, s69, 3\n\t"
        "s_lshl_b32 s78, 1, s33\n\t"
        "v_mov_b32 v29, s78\n\t"
        "s_sub_u32 s32, s78, 1\n\t"
        "s_and_b32 s32, s32, 0x01010101\n\t"
        "s_mov_b32 s35, 0xffffff\n\t"
        "s_mov_b32 s40, 0x13090503\n\t"
        "s_mov_b32 s41, 23\n\t"
        "s_cmp_eq_u32 s69, 1\n\t"
        "s_cselect_b32 s40, 0x12080402, s40\n\t"
        "s_cselect_b32 s41, 22, s41\n\t"
        "s_cmp_eq_u32 s69, 2\n\t"
        "s_cselect_b32 s40, 0x100e0a06, s40\n\t"
        "s_cselect_b32 s41, 20, s41\n\t"
        "s_cmp_eq_u32 s69, 3\n\t"
        "s_cselect_b32 s40, 0x110f0b07, s40\n\t"
        "s_cselect_b32 s41, 21, s41\n\t"
        "s_mul_i32 s41, s41, 0x01010101\n\t"
        "s_and_b32 s78, s40, 4\n\t"
        "v_mov_b32 v61, s78\n\t"
        "v_lshrrev_b32 v12, 4, v1\n\t"
        "s_bfe_u32 s78, s40, 0x80000\n\t"
        "s_lshl_b32 s78, s78, 2\n\t"
        "v_add_u32 v13, s78, v12\n\t"
        "v_lshlrev_b32 v24, 2, v13\n\t"
        "v_lshlrev_b32 v15, 4, v13\n\t"
        "v_and_b32 v15, 0xf0, v15\n\t"
        "v_xor_b32 v15, v15, v6\n\t"
        "v_lshl_add_u32 v56, v13, 8, v15\n\t"
        "s_bfe_u32 s78, s40, 0x80008\n\t"
        "s_lshl_b32 s78, s78, 2\n\t"
        "v_add_u32 v13, s78, v12\n\t"
        "v_lshlrev_b32 v25, 2, v13\n\t"
        "v_lshlrev_b32 v15, 4, v13\n\t"
        "v_and_b32 v15, 0xf0, v15\n\t"
        "v_xor_b32 v15, v15, v6\n\t"
        "v_lshl_add_u32 v57, v13, 8, v15\n\t"
        "s_bfe_u32 s78, s40, 0x80010\n\t"
        "s_lshl_b32 s78, s78, 2\n\t"
        "v_add_u32 v13, s78, v12\n\t"
        "v_lshlrev_b32 v26, 2, v13\n\t"
        "v_lshlrev_b32 v15, 4, v13\n\t"
        "v_and_b32 v15, 0xf0, v15\n\t"
        "v_xor_b32 v15, v15, v6\n\t"
        "v_lshl_add_u32 v58, v13, 8, v15\n\t"
        "s_bfe_u32 s78, s40, 0x80018\n\t"
        "s_lshl_b32 s78, s78, 2\n\t"
        "v_add_u32 v13, s78, v12\n\t"
        "v_lshlrev_b32 v27, 2, v13\n\t"
        "v_lshlrev_b32 v15, 4, v13\n\t"
        "v_and_b32 v15, 0xf0, v15\n\t"
        "v_xor_b32 v15, v15, v6\n\t"
        "v_lshl_add_u32 v59, v13, 8, v15\n\t"
        "s_bfe_u32 s78, s41, 0x80000\n\t"
        "s_lshl_b32 s78, s78, 2\n\t"
        "v_add_u32 v13, s78, v12\n\t"
        "v_lshlrev_b32 v28, 2, v13\n\t"
        "v_lshlrev_b32 v15, 4, v13\n\t"
        "v_and_b32 v15, 0xf0, v15\n\t"
        "v_xor_b32 v15, v15, v6\n\t"
        "v_lshl_add_u32 v60, v13, 8, v15\n\t"
        "v_and_b32 v20, 28, v1\n\t"
        "v_lshlrev_b32 v20, 1, v20\n\t"
        "v_lshrrev_b64 v[12:13], v20, s[40:41]\n\t"
        "v_and_b32 v13, 0xff, v12\n\t"
        "v_and_b32 v12, 3, v1\n\t"
        "v_lshl_add_u32 v13, v13, 2, v12\n\t"
        "v_lshlrev_b32 v31, 2, v13\n\t"
        "s_and_b32 s78, s67, 0x10000\n\t"
        "s_or_b32 s78, s78, 1\n\t"
        "v_mov_b32 v30, s78\n\t"
        "s_mov_b32 s36, 0\n\t"
        "v_and_b32 v0, 48, v5\n\t"
        "s_mov_b32 s78, 0x0f0e\n\t"
        "s_mov_b32 s79, 0\n\t"
        "v_lshlrev_b64 v[8:9], v0, s[78:79]\n\t"
        "v_xor_b32 v8, 0x0c0c0c0c, v8\n\t"
        "v_xor_b32 v9, 0x0c0c0c0c, v9\n\t"
        "s_movk_i32 s48, 0x200\n\t"
        "s_mov_b32 s37, 0x01010101\n\t"
        "s_mov_b32 s49, 0x05040100\n\t"
        "s_getpc_b64 s[50:51]\n"
        ".L7_pc:\n\t"
        "s_add_u32 s52, s50, .L7_ce-.L7_pc\n\t"
        "s_addc_u32 s53, s51, 0\n\t"
        "s_add_u32 s42, s50, .L7_ev-.L7_pc\n\t"
        "s_addc_u32 s43, s51, 0\n\t"
        "s_add_u32 s50, s50, .L7_ie-.L7_pc\n\t"
        "s_addc_u32 s51, s51, 0\n\t"
        "s_cmp_eq_u32 s80, 10\n\t"
        "s_cselect_b32 s80, 0, s80\n\t"
        "s_mul_i32 s78, s80, 0x1080\n\t"
        "s_add_u32 m0, s78, s81\n\t"
        "s_lshr_b32 s78, s80, 1\n\t"
        "s_lshl1_add_u32 s78, s78, s80\n\t"
        "s_lshl_b32 s78, s78, 11\n\t"
        "buffer_load_dwordx4 v4, s[16:19], s78 offen lds\n\t"
        "s_add_u32 s80, s80, 1\n\t"
        "s_mul_i32 s78, s80, 0x1080\n\t"
        "s_add_u32 m0, s78, s81\n\t"
        "s_lshr_b32 s78, s80, 1\n\t"
        "s_lshl1_add_u32 s78, s78, s80\n\t"
        "s_lshl_b32 s78, s78, 11\n\t"
        "buffer_load_dwordx4 v4, s[16:19], s78 offen lds\n\t"
        "s_add_u32 s80, s80, 1\n\t"
        "s_mul_i32 s78, s80, 0x1080\n\t"
        "s_add_u32 m0, s78, s81\n\t"
        "s_lshr_b32 s78, s80, 1\n\t"
        "s_lshl1_add_u32 s78, s78, s80\n\t"
        "s_lshl_b32 s78, s78, 11\n\t"
        "buffer_load_dwordx4 v4, s[16:19], s78 offen lds\n\t"
        "s_add_u32 s80, s80, 1\n\t"
        "s_mul_i32 s78, s80, 0x1080\n\t"
        "s_add_u32 m0, s78, s81\n\t"
        "s_lshr_b32 s78, s80, 1\n\t"
        "s_lshl1_add_u32 s78, s78, s80\n\t"
        "s_lshl_b32 s78, s78, 11\n\t"
        "buffer_load_dwordx4 v4, s[16:19], s78 offen lds\n\t"
        "s_add_u32 s80, s80, 1\n\t"
        "s_mul_i32 s78, s80, 0x1080\n\t"
        "s_add_u32 m0, s78, s81\n\t"
        "s_lshr_b32 s78, s80, 1\n\t"
        "s_lshl1_add_u32 s78, s78, s80\n\t"
        "s_lshl_b32 s78, s78, 11\n\t"
        "buffer_load_dwordx4 v4, s[16:19], s78 offen lds\n\t"
        "s_add_u32 s80, s80, 1\n\t"
        "s_lshl2_add_u32 s78, s82, s69\n\t"
        "s_lshl_b32 s78, s78, 10\n\t"
        "s_add_u32 m0, s78, 46336\n\t"
        "s_add_u32 s82, s82, 1\n\t"
        "s_cmp_eq_u32 s82, 5\n\t"
        "buffer_load_dwordx4 v5, s[56:59], s78 offen lds\n\t"
        "s_cselect_b32 s82, 0, s82\n\t"
        "s_lshl2_add_u32 s78, s82, s69\n\t"
        "s_lshl_b32 s78, s78, 10\n\t"
        "s_add_u32 m0, s78, 46336\n\t"
        "s_add_u32 s82, s82, 1\n\t"
        "s_cmp_eq_u32 s82, 5\n\t"
        "buffer_load_dwordx4 v5, s[56:59], s78 offen lds\n\t"
        "s_cselect_b32 s82, 0, s82\n\t"
        "s_lshl2_add_u32 s78, s82, s69\n\t"
        "s_lshl_b32 s78, s78, 10\n\t"
        "s_add_u32 m0, s78, 46336\n\t"
        "s_add_u32 s82, s82, 1\n\t"
        "s_cmp_eq_u32 s82, 5\n\t"
        "buffer_load_dwordx4 v5, s[56:59], s78 offen lds\n\t"
        "s_cselect_b32 s82, 0, s82\n\t"
        "s_lshl2_add_u32 s78, s82, s69\n\t"
        "s_lshl_b32 s78, s78, 10\n\t"
        "s_add_u32 m0, s78, 46336\n\t"
        "s_add_u32 s82, s82, 1\n\t"
        "s_cmp_eq_u32 s82, 5\n\t"
        "buffer_load_dwordx4 v5, s[56:59], s78 offen lds\n\t"
        "s_cselect_b32 s82, 0, s82\n\t"
        "s_mov_b64 exec, 1\n\t"
        "ds_add_u32 v2, v3 offset:80\n\t"
        "s_mov_b64 exec, -1\n\t"
        "s_waitcnt vmcnt(4)\n\t"
        "s_mov_b64 exec, 1\n\t"
        "ds_add_u32 v2, v3 offset:0\n\t"
        "s_mov_b64 exec, -1\n"
        ".L7_w62:\n\t"
        "ds_read_b64 v[12:13], v2 offset:72\n\t"
        "s_waitcnt lgkmcnt(0)\n\t"
        "v_cmp_gt_u16 vcc, 1, v12\n\t"
        "s_cbranch_vccz .L7_d63\n\t"
        "s_sleep 1\n\t"
        "s_sub_i32 s83, s83, 1\n\t"
        "s_cmp_gt_i32 s83, 0\n\t"
        "s_cbranch_scc1 .L7_w62\n"
        ".L7_d63:\n\t"
        "v_readfirstlane_b32 s68, v13\n"
        ".L7_w64:\n\t"
        "ds_read_b32 v0, v2 offset:8\n\t"
        "s_waitcnt lgkmcnt(0)\n\t"
        "v_cmp_gt_u16 vcc, 2, v0\n\t"
        "s_cbranch_vccz .L7_d65\n\t"
        "s_sleep 1\n\t"
        "s_sub_i32 s83, s83, 1\n\t"
        "s_cmp_gt_i32 s83, 0\n\t"
        "s_cbranch_scc1 .L7_w64\n"
        ".L7_d65:\n\t"
        "ds_read_b32 v5, v31 offset:512\n\t"
        "ds_read_b32 v24, v24 offset:512\n\t"
        "ds_read_b32 v25, v25 offset:512\n\t"
        "ds_read_b32 v26, v26 offset:512\n\t"
        "ds_read_b32 v27, v27 offset:512\n\t"
        "ds_read_b32 v28, v28 offset:512\n\t"
        "s_waitcnt lgkmcnt(0)\n\t"
        "v_dot4_u32_u8 v31, v5, s38, v7\n\t"
        "s_waitcnt vmcnt(0)\n\t"
        "s_mov_b64 exec, 1\n\t"
        "ds_add_u32 v2, v3 offset:4\n\t"
        "s_mov_b64 exec, -1\n\t"
        "s_setprio 3\n\t"
        "s_lshl_b32 s44, s68, 24\n\t"
        "s_xor_b32 s45, s44, 0x5a000000\n\t"
        "s_mul_i32 s46, s68, 0x01010000\n\t"
        "s_mul_i32 s47, s68, 0x02000200\n\t"
        "s_and_b32 s47, s47, 0xff000000\n\t"
        "s_or_b32 s47, s47, 0xff0000\n"
        ".L7_w66:\n\t"
        "ds_read_b32 v0, v2 offset:92\n\t"
        "s_waitcnt lgkmcnt(0)\n\t"
        "v_cmp_gt_u16 vcc, 1, v0\n\t"
        "s_cbranch_vccz .L7_d67\n\t"
        "s_sleep 1\n\t"
        "s_sub_i32 s83, s83, 1\n\t"
        "s_cmp_gt_i32 s83, 0\n\t"
        "s_cbranch_scc1 .L7_w66\n"
        ".L7_d67:\n\t"
        "v_readfirstlane_b32 s78, v0\n\t"
        "s_sub_u32 s78, s78, 1\n\t"
        "s_add_u32 s77, s74, s78\n"
        ".L7_g68:\n\t"
        "s_memrealtime s[78:79]\n\t"
        "s_waitcnt lgkmcnt(0)\n\t"
        "s_sub_u32 s78, s78, s77\n\t"
        "s_cmp_ge_i32 s78, 315\n\t"
        "s_cbranch_scc1 .L7_h69\n\t"
        "s_sleep 1\n\t"
        "s_branch .L7_g68\n"
        ".L7_h69:\n\t"
        "s_branch .L7_ll\n"
        ".L7_again:\n\t"
        "s_waitcnt lgkmcnt(0)\n\t"
        "s_sleep 8\n\t"
        "s_sub_i32 s83, s83, 16\n\t"
        "s_cmp_gt_i32 s83, 0\n\t"
        "s_cbranch_scc0 .L7_got\n"
        ".L7_ll:\n\t"
        "buffer_load_dwordx4 v[64:67], v18, s[20:23], s24 offen sc1\n\t"
        "buffer_load_dwordx4 v[68:71], v18, s[20:23], s25 offen sc1\n\t"
        "buffer_load_dwordx4 v[72:75], v18, s[20:23], s26 offen sc1\n\t"
        "buffer_load_dwordx4 v[76:79], v18, s[20:23], s27 offen sc1\n"
        ".L7_w70:\n\t"
        "ds_read_b32 v0, v2 offset:4\n\t"
        "s_waitcnt lgkmcnt(0)\n\t"
        "v_cmp_gt_u16 vcc, 4, v0\n\t"
        "s_cbranch_vccz .L7_d71\n\t"
        "s_sleep 1\n\t"
        "s_sub_i32 s83, s83, 1\n\t"
        "s_cmp_gt_i32 s83, 0\n\t"
        "s_cbranch_scc1 .L7_w70\n"
        ".L7_d71:\n\t"
        "s_waitcnt vmcnt(3)\n\t"
        "v_xor_b32 v5, s44, v64\n\t"
        "v_bitop3_b32 v5, v65, v5, s45 bitop3:0xde\n\t"
        "v_bitop3_b32 v5, v66, v5, s46 bitop3:0xde\n\t"
        "v_xor_b32 v4, s47, v67\n\t"
        "v_lshlrev_b32_sdwa v112, 1, v64 dst_sel:DWORD dst_unused:UNUSED_PAD src0_sel:DWORD src1_sel:WORD_0\n\t"
        "ds_read_u16 v112, v112 offset:46336\n\t"
        "v_lshlrev_b32_sdwa v113, 1, v65 dst_sel:DWORD dst_unused:UNUSED_PAD src0_sel:DWORD src1_sel:WORD_0\n\t"
        "ds_read_u16 v113, v113 offset:46336\n\t"
        "v_lshlrev_b32_sdwa v114, 1, v66 dst_sel:DWORD dst_unused:UNUSED_PAD src0_sel:DWORD src1_sel:WORD_0\n\t"
        "ds_read_u16 v114, v114 offset:46336\n\t"
        "v_dot4_u32_u8 v80, v64, s39, v7\n\t"
        "v_dot4_u32_u8 v81, v65, s39, v7\n\t"
        "v_dot4_u32_u8 v82, v66, s39, v7\n\t"
        "s_waitcnt vmcnt(2)\n\t"
        "v_bitop3_b32 v5, v68, v5, s44 bitop3:0xde\n\t"
        "v_bitop3_b32 v5, v69, v5, s45 bitop3:0xde\n\t"
        "v_bitop3_b32 v5, v70, v5, s46 bitop3:0xde\n\t"
        "v_bitop3_b32 v4, v71, v4, s47 bitop3:0xde\n\t"
        "v_lshlrev_b32_sdwa v116, 1, v68 dst_sel:DWORD dst_unused:UNUSED_PAD src0_sel:DWORD src1_sel:WORD_0\n\t"
        "ds_read_u16 v116, v116 offset:46336\n\t"
        "v_lshlrev_b32_sdwa v117, 1, v69 dst_sel:DWORD dst_unused:UNUSED_PAD src0_sel:DWORD src1_sel:WORD_0\n\t"
        "ds_read_u16 v117, v117 offset:46336\n\t"
        "v_lshlrev_b32_sdwa v118, 1, v70 dst_sel:DWORD dst_unused:UNUSED_PAD src0_sel:DWORD src1_sel:WORD_0\n\t"
        "ds_read_u16 v118, v118 offset:46336\n\t"
        "v_dot4_u32_u8 v84, v68, s39, v7\n\t"
        "v_dot4_u32_u8 v85, v69, s39, v7\n\t"
        "v_dot4_u32_u8 v86, v70, s39, v7\n\t"
        "s_waitcnt vmcnt(1)\n\t"
        "v_bitop3_b32 v5, v72, v5, s44 bitop3:0xde\n\t"
        "v_bitop3_b32 v5, v73, v5, s45 bitop3:0xde\n\t"
        "v_bitop3_b32 v5, v74, v5, s46 bitop3:0xde\n\t"
        "v_bitop3_b32 v4, v75, v4, s47 bitop3:0xde\n\t"
        "v_lshlrev_b32_sdwa v120, 1, v72 dst_sel:DWORD dst_unused:UNUSED_PAD src0_sel:DWORD src1_sel:WORD_0\n\t"
        "ds_read_u16 v120, v120 offset:46336\n\t"
        "v_lshlrev_b32_sdwa v121, 1, v73 dst_sel:DWORD dst_unused:UNUSED_PAD src0_sel:DWORD src1_sel:WORD_0\n\t"
        "ds_read_u16 v121, v121 offset:46336\n\t"
        "v_lshlrev_b32_sdwa v122, 1, v74 dst_sel:DWORD dst_unused:UNUSED_PAD src0_sel:DWORD src1_sel:WORD_0\n\t"
        "ds_read_u16 v122, v122 offset:46336\n\t"
        "v_dot4_u32_u8 v88, v72, s39, v7\n\t"
        "v_dot4_u32_u8 v89, v73, s39, v7\n\t"
        "v_dot4_u32_u8 v90, v74, s39, v7\n\t"
        "s_waitcnt vmcnt(0)\n\t"
        "v_bitop3_b32 v5, v76, v5, s44 bitop3:0xde\n\t"
        "v_bitop3_b32 v5, v77, v5, s45 bitop3:0xde\n\t"
        "v_bitop3_b32 v5, v78, v5, s46 bitop3:0xde\n\t"
        "v_bitop3_b32 v4, v79, v4, s47 bitop3:0xde\n\t"
        "v_lshlrev_b32_sdwa v124, 1, v76 dst_sel:DWORD dst_unused:UNUSED_PAD src0_sel:DWORD src1_sel:WORD_0\n\t"
        "ds_read_u16 v124, v124 offset:46336\n\t"
        "v_lshlrev_b32_sdwa v125, 1, v77 dst_sel:DWORD dst_unused:UNUSED_PAD src0_sel:DWORD src1_sel:WORD_0\n\t"
        "ds_read_u16 v125, v125 offset:46336\n\t"
        "v_lshlrev_b32_sdwa v126, 1, v78 dst_sel:DWORD dst_unused:UNUSED_PAD src0_sel:DWORD src1_sel:WORD_0\n\t"
        "ds_read_u16 v126, v126 offset:46336\n\t"
        "v_dot4_u32_u8 v92, v76, s39, v7\n\t"
        "v_dot4_u32_u8 v93, v77, s39, v7\n\t"
        "v_dot4_u32_u8 v94, v78, s39, v7\n\t"
        "v_and_b32 v6, 0xfe000000, v4\n\t"
        "v_or_b32 v5, v5, v6\n\t"
        "v_cmp_lt_u32 vcc, s35, v5\n\t"
        "s_cbranch_vccnz .L7_again\n"
        ".L7_got:\n\t"
        "v_and_b32 v6, s35, v4\n\t"
        "s_waitcnt lgkmcnt(2)\n\t"
        "ds_add_rtn_u32 v96, v80, v29 offset:192\n\t"
        "ds_add_rtn_u32 v97, v81, v29 offset:192\n\t"
        "ds_add_rtn_u32 v98, v82, v29 offset:192\n\t"
        "ds_add_rtn_u32 v100, v84, v29 offset:192\n\t"
        "ds_add_rtn_u32 v101, v85, v29 offset:192\n\t"
        "ds_add_rtn_u32 v102, v86, v29 offset:192\n\t"
        "ds_add_rtn_u32 v104, v88, v29 offset:192\n\t"
        "ds_add_rtn_u32 v105, v89, v29 offset:192\n\t"
        "ds_add_rtn_u32 v106, v90, v29 offset:192\n\t"
        "ds_add_rtn_u32 v108, v92, v29 offset:192\n\t"
        "ds_add_rtn_u32 v109, v93, v29 offset:192\n\t"
        "ds_add_rtn_u32 v110, v94, v29 offset:192\n\t"
        "v_cmp_ne_u32 vcc, 0, v6\n\t"
        "s_cbranch_vccnz .L7_x\n"
        ".L7_y:\n\t"
        "s_mov_b64 exec, 1\n\t"
        "ds_add_u32 v2, v3 offset:32\n\t"
        "s_mov_b64 exec, -1\n\t"
        "v_perm_b32 v112, v112, v64, s49\n\t"
        "v_perm_b32 v113, v113, v65, s49\n\t"
        "v_perm_b32 v114, v114, v66, s49\n\t"
        "v_perm_b32 v116, v116, v68, s49\n\t"
        "v_perm_b32 v117, v117, v69, s49\n\t"
        "v_perm_b32 v118, v118, v70, s49\n\t"
        "v_perm_b32 v120, v120, v72, s49\n\t"
        "v_perm_b32 v121, v121, v73, s49\n\t"
        "v_perm_b32 v122, v122, v74, s49\n\t"
        "v_perm_b32 v124, v124, v76, s49\n\t"
        "v_cmp_lt_u32 vcc, s35, v4\n\t"
        "s_cmp_lg_u64 vcc, 0\n\t"
        "s_cselect_b32 s78, 0x100000, 0\n\t"
        "v_or_b32 v30, s78, v30\n\t"
        "s_waitcnt lgkmcnt(1)\n\t"
        "v_perm_b32 v125, v125, v77, s49\n\t"
        "v_perm_b32 v126, v126, v78, s49\n\t"
        "v_bfe_u32 v96, v96, s33, 8\n\t"
        "v_bfe_u32 v97, v97, s33, 8\n\t"
        "v_bfe_u32 v98, v98, s33, 8\n\t"
        "v_bfe_u32 v100, v100, s33, 8\n\t"
        "v_bfe_u32 v101, v101, s33, 8\n\t"
        "v_bfe_u32 v102, v102, s33, 8\n\t"
        "v_bfe_u32 v104, v104, s33, 8\n\t"
        "v_bfe_u32 v105, v105, s33, 8\n\t"
        "v_bfe_u32 v106, v106, s33, 8\n\t"
        "v_bfe_u32 v108, v108, s33, 8\n\t"
        "v_bfe_u32 v109, v109, s33, 8\n\t"
        "v_bfe_u32 v110, v110, s33, 8\n"
        ".L7_w72:\n\t"
        "ds_read_b32 v0, v2 offset:32\n\t"
        "s_waitcnt lgkmcnt(0)\n\t"
        "v_cmp_gt_u16 vcc, 4, v0\n\t"
        "s_cbranch_vccz .L7_d73\n\t"
        "s_sleep 1\n\t"
        "s_sub_i32 s83, s83, 1\n\t"
        "s_cmp_gt_i32 s83, 0\n\t"
        "s_cbranch_scc1 .L7_w72\n"
        ".L7_d73:\n\t"
        "ds_read_b32 v32, v80 offset:192\n\t"
        "ds_read_b32 v33, v81 offset:192\n\t"
        "ds_read_b32 v34, v82 offset:192\n\t"
        "ds_read_b32 v35, v84 offset:192\n\t"
        "ds_read_b32 v36, v85 offset:192\n\t"
        "ds_read_b32 v37, v86 offset:192\n\t"
        "ds_read_b32 v38, v88 offset:192\n\t"
        "ds_read_b32 v39, v89 offset:192\n\t"
        "ds_read_b32 v40, v90 offset:192\n\t"
        "ds_read_b32 v41, v92 offset:192\n\t"
        "ds_read_b32 v42, v93 offset:192\n\t"
        "ds_read_b32 v43, v94 offset:192\n\t"
        "ds_read_b32 v6, v31 offset:192\n\t"
        "s_waitcnt lgkmcnt(10)\n\t"
        "v_dot4_u32_u8 v96, v32, s32, v96\n\t"
        "v_dot4_u32_u8 v97, v33, s32, v97\n\t"
        "v_dot4_u32_u8 v98, v34, s32, v98\n\t"
        "s_waitcnt lgkmcnt(7)\n\t"
        "v_dot4_u32_u8 v100, v35, s32, v100\n\t"
        "v_dot4_u32_u8 v101, v36, s32, v101\n\t"
        "v_dot4_u32_u8 v102, v37, s32, v102\n\t"
        "s_waitcnt lgkmcnt(4)\n\t"
        "v_dot4_u32_u8 v104, v38, s32, v104\n\t"
        "v_dot4_u32_u8 v105, v39, s32, v105\n\t"
        "v_dot4_u32_u8 v106, v40, s32, v106\n\t"
        "s_waitcnt lgkmcnt(1)\n\t"
        "v_dot4_u32_u8 v108, v41, s32, v108\n\t"
        "v_dot4_u32_u8 v109, v42, s32, v109\n\t"
        "v_dot4_u32_u8 v110, v43, s32, v110\n\t"
        "v_min_u32 v96, 47, v96\n\t"
        "v_min_u32 v97, 47, v97\n\t"
        "v_min_u32 v98, 47, v98\n\t"
        "v_min_u32 v100, 47, v100\n\t"
        "v_min_u32 v101, 47, v101\n\t"
        "v_min_u32 v102, 47, v102\n\t"
        "v_min_u32 v104, 47, v104\n\t"
        "v_min_u32 v105, 47, v105\n\t"
        "v_min_u32 v106, 47, v106\n\t"
        "v_min_u32 v108, 47, v108\n\t"
        "v_min_u32 v109, 47, v109\n\t"
        "v_min_u32 v110, 47, v110\n\t"
        "v_lshl_add_u32 v96, v96, 2, v80\n\t"
        "ds_write_b32 v96, v112\n\t"
        "v_lshl_add_u32 v97, v97, 2, v81\n\t"
        "ds_write_b32 v97, v113\n\t"
        "v_lshl_add_u32 v98, v98, 2, v82\n\t"
        "ds_write_b32 v98, v114\n\t"
        "v_lshl_add_u32 v100, v100, 2, v84\n\t"
        "ds_write_b32 v100, v116\n\t"
        "v_lshl_add_u32 v101, v101, 2, v85\n\t"
        "ds_write_b32 v101, v117\n\t"
        "v_lshl_add_u32 v102, v102, 2, v86\n\t"
        "ds_write_b32 v102, v118\n\t"
        "v_lshl_add_u32 v104, v104, 2, v88\n\t"
        "ds_write_b32 v104, v120\n\t"
        "v_lshl_add_u32 v105, v105, 2, v89\n\t"
        "ds_write_b32 v105, v121\n\t"
        "v_lshl_add_u32 v106, v106, 2, v90\n\t"
        "ds_write_b32 v106, v122\n\t"
        "v_lshl_add_u32 v108, v108, 2, v92\n\t"
        "ds_write_b32 v108, v124\n\t"
        "v_lshl_add_u32 v109, v109, 2, v93\n\t"
        "ds_write_b32 v109, v125\n\t"
        "v_lshl_add_u32 v110, v110, 2, v94\n\t"
        "ds_write_b32 v110, v126\n\t"
        "s_cmp_lg_u32 s36, 0\n\t"
        "s_cbranch_scc1 .L7_z\n"
        ".L7_zr:\n\t"
        "s_mov_b64 exec, 1\n\t"
        "ds_add_u32 v2, v30 offset:36\n\t"
        "s_mov_b64 exec, -1\n\t"
        "s_waitcnt lgkmcnt(13)\n\t"
        "v_dot4_u32_u8 v6, v6, s37, -1\n\t"
        "v_mov_b64 v[32:33], 0\n\t"
        "v_mov_b64 v[34:35], 0\n\t"
        "v_mov_b64 v[36:37], 0\n\t"
        "v_med3_i32 v6, v6, 0, 47\n\t"
        "v_lshrrev_b32 v6, 1, v6\n\t"
        "v_mov_b64 v[38:39], 0\n\t"
        "v_dot4_u32_u8 v19, v24, s38, v7\n\t"
        "v_max_u32_dpp v6, v6, v6 quad_perm:[1,0,3,2] row_mask:0xf bank_mask:0xf\n\t"
        "s_nop 1\n\t"
        "v_max_u32_dpp v6, v6, v6 quad_perm:[2,3,0,1] row_mask:0xf bank_mask:0xf\n\t"
        "s_nop 0\n\t"
        "v_readlane_b32 s24, v6, 0\n\t"
        "v_readlane_b32 s25, v6, 4\n\t"
        "v_readlane_b32 s26, v6, 8\n\t"
        "v_readlane_b32 s27, v6, 12\n\t"
        "v_readlane_b32 s28, v6, 16\n\t"
        "s_max_u32 s28, s28, 5\n\t"
        "s_mov_b32 s56, s24\n\t"
        "s_mov_b32 s57, s24\n\t"
        "s_mov_b32 s78, 0xf0000\n"
        ".L7_w74:\n\t"
        "ds_read_b32 v4, v2 offset:36\n\t"
        "ds_read_b32 v12, v31 offset:188\n\t"
        "s_waitcnt lgkmcnt(1)\n\t"
        "v_cmp_gt_u16 vcc, 4, v4\n\t"
        "s_cbranch_vccz .L7_d75\n\t"
        "s_waitcnt lgkmcnt(0)\n\t"
        "s_sleep 1\n\t"
        "s_sub_i32 s83, s83, 1\n\t"
        "s_cmp_gt_i32 s83, 0\n\t"
        "s_cbranch_scc1 .L7_w74\n"
        ".L7_d75:\n\t"
        "ds_read2_b32 v[40:41], v19 offset0:0 offset1:1\n\t"
        "s_sub_u32 s56, s56, 1\n\t"
        "s_cbranch_scc1 .L7_i76\n"
        ".L7_j77:\n\t"
        "ds_read2_b32 v[42:43], v19 offset0:2 offset1:3\n\t"
        "s_sub_u32 s56, s56, 1\n\t"
        "s_cbranch_scc1 .L7_i78\n"
        ".L7_j79:\n\t"
        "ds_read2_b32 v[44:45], v19 offset0:4 offset1:5\n\t"
        "s_sub_u32 s56, s56, 1\n\t"
        "s_cbranch_scc1 .L7_i80\n"
        ".L7_j81:\n\t"
        "ds_read2_b32 v[46:47], v19 offset0:6 offset1:7\n\t"
        "s_sub_u32 s56, s56, 1\n\t"
        "s_cbranch_scc1 .L7_i82\n"
        ".L7_j83:\n\t"
        "ds_read2_b32 v[48:49], v19 offset0:8 offset1:9\n\t"
        "s_sub_u32 s56, s56, 1\n\t"
        "s_cbranch_scc1 .L7_i84\n"
        ".L7_j85:\n\t"
        "ds_read2_b32 v[50:51], v19 offset0:10 offset1:11\n\t"
        "s_sub_u32 s56, s56, 1\n\t"
        "s_cbranch_scc1 .L7_i86\n"
        ".L7_j87:\n\t"
        "ds_read2_b32 v[52:53], v19 offset0:12 offset1:13\n\t"
        "s_sub_u32 s56, s56, 1\n\t"
        "s_cbranch_scc1 .L7_i88\n"
        ".L7_j89:\n\t"
        "ds_read2_b32 v[54:55], v19 offset0:14 offset1:15\n\t"
        "s_sub_u32 s56, s56, 1\n\t"
        "s_cbranch_scc1 .L7_i90\n"
        ".L7_j91:\n\t"
        "s_waitcnt lgkmcnt(8)\n\t"
        "v_and_or_b32 v12, v4, s78, v12\n\t"
        "v_cmp_ne_u32 vcc, 0xffff, v12\n\t"
        "s_cbranch_vccnz .L7_exit4\n\t"
        "s_waitcnt lgkmcnt(7)\n\t"
        "v_mad_u32_u16 v16, v40, s48, v14\n\t"
        "v_mad_u32_u16 v17, v41, s48, v14\n\t"
        "buffer_load_dwordx4 v[64:67], v16, s[16:19], 0 offen\n\t"
        "buffer_load_dwordx4 v[68:71], v17, s[16:19], 0 offen\n\t"
        "s_waitcnt lgkmcnt(6)\n\t"
        "v_mad_u32_u16 v16, v42, s48, v14\n\t"
        "v_mad_u32_u16 v17, v43, s48, v14\n\t"
        "buffer_load_dwordx4 v[72:75], v16, s[16:19], 0 offen\n\t"
        "buffer_load_dwordx4 v[76:79], v17, s[16:19], 0 offen\n\t"
        "s_waitcnt lgkmcnt(5)\n\t"
        "v_mad_u32_u16 v16, v44, s48, v14\n\t"
        "v_mad_u32_u16 v17, v45, s48, v14\n\t"
        "buffer_load_dwordx4 v[80:83], v16, s[16:19], 0 offen\n\t"
        "buffer_load_dwordx4 v[84:87], v17, s[16:19], 0 offen\n\t"
        "s_waitcnt lgkmcnt(4)\n\t"
        "v_mad_u32_u16 v16, v46, s48, v14\n\t"
        "v_mad_u32_u16 v17, v47, s48, v14\n\t"
        "buffer_load_dwordx4 v[88:91], v16, s[16:19], 0 offen\n\t"
        "buffer_load_dwordx4 v[92:95], v17, s[16:19], 0 offen\n\t"
        "s_waitcnt lgkmcnt(3)\n\t"
        "v_mad_u32_u16 v16, v48, s48, v14\n\t"
        "v_mad_u32_u16 v17, v49, s48, v14\n\t"
        "buffer_load_dwordx4 v[96:99], v16, s[16:19], 0 offen\n\t"
        "buffer_load_dwordx4 v[100:103], v17, s[16:19], 0 offen\n\t"
        "s_waitcnt lgkmcnt(2)\n\t"
        "v_mad_u32_u16 v16, v50, s48, v14\n\t"
        "v_mad_u32_u16 v17, v51, s48, v14\n\t"
        "buffer_load_dwordx4 v[104:107], v16, s[16:19], 0 offen\n\t"
        "buffer_load_dwordx4 v[108:111], v17, s[16:19], 0 offen\n\t"
        "s_min_u32 s59, s56, s57\n\t"
        "s_add_u32 s58, s59, 1\n\t"
        "s_setprio 3\n"
        ".L7_loop:\n\t"
        "v_add_u32 v19, 64, v19\n\t"
        "v_perm_b32 v20, v40, v40, v8\n\t"
        "v_perm_b32 v21, v40, v40, v9\n\t"
        "v_perm_b32 v22, v41, v41, v8\n\t"
        "v_perm_b32 v23, v41, v41, v9\n\t"
        "s_waitcnt vmcnt(10) lgkmcnt(1)\n\t"
        "v_mad_u32_u16 v16, v52, s48, v14\n\t"
        "v_mad_u32_u16 v17, v53, s48, v14\n\t"
        "buffer_load_dwordx4 v[112:115], v16, s[16:19], 0 offen\n\t"
        "buffer_load_dwordx4 v[116:119], v17, s[16:19], 0 offen\n\t"
        "v_mfma_f32_4x4x4_16b_bf16 v[32:35], v[20:21], v[64:65], v[32:35]\n\t"
        "v_mfma_f32_4x4x4_16b_bf16 v[36:39], v[20:21], v[66:67], v[36:39]\n\t"
        "ds_read2_b32 v[40:41], v19 offset0:0 offset1:1\n\t"
        "v_mfma_f32_4x4x4_16b_bf16 v[32:35], v[22:23], v[68:69], v[32:35]\n\t"
        "v_mfma_f32_4x4x4_16b_bf16 v[36:39], v[22:23], v[70:71], v[36:39]\n\t"
        "s_sub_u32 s59, s59, 1\n\t"
        "s_cbranch_scc1 .L7_i92\n"
        ".L7_j93:\n\t"
        "v_perm_b32 v20, v42, v42, v8\n\t"
        "v_perm_b32 v21, v42, v42, v9\n\t"
        "v_perm_b32 v22, v43, v43, v8\n\t"
        "v_perm_b32 v23, v43, v43, v9\n\t"
        "s_waitcnt vmcnt(10) lgkmcnt(1)\n\t"
        "v_mad_u32_u16 v16, v54, s48, v14\n\t"
        "v_mad_u32_u16 v17, v55, s48, v14\n\t"
        "buffer_load_dwordx4 v[120:123], v16, s[16:19], 0 offen\n\t"
        "buffer_load_dwordx4 v[124:127], v17, s[16:19], 0 offen\n\t"
        "v_mfma_f32_4x4x4_16b_bf16 v[32:35], v[20:21], v[72:73], v[32:35]\n\t"
        "v_mfma_f32_4x4x4_16b_bf16 v[36:39], v[20:21], v[74:75], v[36:39]\n\t"
        "ds_read2_b32 v[42:43], v19 offset0:2 offset1:3\n\t"
        "v_mfma_f32_4x4x4_16b_bf16 v[32:35], v[22:23], v[76:77], v[32:35]\n\t"
        "v_mfma_f32_4x4x4_16b_bf16 v[36:39], v[22:23], v[78:79], v[36:39]\n\t"
        "s_sub_u32 s59, s59, 1\n\t"
        "s_cbranch_scc1 .L7_i94\n"
        ".L7_j95:\n\t"
        "v_perm_b32 v20, v44, v44, v8\n\t"
        "v_perm_b32 v21, v44, v44, v9\n\t"
        "v_perm_b32 v22, v45, v45, v8\n\t"
        "v_perm_b32 v23, v45, v45, v9\n\t"
        "s_waitcnt vmcnt(10) lgkmcnt(1)\n\t"
        "v_mad_u32_u16 v16, v40, s48, v14\n\t"
        "v_mad_u32_u16 v17, v41, s48, v14\n\t"
        "buffer_load_dwordx4 v[64:67], v16, s[16:19], 0 offen\n\t"
        "buffer_load_dwordx4 v[68:71], v17, s[16:19], 0 offen\n\t"
        "v_mfma_f32_4x4x4_16b_bf16 v[32:35], v[20:21], v[80:81], v[32:35]\n\t"
        "v_mfma_f32_4x4x4_16b_bf16 v[36:39], v[20:21], v[82:83], v[36:39]\n\t"
        "ds_read2_b32 v[44:45], v19 offset0:4 offset1:5\n\t"
        "v_mfma_f32_4x4x4_16b_bf16 v[32:35], v[22:23], v[84:85], v[32:35]\n\t"
        "v_mfma_f32_4x4x4_16b_bf16 v[36:39], v[22:23], v[86:87], v[36:39]\n\t"
        "s_sub_u32 s59, s59, 1\n\t"
        "s_cbranch_scc1 .L7_i96\n"
        ".L7_j97:\n\t"
        "v_perm_b32 v20, v46, v46, v8\n\t"
        "v_perm_b32 v21, v46, v46, v9\n\t"
        "v_perm_b32 v22, v47, v47, v8\n\t"
        "v_perm_b32 v23, v47, v47, v9\n\t"
        "s_waitcnt vmcnt(10) lgkmcnt(1)\n\t"
        "v_mad_u32_u16 v16, v42, s48, v14\n\t"
        "v_mad_u32_u16 v17, v43, s48, v14\n\t"
        "buffer_load_dwordx4 v[72:75], v16, s[16:19], 0 offen\n\t"
        "buffer_load_dwordx4 v[76:79], v17, s[16:19], 0 offen\n\t"
        "v_mfma_f32_4x4x4_16b_bf16 v[32:35], v[20:21], v[88:89], v[32:35]\n\t"
        "v_mfma_f32_4x4x4_16b_bf16 v[36:39], v[20:21], v[90:91], v[36:39]\n\t"
        "ds_read2_b32 v[46:47], v19 offset0:6 offset1:7\n\t"
        "v_mfma_f32_4x4x4_16b_bf16 v[32:35], v[22:23], v[92:93], v[32:35]\n\t"
        "v_mfma_f32_4x4x4_16b_bf16 v[36:39], v[22:23], v[94:95], v[36:39]\n\t"
        "s_sub_u32 s59, s59, 1\n\t"
        "s_cbranch_scc1 .L7_i98\n"
        ".L7_j99:\n\t"
        "v_perm_b32 v20, v48, v48, v8\n\t"
        "v_perm_b32 v21, v48, v48, v9\n\t"
        "v_perm_b32 v22, v49, v49, v8\n\t"
        "v_perm_b32 v23, v49, v49, v9\n\t"
        "s_waitcnt vmcnt(10) lgkmcnt(1)\n\t"
        "v_mad_u32_u16 v16, v44, s48, v14\n\t"
        "v_mad_u32_u16 v17, v45, s48, v14\n\t"
        "buffer_load_dwordx4 v[80:83], v16, s[16:19], 0 offen\n\t"
        "buffer_load_dwordx4 v[84:87], v17, s[16:19], 0 offen\n\t"
        "v_mfma_f32_4x4x4_16b_bf16 v[32:35], v[20:21], v[96:97], v[32:35]\n\t"
        "v_mfma_f32_4x4x4_16b_bf16 v[36:39], v[20:21], v[98:99], v[36:39]\n\t"
        "ds_read2_b32 v[48:49], v19 offset0:8 offset1:9\n\t"
        "v_mfma_f32_4x4x4_16b_bf16 v[32:35], v[22:23], v[100:101], v[32:35]\n\t"
        "v_mfma_f32_4x4x4_16b_bf16 v[36:39], v[22:23], v[102:103], v[36:39]\n\t"
        "s_sub_u32 s59, s59, 1\n\t"
        "s_cbranch_scc1 .L7_i100\n"
        ".L7_j101:\n\t"
        "v_perm_b32 v20, v50, v50, v8\n\t"
        "v_perm_b32 v21, v50, v50, v9\n\t"
        "v_perm_b32 v22, v51, v51, v8\n\t"
        "v_perm_b32 v23, v51, v51, v9\n\t"
        "s_waitcnt vmcnt(10) lgkmcnt(1)\n\t"
        "v_mad_u32_u16 v16, v46, s48, v14\n\t"
        "v_mad_u32_u16 v17, v47, s48, v14\n\t"
        "buffer_load_dwordx4 v[88:91], v16, s[16:19], 0 offen\n\t"
        "buffer_load_dwordx4 v[92:95], v17, s[16:19], 0 offen\n\t"
        "v_mfma_f32_4x4x4_16b_bf16 v[32:35], v[20:21], v[104:105], v[32:35]\n\t"
        "v_mfma_f32_4x4x4_16b_bf16 v[36:39], v[20:21], v[106:107], v[36:39]\n\t"
        "ds_read2_b32 v[50:51], v19 offset0:10 offset1:11\n\t"
        "v_mfma_f32_4x4x4_16b_bf16 v[32:35], v[22:23], v[108:109], v[32:35]\n\t"
        "v_mfma_f32_4x4x4_16b_bf16 v[36:39], v[22:23], v[110:111], v[36:39]\n\t"
        "s_sub_u32 s59, s59, 1\n\t"
        "s_cbranch_scc1 .L7_i102\n"
        ".L7_j103:\n\t"
        "v_perm_b32 v20, v52, v52, v8\n\t"
        "v_perm_b32 v21, v52, v52, v9\n\t"
        "v_perm_b32 v22, v53, v53, v8\n\t"
        "v_perm_b32 v23, v53, v53, v9\n\t"
        "s_waitcnt vmcnt(10) lgkmcnt(1)\n\t"
        "v_mad_u32_u16 v16, v48, s48, v14\n\t"
        "v_mad_u32_u16 v17, v49, s48, v14\n\t"
        "buffer_load_dwordx4 v[96:99], v16, s[16:19], 0 offen\n\t"
        "buffer_load_dwordx4 v[100:103], v17, s[16:19], 0 offen\n\t"
        "v_mfma_f32_4x4x4_16b_bf16 v[32:35], v[20:21], v[112:113], v[32:35]\n\t"
        "v_mfma_f32_4x4x4_16b_bf16 v[36:39], v[20:21], v[114:115], v[36:39]\n\t"
        "ds_read2_b32 v[52:53], v19 offset0:12 offset1:13\n\t"
        "v_mfma_f32_4x4x4_16b_bf16 v[32:35], v[22:23], v[116:117], v[32:35]\n\t"
        "v_mfma_f32_4x4x4_16b_bf16 v[36:39], v[22:23], v[118:119], v[36:39]\n\t"
        "s_sub_u32 s59, s59, 1\n\t"
        "s_cbranch_scc1 .L7_i104\n"
        ".L7_j105:\n\t"
        "v_perm_b32 v20, v54, v54, v8\n\t"
        "v_perm_b32 v21, v54, v54, v9\n\t"
        "v_perm_b32 v22, v55, v55, v8\n\t"
        "v_perm_b32 v23, v55, v55, v9\n\t"
        "s_waitcnt vmcnt(10) lgkmcnt(1)\n\t"
        "v_mad_u32_u16 v16, v50, s48, v14\n\t"
        "v_mad_u32_u16 v17, v51, s48, v14\n\t"
        "buffer_load_dwordx4 v[104:107], v16, s[16:19], 0 offen\n\t"
        "buffer_load_dwordx4 v[108:111], v17, s[16:19], 0 offen\n\t"
        "v_mfma_f32_4x4x4_16b_bf16 v[32:35], v[20:21], v[120:121], v[32:35]\n\t"
        "v_mfma_f32_4x4x4_16b_bf16 v[36:39], v[20:21], v[122:123], v[36:39]\n\t"
        "ds_read2_b32 v[54:55], v19 offset0:14 offset1:15\n\t"
        "v_mfma_f32_4x4x4_16b_bf16 v[32:35], v[22:23], v[124:125], v[32:35]\n\t"
        "v_mfma_f32_4x4x4_16b_bf16 v[36:39], v[22:23], v[126:127], v[36:39]\n\t"
        "s_sub_u32 s59, s59, 1\n\t"
        "s_cbranch_scc0 .L7_loop\n\t"
        "s_branch .L7_i106\n"
        ".L7_i76:\n\t"
        "s_mov_b32 s60, 8\n\t"
        "s_swappc_b64 s[54:55], s[50:51]\n\t"
        "s_branch .L7_j77\n"
        ".L7_i78:\n\t"
        "s_mov_b32 s60, 16\n\t"
        "s_swappc_b64 s[54:55], s[50:51]\n\t"
        "s_branch .L7_j79\n"
        ".L7_i80:\n\t"
        "s_mov_b32 s60, 24\n\t"
        "s_swappc_b64 s[54:55], s[50:51]\n\t"
        "s_branch .L7_j81\n"
        ".L7_i82:\n\t"
        "s_mov_b32 s60, 32\n\t"
        "s_swappc_b64 s[54:55], s[50:51]\n\t"
        "s_branch .L7_j83\n"
        ".L7_i84:\n\t"
        "s_mov_b32 s60, 40\n\t"
        "s_swappc_b64 s[54:55], s[50:51]\n\t"
        "s_branch .L7_j85\n"
        ".L7_i86:\n\t"
        "s_mov_b32 s60, 48\n\t"
        "s_swappc_b64 s[54:55], s[50:51]\n\t"
        "s_branch .L7_j87\n"
        ".L7_i88:\n\t"
        "s_mov_b32 s60, 56\n\t"
        "s_swappc_b64 s[54:55], s[50:51]\n\t"
        "s_branch .L7_j89\n"
        ".L7_i90:\n\t"
        "s_mov_b32 s60, 64\n\t"
        "s_swappc_b64 s[54:55], s[50:51]\n\t"
        "s_branch .L7_j91\n"
        ".L7_i92:\n\t"
        "s_mov_b32 s60, 8\n\t"
        "s_swappc_b64 s[62:63], s[42:43]\n\t"
        "s_cbranch_vccnz .L7_dt1\n\t"
        "s_branch .L7_j93\n"
        ".L7_i94:\n\t"
        "s_mov_b32 s60, 16\n\t"
        "s_swappc_b64 s[62:63], s[42:43]\n\t"
        "s_cbranch_vccnz .L7_dt2\n\t"
        "s_branch .L7_j95\n"
        ".L7_i96:\n\t"
        "s_mov_b32 s60, 24\n\t"
        "s_swappc_b64 s[62:63], s[42:43]\n\t"
        "s_cbranch_vccnz .L7_dt3\n\t"
        "s_branch .L7_j97\n"
        ".L7_i98:\n\t"
        "s_mov_b32 s60, 32\n\t"
        "s_swappc_b64 s[62:63], s[42:43]\n\t"
        "s_cbranch_vccnz .L7_dt4\n\t"
        "s_branch .L7_j99\n"
        ".L7_i100:\n\t"
        "s_mov_b32 s60, 40\n\t"
        "s_swappc_b64 s[62:63], s[42:43]\n\t"
        "s_cbranch_vccnz .L7_dt5\n\t"
        "s_branch .L7_j101\n"
        ".L7_i102:\n\t"
        "s_mov_b32 s60, 48\n\t"
        "s_swappc_b64 s[62:63], s[42:43]\n\t"
        "s_cbranch_vccnz .L7_dt6\n\t"
        "s_branch .L7_j103\n"
        ".L7_i104:\n\t"
        "s_mov_b32 s60, 56\n\t"
        "s_swappc_b64 s[62:63], s[42:43]\n\t"
        "s_cbranch_vccnz .L7_dt7\n\t"
        "s_branch .L7_j105\n"
        ".L7_i106:\n\t"
        "s_mov_b32 s60, 64\n\t"
        "s_swappc_b64 s[62:63], s[42:43]\n\t"
        "s_cbranch_vccnz .L7_dt0\n\t"
        "s_branch .L7_loop\n"
        ".L7_ev:\n\t"
        "s_sub_u32 s56, s56, s58\n\t"
        "s_cbranch_scc0 .L7_e1\n\t"
        "s_swappc_b64 s[54:55], s[50:51]\n"
        ".L7_e1:\n\t"
        "s_sub_u32 s57, s57, s58\n\t"
        "s_cbranch_scc0 .L7_e2\n\t"
        "s_swappc_b64 s[54:55], s[52:53]\n"
        ".L7_e2:\n\t"
        "s_min_u32 s59, s56, s57\n\t"
        "s_add_u32 s58, s59, 1\n\t"
        "s_setpc_b64 s[62:63]\n"
        ".L7_dt0:\n\t"
        "v_perm_b32 v20, v40, v40, v8\n\t"
        "v_perm_b32 v21, v40, v40, v9\n\t"
        "v_perm_b32 v22, v41, v41, v8\n\t"
        "v_perm_b32 v23, v41, v41, v9\n\t"
        "s_waitcnt vmcnt(6)\n\t"
        "s_cmp_lt_u32 s57, 3\n\t"
        "s_cbranch_scc0 .L7_du0\n\t"
        "s_waitcnt vmcnt(2)\n\t"
        "s_cmp_eq_u32 s57, 0\n\t"
        "s_cbranch_scc0 .L7_du0\n\t"
        "s_waitcnt vmcnt(0)\n"
        ".L7_du0:\n\t"
        "v_mfma_f32_4x4x4_16b_bf16 v[32:35], v[20:21], v[64:65], v[32:35]\n\t"
        "v_mfma_f32_4x4x4_16b_bf16 v[36:39], v[20:21], v[66:67], v[36:39]\n\t"
        "s_sub_u32 s57, s57, 1\n\t"
        "v_mfma_f32_4x4x4_16b_bf16 v[32:35], v[22:23], v[68:69], v[32:35]\n\t"
        "v_mfma_f32_4x4x4_16b_bf16 v[36:39], v[22:23], v[70:71], v[36:39]\n\t"
        "s_cbranch_scc1 .L7_last\n"
        ".L7_dt1:\n\t"
        "v_perm_b32 v20, v42, v42, v8\n\t"
        "v_perm_b32 v21, v42, v42, v9\n\t"
        "v_perm_b32 v22, v43, v43, v8\n\t"
        "v_perm_b32 v23, v43, v43, v9\n\t"
        "s_waitcnt vmcnt(6)\n\t"
        "s_cmp_lt_u32 s57, 3\n\t"
        "s_cbranch_scc0 .L7_du1\n\t"
        "s_waitcnt vmcnt(2)\n\t"
        "s_cmp_eq_u32 s57, 0\n\t"
        "s_cbranch_scc0 .L7_du1\n\t"
        "s_waitcnt vmcnt(0)\n"
        ".L7_du1:\n\t"
        "v_mfma_f32_4x4x4_16b_bf16 v[32:35], v[20:21], v[72:73], v[32:35]\n\t"
        "v_mfma_f32_4x4x4_16b_bf16 v[36:39], v[20:21], v[74:75], v[36:39]\n\t"
        "s_sub_u32 s57, s57, 1\n\t"
        "v_mfma_f32_4x4x4_16b_bf16 v[32:35], v[22:23], v[76:77], v[32:35]\n\t"
        "v_mfma_f32_4x4x4_16b_bf16 v[36:39], v[22:23], v[78:79], v[36:39]\n\t"
        "s_cbranch_scc1 .L7_last\n"
        ".L7_dt2:\n\t"
        "v_perm_b32 v20, v44, v44, v8\n\t"
        "v_perm_b32 v21, v44, v44, v9\n\t"
        "v_perm_b32 v22, v45, v45, v8\n\t"
        "v_perm_b32 v23, v45, v45, v9\n\t"
        "s_waitcnt vmcnt(6)\n\t"
        "s_cmp_lt_u32 s57, 3\n\t"
        "s_cbranch_scc0 .L7_du2\n\t"
        "s_waitcnt vmcnt(2)\n\t"
        "s_cmp_eq_u32 s57, 0\n\t"
        "s_cbranch_scc0 .L7_du2\n\t"
        "s_waitcnt vmcnt(0)\n"
        ".L7_du2:\n\t"
        "v_mfma_f32_4x4x4_16b_bf16 v[32:35], v[20:21], v[80:81], v[32:35]\n\t"
        "v_mfma_f32_4x4x4_16b_bf16 v[36:39], v[20:21], v[82:83], v[36:39]\n\t"
        "s_sub_u32 s57, s57, 1\n\t"
        "v_mfma_f32_4x4x4_16b_bf16 v[32:35], v[22:23], v[84:85], v[32:35]\n\t"
        "v_mfma_f32_4x4x4_16b_bf16 v[36:39], v[22:23], v[86:87], v[36:39]\n\t"
        "s_cbranch_scc1 .L7_last\n"
        ".L7_dt3:\n\t"
        "v_perm_b32 v20, v46, v46, v8\n\t"
        "v_perm_b32 v21, v46, v46, v9\n\t"
        "v_perm_b32 v22, v47, v47, v8\n\t"
        "v_perm_b32 v23, v47, v47, v9\n\t"
        "s_waitcnt vmcnt(6)\n\t"
        "s_cmp_lt_u32 s57, 3\n\t"
        "s_cbranch_scc0 .L7_du3\n\t"
        "s_waitcnt vmcnt(2)\n\t"
        "s_cmp_eq_u32 s57, 0\n\t"
        "s_cbranch_scc0 .L7_du3\n\t"
        "s_waitcnt vmcnt(0)\n"
        ".L7_du3:\n\t"
        "v_mfma_f32_4x4x4_16b_bf16 v[32:35], v[20:21], v[88:89], v[32:35]\n\t"
        "v_mfma_f32_4x4x4_16b_bf16 v[36:39], v[20:21], v[90:91], v[36:39]\n\t"
        "s_sub_u32 s57, s57, 1\n\t"
        "v_mfma_f32_4x4x4_16b_bf16 v[32:35], v[22:23], v[92:93], v[32:35]\n\t"
        "v_mfma_f32_4x4x4_16b_bf16 v[36:39], v[22:23], v[94:95], v[36:39]\n\t"
        "s_cbranch_scc1 .L7_last\n"
        ".L7_dt4:\n\t"
        "v_perm_b32 v20, v48, v48, v8\n\t"
        "v_perm_b32 v21, v48, v48, v9\n\t"
        "v_perm_b32 v22, v49, v49, v8\n\t"
        "v_perm_b32 v23, v49, v49, v9\n\t"
        "s_waitcnt vmcnt(6)\n\t"
        "s_cmp_lt_u32 s57, 3\n\t"
        "s_cbranch_scc0 .L7_du4\n\t"
        "s_waitcnt vmcnt(2)\n\t"
        "s_cmp_eq_u32 s57, 0\n\t"
        "s_cbranch_scc0 .L7_du4\n\t"
        "s_waitcnt vmcnt(0)\n"
        ".L7_du4:\n\t"
        "v_mfma_f32_4x4x4_16b_bf16 v[32:35], v[20:21], v[96:97], v[32:35]\n\t"
        "v_mfma_f32_4x4x4_16b_bf16 v[36:39], v[20:21], v[98:99], v[36:39]\n\t"
        "s_sub_u32 s57, s57, 1\n\t"
        "v_mfma_f32_4x4x4_16b_bf16 v[32:35], v[22:23], v[100:101], v[32:35]\n\t"
        "v_mfma_f32_4x4x4_16b_bf16 v[36:39], v[22:23], v[102:103], v[36:39]\n\t"
        "s_cbranch_scc1 .L7_last\n"
        ".L7_dt5:\n\t"
        "v_perm_b32 v20, v50, v50, v8\n\t"
        "v_perm_b32 v21, v50, v50, v9\n\t"
        "v_perm_b32 v22, v51, v51, v8\n\t"
        "v_perm_b32 v23, v51, v51, v9\n\t"
        "s_waitcnt vmcnt(6)\n\t"
        "s_cmp_lt_u32 s57, 3\n\t"
        "s_cbranch_scc0 .L7_du5\n\t"
        "s_waitcnt vmcnt(2)\n\t"
        "s_cmp_eq_u32 s57, 0\n\t"
        "s_cbranch_scc0 .L7_du5\n\t"
        "s_waitcnt vmcnt(0)\n"
        ".L7_du5:\n\t"
        "v_mfma_f32_4x4x4_16b_bf16 v[32:35], v[20:21], v[104:105], v[32:35]\n\t"
        "v_mfma_f32_4x4x4_16b_bf16 v[36:39], v[20:21], v[106:107], v[36:39]\n\t"
        "s_sub_u32 s57, s57, 1\n\t"
        "v_mfma_f32_4x4x4_16b_bf16 v[32:35], v[22:23], v[108:109], v[32:35]\n\t"
        "v_mfma_f32_4x4x4_16b_bf16 v[36:39], v[22:23], v[110:111], v[36:39]\n\t"
        "s_cbranch_scc1 .L7_last\n"
        ".L7_dt6:\n\t"
        "v_perm_b32 v20, v52, v52, v8\n\t"
        "v_perm_b32 v21, v52, v52, v9\n\t"
        "v_perm_b32 v22, v53, v53, v8\n\t"
        "v_perm_b32 v23, v53, v53, v9\n\t"
        "s_waitcnt vmcnt(6)\n\t"
        "s_cmp_lt_u32 s57, 3\n\t"
        "s_cbranch_scc0 .L7_du6\n\t"
        "s_waitcnt vmcnt(2)\n\t"
        "s_cmp_eq_u32 s57, 0\n\t"
        "s_cbranch_scc0 .L7_du6\n\t"
        "s_waitcnt vmcnt(0)\n"
        ".L7_du6:\n\t"
        "v_mfma_f32_4x4x4_16b_bf16 v[32:35], v[20:21], v[112:113], v[32:35]\n\t"
        "v_mfma_f32_4x4x4_16b_bf16 v[36:39], v[20:21], v[114:115], v[36:39]\n\t"
        "s_sub_u32 s57, s57, 1\n\t"
        "v_mfma_f32_4x4x4_16b_bf16 v[32:35], v[22:23], v[116:117], v[32:35]\n\t"
        "v_mfma_f32_4x4x4_16b_bf16 v[36:39], v[22:23], v[118:119], v[36:39]\n\t"
        "s_cbranch_scc1 .L7_last\n"
        ".L7_dt7:\n\t"
        "v_perm_b32 v20, v54, v54, v8\n\t"
        "v_perm_b32 v21, v54, v54, v9\n\t"
        "v_perm_b32 v22, v55, v55, v8\n\t"
        "v_perm_b32 v23, v55, v55, v9\n\t"
        "s_waitcnt vmcnt(6)\n\t"
        "s_cmp_lt_u32 s57, 3\n\t"
        "s_cbranch_scc0 .L7_du7\n\t"
        "s_waitcnt vmcnt(2)\n\t"
        "s_cmp_eq_u32 s57, 0\n\t"
        "s_cbranch_scc0 .L7_du7\n\t"
        "s_waitcnt vmcnt(0)\n"
        ".L7_du7:\n\t"
        "v_mfma_f32_4x4x4_16b_bf16 v[32:35], v[20:21], v[120:121], v[32:35]\n\t"
        "v_mfma_f32_4x4x4_16b_bf16 v[36:39], v[20:21], v[122:123], v[36:39]\n\t"
        "s_sub_u32 s57, s57, 1\n\t"
        "v_mfma_f32_4x4x4_16b_bf16 v[32:35], v[22:23], v[124:125], v[32:35]\n\t"
        "v_mfma_f32_4x4x4_16b_bf16 v[36:39], v[22:23], v[126:127], v[36:39]\n\t"
        "s_cbranch_scc1 .L7_last\n\t"
        "s_branch .L7_dt0\n"
        ".L7_ie:\n\t"
        "v_dot4_u32_u8 v19, v25, s38, v7\n\t"
        "s_nop 2\n\t"
        "v_subrev_u32 v19, s60, v19\n\t"
        "s_mov_b32 s56, s25\n\t"
        "s_swappc_b64 s[50:51], s[54:55]\n\t"
        "v_dot4_u32_u8 v19, v26, s38, v7\n\t"
        "s_nop 2\n\t"
        "v_subrev_u32 v19, s60, v19\n\t"
        "s_mov_b32 s56, s26\n\t"
        "s_swappc_b64 s[50:51], s[54:55]\n\t"
        "v_dot4_u32_u8 v19, v27, s38, v7\n\t"
        "s_nop 2\n\t"
        "v_subrev_u32 v19, s60, v19\n\t"
        "s_mov_b32 s56, s27\n\t"
        "s_swappc_b64 s[50:51], s[54:55]\n\t"
        "v_dot4_u32_u8 v19, v28, s38, v7\n\t"
        "s_nop 2\n\t"
        "v_subrev_u32 v19, s60, v19\n\t"
        "s_add_u32 s56, s28, 2\n\t"
        "s_swappc_b64 s[50:51], s[54:55]\n\t"
        "s_mov_b64 vcc, -1\n\t"
        "s_setpc_b64 s[54:55]\n"
        ".L7_ce:\n\t"
        "v_and_b32 v4, 0xffff0000, v24\n\t"
        "s_nop 2\n\t"
        "v_pk_mul_f32 v[32:33], v[32:33], v[4:5] op_sel_hi:[1,0]\n\t"
        "v_pk_mul_f32 v[34:35], v[34:35], v[4:5] op_sel_hi:[1,0]\n\t"
        "v_pk_mul_f32 v[36:37], v[36:37], v[4:5] op_sel_hi:[1,0]\n\t"
        "v_pk_mul_f32 v[38:39], v[38:39], v[4:5] op_sel_hi:[1,0]\n\t"
        "v_cvt_pk_bf16_f32 v10, v32, v33\n\t"
        "v_cvt_pk_bf16_f32 v11, v34, v35\n\t"
        "v_cvt_pk_bf16_f32 v12, v36, v37\n\t"
        "v_cvt_pk_bf16_f32 v13, v38, v39\n"
        ".L7_w107:\n\t"
        "ds_read_b32 v4, v2 offset:12\n\t"
        "s_waitcnt lgkmcnt(0)\n\t"
        "v_cmp_gt_u16 vcc, 4, v4\n\t"
        "s_cbranch_vccz .L7_d108\n\t"
        "s_sleep 1\n\t"
        "s_sub_i32 s83, s83, 1\n\t"
        "s_cmp_gt_i32 s83, 0\n\t"
        "s_cbranch_scc1 .L7_w107\n"
        ".L7_d108:\n\t"
        "s_mov_b64 vcc, 0\n\t"
        "ds_write_b128 v56, v[10:13] offset:21760\n\t"
        "s_mov_b64 exec, 1\n\t"
        "ds_add_u32 v2, v3 offset:52\n\t"
        "s_mov_b64 exec, -1\n\t"
        "v_mov_b64 v[32:33], 0\n\t"
        "v_mov_b64 v[34:35], 0\n\t"
        "v_mov_b64 v[36:37], 0\n\t"
        "v_mov_b64 v[38:39], 0\n\t"
        "s_mov_b32 s57, s25\n\t"
        "s_swappc_b64 s[52:53], s[54:55]\n\t"
        "v_and_b32 v4, 0xffff0000, v25\n\t"
        "s_nop 2\n\t"
        "v_pk_mul_f32 v[32:33], v[32:33], v[4:5] op_sel_hi:[1,0]\n\t"
        "v_pk_mul_f32 v[34:35], v[34:35], v[4:5] op_sel_hi:[1,0]\n\t"
        "v_pk_mul_f32 v[36:37], v[36:37], v[4:5] op_sel_hi:[1,0]\n\t"
        "v_pk_mul_f32 v[38:39], v[38:39], v[4:5] op_sel_hi:[1,0]\n\t"
        "v_cvt_pk_bf16_f32 v10, v32, v33\n\t"
        "v_cvt_pk_bf16_f32 v11, v34, v35\n\t"
        "v_cvt_pk_bf16_f32 v12, v36, v37\n\t"
        "v_cvt_pk_bf16_f32 v13, v38, v39\n\t"
        "ds_write_b128 v57, v[10:13] offset:21760\n\t"
        "s_mov_b64 exec, 1\n\t"
        "ds_add_u32 v61, v3 offset:52\n\t"
        "s_mov_b64 exec, -1\n\t"
        "v_mov_b64 v[32:33], 0\n\t"
        "v_mov_b64 v[34:35], 0\n\t"
        "v_mov_b64 v[36:37], 0\n\t"
        "v_mov_b64 v[38:39], 0\n\t"
        "s_mov_b32 s57, s26\n\t"
        "s_swappc_b64 s[52:53], s[54:55]\n\t"
        "v_and_b32 v4, 0xffff0000, v26\n\t"
        "s_nop 2\n\t"
        "v_pk_mul_f32 v[32:33], v[32:33], v[4:5] op_sel_hi:[1,0]\n\t"
        "v_pk_mul_f32 v[34:35], v[34:35], v[4:5] op_sel_hi:[1,0]\n\t"
        "v_pk_mul_f32 v[36:37], v[36:37], v[4:5] op_sel_hi:[1,0]\n\t"
        "v_pk_mul_f32 v[38:39], v[38:39], v[4:5] op_sel_hi:[1,0]\n\t"
        "v_cvt_pk_bf16_f32 v10, v32, v33\n\t"
        "v_cvt_pk_bf16_f32 v11, v34, v35\n\t"
        "v_cvt_pk_bf16_f32 v12, v36, v37\n\t"
        "v_cvt_pk_bf16_f32 v13, v38, v39\n\t"
        "ds_write_b128 v58, v[10:13] offset:21760\n\t"
        "s_mov_b64 exec, 1\n\t"
        "ds_add_u32 v61, v3 offset:56\n\t"
        "s_mov_b64 exec, -1\n\t"
        "v_mov_b64 v[32:33], 0\n\t"
        "v_mov_b64 v[34:35], 0\n\t"
        "v_mov_b64 v[36:37], 0\n\t"
        "v_mov_b64 v[38:39], 0\n\t"
        "s_mov_b32 s57, s27\n\t"
        "s_swappc_b64 s[52:53], s[54:55]\n\t"
        "v_and_b32 v4, 0xffff0000, v27\n\t"
        "s_nop 2\n\t"
        "v_pk_mul_f32 v[32:33], v[32:33], v[4:5] op_sel_hi:[1,0]\n\t"
        "v_pk_mul_f32 v[34:35], v[34:35], v[4:5] op_sel_hi:[1,0]\n\t"
        "v_pk_mul_f32 v[36:37], v[36:37], v[4:5] op_sel_hi:[1,0]\n\t"
        "v_pk_mul_f32 v[38:39], v[38:39], v[4:5] op_sel_hi:[1,0]\n\t"
        "v_cvt_pk_bf16_f32 v10, v32, v33\n\t"
        "v_cvt_pk_bf16_f32 v11, v34, v35\n\t"
        "v_cvt_pk_bf16_f32 v12, v36, v37\n\t"
        "v_cvt_pk_bf16_f32 v13, v38, v39\n\t"
        "ds_write_b128 v59, v[10:13] offset:21760\n\t"
        "s_mov_b64 exec, 1\n\t"
        "ds_add_u32 v2, v3 offset:64\n\t"
        "s_mov_b64 exec, -1\n\t"
        "v_mov_b64 v[32:33], 0\n\t"
        "v_mov_b64 v[34:35], 0\n\t"
        "v_mov_b64 v[36:37], 0\n\t"
        "v_mov_b64 v[38:39], 0\n\t"
        "s_mov_b32 s57, s28\n\t"
        "s_swappc_b64 s[52:53], s[54:55]\n"
        ".L7_last:\n\t"
        "v_and_b32 v4, 0xffff0000, v28\n\t"
        "s_nop 2\n\t"
        "v_pk_mul_f32 v[32:33], v[32:33], v[4:5] op_sel_hi:[1,0]\n\t"
        "v_pk_mul_f32 v[34:35], v[34:35], v[4:5] op_sel_hi:[1,0]\n\t"
        "v_pk_mul_f32 v[36:37], v[36:37], v[4:5] op_sel_hi:[1,0]\n\t"
        "v_pk_mul_f32 v[38:39], v[38:39], v[4:5] op_sel_hi:[1,0]\n\t"
        "v_cvt_pk_bf16_f32 v10, v32, v33\n\t"
        "v_cvt_pk_bf16_f32 v11, v34, v35\n\t"
        "v_cvt_pk_bf16_f32 v12, v36, v37\n\t"
        "v_cvt_pk_bf16_f32 v13, v38, v39\n\t"
        "ds_write_b128 v60, v[10:13] offset:21760\n\t"
        "s_mov_b64 exec, 1\n\t"
        "ds_add_u32 v2, v3 offset:68\n\t"
        "s_mov_b64 exec, -1\n\t"
        "s_endpgm\n"
        ".L7_x:\n\t"
        "s_mov_b32 s36, 1\n\t"
        "s_waitcnt lgkmcnt(0)\n\t"
        "v_dot4_u32_u8 v83, v67, s39, v7\n\t"
        "v_lshlrev_b32_sdwa v115, 1, v67 dst_sel:DWORD dst_unused:UNUSED_PAD src0_sel:DWORD src1_sel:WORD_0\n\t"
        "ds_read_u16 v115, v115 offset:46336\n\t"
        "v_cmp_eq_u16 vcc, -1, v67\n\t"
        "s_cmp_lg_u64 vcc, 0\n\t"
        "s_cselect_b32 s78, 0x10000, 0\n\t"
        "v_or_b32 v30, s78, v30\n\t"
        "ds_add_rtn_u32 v99, v83, v29 offset:192\n\t"
        "v_dot4_u32_u8 v87, v71, s39, v7\n\t"
        "v_lshlrev_b32_sdwa v119, 1, v71 dst_sel:DWORD dst_unused:UNUSED_PAD src0_sel:DWORD src1_sel:WORD_0\n\t"
        "ds_read_u16 v119, v119 offset:46336\n\t"
        "v_cmp_eq_u16 vcc, -1, v71\n\t"
        "s_cmp_lg_u64 vcc, 0\n\t"
        "s_cselect_b32 s78, 0x10000, 0\n\t"
        "v_or_b32 v30, s78, v30\n\t"
        "ds_add_rtn_u32 v103, v87, v29 offset:192\n\t"
        "v_dot4_u32_u8 v91, v75, s39, v7\n\t"
        "v_lshlrev_b32_sdwa v123, 1, v75 dst_sel:DWORD dst_unused:UNUSED_PAD src0_sel:DWORD src1_sel:WORD_0\n\t"
        "ds_read_u16 v123, v123 offset:46336\n\t"
        "v_cmp_eq_u16 vcc, -1, v75\n\t"
        "s_cmp_lg_u64 vcc, 0\n\t"
        "s_cselect_b32 s78, 0x10000, 0\n\t"
        "v_or_b32 v30, s78, v30\n\t"
        "ds_add_rtn_u32 v107, v91, v29 offset:192\n\t"
        "v_dot4_u32_u8 v95, v79, s39, v7\n\t"
        "v_lshlrev_b32_sdwa v127, 1, v79 dst_sel:DWORD dst_unused:UNUSED_PAD src0_sel:DWORD src1_sel:WORD_0\n\t"
        "ds_read_u16 v127, v127 offset:46336\n\t"
        "v_cmp_eq_u16 vcc, -1, v79\n\t"
        "s_cmp_lg_u64 vcc, 0\n\t"
        "s_cselect_b32 s78, 0x10000, 0\n\t"
        "v_or_b32 v30, s78, v30\n\t"
        "ds_add_rtn_u32 v111, v95, v29 offset:192\n\t"
        "s_branch .L7_y\n"
        ".L7_z:\n\t"
        "s_waitcnt lgkmcnt(0)\n\t"
        "ds_read_b32 v32, v83 offset:192\n\t"
        "ds_read_b32 v33, v87 offset:192\n\t"
        "ds_read_b32 v34, v91 offset:192\n\t"
        "ds_read_b32 v35, v95 offset:192\n\t"
        "v_bfe_u32 v99, v99, s33, 8\n\t"
        "v_perm_b32 v115, v115, v67, s49\n\t"
        "v_bfe_u32 v103, v103, s33, 8\n\t"
        "v_perm_b32 v119, v119, v71, s49\n\t"
        "v_bfe_u32 v107, v107, s33, 8\n\t"
        "v_perm_b32 v123, v123, v75, s49\n\t"
        "v_bfe_u32 v111, v111, s33, 8\n\t"
        "v_perm_b32 v127, v127, v79, s49\n\t"
        "s_waitcnt lgkmcnt(0)\n\t"
        "v_dot4_u32_u8 v99, v32, s32, v99\n\t"
        "v_dot4_u32_u8 v103, v33, s32, v103\n\t"
        "v_dot4_u32_u8 v107, v34, s32, v107\n\t"
        "v_dot4_u32_u8 v111, v35, s32, v111\n\t"
        "v_min_u32 v99, 47, v99\n\t"
        "v_min_u32 v103, 47, v103\n\t"
        "v_min_u32 v107, 47, v107\n\t"
        "v_min_u32 v111, 47, v111\n\t"
        "v_lshl_add_u32 v99, v99, 2, v83\n\t"
        "ds_write_b32 v99, v115\n\t"
        "v_lshl_add_u32 v103, v103, 2, v87\n\t"
        "ds_write_b32 v103, v119\n\t"
        "v_lshl_add_u32 v107, v107, 2, v91\n\t"
        "ds_write_b32 v107, v123\n\t"
        "v_lshl_add_u32 v111, v111, 2, v95\n\t"
        "ds_write_b32 v111, v127\n\t"
        "s_branch .L7_zr\n"
        ".L7_exit4:\n\t"
        "s_add_u32 s69, s69, 4\n"
        ".L7_exit:\n\t"
        "s_waitcnt vmcnt(0) lgkmcnt(0)\n\t"
        "s_setprio 0\n\t"
        : "+{v0}"(tid), "=&{s69}"(wv)
        : "s"(__builtin_amdgcn_kernarg_segment_ptr()), "s"(blockIdx.x)
        : "scc", "vcc", "memory", "s16", "s17", "s18", "s19", "s20", "s21", "s22", "s23", "s24", "s25", "s26", "s27", "s28", "s29", "s30", "s31", "s32", "s33", "s34", "s35",
          "s36", "s37", "s38", "s39", "s40", "s41", "s42", "s43", "s44", "s45", "s46", "s47", "s48", "s49", "s50", "s51", "s52", "s53", "s54", "s55",
          "s56", "s57", "s58", "s59", "s60", "s61", "s62", "s63", "s64", "s65", "s66", "s67", "s68", "s70", "s71", "s72", "s73", "s74", "s75", "s76",
          "s77", "s78", "s79", "s80", "s81", "s82", "s83", "s84", "s85", "s86", "s87", "s88", "s89", "s90", "s91", "s92", "s93", "s94", "s95", "s96",
          "s97", "s98", "s99", "s100", "s101", "v1", "v2", "v3", "v4", "v5", "v6", "v7", "v8", "v9", "v10", "v11", "v12", "v13", "v14", "v15",
          "v16", "v17", "v18", "v19", "v20", "v21", "v22", "v23", "v24", "v25", "v26", "v27", "v28", "v29", "v30", "v31", "v32", "v33", "v34", "v35",
          "v36", "v37", "v38", "v39", "v40", "v41", "v42", "v43", "v44", "v45", "v46", "v47", "v48", "v49", "v50", "v51", "v52", "v53", "v54", "v55",
          "v56", "v57", "v58", "v59", "v60", "v61", "v62", "v63", "v64", "v65", "v66", "v67", "v68", "v69", "v70", "v71", "v72", "v73", "v74", "v75",
          "v76", "v77", "v78", "v79", "v80", "v81", "v82", "v83", "v84", "v85", "v86", "v87", "v88", "v89", "v90", "v91", "v92", "v93", "v94", "v95",
          "v96", "v97", "v98", "v99", "v100", "v101", "v102", "v103", "v104", "v105", "v106", "v107", "v108", "v109", "v110", "v111", "v112", "v113", "v114", "v115",
          "v116", "v117", "v118", "v119", "v120", "v121", "v122", "v123", "v124", "v125", "v126", "v127");
    const __attribute__((address_space(4))) Args* q = (const __attribute__((address_space(4))) Args*)__builtin_amdgcn_kernarg_segment_ptr();
    asm volatile("" : "+s"(q));
    const u32 wave = __builtin_amdgcn_readfirstlane(wv);
    u32 lane;
    asm volatile("v_mbcnt_lo_u32_b32 %0, -1, 0\n\tv_mbcnt_hi_u32_b32 %0, -1, %0" : "=v"(lane));
    const u32 half = blockIdx.x & 1u, TN = q->TN, r0 = (blockIdx.x >> 1) * TN;
    const u16* x = q->xall;
    _Pragma("nounroll") for (u32 n = 0; n < (1u << 26) && *(volatile __attribute__((address_space(3))) u32*)S_QK < 4u; ++n) __builtin_amdgcn_s_sleep(2);
    if (wave >= 4u) {
        const int E = q->E, *dst = (const int*)q->dst.p, *src = (const int*)q->src.p;
        const u16* dis = q->disall;
        const u32 w = wave - 4u;
        _Pragma("nounroll") for (u32 k = 0; k < 5u; ++k) {
            const u32 grp = (u32)(((w == 0u ? 0x1713090503ull : w == 1u ? 0x1612080402ull : w == 2u ? 0x14100e0a06ull : 0x15110f0b07ull) >> (8u * k)) & 255u);
            const u32 row = grp * 4u + (lane >> 4), iv = *(const u32*)(smem + L_INV + row * 4u);
            const int me = (iv & 0x8000u) ? -1 : q->nbase + (int)(r0 + (iv & 255u) + TN - 255u);
            const int m0 = __builtin_amdgcn_readlane(me, 0), m1 = __builtin_amdgcn_readlane(me, 16), m2 = __builtin_amdgcn_readlane(me, 32), m3 = __builtin_amdgcn_readlane(me, 48);
            float s[8] = {0.f, 0.f, 0.f, 0.f, 0.f, 0.f, 0.f, 0.f};
            for (int e0 = 0; e0 < E; e0 += 64) {
                const int e = min(e0 + (int)lane, E - 1), d = dst[e], si = src[e];
                unsigned long long m = __ballot(e0 + (int)lane < E && (d == m0 || d == m1 || d == m2 || d == m3));
                while (m) {
                    const int l = __builtin_ctzll(m);
                    m &= m - 1;
                    const int sl = __builtin_amdgcn_readlane(si, l);
                    if (__builtin_amdgcn_readlane(d, l) == me) {
                        const u32x4 r = *(const u32x4*)(x + (size_t)sl * 256 + half * 128u + (lane & 15u) * 8u);
                        const float wt = bf2f(dis[sl]);
#pragma unroll
                        for (int j = 0; j < 4; ++j) { s[2 * j] += wt * lo(r[j]); s[2 * j + 1] += wt * hi(r[j]); }
                    }
                }
            }
            const float sc = bf2f(dis[max(me, 0)]);
            *(u32x4*)(smem + L_AL + row * 256u + (((lane ^ row) & 15u) << 4)) =
                (u32x4){pack2(s[0] * sc, s[1] * sc), pack2(s[2] * sc, s[3] * sc), pack2(s[4] * sc, s[5] * sc), pack2(s[6] * sc, s[7] * sc)};
            lds_signal(S_T0 + 4u * max(grp >> 2, 1u), 1u);
        }
        return;
    }
    const int Ee = q->Ee, *edst = (const int*)q->edst.p, *esrc = (const int*)q->esrc.p;
    const u16 *wq = (const u16*)q->wq.p, *wk = (const u16*)q->wk.p;
    float* qs = (float*)(smem + L_QK + wave * 1024u);
    _Pragma("nounroll") for (u32 n = wave; n < TN && r0 + n < q->N; n += 4u) {
        const int me = q->nbase + (int)(r0 + n);
        float c0 = 0.f, c1 = 0.f, c2 = 0.f, c3 = 0.f;
        _Pragma("nounroll") for (int i = 0; i < 256; ++i) {
            const float xv = bf2f(x[(size_t)me * 256 + i]);
            const u32x2 r = *(const u32x2*)(wq + i * 256 + lane * 4u);
            c0 += xv * lo(r[0]); c1 += xv * hi(r[0]); c2 += xv * lo(r[1]); c3 += xv * hi(r[1]);
        }
        qs[lane * 4u] = c0; qs[lane * 4u + 1u] = c1; qs[lane * 4u + 2u] = c2; qs[lane * 4u + 3u] = c3;
        float k[4] = {0.f, 0.f, 0.f, 0.f};
        _Pragma("nounroll") for (int c = 0; c < 256; c += 2) {
            const float qa = qs[c], qb = qs[c + 1];
#pragma unroll
            for (int j = 0; j < 4; ++j) { const u32 r = *(const u32*)(wk + (lane * 4u + j) * 256u + c); k[j] += qa * lo(r) + qb * hi(r); }
        }
        float s0 = 0.f, s1 = 0.f, s2 = 0.f, s3 = 0.f, dn = 0.f;
        for (int e0 = 0; e0 < Ee; e0 += 64) {
            const int e = min(e0 + (int)lane, Ee - 1), si = esrc[e];
            unsigned long long m = __ballot(e0 + (int)lane < Ee && edst[e] == me);
            while (m) {
                const int l = __builtin_ctzll(m);
                m &= m - 1;
                const u32x2 r = *(const u32x2*)(x + (size_t)__builtin_amdgcn_readlane(si, l) * 256 + lane * 4u);
                float d = k[0] * lo(r[0]) + k[1] * hi(r[0]) + k[2] * lo(r[1]) + k[3] * hi(r[1]);
#pragma unroll
                for (int o = 1; o < 64; o <<= 1)
                    d += __uint_as_float((u32)__builtin_amdgcn_ds_bpermute((int)((lane ^ (u32)o) * 4u), (int)__float_as_uint(d)));
                const float wt = __builtin_amdgcn_exp2f(d * 0.09016844f);
                s0 += wt * lo(r[0]); s1 += wt * hi(r[0]); s2 += wt * lo(r[1]); s3 += wt * hi(r[1]);
                dn += wt;
            }
        }
        const float sc = __builtin_amdgcn_rcpf(dn + 1e-9f);
        if ((lane >> 5) == half)
            *(u32x2*)(smem + L_AA + ((u32)*(const u16*)(smem + L_MAP + n * 2u) ^ ((lane & 31u) * 8u))) =
                (u32x2){pack2(s0 * sc, s1 * sc), pack2(s2 * sc, s3 * sc)};
    }
    lds_signal(S_Z, 17u);
}

extern "C" void kernel_launch(void* const* d_in, const int* in_sizes, int n_in,
                              void* d_out, int out_size, void* d_ws, size_t ws_size,
                              hipStream_t stream) {
    const u32 N = in_sizes[5], E = in_sizes[1], Ee = in_sizes[3];
    const u16* x = (const u16*)d_in[0];
    const u16* dis = (const u16*)d_in[5];
    const u32 FL = 0x00020000u;
    const bool generic = N > NMAX || E > EMAX || (E & 3u) || (N & 1u);
    const bool irr = N > NMAX || Ee != 4u * N;
    (void)hipFuncSetAttribute((const void*)agg_kernel, hipFuncAttributeMaxDynamicSharedMemorySize, LDS_BYTES);
    for (u32 nbase = 0; nbase < N; nbase += NMAX) {
        const u32 n = N - nbase < NMAX ? N - nbase : NMAX;
        Args a;
        a.x = Desc{x + (size_t)nbase * 256, n * 512u, FL};
        a.ws = Desc{d_ws, WS_END, FL};
        a.wl = Desc{d_in[6], 131072u, FL}; a.wq = Desc{d_in[7], 131072u, FL}; a.wk = Desc{d_in[8], 131072u, FL}; a.wv = Desc{d_in[9], 131072u, FL};
        a.src = Desc{d_in[1], generic ? 0u : E * 4u, FL}; a.dst = Desc{d_in[2], generic ? 0u : E * 4u, FL};
        a.esrc = Desc{d_in[3], irr ? 0u : Ee * 4u, FL}; a.edst = Desc{d_in[4], irr ? 0u : Ee * 4u, FL};
        a.dis = Desc{dis + nbase, n * 2u, FL};
        a.out = Desc{(u16*)d_out + (size_t)nbase * 256, n * 512u, FL};
        a.N = n; a.TN = n > 256u ? (n + 127u) / 128u : 2u; a.M = ((1u << 24) + a.TN - 1u) / a.TN;
        a.irr = (irr ? 1u : 0u) | (generic ? 0x10000u : 0u);
        a.xall = x; a.disall = dis;
        a.E = (int)E; a.Ee = (int)Ee; a.nbase = (int)nbase; a.pad = 0;
        agg_kernel<<<256, 1024, LDS_BYTES, stream>>>(a);
    }
}
```

```cpp
#include <hip/hip_runtime.h>
#include <hip/hip_bf16.h>
#include <stdint.h>

typedef unsigned short u16;
typedef unsigned int u32;
typedef __attribute__((ext_vector_type(4))) u32 u32x4;
typedef __attribute__((ext_vector_type(2))) u32 u32x2;
struct Desc { const void* p; u32 bytes, flags; };
struct Args {
    Desc x, ws, wl, wv, wq, wk, src, dst, esrc, edst, dis, out;
    u32 N, TN, M, irr;
    const u16* xall;
    const u16* disall;
    int E, Ee, nbase, pad;
};
constexpr u32 NMAX = 10240, EMAX = 163840;
constexpr u32 WS_END = 9437184;
constexpr int LDS_BYTES = 163840;
constexpr u32 L_INV = 512, L_MAP = 1792, L_QK = 2048, L_AL = 23808, L_AA = 46336, S_QK = 16, S_Z = 40, S_T0 = 48;

__device__ __forceinline__ float bf2f(u32 h) { return __uint_as_float(h << 16); }
__device__ __forceinline__ float lo(u32 v) { return __uint_as_float(v << 16); }
__device__ __forceinline__ float hi(u32 v) { return __uint_as_float(v & 0xFFFF0000u); }
__device__ __forceinline__ u32 pack2(float a, float b) {
    return (u32)__builtin_bit_cast(u16, (__bf16)a) | ((u32)__builtin_bit_cast(u16, (__bf16)b) << 16);
}
__device__ __forceinline__ void lds_signal(const u32 addr, const u32 v) {
    asm volatile("s_mov_b64 exec, 1\n\tds_add_u32 %0, %1\n\ts_mov_b64 exec, -1" :: "v"(addr), "v"(v) : "memory");
}

extern "C" __global__ void __launch_bounds__(1024, 4)
agg_kernel(const Args a) {
    extern __shared__ __attribute__((aligned(16))) char smem[];
    u32 tid = threadIdx.x, wv;
    asm volatile(
        "s_memrealtime s[74:75]\n\t"
        "s_load_dwordx16 s[16:31], %2, 0\n\t"
        "s_load_dwordx16 s[32:47], %2, 64\n\t"
        "s_load_dwordx16 s[48:63], %2, 128\n\t"
        "s_load_dwordx4 s[64:67], %2, 192\n\t"
        "v_readfirstlane_b32 s69, v0\n\t"
        "v_and_b32 v1, 63, v0\n\t"
        "v_mov_b32 v2, 0\n\t"
        "v_mov_b32 v3, 1\n\t"
        "s_lshr_b32 s76, %3, 3\n\t"
        "s_and_b32 s70, %3, 1\n\t"
        "s_lshr_b32 s71, %3, 1\n\t"
        "s_mov_b32 s83, 0x2000000\n\t"
        "s_lshr_b32 s69, s69, 6\n\t"
        "s_cbranch_scc0 .L7_U\n\t"
        "s_cmp_lt_u32 s69, 8\n\t"
        "s_cbranch_scc1 .L7_L\n\t"
        "s_sub_u32 s69, s69, 8\n\t"
        "v_lshlrev_b32 v4, 2, v1\n\t"
        "s_lshl_b32 s77, s70, 2\n\t"
        "s_xor_b32 s77, s77, s69\n\t"
        "s_xor_b32 s77, s77, 4\n\t"
        "s_mul_i32 s80, s77, 10\n\t"
        "s_waitcnt lgkmcnt(0)\n\t"
        "s_mul_i32 s72, s71, s65\n\t"
        "s_add_u32 s78, s72, s80\n\t"
        "s_lshl_b32 s78, s78, 4\n\t"
        "s_mov_b32 exec_hi, 0xff\n\t"
        "buffer_load_dword v5, v4, s[48:51], s78 offen\n\t"
        "s_mov_b64 exec, -1\n\t"
        "s_barrier\n\t"
        "s_branch .L7_O2\n"
        ".L7_U:\n\t"
        "s_setprio 3\n\t"
        "s_cmp_eq_u32 s70, 0\n\t"
        "s_cbranch_scc0 .L7_V\n\t"
        "v_lshrrev_b32 v4, 3, v1\n\t"
        "v_and_b32 v5, 7, v1\n\t"
        "v_lshlrev_b32 v4, 14, v4\n\t"
        "v_lshl_add_u32 v4, v5, 4, v4\n\t"
        "s_lshl_b32 s78, s71, 7\n\t"
        "v_add_u32 v4, s78, v4\n\t"
        "v_add_u32 v5, 0x20000, v4\n\t"
        "v_add_u32 v6, 0x40000, v4\n\t"
        "v_add_u32 v7, 0x60000, v4\n\t"
        "v_add_u32 v8, 0x80000, v4\n\t"
        "s_waitcnt lgkmcnt(0)\n\t"
        "s_mul_i32 s72, s71, s65\n\t"
        "buffer_load_dwordx4 v[16:19], v4, s[44:47], 0 offen\n\t"
        "buffer_load_dwordx4 v[20:23], v5, s[44:47], 0 offen\n\t"
        "buffer_load_dwordx4 v[24:27], v6, s[44:47], 0 offen\n\t"
        "buffer_load_dwordx4 v[28:31], v7, s[44:47], 0 offen\n\t"
        "buffer_load_dwordx4 v[32:35], v8, s[44:47], 0 offen\n\t"
        "s_mul_i32 s80, s71, 0x50\n\t"
        "s_lshl_b32 s78, s80, 4\n\t"
        "v_lshlrev_b32 v12, 4, v1\n\t"
        "buffer_load_dwordx4 v[116:119], v12, s[52:55], s78 offen\n\t"
        "s_mov_b64 exec, 0xffff\n\t"
        "buffer_load_dwordx4 v[120:123], v12, s[52:55], s78 offen offset:1024\n\t"
        "s_mov_b64 exec, -1\n\t"
        "buffer_load_dwordx4 v[36:39], v4, s[40:43], 0 offen\n\t"
        "buffer_load_dwordx4 v[40:43], v5, s[40:43], 0 offen\n\t"
        "buffer_load_dwordx4 v[44:47], v6, s[40:43], 0 offen\n\t"
        "buffer_load_dwordx4 v[48:51], v7, s[40:43], 0 offen\n\t"
        "buffer_load_dwordx4 v[52:55], v8, s[40:43], 0 offen\n\t"
        "s_barrier\n\t"
        "s_branch .L7_U2\n"
        ".L7_V:\n\t"
        "s_and_b32 s78, s71, 1\n\t"
        "s_and_b32 s79, s76, 3\n\t"
        "s_lshl2_add_u32 s80, s78, s79\n\t"
        "s_and_b32 s78, s71, 2\n\t"
        "s_lshr_b32 s79, s76, 2\n\t"
        "s_lshl2_add_u32 s81, s78, s79\n\t"
        "v_lshlrev_b32 v12, 4, v1\n\t"
        "v_and_b32 v5, 0x200, v12\n\t"
        "v_mad_u32_u24 v4, v5, 15, v12\n\t"
        "s_lshl_b32 s82, s80, 14\n\t"
        "s_lshr_b32 s78, s81, 1\n\t"
        "s_lshl_b32 s78, s78, 14\n\t"
        "s_and_b32 s79, s81, 1\n\t"
        "s_lshl_b32 s79, s79, 11\n\t"
        "s_add_u32 s77, s78, s79\n\t"
        "s_mov_b32 m0, 68864\n\t"
        "s_waitcnt lgkmcnt(0)\n\t"
        "s_mul_i32 s72, s71, s65\n\t"
        "buffer_load_dwordx4 v4, s[36:39], s77 offen offset:0 lds\n\t"
        "s_add_u32 m0, m0, 544\n\t"
        "s_nop 0\n\t"
        "buffer_load_dwordx4 v4, s[36:39], s77 offen offset:512 lds\n\t"
        "s_add_u32 m0, m0, 544\n\t"
        "s_nop 0\n\t"
        "buffer_load_dwordx4 v4, s[36:39], s77 offen offset:1024 lds\n\t"
        "s_add_u32 m0, m0, 544\n\t"
        "s_nop 0\n\t"
        "buffer_load_dwordx4 v4, s[36:39], s77 offen offset:1536 lds\n\t"
        "s_add_u32 m0, m0, 2592\n\t"
        "s_add_u32 s77, s77, 0x1000\n\t"
        "buffer_load_dwordx4 v4, s[36:39], s77 offen offset:0 lds\n\t"
        "s_add_u32 m0, m0, 544\n\t"
        "s_nop 0\n\t"
        "buffer_load_dwordx4 v4, s[36:39], s77 offen offset:512 lds\n\t"
        "s_add_u32 m0, m0, 544\n\t"
        "s_nop 0\n\t"
        "buffer_load_dwordx4 v4, s[36:39], s77 offen offset:1024 lds\n\t"
        "s_add_u32 m0, m0, 544\n\t"
        "s_nop 0\n\t"
        "buffer_load_dwordx4 v4, s[36:39], s77 offen offset:1536 lds\n\t"
        "s_add_u32 m0, m0, 2592\n\t"
        "s_nop 0\n\t"
        "buffer_load_dwordx4 v4, s[32:35], s82 offen offset:0 lds\n\t"
        "s_add_u32 m0, m0, 544\n\t"
        "s_nop 0\n\t"
        "buffer_load_dwordx4 v4, s[32:35], s82 offen offset:512 lds\n\t"
        "s_add_u32 m0, m0, 544\n\t"
        "s_nop 0\n\t"
        "buffer_load_dwordx4 v4, s[32:35], s82 offen offset:1024 lds\n\t"
        "s_add_u32 m0, m0, 544\n\t"
        "s_nop 0\n\t"
        "buffer_load_dwordx4 v4, s[32:35], s82 offen offset:1536 lds\n\t"
        "s_add_u32 m0, m0, 2592\n\t"
        "s_add_u32 s82, s82, 0x1000\n\t"
        "buffer_load_dwordx4 v4, s[32:35], s82 offen offset:0 lds\n\t"
        "s_add_u32 m0, m0, 544\n\t"
        "s_nop 0\n\t"
        "buffer_load_dwordx4 v4, s[32:35], s82 offen offset:512 lds\n\t"
        "s_add_u32 m0, m0, 544\n\t"
        "s_nop 0\n\t"
        "buffer_load_dwordx4 v4, s[32:35], s82 offen offset:1024 lds\n\t"
        "s_add_u32 m0, m0, 544\n\t"
        "s_nop 0\n\t"
        "buffer_load_dwordx4 v4, s[32:35], s82 offen offset:1536 lds\n\t"
        "s_add_u32 m0, m0, 2592\n\t"
        "s_sub_u32 s82, s82, 0x800\n\t"
        "buffer_load_dwordx4 v4, s[32:35], s82 offen offset:0 lds\n\t"
        "s_add_u32 m0, m0, 544\n\t"
        "s_nop 0\n\t"
        "buffer_load_dwordx4 v4, s[32:35], s82 offen offset:512 lds\n\t"
        "s_add_u32 m0, m0, 544\n\t"
        "s_nop 0\n\t"
        "buffer_load_dwordx4 v4, s[32:35], s82 offen offset:1024 lds\n\t"
        "s_add_u32 m0, m0, 544\n\t"
        "s_nop 0\n\t"
        "buffer_load_dwordx4 v4, s[32:35], s82 offen offset:1536 lds\n\t"
        "s_add_u32 m0, m0, 2592\n\t"
        "s_add_u32 s82, s82, 0x1000\n\t"
        "buffer_load_dwordx4 v4, s[32:35], s82 offen offset:0 lds\n\t"
        "s_add_u32 m0, m0, 544\n\t"
        "s_nop 0\n\t"
        "buffer_load_dwordx4 v4, s[32:35], s82 offen offset:512 lds\n\t"
        "s_add_u32 m0, m0, 544\n\t"
        "s_nop 0\n\t"
        "buffer_load_dwordx4 v4, s[32:35], s82 offen offset:1024 lds\n\t"
        "s_add_u32 m0, m0, 544\n\t"
        "s_nop 0\n\t"
        "buffer_load_dwordx4 v4, s[32:35], s82 offen offset:1536 lds\n\t"
        "s_barrier\n\t"
        "s_branch .L7_V2\n"
        ".L7_L:\n\t"
        "s_cmp_lt_u32 s69, 4\n\t"
        "s_cbranch_scc0 .L7_G\n\t"
        "s_cmp_eq_u32 s69, 2\n\t"
        "s_cbranch_scc0 .L7_L1\n\t"
        "s_waitcnt lgkmcnt(0)\n\t"
        "s_mul_i32 s72, s71, s65\n\t"
        "s_buffer_load_dword s68, s[20:23], 0\n\t"
        "s_barrier\n\t"
        "s_branch .L7_P2\n"
        ".L7_L1:\n\t"
        "s_cmp_eq_u32 s69, 1\n\t"
        "s_cbranch_scc0 .L7_T\n\t"
        "v_lshlrev_b32 v4, 3, v1\n\t"
        "v_mov_b64 v[8:9], 0\n\t"
        "ds_write_b64 v4, v[8:9]\n\t"
        "s_waitcnt lgkmcnt(0)\n\t"
        "s_mul_i32 s72, s71, s65\n\t"
        "v_add_lshl_u32 v4, v1, s72, 1\n\t"
        "buffer_load_ushort v16, v4, s[56:59], 0 offen\n\t"
        "buffer_load_ushort v17, v4, s[56:59], 0 offen offset:128\n\t"
        "s_barrier\n\t"
        "s_branch .L7_R2\n"
        ".L7_T:\n\t"
        "v_lshlrev_b32 v6, 7, v1\n\t"
        "s_mov_b32 m0, 134400\n\t"
        "s_lshl_b32 s78, s70, 16\n\t"
        "s_lshl_b32 s79, s76, 11\n\t"
        "s_add_u32 s80, s78, s79\n\t"
        "s_mul_i32 s81, s76, 0x300\n\t"
        "s_lshl_b32 s82, %3, 7\n\t"
        "s_add_u32 s82, s82, 4096\n\t"
        "v_lshlrev_b32 v8, 2, v1\n\t"
        "s_waitcnt lgkmcnt(0)\n\t"
        "s_mul_i32 s72, s71, s65\n\t"
        "v_mov_b32 v7, s74\n\t"
        "s_mov_b32 exec_hi, 0\n\t"
        "buffer_store_dword v7, v8, s[20:23], s82 offen sc1\n\t"
        "s_getpc_b64 s[32:33]\n\t"
        "s_add_u32 s32, s32, agg_kernel@rel32@lo+4\n\t"
        "s_addc_u32 s33, s33, agg_kernel@rel32@hi+12\n\t"
        "s_and_b32 s33, s33, 0xffff\n\t"
        "s_mov_b32 s34, .L7_x-agg_kernel\n\t"
        "s_mov_b64 exec, 63\n\t"
        "buffer_load_dword v6, s[32:35], s81 offen lds\n\t"
        "s_barrier\n\t"
        "s_mov_b64 exec, -1\n\t"
        "s_mov_b64 exec, 0xffff\n\t"
        "buffer_load_dword v6, s[28:31], s80 offen lds\n\t"
        "buffer_load_dword v6, s[24:27], s80 offen lds\n\t"
        "s_mov_b64 exec, -1\n\t"
        "s_branch .L7_T2\n"
        ".L7_G:\n\t"
        "s_sub_u32 s69, s69, 4\n\t"
        "v_lshlrev_b32 v4, 4, v1\n\t"
        "v_and_b32 v5, 0x200, v4\n\t"
        "v_mad_u32_u24 v4, v5, 7, v4\n\t"
        "s_mul_i32 s80, s70, 5\n\t"
        "s_mul_i32 s81, s69, 0x420\n\t"
        "s_add_u32 s81, s81, 2048\n\t"
        "s_mul_i32 s78, s80, 0x1080\n\t"
        "s_add_u32 m0, s78, s81\n\t"
        "s_lshr_b32 s78, s80, 1\n\t"
        "s_lshl1_add_u32 s78, s78, s80\n\t"
        "s_lshl_b32 s78, s78, 11\n\t"
        "s_waitcnt lgkmcnt(0)\n\t"
        "s_mul_i32 s72, s71, s65\n\t"
        "s_add_u32 s79, s72, s69\n\t"
        "s_lshl_b32 s79, s79, 9\n\t"
        "v_add_u32 v4, s79, v4\n\t"
        "buffer_load_dwordx4 v4, s[16:19], s78 offen lds\n\t"
        "s_add_u32 s80, s80, 1\n\t"
        "s_mul_i32 s78, s80, 0x1080\n\t"
        "s_add_u32 m0, s78, s81\n\t"
        "s_lshr_b32 s78, s80, 1\n\t"
        "s_lshl1_add_u32 s78, s78, s80\n\t"
        "s_lshl_b32 s78, s78, 11\n\t"
        "buffer_load_dwordx4 v4, s[16:19], s78 offen lds\n\t"
        "s_add_u32 s80, s80, 1\n\t"
        "s_mul_i32 s78, s80, 0x1080\n\t"
        "s_add_u32 m0, s78, s81\n\t"
        "s_lshr_b32 s78, s80, 1\n\t"
        "s_lshl1_add_u32 s78, s78, s80\n\t"
        "s_lshl_b32 s78, s78, 11\n\t"
        "buffer_load_dwordx4 v4, s[16:19], s78 offen lds\n\t"
        "s_add_u32 s80, s80, 1\n\t"
        "s_mul_i32 s78, s80, 0x1080\n\t"
        "s_add_u32 m0, s78, s81\n\t"
        "s_lshr_b32 s78, s80, 1\n\t"
        "s_lshl1_add_u32 s78, s78, s80\n\t"
        "s_lshl_b32 s78, s78, 11\n\t"
        "buffer_load_dwordx4 v4, s[16:19], s78 offen lds\n\t"
        "s_add_u32 s80, s80, 1\n\t"
        "s_mul_i32 s78, s80, 0x1080\n\t"
        "s_add_u32 m0, s78, s81\n\t"
        "s_lshr_b32 s78, s80, 1\n\t"
        "s_lshl1_add_u32 s78, s78, s80\n\t"
        "s_lshl_b32 s78, s78, 11\n\t"
        "buffer_load_dwordx4 v4, s[16:19], s78 offen lds\n\t"
        "s_add_u32 s80, s80, 1\n\t"
        "s_barrier\n\t"
        "s_branch .L7_G2\n"
        ".L7_U2:\n\t"
        "v_cmp_gt_u32 s[24:25], s46, v4\n\t"
        "v_cmp_gt_u32 s[26:27], s46, v5\n\t"
        "v_cmp_gt_u32 s[28:29], s46, v6\n\t"
        "v_cmp_gt_u32 s[30:31], s46, v7\n\t"
        "v_cmp_gt_u32 s[32:33], s46, v8\n\t"
        "v_mul_u32_u24 v112, 0xa0, v1\n\t"
        "v_add_u32 v112, 68864, v112\n\t"
        "v_add_u32 v113, 10240, v112\n\t"
        "v_lshlrev_b32 v7, 2, v1\n\t"
        "ds_write_b32 v7, v112 offset:1028\n\t"
        "ds_write_b32 v7, v113 offset:1284\n\t"
        "v_lshrrev_b32 v15, 3, v1\n\t"
        "v_mul_u32_u24 v15, 0xa0, v15\n\t"
        "v_add_u32 v15, 68864, v15\n\t"
        "v_and_b32 v4, 7, v1\n\t"
        "v_lshl_add_u32 v15, v4, 2, v15\n\t"
        "v_mov_b32 v13, 0x1000000\n\t"
        "v_mov_b32 v14, 4\n\t"
        "s_lshl_b32 s54, s65, 14\n\t"
        "s_sub_u32 s54, 0, s54\n\t"
        "s_lshl_b32 s48, s71, 14\n\t"
        "s_add_u32 s48, s48, 1048576\n\t"
        "s_add_u32 s49, s48, 4096\n\t"
        "v_add_u32 v97, 1280, v15\n\t"
        "v_add_u32 v98, 2560, v15\n\t"
        "v_add_u32 v99, 3840, v15\n\t"
        "v_add_u32 v100, 5120, v15\n\t"
        "v_add_u32 v101, 6400, v15\n\t"
        "v_add_u32 v102, 7680, v15\n\t"
        "v_add_u32 v103, 8960, v15\n\t"
        "v_add_u32 v112, 0x80, v112\n\t"
        "v_add_u32 v113, 0x80, v113\n\t"
        "v_add_u32 v114, s80, v1\n\t"
        "v_add_u32 v115, 64, v114\n\t"
        "v_cmp_gt_u32 s[36:37], s64, v114\n\t"
        "v_cmp_gt_u32 s[38:39], s64, v115\n\t"
        "s_waitcnt vmcnt(11) lgkmcnt(8)\n\t"
        "s_mov_b64 exec, s[24:25]\n\t"
        "v_mad_u32_u24 v56, v16, s66, v13\n\t"
        "v_mad_u32_u24 v57, v17, s66, v13\n\t"
        "v_mad_u32_u24 v58, v18, s66, v13\n\t"
        "v_mad_u32_u24 v59, v19, s66, v13\n\t"
        "v_lshlrev_b32_sdwa v56, 2, v56 dst_sel:DWORD dst_unused:UNUSED_PAD src0_sel:DWORD src1_sel:BYTE_3\n\t"
        "ds_add_rtn_u32 v76, v56, v14 offset:1024\n\t"
        "v_lshlrev_b32_sdwa v57, 2, v57 dst_sel:DWORD dst_unused:UNUSED_PAD src0_sel:DWORD src1_sel:BYTE_3\n\t"
        "ds_add_rtn_u32 v77, v57, v14 offset:1024\n\t"
        "v_lshlrev_b32_sdwa v58, 2, v58 dst_sel:DWORD dst_unused:UNUSED_PAD src0_sel:DWORD src1_sel:BYTE_3\n\t"
        "ds_add_rtn_u32 v78, v58, v14 offset:1024\n\t"
        "v_lshlrev_b32_sdwa v59, 2, v59 dst_sel:DWORD dst_unused:UNUSED_PAD src0_sel:DWORD src1_sel:BYTE_3\n\t"
        "ds_add_rtn_u32 v79, v59, v14 offset:1024\n\t"
        "s_waitcnt vmcnt(10) lgkmcnt(8)\n\t"
        "s_mov_b64 exec, s[26:27]\n\t"
        "v_mad_u32_u24 v60, v20, s66, v13\n\t"
        "v_mad_u32_u24 v61, v21, s66, v13\n\t"
        "v_mad_u32_u24 v62, v22, s66, v13\n\t"
        "v_mad_u32_u24 v63, v23, s66, v13\n\t"
        "v_lshlrev_b32_sdwa v60, 2, v60 dst_sel:DWORD dst_unused:UNUSED_PAD src0_sel:DWORD src1_sel:BYTE_3\n\t"
        "ds_add_rtn_u32 v80, v60, v14 offset:1024\n\t"
        "v_lshlrev_b32_sdwa v61, 2, v61 dst_sel:DWORD dst_unused:UNUSED_PAD src0_sel:DWORD src1_sel:BYTE_3\n\t"
        "ds_add_rtn_u32 v81, v61, v14 offset:1024\n\t"
        "v_lshlrev_b32_sdwa v62, 2, v62 dst_sel:DWORD dst_unused:UNUSED_PAD src0_sel:DWORD src1_sel:BYTE_3\n\t"
        "ds_add_rtn_u32 v82, v62, v14 offset:1024\n\t"
        "v_lshlrev_b32_sdwa v63, 2, v63 dst_sel:DWORD dst_unused:UNUSED_PAD src0_sel:DWORD src1_sel:BYTE_3\n\t"
        "ds_add_rtn_u32 v83, v63, v14 offset:1024\n\t"
        "s_waitcnt vmcnt(9) lgkmcnt(8)\n\t"
        "s_mov_b64 exec, s[28:29]\n\t"
        "v_mad_u32_u24 v64, v24, s66, v13\n\t"
        "v_mad_u32_u24 v65, v25, s66, v13\n\t"
        "v_mad_u32_u24 v66, v26, s66, v13\n\t"
        "v_mad_u32_u24 v67, v27, s66, v13\n\t"
        "v_lshlrev_b32_sdwa v64, 2, v64 dst_sel:DWORD dst_unused:UNUSED_PAD src0_sel:DWORD src1_sel:BYTE_3\n\t"
        "ds_add_rtn_u32 v84, v64, v14 offset:1024\n\t"
        "v_lshlrev_b32_sdwa v65, 2, v65 dst_sel:DWORD dst_unused:UNUSED_PAD src0_sel:DWORD src1_sel:BYTE_3\n\t"
        "ds_add_rtn_u32 v85, v65, v14 offset:1024\n\t"
        "v_lshlrev_b32_sdwa v66, 2, v66 dst_sel:DWORD dst_unused:UNUSED_PAD src0_sel:DWORD src1_sel:BYTE_3\n\t"
        "ds_add_rtn_u32 v86, v66, v14 offset:1024\n\t"
        "v_lshlrev_b32_sdwa v67, 2, v67 dst_sel:DWORD dst_unused:UNUSED_PAD src0_sel:DWORD src1_sel:BYTE_3\n\t"
        "ds_add_rtn_u32 v87, v67, v14 offset:1024\n\t"
        "s_waitcnt vmcnt(8) lgkmcnt(8)\n\t"
        "s_mov_b64 exec, s[30:31]\n\t"
        "v_mad_u32_u24 v68, v28, s66, v13\n\t"
        "v_mad_u32_u24 v69, v29, s66, v13\n\t"
        "v_mad_u32_u24 v70, v30, s66, v13\n\t"
        "v_mad_u32_u24 v71, v31, s66, v13\n\t"
        "v_lshlrev_b32_sdwa v68, 2, v68 dst_sel:DWORD dst_unused:UNUSED_PAD src0_sel:DWORD src1_sel:BYTE_3\n\t"
        "ds_add_rtn_u32 v88, v68, v14 offset:1024\n\t"
        "v_lshlrev_b32_sdwa v69, 2, v69 dst_sel:DWORD dst_unused:UNUSED_PAD src0_sel:DWORD src1_sel:BYTE_3\n\t"
        "ds_add_rtn_u32 v89, v69, v14 offset:1024\n\t"
        "v_lshlrev_b32_sdwa v70, 2, v70 dst_sel:DWORD dst_unused:UNUSED_PAD src0_sel:DWORD src1_sel:BYTE_3\n\t"
        "ds_add_rtn_u32 v90, v70, v14 offset:1024\n\t"
        "v_lshlrev_b32_sdwa v71, 2, v71 dst_sel:DWORD dst_unused:UNUSED_PAD src0_sel:DWORD src1_sel:BYTE_3\n\t"
        "ds_add_rtn_u32 v91, v71, v14 offset:1024\n\t"
        "s_waitcnt vmcnt(7) lgkmcnt(8)\n\t"
        "s_mov_b64 exec, s[32:33]\n\t"
        "v_mad_u32_u24 v72, v32, s66, v13\n\t"
        "v_mad_u32_u24 v73, v33, s66, v13\n\t"
        "v_mad_u32_u24 v74, v34, s66, v13\n\t"
        "v_mad_u32_u24 v75, v35, s66, v13\n\t"
        "v_lshlrev_b32_sdwa v72, 2, v72 dst_sel:DWORD dst_unused:UNUSED_PAD src0_sel:DWORD src1_sel:BYTE_3\n\t"
        "ds_add_rtn_u32 v92, v72, v14 offset:1024\n\t"
        "v_lshlrev_b32_sdwa v73, 2, v73 dst_sel:DWORD dst_unused:UNUSED_PAD src0_sel:DWORD src1_sel:BYTE_3\n\t"
        "ds_add_rtn_u32 v93, v73, v14 offset:1024\n\t"
        "v_lshlrev_b32_sdwa v74, 2, v74 dst_sel:DWORD dst_unused:UNUSED_PAD src0_sel:DWORD src1_sel:BYTE_3\n\t"
        "ds_add_rtn_u32 v94, v74, v14 offset:1024\n\t"
        "v_lshlrev_b32_sdwa v75, 2, v75 dst_sel:DWORD dst_unused:UNUSED_PAD src0_sel:DWORD src1_sel:BYTE_3\n\t"
        "ds_add_rtn_u32 v95, v75, v14 offset:1024\n\t"
        "s_mov_b64 exec, -1\n\t"
        "ds_read_b32 v4, v7 offset:1028\n\t"
        "ds_read_b32 v5, v7 offset:1284\n\t"
        "s_waitcnt vmcnt(5)\n\t"
        "v_xor_b32 v116, v116, v114\n\t"
        "v_bitop3_b32 v116, v117, v116, v114 bitop3:0xde\n\t"
        "v_bitop3_b32 v116, v118, v116, v114 bitop3:0xde\n\t"
        "v_bitop3_b32 v116, v119, v116, v114 bitop3:0xde\n\t"
        "v_xor_b32 v120, v120, v115\n\t"
        "v_bitop3_b32 v120, v121, v120, v115 bitop3:0xde\n\t"
        "v_bitop3_b32 v120, v122, v120, v115 bitop3:0xde\n\t"
        "v_bitop3_b32 v120, v123, v120, v115 bitop3:0xde\n\t"
        "v_cmp_ne_u32 s[40:41], 0, v116\n\t"
        "v_cmp_ne_u32 s[42:43], 0, v120\n\t"
        "s_and_b64 s[38:39], s[38:39], 0xffff\n\t"
        "s_and_b64 s[36:37], s[36:37], s[40:41]\n\t"
        "s_and_b64 s[38:39], s[38:39], s[42:43]\n\t"
        "s_or_b64 s[36:37], s[36:37], s[38:39]\n\t"
        "s_cselect_b32 s67, 1, s67\n"
        ".L7_w38:\n\t"
        "ds_read_b32 v0, v2 offset:104\n\t"
        "ds_read_b32 v6, v2 offset:76\n\t"
        "s_waitcnt lgkmcnt(1)\n\t"
        "v_cmp_gt_u16 vcc, 1, v0\n\t"
        "s_cbranch_vccz .L7_d39\n\t"
        "s_waitcnt lgkmcnt(0)\n\t"
        "s_sleep 0\n\t"
        "s_sub_i32 s83, s83, 1\n\t"
        "s_cmp_gt_i32 s83, 0\n\t"
        "s_cbranch_scc1 .L7_w38\n"
        ".L7_d39:\n\t"
        "v_cmp_gt_u32 s[34:35], v4, v112\n\t"
        "v_cmp_gt_u32 s[36:37], v5, v113\n\t"
        "s_or_b64 s[38:39], s[34:35], s[36:37]\n\t"
        "s_cbranch_scc1 .L7_ov\n"
        ".L7_ov1:\n\t"
        "s_waitcnt lgkmcnt(0)\n\t"
        "v_readfirstlane_b32 s68, v6\n\t"
        "s_waitcnt vmcnt(4) lgkmcnt(8)\n\t"
        "s_mov_b64 exec, s[24:25]\n\t"
        "v_lshl_add_u32 v16, v16, 16, v36\n\t"
        "v_lshl_add_u32 v17, v17, 16, v37\n\t"
        "v_lshl_add_u32 v18, v18, 16, v38\n\t"
        "v_lshl_add_u32 v19, v19, 16, v39\n\t"
        "v_mad_i32_i24 v16, v56, s54, v16\n\t"
        "ds_add_u32 v76, v16\n\t"
        "v_mad_i32_i24 v17, v57, s54, v17\n\t"
        "ds_add_u32 v77, v17\n\t"
        "v_mad_i32_i24 v18, v58, s54, v18\n\t"
        "ds_add_u32 v78, v18\n\t"
        "v_mad_i32_i24 v19, v59, s54, v19\n\t"
        "ds_add_u32 v79, v19\n\t"
        "s_waitcnt vmcnt(3) lgkmcnt(8)\n\t"
        "s_mov_b64 exec, s[26:27]\n\t"
        "v_lshl_add_u32 v20, v20, 16, v40\n\t"
        "v_lshl_add_u32 v21, v21, 16, v41\n\t"
        "v_lshl_add_u32 v22, v22, 16, v42\n\t"
        "v_lshl_add_u32 v23, v23, 16, v43\n\t"
        "v_mad_i32_i24 v20, v60, s54, v20\n\t"
        "ds_add_u32 v80, v20\n\t"
        "v_mad_i32_i24 v21, v61, s54, v21\n\t"
        "ds_add_u32 v81, v21\n\t"
        "v_mad_i32_i24 v22, v62, s54, v22\n\t"
        "ds_add_u32 v82, v22\n\t"
        "v_mad_i32_i24 v23, v63, s54, v23\n\t"
        "ds_add_u32 v83, v23\n\t"
        "s_waitcnt vmcnt(2) lgkmcnt(8)\n\t"
        "s_mov_b64 exec, s[28:29]\n\t"
        "v_lshl_add_u32 v24, v24, 16, v44\n\t"
        "v_lshl_add_u32 v25, v25, 16, v45\n\t"
        "v_lshl_add_u32 v26, v26, 16, v46\n\t"
        "v_lshl_add_u32 v27, v27, 16, v47\n\t"
        "v_mad_i32_i24 v24, v64, s54, v24\n\t"
        "ds_add_u32 v84, v24\n\t"
        "v_mad_i32_i24 v25, v65, s54, v25\n\t"
        "ds_add_u32 v85, v25\n\t"
        "v_mad_i32_i24 v26, v66, s54, v26\n\t"
        "ds_add_u32 v86, v26\n\t"
        "v_mad_i32_i24 v27, v67, s54, v27\n\t"
        "ds_add_u32 v87, v27\n\t"
        "s_waitcnt vmcnt(1) lgkmcnt(8)\n\t"
        "s_mov_b64 exec, s[30:31]\n\t"
        "v_lshl_add_u32 v28, v28, 16, v48\n\t"
        "v_lshl_add_u32 v29, v29, 16, v49\n\t"
        "v_lshl_add_u32 v30, v30, 16, v50\n\t"
        "v_lshl_add_u32 v31, v31, 16, v51\n\t"
        "v_mad_i32_i24 v28, v68, s54, v28\n\t"
        "ds_add_u32 v88, v28\n\t"
        "v_mad_i32_i24 v29, v69, s54, v29\n\t"
        "ds_add_u32 v89, v29\n\t"
        "v_mad_i32_i24 v30, v70, s54, v30\n\t"
        "ds_add_u32 v90, v30\n\t"
        "v_mad_i32_i24 v31, v71, s54, v31\n\t"
        "ds_add_u32 v91, v31\n\t"
        "s_waitcnt vmcnt(0) lgkmcnt(8)\n\t"
        "s_mov_b64 exec, s[32:33]\n\t"
        "v_lshl_add_u32 v32, v32, 16, v52\n\t"
        "v_lshl_add_u32 v33, v33, 16, v53\n\t"
        "v_lshl_add_u32 v34, v34, 16, v54\n\t"
        "v_lshl_add_u32 v35, v35, 16, v55\n\t"
        "v_mad_i32_i24 v32, v72, s54, v32\n\t"
        "ds_add_u32 v92, v32\n\t"
        "v_mad_i32_i24 v33, v73, s54, v33\n\t"
        "ds_add_u32 v93, v33\n\t"
        "v_mad_i32_i24 v34, v74, s54, v34\n\t"
        "ds_add_u32 v94, v34\n\t"
        "v_mad_i32_i24 v35, v75, s54, v35\n\t"
        "ds_add_u32 v95, v35\n\t"
        "s_mov_b64 exec, -1\n\t"
        "s_and_b32 s78, s67, 1\n\t"
        "s_or_b32 s78, s78, s38\n\t"
        "s_or_b32 s78, s78, s39\n\t"
        "s_cbranch_scc1 .L7_mk\n"
        ".L7_mk1:\n\t"
        "s_mov_b64 exec, 1\n\t"
        "ds_add_u32 v2, v3 offset:112\n\t"
        "s_mov_b64 exec, -1\n\t"
        "s_waitcnt lgkmcnt(3)\n\t"
        "ds_read2_b32 v[16:17], v15 offset0:0 offset1:8\n\t"
        "ds_read2_b32 v[18:19], v15 offset0:16 offset1:24\n\t"
        "ds_read2_b32 v[20:21], v97 offset0:0 offset1:8\n\t"
        "ds_read2_b32 v[22:23], v97 offset0:16 offset1:24\n\t"
        "ds_read2_b32 v[24:25], v98 offset0:0 offset1:8\n\t"
        "ds_read2_b32 v[26:27], v98 offset0:16 offset1:24\n\t"
        "ds_read2_b32 v[28:29], v99 offset0:0 offset1:8\n\t"
        "ds_read2_b32 v[30:31], v99 offset0:16 offset1:24\n\t"
        "ds_read2_b32 v[32:33], v100 offset0:0 offset1:8\n\t"
        "ds_read2_b32 v[34:35], v100 offset0:16 offset1:24\n\t"
        "ds_read2_b32 v[36:37], v101 offset0:0 offset1:8\n\t"
        "ds_read2_b32 v[38:39], v101 offset0:16 offset1:24\n\t"
        "s_waitcnt lgkmcnt(10)\n\t"
        "buffer_store_dwordx4 v[16:19], v12, s[20:23], s48 offen offset:0 sc1\n\t"
        "ds_read2_b32 v[40:41], v102 offset0:0 offset1:8\n\t"
        "ds_read2_b32 v[42:43], v102 offset0:16 offset1:24\n\t"
        "s_waitcnt lgkmcnt(10)\n\t"
        "buffer_store_dwordx4 v[20:23], v12, s[20:23], s48 offen offset:1024 sc1\n\t"
        "ds_read2_b32 v[44:45], v103 offset0:0 offset1:8\n\t"
        "ds_read2_b32 v[46:47], v103 offset0:16 offset1:24\n\t"
        "s_mov_b64 exec, 1\n\t"
        "ds_add_u32 v2, v3 offset:72\n\t"
        "s_mov_b64 exec, -1\n\t"
        "s_waitcnt lgkmcnt(11)\n\t"
        "buffer_store_dwordx4 v[24:27], v12, s[20:23], s48 offen offset:2048 sc1\n\t"
        "s_waitcnt lgkmcnt(9)\n\t"
        "buffer_store_dwordx4 v[28:31], v12, s[20:23], s48 offen offset:3072 sc1\n\t"
        "s_waitcnt lgkmcnt(7)\n\t"
        "buffer_store_dwordx4 v[32:35], v12, s[20:23], s49 offen offset:0 sc1\n\t"
        "s_waitcnt lgkmcnt(5)\n\t"
        "buffer_store_dwordx4 v[36:39], v12, s[20:23], s49 offen offset:1024 sc1\n\t"
        "s_waitcnt lgkmcnt(3)\n\t"
        "buffer_store_dwordx4 v[40:43], v12, s[20:23], s49 offen offset:2048 sc1\n\t"
        "s_waitcnt lgkmcnt(1)\n\t"
        "buffer_store_dwordx4 v[44:47], v12, s[20:23], s49 offen offset:3072 sc1\n\t"
        "s_mov_b64 exec, 1\n\t"
        "ds_add_u32 v2, v3 offset:44\n\t"
        "s_mov_b64 exec, -1\n\t"
        "s_branch .L7_Q\n"
        ".L7_ov:\n\t"
        "s_mov_b32 s78, 68828\n\t"
        "v_mov_b32 v9, 40\n\t"
        "v_mad_u32_u24 v8, v56, v9, s78\n\t"
        "v_min_u32 v76, v76, v8\n\t"
        "v_mad_u32_u24 v8, v57, v9, s78\n\t"
        "v_min_u32 v77, v77, v8\n\t"
        "v_mad_u32_u24 v8, v58, v9, s78\n\t"
        "v_min_u32 v78, v78, v8\n\t"
        "v_mad_u32_u24 v8, v59, v9, s78\n\t"
        "v_min_u32 v79, v79, v8\n\t"
        "v_mad_u32_u24 v8, v60, v9, s78\n\t"
        "v_min_u32 v80, v80, v8\n\t"
        "v_mad_u32_u24 v8, v61, v9, s78\n\t"
        "v_min_u32 v81, v81, v8\n\t"
        "v_mad_u32_u24 v8, v62, v9, s78\n\t"
        "v_min_u32 v82, v82, v8\n\t"
        "v_mad_u32_u24 v8, v63, v9, s78\n\t"
        "v_min_u32 v83, v83, v8\n\t"
        "v_mad_u32_u24 v8, v64, v9, s78\n\t"
        "v_min_u32 v84, v84, v8\n\t"
        "v_mad_u32_u24 v8, v65, v9, s78\n\t"
        "v_min_u32 v85, v85, v8\n\t"
        "v_mad_u32_u24 v8, v66, v9, s78\n\t"
        "v_min_u32 v86, v86, v8\n\t"
        "v_mad_u32_u24 v8, v67, v9, s78\n\t"
        "v_min_u32 v87, v87, v8\n\t"
        "v_mad_u32_u24 v8, v68, v9, s78\n\t"
        "v_min_u32 v88, v88, v8\n\t"
        "v_mad_u32_u24 v8, v69, v9, s78\n\t"
        "v_min_u32 v89, v89, v8\n\t"
        "v_mad_u32_u24 v8, v70, v9, s78\n\t"
        "v_min_u32 v90, v90, v8\n\t"
        "v_mad_u32_u24 v8, v71, v9, s78\n\t"
        "v_min_u32 v91, v91, v8\n\t"
        "v_mad_u32_u24 v8, v72, v9, s78\n\t"
        "v_min_u32 v92, v92, v8\n\t"
        "v_mad_u32_u24 v8, v73, v9, s78\n\t"
        "v_min_u32 v93, v93, v8\n\t"
        "v_mad_u32_u24 v8, v74, v9, s78\n\t"
        "v_min_u32 v94, v94, v8\n\t"
        "v_mad_u32_u24 v8, v75, v9, s78\n\t"
        "v_min_u32 v95, v95, v8\n\t"
        "s_branch .L7_ov1\n"
        ".L7_mk:\n\t"
        "s_mul_i32 s78, s68, 0x02000200\n\t"
        "s_or_b32 s78, s78, 0xffffff\n\t"
        "v_mov_b32 v6, s78\n\t"
        "v_add_u32 v112, -4, v112\n\t"
        "v_add_u32 v113, -4, v113\n\t"
        "s_mov_b64 exec, s[34:35]\n\t"
        "ds_write_b32 v112, v6\n\t"
        "s_mov_b64 exec, s[36:37]\n\t"
        "ds_write_b32 v113, v6\n\t"
        "s_mov_b64 exec, -1\n\t"
        "s_and_b32 s78, s67, 1\n\t"
        "s_lshl_b32 s78, s78, 24\n\t"
        "v_mov_b32 v6, s78\n\t"
        "ds_xor_b32 v112, v6\n\t"
        "ds_xor_b32 v113, v6\n\t"
        "s_branch .L7_mk1\n"
        ".L7_P2:\n\t"
        "s_setprio 3\n\t"
        "v_lshrrev_b32 v15, 3, v1\n\t"
        "v_mul_u32_u24 v15, 0xa0, v15\n\t"
        "v_add_u32 v15, 68864, v15\n\t"
        "v_and_b32 v4, 7, v1\n\t"
        "v_lshl_add_u32 v5, v4, 4, v15\n\t"
        "v_lshl_add_u32 v15, v4, 2, v15\n\t"
        "s_mov_b32 s34, 0x0c0c0c0c\n\t"
        "s_mov_b32 s35, s34\n\t"
        "s_lshl_b64 s[36:37], s[34:35], 2\n\t"
        "s_lshl_b64 s[38:39], s[34:35], 4\n\t"
        "s_waitcnt lgkmcnt(0)\n\t"
        "s_add_u32 s68, s68, 1\n\t"
        "v_mov_b32 v4, s68\n\t"
        "ds_write_b32 v2, v4 offset:76\n\t"
        "s_cmp_eq_u32 s70, 0\n\t"
        "s_cbranch_scc0 .L7_P3\n\t"
        "s_lshl_b32 s78, s68, 24\n\t"
        "v_mov_b32 v8, s78\n\t"
        "s_xor_b32 s78, s78, 0x5a000000\n\t"
        "v_mov_b32 v9, s78\n\t"
        "v_cndmask_b32 v8, v8, v9, s[34:35]\n\t"
        "s_mul_i32 s78, s68, 0x01010000\n\t"
        "v_mov_b32 v9, s78\n\t"
        "v_cndmask_b32 v8, v8, v9, s[36:37]\n\t"
        "s_mul_i32 s55, s68, 0x02000200\n\t"
        "v_mov_b32 v9, s55\n\t"
        "v_cndmask_b32 v8, v8, v9, s[38:39]\n\t"
        "v_and_b32 v8, 0xff000000, v8\n\t"
        "v_or_b32 v8, 0xff0000, v8\n\t"
        "v_mov_b32 v9, v8\n\t"
        "v_mov_b64 v[10:11], v[8:9]\n\t"
        "ds_write_b128 v5, v[8:11] offset:0\n\t"
        "ds_write_b128 v5, v[8:11] offset:1280\n\t"
        "ds_write_b128 v5, v[8:11] offset:2560\n\t"
        "ds_write_b128 v5, v[8:11] offset:3840\n\t"
        "ds_write_b128 v5, v[8:11] offset:5120\n\t"
        "ds_write_b128 v5, v[8:11] offset:6400\n\t"
        "ds_write_b128 v5, v[8:11] offset:7680\n\t"
        "ds_write_b128 v5, v[8:11] offset:8960\n\t"
        "ds_write_b128 v5, v[8:11] offset:10240\n\t"
        "ds_write_b128 v5, v[8:11] offset:11520\n\t"
        "ds_write_b128 v5, v[8:11] offset:12800\n\t"
        "ds_write_b128 v5, v[8:11] offset:14080\n\t"
        "s_waitcnt lgkmcnt(6)\n\t"
        "ds_write_b128 v5, v[8:11] offset:15360\n\t"
        "ds_write_b128 v5, v[8:11] offset:16640\n\t"
        "ds_write_b128 v5, v[8:11] offset:17920\n\t"
        "ds_write_b128 v5, v[8:11] offset:19200\n"
        ".L7_P3:\n\t"
        "s_mov_b64 exec, 1\n\t"
        "ds_add_u32 v2, v3 offset:104\n\t"
        "s_mov_b64 exec, -1\n\t"
        "v_lshlrev_b32 v12, 4, v1\n\t"
        "v_add_u32 v4, 148160, v12\n\t"
        "v_mul_u32_u24 v5, 196, v1\n\t"
        "v_add_u32 v5, 148160, v5\n\t"
        "v_mov_b32 v8, 0xffff\n\t"
        "v_mov_b32 v9, v8\n\t"
        "v_mov_b64 v[10:11], v[8:9]\n\t"
        "s_cmp_eq_u32 s70, 0\n\t"
        "s_cbranch_scc0 .L7_P4\n\t"
        "v_add_u32 v15, 10240, v15\n\t"
        "v_add_u32 v97, 1280, v15\n\t"
        "v_add_u32 v98, 2560, v15\n\t"
        "v_add_u32 v99, 3840, v15\n\t"
        "v_add_u32 v100, 5120, v15\n\t"
        "v_add_u32 v101, 6400, v15\n\t"
        "v_add_u32 v102, 7680, v15\n\t"
        "v_add_u32 v103, 8960, v15\n\t"
        "s_lshl_b32 s48, s71, 14\n\t"
        "s_add_u32 s48, s48, 1056768\n\t"
        "s_add_u32 s49, s48, 4096\n"
        ".L7_w44:\n\t"
        "ds_read_b32 v0, v2 offset:112\n\t"
        "s_waitcnt lgkmcnt(0)\n\t"
        "v_cmp_gt_u16 vcc, 1, v0\n\t"
        "s_cbranch_vccz .L7_d45\n\t"
        "s_sleep 0\n\t"
        "s_sub_i32 s83, s83, 1\n\t"
        "s_cmp_gt_i32 s83, 0\n\t"
        "s_cbranch_scc1 .L7_w44\n"
        ".L7_d45:\n\t"
        "ds_read2_b32 v[16:17], v15 offset0:0 offset1:8\n\t"
        "ds_read2_b32 v[18:19], v15 offset0:16 offset1:24\n\t"
        "ds_read2_b32 v[20:21], v97 offset0:0 offset1:8\n\t"
        "ds_read2_b32 v[22:23], v97 offset0:16 offset1:24\n\t"
        "ds_read2_b32 v[24:25], v98 offset0:0 offset1:8\n\t"
        "ds_read2_b32 v[26:27], v98 offset0:16 offset1:24\n\t"
        "ds_read2_b32 v[28:29], v99 offset0:0 offset1:8\n\t"
        "ds_read2_b32 v[30:31], v99 offset0:16 offset1:24\n\t"
        "ds_read2_b32 v[32:33], v100 offset0:0 offset1:8\n\t"
        "ds_read2_b32 v[34:35], v100 offset0:16 offset1:24\n\t"
        "ds_read2_b32 v[36:37], v101 offset0:0 offset1:8\n\t"
        "ds_read2_b32 v[38:39], v101 offset0:16 offset1:24\n\t"
        "s_waitcnt lgkmcnt(10)\n\t"
        "buffer_store_dwordx4 v[16:19], v12, s[20:23], s48 offen offset:0 sc1\n\t"
        "ds_read2_b32 v[40:41], v102 offset0:0 offset1:8\n\t"
        "ds_read2_b32 v[42:43], v102 offset0:16 offset1:24\n\t"
        "s_waitcnt lgkmcnt(10)\n\t"
        "buffer_store_dwordx4 v[20:23], v12, s[20:23], s48 offen offset:1024 sc1\n\t"
        "ds_read2_b32 v[44:45], v103 offset0:0 offset1:8\n\t"
        "ds_read2_b32 v[46:47], v103 offset0:16 offset1:24\n\t"
        "s_mov_b64 exec, 1\n\t"
        "ds_add_u32 v2, v3 offset:72\n\t"
        "s_mov_b64 exec, -1\n\t"
        "s_waitcnt lgkmcnt(11)\n\t"
        "buffer_store_dwordx4 v[24:27], v12, s[20:23], s48 offen offset:2048 sc1\n\t"
        "s_waitcnt lgkmcnt(9)\n\t"
        "buffer_store_dwordx4 v[28:31], v12, s[20:23], s48 offen offset:3072 sc1\n\t"
        "s_waitcnt lgkmcnt(7)\n\t"
        "buffer_store_dwordx4 v[32:35], v12, s[20:23], s49 offen offset:0 sc1\n\t"
        "s_waitcnt lgkmcnt(5)\n\t"
        "buffer_store_dwordx4 v[36:39], v12, s[20:23], s49 offen offset:1024 sc1\n\t"
        "s_waitcnt lgkmcnt(3)\n\t"
        "buffer_store_dwordx4 v[40:43], v12, s[20:23], s49 offen offset:2048 sc1\n\t"
        "s_waitcnt lgkmcnt(1)\n\t"
        "buffer_store_dwordx4 v[44:47], v12, s[20:23], s49 offen offset:3072 sc1\n\t"
        "s_mov_b64 exec, 1\n\t"
        "ds_add_u32 v2, v3 offset:44\n\t"
        "s_mov_b64 exec, -1\n"
        ".L7_P4:\n\t"
        "s_setprio 0\n\t"
        "s_waitcnt lgkmcnt(0)\n\t"
        "ds_write_b128 v4, v[8:11] offset:0\n\t"
        "ds_write_b128 v4, v[8:11] offset:1024\n\t"
        "ds_write_b128 v4, v[8:11] offset:2048\n\t"
        "ds_write_b128 v4, v[8:11] offset:3072\n\t"
        "ds_write_b128 v4, v[8:11] offset:4096\n\t"
        "ds_write_b128 v4, v[8:11] offset:5120\n\t"
        "ds_write_b128 v4, v[8:11] offset:6144\n\t"
        "ds_write_b128 v4, v[8:11] offset:7168\n\t"
        "ds_write_b128 v4, v[8:11] offset:8192\n\t"
        "ds_write_b128 v4, v[8:11] offset:9216\n\t"
        "ds_write_b128 v4, v[8:11] offset:10240\n\t"
        "ds_write_b128 v4, v[8:11] offset:11264\n\t"
        "s_waitcnt lgkmcnt(6)\n\t"
        "ds_write_b128 v4, v[8:11] offset:12288\n\t"
        "ds_write_b128 v4, v[8:11] offset:13312\n\t"
        "ds_write_b128 v4, v[8:11] offset:14336\n\t"
        "ds_write_b128 v4, v[8:11] offset:15360\n\t"
        "ds_write_b32 v5, v2 offset:192\n\t"
        "s_mov_b64 exec, 0xffff\n\t"
        "ds_write_b32 v5, v2 offset:12736\n\t"
        "s_mov_b64 exec, 1\n\t"
        "ds_add_u32 v2, v3 offset:8\n\t"
        "s_mov_b64 exec, -1\n\t"
        "s_branch .L7_Q\n"
        ".L7_V2:\n\t"
        "v_and_b32 v5, 7, v1\n\t"
        "v_mul_u32_u24 v5, 0x420, v5\n\t"
        "v_and_b32 v6, 8, v1\n\t"
        "v_lshl_add_u32 v5, v6, 6, v5\n\t"
        "v_and_b32 v6, 48, v1\n\t"
        "v_add_u32 v5, v5, v6\n\t"
        "v_add_u32 v5, 68864, v5\n\t"
        "s_lshr_b32 s78, s81, 1\n\t"
        "s_lshl3_add_u32 s78, s78, s80\n\t"
        "s_and_b32 s79, s81, 1\n\t"
        "s_lshl1_add_u32 s78, s78, s79\n\t"
        "s_lshl_b32 s82, s78, 10\n\t"
        "s_add_u32 s82, s82, 131072\n"
        ".L7_w40:\n\t"
        "ds_read_b32 v0, v2 offset:104\n\t"
        "ds_read_b32 v6, v2 offset:76\n\t"
        "s_waitcnt lgkmcnt(1)\n\t"
        "v_cmp_gt_u16 vcc, 1, v0\n\t"
        "s_cbranch_vccz .L7_d41\n\t"
        "s_waitcnt lgkmcnt(0)\n\t"
        "s_sleep 1\n\t"
        "s_sub_i32 s83, s83, 1\n\t"
        "s_cmp_gt_i32 s83, 0\n\t"
        "s_cbranch_scc1 .L7_w40\n"
        ".L7_d41:\n\t"
        "s_waitcnt lgkmcnt(0)\n\t"
        "v_readfirstlane_b32 s68, v6\n\t"
        "v_mov_b32 v7, 2\n\t"
        "s_and_b32 s73, s68, 1\n\t"
        "s_cselect_b32 s73, 1, 0x10000\n\t"
        "v_mov_b32 v16, s73\n\t"
        "v_and_b32 v17, 7, v1\n\t"
        "v_add_u32 v17, 1, v17\n\t"
        "v_lshrrev_b32 v17, v17, s68\n\t"
        "s_mov_b32 s49, 0x10001\n\t"
        "v_and_b32 v17, s49, v17\n\t"
        "s_mov_b32 s48, 0x3db8aa3b\n\t"
        "s_waitcnt vmcnt(0)\n\t"
        "ds_read_b128 v[32:35], v5 offset:0\n\t"
        "ds_read_b128 v[36:39], v5 offset:8448\n\t"
        "ds_read_b128 v[40:43], v5 offset:16896\n\t"
        "ds_read_b128 v[44:47], v5 offset:64\n\t"
        "ds_read_b128 v[48:51], v5 offset:8512\n\t"
        "ds_read_b128 v[52:55], v5 offset:16960\n\t"
        "ds_read_b128 v[56:59], v5 offset:128\n\t"
        "ds_read_b128 v[60:63], v5 offset:8576\n\t"
        "ds_read_b128 v[64:67], v5 offset:17024\n\t"
        "ds_read_b128 v[68:71], v5 offset:192\n\t"
        "ds_read_b128 v[72:75], v5 offset:8640\n\t"
        "ds_read_b128 v[76:79], v5 offset:17088\n\t"
        "s_waitcnt lgkmcnt(9)\n\t"
        "v_mfma_f32_16x16x32_bf16 v[20:23], v[36:39], v[32:35], 0\n\t"
        "v_mfma_f32_16x16x32_bf16 v[24:27], v[40:43], v[32:35], 0\n\t"
        "ds_read_b128 v[80:83], v5 offset:256\n\t"
        "ds_read_b128 v[84:87], v5 offset:8704\n\t"
        "ds_read_b128 v[88:91], v5 offset:17152\n\t"
        "s_waitcnt lgkmcnt(9)\n\t"
        "v_mfma_f32_16x16x32_bf16 v[20:23], v[48:51], v[44:47], v[20:23]\n\t"
        "v_mfma_f32_16x16x32_bf16 v[24:27], v[52:55], v[44:47], v[24:27]\n\t"
        "ds_read_b128 v[92:95], v5 offset:320\n\t"
        "ds_read_b128 v[96:99], v5 offset:8768\n\t"
        "ds_read_b128 v[100:103], v5 offset:17216\n\t"
        "s_waitcnt lgkmcnt(9)\n\t"
        "v_mfma_f32_16x16x32_bf16 v[20:23], v[60:63], v[56:59], v[20:23]\n\t"
        "v_mfma_f32_16x16x32_bf16 v[24:27], v[64:67], v[56:59], v[24:27]\n\t"
        "ds_read_b128 v[104:107], v5 offset:384\n\t"
        "ds_read_b128 v[108:111], v5 offset:8832\n\t"
        "ds_read_b128 v[112:115], v5 offset:17280\n\t"
        "s_waitcnt lgkmcnt(9)\n\t"
        "v_mfma_f32_16x16x32_bf16 v[20:23], v[72:75], v[68:71], v[20:23]\n\t"
        "v_mfma_f32_16x16x32_bf16 v[24:27], v[76:79], v[68:71], v[24:27]\n\t"
        "ds_read_b128 v[116:119], v5 offset:448\n\t"
        "ds_read_b128 v[120:123], v5 offset:8896\n\t"
        "ds_read_b128 v[124:127], v5 offset:17344\n\t"
        "s_waitcnt lgkmcnt(9)\n\t"
        "v_mfma_f32_16x16x32_bf16 v[20:23], v[84:87], v[80:83], v[20:23]\n\t"
        "v_mfma_f32_16x16x32_bf16 v[24:27], v[88:91], v[80:83], v[24:27]\n\t"
        "s_mov_b64 exec, 1\n\t"
        "ds_add_u32 v2, v7 offset:72\n\t"
        "s_mov_b64 exec, -1\n\t"
        "s_waitcnt lgkmcnt(7)\n\t"
        "v_mfma_f32_16x16x32_bf16 v[20:23], v[96:99], v[92:95], v[20:23]\n\t"
        "v_mfma_f32_16x16x32_bf16 v[24:27], v[100:103], v[92:95], v[24:27]\n\t"
        "s_waitcnt lgkmcnt(4)\n\t"
        "v_mfma_f32_16x16x32_bf16 v[20:23], v[108:111], v[104:107], v[20:23]\n\t"
        "v_mfma_f32_16x16x32_bf16 v[24:27], v[112:115], v[104:107], v[24:27]\n\t"
        "s_waitcnt lgkmcnt(1)\n\t"
        "v_mfma_f32_16x16x32_bf16 v[20:23], v[120:123], v[116:119], v[20:23]\n\t"
        "v_mfma_f32_16x16x32_bf16 v[24:27], v[124:127], v[116:119], v[24:27]\n\t"
        "s_nop 6\n\t"
        "v_mul_f32 v20, s48, v20\n\t"
        "v_mul_f32 v21, s48, v21\n\t"
        "v_mul_f32 v22, s48, v22\n\t"
        "v_mul_f32 v23, s48, v23\n\t"
        "v_mul_f32 v24, s48, v24\n\t"
        "v_mul_f32 v25, s48, v25\n\t"
        "v_mul_f32 v26, s48, v26\n\t"
        "v_mul_f32 v27, s48, v27\n\t"
        "s_mov_b32 s48, 0x07060302\n\t"
        "v_add_u32 v20, 0x10000, v20\n\t"
        "v_add_u32 v21, 0x10000, v21\n\t"
        "v_add_u32 v22, 0x10000, v22\n\t"
        "v_add_u32 v23, 0x10000, v23\n\t"
        "v_perm_b32 v20, v21, v20, s48\n\t"
        "v_perm_b32 v21, v23, v22, s48\n\t"
        "v_cvt_pk_bf16_f32 v22, v24, v25\n\t"
        "v_cvt_pk_bf16_f32 v23, v26, v27\n\t"
        "v_bfi_b32 v20, s49, v16, v20\n\t"
        "v_bfi_b32 v21, s49, v17, v21\n\t"
        "buffer_store_dwordx4 v[20:23], v12, s[20:23], s82 offen sc1\n\t"
        "s_mov_b64 exec, 1\n\t"
        "ds_add_u32 v2, v7 offset:44\n\t"
        "s_mov_b64 exec, -1\n\t"
        "s_branch .L7_Q\n"
        ".L7_T2:\n\t"
        "s_and_b32 s82, s82, -1024\n\t"
        "v_mov_b32 v9, 4\n"
        ".L7_g42:\n\t"
        "s_memrealtime s[78:79]\n\t"
        "s_waitcnt lgkmcnt(0)\n\t"
        "s_sub_u32 s78, s78, s74\n\t"
        "s_cmp_ge_i32 s78, 150\n\t"
        "s_cbranch_scc1 .L7_h43\n\t"
        "s_sleep 1\n\t"
        "s_branch .L7_g42\n"
        ".L7_h43:\n"
        ".L7_b1:\n\t"
        "s_buffer_load_dword s40, s[20:23], s82 offset:0 glc\n\t"
        "s_buffer_load_dword s41, s[20:23], s82 offset:128 glc\n\t"
        "s_buffer_load_dword s42, s[20:23], s82 offset:256 glc\n\t"
        "s_buffer_load_dword s43, s[20:23], s82 offset:384 glc\n\t"
        "s_buffer_load_dword s44, s[20:23], s82 offset:512 glc\n\t"
        "s_buffer_load_dword s45, s[20:23], s82 offset:640 glc\n\t"
        "s_buffer_load_dword s46, s[20:23], s82 offset:768 glc\n\t"
        "s_buffer_load_dword s47, s[20:23], s82 offset:896 glc\n\t"
        "s_waitcnt lgkmcnt(0)\n\t"
        "s_mov_b32 s80, 0\n\t"
        "s_mov_b32 s81, 0\n\t"
        "s_sub_u32 s40, s40, s74\n\t"
        "s_max_i32 s80, s80, s40\n\t"
        "s_min_i32 s81, s81, s40\n\t"
        "s_sub_u32 s41, s41, s74\n\t"
        "s_max_i32 s80, s80, s41\n\t"
        "s_min_i32 s81, s81, s41\n\t"
        "s_sub_u32 s42, s42, s74\n\t"
        "s_max_i32 s80, s80, s42\n\t"
        "s_min_i32 s81, s81, s42\n\t"
        "s_sub_u32 s43, s43, s74\n\t"
        "s_max_i32 s80, s80, s43\n\t"
        "s_min_i32 s81, s81, s43\n\t"
        "s_sub_u32 s44, s44, s74\n\t"
        "s_max_i32 s80, s80, s44\n\t"
        "s_min_i32 s81, s81, s44\n\t"
        "s_sub_u32 s45, s45, s74\n\t"
        "s_max_i32 s80, s80, s45\n\t"
        "s_min_i32 s81, s81, s45\n\t"
        "s_sub_u32 s46, s46, s74\n\t"
        "s_max_i32 s80, s80, s46\n\t"
        "s_min_i32 s81, s81, s46\n\t"
        "s_sub_u32 s47, s47, s74\n\t"
        "s_max_i32 s80, s80, s47\n\t"
        "s_min_i32 s81, s81, s47\n\t"
        "s_sub_u32 s81, s80, s81\n\t"
        "s_cmp_lt_u32 s81, 0x3e8\n\t"
        "s_cbranch_scc1 .L7_b2\n\t"
        "s_mov_b64 exec, 1\n\t"
        "ds_add_u32 v2, v9 offset:84\n\t"
        "s_mov_b64 exec, -1\n\t"
        "s_mov_b64 exec, 1\n\t"
        "ds_add_u32 v2, v9 offset:108\n\t"
        "s_mov_b64 exec, -1\n\t"
        "v_mov_b32 v9, 0\n\t"
        "s_mov_b32 s80, 0\n\t"
        "s_sleep 4\n\t"
        "s_memrealtime s[78:79]\n\t"
        "s_waitcnt lgkmcnt(0)\n\t"
        "s_sub_u32 s78, s78, s74\n\t"
        "s_cmp_lt_u32 s78, 0x4b0\n\t"
        "s_cbranch_scc1 .L7_b1\n"
        ".L7_b2:\n\t"
        "s_add_u32 s80, s80, 1\n\t"
        "v_mov_b32 v7, s80\n\t"
        "s_mov_b64 exec, 1\n\t"
        "ds_add_u32 v2, v7 offset:92\n\t"
        "s_mov_b64 exec, -1\n\t"
        "s_branch .L7_Q0\n"
        ".L7_R2:\n\t"
        "s_sub_u32 s79, s64, s72\n\t"
        "s_min_i32 s79, s79, s65\n\t"
        "s_max_i32 s79, s79, 0\n\t"
        "v_mov_b32 v32, v1\n\t"
        "v_add_u32 v33, 64, v1\n\t"
        "v_cmp_gt_u32 s[32:33], s79, v32\n\t"
        "v_cmp_gt_u32 s[34:35], s79, v33\n\t"
        "v_cmp_gt_u32 s[36:37], 40, v1\n\t"
        "s_sub_u32 s78, 0xff, s65\n\t"
        "v_add_u32 v34, s78, v32\n\t"
        "v_add_u32 v6, 0xffffffb0, v34\n\t"
        "v_cmp_gt_u32 vcc, 0xff, v34\n\t"
        "s_nop 1\n\t"
        "v_cndmask_b32 v34, v6, v34, vcc\n\t"
        "v_add_u32 v35, s78, v33\n\t"
        "v_add_u32 v6, 0xffffffb0, v35\n\t"
        "v_cmp_gt_u32 vcc, 0xff, v35\n\t"
        "s_nop 1\n\t"
        "v_cndmask_b32 v35, v6, v35, vcc\n\t"
        "v_mov_b32 v12, 0x8000\n\t"
        "v_cndmask_b32 v44, v12, 0, s[32:33]\n\t"
        "v_cndmask_b32 v45, v12, 0, s[34:35]\n\t"
        "v_cndmask_b32 v46, 63, 31, s[36:37]\n\t"
        "v_mov_b32 v47, 63\n\t"
        "s_waitcnt vmcnt(0)\n\t"
        "v_lshlrev_b32 v16, 16, v16\n\t"
        "v_lshlrev_b32 v17, 16, v17\n\t"
        "v_mul_f32 v36, v16, v16\n\t"
        "v_mul_f32 v37, v17, v17\n\t"
        "v_rcp_f32 v36, v36\n\t"
        "v_rcp_f32 v37, v37\n\t"
        "v_or3_b32 v16, v16, v34, v44\n\t"
        "v_or3_b32 v17, v17, v35, v45\n\t"
        "v_add_f32 v36, 0xc0600000, v36\n\t"
        "v_add_f32 v37, 0xc0600000, v37\n\t"
        "v_cvt_u32_f32 v36, v36\n\t"
        "v_cvt_u32_f32 v37, v37\n\t"
        "v_min_u32 v36, 31, v36\n\t"
        "v_min_u32 v37, 31, v37\n\t"
        "v_cndmask_b32 v36, 0, v36, s[32:33]\n\t"
        "v_cndmask_b32 v37, 0, v37, s[34:35]\n\t"
        "v_sub_u32 v36, v46, v36\n\t"
        "v_sub_u32 v37, v47, v37\n\t"
        "v_lshlrev_b32 v36, 2, v36\n\t"
        "v_lshlrev_b32 v37, 2, v37\n\t"
        "ds_add_rtn_u32 v40, v36, v3 offset:256\n\t"
        "s_mov_b64 exec, 0xffff\n\t"
        "ds_add_rtn_u32 v41, v37, v3 offset:256\n\t"
        "s_mov_b64 exec, -1\n\t"
        "v_lshlrev_b32 v9, 2, v1\n\t"
        "ds_read_b32 v10, v9 offset:256\n\t"
        "s_cmp_eq_u32 s70, 0\n\t"
        "s_cselect_b32 s78, 56, 0\n\t"
        "s_cselect_b32 s79, 0, 56\n\t"
        "v_mov_b32 v49, -40\n\t"
        "v_cndmask_b32 v48, v49, 0, s[36:37]\n\t"
        "s_mov_b32 s40, 0x23108954\n\t"
        "s_mov_b32 s41, 0x67\n\t"
        "s_mov_b32 s42, 0x1986732\n\t"
        "s_mov_b32 s43, 0x45\n\t"
        "v_mov_b32 v13, s78\n\t"
        "v_mov_b32 v14, s79\n\t"
        "v_cndmask_b32 v13, v14, v13, s[36:37]\n\t"
        "s_waitcnt lgkmcnt(0)\n\t"
        "v_mov_b32 v11, v10\n\t"
        "s_nop 1\n\t"
        "v_add_u32_dpp v11, v11, v11 row_shr:1 row_mask:0xf bank_mask:0xf bound_ctrl:1\n\t"
        "s_nop 1\n\t"
        "v_add_u32_dpp v11, v11, v11 row_shr:2 row_mask:0xf bank_mask:0xf bound_ctrl:1\n\t"
        "s_nop 1\n\t"
        "v_add_u32_dpp v11, v11, v11 row_shr:4 row_mask:0xf bank_mask:0xf bound_ctrl:1\n\t"
        "s_nop 1\n\t"
        "v_add_u32_dpp v11, v11, v11 row_shr:8 row_mask:0xf bank_mask:0xf bound_ctrl:1\n\t"
        "s_nop 1\n\t"
        "v_add_u32_dpp v11, v11, v11 row_bcast:15 row_mask:0xa bank_mask:0xf\n\t"
        "s_nop 1\n\t"
        "v_add_u32_dpp v11, v11, v11 row_bcast:31 row_mask:0xc bank_mask:0xf\n\t"
        "v_sub_u32 v11, v11, v10\n\t"
        "ds_bpermute_b32 v36, v36, v11\n\t"
        "ds_bpermute_b32 v37, v37, v11\n\t"
        "v_lshlrev_b32 v9, 1, v1\n\t"
        "s_waitcnt lgkmcnt(0)\n\t"
        "v_add3_u32 v36, v36, v40, v48\n\t"
        "v_and_b32 v6, -4, v36\n\t"
        "v_lshrrev_b64 v[10:11], v6, s[40:41]\n\t"
        "v_lshrrev_b64 v[50:51], v6, s[42:43]\n\t"
        "v_cmp_eq_u32 vcc, 0, v13\n\t"
        "s_nop 1\n\t"
        "v_cndmask_b32 v10, v10, v50, vcc\n\t"
        "v_and_b32 v10, 15, v10\n\t"
        "v_and_b32 v6, 3, v36\n\t"
        "v_lshl_add_u32 v6, v10, 2, v6\n\t"
        "v_add_u32 v36, v6, v13\n\t"
        "v_and_b32 v6, 15, v36\n\t"
        "v_lshlrev_b32 v6, 4, v6\n\t"
        "v_lshl_add_u32 v6, v36, 8, v6\n\t"
        "v_and_b32 v10, 8, v13\n\t"
        "v_lshlrev_b32 v10, 8, v10\n\t"
        "v_sub_u32 v6, v6, v10\n\t"
        "v_lshlrev_b32 v36, 2, v36\n\t"
        "ds_write_b32 v36, v16 offset:512\n\t"
        "ds_write_b16 v9, v6 offset:1792\n\t"
        "v_add3_u32 v37, v37, v41, v49\n\t"
        "v_and_b32 v6, -4, v37\n\t"
        "v_lshrrev_b64 v[10:11], v6, s[40:41]\n\t"
        "v_lshrrev_b64 v[50:51], v6, s[42:43]\n\t"
        "v_cmp_eq_u32 vcc, 0, v14\n\t"
        "s_nop 1\n\t"
        "v_cndmask_b32 v10, v10, v50, vcc\n\t"
        "v_and_b32 v10, 15, v10\n\t"
        "v_and_b32 v6, 3, v37\n\t"
        "v_lshl_add_u32 v6, v10, 2, v6\n\t"
        "v_add_u32 v37, v6, v14\n\t"
        "v_and_b32 v6, 15, v37\n\t"
        "v_lshlrev_b32 v6, 4, v6\n\t"
        "v_lshl_add_u32 v6, v37, 8, v6\n\t"
        "v_and_b32 v10, 8, v14\n\t"
        "v_lshlrev_b32 v10, 8, v10\n\t"
        "v_sub_u32 v6, v6, v10\n\t"
        "v_lshlrev_b32 v37, 2, v37\n\t"
        "s_mov_b64 exec, 0xffff\n\t"
        "ds_write_b32 v37, v17 offset:512\n\t"
        "ds_write_b16 v9, v6 offset:1920\n\t"
        "s_mov_b64 exec, 1\n\t"
        "ds_add_u32 v2, v3 offset:8\n\t"
        "s_mov_b64 exec, -1\n"
        ".L7_w46:\n\t"
        "ds_read_b64 v[4:5], v2 offset:72\n\t"
        "s_waitcnt lgkmcnt(0)\n\t"
        "v_cmp_gt_u16 vcc, 2, v4\n\t"
        "s_cbranch_vccz .L7_d47\n\t"
        "s_sleep 2\n\t"
        "s_sub_i32 s83, s83, 1\n\t"
        "s_cmp_gt_i32 s83, 0\n\t"
        "s_cbranch_scc1 .L7_w46\n"
        ".L7_d47:\n\t"
        "v_readfirstlane_b32 s68, v5\n\t"
        "v_lshlrev_b32 v22, 4, v1\n\t"
        "v_add_u32 v9, 142848, v22\n\t"
        "v_and_b32 v10, 60, v1\n\t"
        "ds_read_b32 v24, v10 offset:704\n\t"
        "ds_read_b32 v25, v10 offset:768\n\t"
        "ds_read_b32 v26, v10 offset:832\n\t"
        "v_and_b32 v11, 15, v1\n\t"
        "v_lshrrev_b32 v12, 4, v1\n\t"
        "v_xor_b32 v16, v11, v12\n\t"
        "v_lshlrev_b32 v16, 4, v16\n\t"
        "v_lshl_add_u32 v16, v11, 8, v16\n\t"
        "v_xor_b32 v17, 64, v16\n\t"
        "v_xor_b32 v18, 128, v16\n\t"
        "v_xor_b32 v19, 192, v16\n\t"
        "ds_write_b128 v9, v[16:19]\n\t"
        "s_and_b32 s73, s68, 1\n\t"
        "s_cselect_b32 s73, 1, 0x10000\n\t"
        "v_mov_b32 v20, s73\n\t"
        "v_and_b32 v21, 7, v1\n\t"
        "v_add_u32 v21, 1, v21\n\t"
        "v_lshrrev_b32 v21, v21, s68\n\t"
        "s_mov_b32 s49, 0x10001\n\t"
        "v_and_b32 v21, s49, v21\n\t"
        "v_and_b32 v23, 0x37f, v22\n\t"
        "ds_write_b128 v9, v[20:23] offset:1024\n\t"
        "s_add_u32 s78, s72, s65\n\t"
        "s_sub_u32 s78, s78, 0xff\n\t"
        "s_lshl_b32 s78, s78, 9\n\t"
        "v_and_b32 v13, 48, v22\n\t"
        "v_add_u32 v14, s78, v13\n\t"
        "s_movk_i32 s78, 0x200\n\t"
        "v_bfe_u32 v27, v1, 1, 2\n\t"
        "v_xor_b32 v27, v27, v12\n\t"
        "v_lshlrev_b32 v27, 4, v27\n\t"
        "v_lshl_add_u32 v27, v11, 6, v27\n\t"
        "v_lshlrev_b32 v28, 1, v1\n\t"
        "v_and_b32 v28, 48, v28\n\t"
        "v_xor_b32 v28, v28, v22\n\t"
        "s_waitcnt lgkmcnt(2)\n\t"
        "v_mad_u32_u16 v24, v24, s78, v14\n\t"
        "v_mad_u32_u16 v25, v25, s78, v14\n\t"
        "v_mad_u32_u16 v26, v26, s78, v14\n\t"
        "s_mov_b32 exec_hi, 0\n\t"
        "v_mov_b32 v24, -2.0\n\t"
        "s_mov_b64 exec, -1\n\t"
        "ds_write_b128 v9, v[24:27] offset:2048\n\t"
        "s_mov_b32 s78, 0x3f80\n\t"
        "s_mov_b32 s79, 0\n\t"
        "v_lshlrev_b64 v[30:31], v13, s[78:79]\n\t"
        "s_mov_b32 s78, 0x00f0000f\n\t"
        "s_mov_b32 s79, 0xf0000f00\n\t"
        "v_cndmask_b32 v30, 0, v30, s[78:79]\n\t"
        "v_cndmask_b32 v31, 0, v31, s[78:79]\n\t"
        "ds_write_b128 v9, v[28:31] offset:3072\n\t"
        "s_branch .L7_Q\n"
        ".L7_Q0:\n"
        ".L7_w48:\n\t"
        "ds_read_b64 v[4:5], v2 offset:72\n\t"
        "s_waitcnt lgkmcnt(0)\n\t"
        "v_cmp_gt_u16 vcc, 2, v4\n\t"
        "s_cbranch_vccz .L7_d49\n\t"
        "s_sleep 2\n\t"
        "s_sub_i32 s83, s83, 1\n\t"
        "s_cmp_gt_i32 s83, 0\n\t"
        "s_cbranch_scc1 .L7_w48\n"
        ".L7_d49:\n\t"
        "v_readfirstlane_b32 s68, v5\n"
        ".L7_Q:\n\t"
        "s_setprio 3\n\t"
        "v_and_b32 v5, 7, v1\n\t"
        "v_mul_u32_u24 v5, 0x420, v5\n\t"
        "v_and_b32 v6, 8, v1\n\t"
        "v_lshl_add_u32 v5, v6, 6, v5\n\t"
        "v_and_b32 v6, 48, v1\n\t"
        "v_add_u32 v5, v5, v6\n\t"
        "s_and_b32 s73, s68, 1\n\t"
        "s_cselect_b32 s73, 1, 0x10000\n\t"
        "v_and_b32 v6, 7, v1\n\t"
        "v_add_u32 v6, 1, v6\n\t"
        "v_lshrrev_b32 v6, v6, s68\n\t"
        "s_mov_b32 s49, 0x10001\n\t"
        "v_and_b32 v6, s49, v6\n\t"
        "s_add_u32 s80, s69, s76\n\t"
        "s_and_b32 s80, s80, 3\n\t"
        "s_lshl2_add_u32 s78, s70, s80\n\t"
        "s_lshl_b32 s78, s78, 14\n\t"
        "s_add_u32 s78, s78, 131072\n\t"
        "v_lshl_add_u32 v4, v1, 4, s78\n\t"
        "s_mov_b32 s24, 4096\n\t"
        "s_mov_b32 s25, 8192\n\t"
        "s_mov_b32 s26, 12288\n"
        ".L7_w50:\n\t"
        "ds_read_b32 v0, v2 offset:92\n\t"
        "s_waitcnt lgkmcnt(0)\n\t"
        "v_cmp_gt_u16 vcc, 1, v0\n\t"
        "s_cbranch_vccz .L7_d51\n\t"
        "s_sleep 1\n\t"
        "s_sub_i32 s83, s83, 1\n\t"
        "s_cmp_gt_i32 s83, 0\n\t"
        "s_cbranch_scc1 .L7_w50\n"
        ".L7_d51:\n\t"
        "v_readfirstlane_b32 s78, v0\n\t"
        "s_sub_u32 s78, s78, 1\n\t"
        "s_add_u32 s77, s74, s78\n"
        ".L7_g52:\n\t"
        "s_memrealtime s[78:79]\n\t"
        "s_waitcnt lgkmcnt(0)\n\t"
        "s_sub_u32 s78, s78, s77\n\t"
        "s_cmp_ge_i32 s78, 280\n\t"
        "s_cbranch_scc1 .L7_h53\n\t"
        "s_sleep 1\n\t"
        "s_branch .L7_g52\n"
        ".L7_h53:\n"
        ".L7_w54:\n\t"
        "ds_read_b32 v0, v2 offset:88\n\t"
        "s_waitcnt lgkmcnt(0)\n\t"
        "v_cmp_gt_u16 vcc, 8, v0\n\t"
        "s_cbranch_vccz .L7_d55\n\t"
        "s_sleep 1\n\t"
        "s_sub_i32 s83, s83, 1\n\t"
        "s_cmp_gt_i32 s83, 0\n\t"
        "s_cbranch_scc1 .L7_w54\n"
        ".L7_d55:\n\t"
        "buffer_load_dwordx4 v[64:67], v4, s[20:23], 0 offen offset:0 sc1\n\t"
        "buffer_load_dwordx4 v[68:71], v4, s[20:23], 0 offen offset:1024 sc1\n\t"
        "buffer_load_dwordx4 v[72:75], v4, s[20:23], 0 offen offset:2048 sc1\n\t"
        "buffer_load_dwordx4 v[76:79], v4, s[20:23], 0 offen offset:3072 sc1\n\t"
        "buffer_load_dwordx4 v[80:83], v4, s[20:23], s24 offen offset:0 sc1\n\t"
        "buffer_load_dwordx4 v[84:87], v4, s[20:23], s24 offen offset:1024 sc1\n\t"
        "buffer_load_dwordx4 v[88:91], v4, s[20:23], s24 offen offset:2048 sc1\n\t"
        "buffer_load_dwordx4 v[92:95], v4, s[20:23], s24 offen offset:3072 sc1\n\t"
        "buffer_load_dwordx4 v[96:99], v4, s[20:23], s25 offen offset:0 sc1\n\t"
        "buffer_load_dwordx4 v[100:103], v4, s[20:23], s25 offen offset:1024 sc1\n\t"
        "buffer_load_dwordx4 v[104:107], v4, s[20:23], s25 offen offset:2048 sc1\n\t"
        "buffer_load_dwordx4 v[108:111], v4, s[20:23], s25 offen offset:3072 sc1\n\t"
        "buffer_load_dwordx4 v[112:115], v4, s[20:23], s26 offen offset:0 sc1\n\t"
        "buffer_load_dwordx4 v[116:119], v4, s[20:23], s26 offen offset:1024 sc1\n\t"
        "buffer_load_dwordx4 v[120:123], v4, s[20:23], s26 offen offset:2048 sc1\n\t"
        "buffer_load_dwordx4 v[124:127], v4, s[20:23], s26 offen offset:3072 sc1\n\t"
        "s_mov_b64 exec, 1\n\t"
        "ds_add_u32 v2, v3 offset:84\n\t"
        "s_mov_b64 exec, -1\n"
        ".L7_w56:\n\t"
        "ds_read_b32 v0, v2 offset:0\n\t"
        "s_waitcnt lgkmcnt(0)\n\t"
        "v_cmp_gt_u16 vcc, 4, v0\n\t"
        "s_cbranch_vccz .L7_d57\n\t"
        "s_sleep 1\n\t"
        "s_sub_i32 s83, s83, 1\n\t"
        "s_cmp_gt_i32 s83, 0\n\t"
        "s_cbranch_scc1 .L7_w56\n"
        ".L7_d57:\n\t"
        "s_branch .L7_qg\n"
        ".L7_qr:\n\t"
        "s_waitcnt lgkmcnt(0)\n\t"
        "s_sleep 8\n\t"
        "s_sub_i32 s83, s83, 64\n\t"
        "s_cmp_gt_i32 s83, 0\n\t"
        "s_cbranch_scc0 .L7_qd\n\t"
        "buffer_load_dwordx4 v[64:67], v4, s[20:23], 0 offen offset:0 sc1\n\t"
        "buffer_load_dwordx4 v[68:71], v4, s[20:23], 0 offen offset:1024 sc1\n\t"
        "buffer_load_dwordx4 v[72:75], v4, s[20:23], 0 offen offset:2048 sc1\n\t"
        "buffer_load_dwordx4 v[76:79], v4, s[20:23], 0 offen offset:3072 sc1\n\t"
        "buffer_load_dwordx4 v[80:83], v4, s[20:23], s24 offen offset:0 sc1\n\t"
        "buffer_load_dwordx4 v[84:87], v4, s[20:23], s24 offen offset:1024 sc1\n\t"
        "buffer_load_dwordx4 v[88:91], v4, s[20:23], s24 offen offset:2048 sc1\n\t"
        "buffer_load_dwordx4 v[92:95], v4, s[20:23], s24 offen offset:3072 sc1\n\t"
        "buffer_load_dwordx4 v[96:99], v4, s[20:23], s25 offen offset:0 sc1\n\t"
        "buffer_load_dwordx4 v[100:103], v4, s[20:23], s25 offen offset:1024 sc1\n\t"
        "buffer_load_dwordx4 v[104:107], v4, s[20:23], s25 offen offset:2048 sc1\n\t"
        "buffer_load_dwordx4 v[108:111], v4, s[20:23], s25 offen offset:3072 sc1\n\t"
        "buffer_load_dwordx4 v[112:115], v4, s[20:23], s26 offen offset:0 sc1\n\t"
        "buffer_load_dwordx4 v[116:119], v4, s[20:23], s26 offen offset:1024 sc1\n\t"
        "buffer_load_dwordx4 v[120:123], v4, s[20:23], s26 offen offset:2048 sc1\n\t"
        "buffer_load_dwordx4 v[124:127], v4, s[20:23], s26 offen offset:3072 sc1\n"
        ".L7_qg:\n\t"
        "v_mov_b32 v7, 0\n\t"
        "ds_read_b128 v[8:11], v5 offset:2048\n\t"
        "ds_read_b128 v[12:15], v5 offset:10496\n\t"
        "ds_read_b128 v[16:19], v5 offset:18944\n\t"
        "ds_read_b128 v[20:23], v5 offset:27392\n\t"
        "s_waitcnt vmcnt(14) lgkmcnt(3)\n\t"
        "v_xor_b32 v64, s73, v64\n\t"
        "v_xor_b32 v65, v6, v65\n\t"
        "v_xor_b32 v68, s73, v68\n\t"
        "v_xor_b32 v69, v6, v69\n\t"
        "v_mfma_f32_16x16x32_bf16 v[24:27], v[64:67], v[8:11], 0\n\t"
        "s_nop 0\n\t"
        "v_mfma_f32_16x16x32_bf16 v[28:31], v[68:71], v[8:11], 0\n\t"
        "v_or3_b32 v7, v7, v64, v65\n\t"
        "ds_read_b128 v[8:11], v5 offset:35840\n\t"
        "s_waitcnt lgkmcnt(3)\n\t"
        "v_mfma_f32_16x16x32_bf16 v[32:35], v[64:67], v[12:15], 0\n\t"
        "v_mfma_f32_16x16x32_bf16 v[36:39], v[68:71], v[12:15], 0\n\t"
        "v_or3_b32 v7, v7, v68, v69\n\t"
        "ds_read_b128 v[12:15], v5 offset:2112\n\t"
        "s_waitcnt lgkmcnt(3)\n\t"
        "v_mfma_f32_16x16x32_bf16 v[40:43], v[64:67], v[16:19], 0\n\t"
        "v_mfma_f32_16x16x32_bf16 v[44:47], v[68:71], v[16:19], 0\n\t"
        "ds_read_b128 v[16:19], v5 offset:10560\n\t"
        "s_waitcnt lgkmcnt(3)\n\t"
        "v_mfma_f32_16x16x32_bf16 v[48:51], v[64:67], v[20:23], 0\n\t"
        "v_mfma_f32_16x16x32_bf16 v[52:55], v[68:71], v[20:23], 0\n\t"
        "ds_read_b128 v[20:23], v5 offset:19008\n\t"
        "s_waitcnt vmcnt(12) lgkmcnt(3)\n\t"
        "v_xor_b32 v72, s73, v72\n\t"
        "v_xor_b32 v73, v6, v73\n\t"
        "v_xor_b32 v76, s73, v76\n\t"
        "v_xor_b32 v77, v6, v77\n\t"
        "v_mfma_f32_16x16x32_bf16 v[56:59], v[64:67], v[8:11], 0\n\t"
        "v_mfma_f32_16x16x32_bf16 v[60:63], v[68:71], v[8:11], 0\n\t"
        "ds_read_b128 v[8:11], v5 offset:27456\n\t"
        "ds_read_b128 v[64:67], v5 offset:35904\n\t"
        "ds_read_b128 v[68:71], v5 offset:2176\n\t"
        "s_waitcnt lgkmcnt(4)\n\t"
        "v_mfma_f32_16x16x32_bf16 v[24:27], v[72:75], v[12:15], v[24:27]\n\t"
        "v_mfma_f32_16x16x32_bf16 v[28:31], v[76:79], v[12:15], v[28:31]\n\t"
        "v_or3_b32 v7, v7, v72, v73\n\t"
        "ds_read_b128 v[12:15], v5 offset:10624\n\t"
        "v_mfma_f32_16x16x32_bf16 v[32:35], v[72:75], v[16:19], v[32:35]\n\t"
        "v_mfma_f32_16x16x32_bf16 v[36:39], v[76:79], v[16:19], v[36:39]\n\t"
        "v_or3_b32 v7, v7, v76, v77\n\t"
        "ds_read_b128 v[16:19], v5 offset:19072\n\t"
        "s_waitcnt lgkmcnt(4)\n\t"
        "v_mfma_f32_16x16x32_bf16 v[40:43], v[72:75], v[20:23], v[40:43]\n\t"
        "v_mfma_f32_16x16x32_bf16 v[44:47], v[76:79], v[20:23], v[44:47]\n\t"
        "ds_read_b128 v[20:23], v5 offset:27520\n\t"
        "v_mfma_f32_16x16x32_bf16 v[48:51], v[72:75], v[8:11], v[48:51]\n\t"
        "v_mfma_f32_16x16x32_bf16 v[52:55], v[76:79], v[8:11], v[52:55]\n\t"
        "ds_read_b128 v[8:11], v5 offset:35968\n\t"
        "s_waitcnt vmcnt(10) lgkmcnt(4)\n\t"
        "v_xor_b32 v80, s73, v80\n\t"
        "v_xor_b32 v81, v6, v81\n\t"
        "v_xor_b32 v84, s73, v84\n\t"
        "v_xor_b32 v85, v6, v85\n\t"
        "v_mfma_f32_16x16x32_bf16 v[56:59], v[72:75], v[64:67], v[56:59]\n\t"
        "v_mfma_f32_16x16x32_bf16 v[60:63], v[76:79], v[64:67], v[60:63]\n\t"
        "ds_read_b128 v[64:67], v5 offset:2240\n\t"
        "ds_read_b128 v[72:75], v5 offset:10688\n\t"
        "ds_read_b128 v[76:79], v5 offset:19136\n\t"
        "v_mfma_f32_16x16x32_bf16 v[24:27], v[80:83], v[68:71], v[24:27]\n\t"
        "v_mfma_f32_16x16x32_bf16 v[28:31], v[84:87], v[68:71], v[28:31]\n\t"
        "v_or3_b32 v7, v7, v80, v81\n\t"
        "ds_read_b128 v[68:71], v5 offset:27584\n\t"
        "s_waitcnt lgkmcnt(6)\n\t"
        "v_mfma_f32_16x16x32_bf16 v[32:35], v[80:83], v[12:15], v[32:35]\n\t"
        "v_mfma_f32_16x16x32_bf16 v[36:39], v[84:87], v[12:15], v[36:39]\n\t"
        "v_or3_b32 v7, v7, v84, v85\n\t"
        "ds_read_b128 v[12:15], v5 offset:36032\n\t"
        "v_mfma_f32_16x16x32_bf16 v[40:43], v[80:83], v[16:19], v[40:43]\n\t"
        "v_mfma_f32_16x16x32_bf16 v[44:47], v[84:87], v[16:19], v[44:47]\n\t"
        "ds_read_b128 v[16:19], v5 offset:2304\n\t"
        "s_waitcnt lgkmcnt(6)\n\t"
        "v_mfma_f32_16x16x32_bf16 v[48:51], v[80:83], v[20:23], v[48:51]\n\t"
        "v_mfma_f32_16x16x32_bf16 v[52:55], v[84:87], v[20:23], v[52:55]\n\t"
        "ds_read_b128 v[20:23], v5 offset:10752\n\t"
        "s_waitcnt vmcnt(8) lgkmcnt(6)\n\t"
        "v_xor_b32 v88, s73, v88\n\t"
        "v_xor_b32 v89, v6, v89\n\t"
        "v_xor_b32 v92, s73, v92\n\t"
        "v_xor_b32 v93, v6, v93\n\t"
        "v_mfma_f32_16x16x32_bf16 v[56:59], v[80:83], v[8:11], v[56:59]\n\t"
        "v_mfma_f32_16x16x32_bf16 v[60:63], v[84:87], v[8:11], v[60:63]\n\t"
        "ds_read_b128 v[8:11], v5 offset:19200\n\t"
        "ds_read_b128 v[80:83], v5 offset:27648\n\t"
        "v_mfma_f32_16x16x32_bf16 v[24:27], v[88:91], v[64:67], v[24:27]\n\t"
        "v_mfma_f32_16x16x32_bf16 v[28:31], v[92:95], v[64:67], v[28:31]\n\t"
        "v_or3_b32 v7, v7, v88, v89\n\t"
        "ds_read_b128 v[84:87], v5 offset:36096\n\t"
        "s_waitcnt lgkmcnt(7)\n\t"
        "v_mfma_f32_16x16x32_bf16 v[32:35], v[88:91], v[72:75], v[32:35]\n\t"
        "v_mfma_f32_16x16x32_bf16 v[36:39], v[92:95], v[72:75], v[36:39]\n\t"
        "v_or3_b32 v7, v7, v92, v93\n\t"
        "ds_read_b128 v[64:67], v5 offset:2368\n\t"
        "v_mfma_f32_16x16x32_bf16 v[40:43], v[88:91], v[76:79], v[40:43]\n\t"
        "v_mfma_f32_16x16x32_bf16 v[44:47], v[92:95], v[76:79], v[44:47]\n\t"
        "ds_read_b128 v[72:75], v5 offset:10816\n\t"
        "s_waitcnt lgkmcnt(7)\n\t"
        "v_mfma_f32_16x16x32_bf16 v[48:51], v[88:91], v[68:71], v[48:51]\n\t"
        "v_mfma_f32_16x16x32_bf16 v[52:55], v[92:95], v[68:71], v[52:55]\n\t"
        "ds_read_b128 v[76:79], v5 offset:19264\n\t"
        "s_waitcnt vmcnt(6) lgkmcnt(7)\n\t"
        "v_xor_b32 v96, s73, v96\n\t"
        "v_xor_b32 v97, v6, v97\n\t"
        "v_xor_b32 v100, s73, v100\n\t"
        "v_xor_b32 v101, v6, v101\n\t"
        "v_mfma_f32_16x16x32_bf16 v[56:59], v[88:91], v[12:15], v[56:59]\n\t"
        "v_mfma_f32_16x16x32_bf16 v[60:63], v[92:95], v[12:15], v[60:63]\n\t"
        "ds_read_b128 v[68:71], v5 offset:27712\n\t"
        "v_mfma_f32_16x16x32_bf16 v[24:27], v[96:99], v[16:19], v[24:27]\n\t"
        "v_mfma_f32_16x16x32_bf16 v[28:31], v[100:103], v[16:19], v[28:31]\n\t"
        "v_or3_b32 v7, v7, v96, v97\n\t"
        "ds_read_b128 v[12:15], v5 offset:36160\n\t"
        "s_waitcnt lgkmcnt(7)\n\t"
        "v_mfma_f32_16x16x32_bf16 v[32:35], v[96:99], v[20:23], v[32:35]\n\t"
        "v_mfma_f32_16x16x32_bf16 v[36:39], v[100:103], v[20:23], v[36:39]\n\t"
        "v_or3_b32 v7, v7, v100, v101\n\t"
        "ds_read_b128 v[88:91], v5 offset:2432\n\t"
        "v_mfma_f32_16x16x32_bf16 v[40:43], v[96:99], v[8:11], v[40:43]\n\t"
        "v_mfma_f32_16x16x32_bf16 v[44:47], v[100:103], v[8:11], v[44:47]\n\t"
        "ds_read_b128 v[92:95], v5 offset:10880\n\t"
        "s_waitcnt lgkmcnt(7)\n\t"
        "v_mfma_f32_16x16x32_bf16 v[48:51], v[96:99], v[80:83], v[48:51]\n\t"
        "v_mfma_f32_16x16x32_bf16 v[52:55], v[100:103], v[80:83], v[52:55]\n\t"
        "ds_read_b128 v[16:19], v5 offset:19328\n\t"
        "s_waitcnt vmcnt(4) lgkmcnt(7)\n\t"
        "v_xor_b32 v104, s73, v104\n\t"
        "v_xor_b32 v105, v6, v105\n\t"
        "v_xor_b32 v108, s73, v108\n\t"
        "v_xor_b32 v109, v6, v109\n\t"
        "v_mfma_f32_16x16x32_bf16 v[56:59], v[96:99], v[84:87], v[56:59]\n\t"
        "v_mfma_f32_16x16x32_bf16 v[60:63], v[100:103], v[84:87], v[60:63]\n\t"
        "ds_read_b128 v[20:23], v5 offset:27776\n\t"
        "v_mfma_f32_16x16x32_bf16 v[24:27], v[104:107], v[64:67], v[24:27]\n\t"
        "v_mfma_f32_16x16x32_bf16 v[28:31], v[108:111], v[64:67], v[28:31]\n\t"
        "v_or3_b32 v7, v7, v104, v105\n\t"
        "ds_read_b128 v[8:11], v5 offset:36224\n\t"
        "s_waitcnt lgkmcnt(7)\n\t"
        "v_mfma_f32_16x16x32_bf16 v[32:35], v[104:107], v[72:75], v[32:35]\n\t"
        "v_mfma_f32_16x16x32_bf16 v[36:39], v[108:111], v[72:75], v[36:39]\n\t"
        "v_or3_b32 v7, v7, v108, v109\n\t"
        "ds_read_b128 v[80:83], v5 offset:2496\n\t"
        "v_mfma_f32_16x16x32_bf16 v[40:43], v[104:107], v[76:79], v[40:43]\n\t"
        "v_mfma_f32_16x16x32_bf16 v[44:47], v[108:111], v[76:79], v[44:47]\n\t"
        "ds_read_b128 v[84:87], v5 offset:10944\n\t"
        "s_waitcnt lgkmcnt(7)\n\t"
        "v_mfma_f32_16x16x32_bf16 v[48:51], v[104:107], v[68:71], v[48:51]\n\t"
        "v_mfma_f32_16x16x32_bf16 v[52:55], v[108:111], v[68:71], v[52:55]\n\t"
        "ds_read_b128 v[96:99], v5 offset:19392\n\t"
        "s_waitcnt vmcnt(2) lgkmcnt(7)\n\t"
        "v_xor_b32 v112, s73, v112\n\t"
        "v_xor_b32 v113, v6, v113\n\t"
        "v_xor_b32 v116, s73, v116\n\t"
        "v_xor_b32 v117, v6, v117\n\t"
        "v_mfma_f32_16x16x32_bf16 v[56:59], v[104:107], v[12:15], v[56:59]\n\t"
        "v_mfma_f32_16x16x32_bf16 v[60:63], v[108:111], v[12:15], v[60:63]\n\t"
        "ds_read_b128 v[100:103], v5 offset:27840\n\t"
        "v_mfma_f32_16x16x32_bf16 v[24:27], v[112:115], v[88:91], v[24:27]\n\t"
        "v_mfma_f32_16x16x32_bf16 v[28:31], v[116:119], v[88:91], v[28:31]\n\t"
        "v_or3_b32 v7, v7, v112, v113\n\t"
        "ds_read_b128 v[64:67], v5 offset:36288\n\t"
        "s_waitcnt lgkmcnt(7)\n\t"
        "v_mfma_f32_16x16x32_bf16 v[32:35], v[112:115], v[92:95], v[32:35]\n\t"
        "v_mfma_f32_16x16x32_bf16 v[36:39], v[116:119], v[92:95], v[36:39]\n\t"
        "v_or3_b32 v7, v7, v116, v117\n\t"
        "v_mfma_f32_16x16x32_bf16 v[40:43], v[112:115], v[16:19], v[40:43]\n\t"
        "v_mfma_f32_16x16x32_bf16 v[44:47], v[116:119], v[16:19], v[44:47]\n\t"
        "s_waitcnt lgkmcnt(5)\n\t"
        "v_mfma_f32_16x16x32_bf16 v[48:51], v[112:115], v[20:23], v[48:51]\n\t"
        "v_mfma_f32_16x16x32_bf16 v[52:55], v[116:119], v[20:23], v[52:55]\n\t"
        "s_waitcnt vmcnt(0) lgkmcnt(4)\n\t"
        "v_xor_b32 v120, s73, v120\n\t"
        "v_xor_b32 v121, v6, v121\n\t"
        "v_xor_b32 v124, s73, v124\n\t"
        "v_xor_b32 v125, v6, v125\n\t"
        "v_mfma_f32_16x16x32_bf16 v[56:59], v[112:115], v[8:11], v[56:59]\n\t"
        "v_mfma_f32_16x16x32_bf16 v[60:63], v[116:119], v[8:11], v[60:63]\n\t"
        "v_mfma_f32_16x16x32_bf16 v[24:27], v[120:123], v[80:83], v[24:27]\n\t"
        "v_mfma_f32_16x16x32_bf16 v[28:31], v[124:127], v[80:83], v[28:31]\n\t"
        "v_or3_b32 v7, v7, v120, v121\n\t"
        "s_waitcnt lgkmcnt(3)\n\t"
        "v_mfma_f32_16x16x32_bf16 v[32:35], v[120:123], v[84:87], v[32:35]\n\t"
        "v_mfma_f32_16x16x32_bf16 v[36:39], v[124:127], v[84:87], v[36:39]\n\t"
        "v_or3_b32 v7, v7, v124, v125\n\t"
        "v_and_b32 v7, s49, v7\n\t"
        "v_cmp_ne_u32 vcc, 0, v7\n\t"
        "s_cbranch_vccnz .L7_qr\n"
        ".L7_qd:\n\t"
        "s_mov_b64 exec, 1\n\t"
        "ds_add_u32 v2, v3 offset:12\n\t"
        "s_mov_b64 exec, -1\n\t"
        "v_and_b32 v6, 15, v1\n\t"
        "v_mul_u32_u24 v6, 272, v6\n\t"
        "v_and_b32 v7, 48, v1\n\t"
        "s_lshl_b32 s78, s80, 6\n\t"
        "v_add3_u32 v6, v6, v7, s78\n\t"
        "s_waitcnt lgkmcnt(2)\n\t"
        "v_mfma_f32_16x16x32_bf16 v[40:43], v[120:123], v[96:99], v[40:43]\n\t"
        "v_mfma_f32_16x16x32_bf16 v[44:47], v[124:127], v[96:99], v[44:47]\n\t"
        "v_cvt_pk_bf16_f32 v24, v24, v25\n\t"
        "v_cvt_pk_bf16_f32 v25, v26, v27\n\t"
        "v_cvt_pk_bf16_f32 v26, v28, v29\n\t"
        "v_cvt_pk_bf16_f32 v27, v30, v31\n\t"
        "s_waitcnt lgkmcnt(0)\n\t"
        "v_mfma_f32_16x16x32_bf16 v[48:51], v[120:123], v[100:103], v[48:51]\n\t"
        "v_mfma_f32_16x16x32_bf16 v[52:55], v[124:127], v[100:103], v[52:55]\n\t"
        "v_cvt_pk_bf16_f32 v32, v32, v33\n\t"
        "v_cvt_pk_bf16_f32 v33, v34, v35\n\t"
        "v_cvt_pk_bf16_f32 v34, v36, v37\n\t"
        "v_cvt_pk_bf16_f32 v35, v38, v39\n\t"
        "v_mfma_f32_16x16x32_bf16 v[56:59], v[120:123], v[64:67], v[56:59]\n\t"
        "v_mfma_f32_16x16x32_bf16 v[60:63], v[124:127], v[64:67], v[60:63]\n\t"
        "v_cvt_pk_bf16_f32 v40, v40, v41\n\t"
        "v_cvt_pk_bf16_f32 v41, v42, v43\n\t"
        "v_cvt_pk_bf16_f32 v42, v44, v45\n\t"
        "v_cvt_pk_bf16_f32 v43, v46, v47\n\t"
        "ds_read_b32 v0, v2 offset:12\n\t"
        "v_cvt_pk_bf16_f32 v48, v48, v49\n\t"
        "v_cvt_pk_bf16_f32 v49, v50, v51\n\t"
        "v_cvt_pk_bf16_f32 v50, v52, v53\n\t"
        "v_cvt_pk_bf16_f32 v51, v54, v55\n\t"
        "v_cvt_pk_bf16_f32 v56, v56, v57\n\t"
        "v_cvt_pk_bf16_f32 v57, v58, v59\n\t"
        "v_cvt_pk_bf16_f32 v58, v60, v61\n\t"
        "v_cvt_pk_bf16_f32 v59, v62, v63\n\t"
        "s_waitcnt lgkmcnt(0)\n\t"
        "v_cmp_gt_u16 vcc, 4, v0\n\t"
        "s_cbranch_vccz .L7_qx\n"
        ".L7_w58:\n\t"
        "ds_read_b32 v0, v2 offset:12\n\t"
        "s_waitcnt lgkmcnt(0)\n\t"
        "v_cmp_gt_u16 vcc, 4, v0\n\t"
        "s_cbranch_vccz .L7_d59\n\t"
        "s_sleep 0\n\t"
        "s_sub_i32 s83, s83, 1\n\t"
        "s_cmp_gt_i32 s83, 0\n\t"
        "s_cbranch_scc1 .L7_w58\n"
        ".L7_d59:\n"
        ".L7_qx:\n\t"
        "ds_write_b128 v6, v[24:27] offset:2048\n\t"
        "ds_write_b128 v6, v[32:35] offset:6400\n\t"
        "ds_write_b128 v6, v[40:43] offset:10752\n\t"
        "ds_write_b128 v6, v[48:51] offset:15104\n\t"
        "ds_write_b128 v6, v[56:59] offset:19456\n\t"
        "s_lshl_b32 s78, s69, 9\n\t"
        "v_lshl_add_u32 v7, v1, 3, s78\n\t"
        "v_mov_b64 v[8:9], 0\n\t"
        "ds_write_b64 v7, v[8:9] offset:34048\n\t"
        "s_mov_b64 exec, 1\n\t"
        "ds_add_u32 v2, v3 offset:16\n\t"
        "s_mov_b64 exec, -1\n\t"
        "v_lshrrev_b32 v10, 3, v1\n\t"
        "v_lshlrev_b32 v10, 9, v10\n\t"
        "v_and_b32 v11, 7, v1\n\t"
        "v_lshl_add_u32 v10, v11, 4, v10\n\t"
        "v_and_b32 v11, 16, v1\n\t"
        "v_lshlrev_b32 v11, 2, v11\n\t"
        "v_xor_b32 v10, v10, v11\n\t"
        "s_load_dwordx4 s[24:27], %2, 32\n\t"
        "s_lshl_b32 s78, s80, 14\n\t"
        "s_add_u32 m0, s78, 68864\n\t"
        "s_lshl_b32 s78, s70, 9\n\t"
        "s_add_u32 s78, s78, s80\n\t"
        "s_lshl_b32 s78, s78, 7\n"
        ".L7_w60:\n\t"
        "ds_read_b32 v0, v2 offset:24\n\t"
        "s_waitcnt lgkmcnt(0)\n\t"
        "v_cmp_gt_u16 vcc, 8, v0\n\t"
        "s_cbranch_vccz .L7_d61\n\t"
        "s_sleep 1\n\t"
        "s_sub_i32 s83, s83, 1\n\t"
        "s_cmp_gt_i32 s83, 0\n\t"
        "s_cbranch_scc1 .L7_w60\n"
        ".L7_d61:\n"
        ".L7_w62:\n\t"
        "ds_read_b32 v0, v2 offset:88\n\t"
        "s_waitcnt lgkmcnt(0)\n\t"
        "v_cmp_gt_u16 vcc, 16, v0\n\t"
        "s_cbranch_vccz .L7_d63\n\t"
        "s_sleep 1\n\t"
        "s_sub_i32 s83, s83, 1\n\t"
        "s_cmp_gt_i32 s83, 0\n\t"
        "s_cbranch_scc1 .L7_w62\n"
        ".L7_d63:\n\t"
        "s_setprio 3\n\t"
        "buffer_load_dwordx4 v10, s[24:27], s78 offen lds\n\t"
        "s_add_u32 m0, m0, 0x400\n\t"
        "s_add_u32 s78, s78, 0x1000\n\t"
        "buffer_load_dwordx4 v10, s[24:27], s78 offen lds\n\t"
        "s_add_u32 m0, m0, 0x400\n\t"
        "s_add_u32 s78, s78, 0x1000\n\t"
        "buffer_load_dwordx4 v10, s[24:27], s78 offen lds\n\t"
        "s_add_u32 m0, m0, 0x400\n\t"
        "s_add_u32 s78, s78, 0x1000\n\t"
        "buffer_load_dwordx4 v10, s[24:27], s78 offen lds\n\t"
        "s_add_u32 m0, m0, 0x400\n\t"
        "s_add_u32 s78, s78, 0x1000\n\t"
        "buffer_load_dwordx4 v10, s[24:27], s78 offen lds\n\t"
        "s_add_u32 m0, m0, 0x400\n\t"
        "s_add_u32 s78, s78, 0x1000\n\t"
        "buffer_load_dwordx4 v10, s[24:27], s78 offen lds\n\t"
        "s_add_u32 m0, m0, 0x400\n\t"
        "s_add_u32 s78, s78, 0x1000\n\t"
        "buffer_load_dwordx4 v10, s[24:27], s78 offen lds\n\t"
        "s_add_u32 m0, m0, 0x400\n\t"
        "s_add_u32 s78, s78, 0x1000\n\t"
        "buffer_load_dwordx4 v10, s[24:27], s78 offen lds\n\t"
        "s_add_u32 m0, m0, 0x400\n\t"
        "s_add_u32 s78, s78, 0x1000\n\t"
        "buffer_load_dwordx4 v10, s[24:27], s78 offen lds\n\t"
        "s_add_u32 m0, m0, 0x400\n\t"
        "s_add_u32 s78, s78, 0x1000\n\t"
        "buffer_load_dwordx4 v10, s[24:27], s78 offen lds\n\t"
        "s_add_u32 m0, m0, 0x400\n\t"
        "s_add_u32 s78, s78, 0x1000\n\t"
        "buffer_load_dwordx4 v10, s[24:27], s78 offen lds\n\t"
        "s_add_u32 m0, m0, 0x400\n\t"
        "s_add_u32 s78, s78, 0x1000\n\t"
        "buffer_load_dwordx4 v10, s[24:27], s78 offen lds\n\t"
        "s_add_u32 m0, m0, 0x400\n\t"
        "s_add_u32 s78, s78, 0x1000\n\t"
        "buffer_load_dwordx4 v10, s[24:27], s78 offen lds\n\t"
        "s_add_u32 m0, m0, 0x400\n\t"
        "s_add_u32 s78, s78, 0x1000\n\t"
        "buffer_load_dwordx4 v10, s[24:27], s78 offen lds\n\t"
        "s_add_u32 m0, m0, 0x400\n\t"
        "s_add_u32 s78, s78, 0x1000\n\t"
        "buffer_load_dwordx4 v10, s[24:27], s78 offen lds\n\t"
        "s_add_u32 m0, m0, 0x400\n\t"
        "s_add_u32 s78, s78, 0x1000\n\t"
        "buffer_load_dwordx4 v10, s[24:27], s78 offen lds\n\t"
        "s_waitcnt vmcnt(0)\n\t"
        "s_mov_b64 exec, 1\n\t"
        "ds_add_u32 v2, v3 offset:28\n\t"
        "s_mov_b64 exec, -1\n\t"
        "s_setprio 0\n"
        ".L7_w64:\n\t"
        "ds_read_b32 v0, v2 offset:36\n\t"
        "s_waitcnt lgkmcnt(0)\n\t"
        "v_cmp_gt_u16 vcc, 4, v0\n\t"
        "s_cbranch_vccz .L7_d65\n\t"
        "s_sleep 2\n\t"
        "s_sub_i32 s83, s83, 1\n\t"
        "s_cmp_gt_i32 s83, 0\n\t"
        "s_cbranch_scc1 .L7_w64\n"
        ".L7_d65:\n\t"
        "v_cmp_lt_u32 vcc, 0xfffff, v0\n\t"
        "s_cbranch_vccnz .L7_exh\n\t"
        "s_cmp_eq_u32 s69, 0\n\t"
        "s_cbranch_scc0 .L7_hz\n\t"
        "s_lshr_b32 s47, s68, 20\n\t"
        "s_add_u32 s47, s47, s68\n\t"
        "s_and_b32 s47, s47, 0xfff\n\t"
        "s_mul_i32 s46, s68, 0x9e377000\n\t"
        "s_or_b32 s46, s46, s47\n\t"
        "v_mov_b32 v4, s46\n\t"
        "s_xor_b32 s44, s70, 1\n\t"
        "s_lshl1_add_u32 s44, %3, s44\n\t"
        "s_lshl_b32 s44, s44, 10\n\t"
        "s_add_u32 s44, s44, 262144\n\t"
        "s_xor_b32 s45, s44, 0xc00\n"
        ".L7_w66:\n\t"
        "ds_read_b32 v0, v2 offset:56\n\t"
        "s_waitcnt lgkmcnt(0)\n\t"
        "v_cmp_gt_u16 vcc, 3, v0\n\t"
        "s_cbranch_vccz .L7_d67\n\t"
        "s_sleep 8\n\t"
        "s_sub_i32 s83, s83, 1\n\t"
        "s_cmp_gt_i32 s83, 0\n\t"
        "s_cbranch_scc1 .L7_w66\n"
        ".L7_d67:\n\t"
        "s_setprio 3\n"
        ".L7_w68:\n\t"
        "ds_read_b32 v0, v2 offset:96\n\t"
        "s_waitcnt lgkmcnt(0)\n\t"
        "v_cmp_gt_u16 vcc, 7, v0\n\t"
        "s_cbranch_vccz .L7_d69\n\t"
        "s_sleep 1\n\t"
        "s_sub_i32 s83, s83, 1\n\t"
        "s_cmp_gt_i32 s83, 0\n\t"
        "s_cbranch_scc1 .L7_w68\n"
        ".L7_d69:\n\t"
        "s_mov_b64 exec, 1\n\t"
        "buffer_store_dword v4, off, s[20:23], s44 offset:12 sc1\n\t"
        "s_mov_b32 s83, 64\n\t"
        "s_sleep 4\n"
        ".L7_h1:\n\t"
        "s_buffer_load_dword s47, s[20:23], s45 offset:12 glc\n\t"
        "s_waitcnt lgkmcnt(0)\n\t"
        "s_cmp_eq_u32 s47, s46\n\t"
        "s_cbranch_scc1 .L7_h2\n\t"
        "s_sleep 3\n\t"
        "s_sub_i32 s83, s83, 1\n\t"
        "s_cmp_gt_i32 s83, 0\n\t"
        "s_cbranch_scc1 .L7_h1\n"
        ".L7_h2:\n\t"
        "s_mov_b64 exec, 1\n\t"
        "ds_add_u32 v2, v3 offset:100\n\t"
        "s_mov_b64 exec, -1\n"
        ".L7_hz:\n\t"
        "s_endpgm\n"
        ".L7_exh:\n\t"
        "s_mov_b64 exec, 1\n\t"
        "ds_add_u32 v2, v3 offset:100\n\t"
        "s_mov_b64 exec, -1\n\t"
        "s_branch .L7_exit\n"
        ".L7_O2:\n\t"
        "v_lshrrev_b32 v6, 4, v1\n\t"
        "v_lshlrev_b32 v7, 2, v6\n\t"
        "v_and_b32 v8, 15, v1\n\t"
        "s_lshl_b32 s78, s70, 8\n\t"
        "v_lshl_add_u32 v9, v8, 4, s78\n\t"
        "v_lshrrev_b32 v10, 3, v1\n\t"
        "v_lshlrev_b32 v10, 9, v10\n\t"
        "v_and_b32 v11, 7, v1\n\t"
        "v_lshl_add_u32 v10, v11, 4, v10\n\t"
        "v_and_b32 v11, 16, v1\n\t"
        "v_lshlrev_b32 v11, 2, v11\n\t"
        "v_xor_b32 v10, v10, v11\n\t"
        "s_add_u32 s81, s69, s76\n\t"
        "s_and_b32 s81, s81, 7\n\t"
        "s_lshr_b32 s78, s81, 1\n\t"
        "s_lshl_b32 s78, s78, 7\n\t"
        "s_and_b32 s79, s81, 1\n\t"
        "s_lshl_b32 s79, s79, 15\n\t"
        "s_add_u32 s78, s78, s79\n\t"
        "s_lshl_b32 s79, s70, 16\n\t"
        "s_add_u32 s78, s78, s79\n\t"
        "v_add_u32 v10, s78, v10\n\t"
        "s_lshl_b32 s82, s81, 13\n\t"
        "s_add_u32 s82, s82, 68864\n\t"
        "v_bfe_u32 v12, v1, 2, 2\n\t"
        "v_lshlrev_b32 v11, 10, v6\n\t"
        "v_lshl_add_u32 v11, v12, 7, v11\n\t"
        "v_lshrrev_b32 v12, 1, v12\n\t"
        "s_and_b32 s78, s81, 1\n\t"
        "v_xor_b32 v12, s78, v12\n\t"
        "v_lshl_add_u32 v11, v12, 6, v11\n\t"
        "v_and_b32 v12, 3, v1\n\t"
        "v_lshl_add_u32 v11, v12, 4, v11\n\t"
        "s_lshr_b32 s78, s81, 1\n\t"
        "s_lshl_b32 s78, s78, 14\n\t"
        "s_add_u32 s78, s78, 68864\n\t"
        "v_add_u32 v23, s78, v11\n\t"
        "s_waitcnt vmcnt(0)\n\t"
        "ds_bpermute_b32 v12, v7, v5 offset:0\n\t"
        "ds_bpermute_b32 v13, v7, v5 offset:16\n\t"
        "ds_bpermute_b32 v14, v7, v5 offset:32\n\t"
        "ds_bpermute_b32 v15, v7, v5 offset:48\n\t"
        "ds_bpermute_b32 v16, v7, v5 offset:64\n\t"
        "ds_bpermute_b32 v17, v7, v5 offset:80\n\t"
        "ds_bpermute_b32 v18, v7, v5 offset:96\n\t"
        "ds_bpermute_b32 v19, v7, v5 offset:112\n\t"
        "ds_bpermute_b32 v20, v7, v5 offset:128\n\t"
        "ds_bpermute_b32 v21, v7, v5 offset:144\n\t"
        "s_waitcnt lgkmcnt(0)\n\t"
        "v_lshl_add_u32 v12, v12, 9, v9\n\t"
        "v_lshl_add_u32 v13, v13, 9, v9\n\t"
        "v_lshl_add_u32 v14, v14, 9, v9\n\t"
        "v_lshl_add_u32 v15, v15, 9, v9\n\t"
        "v_lshl_add_u32 v16, v16, 9, v9\n\t"
        "v_lshl_add_u32 v17, v17, 9, v9\n\t"
        "v_lshl_add_u32 v18, v18, 9, v9\n\t"
        "v_lshl_add_u32 v19, v19, 9, v9\n\t"
        "v_lshl_add_u32 v20, v20, 9, v9\n\t"
        "v_lshl_add_u32 v21, v21, 9, v9\n\t"
        "s_setprio 0\n"
        ".L7_w1:\n\t"
        "ds_read_b32 v0, v2 offset:44\n\t"
        "s_waitcnt lgkmcnt(0)\n\t"
        "v_cmp_gt_u16 vcc, 2, v0\n\t"
        "s_cbranch_vccz .L7_d2\n\t"
        "s_sleep 1\n\t"
        "s_sub_i32 s83, s83, 1\n\t"
        "s_cmp_gt_i32 s83, 0\n\t"
        "s_cbranch_scc1 .L7_w1\n"
        ".L7_d2:\n"
        ".L7_w3:\n\t"
        "ds_read_b64 v[4:5], v2 offset:72\n\t"
        "s_waitcnt lgkmcnt(0)\n\t"
        "v_cmp_gt_u16 vcc, 2, v4\n\t"
        "s_cbranch_vccz .L7_d4\n\t"
        "s_sleep 1\n\t"
        "s_sub_i32 s83, s83, 1\n\t"
        "s_cmp_gt_i32 s83, 0\n\t"
        "s_cbranch_scc1 .L7_w3\n"
        ".L7_d4:\n\t"
        "v_readfirstlane_b32 s68, v5\n\t"
        "s_add_u32 m0, s82, 0\n\t"
        "s_mov_b32 s78, 0\n\t"
        "buffer_load_dwordx4 v10, s[28:31], s78 offen lds\n\t"
        "s_add_u32 m0, m0, 0x400\n\t"
        "s_add_u32 s78, s78, 0x1000\n\t"
        "buffer_load_dwordx4 v10, s[28:31], s78 offen lds\n\t"
        "s_add_u32 m0, m0, 0x400\n\t"
        "s_add_u32 s78, s78, 0x1000\n\t"
        "buffer_load_dwordx4 v10, s[28:31], s78 offen lds\n\t"
        "s_add_u32 m0, m0, 0x400\n\t"
        "s_add_u32 s78, s78, 0x1000\n\t"
        "buffer_load_dwordx4 v10, s[28:31], s78 offen lds\n\t"
        "s_add_u32 m0, m0, 0x400\n\t"
        "s_add_u32 s78, s78, 0x1000\n\t"
        "buffer_load_dwordx4 v10, s[28:31], s78 offen lds\n\t"
        "s_add_u32 m0, m0, 0x400\n\t"
        "s_add_u32 s78, s78, 0x1000\n\t"
        "buffer_load_dwordx4 v10, s[28:31], s78 offen lds\n\t"
        "s_add_u32 m0, m0, 0x400\n\t"
        "s_add_u32 s78, s78, 0x1000\n\t"
        "buffer_load_dwordx4 v10, s[28:31], s78 offen lds\n\t"
        "s_add_u32 m0, m0, 0x400\n\t"
        "s_add_u32 s78, s78, 0x1000\n\t"
        "buffer_load_dwordx4 v10, s[28:31], s78 offen lds\n\t"
        "s_mov_b64 exec, 1\n\t"
        "ds_add_u32 v2, v3 offset:88\n\t"
        "s_mov_b64 exec, -1\n\t"
        "s_mov_b32 s36, 0x22222222\n\t"
        "s_mov_b32 s37, s36\n\t"
        "s_lshl_b64 s[38:39], s[36:37], 1\n\t"
        "s_lshl_b64 s[40:41], s[36:37], 2\n\t"
        "s_and_b32 s44, s68, 0xfff\n\t"
        "s_xor_b32 s45, s44, 0x5a5\n\t"
        "s_lshr_b32 s46, s68, 12\n\t"
        "s_lshr_b32 s47, s68, 20\n\t"
        "s_add_u32 s46, s46, s68\n\t"
        "s_and_b32 s46, s46, 0xfff\n\t"
        "s_add_u32 s47, s47, s68\n\t"
        "s_and_b32 s47, s47, 0xfff\n\t"
        "v_mov_b32 v22, s44\n\t"
        "v_mov_b32 v0, s45\n\t"
        "v_cndmask_b32 v22, v22, v0, s[36:37]\n\t"
        "v_mov_b32 v0, s46\n\t"
        "v_cndmask_b32 v22, v22, v0, s[38:39]\n\t"
        "v_mov_b32 v0, s47\n\t"
        "v_cndmask_b32 v22, v22, v0, s[40:41]\n\t"
        "s_lshl3_add_u32 s42, %3, s77\n\t"
        "s_lshl_b32 s42, s42, 8\n\t"
        "s_add_u32 s42, s42, 262144\n\t"
        "s_xor_b32 s43, s42, 0x800\n\t"
        "s_movk_i32 s48, 0xfff\n\t"
        "s_waitcnt vmcnt(0)\n\t"
        "s_mov_b64 exec, 1\n\t"
        "ds_add_u32 v2, v3 offset:20\n\t"
        "s_mov_b64 exec, -1\n"
        ".L7_w5:\n\t"
        "ds_read_b32 v0, v2 offset:20\n\t"
        "s_waitcnt lgkmcnt(0)\n\t"
        "v_cmp_gt_u16 vcc, 8, v0\n\t"
        "s_cbranch_vccz .L7_d6\n\t"
        "s_sleep 1\n\t"
        "s_sub_i32 s83, s83, 1\n\t"
        "s_cmp_gt_i32 s83, 0\n\t"
        "s_cbranch_scc1 .L7_w5\n"
        ".L7_d6:\n\t"
        "ds_read_b64_tr_b16 v[64:65], v23 offset:0\n\t"
        "ds_read_b64_tr_b16 v[66:67], v23 offset:512\n\t"
        "ds_read_b64_tr_b16 v[68:69], v23 offset:8\n\t"
        "ds_read_b64_tr_b16 v[70:71], v23 offset:520\n\t"
        "ds_read_b64_tr_b16 v[72:73], v23 offset:4096\n\t"
        "ds_read_b64_tr_b16 v[74:75], v23 offset:4608\n\t"
        "ds_read_b64_tr_b16 v[76:77], v23 offset:4104\n\t"
        "ds_read_b64_tr_b16 v[78:79], v23 offset:4616\n\t"
        "ds_read_b64_tr_b16 v[80:81], v23 offset:8192\n\t"
        "ds_read_b64_tr_b16 v[82:83], v23 offset:8704\n\t"
        "ds_read_b64_tr_b16 v[84:85], v23 offset:8200\n\t"
        "ds_read_b64_tr_b16 v[86:87], v23 offset:8712\n\t"
        "ds_read_b64_tr_b16 v[88:89], v23 offset:12288\n\t"
        "ds_read_b64_tr_b16 v[90:91], v23 offset:12800\n\t"
        "s_waitcnt lgkmcnt(10)\n\t"
        "ds_read_b64_tr_b16 v[92:93], v23 offset:12296\n\t"
        "ds_read_b64_tr_b16 v[94:95], v23 offset:12808\n\t"
        "s_mov_b64 exec, 1\n\t"
        "ds_add_u32 v2, v3 offset:24\n\t"
        "s_mov_b64 exec, -1\n\t"
        "buffer_load_dwordx4 v[24:27], v12, s[16:19], 0 offen\n\t"
        "buffer_load_dwordx4 v[28:31], v13, s[16:19], 0 offen\n\t"
        "buffer_load_dwordx4 v[32:35], v14, s[16:19], 0 offen\n\t"
        "buffer_load_dwordx4 v[36:39], v15, s[16:19], 0 offen\n\t"
        "buffer_load_dwordx4 v[40:43], v16, s[16:19], 0 offen\n\t"
        "buffer_load_dwordx4 v[44:47], v17, s[16:19], 0 offen\n\t"
        "buffer_load_dwordx4 v[48:51], v18, s[16:19], 0 offen\n\t"
        "buffer_load_dwordx4 v[52:55], v19, s[16:19], 0 offen\n\t"
        "buffer_load_dwordx4 v[56:59], v20, s[16:19], 0 offen\n\t"
        "buffer_load_dwordx4 v[60:63], v21, s[16:19], 0 offen\n\t"
        "s_mov_b64 exec, 1\n\t"
        "ds_add_u32 v2, v3 offset:88\n\t"
        "s_mov_b64 exec, -1\n"
        ".L7_o2:\n\t"
        "s_setprio 2\n\t"
        "s_mul_i32 s78, s80, 272\n\t"
        "v_lshl_add_u32 v21, v8, 4, s78\n"
        ".L7_w7:\n\t"
        "ds_read_b32 v0, v2 offset:16\n\t"
        "s_waitcnt lgkmcnt(0)\n\t"
        "v_cmp_gt_u16 vcc, 4, v0\n\t"
        "s_cbranch_vccz .L7_d8\n\t"
        "s_sleep 1\n\t"
        "s_sub_i32 s83, s83, 1\n\t"
        "s_cmp_gt_i32 s83, 0\n\t"
        "s_cbranch_scc1 .L7_w7\n"
        ".L7_d8:\n\t"
        "ds_read_b128 v[96:99], v21 offset:2048\n\t"
        "ds_read_b128 v[100:103], v21 offset:2320\n\t"
        "ds_read_b128 v[104:107], v21 offset:2592\n\t"
        "ds_read_b128 v[108:111], v21 offset:2864\n\t"
        "ds_read_b128 v[112:115], v21 offset:3136\n\t"
        "ds_read_b128 v[116:119], v21 offset:3408\n\t"
        "ds_read_b128 v[120:123], v21 offset:3680\n\t"
        "ds_read_b128 v[124:127], v21 offset:3952\n\t"
        "ds_read_b128 v[4:7], v21 offset:4224\n\t"
        "ds_read_b128 v[8:11], v21 offset:4496\n\t"
        "s_waitcnt vmcnt(0)\n\t"
        "s_waitcnt lgkmcnt(8)\n\t"
        "v_dot2_f32_bf16 v12, v24, v96, 0\n\t"
        "v_dot2_f32_bf16 v13, v28, v100, 0\n\t"
        "v_dot2_f32_bf16 v12, v25, v97, v12\n\t"
        "v_dot2_f32_bf16 v13, v29, v101, v13\n\t"
        "v_dot2_f32_bf16 v12, v26, v98, v12\n\t"
        "v_dot2_f32_bf16 v13, v30, v102, v13\n\t"
        "v_dot2_f32_bf16 v12, v27, v99, v12\n\t"
        "v_dot2_f32_bf16 v13, v31, v103, v13\n\t"
        "s_waitcnt lgkmcnt(6)\n\t"
        "v_dot2_f32_bf16 v14, v32, v104, 0\n\t"
        "v_dot2_f32_bf16 v15, v36, v108, 0\n\t"
        "v_dot2_f32_bf16 v14, v33, v105, v14\n\t"
        "v_dot2_f32_bf16 v15, v37, v109, v15\n\t"
        "v_dot2_f32_bf16 v14, v34, v106, v14\n\t"
        "v_dot2_f32_bf16 v15, v38, v110, v15\n\t"
        "v_dot2_f32_bf16 v14, v35, v107, v14\n\t"
        "v_dot2_f32_bf16 v15, v39, v111, v15\n\t"
        "s_waitcnt lgkmcnt(4)\n\t"
        "v_dot2_f32_bf16 v16, v40, v112, 0\n\t"
        "v_dot2_f32_bf16 v17, v44, v116, 0\n\t"
        "v_dot2_f32_bf16 v16, v41, v113, v16\n\t"
        "v_dot2_f32_bf16 v17, v45, v117, v17\n\t"
        "v_dot2_f32_bf16 v16, v42, v114, v16\n\t"
        "v_dot2_f32_bf16 v17, v46, v118, v17\n\t"
        "v_dot2_f32_bf16 v16, v43, v115, v16\n\t"
        "v_dot2_f32_bf16 v17, v47, v119, v17\n\t"
        "s_waitcnt lgkmcnt(2)\n\t"
        "v_dot2_f32_bf16 v18, v48, v120, 0\n\t"
        "v_dot2_f32_bf16 v19, v52, v124, 0\n\t"
        "v_dot2_f32_bf16 v18, v49, v121, v18\n\t"
        "v_dot2_f32_bf16 v19, v53, v125, v19\n\t"
        "v_dot2_f32_bf16 v18, v50, v122, v18\n\t"
        "v_dot2_f32_bf16 v19, v54, v126, v19\n\t"
        "v_dot2_f32_bf16 v18, v51, v123, v18\n\t"
        "v_dot2_f32_bf16 v19, v55, v127, v19\n\t"
        "s_waitcnt lgkmcnt(0)\n\t"
        "v_dot2_f32_bf16 v20, v56, v4, 0\n\t"
        "v_dot2_f32_bf16 v21, v60, v8, 0\n\t"
        "v_dot2_f32_bf16 v20, v57, v5, v20\n\t"
        "v_dot2_f32_bf16 v21, v61, v9, v21\n\t"
        "v_dot2_f32_bf16 v20, v58, v6, v20\n\t"
        "v_dot2_f32_bf16 v21, v62, v10, v21\n\t"
        "v_dot2_f32_bf16 v20, v59, v7, v20\n\t"
        "v_dot2_f32_bf16 v21, v63, v11, v21\n\t"
        "v_add_f32_dpp v12, v12, v12 row_ror:8 row_mask:0xf bank_mask:0x3\n\t"
        "v_add_f32_dpp v14, v14, v14 row_ror:8 row_mask:0xf bank_mask:0x3\n\t"
        "v_add_f32_dpp v16, v16, v16 row_ror:8 row_mask:0xf bank_mask:0x3\n\t"
        "v_add_f32_dpp v18, v18, v18 row_ror:8 row_mask:0xf bank_mask:0x3\n\t"
        "v_add_f32_dpp v20, v20, v20 row_ror:8 row_mask:0xf bank_mask:0x3\n\t"
        "v_add_f32_dpp v12, v13, v13 row_ror:8 row_mask:0xf bank_mask:0xc\n\t"
        "v_add_f32_dpp v14, v15, v15 row_ror:8 row_mask:0xf bank_mask:0xc\n\t"
        "v_add_f32_dpp v16, v17, v17 row_ror:8 row_mask:0xf bank_mask:0xc\n\t"
        "v_add_f32_dpp v18, v19, v19 row_ror:8 row_mask:0xf bank_mask:0xc\n\t"
        "v_add_f32_dpp v20, v21, v21 row_ror:8 row_mask:0xf bank_mask:0xc\n\t"
        "v_add_f32_dpp v12, v12, v12 row_half_mirror row_mask:0xf bank_mask:0x5\n\t"
        "v_add_f32_dpp v16, v16, v16 row_half_mirror row_mask:0xf bank_mask:0x5\n\t"
        "v_add_f32_dpp v20, v20, v20 row_half_mirror row_mask:0xf bank_mask:0xf\n\t"
        "v_add_f32_dpp v12, v14, v14 row_half_mirror row_mask:0xf bank_mask:0xa\n\t"
        "v_add_f32_dpp v16, v18, v18 row_half_mirror row_mask:0xf bank_mask:0xa\n\t"
        "v_add_f32_dpp v20, v20, v20 quad_perm:[1,0,3,2] row_mask:0xf bank_mask:0xf\n\t"
        "v_add_f32_dpp v12, v12, v12 quad_perm:[1,0,3,2] row_mask:0xf bank_mask:0xf\n\t"
        "v_add_f32_dpp v16, v16, v16 quad_perm:[1,0,3,2] row_mask:0xf bank_mask:0xf\n\t"
        "v_add_f32_dpp v20, v20, v20 quad_perm:[2,3,0,1] row_mask:0xf bank_mask:0xf\n\t"
        "v_add_f32_dpp v12, v12, v12 quad_perm:[2,3,0,1] row_mask:0xf bank_mask:0xf\n\t"
        "v_add_f32_dpp v16, v16, v16 quad_perm:[2,3,0,1] row_mask:0xf bank_mask:0xf\n\t"
        "v_cndmask_b32 v12, v12, v16, s[36:37]\n\t"
        "v_cndmask_b32 v12, v12, v20, s[38:39]\n\t"
        "v_add_u32 v12, 0x800, v12\n\t"
        "v_bfi_b32 v21, s48, v22, v12\n\t"
        "v_lshlrev_b32 v13, 2, v1\n\t"
        "s_setprio 3\n\t"
        "buffer_store_dword v21, v13, s[20:23], s42 offen sc1\n\t"
        "s_setprio 2\n\t"
        "v_and_b32 v14, 3, v1\n\t"
        "v_lshlrev_b32 v14, 4, v14\n\t"
        "s_mov_b32 s78, 0xffff\n\t"
        "s_mov_b32 s79, 0\n\t"
        "v_lshlrev_b64 v[4:5], v14, s[78:79]\n\t"
        "s_mov_b32 s50, 0x0f0f0f0f\n\t"
        "s_mov_b32 s51, s50\n\t"
        "v_cndmask_b32 v6, v4, 0, s[50:51]\n\t"
        "v_cndmask_b32 v7, v5, 0, s[50:51]\n\t"
        "v_cndmask_b32 v4, 0, v4, s[50:51]\n\t"
        "v_cndmask_b32 v5, 0, v5, s[50:51]\n\t"
        "v_lshrrev_b32 v14, 5, v1\n\t"
        "v_add_lshl_u32 v14, v14, s80, 1\n"
        ".L7_w9:\n\t"
        "ds_read_b32 v0, v2 offset:8\n\t"
        "ds_read_u16 v16, v14 offset:1792\n\t"
        "ds_read_u16 v17, v14 offset:1796\n\t"
        "ds_read_u16 v18, v14 offset:1800\n\t"
        "ds_read_u16 v19, v14 offset:1804\n\t"
        "ds_read_u16 v20, v14 offset:1808\n\t"
        "s_waitcnt lgkmcnt(5)\n\t"
        "v_cmp_gt_u16 vcc, 2, v0\n\t"
        "s_cbranch_vccz .L7_d10\n\t"
        "s_waitcnt lgkmcnt(0)\n\t"
        "s_sleep 1\n\t"
        "s_sub_i32 s83, s83, 1\n\t"
        "s_cmp_gt_i32 s83, 0\n\t"
        "s_cbranch_scc1 .L7_w9\n"
        ".L7_d10:\n\t"
        "v_and_b32 v14, 15, v1\n\t"
        "v_lshlrev_b32 v14, 4, v14\n\t"
        "v_bfe_u32 v15, v1, 4, 1\n\t"
        "v_lshl_add_u32 v14, v15, 3, v14\n\t"
        "s_waitcnt lgkmcnt(0)\n\t"
        "v_xor_b32 v16, v16, v14\n\t"
        "v_xor_b32 v17, v17, v14\n\t"
        "v_xor_b32 v18, v18, v14\n\t"
        "v_xor_b32 v19, v19, v14\n\t"
        "v_xor_b32 v20, v20, v14\n\t"
        "v_xor_b32 v21, v21, v22\n\t"
        "v_mov_b64 v[8:9], 0\n\t"
        "v_mov_b64 v[10:11], 0\n"
        ".L7_w11:\n\t"
        "ds_read_b32 v0, v2 offset:28\n\t"
        "s_waitcnt lgkmcnt(0)\n\t"
        "v_cmp_gt_u16 vcc, 4, v0\n\t"
        "s_cbranch_vccz .L7_d12\n\t"
        "s_sleep 1\n\t"
        "s_sub_i32 s83, s83, 1\n\t"
        "s_cmp_gt_i32 s83, 0\n\t"
        "s_cbranch_scc1 .L7_w11\n"
        ".L7_d12:\n\t"
        "ds_read_b64_tr_b16 v[96:97], v23 offset:0\n\t"
        "ds_read_b64_tr_b16 v[98:99], v23 offset:512\n\t"
        "ds_read_b64_tr_b16 v[100:101], v23 offset:8\n\t"
        "ds_read_b64_tr_b16 v[102:103], v23 offset:520\n\t"
        "ds_read_b64_tr_b16 v[104:105], v23 offset:4096\n\t"
        "ds_read_b64_tr_b16 v[106:107], v23 offset:4608\n\t"
        "ds_read_b64_tr_b16 v[108:109], v23 offset:4104\n\t"
        "ds_read_b64_tr_b16 v[110:111], v23 offset:4616\n\t"
        "ds_read_b64_tr_b16 v[112:113], v23 offset:8192\n\t"
        "ds_read_b64_tr_b16 v[114:115], v23 offset:8704\n\t"
        "ds_read_b64_tr_b16 v[116:117], v23 offset:8200\n\t"
        "ds_read_b64_tr_b16 v[118:119], v23 offset:8712\n\t"
        "ds_read_b64_tr_b16 v[120:121], v23 offset:12288\n\t"
        "ds_read_b64_tr_b16 v[122:123], v23 offset:12800\n"
        ".L7_sp:\n\t"
        "buffer_load_dword v12, v13, s[20:23], s43 offen sc1\n"
        ".L7_sw:\n\t"
        "s_waitcnt vmcnt(0)\n\t"
        "v_xor_b32 v12, v12, v22\n\t"
        "v_and_b32 v0, s48, v12\n\t"
        "v_cmp_ne_u32 vcc, 0, v0\n\t"
        "s_cbranch_vccz .L7_sq\n\t"
        "s_sleep 4\n\t"
        "s_sub_i32 s83, s83, 1\n\t"
        "s_cmp_gt_i32 s83, 0\n\t"
        "s_cbranch_scc1 .L7_sp\n"
        ".L7_sq:\n\t"
        "v_add_f32 v21, v21, v12\n\t"
        "v_exp_f32 v21, v21\n\t"
        "v_mov_b64 v[12:13], 0\n\t"
        "v_mov_b64 v[14:15], 0\n\t"
        "v_mov_b32 v22, v21\n\t"
        "v_mov_b32 v0, v21\n\t"
        "s_nop 1\n\t"
        "v_permlane16_swap_b32 v22, v0\n\t"
        "v_add_f32 v22, v22, v0\n\t"
        "v_mov_b32 v0, v22\n\t"
        "s_nop 1\n\t"
        "v_permlane32_swap_b32 v22, v0\n\t"
        "v_add_f32 v22, v22, v0\n\t"
        "v_add_f32 v22, 0x3089705f, v22\n\t"
        "v_rcp_f32 v22, v22\n\t"
        "s_nop 0\n\t"
        "v_mul_f32 v21, v21, v22\n\t"
        "v_cvt_pk_bf16_f32 v21, v21, v21\n\t"
        "v_lshlrev_b32 v22, 4, v1\n\t"
        "v_add_u32 v22, 142848, v22\n"
        ".L7_w13:\n\t"
        "ds_read_b32 v0, v2 offset:36\n\t"
        "s_waitcnt lgkmcnt(0)\n\t"
        "v_cmp_gt_u16 vcc, 4, v0\n\t"
        "s_cbranch_vccz .L7_d14\n\t"
        "s_sleep 1\n\t"
        "s_sub_i32 s83, s83, 1\n\t"
        "s_cmp_gt_i32 s83, 0\n\t"
        "s_cbranch_scc1 .L7_w13\n"
        ".L7_d14:\n\t"
        "v_cmp_lt_u32 vcc, 0xfffff, v0\n\t"
        "s_cbranch_vccnz .L7_na\n\t"
        "v_and_b32_dpp v8, v21, v4 row_newbcast:0 row_mask:0xf bank_mask:0x3\n\t"
        "v_and_b32_dpp v9, v21, v5 row_newbcast:0 row_mask:0xf bank_mask:0x3\n\t"
        "v_and_b32_dpp v10, v21, v6 row_newbcast:0 row_mask:0xf bank_mask:0x3\n\t"
        "v_and_b32_dpp v11, v21, v7 row_newbcast:0 row_mask:0xf bank_mask:0x3\n\t"
        "v_and_b32_dpp v12, v21, v4 row_newbcast:8 row_mask:0xf bank_mask:0xc\n\t"
        "v_and_b32_dpp v13, v21, v5 row_newbcast:8 row_mask:0xf bank_mask:0xc\n\t"
        "v_and_b32_dpp v14, v21, v6 row_newbcast:8 row_mask:0xf bank_mask:0xc\n\t"
        "v_and_b32_dpp v15, v21, v7 row_newbcast:8 row_mask:0xf bank_mask:0xc\n\t"
        "v_mfma_f32_16x16x32_bf16 v[124:127], v[8:11], v[24:27], 0\n\t"
        "s_nop 0\n\t"
        "v_mfma_f32_16x16x32_bf16 v[124:127], v[12:15], v[28:31], v[124:127]\n\t"
        "v_and_b32_dpp v8, v21, v4 row_newbcast:4 row_mask:0xf bank_mask:0x3\n\t"
        "v_and_b32_dpp v9, v21, v5 row_newbcast:4 row_mask:0xf bank_mask:0x3\n\t"
        "v_and_b32_dpp v10, v21, v6 row_newbcast:4 row_mask:0xf bank_mask:0x3\n\t"
        "v_and_b32_dpp v11, v21, v7 row_newbcast:4 row_mask:0xf bank_mask:0x3\n\t"
        "v_and_b32_dpp v12, v21, v4 row_newbcast:12 row_mask:0xf bank_mask:0xc\n\t"
        "v_and_b32_dpp v13, v21, v5 row_newbcast:12 row_mask:0xf bank_mask:0xc\n\t"
        "v_and_b32_dpp v14, v21, v6 row_newbcast:12 row_mask:0xf bank_mask:0xc\n\t"
        "v_and_b32_dpp v15, v21, v7 row_newbcast:12 row_mask:0xf bank_mask:0xc\n\t"
        "v_mfma_f32_16x16x32_bf16 v[24:27], v[8:11], v[32:35], 0\n\t"
        "s_nop 0\n\t"
        "v_mfma_f32_16x16x32_bf16 v[24:27], v[12:15], v[36:39], v[24:27]\n\t"
        "v_cvt_pk_bf16_f32 v124, v124, v125\n\t"
        "v_cvt_pk_bf16_f32 v125, v126, v127\n\t"
        "ds_write_b64 v16, v[124:125] offset:46336\n\t"
        "v_and_b32_dpp v8, v21, v4 row_newbcast:1 row_mask:0xf bank_mask:0x3\n\t"
        "v_and_b32_dpp v9, v21, v5 row_newbcast:1 row_mask:0xf bank_mask:0x3\n\t"
        "v_and_b32_dpp v10, v21, v6 row_newbcast:1 row_mask:0xf bank_mask:0x3\n\t"
        "v_and_b32_dpp v11, v21, v7 row_newbcast:1 row_mask:0xf bank_mask:0x3\n\t"
        "v_and_b32_dpp v12, v21, v4 row_newbcast:9 row_mask:0xf bank_mask:0xc\n\t"
        "v_and_b32_dpp v13, v21, v5 row_newbcast:9 row_mask:0xf bank_mask:0xc\n\t"
        "v_and_b32_dpp v14, v21, v6 row_newbcast:9 row_mask:0xf bank_mask:0xc\n\t"
        "v_and_b32_dpp v15, v21, v7 row_newbcast:9 row_mask:0xf bank_mask:0xc\n\t"
        "v_mfma_f32_16x16x32_bf16 v[32:35], v[8:11], v[40:43], 0\n\t"
        "s_nop 0\n\t"
        "v_mfma_f32_16x16x32_bf16 v[32:35], v[12:15], v[44:47], v[32:35]\n\t"
        "v_cvt_pk_bf16_f32 v24, v24, v25\n\t"
        "v_cvt_pk_bf16_f32 v25, v26, v27\n\t"
        "ds_write_b64 v17, v[24:25] offset:46336\n\t"
        "v_and_b32_dpp v8, v21, v4 row_newbcast:5 row_mask:0xf bank_mask:0x3\n\t"
        "v_and_b32_dpp v9, v21, v5 row_newbcast:5 row_mask:0xf bank_mask:0x3\n\t"
        "v_and_b32_dpp v10, v21, v6 row_newbcast:5 row_mask:0xf bank_mask:0x3\n\t"
        "v_and_b32_dpp v11, v21, v7 row_newbcast:5 row_mask:0xf bank_mask:0x3\n\t"
        "v_and_b32_dpp v12, v21, v4 row_newbcast:13 row_mask:0xf bank_mask:0xc\n\t"
        "v_and_b32_dpp v13, v21, v5 row_newbcast:13 row_mask:0xf bank_mask:0xc\n\t"
        "v_and_b32_dpp v14, v21, v6 row_newbcast:13 row_mask:0xf bank_mask:0xc\n\t"
        "v_and_b32_dpp v15, v21, v7 row_newbcast:13 row_mask:0xf bank_mask:0xc\n\t"
        "v_mfma_f32_16x16x32_bf16 v[40:43], v[8:11], v[48:51], 0\n\t"
        "s_nop 0\n\t"
        "v_mfma_f32_16x16x32_bf16 v[40:43], v[12:15], v[52:55], v[40:43]\n\t"
        "v_cvt_pk_bf16_f32 v32, v32, v33\n\t"
        "v_cvt_pk_bf16_f32 v33, v34, v35\n\t"
        "ds_write_b64 v18, v[32:33] offset:46336\n\t"
        "v_and_b32_dpp v8, v21, v4 row_newbcast:2 row_mask:0xf bank_mask:0x3\n\t"
        "v_and_b32_dpp v9, v21, v5 row_newbcast:2 row_mask:0xf bank_mask:0x3\n\t"
        "v_and_b32_dpp v10, v21, v6 row_newbcast:2 row_mask:0xf bank_mask:0x3\n\t"
        "v_and_b32_dpp v11, v21, v7 row_newbcast:2 row_mask:0xf bank_mask:0x3\n\t"
        "v_and_b32_dpp v12, v21, v4 row_newbcast:10 row_mask:0xf bank_mask:0xc\n\t"
        "v_and_b32_dpp v13, v21, v5 row_newbcast:10 row_mask:0xf bank_mask:0xc\n\t"
        "v_and_b32_dpp v14, v21, v6 row_newbcast:10 row_mask:0xf bank_mask:0xc\n\t"
        "v_and_b32_dpp v15, v21, v7 row_newbcast:10 row_mask:0xf bank_mask:0xc\n\t"
        "v_mfma_f32_16x16x32_bf16 v[48:51], v[8:11], v[56:59], 0\n\t"
        "s_nop 0\n\t"
        "v_mfma_f32_16x16x32_bf16 v[48:51], v[12:15], v[60:63], v[48:51]\n\t"
        "v_cvt_pk_bf16_f32 v40, v40, v41\n\t"
        "v_cvt_pk_bf16_f32 v41, v42, v43\n\t"
        "ds_write_b64 v19, v[40:41] offset:46336\n\t"
        "s_cmp_lt_u32 s69, 4\n\t"
        "s_cselect_b32 s78, 16, 1\n\t"
        "v_mov_b32 v0, s78\n\t"
        "s_nop 1\n\t"
        "v_cvt_pk_bf16_f32 v48, v48, v49\n\t"
        "v_cvt_pk_bf16_f32 v49, v50, v51\n\t"
        "ds_write_b64 v20, v[48:49] offset:46336\n\t"
        "s_mov_b64 exec, 1\n\t"
        "ds_add_u32 v2, v0 offset:40\n\t"
        "s_mov_b64 exec, -1\n"
        ".L7_na:\n\t"
        "ds_read_b64_tr_b16 v[124:125], v23 offset:12296\n\t"
        "ds_read_b64_tr_b16 v[126:127], v23 offset:12808\n\t"
        "ds_read_b128 v[4:7], v22\n\t"
        "s_lshl3_add_u32 s42, %3, s81\n\t"
        "s_mul_i32 s42, s42, 0xc00\n\t"
        "s_add_u32 s42, s42, 3145728\n\t"
        "s_xor_b32 s78, %3, 1\n\t"
        "s_lshl3_add_u32 s43, s78, s81\n\t"
        "s_mul_i32 s43, s43, 0xc00\n\t"
        "s_add_u32 s43, s43, 3145728\n\t"
        "s_lshl_b32 s50, s81, 6\n\t"
        "s_lshl_b32 s51, s69, 10\n\t"
        "s_add_u32 s51, s51, 134656\n\t"
        "s_mov_b32 s49, 0x10001\n\t"
        "s_mov_b32 s48, 0x07060302\n\t"
        "s_mov_b32 s55, 142848\n"
        ".L7_w15:\n\t"
        "ds_read_b32 v0, v2 offset:40\n\t"
        "s_waitcnt lgkmcnt(0)\n\t"
        "v_cmp_gt_u16 vcc, 64, v0\n\t"
        "s_cbranch_vccz .L7_d16\n\t"
        "s_setprio 0\n\t"
        "s_sleep 1\n\t"
        "s_sub_i32 s83, s83, 1\n\t"
        "s_cmp_gt_i32 s83, 0\n\t"
        "s_cbranch_scc1 .L7_w15\n"
        ".L7_d16:\n\t"
        "s_setprio 2\n\t"
        "ds_read_b128 v[8:11], v4 offset:46336\n\t"
        "ds_read_b128 v[12:15], v5 offset:46336\n\t"
        "ds_read_b128 v[16:19], v6 offset:46336\n\t"
        "ds_read_b128 v[20:23], v7 offset:46336\n\t"
        "ds_read_b128 v[48:51], v4 offset:50432\n\t"
        "ds_read_b128 v[52:55], v5 offset:50432\n\t"
        "ds_read_b128 v[56:59], v6 offset:50432\n\t"
        "ds_read_b128 v[60:63], v7 offset:50432\n\t"
        "v_and_b32 v3, 8, v1\n\t"
        "s_waitcnt lgkmcnt(6)\n\t"
        "v_mfma_f32_16x16x32_bf16 v[32:35], v[64:67], v[8:11], 0\n\t"
        "v_mfma_f32_16x16x32_bf16 v[36:39], v[68:71], v[8:11], 0\n\t"
        "v_mfma_f32_16x16x32_bf16 v[32:35], v[72:75], v[12:15], v[32:35]\n\t"
        "v_mfma_f32_16x16x32_bf16 v[36:39], v[76:79], v[12:15], v[36:39]\n\t"
        "s_waitcnt lgkmcnt(4)\n\t"
        "v_mfma_f32_16x16x32_bf16 v[32:35], v[80:83], v[16:19], v[32:35]\n\t"
        "v_mfma_f32_16x16x32_bf16 v[36:39], v[84:87], v[16:19], v[36:39]\n\t"
        "v_mfma_f32_16x16x32_bf16 v[32:35], v[88:91], v[20:23], v[32:35]\n\t"
        "v_mfma_f32_16x16x32_bf16 v[36:39], v[92:95], v[20:23], v[36:39]\n\t"
        "ds_read_b32 v0, v2 offset:40\n\t"
        "v_lshl_add_u32 v8, v3, 8, v4\n\t"
        "v_lshl_add_u32 v12, v3, 8, v5\n\t"
        "v_lshl_add_u32 v16, v3, 8, v6\n\t"
        "v_lshl_add_u32 v20, v3, 8, v7\n\t"
        "ds_read_b128 v[8:11], v8 offset:54528\n\t"
        "ds_read_b128 v[12:15], v12 offset:54528\n\t"
        "ds_read_b128 v[16:19], v16 offset:54528\n\t"
        "ds_read_b128 v[20:23], v20 offset:54528\n\t"
        "s_waitcnt lgkmcnt(7)\n\t"
        "v_mfma_f32_16x16x32_bf16 v[40:43], v[64:67], v[48:51], 0\n\t"
        "v_mfma_f32_16x16x32_bf16 v[44:47], v[68:71], v[48:51], 0\n\t"
        "v_mfma_f32_16x16x32_bf16 v[40:43], v[72:75], v[52:55], v[40:43]\n\t"
        "v_mfma_f32_16x16x32_bf16 v[44:47], v[76:79], v[52:55], v[44:47]\n\t"
        "s_waitcnt lgkmcnt(5)\n\t"
        "v_mfma_f32_16x16x32_bf16 v[40:43], v[80:83], v[56:59], v[40:43]\n\t"
        "v_mfma_f32_16x16x32_bf16 v[44:47], v[84:87], v[56:59], v[44:47]\n\t"
        "v_mfma_f32_16x16x32_bf16 v[40:43], v[88:91], v[60:63], v[40:43]\n\t"
        "v_mfma_f32_16x16x32_bf16 v[44:47], v[92:95], v[60:63], v[44:47]\n\t"
        "s_waitcnt lgkmcnt(4)\n\t"
        "v_cmp_gt_u16 vcc, 68, v0\n\t"
        "s_cbranch_vccz .L7_t0\n"
        ".L7_w17:\n\t"
        "ds_read_b32 v0, v2 offset:40\n\t"
        "s_waitcnt lgkmcnt(0)\n\t"
        "v_cmp_gt_u16 vcc, 68, v0\n\t"
        "s_cbranch_vccz .L7_d18\n\t"
        "s_setprio 0\n\t"
        "s_sleep 1\n\t"
        "s_sub_i32 s83, s83, 1\n\t"
        "s_cmp_gt_i32 s83, 0\n\t"
        "s_cbranch_scc1 .L7_w17\n"
        ".L7_d18:\n\t"
        "s_setprio 2\n\t"
        "v_lshl_add_u32 v8, v3, 8, v4\n\t"
        "v_lshl_add_u32 v12, v3, 8, v5\n\t"
        "v_lshl_add_u32 v16, v3, 8, v6\n\t"
        "v_lshl_add_u32 v20, v3, 8, v7\n\t"
        "ds_read_b128 v[8:11], v8 offset:54528\n\t"
        "ds_read_b128 v[12:15], v12 offset:54528\n\t"
        "ds_read_b128 v[16:19], v16 offset:54528\n\t"
        "ds_read_b128 v[20:23], v20 offset:54528\n"
        ".L7_t0:\n\t"
        "ds_read_b32 v0, v2 offset:52\n\t"
        "ds_read_b128 v[48:51], v4 offset:23808\n\t"
        "ds_read_b128 v[52:55], v5 offset:23808\n\t"
        "ds_read_b128 v[56:59], v6 offset:23808\n\t"
        "ds_read_b128 v[60:63], v7 offset:23808\n\t"
        "s_waitcnt lgkmcnt(7)\n\t"
        "v_mfma_f32_16x16x32_bf16 v[24:27], v[64:67], v[8:11], 0\n\t"
        "v_mfma_f32_16x16x32_bf16 v[28:31], v[68:71], v[8:11], 0\n\t"
        "v_mfma_f32_16x16x32_bf16 v[24:27], v[72:75], v[12:15], v[24:27]\n\t"
        "v_mfma_f32_16x16x32_bf16 v[28:31], v[76:79], v[12:15], v[28:31]\n\t"
        "s_waitcnt lgkmcnt(5)\n\t"
        "v_mfma_f32_16x16x32_bf16 v[24:27], v[80:83], v[16:19], v[24:27]\n\t"
        "v_mfma_f32_16x16x32_bf16 v[28:31], v[84:87], v[16:19], v[28:31]\n\t"
        "v_mfma_f32_16x16x32_bf16 v[24:27], v[88:91], v[20:23], v[24:27]\n\t"
        "v_mfma_f32_16x16x32_bf16 v[28:31], v[92:95], v[20:23], v[28:31]\n\t"
        "ds_read_b128 v[8:11], v4 offset:32000\n\t"
        "ds_read_b128 v[12:15], v5 offset:32000\n\t"
        "ds_read_b128 v[16:19], v6 offset:32000\n\t"
        "ds_read_b128 v[20:23], v7 offset:32000\n\t"
        "s_waitcnt lgkmcnt(8)\n\t"
        "v_cmp_gt_u16 vcc, 6, v0\n\t"
        "s_cbranch_vccz .L7_t1\n"
        ".L7_w19:\n\t"
        "ds_read_b32 v0, v2 offset:52\n\t"
        "s_waitcnt lgkmcnt(0)\n\t"
        "v_cmp_gt_u16 vcc, 6, v0\n\t"
        "s_cbranch_vccz .L7_d20\n\t"
        "s_setprio 0\n\t"
        "s_sleep 1\n\t"
        "s_sub_i32 s83, s83, 1\n\t"
        "s_cmp_gt_i32 s83, 0\n\t"
        "s_cbranch_scc1 .L7_w19\n"
        ".L7_d20:\n\t"
        "s_setprio 2\n\t"
        "ds_read_b128 v[48:51], v4 offset:23808\n\t"
        "ds_read_b128 v[52:55], v5 offset:23808\n\t"
        "ds_read_b128 v[56:59], v6 offset:23808\n\t"
        "ds_read_b128 v[60:63], v7 offset:23808\n\t"
        "ds_read_b128 v[8:11], v4 offset:32000\n\t"
        "ds_read_b128 v[12:15], v5 offset:32000\n\t"
        "ds_read_b128 v[16:19], v6 offset:32000\n\t"
        "ds_read_b128 v[20:23], v7 offset:32000\n"
        ".L7_t1:\n\t"
        "v_lshl_add_u32 v3, v1, 4, s55\n\t"
        "s_waitcnt lgkmcnt(6)\n\t"
        "v_mfma_f32_16x16x32_bf16 v[32:35], v[96:99], v[48:51], v[32:35]\n\t"
        "v_mfma_f32_16x16x32_bf16 v[36:39], v[100:103], v[48:51], v[36:39]\n\t"
        "v_mfma_f32_16x16x32_bf16 v[32:35], v[104:107], v[52:55], v[32:35]\n\t"
        "v_mfma_f32_16x16x32_bf16 v[36:39], v[108:111], v[52:55], v[36:39]\n\t"
        "s_waitcnt lgkmcnt(4)\n\t"
        "v_mfma_f32_16x16x32_bf16 v[32:35], v[112:115], v[56:59], v[32:35]\n\t"
        "v_mfma_f32_16x16x32_bf16 v[36:39], v[116:119], v[56:59], v[36:39]\n\t"
        "v_mfma_f32_16x16x32_bf16 v[32:35], v[120:123], v[60:63], v[32:35]\n\t"
        "v_mfma_f32_16x16x32_bf16 v[36:39], v[124:127], v[60:63], v[36:39]\n\t"
        "ds_read_b128 v[60:63], v3 offset:1024\n\t"
        "s_waitcnt lgkmcnt(3)\n\t"
        "v_mfma_f32_16x16x32_bf16 v[24:27], v[96:99], v[8:11], v[24:27]\n\t"
        "v_mfma_f32_16x16x32_bf16 v[28:31], v[100:103], v[8:11], v[28:31]\n\t"
        "v_mfma_f32_16x16x32_bf16 v[24:27], v[104:107], v[12:15], v[24:27]\n\t"
        "v_mfma_f32_16x16x32_bf16 v[28:31], v[108:111], v[12:15], v[28:31]\n\t"
        "s_waitcnt lgkmcnt(1)\n\t"
        "v_mfma_f32_16x16x32_bf16 v[24:27], v[112:115], v[16:19], v[24:27]\n\t"
        "v_mfma_f32_16x16x32_bf16 v[28:31], v[116:119], v[16:19], v[28:31]\n\t"
        "v_mfma_f32_16x16x32_bf16 v[24:27], v[120:123], v[20:23], v[24:27]\n\t"
        "v_mfma_f32_16x16x32_bf16 v[28:31], v[124:127], v[20:23], v[28:31]\n\t"
        "ds_read_b32 v0, v2 offset:56\n\t"
        "ds_read_b128 v[8:11], v4 offset:27904\n\t"
        "ds_read_b128 v[12:15], v5 offset:27904\n\t"
        "ds_read_b128 v[16:19], v6 offset:27904\n\t"
        "ds_read_b128 v[20:23], v7 offset:27904\n\t"
        "s_waitcnt lgkmcnt(5)\n\t"
        "v_add_u32 v32, 0x10000, v32\n\t"
        "v_add_u32 v33, 0x10000, v33\n\t"
        "v_add_u32 v34, 0x10000, v34\n\t"
        "v_add_u32 v35, 0x10000, v35\n\t"
        "v_perm_b32 v52, v33, v32, s48\n\t"
        "v_perm_b32 v53, v35, v34, s48\n\t"
        "v_cvt_pk_bf16_f32 v54, v36, v37\n\t"
        "v_cvt_pk_bf16_f32 v55, v38, v39\n\t"
        "v_bfi_b32 v52, s49, v60, v52\n\t"
        "v_bfi_b32 v53, s49, v61, v53\n\t"
        "buffer_store_dwordx4 v[52:55], v62, s[20:23], s42 offen offset:1024 sc1\n\t"
        "s_mov_b32 exec_lo, 0x00ff00ff\n\t"
        "s_mov_b32 exec_hi, 0x00ff00ff\n\t"
        "v_add_u32 v24, 0x10000, v24\n\t"
        "v_add_u32 v25, 0x10000, v25\n\t"
        "v_add_u32 v26, 0x10000, v26\n\t"
        "v_add_u32 v27, 0x10000, v27\n\t"
        "v_perm_b32 v48, v25, v24, s48\n\t"
        "v_perm_b32 v49, v27, v26, s48\n\t"
        "v_cvt_pk_bf16_f32 v50, v28, v29\n\t"
        "v_cvt_pk_bf16_f32 v51, v30, v31\n\t"
        "v_bfi_b32 v48, s49, v60, v48\n\t"
        "v_bfi_b32 v49, s49, v61, v49\n\t"
        "buffer_store_dwordx4 v[48:51], v62, s[20:23], s42 offen offset:0 sc1\n\t"
        "s_mov_b64 exec, -1\n\t"
        "s_waitcnt lgkmcnt(4)\n\t"
        "v_cmp_gt_u16 vcc, 4, v0\n\t"
        "s_cbranch_vccz .L7_t2\n"
        ".L7_w21:\n\t"
        "ds_read_b32 v0, v2 offset:56\n\t"
        "s_waitcnt lgkmcnt(0)\n\t"
        "v_cmp_gt_u16 vcc, 3, v0\n\t"
        "s_cbranch_vccz .L7_d22\n\t"
        "s_setprio 0\n\t"
        "s_sleep 1\n\t"
        "s_sub_i32 s83, s83, 1\n\t"
        "s_cmp_gt_i32 s83, 0\n\t"
        "s_cbranch_scc1 .L7_w21\n"
        ".L7_d22:\n\t"
        "s_setprio 2\n"
        ".L7_w23:\n\t"
        "ds_read_b32 v0, v2 offset:56\n\t"
        "ds_read_b128 v[8:11], v4 offset:27904\n\t"
        "ds_read_b128 v[12:15], v5 offset:27904\n\t"
        "ds_read_b128 v[16:19], v6 offset:27904\n\t"
        "ds_read_b128 v[20:23], v7 offset:27904\n\t"
        "s_waitcnt lgkmcnt(4)\n\t"
        "v_cmp_gt_u16 vcc, 4, v0\n\t"
        "s_cbranch_vccz .L7_d24\n\t"
        "s_waitcnt lgkmcnt(0)\n\t"
        "s_setprio 0\n\t"
        "s_sleep 1\n\t"
        "s_sub_i32 s83, s83, 1\n\t"
        "s_cmp_gt_i32 s83, 0\n\t"
        "s_cbranch_scc1 .L7_w23\n"
        ".L7_d24:\n\t"
        "s_setprio 2\n"
        ".L7_t2:\n\t"
        "s_waitcnt lgkmcnt(2)\n\t"
        "v_mfma_f32_16x16x32_bf16 v[40:43], v[96:99], v[8:11], v[40:43]\n\t"
        "v_mfma_f32_16x16x32_bf16 v[44:47], v[100:103], v[8:11], v[44:47]\n\t"
        "v_mfma_f32_16x16x32_bf16 v[40:43], v[104:107], v[12:15], v[40:43]\n\t"
        "v_mfma_f32_16x16x32_bf16 v[44:47], v[108:111], v[12:15], v[44:47]\n\t"
        "s_waitcnt lgkmcnt(0)\n\t"
        "v_mfma_f32_16x16x32_bf16 v[40:43], v[112:115], v[16:19], v[40:43]\n\t"
        "v_mfma_f32_16x16x32_bf16 v[44:47], v[116:119], v[16:19], v[44:47]\n\t"
        "v_mfma_f32_16x16x32_bf16 v[40:43], v[120:123], v[20:23], v[40:43]\n\t"
        "v_mfma_f32_16x16x32_bf16 v[44:47], v[124:127], v[20:23], v[44:47]\n\t"
        "ds_read_b128 v[8:11], v4 offset:60672\n\t"
        "ds_read_b128 v[12:15], v5 offset:60672\n\t"
        "ds_read_b128 v[16:19], v6 offset:60672\n\t"
        "ds_read_b128 v[20:23], v7 offset:60672\n\t"
        "s_nop 2\n\t"
        "v_add_u32 v40, 0x10000, v40\n\t"
        "v_add_u32 v41, 0x10000, v41\n\t"
        "v_add_u32 v42, 0x10000, v42\n\t"
        "v_add_u32 v43, 0x10000, v43\n\t"
        "v_perm_b32 v56, v41, v40, s48\n\t"
        "v_perm_b32 v57, v43, v42, s48\n\t"
        "v_cvt_pk_bf16_f32 v58, v44, v45\n\t"
        "v_cvt_pk_bf16_f32 v59, v46, v47\n\t"
        "v_bfi_b32 v56, s49, v60, v56\n\t"
        "v_bfi_b32 v57, s49, v61, v57\n\t"
        "buffer_store_dwordx4 v[56:59], v62, s[20:23], s42 offen offset:2048 sc1\n\t"
        "v_mov_b32 v0, 1\n\t"
        "s_mov_b64 exec, 1\n\t"
        "ds_add_u32 v2, v0 offset:96\n\t"
        "s_mov_b64 exec, -1\n\t"
        "s_waitcnt lgkmcnt(3)\n\t"
        "v_mfma_f32_16x16x32_bf16 v[32:35], v[64:67], v[8:11], 0\n\t"
        "v_mfma_f32_16x16x32_bf16 v[36:39], v[68:71], v[8:11], 0\n\t"
        "v_mfma_f32_16x16x32_bf16 v[32:35], v[72:75], v[12:15], v[32:35]\n\t"
        "v_mfma_f32_16x16x32_bf16 v[36:39], v[76:79], v[12:15], v[36:39]\n\t"
        "s_waitcnt lgkmcnt(1)\n\t"
        "v_mfma_f32_16x16x32_bf16 v[32:35], v[80:83], v[16:19], v[32:35]\n\t"
        "v_mfma_f32_16x16x32_bf16 v[36:39], v[84:87], v[16:19], v[36:39]\n\t"
        "v_mfma_f32_16x16x32_bf16 v[32:35], v[88:91], v[20:23], v[32:35]\n\t"
        "v_mfma_f32_16x16x32_bf16 v[36:39], v[92:95], v[20:23], v[36:39]\n\t"
        "ds_read_b128 v[8:11], v4 offset:64768\n\t"
        "ds_read_b128 v[12:15], v5 offset:64768\n\t"
        "ds_read_b128 v[16:19], v6 offset:64768\n\t"
        "ds_read_b128 v[20:23], v7 offset:64768\n\t"
        "s_waitcnt lgkmcnt(2)\n\t"
        "v_mfma_f32_16x16x32_bf16 v[40:43], v[64:67], v[8:11], 0\n\t"
        "v_mfma_f32_16x16x32_bf16 v[44:47], v[68:71], v[8:11], 0\n\t"
        "v_mfma_f32_16x16x32_bf16 v[40:43], v[72:75], v[12:15], v[40:43]\n\t"
        "v_mfma_f32_16x16x32_bf16 v[44:47], v[76:79], v[12:15], v[44:47]\n\t"
        "s_waitcnt lgkmcnt(0)\n\t"
        "v_mfma_f32_16x16x32_bf16 v[40:43], v[80:83], v[16:19], v[40:43]\n\t"
        "v_mfma_f32_16x16x32_bf16 v[44:47], v[84:87], v[16:19], v[44:47]\n\t"
        "v_mfma_f32_16x16x32_bf16 v[40:43], v[88:91], v[20:23], v[40:43]\n\t"
        "v_mfma_f32_16x16x32_bf16 v[44:47], v[92:95], v[20:23], v[44:47]\n\t"
        "ds_read_b128 v[68:71], v3 offset:2048\n\t"
        "ds_read_b128 v[74:77], v3 offset:3072\n"
        ".L7_w25:\n\t"
        "ds_read_b32 v0, v2 offset:60\n\t"
        "s_waitcnt lgkmcnt(0)\n\t"
        "v_cmp_gt_u16 vcc, 2, v0\n\t"
        "s_cbranch_vccz .L7_d26\n\t"
        "s_setprio 0\n\t"
        "s_sleep 1\n\t"
        "s_sub_i32 s83, s83, 1\n\t"
        "s_cmp_gt_i32 s83, 0\n\t"
        "s_cbranch_scc1 .L7_w25\n"
        ".L7_d26:\n\t"
        "s_setprio 2\n\t"
        "ds_read_b128 v[8:11], v4 offset:34048\n\t"
        "ds_read_b128 v[12:15], v5 offset:34048\n\t"
        "ds_read_b128 v[16:19], v6 offset:34048\n\t"
        "ds_read_b128 v[20:23], v7 offset:34048\n\t"
        "v_add_u32 v71, s51, v71\n\t"
        "v_add_u32 v72, s51, v74\n\t"
        "v_mov_b64 v[74:75], 0\n\t"
        "v_mov_b64 v[78:79], 0\n\t"
        "s_waitcnt lgkmcnt(2)\n\t"
        "v_mfma_f32_16x16x32_bf16 v[24:27], v[96:99], v[8:11], v[24:27]\n\t"
        "v_mfma_f32_16x16x32_bf16 v[28:31], v[100:103], v[8:11], v[28:31]\n\t"
        "v_mfma_f32_16x16x32_bf16 v[24:27], v[104:107], v[12:15], v[24:27]\n\t"
        "v_mfma_f32_16x16x32_bf16 v[28:31], v[108:111], v[12:15], v[28:31]\n\t"
        "s_waitcnt lgkmcnt(0)\n\t"
        "v_mfma_f32_16x16x32_bf16 v[24:27], v[112:115], v[16:19], v[24:27]\n\t"
        "v_mfma_f32_16x16x32_bf16 v[28:31], v[116:119], v[16:19], v[28:31]\n\t"
        "v_mfma_f32_16x16x32_bf16 v[24:27], v[120:123], v[20:23], v[24:27]\n\t"
        "v_mfma_f32_16x16x32_bf16 v[28:31], v[124:127], v[20:23], v[28:31]\n"
        ".L7_w27:\n\t"
        "ds_read_b32 v0, v2 offset:100\n\t"
        "s_waitcnt lgkmcnt(0)\n\t"
        "v_cmp_gt_u16 vcc, 1, v0\n\t"
        "s_cbranch_vccz .L7_d28\n\t"
        "s_setprio 0\n\t"
        "s_sleep 1\n\t"
        "s_sub_i32 s83, s83, 1\n\t"
        "s_cmp_gt_i32 s83, 0\n\t"
        "s_cbranch_scc1 .L7_w27\n"
        ".L7_d28:\n\t"
        "s_setprio 2\n\t"
        "buffer_load_dwordx4 v[48:51], v63, s[20:23], s43 offen offset:0 sc1\n\t"
        "buffer_load_dwordx4 v[52:55], v62, s[20:23], s43 offen offset:1024 sc1\n\t"
        "buffer_load_dwordx4 v[56:59], v62, s[20:23], s43 offen offset:2048 sc1\n"
        ".L7_w29:\n\t"
        "ds_read_b32 v0, v2 offset:64\n\t"
        "s_waitcnt lgkmcnt(0)\n\t"
        "v_cmp_gt_u16 vcc, 4, v0\n\t"
        "s_cbranch_vccz .L7_d30\n\t"
        "s_setprio 0\n\t"
        "s_sleep 1\n\t"
        "s_sub_i32 s83, s83, 1\n\t"
        "s_cmp_gt_i32 s83, 0\n\t"
        "s_cbranch_scc1 .L7_w29\n"
        ".L7_d30:\n\t"
        "s_setprio 2\n\t"
        "ds_read_b128 v[8:11], v4 offset:38144\n\t"
        "ds_read_b128 v[12:15], v5 offset:38144\n\t"
        "ds_read_b128 v[16:19], v6 offset:38144\n\t"
        "ds_read_b128 v[20:23], v7 offset:38144\n\t"
        "s_waitcnt lgkmcnt(2)\n\t"
        "v_mfma_f32_16x16x32_bf16 v[32:35], v[96:99], v[8:11], v[32:35]\n\t"
        "v_mfma_f32_16x16x32_bf16 v[36:39], v[100:103], v[8:11], v[36:39]\n\t"
        "v_mfma_f32_16x16x32_bf16 v[32:35], v[104:107], v[12:15], v[32:35]\n\t"
        "v_mfma_f32_16x16x32_bf16 v[36:39], v[108:111], v[12:15], v[36:39]\n\t"
        "s_waitcnt lgkmcnt(0)\n\t"
        "v_mfma_f32_16x16x32_bf16 v[32:35], v[112:115], v[16:19], v[32:35]\n\t"
        "v_mfma_f32_16x16x32_bf16 v[36:39], v[116:119], v[16:19], v[36:39]\n\t"
        "v_mfma_f32_16x16x32_bf16 v[32:35], v[120:123], v[20:23], v[32:35]\n\t"
        "v_mfma_f32_16x16x32_bf16 v[36:39], v[124:127], v[20:23], v[36:39]\n\t"
        "s_waitcnt vmcnt(0)\n\t"
        "s_branch .L7_s32\n"
        ".L7_r31:\n\t"
        "buffer_load_dwordx4 v[48:51], v63, s[20:23], s43 offen offset:0 sc1\n\t"
        "buffer_load_dwordx4 v[52:55], v62, s[20:23], s43 offen offset:1024 sc1\n\t"
        "buffer_load_dwordx4 v[56:59], v62, s[20:23], s43 offen offset:2048 sc1\n\t"
        "s_waitcnt vmcnt(0)\n"
        ".L7_s32:\n\t"
        "v_xor_b32 v48, v48, v60\n\t"
        "v_xor_b32 v49, v49, v61\n\t"
        "v_xor_b32 v52, v52, v60\n\t"
        "v_xor_b32 v53, v53, v61\n\t"
        "v_xor_b32 v56, v56, v60\n\t"
        "v_xor_b32 v57, v57, v61\n\t"
        "v_or_b32 v0, v48, v49\n\t"
        "v_or3_b32 v0, v0, v52, v53\n\t"
        "v_or3_b32 v0, v0, v56, v57\n\t"
        "v_and_b32 v0, s49, v0\n\t"
        "v_cmp_ne_u32 vcc, 0, v0\n\t"
        "s_cbranch_vccz .L7_t33\n\t"
        "s_sleep 8\n\t"
        "s_sub_i32 s83, s83, 1\n\t"
        "s_cmp_gt_i32 s83, 0\n\t"
        "s_cbranch_scc1 .L7_r31\n"
        ".L7_t33:\n\t"
        "v_mfma_f32_16x16x32_bf16 v[24:27], v[76:79], v[48:51], v[24:27]\n\t"
        "v_mfma_f32_16x16x32_bf16 v[28:31], v[74:77], v[48:51], v[28:31]\n\t"
        "v_mfma_f32_16x16x32_bf16 v[32:35], v[76:79], v[52:55], v[32:35]\n\t"
        "v_mfma_f32_16x16x32_bf16 v[36:39], v[74:77], v[52:55], v[36:39]\n\t"
        "v_mfma_f32_16x16x32_bf16 v[40:43], v[76:79], v[56:59], v[40:43]\n\t"
        "v_mfma_f32_16x16x32_bf16 v[44:47], v[74:77], v[56:59], v[44:47]\n\t"
        "s_nop 2\n\t"
        "v_cvt_pk_bf16_f32 v48, v24, v25\n\t"
        "v_cvt_pk_bf16_f32 v49, v26, v27\n\t"
        "v_cvt_pk_bf16_f32 v50, v28, v29\n\t"
        "v_cvt_pk_bf16_f32 v51, v30, v31\n\t"
        "ds_write_b128 v71, v[48:51]\n\t"
        "ds_read_b128 v[48:51], v72\n\t"
        "v_cvt_pk_bf16_f32 v52, v32, v33\n\t"
        "v_cvt_pk_bf16_f32 v53, v34, v35\n\t"
        "v_cvt_pk_bf16_f32 v54, v36, v37\n\t"
        "v_cvt_pk_bf16_f32 v55, v38, v39\n\t"
        "ds_write_b128 v71, v[52:55]\n\t"
        "ds_read_b128 v[52:55], v72\n\t"
        "ds_read_b32 v0, v2 offset:68\n\t"
        "ds_read_b128 v[8:11], v4 offset:42240\n\t"
        "ds_read_b128 v[12:15], v5 offset:42240\n\t"
        "ds_read_b128 v[16:19], v6 offset:42240\n\t"
        "ds_read_b128 v[20:23], v7 offset:42240\n\t"
        "s_waitcnt lgkmcnt(4)\n\t"
        "buffer_store_dwordx4 v[48:51], v68, s[60:63], s50 offen sc1\n\t"
        "buffer_store_dwordx4 v[52:55], v69, s[60:63], s50 offen sc1\n\t"
        "v_cmp_gt_u16 vcc, 4, v0\n\t"
        "s_cbranch_vccz .L7_t5\n"
        ".L7_w34:\n\t"
        "ds_read_b32 v0, v2 offset:68\n\t"
        "s_waitcnt lgkmcnt(0)\n\t"
        "v_cmp_gt_u16 vcc, 3, v0\n\t"
        "s_cbranch_vccz .L7_d35\n\t"
        "s_setprio 0\n\t"
        "s_sleep 1\n\t"
        "s_sub_i32 s83, s83, 1\n\t"
        "s_cmp_gt_i32 s83, 0\n\t"
        "s_cbranch_scc1 .L7_w34\n"
        ".L7_d35:\n\t"
        "s_setprio 2\n"
        ".L7_w36:\n\t"
        "ds_read_b32 v0, v2 offset:68\n\t"
        "ds_read_b128 v[8:11], v4 offset:42240\n\t"
        "ds_read_b128 v[12:15], v5 offset:42240\n\t"
        "ds_read_b128 v[16:19], v6 offset:42240\n\t"
        "ds_read_b128 v[20:23], v7 offset:42240\n\t"
        "s_waitcnt lgkmcnt(4)\n\t"
        "v_cmp_gt_u16 vcc, 4, v0\n\t"
        "s_cbranch_vccz .L7_d37\n\t"
        "s_waitcnt lgkmcnt(0)\n\t"
        "s_setprio 0\n\t"
        "s_sleep 0\n\t"
        "s_sub_i32 s83, s83, 1\n\t"
        "s_cmp_gt_i32 s83, 0\n\t"
        "s_cbranch_scc1 .L7_w36\n"
        ".L7_d37:\n\t"
        "s_setprio 2\n"
        ".L7_t5:\n\t"
        "s_waitcnt lgkmcnt(2)\n\t"
        "v_mfma_f32_16x16x32_bf16 v[40:43], v[96:99], v[8:11], v[40:43]\n\t"
        "v_mfma_f32_16x16x32_bf16 v[44:47], v[100:103], v[8:11], v[44:47]\n\t"
        "v_mfma_f32_16x16x32_bf16 v[40:43], v[104:107], v[12:15], v[40:43]\n\t"
        "v_mfma_f32_16x16x32_bf16 v[44:47], v[108:111], v[12:15], v[44:47]\n\t"
        "s_waitcnt lgkmcnt(0)\n\t"
        "v_mfma_f32_16x16x32_bf16 v[40:43], v[112:115], v[16:19], v[40:43]\n\t"
        "v_mfma_f32_16x16x32_bf16 v[44:47], v[116:119], v[16:19], v[44:47]\n\t"
        "v_mfma_f32_16x16x32_bf16 v[40:43], v[120:123], v[20:23], v[40:43]\n\t"
        "v_mfma_f32_16x16x32_bf16 v[44:47], v[124:127], v[20:23], v[44:47]\n\t"
        "s_nop 6\n\t"
        "v_cvt_pk_bf16_f32 v56, v40, v41\n\t"
        "v_cvt_pk_bf16_f32 v57, v42, v43\n\t"
        "v_cvt_pk_bf16_f32 v58, v44, v45\n\t"
        "v_cvt_pk_bf16_f32 v59, v46, v47\n\t"
        "ds_write_b128 v71, v[56:59]\n\t"
        "ds_read_b128 v[56:59], v72\n\t"
        "s_waitcnt lgkmcnt(0)\n\t"
        "buffer_store_dwordx4 v[56:59], v70, s[60:63], s50 offen sc1\n\t"
        "s_or_b32 s78, %3, s69\n\t"
        "s_cbranch_scc1 .L7_ng\n\t"
        "v_mov_b32 v0, s68\n\t"
        "buffer_store_dword v0, off, s[20:23], 0 sc1\n"
        ".L7_ng:\n\t"
        "s_endpgm\n"
        ".L7_G2:\n\t"
        "s_setprio 1\n\t"
        "v_lshlrev_b32 v5, 4, v1\n\t"
        "s_mul_i32 s82, s76, 13\n\t"
        "s_lshr_b32 s82, s82, 6\n\t"
        "s_mul_i32 s82, s82, 5\n\t"
        "s_sub_u32 s82, s76, s82\n\t"
        "s_lshl2_add_u32 s78, s82, s69\n\t"
        "s_lshl_b32 s78, s78, 10\n\t"
        "s_add_u32 m0, s78, 46336\n\t"
        "s_add_u32 s82, s82, 1\n\t"
        "s_cmp_eq_u32 s82, 5\n\t"
        "buffer_load_dwordx4 v5, s[56:59], s78 offen lds\n\t"
        "s_cselect_b32 s82, 0, s82\n\t"
        "v_lshrrev_b32 v18, 3, v1\n\t"
        "v_lshlrev_b32 v18, 14, v18\n\t"
        "v_and_b32 v6, 0x70, v5\n\t"
        "s_lshl_b32 s78, s69, 19\n\t"
        "s_lshl_b32 s79, s71, 7\n\t"
        "s_add_u32 s78, s78, s79\n\t"
        "v_add3_u32 v18, v18, v6, s78\n\t"
        "s_mov_b32 s24, 1048576\n\t"
        "s_mov_b32 s25, 1179648\n\t"
        "s_mov_b32 s26, 1310720\n\t"
        "s_mov_b32 s27, 1441792\n\t"
        "v_and_b32 v6, 0xf0, v5\n\t"
        "s_lshl_b32 s78, s70, 8\n\t"
        "v_add_u32 v62, s78, v6\n\t"
        "v_mov_b32 v7, 113860\n\t"
        "s_movk_i32 s38, 196\n\t"
        "s_mov_b32 s39, 12845056\n\t"
        "s_lshl_b32 s33, s69, 3\n\t"
        "s_lshl_b32 s78, 1, s33\n\t"
        "v_mov_b32 v29, s78\n\t"
        "s_sub_u32 s32, s78, 1\n\t"
        "s_and_b32 s32, s32, 0x01010101\n\t"
        "s_mov_b32 s35, 0xffffff\n\t"
        "s_mov_b32 s40, 0x13050109\n\t"
        "s_mov_b32 s41, 23\n\t"
        "s_cmp_eq_u32 s69, 1\n\t"
        "s_cselect_b32 s40, 0x12040008, s40\n\t"
        "s_cselect_b32 s41, 22, s41\n\t"
        "s_cmp_eq_u32 s69, 2\n\t"
        "s_cselect_b32 s40, 0x100e0602, s40\n\t"
        "s_cselect_b32 s41, 20, s41\n\t"
        "s_cmp_eq_u32 s69, 3\n\t"
        "s_cselect_b32 s40, 0x110f0703, s40\n\t"
        "s_cselect_b32 s41, 21, s41\n\t"
        "s_mul_i32 s41, s41, 0x01010101\n\t"
        "s_bitcmp1_b32 s40, 3\n\t"
        "s_cselect_b32 s78, 0, 4\n\t"
        "v_mov_b32 v61, s78\n\t"
        "v_lshrrev_b32 v12, 4, v1\n\t"
        "s_bfe_u32 s78, s40, 0x80000\n\t"
        "s_lshl_b32 s78, s78, 2\n\t"
        "v_add_u32 v13, s78, v12\n\t"
        "v_lshlrev_b32 v24, 2, v13\n\t"
        "v_lshlrev_b32 v15, 4, v13\n\t"
        "v_and_b32 v15, 0xf0, v15\n\t"
        "v_xor_b32 v15, v15, v6\n\t"
        "v_lshl_add_u32 v56, v13, 8, v15\n\t"
        "s_bfe_u32 s78, s40, 0x80008\n\t"
        "s_lshl_b32 s78, s78, 2\n\t"
        "v_add_u32 v13, s78, v12\n\t"
        "v_lshlrev_b32 v25, 2, v13\n\t"
        "v_lshlrev_b32 v15, 4, v13\n\t"
        "v_and_b32 v15, 0xf0, v15\n\t"
        "v_xor_b32 v15, v15, v6\n\t"
        "v_lshl_add_u32 v57, v13, 8, v15\n\t"
        "s_bfe_u32 s78, s40, 0x80010\n\t"
        "s_lshl_b32 s78, s78, 2\n\t"
        "v_add_u32 v13, s78, v12\n\t"
        "v_lshlrev_b32 v26, 2, v13\n\t"
        "v_lshlrev_b32 v15, 4, v13\n\t"
        "v_and_b32 v15, 0xf0, v15\n\t"
        "v_xor_b32 v15, v15, v6\n\t"
        "v_lshl_add_u32 v58, v13, 8, v15\n\t"
        "v_lshlrev_b32 v15, 9, v61\n\t"
        "v_sub_u32 v58, v58, v15\n\t"
        "s_bfe_u32 s78, s40, 0x80018\n\t"
        "s_lshl_b32 s78, s78, 2\n\t"
        "v_add_u32 v13, s78, v12\n\t"
        "v_lshlrev_b32 v27, 2, v13\n\t"
        "v_lshlrev_b32 v15, 4, v13\n\t"
        "v_and_b32 v15, 0xf0, v15\n\t"
        "v_xor_b32 v15, v15, v6\n\t"
        "v_lshl_add_u32 v59, v13, 8, v15\n\t"
        "s_bfe_u32 s78, s41, 0x80000\n\t"
        "s_lshl_b32 s78, s78, 2\n\t"
        "v_add_u32 v13, s78, v12\n\t"
        "v_lshlrev_b32 v28, 2, v13\n\t"
        "v_lshlrev_b32 v15, 4, v13\n\t"
        "v_and_b32 v15, 0xf0, v15\n\t"
        "v_xor_b32 v15, v15, v6\n\t"
        "v_lshl_add_u32 v60, v13, 8, v15\n\t"
        "v_and_b32 v20, 28, v1\n\t"
        "v_lshlrev_b32 v20, 1, v20\n\t"
        "v_lshrrev_b64 v[12:13], v20, s[40:41]\n\t"
        "v_and_b32 v13, 0xff, v12\n\t"
        "v_and_b32 v12, 3, v1\n\t"
        "v_lshl_add_u32 v13, v13, 2, v12\n\t"
        "v_lshlrev_b32 v31, 2, v13\n\t"
        "s_and_b32 s78, s67, 0x10000\n\t"
        "s_or_b32 s78, s78, 1\n\t"
        "v_mov_b32 v30, s78\n\t"
        "s_mov_b32 s36, 0\n\t"
        "v_and_b32 v0, 48, v5\n\t"
        "s_mov_b32 s78, 0x0f0e\n\t"
        "s_mov_b32 s79, 0\n\t"
        "v_lshlrev_b64 v[8:9], v0, s[78:79]\n\t"
        "s_mov_b32 s78, 0x0b0a\n\t"
        "v_lshlrev_b64 v[12:13], v0, s[78:79]\n\t"
        "s_mov_b32 s78, 0xf0f0f0f0\n\t"
        "s_mov_b32 s79, s78\n\t"
        "v_cndmask_b32 v8, v8, v12, s[78:79]\n\t"
        "v_cndmask_b32 v9, v9, v13, s[78:79]\n\t"
        "v_xor_b32 v8, 0x0c0c0c0c, v8\n\t"
        "v_xor_b32 v9, 0x0c0c0c0c, v9\n\t"
        "s_movk_i32 s48, 0x200\n\t"
        "s_mov_b32 s37, 0x01010101\n\t"
        "s_mov_b32 s49, 0x05040100\n\t"
        "s_getpc_b64 s[50:51]\n"
        ".L7_pc:\n\t"
        "s_add_u32 s52, s50, .L7_ce-.L7_pc\n\t"
        "s_addc_u32 s53, s51, 0\n\t"
        "s_add_u32 s42, s50, .L7_ev-.L7_pc\n\t"
        "s_addc_u32 s43, s51, 0\n\t"
        "s_add_u32 s50, s50, .L7_ie-.L7_pc\n\t"
        "s_addc_u32 s51, s51, 0\n\t"
        "s_cmp_eq_u32 s80, 10\n\t"
        "s_cselect_b32 s80, 0, s80\n\t"
        "s_mul_i32 s78, s80, 0x1080\n\t"
        "s_add_u32 m0, s78, s81\n\t"
        "s_lshr_b32 s78, s80, 1\n\t"
        "s_lshl1_add_u32 s78, s78, s80\n\t"
        "s_lshl_b32 s78, s78, 11\n\t"
        "buffer_load_dwordx4 v4, s[16:19], s78 offen lds\n\t"
        "s_add_u32 s80, s80, 1\n\t"
        "s_mul_i32 s78, s80, 0x1080\n\t"
        "s_add_u32 m0, s78, s81\n\t"
        "s_lshr_b32 s78, s80, 1\n\t"
        "s_lshl1_add_u32 s78, s78, s80\n\t"
        "s_lshl_b32 s78, s78, 11\n\t"
        "buffer_load_dwordx4 v4, s[16:19], s78 offen lds\n\t"
        "s_add_u32 s80, s80, 1\n\t"
        "s_mul_i32 s78, s80, 0x1080\n\t"
        "s_add_u32 m0, s78, s81\n\t"
        "s_lshr_b32 s78, s80, 1\n\t"
        "s_lshl1_add_u32 s78, s78, s80\n\t"
        "s_lshl_b32 s78, s78, 11\n\t"
        "buffer_load_dwordx4 v4, s[16:19], s78 offen lds\n\t"
        "s_add_u32 s80, s80, 1\n\t"
        "s_mul_i32 s78, s80, 0x1080\n\t"
        "s_add_u32 m0, s78, s81\n\t"
        "s_lshr_b32 s78, s80, 1\n\t"
        "s_lshl1_add_u32 s78, s78, s80\n\t"
        "s_lshl_b32 s78, s78, 11\n\t"
        "buffer_load_dwordx4 v4, s[16:19], s78 offen lds\n\t"
        "s_add_u32 s80, s80, 1\n\t"
        "s_mul_i32 s78, s80, 0x1080\n\t"
        "s_add_u32 m0, s78, s81\n\t"
        "s_lshr_b32 s78, s80, 1\n\t"
        "s_lshl1_add_u32 s78, s78, s80\n\t"
        "s_lshl_b32 s78, s78, 11\n\t"
        "buffer_load_dwordx4 v4, s[16:19], s78 offen lds\n\t"
        "s_add_u32 s80, s80, 1\n\t"
        "s_lshl2_add_u32 s78, s82, s69\n\t"
        "s_lshl_b32 s78, s78, 10\n\t"
        "s_add_u32 m0, s78, 46336\n\t"
        "s_add_u32 s82, s82, 1\n\t"
        "s_cmp_eq_u32 s82, 5\n\t"
        "buffer_load_dwordx4 v5, s[56:59], s78 offen lds\n\t"
        "s_cselect_b32 s82, 0, s82\n\t"
        "s_lshl2_add_u32 s78, s82, s69\n\t"
        "s_lshl_b32 s78, s78, 10\n\t"
        "s_add_u32 m0, s78, 46336\n\t"
        "s_add_u32 s82, s82, 1\n\t"
        "s_cmp_eq_u32 s82, 5\n\t"
        "buffer_load_dwordx4 v5, s[56:59], s78 offen lds\n\t"
        "s_cselect_b32 s82, 0, s82\n\t"
        "s_lshl2_add_u32 s78, s82, s69\n\t"
        "s_lshl_b32 s78, s78, 10\n\t"
        "s_add_u32 m0, s78, 46336\n\t"
        "s_add_u32 s82, s82, 1\n\t"
        "s_cmp_eq_u32 s82, 5\n\t"
        "buffer_load_dwordx4 v5, s[56:59], s78 offen lds\n\t"
        "s_cselect_b32 s82, 0, s82\n\t"
        "s_lshl2_add_u32 s78, s82, s69\n\t"
        "s_lshl_b32 s78, s78, 10\n\t"
        "s_add_u32 m0, s78, 46336\n\t"
        "s_add_u32 s82, s82, 1\n\t"
        "s_cmp_eq_u32 s82, 5\n\t"
        "buffer_load_dwordx4 v5, s[56:59], s78 offen lds\n\t"
        "s_cselect_b32 s82, 0, s82\n\t"
        "s_mov_b64 exec, 1\n\t"
        "ds_add_u32 v2, v3 offset:80\n\t"
        "s_mov_b64 exec, -1\n\t"
        "s_waitcnt vmcnt(4)\n\t"
        "s_mov_b64 exec, 1\n\t"
        "ds_add_u32 v2, v3 offset:0\n\t"
        "s_mov_b64 exec, -1\n"
        ".L7_w70:\n\t"
        "ds_read_b64 v[12:13], v2 offset:72\n\t"
        "s_waitcnt lgkmcnt(0)\n\t"
        "v_cmp_gt_u16 vcc, 2, v12\n\t"
        "s_cbranch_vccz .L7_d71\n\t"
        "s_sleep 1\n\t"
        "s_sub_i32 s83, s83, 1\n\t"
        "s_cmp_gt_i32 s83, 0\n\t"
        "s_cbranch_scc1 .L7_w70\n"
        ".L7_d71:\n\t"
        "v_readfirstlane_b32 s68, v13\n"
        ".L7_w72:\n\t"
        "ds_read_b32 v0, v2 offset:8\n\t"
        "s_waitcnt lgkmcnt(0)\n\t"
        "v_cmp_gt_u16 vcc, 2, v0\n\t"
        "s_cbranch_vccz .L7_d73\n\t"
        "s_sleep 1\n\t"
        "s_sub_i32 s83, s83, 1\n\t"
        "s_cmp_gt_i32 s83, 0\n\t"
        "s_cbranch_scc1 .L7_w72\n"
        ".L7_d73:\n\t"
        "ds_read_b32 v5, v31 offset:512\n\t"
        "ds_read_b32 v24, v24 offset:512\n\t"
        "ds_read_b32 v25, v25 offset:512\n\t"
        "ds_read_b32 v26, v26 offset:512\n\t"
        "ds_read_b32 v27, v27 offset:512\n\t"
        "ds_read_b32 v28, v28 offset:512\n\t"
        "s_waitcnt lgkmcnt(0)\n\t"
        "v_dot4_u32_u8 v31, v5, s38, v7\n\t"
        "s_waitcnt vmcnt(0)\n\t"
        "s_mov_b64 exec, 1\n\t"
        "ds_add_u32 v2, v3 offset:4\n\t"
        "s_mov_b64 exec, -1\n\t"
        "s_setprio 3\n\t"
        "s_lshl_b32 s44, s68, 24\n\t"
        "s_xor_b32 s45, s44, 0x5a000000\n\t"
        "s_mul_i32 s46, s68, 0x01010000\n\t"
        "s_mul_i32 s47, s68, 0x02000200\n\t"
        "s_and_b32 s47, s47, 0xff000000\n\t"
        "s_or_b32 s47, s47, 0xff0000\n"
        ".L7_w74:\n\t"
        "ds_read_b32 v0, v2 offset:92\n\t"
        "s_waitcnt lgkmcnt(0)\n\t"
        "v_cmp_gt_u16 vcc, 1, v0\n\t"
        "s_cbranch_vccz .L7_d75\n\t"
        "s_sleep 1\n\t"
        "s_sub_i32 s83, s83, 1\n\t"
        "s_cmp_gt_i32 s83, 0\n\t"
        "s_cbranch_scc1 .L7_w74\n"
        ".L7_d75:\n\t"
        "v_readfirstlane_b32 s78, v0\n\t"
        "s_sub_u32 s78, s78, 1\n\t"
        "s_add_u32 s77, s74, s78\n"
        ".L7_g76:\n\t"
        "s_memrealtime s[78:79]\n\t"
        "s_waitcnt lgkmcnt(0)\n\t"
        "s_sub_u32 s78, s78, s77\n\t"
        "s_cmp_ge_i32 s78, 315\n\t"
        "s_cbranch_scc1 .L7_h77\n\t"
        "s_sleep 1\n\t"
        "s_branch .L7_g76\n"
        ".L7_h77:\n\t"
        "s_branch .L7_ll\n"
        ".L7_again:\n\t"
        "s_waitcnt lgkmcnt(0)\n\t"
        "s_sleep 8\n\t"
        "s_sub_i32 s83, s83, 16\n\t"
        "s_cmp_gt_i32 s83, 0\n\t"
        "s_cbranch_scc0 .L7_got\n"
        ".L7_ll:\n\t"
        "buffer_load_dwordx4 v[64:67], v18, s[20:23], s24 offen sc1\n\t"
        "buffer_load_dwordx4 v[68:71], v18, s[20:23], s25 offen sc1\n\t"
        "buffer_load_dwordx4 v[72:75], v18, s[20:23], s26 offen sc1\n\t"
        "buffer_load_dwordx4 v[76:79], v18, s[20:23], s27 offen sc1\n\t"
        "s_mov_b64 exec, 1\n\t"
        "ds_add_u32 v2, v3 offset:108\n\t"
        "s_mov_b64 exec, -1\n"
        ".L7_w78:\n\t"
        "ds_read_b32 v0, v2 offset:4\n\t"
        "s_waitcnt lgkmcnt(0)\n\t"
        "v_cmp_gt_u16 vcc, 4, v0\n\t"
        "s_cbranch_vccz .L7_d79\n\t"
        "s_sleep 1\n\t"
        "s_sub_i32 s83, s83, 1\n\t"
        "s_cmp_gt_i32 s83, 0\n\t"
        "s_cbranch_scc1 .L7_w78\n"
        ".L7_d79:\n\t"
        "s_waitcnt vmcnt(3)\n\t"
        "v_xor_b32 v5, s44, v64\n\t"
        "v_bitop3_b32 v5, v65, v5, s45 bitop3:0xde\n\t"
        "v_bitop3_b32 v5, v66, v5, s46 bitop3:0xde\n\t"
        "v_xor_b32 v4, s47, v67\n\t"
        "v_lshlrev_b32_sdwa v112, 1, v64 dst_sel:DWORD dst_unused:UNUSED_PAD src0_sel:DWORD src1_sel:WORD_0\n\t"
        "ds_read_u16 v112, v112 offset:46336\n\t"
        "v_lshlrev_b32_sdwa v113, 1, v65 dst_sel:DWORD dst_unused:UNUSED_PAD src0_sel:DWORD src1_sel:WORD_0\n\t"
        "ds_read_u16 v113, v113 offset:46336\n\t"
        "v_lshlrev_b32_sdwa v114, 1, v66 dst_sel:DWORD dst_unused:UNUSED_PAD src0_sel:DWORD src1_sel:WORD_0\n\t"
        "ds_read_u16 v114, v114 offset:46336\n\t"
        "v_dot4_u32_u8 v80, v64, s39, v7\n\t"
        "v_dot4_u32_u8 v81, v65, s39, v7\n\t"
        "v_dot4_u32_u8 v82, v66, s39, v7\n\t"
        "s_waitcnt vmcnt(2)\n\t"
        "v_bitop3_b32 v5, v68, v5, s44 bitop3:0xde\n\t"
        "v_bitop3_b32 v5, v69, v5, s45 bitop3:0xde\n\t"
        "v_bitop3_b32 v5, v70, v5, s46 bitop3:0xde\n\t"
        "v_bitop3_b32 v4, v71, v4, s47 bitop3:0xde\n\t"
        "v_lshlrev_b32_sdwa v116, 1, v68 dst_sel:DWORD dst_unused:UNUSED_PAD src0_sel:DWORD src1_sel:WORD_0\n\t"
        "ds_read_u16 v116, v116 offset:46336\n\t"
        "v_lshlrev_b32_sdwa v117, 1, v69 dst_sel:DWORD dst_unused:UNUSED_PAD src0_sel:DWORD src1_sel:WORD_0\n\t"
        "ds_read_u16 v117, v117 offset:46336\n\t"
        "v_lshlrev_b32_sdwa v118, 1, v70 dst_sel:DWORD dst_unused:UNUSED_PAD src0_sel:DWORD src1_sel:WORD_0\n\t"
        "ds_read_u16 v118, v118 offset:46336\n\t"
        "v_dot4_u32_u8 v84, v68, s39, v7\n\t"
        "v_dot4_u32_u8 v85, v69, s39, v7\n\t"
        "v_dot4_u32_u8 v86, v70, s39, v7\n\t"
        "s_waitcnt vmcnt(1)\n\t"
        "v_bitop3_b32 v5, v72, v5, s44 bitop3:0xde\n\t"
        "v_bitop3_b32 v5, v73, v5, s45 bitop3:0xde\n\t"
        "v_bitop3_b32 v5, v74, v5, s46 bitop3:0xde\n\t"
        "v_bitop3_b32 v4, v75, v4, s47 bitop3:0xde\n\t"
        "v_lshlrev_b32_sdwa v120, 1, v72 dst_sel:DWORD dst_unused:UNUSED_PAD src0_sel:DWORD src1_sel:WORD_0\n\t"
        "ds_read_u16 v120, v120 offset:46336\n\t"
        "v_lshlrev_b32_sdwa v121, 1, v73 dst_sel:DWORD dst_unused:UNUSED_PAD src0_sel:DWORD src1_sel:WORD_0\n\t"
        "ds_read_u16 v121, v121 offset:46336\n\t"
        "v_lshlrev_b32_sdwa v122, 1, v74 dst_sel:DWORD dst_unused:UNUSED_PAD src0_sel:DWORD src1_sel:WORD_0\n\t"
        "ds_read_u16 v122, v122 offset:46336\n\t"
        "v_dot4_u32_u8 v88, v72, s39, v7\n\t"
        "v_dot4_u32_u8 v89, v73, s39, v7\n\t"
        "v_dot4_u32_u8 v90, v74, s39, v7\n\t"
        "s_waitcnt vmcnt(0)\n\t"
        "v_bitop3_b32 v5, v76, v5, s44 bitop3:0xde\n\t"
        "v_bitop3_b32 v5, v77, v5, s45 bitop3:0xde\n\t"
        "v_bitop3_b32 v5, v78, v5, s46 bitop3:0xde\n\t"
        "v_bitop3_b32 v4, v79, v4, s47 bitop3:0xde\n\t"
        "v_lshlrev_b32_sdwa v124, 1, v76 dst_sel:DWORD dst_unused:UNUSED_PAD src0_sel:DWORD src1_sel:WORD_0\n\t"
        "ds_read_u16 v124, v124 offset:46336\n\t"
        "v_lshlrev_b32_sdwa v125, 1, v77 dst_sel:DWORD dst_unused:UNUSED_PAD src0_sel:DWORD src1_sel:WORD_0\n\t"
        "ds_read_u16 v125, v125 offset:46336\n\t"
        "v_lshlrev_b32_sdwa v126, 1, v78 dst_sel:DWORD dst_unused:UNUSED_PAD src0_sel:DWORD src1_sel:WORD_0\n\t"
        "ds_read_u16 v126, v126 offset:46336\n\t"
        "v_dot4_u32_u8 v92, v76, s39, v7\n\t"
        "v_dot4_u32_u8 v93, v77, s39, v7\n\t"
        "v_dot4_u32_u8 v94, v78, s39, v7\n\t"
        "v_and_b32 v6, 0xfe000000, v4\n\t"
        "v_or_b32 v5, v5, v6\n\t"
        "v_cmp_lt_u32 vcc, s35, v5\n\t"
        "s_cbranch_vccnz .L7_again\n"
        ".L7_got:\n\t"
        "v_and_b32 v6, s35, v4\n\t"
        "s_waitcnt lgkmcnt(2)\n\t"
        "ds_add_rtn_u32 v96, v80, v29 offset:192\n\t"
        "ds_add_rtn_u32 v97, v81, v29 offset:192\n\t"
        "ds_add_rtn_u32 v98, v82, v29 offset:192\n\t"
        "ds_add_rtn_u32 v100, v84, v29 offset:192\n\t"
        "ds_add_rtn_u32 v101, v85, v29 offset:192\n\t"
        "ds_add_rtn_u32 v102, v86, v29 offset:192\n\t"
        "ds_add_rtn_u32 v104, v88, v29 offset:192\n\t"
        "ds_add_rtn_u32 v105, v89, v29 offset:192\n\t"
        "ds_add_rtn_u32 v106, v90, v29 offset:192\n\t"
        "ds_add_rtn_u32 v108, v92, v29 offset:192\n\t"
        "ds_add_rtn_u32 v109, v93, v29 offset:192\n\t"
        "ds_add_rtn_u32 v110, v94, v29 offset:192\n\t"
        "v_cmp_ne_u32 vcc, 0, v6\n\t"
        "s_cbranch_vccnz .L7_x\n"
        ".L7_y:\n\t"
        "s_mov_b64 exec, 1\n\t"
        "ds_add_u32 v2, v3 offset:32\n\t"
        "s_mov_b64 exec, -1\n\t"
        "v_perm_b32 v112, v112, v64, s49\n\t"
        "v_perm_b32 v113, v113, v65, s49\n\t"
        "v_perm_b32 v114, v114, v66, s49\n\t"
        "v_perm_b32 v116, v116, v68, s49\n\t"
        "v_perm_b32 v117, v117, v69, s49\n\t"
        "v_perm_b32 v118, v118, v70, s49\n\t"
        "v_perm_b32 v120, v120, v72, s49\n\t"
        "v_perm_b32 v121, v121, v73, s49\n\t"
        "v_perm_b32 v122, v122, v74, s49\n\t"
        "v_perm_b32 v124, v124, v76, s49\n\t"
        "v_cmp_lt_u32 vcc, s35, v4\n\t"
        "s_cmp_lg_u64 vcc, 0\n\t"
        "s_cselect_b32 s78, 0x100000, 0\n\t"
        "v_or_b32 v30, s78, v30\n\t"
        "s_waitcnt lgkmcnt(1)\n\t"
        "v_perm_b32 v125, v125, v77, s49\n\t"
        "v_perm_b32 v126, v126, v78, s49\n\t"
        "v_bfe_u32 v96, v96, s33, 8\n\t"
        "v_bfe_u32 v97, v97, s33, 8\n\t"
        "v_bfe_u32 v98, v98, s33, 8\n\t"
        "v_bfe_u32 v100, v100, s33, 8\n\t"
        "v_bfe_u32 v101, v101, s33, 8\n\t"
        "v_bfe_u32 v102, v102, s33, 8\n\t"
        "v_bfe_u32 v104, v104, s33, 8\n\t"
        "v_bfe_u32 v105, v105, s33, 8\n\t"
        "v_bfe_u32 v106, v106, s33, 8\n\t"
        "v_bfe_u32 v108, v108, s33, 8\n\t"
        "v_bfe_u32 v109, v109, s33, 8\n\t"
        "v_bfe_u32 v110, v110, s33, 8\n"
        ".L7_w80:\n\t"
        "ds_read_b32 v0, v2 offset:32\n\t"
        "s_waitcnt lgkmcnt(0)\n\t"
        "v_cmp_gt_u16 vcc, 4, v0\n\t"
        "s_cbranch_vccz .L7_d81\n\t"
        "s_sleep 1\n\t"
        "s_sub_i32 s83, s83, 1\n\t"
        "s_cmp_gt_i32 s83, 0\n\t"
        "s_cbranch_scc1 .L7_w80\n"
        ".L7_d81:\n\t"
        "ds_read_b32 v32, v80 offset:192\n\t"
        "ds_read_b32 v33, v81 offset:192\n\t"
        "ds_read_b32 v34, v82 offset:192\n\t"
        "ds_read_b32 v35, v84 offset:192\n\t"
        "ds_read_b32 v36, v85 offset:192\n\t"
        "ds_read_b32 v37, v86 offset:192\n\t"
        "ds_read_b32 v38, v88 offset:192\n\t"
        "ds_read_b32 v39, v89 offset:192\n\t"
        "ds_read_b32 v40, v90 offset:192\n\t"
        "ds_read_b32 v41, v92 offset:192\n\t"
        "ds_read_b32 v42, v93 offset:192\n\t"
        "ds_read_b32 v43, v94 offset:192\n\t"
        "ds_read_b32 v6, v31 offset:192\n\t"
        "s_waitcnt lgkmcnt(10)\n\t"
        "v_dot4_u32_u8 v96, v32, s32, v96\n\t"
        "v_dot4_u32_u8 v97, v33, s32, v97\n\t"
        "v_dot4_u32_u8 v98, v34, s32, v98\n\t"
        "s_waitcnt lgkmcnt(7)\n\t"
        "v_dot4_u32_u8 v100, v35, s32, v100\n\t"
        "v_dot4_u32_u8 v101, v36, s32, v101\n\t"
        "v_dot4_u32_u8 v102, v37, s32, v102\n\t"
        "s_waitcnt lgkmcnt(4)\n\t"
        "v_dot4_u32_u8 v104, v38, s32, v104\n\t"
        "v_dot4_u32_u8 v105, v39, s32, v105\n\t"
        "v_dot4_u32_u8 v106, v40, s32, v106\n\t"
        "s_waitcnt lgkmcnt(1)\n\t"
        "v_dot4_u32_u8 v108, v41, s32, v108\n\t"
        "v_dot4_u32_u8 v109, v42, s32, v109\n\t"
        "v_dot4_u32_u8 v110, v43, s32, v110\n\t"
        "v_min_u32 v96, 47, v96\n\t"
        "v_min_u32 v97, 47, v97\n\t"
        "v_min_u32 v98, 47, v98\n\t"
        "v_min_u32 v100, 47, v100\n\t"
        "v_min_u32 v101, 47, v101\n\t"
        "v_min_u32 v102, 47, v102\n\t"
        "v_min_u32 v104, 47, v104\n\t"
        "v_min_u32 v105, 47, v105\n\t"
        "v_min_u32 v106, 47, v106\n\t"
        "v_min_u32 v108, 47, v108\n\t"
        "v_min_u32 v109, 47, v109\n\t"
        "v_min_u32 v110, 47, v110\n\t"
        "v_lshl_add_u32 v96, v96, 2, v80\n\t"
        "ds_write_b32 v96, v112\n\t"
        "v_lshl_add_u32 v97, v97, 2, v81\n\t"
        "ds_write_b32 v97, v113\n\t"
        "v_lshl_add_u32 v98, v98, 2, v82\n\t"
        "ds_write_b32 v98, v114\n\t"
        "v_lshl_add_u32 v100, v100, 2, v84\n\t"
        "ds_write_b32 v100, v116\n\t"
        "v_lshl_add_u32 v101, v101, 2, v85\n\t"
        "ds_write_b32 v101, v117\n\t"
        "v_lshl_add_u32 v102, v102, 2, v86\n\t"
        "ds_write_b32 v102, v118\n\t"
        "v_lshl_add_u32 v104, v104, 2, v88\n\t"
        "ds_write_b32 v104, v120\n\t"
        "v_lshl_add_u32 v105, v105, 2, v89\n\t"
        "ds_write_b32 v105, v121\n\t"
        "v_lshl_add_u32 v106, v106, 2, v90\n\t"
        "ds_write_b32 v106, v122\n\t"
        "v_lshl_add_u32 v108, v108, 2, v92\n\t"
        "ds_write_b32 v108, v124\n\t"
        "v_lshl_add_u32 v109, v109, 2, v93\n\t"
        "ds_write_b32 v109, v125\n\t"
        "v_lshl_add_u32 v110, v110, 2, v94\n\t"
        "ds_write_b32 v110, v126\n\t"
        "s_cmp_lg_u32 s36, 0\n\t"
        "s_cbranch_scc1 .L7_z\n"
        ".L7_zr:\n\t"
        "s_mov_b64 exec, 1\n\t"
        "ds_add_u32 v2, v30 offset:36\n\t"
        "s_mov_b64 exec, -1\n\t"
        "s_waitcnt lgkmcnt(13)\n\t"
        "v_dot4_u32_u8 v6, v6, s37, -1\n\t"
        "v_mov_b64 v[32:33], 0\n\t"
        "v_mov_b64 v[34:35], 0\n\t"
        "v_mov_b64 v[36:37], 0\n\t"
        "v_med3_i32 v6, v6, 0, 47\n\t"
        "v_lshrrev_b32 v6, 1, v6\n\t"
        "v_mov_b64 v[38:39], 0\n\t"
        "v_dot4_u32_u8 v63, v24, s38, v7\n\t"
        "v_max_u32_dpp v6, v6, v6 quad_perm:[1,0,3,2] row_mask:0xf bank_mask:0xf\n\t"
        "s_nop 1\n\t"
        "v_max_u32_dpp v6, v6, v6 quad_perm:[2,3,0,1] row_mask:0xf bank_mask:0xf\n\t"
        "s_nop 0\n\t"
        "v_readlane_b32 s24, v6, 0\n\t"
        "v_readlane_b32 s25, v6, 4\n\t"
        "v_readlane_b32 s26, v6, 8\n\t"
        "v_readlane_b32 s27, v6, 12\n\t"
        "v_readlane_b32 s28, v6, 16\n\t"
        "s_max_u32 s28, s28, 5\n\t"
        "s_mov_b32 s56, s24\n\t"
        "s_mov_b32 s57, s24\n\t"
        "s_mov_b32 s78, 0xf0000\n"
        ".L7_w82:\n\t"
        "ds_read_b32 v4, v2 offset:36\n\t"
        "ds_read_b32 v12, v31 offset:188\n\t"
        "s_waitcnt lgkmcnt(1)\n\t"
        "v_cmp_gt_u16 vcc, 4, v4\n\t"
        "s_cbranch_vccz .L7_d83\n\t"
        "s_waitcnt lgkmcnt(0)\n\t"
        "s_sleep 1\n\t"
        "s_sub_i32 s83, s83, 1\n\t"
        "s_cmp_gt_i32 s83, 0\n\t"
        "s_cbranch_scc1 .L7_w82\n"
        ".L7_d83:\n\t"
        "ds_read2_b32 v[40:41], v63 offset0:0 offset1:1\n\t"
        "s_sub_u32 s56, s56, 1\n\t"
        "s_cbranch_scc1 .L7_i84\n"
        ".L7_j85:\n\t"
        "ds_read2_b32 v[42:43], v63 offset0:2 offset1:3\n\t"
        "s_sub_u32 s56, s56, 1\n\t"
        "s_cbranch_scc1 .L7_i86\n"
        ".L7_j87:\n\t"
        "ds_read2_b32 v[44:45], v63 offset0:4 offset1:5\n\t"
        "s_sub_u32 s56, s56, 1\n\t"
        "s_cbranch_scc1 .L7_i88\n"
        ".L7_j89:\n\t"
        "ds_read2_b32 v[46:47], v63 offset0:6 offset1:7\n\t"
        "s_sub_u32 s56, s56, 1\n\t"
        "s_cbranch_scc1 .L7_i90\n"
        ".L7_j91:\n\t"
        "ds_read2_b32 v[48:49], v63 offset0:8 offset1:9\n\t"
        "s_sub_u32 s56, s56, 1\n\t"
        "s_cbranch_scc1 .L7_i92\n"
        ".L7_j93:\n\t"
        "ds_read2_b32 v[50:51], v63 offset0:10 offset1:11\n\t"
        "s_sub_u32 s56, s56, 1\n\t"
        "s_cbranch_scc1 .L7_i94\n"
        ".L7_j95:\n\t"
        "ds_read2_b32 v[52:53], v63 offset0:12 offset1:13\n\t"
        "s_sub_u32 s56, s56, 1\n\t"
        "s_cbranch_scc1 .L7_i96\n"
        ".L7_j97:\n\t"
        "ds_read2_b32 v[54:55], v63 offset0:14 offset1:15\n\t"
        "s_sub_u32 s56, s56, 1\n\t"
        "s_cbranch_scc1 .L7_i98\n"
        ".L7_j99:\n\t"
        "s_waitcnt lgkmcnt(8)\n\t"
        "v_and_or_b32 v12, v4, s78, v12\n\t"
        "v_cmp_ne_u32 vcc, 0xffff, v12\n\t"
        "s_cbranch_vccnz .L7_exit4\n\t"
        "s_waitcnt lgkmcnt(7)\n\t"
        "v_mad_u32_u16 v10, v40, s48, v62\n\t"
        "v_mad_u32_u16 v11, v41, s48, v62\n\t"
        "buffer_load_dwordx4 v[64:67], v10, s[16:19], 0 offen\n\t"
        "buffer_load_dwordx4 v[68:71], v11, s[16:19], 0 offen\n\t"
        "s_waitcnt lgkmcnt(6)\n\t"
        "v_mad_u32_u16 v10, v42, s48, v62\n\t"
        "v_mad_u32_u16 v11, v43, s48, v62\n\t"
        "buffer_load_dwordx4 v[72:75], v10, s[16:19], 0 offen\n\t"
        "buffer_load_dwordx4 v[76:79], v11, s[16:19], 0 offen\n\t"
        "s_waitcnt lgkmcnt(5)\n\t"
        "v_mad_u32_u16 v10, v44, s48, v62\n\t"
        "v_mad_u32_u16 v11, v45, s48, v62\n\t"
        "buffer_load_dwordx4 v[80:83], v10, s[16:19], 0 offen\n\t"
        "buffer_load_dwordx4 v[84:87], v11, s[16:19], 0 offen\n\t"
        "s_waitcnt lgkmcnt(4)\n\t"
        "v_mad_u32_u16 v10, v46, s48, v62\n\t"
        "v_mad_u32_u16 v11, v47, s48, v62\n\t"
        "buffer_load_dwordx4 v[88:91], v10, s[16:19], 0 offen\n\t"
        "buffer_load_dwordx4 v[92:95], v11, s[16:19], 0 offen\n\t"
        "s_waitcnt lgkmcnt(3)\n\t"
        "v_mad_u32_u16 v10, v48, s48, v62\n\t"
        "v_mad_u32_u16 v11, v49, s48, v62\n\t"
        "buffer_load_dwordx4 v[96:99], v10, s[16:19], 0 offen\n\t"
        "buffer_load_dwordx4 v[100:103], v11, s[16:19], 0 offen\n\t"
        "s_waitcnt lgkmcnt(2)\n\t"
        "v_mad_u32_u16 v10, v50, s48, v62\n\t"
        "v_mad_u32_u16 v11, v51, s48, v62\n\t"
        "buffer_load_dwordx4 v[104:107], v10, s[16:19], 0 offen\n\t"
        "buffer_load_dwordx4 v[108:111], v11, s[16:19], 0 offen\n\t"
        "s_min_u32 s59, s56, s57\n\t"
        "s_add_u32 s58, s59, 1\n\t"
        "s_setprio 3\n"
        ".L7_loop:\n\t"
        "v_add_u32 v63, 64, v63\n\t"
        "v_perm_b32 v12, v41, v40, v8\n\t"
        "v_perm_b32 v13, v41, v40, v9\n\t"
        "s_waitcnt vmcnt(10) lgkmcnt(1)\n\t"
        "v_mad_u32_u16 v10, v52, s48, v62\n\t"
        "v_mad_u32_u16 v11, v53, s48, v62\n\t"
        "buffer_load_dwordx4 v[112:115], v10, s[16:19], 0 offen\n\t"
        "buffer_load_dwordx4 v[116:119], v11, s[16:19], 0 offen\n\t"
        "v_mfma_f32_4x4x4_16b_bf16 v[32:35], v[12:13], v[64:65], v[32:35] cbsz:1 abid:0\n\t"
        "v_mfma_f32_4x4x4_16b_bf16 v[36:39], v[12:13], v[66:67], v[36:39] cbsz:1 abid:0\n\t"
        "ds_read2_b32 v[40:41], v63 offset0:0 offset1:1\n\t"
        "v_mfma_f32_4x4x4_16b_bf16 v[32:35], v[12:13], v[68:69], v[32:35] cbsz:1 abid:1\n\t"
        "v_mfma_f32_4x4x4_16b_bf16 v[36:39], v[12:13], v[70:71], v[36:39] cbsz:1 abid:1\n\t"
        "s_sub_u32 s59, s59, 1\n\t"
        "s_cbranch_scc1 .L7_i100\n"
        ".L7_j101:\n\t"
        "v_perm_b32 v12, v43, v42, v8\n\t"
        "v_perm_b32 v13, v43, v42, v9\n\t"
        "s_waitcnt vmcnt(10) lgkmcnt(1)\n\t"
        "v_mad_u32_u16 v10, v54, s48, v62\n\t"
        "v_mad_u32_u16 v11, v55, s48, v62\n\t"
        "buffer_load_dwordx4 v[120:123], v10, s[16:19], 0 offen\n\t"
        "buffer_load_dwordx4 v[124:127], v11, s[16:19], 0 offen\n\t"
        "v_mfma_f32_4x4x4_16b_bf16 v[32:35], v[12:13], v[72:73], v[32:35] cbsz:1 abid:0\n\t"
        "v_mfma_f32_4x4x4_16b_bf16 v[36:39], v[12:13], v[74:75], v[36:39] cbsz:1 abid:0\n\t"
        "ds_read2_b32 v[42:43], v63 offset0:2 offset1:3\n\t"
        "v_mfma_f32_4x4x4_16b_bf16 v[32:35], v[12:13], v[76:77], v[32:35] cbsz:1 abid:1\n\t"
        "v_mfma_f32_4x4x4_16b_bf16 v[36:39], v[12:13], v[78:79], v[36:39] cbsz:1 abid:1\n\t"
        "s_sub_u32 s59, s59, 1\n\t"
        "s_cbranch_scc1 .L7_i102\n"
        ".L7_j103:\n\t"
        "v_perm_b32 v12, v45, v44, v8\n\t"
        "v_perm_b32 v13, v45, v44, v9\n\t"
        "s_waitcnt vmcnt(10) lgkmcnt(1)\n\t"
        "v_mad_u32_u16 v10, v40, s48, v62\n\t"
        "v_mad_u32_u16 v11, v41, s48, v62\n\t"
        "buffer_load_dwordx4 v[64:67], v10, s[16:19], 0 offen\n\t"
        "buffer_load_dwordx4 v[68:71], v11, s[16:19], 0 offen\n\t"
        "v_mfma_f32_4x4x4_16b_bf16 v[32:35], v[12:13], v[80:81], v[32:35] cbsz:1 abid:0\n\t"
        "v_mfma_f32_4x4x4_16b_bf16 v[36:39], v[12:13], v[82:83], v[36:39] cbsz:1 abid:0\n\t"
        "ds_read2_b32 v[44:45], v63 offset0:4 offset1:5\n\t"
        "v_mfma_f32_4x4x4_16b_bf16 v[32:35], v[12:13], v[84:85], v[32:35] cbsz:1 abid:1\n\t"
        "v_mfma_f32_4x4x4_16b_bf16 v[36:39], v[12:13], v[86:87], v[36:39] cbsz:1 abid:1\n\t"
        "s_sub_u32 s59, s59, 1\n\t"
        "s_cbranch_scc1 .L7_i104\n"
        ".L7_j105:\n\t"
        "v_perm_b32 v12, v47, v46, v8\n\t"
        "v_perm_b32 v13, v47, v46, v9\n\t"
        "s_waitcnt vmcnt(10) lgkmcnt(1)\n\t"
        "v_mad_u32_u16 v10, v42, s48, v62\n\t"
        "v_mad_u32_u16 v11, v43, s48, v62\n\t"
        "buffer_load_dwordx4 v[72:75], v10, s[16:19], 0 offen\n\t"
        "buffer_load_dwordx4 v[76:79], v11, s[16:19], 0 offen\n\t"
        "v_mfma_f32_4x4x4_16b_bf16 v[32:35], v[12:13], v[88:89], v[32:35] cbsz:1 abid:0\n\t"
        "v_mfma_f32_4x4x4_16b_bf16 v[36:39], v[12:13], v[90:91], v[36:39] cbsz:1 abid:0\n\t"
        "ds_read2_b32 v[46:47], v63 offset0:6 offset1:7\n\t"
        "v_mfma_f32_4x4x4_16b_bf16 v[32:35], v[12:13], v[92:93], v[32:35] cbsz:1 abid:1\n\t"
        "v_mfma_f32_4x4x4_16b_bf16 v[36:39], v[12:13], v[94:95], v[36:39] cbsz:1 abid:1\n\t"
        "s_sub_u32 s59, s59, 1\n\t"
        "s_cbranch_scc1 .L7_i106\n"
        ".L7_j107:\n\t"
        "v_perm_b32 v12, v49, v48, v8\n\t"
        "v_perm_b32 v13, v49, v48, v9\n\t"
        "s_waitcnt vmcnt(10) lgkmcnt(1)\n\t"
        "v_mad_u32_u16 v10, v44, s48, v62\n\t"
        "v_mad_u32_u16 v11, v45, s48, v62\n\t"
        "buffer_load_dwordx4 v[80:83], v10, s[16:19], 0 offen\n\t"
        "buffer_load_dwordx4 v[84:87], v11, s[16:19], 0 offen\n\t"
        "v_mfma_f32_4x4x4_16b_bf16 v[32:35], v[12:13], v[96:97], v[32:35] cbsz:1 abid:0\n\t"
        "v_mfma_f32_4x4x4_16b_bf16 v[36:39], v[12:13], v[98:99], v[36:39] cbsz:1 abid:0\n\t"
        "ds_read2_b32 v[48:49], v63 offset0:8 offset1:9\n\t"
        "v_mfma_f32_4x4x4_16b_bf16 v[32:35], v[12:13], v[100:101], v[32:35] cbsz:1 abid:1\n\t"
        "v_mfma_f32_4x4x4_16b_bf16 v[36:39], v[12:13], v[102:103], v[36:39] cbsz:1 abid:1\n\t"
        "s_sub_u32 s59, s59, 1\n\t"
        "s_cbranch_scc1 .L7_i108\n"
        ".L7_j109:\n\t"
        "v_perm_b32 v12, v51, v50, v8\n\t"
        "v_perm_b32 v13, v51, v50, v9\n\t"
        "s_waitcnt vmcnt(10) lgkmcnt(1)\n\t"
        "v_mad_u32_u16 v10, v46, s48, v62\n\t"
        "v_mad_u32_u16 v11, v47, s48, v62\n\t"
        "buffer_load_dwordx4 v[88:91], v10, s[16:19], 0 offen\n\t"
        "buffer_load_dwordx4 v[92:95], v11, s[16:19], 0 offen\n\t"
        "v_mfma_f32_4x4x4_16b_bf16 v[32:35], v[12:13], v[104:105], v[32:35] cbsz:1 abid:0\n\t"
        "v_mfma_f32_4x4x4_16b_bf16 v[36:39], v[12:13], v[106:107], v[36:39] cbsz:1 abid:0\n\t"
        "ds_read2_b32 v[50:51], v63 offset0:10 offset1:11\n\t"
        "v_mfma_f32_4x4x4_16b_bf16 v[32:35], v[12:13], v[108:109], v[32:35] cbsz:1 abid:1\n\t"
        "v_mfma_f32_4x4x4_16b_bf16 v[36:39], v[12:13], v[110:111], v[36:39] cbsz:1 abid:1\n\t"
        "s_sub_u32 s59, s59, 1\n\t"
        "s_cbranch_scc1 .L7_i110\n"
        ".L7_j111:\n\t"
        "v_perm_b32 v12, v53, v52, v8\n\t"
        "v_perm_b32 v13, v53, v52, v9\n\t"
        "s_waitcnt vmcnt(10) lgkmcnt(1)\n\t"
        "v_mad_u32_u16 v10, v48, s48, v62\n\t"
        "v_mad_u32_u16 v11, v49, s48, v62\n\t"
        "buffer_load_dwordx4 v[96:99], v10, s[16:19], 0 offen\n\t"
        "buffer_load_dwordx4 v[100:103], v11, s[16:19], 0 offen\n\t"
        "v_mfma_f32_4x4x4_16b_bf16 v[32:35], v[12:13], v[112:113], v[32:35] cbsz:1 abid:0\n\t"
        "v_mfma_f32_4x4x4_16b_bf16 v[36:39], v[12:13], v[114:115], v[36:39] cbsz:1 abid:0\n\t"
        "ds_read2_b32 v[52:53], v63 offset0:12 offset1:13\n\t"
        "v_mfma_f32_4x4x4_16b_bf16 v[32:35], v[12:13], v[116:117], v[32:35] cbsz:1 abid:1\n\t"
        "v_mfma_f32_4x4x4_16b_bf16 v[36:39], v[12:13], v[118:119], v[36:39] cbsz:1 abid:1\n\t"
        "s_sub_u32 s59, s59, 1\n\t"
        "s_cbranch_scc1 .L7_i112\n"
        ".L7_j113:\n\t"
        "v_perm_b32 v12, v55, v54, v8\n\t"
        "v_perm_b32 v13, v55, v54, v9\n\t"
        "s_waitcnt vmcnt(10) lgkmcnt(1)\n\t"
        "v_mad_u32_u16 v10, v50, s48, v62\n\t"
        "v_mad_u32_u16 v11, v51, s48, v62\n\t"
        "buffer_load_dwordx4 v[104:107], v10, s[16:19], 0 offen\n\t"
        "buffer_load_dwordx4 v[108:111], v11, s[16:19], 0 offen\n\t"
        "v_mfma_f32_4x4x4_16b_bf16 v[32:35], v[12:13], v[120:121], v[32:35] cbsz:1 abid:0\n\t"
        "v_mfma_f32_4x4x4_16b_bf16 v[36:39], v[12:13], v[122:123], v[36:39] cbsz:1 abid:0\n\t"
        "ds_read2_b32 v[54:55], v63 offset0:14 offset1:15\n\t"
        "v_mfma_f32_4x4x4_16b_bf16 v[32:35], v[12:13], v[124:125], v[32:35] cbsz:1 abid:1\n\t"
        "v_mfma_f32_4x4x4_16b_bf16 v[36:39], v[12:13], v[126:127], v[36:39] cbsz:1 abid:1\n\t"
        "s_sub_u32 s59, s59, 1\n\t"
        "s_cbranch_scc0 .L7_loop\n\t"
        "s_branch .L7_i114\n"
        ".L7_i84:\n\t"
        "s_mov_b32 s60, 8\n\t"
        "s_swappc_b64 s[54:55], s[50:51]\n\t"
        "s_branch .L7_j85\n"
        ".L7_i86:\n\t"
        "s_mov_b32 s60, 16\n\t"
        "s_swappc_b64 s[54:55], s[50:51]\n\t"
        "s_branch .L7_j87\n"
        ".L7_i88:\n\t"
        "s_mov_b32 s60, 24\n\t"
        "s_swappc_b64 s[54:55], s[50:51]\n\t"
        "s_branch .L7_j89\n"
        ".L7_i90:\n\t"
        "s_mov_b32 s60, 32\n\t"
        "s_swappc_b64 s[54:55], s[50:51]\n\t"
        "s_branch .L7_j91\n"
        ".L7_i92:\n\t"
        "s_mov_b32 s60, 40\n\t"
        "s_swappc_b64 s[54:55], s[50:51]\n\t"
        "s_branch .L7_j93\n"
        ".L7_i94:\n\t"
        "s_mov_b32 s60, 48\n\t"
        "s_swappc_b64 s[54:55], s[50:51]\n\t"
        "s_branch .L7_j95\n"
        ".L7_i96:\n\t"
        "s_mov_b32 s60, 56\n\t"
        "s_swappc_b64 s[54:55], s[50:51]\n\t"
        "s_branch .L7_j97\n"
        ".L7_i98:\n\t"
        "s_mov_b32 s60, 64\n\t"
        "s_swappc_b64 s[54:55], s[50:51]\n\t"
        "s_branch .L7_j99\n"
        ".L7_i100:\n\t"
        "s_mov_b32 s60, 8\n\t"
        "s_swappc_b64 s[62:63], s[42:43]\n\t"
        "s_cbranch_vccnz .L7_dt1\n\t"
        "s_branch .L7_j101\n"
        ".L7_i102:\n\t"
        "s_mov_b32 s60, 16\n\t"
        "s_swappc_b64 s[62:63], s[42:43]\n\t"
        "s_cbranch_vccnz .L7_dt2\n\t"
        "s_branch .L7_j103\n"
        ".L7_i104:\n\t"
        "s_mov_b32 s60, 24\n\t"
        "s_swappc_b64 s[62:63], s[42:43]\n\t"
        "s_cbranch_vccnz .L7_dt3\n\t"
        "s_branch .L7_j105\n"
        ".L7_i106:\n\t"
        "s_mov_b32 s60, 32\n\t"
        "s_swappc_b64 s[62:63], s[42:43]\n\t"
        "s_cbranch_vccnz .L7_dt4\n\t"
        "s_branch .L7_j107\n"
        ".L7_i108:\n\t"
        "s_mov_b32 s60, 40\n\t"
        "s_swappc_b64 s[62:63], s[42:43]\n\t"
        "s_cbranch_vccnz .L7_dt5\n\t"
        "s_branch .L7_j109\n"
        ".L7_i110:\n\t"
        "s_mov_b32 s60, 48\n\t"
        "s_swappc_b64 s[62:63], s[42:43]\n\t"
        "s_cbranch_vccnz .L7_dt6\n\t"
        "s_branch .L7_j111\n"
        ".L7_i112:\n\t"
        "s_mov_b32 s60, 56\n\t"
        "s_swappc_b64 s[62:63], s[42:43]\n\t"
        "s_cbranch_vccnz .L7_dt7\n\t"
        "s_branch .L7_j113\n"
        ".L7_i114:\n\t"
        "s_mov_b32 s60, 64\n\t"
        "s_swappc_b64 s[62:63], s[42:43]\n\t"
        "s_cbranch_vccnz .L7_dt0\n\t"
        "s_branch .L7_loop\n"
        ".L7_ev:\n\t"
        "s_sub_u32 s56, s56, s58\n\t"
        "s_cbranch_scc0 .L7_e1\n\t"
        "s_swappc_b64 s[54:55], s[50:51]\n"
        ".L7_e1:\n\t"
        "s_sub_u32 s57, s57, s58\n\t"
        "s_cbranch_scc0 .L7_e2\n\t"
        "s_swappc_b64 s[54:55], s[52:53]\n"
        ".L7_e2:\n\t"
        "s_min_u32 s59, s56, s57\n\t"
        "s_add_u32 s58, s59, 1\n\t"
        "s_setpc_b64 s[62:63]\n"
        ".L7_dt0:\n\t"
        "v_perm_b32 v12, v41, v40, v8\n\t"
        "v_perm_b32 v13, v41, v40, v9\n\t"
        "s_waitcnt vmcnt(6)\n\t"
        "s_cmp_lt_u32 s57, 3\n\t"
        "s_cbranch_scc0 .L7_du0\n\t"
        "s_waitcnt vmcnt(2)\n\t"
        "s_cmp_eq_u32 s57, 0\n\t"
        "s_cbranch_scc0 .L7_du0\n\t"
        "s_waitcnt vmcnt(0)\n"
        ".L7_du0:\n\t"
        "v_mfma_f32_4x4x4_16b_bf16 v[32:35], v[12:13], v[64:65], v[32:35] cbsz:1 abid:0\n\t"
        "v_mfma_f32_4x4x4_16b_bf16 v[36:39], v[12:13], v[66:67], v[36:39] cbsz:1 abid:0\n\t"
        "s_sub_u32 s57, s57, 1\n\t"
        "v_mfma_f32_4x4x4_16b_bf16 v[32:35], v[12:13], v[68:69], v[32:35] cbsz:1 abid:1\n\t"
        "v_mfma_f32_4x4x4_16b_bf16 v[36:39], v[12:13], v[70:71], v[36:39] cbsz:1 abid:1\n\t"
        "s_cbranch_scc1 .L7_last\n"
        ".L7_dt1:\n\t"
        "v_perm_b32 v12, v43, v42, v8\n\t"
        "v_perm_b32 v13, v43, v42, v9\n\t"
        "s_waitcnt vmcnt(6)\n\t"
        "s_cmp_lt_u32 s57, 3\n\t"
        "s_cbranch_scc0 .L7_du1\n\t"
        "s_waitcnt vmcnt(2)\n\t"
        "s_cmp_eq_u32 s57, 0\n\t"
        "s_cbranch_scc0 .L7_du1\n\t"
        "s_waitcnt vmcnt(0)\n"
        ".L7_du1:\n\t"
        "v_mfma_f32_4x4x4_16b_bf16 v[32:35], v[12:13], v[72:73], v[32:35] cbsz:1 abid:0\n\t"
        "v_mfma_f32_4x4x4_16b_bf16 v[36:39], v[12:13], v[74:75], v[36:39] cbsz:1 abid:0\n\t"
        "s_sub_u32 s57, s57, 1\n\t"
        "v_mfma_f32_4x4x4_16b_bf16 v[32:35], v[12:13], v[76:77], v[32:35] cbsz:1 abid:1\n\t"
        "v_mfma_f32_4x4x4_16b_bf16 v[36:39], v[12:13], v[78:79], v[36:39] cbsz:1 abid:1\n\t"
        "s_cbranch_scc1 .L7_last\n"
        ".L7_dt2:\n\t"
        "v_perm_b32 v12, v45, v44, v8\n\t"
        "v_perm_b32 v13, v45, v44, v9\n\t"
        "s_waitcnt vmcnt(6)\n\t"
        "s_cmp_lt_u32 s57, 3\n\t"
        "s_cbranch_scc0 .L7_du2\n\t"
        "s_waitcnt vmcnt(2)\n\t"
        "s_cmp_eq_u32 s57, 0\n\t"
        "s_cbranch_scc0 .L7_du2\n\t"
        "s_waitcnt vmcnt(0)\n"
        ".L7_du2:\n\t"
        "v_mfma_f32_4x4x4_16b_bf16 v[32:35], v[12:13], v[80:81], v[32:35] cbsz:1 abid:0\n\t"
        "v_mfma_f32_4x4x4_16b_bf16 v[36:39], v[12:13], v[82:83], v[36:39] cbsz:1 abid:0\n\t"
        "s_sub_u32 s57, s57, 1\n\t"
        "v_mfma_f32_4x4x4_16b_bf16 v[32:35], v[12:13], v[84:85], v[32:35] cbsz:1 abid:1\n\t"
        "v_mfma_f32_4x4x4_16b_bf16 v[36:39], v[12:13], v[86:87], v[36:39] cbsz:1 abid:1\n\t"
        "s_cbranch_scc1 .L7_last\n"
        ".L7_dt3:\n\t"
        "v_perm_b32 v12, v47, v46, v8\n\t"
        "v_perm_b32 v13, v47, v46, v9\n\t"
        "s_waitcnt vmcnt(6)\n\t"
        "s_cmp_lt_u32 s57, 3\n\t"
        "s_cbranch_scc0 .L7_du3\n\t"
        "s_waitcnt vmcnt(2)\n\t"
        "s_cmp_eq_u32 s57, 0\n\t"
        "s_cbranch_scc0 .L7_du3\n\t"
        "s_waitcnt vmcnt(0)\n"
        ".L7_du3:\n\t"
        "v_mfma_f32_4x4x4_16b_bf16 v[32:35], v[12:13], v[88:89], v[32:35] cbsz:1 abid:0\n\t"
        "v_mfma_f32_4x4x4_16b_bf16 v[36:39], v[12:13], v[90:91], v[36:39] cbsz:1 abid:0\n\t"
        "s_sub_u32 s57, s57, 1\n\t"
        "v_mfma_f32_4x4x4_16b_bf16 v[32:35], v[12:13], v[92:93], v[32:35] cbsz:1 abid:1\n\t"
        "v_mfma_f32_4x4x4_16b_bf16 v[36:39], v[12:13], v[94:95], v[36:39] cbsz:1 abid:1\n\t"
        "s_cbranch_scc1 .L7_last\n"
        ".L7_dt4:\n\t"
        "v_perm_b32 v12, v49, v48, v8\n\t"
        "v_perm_b32 v13, v49, v48, v9\n\t"
        "s_waitcnt vmcnt(6)\n\t"
        "s_cmp_lt_u32 s57, 3\n\t"
        "s_cbranch_scc0 .L7_du4\n\t"
        "s_waitcnt vmcnt(2)\n\t"
        "s_cmp_eq_u32 s57, 0\n\t"
        "s_cbranch_scc0 .L7_du4\n\t"
        "s_waitcnt vmcnt(0)\n"
        ".L7_du4:\n\t"
        "v_mfma_f32_4x4x4_16b_bf16 v[32:35], v[12:13], v[96:97], v[32:35] cbsz:1 abid:0\n\t"
        "v_mfma_f32_4x4x4_16b_bf16 v[36:39], v[12:13], v[98:99], v[36:39] cbsz:1 abid:0\n\t"
        "s_sub_u32 s57, s57, 1\n\t"
        "v_mfma_f32_4x4x4_16b_bf16 v[32:35], v[12:13], v[100:101], v[32:35] cbsz:1 abid:1\n\t"
        "v_mfma_f32_4x4x4_16b_bf16 v[36:39], v[12:13], v[102:103], v[36:39] cbsz:1 abid:1\n\t"
        "s_cbranch_scc1 .L7_last\n"
        ".L7_dt5:\n\t"
        "v_perm_b32 v12, v51, v50, v8\n\t"
        "v_perm_b32 v13, v51, v50, v9\n\t"
        "s_waitcnt vmcnt(6)\n\t"
        "s_cmp_lt_u32 s57, 3\n\t"
        "s_cbranch_scc0 .L7_du5\n\t"
        "s_waitcnt vmcnt(2)\n\t"
        "s_cmp_eq_u32 s57, 0\n\t"
        "s_cbranch_scc0 .L7_du5\n\t"
        "s_waitcnt vmcnt(0)\n"
        ".L7_du5:\n\t"
        "v_mfma_f32_4x4x4_16b_bf16 v[32:35], v[12:13], v[104:105], v[32:35] cbsz:1 abid:0\n\t"
        "v_mfma_f32_4x4x4_16b_bf16 v[36:39], v[12:13], v[106:107], v[36:39] cbsz:1 abid:0\n\t"
        "s_sub_u32 s57, s57, 1\n\t"
        "v_mfma_f32_4x4x4_16b_bf16 v[32:35], v[12:13], v[108:109], v[32:35] cbsz:1 abid:1\n\t"
        "v_mfma_f32_4x4x4_16b_bf16 v[36:39], v[12:13], v[110:111], v[36:39] cbsz:1 abid:1\n\t"
        "s_cbranch_scc1 .L7_last\n"
        ".L7_dt6:\n\t"
        "v_perm_b32 v12, v53, v52, v8\n\t"
        "v_perm_b32 v13, v53, v52, v9\n\t"
        "s_waitcnt vmcnt(6)\n\t"
        "s_cmp_lt_u32 s57, 3\n\t"
        "s_cbranch_scc0 .L7_du6\n\t"
        "s_waitcnt vmcnt(2)\n\t"
        "s_cmp_eq_u32 s57, 0\n\t"
        "s_cbranch_scc0 .L7_du6\n\t"
        "s_waitcnt vmcnt(0)\n"
        ".L7_du6:\n\t"
        "v_mfma_f32_4x4x4_16b_bf16 v[32:35], v[12:13], v[112:113], v[32:35] cbsz:1 abid:0\n\t"
        "v_mfma_f32_4x4x4_16b_bf16 v[36:39], v[12:13], v[114:115], v[36:39] cbsz:1 abid:0\n\t"
        "s_sub_u32 s57, s57, 1\n\t"
        "v_mfma_f32_4x4x4_16b_bf16 v[32:35], v[12:13], v[116:117], v[32:35] cbsz:1 abid:1\n\t"
        "v_mfma_f32_4x4x4_16b_bf16 v[36:39], v[12:13], v[118:119], v[36:39] cbsz:1 abid:1\n\t"
        "s_cbranch_scc1 .L7_last\n"
        ".L7_dt7:\n\t"
        "v_perm_b32 v12, v55, v54, v8\n\t"
        "v_perm_b32 v13, v55, v54, v9\n\t"
        "s_waitcnt vmcnt(6)\n\t"
        "s_cmp_lt_u32 s57, 3\n\t"
        "s_cbranch_scc0 .L7_du7\n\t"
        "s_waitcnt vmcnt(2)\n\t"
        "s_cmp_eq_u32 s57, 0\n\t"
        "s_cbranch_scc0 .L7_du7\n\t"
        "s_waitcnt vmcnt(0)\n"
        ".L7_du7:\n\t"
        "v_mfma_f32_4x4x4_16b_bf16 v[32:35], v[12:13], v[120:121], v[32:35] cbsz:1 abid:0\n\t"
        "v_mfma_f32_4x4x4_16b_bf16 v[36:39], v[12:13], v[122:123], v[36:39] cbsz:1 abid:0\n\t"
        "s_sub_u32 s57, s57, 1\n\t"
        "v_mfma_f32_4x4x4_16b_bf16 v[32:35], v[12:13], v[124:125], v[32:35] cbsz:1 abid:1\n\t"
        "v_mfma_f32_4x4x4_16b_bf16 v[36:39], v[12:13], v[126:127], v[36:39] cbsz:1 abid:1\n\t"
        "s_cbranch_scc1 .L7_last\n\t"
        "s_branch .L7_dt0\n"
        ".L7_ie:\n\t"
        "v_dot4_u32_u8 v63, v25, s38, v7\n\t"
        "s_nop 2\n\t"
        "v_subrev_u32 v63, s60, v63\n\t"
        "s_mov_b32 s56, s25\n\t"
        "s_swappc_b64 s[50:51], s[54:55]\n\t"
        "v_dot4_u32_u8 v63, v26, s38, v7\n\t"
        "s_nop 2\n\t"
        "v_subrev_u32 v63, s60, v63\n\t"
        "s_mov_b32 s56, s26\n\t"
        "s_swappc_b64 s[50:51], s[54:55]\n\t"
        "v_dot4_u32_u8 v63, v27, s38, v7\n\t"
        "s_nop 2\n\t"
        "v_subrev_u32 v63, s60, v63\n\t"
        "s_mov_b32 s56, s27\n\t"
        "s_swappc_b64 s[50:51], s[54:55]\n\t"
        "v_dot4_u32_u8 v63, v28, s38, v7\n\t"
        "s_nop 2\n\t"
        "v_subrev_u32 v63, s60, v63\n\t"
        "s_add_u32 s56, s28, 2\n\t"
        "s_swappc_b64 s[50:51], s[54:55]\n\t"
        "s_mov_b64 vcc, -1\n\t"
        "s_setpc_b64 s[54:55]\n"
        ".L7_ce:\n\t"
        "v_and_b32 v4, 0xffff0000, v24\n\t"
        "s_nop 2\n\t"
        "v_pk_mul_f32 v[32:33], v[32:33], v[4:5] op_sel_hi:[1,0]\n\t"
        "v_pk_mul_f32 v[34:35], v[34:35], v[4:5] op_sel_hi:[1,0]\n\t"
        "v_pk_mul_f32 v[36:37], v[36:37], v[4:5] op_sel_hi:[1,0]\n\t"
        "v_pk_mul_f32 v[38:39], v[38:39], v[4:5] op_sel_hi:[1,0]\n\t"
        "v_cvt_pk_bf16_f32 v12, v32, v33\n\t"
        "v_cvt_pk_bf16_f32 v13, v34, v35\n\t"
        "v_cvt_pk_bf16_f32 v14, v36, v37\n\t"
        "v_cvt_pk_bf16_f32 v15, v38, v39\n"
        ".L7_w115:\n\t"
        "ds_read_b32 v4, v2 offset:12\n\t"
        "s_waitcnt lgkmcnt(0)\n\t"
        "v_cmp_gt_u16 vcc, 4, v4\n\t"
        "s_cbranch_vccz .L7_d116\n\t"
        "s_sleep 1\n\t"
        "s_sub_i32 s83, s83, 1\n\t"
        "s_cmp_gt_i32 s83, 0\n\t"
        "s_cbranch_scc1 .L7_w115\n"
        ".L7_d116:\n\t"
        "s_mov_b64 vcc, 0\n\t"
        "ds_write_b128 v56, v[12:15] offset:23808\n\t"
        "s_mov_b64 exec, 1\n\t"
        "ds_add_u32 v2, v3 offset:52\n\t"
        "s_mov_b64 exec, -1\n\t"
        "v_mov_b64 v[32:33], 0\n\t"
        "v_mov_b64 v[34:35], 0\n\t"
        "v_mov_b64 v[36:37], 0\n\t"
        "v_mov_b64 v[38:39], 0\n\t"
        "s_mov_b32 s57, s25\n\t"
        "s_swappc_b64 s[52:53], s[54:55]\n\t"
        "v_and_b32 v4, 0xffff0000, v25\n\t"
        "s_nop 2\n\t"
        "v_pk_mul_f32 v[32:33], v[32:33], v[4:5] op_sel_hi:[1,0]\n\t"
        "v_pk_mul_f32 v[34:35], v[34:35], v[4:5] op_sel_hi:[1,0]\n\t"
        "v_pk_mul_f32 v[36:37], v[36:37], v[4:5] op_sel_hi:[1,0]\n\t"
        "v_pk_mul_f32 v[38:39], v[38:39], v[4:5] op_sel_hi:[1,0]\n\t"
        "v_cvt_pk_bf16_f32 v12, v32, v33\n\t"
        "v_cvt_pk_bf16_f32 v13, v34, v35\n\t"
        "v_cvt_pk_bf16_f32 v14, v36, v37\n\t"
        "v_cvt_pk_bf16_f32 v15, v38, v39\n\t"
        "ds_write_b128 v57, v[12:15] offset:23808\n\t"
        "s_mov_b64 exec, 1\n\t"
        "ds_add_u32 v61, v3 offset:52\n\t"
        "s_mov_b64 exec, -1\n\t"
        "v_mov_b64 v[32:33], 0\n\t"
        "v_mov_b64 v[34:35], 0\n\t"
        "v_mov_b64 v[36:37], 0\n\t"
        "v_mov_b64 v[38:39], 0\n\t"
        "s_mov_b32 s57, s26\n\t"
        "s_swappc_b64 s[52:53], s[54:55]\n\t"
        "v_and_b32 v4, 0xffff0000, v26\n\t"
        "s_nop 2\n\t"
        "v_pk_mul_f32 v[32:33], v[32:33], v[4:5] op_sel_hi:[1,0]\n\t"
        "v_pk_mul_f32 v[34:35], v[34:35], v[4:5] op_sel_hi:[1,0]\n\t"
        "v_pk_mul_f32 v[36:37], v[36:37], v[4:5] op_sel_hi:[1,0]\n\t"
        "v_pk_mul_f32 v[38:39], v[38:39], v[4:5] op_sel_hi:[1,0]\n\t"
        "v_cvt_pk_bf16_f32 v12, v32, v33\n\t"
        "v_cvt_pk_bf16_f32 v13, v34, v35\n\t"
        "v_cvt_pk_bf16_f32 v14, v36, v37\n\t"
        "v_cvt_pk_bf16_f32 v15, v38, v39\n\t"
        "ds_write_b128 v58, v[12:15] offset:23808\n\t"
        "s_mov_b64 exec, 1\n\t"
        "ds_add_u32 v61, v3 offset:56\n\t"
        "s_mov_b64 exec, -1\n\t"
        "v_mov_b64 v[32:33], 0\n\t"
        "v_mov_b64 v[34:35], 0\n\t"
        "v_mov_b64 v[36:37], 0\n\t"
        "v_mov_b64 v[38:39], 0\n\t"
        "s_mov_b32 s57, s27\n\t"
        "s_swappc_b64 s[52:53], s[54:55]\n\t"
        "v_and_b32 v4, 0xffff0000, v27\n\t"
        "s_nop 2\n\t"
        "v_pk_mul_f32 v[32:33], v[32:33], v[4:5] op_sel_hi:[1,0]\n\t"
        "v_pk_mul_f32 v[34:35], v[34:35], v[4:5] op_sel_hi:[1,0]\n\t"
        "v_pk_mul_f32 v[36:37], v[36:37], v[4:5] op_sel_hi:[1,0]\n\t"
        "v_pk_mul_f32 v[38:39], v[38:39], v[4:5] op_sel_hi:[1,0]\n\t"
        "v_cvt_pk_bf16_f32 v12, v32, v33\n\t"
        "v_cvt_pk_bf16_f32 v13, v34, v35\n\t"
        "v_cvt_pk_bf16_f32 v14, v36, v37\n\t"
        "v_cvt_pk_bf16_f32 v15, v38, v39\n\t"
        "ds_write_b128 v59, v[12:15] offset:21760\n\t"
        "s_mov_b64 exec, 1\n\t"
        "ds_add_u32 v2, v3 offset:64\n\t"
        "s_mov_b64 exec, -1\n\t"
        "v_mov_b64 v[32:33], 0\n\t"
        "v_mov_b64 v[34:35], 0\n\t"
        "v_mov_b64 v[36:37], 0\n\t"
        "v_mov_b64 v[38:39], 0\n\t"
        "s_mov_b32 s57, s28\n\t"
        "s_swappc_b64 s[52:53], s[54:55]\n"
        ".L7_last:\n\t"
        "v_and_b32 v4, 0xffff0000, v28\n\t"
        "s_nop 2\n\t"
        "v_pk_mul_f32 v[32:33], v[32:33], v[4:5] op_sel_hi:[1,0]\n\t"
        "v_pk_mul_f32 v[34:35], v[34:35], v[4:5] op_sel_hi:[1,0]\n\t"
        "v_pk_mul_f32 v[36:37], v[36:37], v[4:5] op_sel_hi:[1,0]\n\t"
        "v_pk_mul_f32 v[38:39], v[38:39], v[4:5] op_sel_hi:[1,0]\n\t"
        "v_cvt_pk_bf16_f32 v12, v32, v33\n\t"
        "v_cvt_pk_bf16_f32 v13, v34, v35\n\t"
        "v_cvt_pk_bf16_f32 v14, v36, v37\n\t"
        "v_cvt_pk_bf16_f32 v15, v38, v39\n\t"
        "ds_write_b128 v60, v[12:15] offset:21760\n\t"
        "s_mov_b64 exec, 1\n\t"
        "ds_add_u32 v2, v3 offset:68\n\t"
        "s_mov_b64 exec, -1\n\t"
        "s_endpgm\n"
        ".L7_x:\n\t"
        "s_mov_b32 s36, 1\n\t"
        "s_waitcnt lgkmcnt(0)\n\t"
        "v_dot4_u32_u8 v83, v67, s39, v7\n\t"
        "v_lshlrev_b32_sdwa v115, 1, v67 dst_sel:DWORD dst_unused:UNUSED_PAD src0_sel:DWORD src1_sel:WORD_0\n\t"
        "ds_read_u16 v115, v115 offset:46336\n\t"
        "v_cmp_eq_u16 vcc, -1, v67\n\t"
        "s_cmp_lg_u64 vcc, 0\n\t"
        "s_cselect_b32 s78, 0x10000, 0\n\t"
        "v_or_b32 v30, s78, v30\n\t"
        "ds_add_rtn_u32 v99, v83, v29 offset:192\n\t"
        "v_dot4_u32_u8 v87, v71, s39, v7\n\t"
        "v_lshlrev_b32_sdwa v119, 1, v71 dst_sel:DWORD dst_unused:UNUSED_PAD src0_sel:DWORD src1_sel:WORD_0\n\t"
        "ds_read_u16 v119, v119 offset:46336\n\t"
        "v_cmp_eq_u16 vcc, -1, v71\n\t"
        "s_cmp_lg_u64 vcc, 0\n\t"
        "s_cselect_b32 s78, 0x10000, 0\n\t"
        "v_or_b32 v30, s78, v30\n\t"
        "ds_add_rtn_u32 v103, v87, v29 offset:192\n\t"
        "v_dot4_u32_u8 v91, v75, s39, v7\n\t"
        "v_lshlrev_b32_sdwa v123, 1, v75 dst_sel:DWORD dst_unused:UNUSED_PAD src0_sel:DWORD src1_sel:WORD_0\n\t"
        "ds_read_u16 v123, v123 offset:46336\n\t"
        "v_cmp_eq_u16 vcc, -1, v75\n\t"
        "s_cmp_lg_u64 vcc, 0\n\t"
        "s_cselect_b32 s78, 0x10000, 0\n\t"
        "v_or_b32 v30, s78, v30\n\t"
        "ds_add_rtn_u32 v107, v91, v29 offset:192\n\t"
        "v_dot4_u32_u8 v95, v79, s39, v7\n\t"
        "v_lshlrev_b32_sdwa v127, 1, v79 dst_sel:DWORD dst_unused:UNUSED_PAD src0_sel:DWORD src1_sel:WORD_0\n\t"
        "ds_read_u16 v127, v127 offset:46336\n\t"
        "v_cmp_eq_u16 vcc, -1, v79\n\t"
        "s_cmp_lg_u64 vcc, 0\n\t"
        "s_cselect_b32 s78, 0x10000, 0\n\t"
        "v_or_b32 v30, s78, v30\n\t"
        "ds_add_rtn_u32 v111, v95, v29 offset:192\n\t"
        "s_branch .L7_y\n"
        ".L7_z:\n\t"
        "s_waitcnt lgkmcnt(0)\n\t"
        "ds_read_b32 v32, v83 offset:192\n\t"
        "ds_read_b32 v33, v87 offset:192\n\t"
        "ds_read_b32 v34, v91 offset:192\n\t"
        "ds_read_b32 v35, v95 offset:192\n\t"
        "v_bfe_u32 v99, v99, s33, 8\n\t"
        "v_perm_b32 v115, v115, v67, s49\n\t"
        "v_bfe_u32 v103, v103, s33, 8\n\t"
        "v_perm_b32 v119, v119, v71, s49\n\t"
        "v_bfe_u32 v107, v107, s33, 8\n\t"
        "v_perm_b32 v123, v123, v75, s49\n\t"
        "v_bfe_u32 v111, v111, s33, 8\n\t"
        "v_perm_b32 v127, v127, v79, s49\n\t"
        "s_waitcnt lgkmcnt(0)\n\t"
        "v_dot4_u32_u8 v99, v32, s32, v99\n\t"
        "v_dot4_u32_u8 v103, v33, s32, v103\n\t"
        "v_dot4_u32_u8 v107, v34, s32, v107\n\t"
        "v_dot4_u32_u8 v111, v35, s32, v111\n\t"
        "v_min_u32 v99, 47, v99\n\t"
        "v_min_u32 v103, 47, v103\n\t"
        "v_min_u32 v107, 47, v107\n\t"
        "v_min_u32 v111, 47, v111\n\t"
        "v_lshl_add_u32 v99, v99, 2, v83\n\t"
        "ds_write_b32 v99, v115\n\t"
        "v_lshl_add_u32 v103, v103, 2, v87\n\t"
        "ds_write_b32 v103, v119\n\t"
        "v_lshl_add_u32 v107, v107, 2, v91\n\t"
        "ds_write_b32 v107, v123\n\t"
        "v_lshl_add_u32 v111, v111, 2, v95\n\t"
        "ds_write_b32 v111, v127\n\t"
        "s_branch .L7_zr\n"
        ".L7_exit4:\n\t"
        "s_add_u32 s69, s69, 4\n"
        ".L7_exit:\n\t"
        "s_waitcnt vmcnt(0) lgkmcnt(0)\n\t"
        "s_setprio 0\n\t"
        : "+{v0}"(tid), "=&{s69}"(wv)
        : "s"(__builtin_amdgcn_kernarg_segment_ptr()), "s"(blockIdx.x)
        : "scc", "vcc", "memory", "s16", "s17", "s18", "s19", "s20", "s21", "s22", "s23", "s24", "s25", "s26", "s27", "s28", "s29", "s30", "s31", "s32", "s33", "s34", "s35",
          "s36", "s37", "s38", "s39", "s40", "s41", "s42", "s43", "s44", "s45", "s46", "s47", "s48", "s49", "s50", "s51", "s52", "s53", "s54", "s55",
          "s56", "s57", "s58", "s59", "s60", "s61", "s62", "s63", "s64", "s65", "s66", "s67", "s68", "s70", "s71", "s72", "s73", "s74", "s75", "s76",
          "s77", "s78", "s79", "s80", "s81", "s82", "s83", "s84", "s85", "s86", "s87", "s88", "s89", "s90", "s91", "s92", "s93", "s94", "s95", "s96",
          "s97", "s98", "s99", "s100", "s101", "v1", "v2", "v3", "v4", "v5", "v6", "v7", "v8", "v9", "v10", "v11", "v12", "v13", "v14", "v15",
          "v16", "v17", "v18", "v19", "v20", "v21", "v22", "v23", "v24", "v25", "v26", "v27", "v28", "v29", "v30", "v31", "v32", "v33", "v34", "v35",
          "v36", "v37", "v38", "v39", "v40", "v41", "v42", "v43", "v44", "v45", "v46", "v47", "v48", "v49", "v50", "v51", "v52", "v53", "v54", "v55",
          "v56", "v57", "v58", "v59", "v60", "v61", "v62", "v63", "v64", "v65", "v66", "v67", "v68", "v69", "v70", "v71", "v72", "v73", "v74", "v75",
          "v76", "v77", "v78", "v79", "v80", "v81", "v82", "v83", "v84", "v85", "v86", "v87", "v88", "v89", "v90", "v91", "v92", "v93", "v94", "v95",
          "v96", "v97", "v98", "v99", "v100", "v101", "v102", "v103", "v104", "v105", "v106", "v107", "v108", "v109", "v110", "v111", "v112", "v113", "v114", "v115",
          "v116", "v117", "v118", "v119", "v120", "v121", "v122", "v123", "v124", "v125", "v126", "v127");
    const __attribute__((address_space(4))) Args* q = (const __attribute__((address_space(4))) Args*)__builtin_amdgcn_kernarg_segment_ptr();
    asm volatile("" : "+s"(q));
    const u32 wave = __builtin_amdgcn_readfirstlane(wv);
    u32 lane;
    asm volatile("v_mbcnt_lo_u32_b32 %0, -1, 0\n\tv_mbcnt_hi_u32_b32 %0, -1, %0" : "=v"(lane));
    const u32 half = blockIdx.x & 1u, TN = q->TN, r0 = (blockIdx.x >> 1) * TN;
    const u16* x = q->xall;
    _Pragma("nounroll") for (u32 n = 0; n < (1u << 26) && *(volatile __attribute__((address_space(3))) u32*)S_QK < 4u; ++n) __builtin_amdgcn_s_sleep(2);
    if (wave >= 4u) {
        const int E = q->E, *dst = (const int*)q->dst.p, *src = (const int*)q->src.p;
        const u16* dis = q->disall;
        const u32 w = wave - 4u;
        _Pragma("nounroll") for (u32 k = 0; k < 5u; ++k) {
            const u32 grp = (u32)(((w == 0u ? 0x1713050109ull : w == 1u ? 0x1612040008ull : w == 2u ? 0x14100e0602ull : 0x15110f0703ull) >> (8u * k)) & 255u);
            const u32 row = grp * 4u + (lane >> 4), iv = *(const u32*)(smem + L_INV + row * 4u);
            const int me = (iv & 0x8000u) ? -1 : q->nbase + (int)(r0 + (iv & 255u) + TN - 255u);
            const int m0 = __builtin_amdgcn_readlane(me, 0), m1 = __builtin_amdgcn_readlane(me, 16), m2 = __builtin_amdgcn_readlane(me, 32), m3 = __builtin_amdgcn_readlane(me, 48);
            float s[8] = {0.f, 0.f, 0.f, 0.f, 0.f, 0.f, 0.f, 0.f};
            for (int e0 = 0; e0 < E; e0 += 64) {
                const int e = min(e0 + (int)lane, E - 1), d = dst[e], si = src[e];
                unsigned long long m = __ballot(e0 + (int)lane < E && (d == m0 || d == m1 || d == m2 || d == m3));
                while (m) {
                    const int l = __builtin_ctzll(m);
                    m &= m - 1;
                    const int sl = __builtin_amdgcn_readlane(si, l);
                    if (__builtin_amdgcn_readlane(d, l) == me) {
                        const u32x4 r = *(const u32x4*)(x + (size_t)sl * 256 + half * 128u + (lane & 15u) * 8u);
                        const float wt = bf2f(dis[sl]);
#pragma unroll
                        for (int j = 0; j < 4; ++j) { s[2 * j] += wt * lo(r[j]); s[2 * j + 1] += wt * hi(r[j]); }
                    }
                }
            }
            const float sc = bf2f(dis[max(me, 0)]);
            *(u32x4*)(smem + L_AL + (row < 48u ? row : row - 8u) * 256u + (((lane ^ row) & 15u) << 4)) =
                (u32x4){pack2(s[0] * sc, s[1] * sc), pack2(s[2] * sc, s[3] * sc), pack2(s[4] * sc, s[5] * sc), pack2(s[6] * sc, s[7] * sc)};
            lds_signal(S_T0 + 4u * (grp < 12u ? 1u + ((grp >> 2) & 1u) : grp >> 2), 1u);
        }
        return;
    }
    const int Ee = q->Ee, *edst = (const int*)q->edst.p, *esrc = (const int*)q->esrc.p;
    const u16 *wq = (const u16*)q->wq.p, *wk = (const u16*)q->wk.p;
    float* qs = (float*)(smem + L_QK + wave * 1024u);
    _Pragma("nounroll") for (u32 n = wave; n < TN && r0 + n < q->N; n += 4u) {
        const int me = q->nbase + (int)(r0 + n);
        float c0 = 0.f, c1 = 0.f, c2 = 0.f, c3 = 0.f;
        _Pragma("nounroll") for (int i = 0; i < 256; ++i) {
            const float xv = bf2f(x[(size_t)me * 256 + i]);
            const u32x2 r = *(const u32x2*)(wq + i * 256 + lane * 4u);
            c0 += xv * lo(r[0]); c1 += xv * hi(r[0]); c2 += xv * lo(r[1]); c3 += xv * hi(r[1]);
        }
        qs[lane * 4u] = c0; qs[lane * 4u + 1u] = c1; qs[lane * 4u + 2u] = c2; qs[lane * 4u + 3u] = c3;
        float k[4] = {0.f, 0.f, 0.f, 0.f};
        _Pragma("nounroll") for (int c = 0; c < 256; c += 2) {
            const float qa = qs[c], qb = qs[c + 1];
#pragma unroll
            for (int j = 0; j < 4; ++j) { const u32 r = *(const u32*)(wk + (lane * 4u + j) * 256u + c); k[j] += qa * lo(r) + qb * hi(r); }
        }
        float s0 = 0.f, s1 = 0.f, s2 = 0.f, s3 = 0.f, dn = 0.f;
        for (int e0 = 0; e0 < Ee; e0 += 64) {
            const int e = min(e0 + (int)lane, Ee - 1), si = esrc[e];
            unsigned long long m = __ballot(e0 + (int)lane < Ee && edst[e] == me);
            while (m) {
                const int l = __builtin_ctzll(m);
                m &= m - 1;
                const u32x2 r = *(const u32x2*)(x + (size_t)__builtin_amdgcn_readlane(si, l) * 256 + lane * 4u);
                float d = k[0] * lo(r[0]) + k[1] * hi(r[0]) + k[2] * lo(r[1]) + k[3] * hi(r[1]);
#pragma unroll
                for (int o = 1; o < 64; o <<= 1)
                    d += __uint_as_float((u32)__builtin_amdgcn_ds_bpermute((int)((lane ^ (u32)o) * 4u), (int)__float_as_uint(d)));
                const float wt = __builtin_amdgcn_exp2f(d * 0.09016844f);
                s0 += wt * lo(r[0]); s1 += wt * hi(r[0]); s2 += wt * lo(r[1]); s3 += wt * hi(r[1]);
                dn += wt;
            }
        }
        const float sc = __builtin_amdgcn_rcpf(dn + 1e-9f);
        if ((lane >> 5) == half)
            *(u32x2*)(smem + L_AA + ((u32)*(const u16*)(smem + L_MAP + n * 2u) ^ ((lane & 31u) * 8u))) =
                (u32x2){pack2(s0 * sc, s1 * sc), pack2(s2 * sc, s3 * sc)};
    }
    lds_signal(S_Z, 17u);
}

extern "C" void kernel_launch(void* const* d_in, const int* in_sizes, int n_in,
                              void* d_out, int out_size, void* d_ws, size_t ws_size,
                              hipStream_t stream) {
    const u32 N = in_sizes[5], E = in_sizes[1], Ee = in_sizes[3];
    const u16* x = (const u16*)d_in[0];
    const u16* dis = (const u16*)d_in[5];
    const u32 FL = 0x00020000u;
    const bool generic = N > NMAX || E > EMAX || (E & 3u) || (N & 1u);
    const bool irr = N > NMAX || Ee != 4u * N;
    (void)hipFuncSetAttribute((const void*)agg_kernel, hipFuncAttributeMaxDynamicSharedMemorySize, LDS_BYTES);
    for (u32 nbase = 0; nbase < N; nbase += NMAX) {
        const u32 n = N - nbase < NMAX ? N - nbase : NMAX;
        Args a;
        a.x = Desc{x + (size_t)nbase * 256, n * 512u, FL};
        a.ws = Desc{d_ws, WS_END, FL};
        a.wl = Desc{d_in[6], 131072u, FL}; a.wq = Desc{d_in[7], 131072u, FL}; a.wk = Desc{d_in[8], 131072u, FL}; a.wv = Desc{d_in[9], 131072u, FL};
        a.src = Desc{d_in[1], generic ? 0u : E * 4u, FL}; a.dst = Desc{d_in[2], generic ? 0u : E * 4u, FL};
        a.esrc = Desc{d_in[3], irr ? 0u : Ee * 4u, FL}; a.edst = Desc{d_in[4], irr ? 0u : Ee * 4u, FL};
        a.dis = Desc{dis + nbase, n * 2u, FL};
        a.out = Desc{(u16*)d_out + (size_t)nbase * 256, n * 512u, FL};
        a.N = n; a.TN = n > 256u ? (n + 127u) / 128u : 2u; a.M = ((1u << 24) + a.TN - 1u) / a.TN;
        a.irr = (irr ? 1u : 0u) | (generic ? 0x10000u : 0u);
        a.xall = x; a.disall = dis;
        a.E = (int)E; a.Ee = (int)Ee; a.nbase = (int)nbase; a.pad = 0;
        agg_kernel<<<256, 1024, LDS_BYTES, stream>>>(a);
    }
}
```

```cpp
#include <hip/hip_runtime.h>
#include <hip/hip_bf16.h>
#include <stdint.h>

typedef unsigned short u16;
typedef unsigned int u32;
typedef __attribute__((ext_vector_type(4))) u32 u32x4;
typedef __attribute__((ext_vector_type(2))) u32 u32x2;
struct Desc { const void* p; u32 bytes, flags; };
struct Args {
    Desc x, ws, wl, wv, wq, wk, src, dst, esrc, edst, dis, out;
    u32 N, TN, M, irr;
    const u16* xall;
    const u16* disall;
    int E, Ee, nbase, pad;
};
constexpr u32 NMAX = 10240, EMAX = 163840;
constexpr u32 WS_END = 9437184;
constexpr int LDS_BYTES = 163840;
constexpr u32 L_INV = 512, L_MAP = 1792, L_QK = 2048, L_AL = 23808, L_AA = 46336, S_QK = 16, S_Z = 40, S_T0 = 48;

__device__ __forceinline__ float bf2f(u32 h) { return __uint_as_float(h << 16); }
__device__ __forceinline__ float lo(u32 v) { return __uint_as_float(v << 16); }
__device__ __forceinline__ float hi(u32 v) { return __uint_as_float(v & 0xFFFF0000u); }
__device__ __forceinline__ u32 pack2(float a, float b) {
    return (u32)__builtin_bit_cast(u16, (__bf16)a) | ((u32)__builtin_bit_cast(u16, (__bf16)b) << 16);
}
__device__ __forceinline__ void lds_signal(const u32 addr, const u32 v) {
    asm volatile("s_mov_b64 exec, 1\n\tds_add_u32 %0, %1\n\ts_mov_b64 exec, -1" :: "v"(addr), "v"(v) : "memory");
}

extern "C" __global__ void __launch_bounds__(1024, 4)
agg_kernel(const Args a) {
    extern __shared__ __attribute__((aligned(16))) char smem[];
    u32 tid = threadIdx.x, wv;
    asm volatile(
        "s_memrealtime s[74:75]\n\t"
        "s_load_dwordx8 s[16:23], %2, 0\n\t"
        "s_load_dwordx4 s[64:67], %2, 192\n\t"
        "v_readfirstlane_b32 s69, v0\n\t"
        "v_and_b32 v1, 63, v0\n\t"
        "v_mov_b32 v2, 0\n\t"
        "v_mov_b32 v3, 1\n\t"
        "s_lshr_b32 s76, %3, 3\n\t"
        "s_and_b32 s70, %3, 1\n\t"
        "s_lshr_b32 s71, %3, 1\n\t"
        "s_mov_b32 s83, 0x2000000\n\t"
        "s_lshr_b32 s69, s69, 6\n\t"
        "s_cbranch_scc0 .L7_U\n\t"
        "s_cmp_lt_u32 s69, 8\n\t"
        "s_cbranch_scc1 .L7_G0\n\t"
        "s_load_dwordx4 s[48:51], %2, 128\n\t"
        "s_sub_u32 s69, s69, 8\n\t"
        "v_lshlrev_b32 v4, 2, v1\n\t"
        "s_lshl_b32 s77, s70, 2\n\t"
        "s_xor_b32 s77, s77, s69\n\t"
        "s_xor_b32 s77, s77, 4\n\t"
        "s_mul_i32 s80, s77, 10\n\t"
        "s_waitcnt lgkmcnt(0)\n\t"
        "s_mul_i32 s72, s71, s65\n\t"
        "s_add_u32 s78, s72, s80\n\t"
        "s_lshl_b32 s78, s78, 4\n\t"
        "s_mov_b32 exec_hi, 0xff\n\t"
        "buffer_load_dword v5, v4, s[48:51], s78 offen\n\t"
        "s_mov_b64 exec, -1\n\t"
        "s_barrier\n\t"
        "s_branch .L7_O2\n"
        ".L7_U:\n\t"
        "s_load_dwordx16 s[32:47], %2, 64\n\t"
        "s_load_dwordx16 s[48:63], %2, 128\n\t"
        "s_setprio 3\n\t"
        "s_getpc_b64 s[24:25]\n\t"
        "s_add_u32 s24, s24, agg_kernel@rel32@lo+4\n\t"
        "s_addc_u32 s25, s25, agg_kernel@rel32@hi+12\n\t"
        "s_and_b32 s25, s25, 0xffff\n\t"
        "s_mov_b32 s26, .L7_x-agg_kernel\n\t"
        "s_mov_b32 s27, 0x20000\n\t"
        "v_lshlrev_b32 v6, 12, v1\n\t"
        "s_lshl_b32 s78, s76, 7\n\t"
        "s_mov_b32 m0, 134400\n\t"
        "s_mov_b64 exec, 63\n\t"
        "buffer_load_dword v6, s[24:27], s78 offen lds\n\t"
        "s_mov_b64 exec, -1\n\t"
        "s_cmp_eq_u32 s70, 0\n\t"
        "s_cbranch_scc0 .L7_V\n\t"
        "v_lshrrev_b32 v4, 3, v1\n\t"
        "v_and_b32 v5, 7, v1\n\t"
        "v_lshlrev_b32 v4, 14, v4\n\t"
        "v_lshl_add_u32 v4, v5, 4, v4\n\t"
        "s_lshl_b32 s78, s71, 7\n\t"
        "v_add_u32 v4, s78, v4\n\t"
        "v_add_u32 v5, 0x20000, v4\n\t"
        "v_add_u32 v6, 0x40000, v4\n\t"
        "v_add_u32 v7, 0x60000, v4\n\t"
        "v_add_u32 v8, 0x80000, v4\n\t"
        "s_waitcnt lgkmcnt(0)\n\t"
        "s_mul_i32 s72, s71, s65\n\t"
        "buffer_load_dwordx4 v[16:19], v4, s[44:47], 0 offen\n\t"
        "buffer_load_dwordx4 v[20:23], v5, s[44:47], 0 offen\n\t"
        "buffer_load_dwordx4 v[24:27], v6, s[44:47], 0 offen\n\t"
        "buffer_load_dwordx4 v[28:31], v7, s[44:47], 0 offen\n\t"
        "buffer_load_dwordx4 v[32:35], v8, s[44:47], 0 offen\n\t"
        "s_mul_i32 s80, s71, 0x50\n\t"
        "s_lshl_b32 s78, s80, 4\n\t"
        "v_lshlrev_b32 v12, 4, v1\n\t"
        "buffer_load_dwordx4 v[116:119], v12, s[52:55], s78 offen\n\t"
        "s_mov_b64 exec, 0xffff\n\t"
        "buffer_load_dwordx4 v[120:123], v12, s[52:55], s78 offen offset:1024\n\t"
        "s_mov_b64 exec, -1\n\t"
        "buffer_load_dwordx4 v[36:39], v4, s[40:43], 0 offen\n\t"
        "buffer_load_dwordx4 v[40:43], v5, s[40:43], 0 offen\n\t"
        "buffer_load_dwordx4 v[44:47], v6, s[40:43], 0 offen\n\t"
        "buffer_load_dwordx4 v[48:51], v7, s[40:43], 0 offen\n\t"
        "buffer_load_dwordx4 v[52:55], v8, s[40:43], 0 offen\n\t"
        "s_barrier\n\t"
        "s_branch .L7_U2\n"
        ".L7_G0:\n\t"
        "s_cmp_lt_u32 s69, 4\n\t"
        "s_cbranch_scc1 .L7_L\n\t"
        "s_sub_u32 s69, s69, 4\n\t"
        "v_lshlrev_b32 v4, 4, v1\n\t"
        "v_and_b32 v5, 0x200, v4\n\t"
        "v_mad_u32_u24 v4, v5, 7, v4\n\t"
        "s_mul_i32 s80, s70, 5\n\t"
        "s_mul_i32 s81, s69, 0x420\n\t"
        "s_add_u32 s81, s81, 2048\n\t"
        "s_mul_i32 s78, s80, 0x1080\n\t"
        "s_add_u32 m0, s78, s81\n\t"
        "s_lshr_b32 s78, s80, 1\n\t"
        "s_lshl1_add_u32 s78, s78, s80\n\t"
        "s_lshl_b32 s78, s78, 11\n\t"
        "s_waitcnt lgkmcnt(0)\n\t"
        "s_mul_i32 s72, s71, s65\n\t"
        "s_add_u32 s79, s72, s69\n\t"
        "s_lshl_b32 s79, s79, 9\n\t"
        "v_add_u32 v4, s79, v4\n\t"
        "s_sleep 3\n\t"
        "s_cmp_eq_u32 s70, 0\n\t"
        "s_cbranch_scc1 .L7_gs\n\t"
        "s_sleep 4\n"
        ".L7_gs:\n\t"
        "buffer_load_dwordx4 v4, s[16:19], s78 offen lds\n\t"
        "s_add_u32 s80, s80, 1\n\t"
        "s_load_dwordx4 s[56:59], %2, 160\n\t"
        "s_mul_i32 s78, s80, 0x1080\n\t"
        "s_add_u32 m0, s78, s81\n\t"
        "s_lshr_b32 s78, s80, 1\n\t"
        "s_lshl1_add_u32 s78, s78, s80\n\t"
        "s_lshl_b32 s78, s78, 11\n\t"
        "buffer_load_dwordx4 v4, s[16:19], s78 offen lds\n\t"
        "s_add_u32 s80, s80, 1\n\t"
        "s_mul_i32 s78, s80, 0x1080\n\t"
        "s_add_u32 m0, s78, s81\n\t"
        "s_lshr_b32 s78, s80, 1\n\t"
        "s_lshl1_add_u32 s78, s78, s80\n\t"
        "s_lshl_b32 s78, s78, 11\n\t"
        "buffer_load_dwordx4 v4, s[16:19], s78 offen lds\n\t"
        "s_add_u32 s80, s80, 1\n\t"
        "s_mul_i32 s78, s80, 0x1080\n\t"
        "s_add_u32 m0, s78, s81\n\t"
        "s_lshr_b32 s78, s80, 1\n\t"
        "s_lshl1_add_u32 s78, s78, s80\n\t"
        "s_lshl_b32 s78, s78, 11\n\t"
        "buffer_load_dwordx4 v4, s[16:19], s78 offen lds\n\t"
        "s_add_u32 s80, s80, 1\n\t"
        "s_mul_i32 s78, s80, 0x1080\n\t"
        "s_add_u32 m0, s78, s81\n\t"
        "s_lshr_b32 s78, s80, 1\n\t"
        "s_lshl1_add_u32 s78, s78, s80\n\t"
        "s_lshl_b32 s78, s78, 11\n\t"
        "buffer_load_dwordx4 v4, s[16:19], s78 offen lds\n\t"
        "s_add_u32 s80, s80, 1\n\t"
        "s_barrier\n\t"
        "s_branch .L7_G2\n"
        ".L7_L:\n\t"
        "s_cmp_eq_u32 s69, 2\n\t"
        "s_cbranch_scc0 .L7_L1\n\t"
        "s_waitcnt lgkmcnt(0)\n\t"
        "s_mul_i32 s72, s71, s65\n\t"
        "s_buffer_load_dword s68, s[20:23], 0\n\t"
        "s_barrier\n\t"
        "s_branch .L7_P2\n"
        ".L7_L1:\n\t"
        "s_cmp_eq_u32 s69, 1\n\t"
        "s_cbranch_scc0 .L7_T\n\t"
        "s_load_dwordx4 s[56:59], %2, 160\n\t"
        "v_lshlrev_b32 v4, 3, v1\n\t"
        "v_mov_b64 v[8:9], 0\n\t"
        "ds_write_b64 v4, v[8:9]\n\t"
        "s_waitcnt lgkmcnt(0)\n\t"
        "s_mul_i32 s72, s71, s65\n\t"
        "v_add_lshl_u32 v4, v1, s72, 1\n\t"
        "buffer_load_ushort v16, v4, s[56:59], 0 offen\n\t"
        "buffer_load_ushort v17, v4, s[56:59], 0 offen offset:128\n\t"
        "s_barrier\n\t"
        "s_branch .L7_R2\n"
        ".L7_T:\n\t"
        "s_load_dwordx8 s[24:31], %2, 32\n\t"
        "v_lshlrev_b32 v6, 7, v1\n\t"
        "s_mov_b32 m0, 134400\n\t"
        "s_lshl_b32 s78, s70, 16\n\t"
        "s_lshl_b32 s79, s76, 11\n\t"
        "s_add_u32 s80, s78, s79\n\t"
        "s_mul_i32 s81, s76, 0x300\n\t"
        "s_lshl_b32 s82, %3, 7\n\t"
        "s_add_u32 s82, s82, 4096\n\t"
        "v_lshlrev_b32 v8, 2, v1\n\t"
        "s_waitcnt lgkmcnt(0)\n\t"
        "s_mul_i32 s72, s71, s65\n\t"
        "v_mov_b32 v7, s74\n\t"
        "s_mov_b32 exec_hi, 0\n\t"
        "buffer_store_dword v7, v8, s[20:23], s82 offen sc1\n\t"
        "s_barrier\n\t"
        "s_mov_b64 exec, -1\n\t"
        "s_mov_b64 exec, 0xffff\n\t"
        "buffer_load_dword v6, s[28:31], s80 offen lds\n\t"
        "buffer_load_dword v6, s[24:27], s80 offen lds\n\t"
        "s_mov_b64 exec, -1\n\t"
        "s_branch .L7_T2\n"
        ".L7_V:\n\t"
        "s_and_b32 s78, s71, 1\n\t"
        "s_and_b32 s79, s76, 3\n\t"
        "s_lshl2_add_u32 s80, s78, s79\n\t"
        "s_and_b32 s78, s71, 2\n\t"
        "s_lshr_b32 s79, s76, 2\n\t"
        "s_lshl2_add_u32 s81, s78, s79\n\t"
        "v_lshlrev_b32 v12, 4, v1\n\t"
        "v_and_b32 v5, 0x200, v12\n\t"
        "v_mad_u32_u24 v4, v5, 15, v12\n\t"
        "s_lshl_b32 s82, s80, 14\n\t"
        "s_lshr_b32 s78, s81, 1\n\t"
        "s_lshl_b32 s78, s78, 14\n\t"
        "s_and_b32 s79, s81, 1\n\t"
        "s_lshl_b32 s79, s79, 11\n\t"
        "s_add_u32 s77, s78, s79\n\t"
        "s_mov_b32 m0, 68864\n\t"
        "s_waitcnt lgkmcnt(0)\n\t"
        "s_mul_i32 s72, s71, s65\n\t"
        "buffer_load_dwordx4 v4, s[36:39], s77 offen offset:0 lds\n\t"
        "s_add_u32 m0, m0, 544\n\t"
        "s_nop 0\n\t"
        "buffer_load_dwordx4 v4, s[36:39], s77 offen offset:512 lds\n\t"
        "s_add_u32 m0, m0, 544\n\t"
        "s_nop 0\n\t"
        "buffer_load_dwordx4 v4, s[36:39], s77 offen offset:1024 lds\n\t"
        "s_add_u32 m0, m0, 544\n\t"
        "s_nop 0\n\t"
        "buffer_load_dwordx4 v4, s[36:39], s77 offen offset:1536 lds\n\t"
        "s_add_u32 m0, m0, 2592\n\t"
        "s_add_u32 s77, s77, 0x1000\n\t"
        "buffer_load_dwordx4 v4, s[36:39], s77 offen offset:0 lds\n\t"
        "s_add_u32 m0, m0, 544\n\t"
        "s_nop 0\n\t"
        "buffer_load_dwordx4 v4, s[36:39], s77 offen offset:512 lds\n\t"
        "s_add_u32 m0, m0, 544\n\t"
        "s_nop 0\n\t"
        "buffer_load_dwordx4 v4, s[36:39], s77 offen offset:1024 lds\n\t"
        "s_add_u32 m0, m0, 544\n\t"
        "s_nop 0\n\t"
        "buffer_load_dwordx4 v4, s[36:39], s77 offen offset:1536 lds\n\t"
        "s_add_u32 m0, m0, 2592\n\t"
        "s_nop 0\n\t"
        "buffer_load_dwordx4 v4, s[32:35], s82 offen offset:0 lds\n\t"
        "s_add_u32 m0, m0, 544\n\t"
        "s_nop 0\n\t"
        "buffer_load_dwordx4 v4, s[32:35], s82 offen offset:512 lds\n\t"
        "s_add_u32 m0, m0, 544\n\t"
        "s_nop 0\n\t"
        "buffer_load_dwordx4 v4, s[32:35], s82 offen offset:1024 lds\n\t"
        "s_add_u32 m0, m0, 544\n\t"
        "s_nop 0\n\t"
        "buffer_load_dwordx4 v4, s[32:35], s82 offen offset:1536 lds\n\t"
        "s_add_u32 m0, m0, 2592\n\t"
        "s_add_u32 s82, s82, 0x1000\n\t"
        "buffer_load_dwordx4 v4, s[32:35], s82 offen offset:0 lds\n\t"
        "s_add_u32 m0, m0, 544\n\t"
        "s_nop 0\n\t"
        "buffer_load_dwordx4 v4, s[32:35], s82 offen offset:512 lds\n\t"
        "s_add_u32 m0, m0, 544\n\t"
        "s_nop 0\n\t"
        "buffer_load_dwordx4 v4, s[32:35], s82 offen offset:1024 lds\n\t"
        "s_add_u32 m0, m0, 544\n\t"
        "s_nop 0\n\t"
        "buffer_load_dwordx4 v4, s[32:35], s82 offen offset:1536 lds\n\t"
        "s_add_u32 m0, m0, 2592\n\t"
        "s_sub_u32 s82, s82, 0x800\n\t"
        "buffer_load_dwordx4 v4, s[32:35], s82 offen offset:0 lds\n\t"
        "s_add_u32 m0, m0, 544\n\t"
        "s_nop 0\n\t"
        "buffer_load_dwordx4 v4, s[32:35], s82 offen offset:512 lds\n\t"
        "s_add_u32 m0, m0, 544\n\t"
        "s_nop 0\n\t"
        "buffer_load_dwordx4 v4, s[32:35], s82 offen offset:1024 lds\n\t"
        "s_add_u32 m0, m0, 544\n\t"
        "s_nop 0\n\t"
        "buffer_load_dwordx4 v4, s[32:35], s82 offen offset:1536 lds\n\t"
        "s_add_u32 m0, m0, 2592\n\t"
        "s_add_u32 s82, s82, 0x1000\n\t"
        "buffer_load_dwordx4 v4, s[32:35], s82 offen offset:0 lds\n\t"
        "s_add_u32 m0, m0, 544\n\t"
        "s_nop 0\n\t"
        "buffer_load_dwordx4 v4, s[32:35], s82 offen offset:512 lds\n\t"
        "s_add_u32 m0, m0, 544\n\t"
        "s_nop 0\n\t"
        "buffer_load_dwordx4 v4, s[32:35], s82 offen offset:1024 lds\n\t"
        "s_add_u32 m0, m0, 544\n\t"
        "s_nop 0\n\t"
        "buffer_load_dwordx4 v4, s[32:35], s82 offen offset:1536 lds\n\t"
        "s_barrier\n\t"
        "s_branch .L7_V2\n"
        ".L7_U2:\n\t"
        "v_cmp_gt_u32 s[24:25], s46, v4\n\t"
        "v_cmp_gt_u32 s[26:27], s46, v5\n\t"
        "v_cmp_gt_u32 s[28:29], s46, v6\n\t"
        "v_cmp_gt_u32 s[30:31], s46, v7\n\t"
        "v_cmp_gt_u32 s[32:33], s46, v8\n\t"
        "v_mul_u32_u24 v112, 0xa0, v1\n\t"
        "v_add_u32 v112, 68864, v112\n\t"
        "v_add_u32 v113, 10240, v112\n\t"
        "v_lshlrev_b32 v7, 2, v1\n\t"
        "ds_write_b32 v7, v112 offset:1028\n\t"
        "ds_write_b32 v7, v113 offset:1284\n\t"
        "v_lshrrev_b32 v15, 3, v1\n\t"
        "v_mul_u32_u24 v15, 0xa0, v15\n\t"
        "v_add_u32 v15, 68864, v15\n\t"
        "v_and_b32 v4, 7, v1\n\t"
        "v_lshl_add_u32 v15, v4, 2, v15\n\t"
        "v_mov_b32 v13, 0x1000000\n\t"
        "v_mov_b32 v14, 4\n\t"
        "s_lshl_b32 s54, s65, 14\n\t"
        "s_sub_u32 s54, 0, s54\n\t"
        "s_lshl_b32 s48, s71, 14\n\t"
        "s_add_u32 s48, s48, 1048576\n\t"
        "s_add_u32 s49, s48, 4096\n\t"
        "v_add_u32 v97, 1280, v15\n\t"
        "v_add_u32 v98, 2560, v15\n\t"
        "v_add_u32 v99, 3840, v15\n\t"
        "v_add_u32 v100, 5120, v15\n\t"
        "v_add_u32 v101, 6400, v15\n\t"
        "v_add_u32 v102, 7680, v15\n\t"
        "v_add_u32 v103, 8960, v15\n\t"
        "v_add_u32 v112, 0x80, v112\n\t"
        "v_add_u32 v113, 0x80, v113\n\t"
        "v_add_u32 v114, s80, v1\n\t"
        "v_add_u32 v115, 64, v114\n\t"
        "v_cmp_gt_u32 s[36:37], s64, v114\n\t"
        "v_cmp_gt_u32 s[38:39], s64, v115\n\t"
        "s_waitcnt vmcnt(11) lgkmcnt(8)\n\t"
        "s_mov_b64 exec, s[24:25]\n\t"
        "v_mad_u32_u24 v56, v16, s66, v13\n\t"
        "v_mad_u32_u24 v57, v17, s66, v13\n\t"
        "v_mad_u32_u24 v58, v18, s66, v13\n\t"
        "v_mad_u32_u24 v59, v19, s66, v13\n\t"
        "v_lshlrev_b32_sdwa v56, 2, v56 dst_sel:DWORD dst_unused:UNUSED_PAD src0_sel:DWORD src1_sel:BYTE_3\n\t"
        "ds_add_rtn_u32 v76, v56, v14 offset:1024\n\t"
        "v_lshlrev_b32_sdwa v57, 2, v57 dst_sel:DWORD dst_unused:UNUSED_PAD src0_sel:DWORD src1_sel:BYTE_3\n\t"
        "ds_add_rtn_u32 v77, v57, v14 offset:1024\n\t"
        "v_lshlrev_b32_sdwa v58, 2, v58 dst_sel:DWORD dst_unused:UNUSED_PAD src0_sel:DWORD src1_sel:BYTE_3\n\t"
        "ds_add_rtn_u32 v78, v58, v14 offset:1024\n\t"
        "v_lshlrev_b32_sdwa v59, 2, v59 dst_sel:DWORD dst_unused:UNUSED_PAD src0_sel:DWORD src1_sel:BYTE_3\n\t"
        "ds_add_rtn_u32 v79, v59, v14 offset:1024\n\t"
        "s_waitcnt vmcnt(10) lgkmcnt(8)\n\t"
        "s_mov_b64 exec, s[26:27]\n\t"
        "v_mad_u32_u24 v60, v20, s66, v13\n\t"
        "v_mad_u32_u24 v61, v21, s66, v13\n\t"
        "v_mad_u32_u24 v62, v22, s66, v13\n\t"
        "v_mad_u32_u24 v63, v23, s66, v13\n\t"
        "v_lshlrev_b32_sdwa v60, 2, v60 dst_sel:DWORD dst_unused:UNUSED_PAD src0_sel:DWORD src1_sel:BYTE_3\n\t"
        "ds_add_rtn_u32 v80, v60, v14 offset:1024\n\t"
        "v_lshlrev_b32_sdwa v61, 2, v61 dst_sel:DWORD dst_unused:UNUSED_PAD src0_sel:DWORD src1_sel:BYTE_3\n\t"
        "ds_add_rtn_u32 v81, v61, v14 offset:1024\n\t"
        "v_lshlrev_b32_sdwa v62, 2, v62 dst_sel:DWORD dst_unused:UNUSED_PAD src0_sel:DWORD src1_sel:BYTE_3\n\t"
        "ds_add_rtn_u32 v82, v62, v14 offset:1024\n\t"
        "v_lshlrev_b32_sdwa v63, 2, v63 dst_sel:DWORD dst_unused:UNUSED_PAD src0_sel:DWORD src1_sel:BYTE_3\n\t"
        "ds_add_rtn_u32 v83, v63, v14 offset:1024\n\t"
        "s_waitcnt vmcnt(9) lgkmcnt(8)\n\t"
        "s_mov_b64 exec, s[28:29]\n\t"
        "v_mad_u32_u24 v64, v24, s66, v13\n\t"
        "v_mad_u32_u24 v65, v25, s66, v13\n\t"
        "v_mad_u32_u24 v66, v26, s66, v13\n\t"
        "v_mad_u32_u24 v67, v27, s66, v13\n\t"
        "v_lshlrev_b32_sdwa v64, 2, v64 dst_sel:DWORD dst_unused:UNUSED_PAD src0_sel:DWORD src1_sel:BYTE_3\n\t"
        "ds_add_rtn_u32 v84, v64, v14 offset:1024\n\t"
        "v_lshlrev_b32_sdwa v65, 2, v65 dst_sel:DWORD dst_unused:UNUSED_PAD src0_sel:DWORD src1_sel:BYTE_3\n\t"
        "ds_add_rtn_u32 v85, v65, v14 offset:1024\n\t"
        "v_lshlrev_b32_sdwa v66, 2, v66 dst_sel:DWORD dst_unused:UNUSED_PAD src0_sel:DWORD src1_sel:BYTE_3\n\t"
        "ds_add_rtn_u32 v86, v66, v14 offset:1024\n\t"
        "v_lshlrev_b32_sdwa v67, 2, v67 dst_sel:DWORD dst_unused:UNUSED_PAD src0_sel:DWORD src1_sel:BYTE_3\n\t"
        "ds_add_rtn_u32 v87, v67, v14 offset:1024\n\t"
        "s_waitcnt vmcnt(8) lgkmcnt(8)\n\t"
        "s_mov_b64 exec, s[30:31]\n\t"
        "v_mad_u32_u24 v68, v28, s66, v13\n\t"
        "v_mad_u32_u24 v69, v29, s66, v13\n\t"
        "v_mad_u32_u24 v70, v30, s66, v13\n\t"
        "v_mad_u32_u24 v71, v31, s66, v13\n\t"
        "v_lshlrev_b32_sdwa v68, 2, v68 dst_sel:DWORD dst_unused:UNUSED_PAD src0_sel:DWORD src1_sel:BYTE_3\n\t"
        "ds_add_rtn_u32 v88, v68, v14 offset:1024\n\t"
        "v_lshlrev_b32_sdwa v69, 2, v69 dst_sel:DWORD dst_unused:UNUSED_PAD src0_sel:DWORD src1_sel:BYTE_3\n\t"
        "ds_add_rtn_u32 v89, v69, v14 offset:1024\n\t"
        "v_lshlrev_b32_sdwa v70, 2, v70 dst_sel:DWORD dst_unused:UNUSED_PAD src0_sel:DWORD src1_sel:BYTE_3\n\t"
        "ds_add_rtn_u32 v90, v70, v14 offset:1024\n\t"
        "v_lshlrev_b32_sdwa v71, 2, v71 dst_sel:DWORD dst_unused:UNUSED_PAD src0_sel:DWORD src1_sel:BYTE_3\n\t"
        "ds_add_rtn_u32 v91, v71, v14 offset:1024\n\t"
        "s_waitcnt vmcnt(7) lgkmcnt(8)\n\t"
        "s_mov_b64 exec, s[32:33]\n\t"
        "v_mad_u32_u24 v72, v32, s66, v13\n\t"
        "v_mad_u32_u24 v73, v33, s66, v13\n\t"
        "v_mad_u32_u24 v74, v34, s66, v13\n\t"
        "v_mad_u32_u24 v75, v35, s66, v13\n\t"
        "v_lshlrev_b32_sdwa v72, 2, v72 dst_sel:DWORD dst_unused:UNUSED_PAD src0_sel:DWORD src1_sel:BYTE_3\n\t"
        "ds_add_rtn_u32 v92, v72, v14 offset:1024\n\t"
        "v_lshlrev_b32_sdwa v73, 2, v73 dst_sel:DWORD dst_unused:UNUSED_PAD src0_sel:DWORD src1_sel:BYTE_3\n\t"
        "ds_add_rtn_u32 v93, v73, v14 offset:1024\n\t"
        "v_lshlrev_b32_sdwa v74, 2, v74 dst_sel:DWORD dst_unused:UNUSED_PAD src0_sel:DWORD src1_sel:BYTE_3\n\t"
        "ds_add_rtn_u32 v94, v74, v14 offset:1024\n\t"
        "v_lshlrev_b32_sdwa v75, 2, v75 dst_sel:DWORD dst_unused:UNUSED_PAD src0_sel:DWORD src1_sel:BYTE_3\n\t"
        "ds_add_rtn_u32 v95, v75, v14 offset:1024\n\t"
        "s_mov_b64 exec, -1\n\t"
        "ds_read_b32 v4, v7 offset:1028\n\t"
        "ds_read_b32 v5, v7 offset:1284\n\t"
        "s_waitcnt vmcnt(5)\n\t"
        "v_xor_b32 v116, v116, v114\n\t"
        "v_bitop3_b32 v116, v117, v116, v114 bitop3:0xde\n\t"
        "v_bitop3_b32 v116, v118, v116, v114 bitop3:0xde\n\t"
        "v_bitop3_b32 v116, v119, v116, v114 bitop3:0xde\n\t"
        "v_xor_b32 v120, v120, v115\n\t"
        "v_bitop3_b32 v120, v121, v120, v115 bitop3:0xde\n\t"
        "v_bitop3_b32 v120, v122, v120, v115 bitop3:0xde\n\t"
        "v_bitop3_b32 v120, v123, v120, v115 bitop3:0xde\n\t"
        "v_cmp_ne_u32 s[40:41], 0, v116\n\t"
        "v_cmp_ne_u32 s[42:43], 0, v120\n\t"
        "s_and_b64 s[38:39], s[38:39], 0xffff\n\t"
        "s_and_b64 s[36:37], s[36:37], s[40:41]\n\t"
        "s_and_b64 s[38:39], s[38:39], s[42:43]\n\t"
        "s_or_b64 s[36:37], s[36:37], s[38:39]\n\t"
        "s_cselect_b32 s67, 1, s67\n"
        ".L7_w38:\n\t"
        "ds_read_b32 v0, v2 offset:104\n\t"
        "ds_read_b32 v6, v2 offset:76\n\t"
        "s_waitcnt lgkmcnt(1)\n\t"
        "v_cmp_gt_u16 vcc, 1, v0\n\t"
        "s_cbranch_vccz .L7_d39\n\t"
        "s_waitcnt lgkmcnt(0)\n\t"
        "s_sleep 0\n\t"
        "s_sub_i32 s83, s83, 1\n\t"
        "s_cmp_gt_i32 s83, 0\n\t"
        "s_cbranch_scc1 .L7_w38\n"
        ".L7_d39:\n\t"
        "v_cmp_gt_u32 s[34:35], v4, v112\n\t"
        "v_cmp_gt_u32 s[36:37], v5, v113\n\t"
        "s_or_b64 s[38:39], s[34:35], s[36:37]\n\t"
        "s_cbranch_scc1 .L7_ov\n"
        ".L7_ov1:\n\t"
        "s_waitcnt lgkmcnt(0)\n\t"
        "v_readfirstlane_b32 s68, v6\n\t"
        "s_waitcnt vmcnt(4) lgkmcnt(8)\n\t"
        "s_mov_b64 exec, s[24:25]\n\t"
        "v_lshl_add_u32 v16, v16, 16, v36\n\t"
        "v_lshl_add_u32 v17, v17, 16, v37\n\t"
        "v_lshl_add_u32 v18, v18, 16, v38\n\t"
        "v_lshl_add_u32 v19, v19, 16, v39\n\t"
        "v_mad_i32_i24 v16, v56, s54, v16\n\t"
        "ds_add_u32 v76, v16\n\t"
        "v_mad_i32_i24 v17, v57, s54, v17\n\t"
        "ds_add_u32 v77, v17\n\t"
        "v_mad_i32_i24 v18, v58, s54, v18\n\t"
        "ds_add_u32 v78, v18\n\t"
        "v_mad_i32_i24 v19, v59, s54, v19\n\t"
        "ds_add_u32 v79, v19\n\t"
        "s_waitcnt vmcnt(3) lgkmcnt(8)\n\t"
        "s_mov_b64 exec, s[26:27]\n\t"
        "v_lshl_add_u32 v20, v20, 16, v40\n\t"
        "v_lshl_add_u32 v21, v21, 16, v41\n\t"
        "v_lshl_add_u32 v22, v22, 16, v42\n\t"
        "v_lshl_add_u32 v23, v23, 16, v43\n\t"
        "v_mad_i32_i24 v20, v60, s54, v20\n\t"
        "ds_add_u32 v80, v20\n\t"
        "v_mad_i32_i24 v21, v61, s54, v21\n\t"
        "ds_add_u32 v81, v21\n\t"
        "v_mad_i32_i24 v22, v62, s54, v22\n\t"
        "ds_add_u32 v82, v22\n\t"
        "v_mad_i32_i24 v23, v63, s54, v23\n\t"
        "ds_add_u32 v83, v23\n\t"
        "s_waitcnt vmcnt(2) lgkmcnt(8)\n\t"
        "s_mov_b64 exec, s[28:29]\n\t"
        "v_lshl_add_u32 v24, v24, 16, v44\n\t"
        "v_lshl_add_u32 v25, v25, 16, v45\n\t"
        "v_lshl_add_u32 v26, v26, 16, v46\n\t"
        "v_lshl_add_u32 v27, v27, 16, v47\n\t"
        "v_mad_i32_i24 v24, v64, s54, v24\n\t"
        "ds_add_u32 v84, v24\n\t"
        "v_mad_i32_i24 v25, v65, s54, v25\n\t"
        "ds_add_u32 v85, v25\n\t"
        "v_mad_i32_i24 v26, v66, s54, v26\n\t"
        "ds_add_u32 v86, v26\n\t"
        "v_mad_i32_i24 v27, v67, s54, v27\n\t"
        "ds_add_u32 v87, v27\n\t"
        "s_waitcnt vmcnt(1) lgkmcnt(8)\n\t"
        "s_mov_b64 exec, s[30:31]\n\t"
        "v_lshl_add_u32 v28, v28, 16, v48\n\t"
        "v_lshl_add_u32 v29, v29, 16, v49\n\t"
        "v_lshl_add_u32 v30, v30, 16, v50\n\t"
        "v_lshl_add_u32 v31, v31, 16, v51\n\t"
        "v_mad_i32_i24 v28, v68, s54, v28\n\t"
        "ds_add_u32 v88, v28\n\t"
        "v_mad_i32_i24 v29, v69, s54, v29\n\t"
        "ds_add_u32 v89, v29\n\t"
        "v_mad_i32_i24 v30, v70, s54, v30\n\t"
        "ds_add_u32 v90, v30\n\t"
        "v_mad_i32_i24 v31, v71, s54, v31\n\t"
        "ds_add_u32 v91, v31\n\t"
        "s_waitcnt vmcnt(0) lgkmcnt(8)\n\t"
        "s_mov_b64 exec, s[32:33]\n\t"
        "v_lshl_add_u32 v32, v32, 16, v52\n\t"
        "v_lshl_add_u32 v33, v33, 16, v53\n\t"
        "v_lshl_add_u32 v34, v34, 16, v54\n\t"
        "v_lshl_add_u32 v35, v35, 16, v55\n\t"
        "v_mad_i32_i24 v32, v72, s54, v32\n\t"
        "ds_add_u32 v92, v32\n\t"
        "v_mad_i32_i24 v33, v73, s54, v33\n\t"
        "ds_add_u32 v93, v33\n\t"
        "v_mad_i32_i24 v34, v74, s54, v34\n\t"
        "ds_add_u32 v94, v34\n\t"
        "v_mad_i32_i24 v35, v75, s54, v35\n\t"
        "ds_add_u32 v95, v35\n\t"
        "s_mov_b64 exec, -1\n\t"
        "s_and_b32 s78, s67, 1\n\t"
        "s_or_b32 s78, s78, s38\n\t"
        "s_or_b32 s78, s78, s39\n\t"
        "s_cbranch_scc1 .L7_mk\n"
        ".L7_mk1:\n\t"
        "s_mov_b64 exec, 1\n\t"
        "ds_add_u32 v2, v3 offset:112\n\t"
        "s_mov_b64 exec, -1\n\t"
        "s_waitcnt lgkmcnt(3)\n\t"
        "ds_read2_b32 v[16:17], v15 offset0:0 offset1:8\n\t"
        "ds_read2_b32 v[18:19], v15 offset0:16 offset1:24\n\t"
        "ds_read2_b32 v[20:21], v97 offset0:0 offset1:8\n\t"
        "ds_read2_b32 v[22:23], v97 offset0:16 offset1:24\n\t"
        "ds_read2_b32 v[24:25], v98 offset0:0 offset1:8\n\t"
        "ds_read2_b32 v[26:27], v98 offset0:16 offset1:24\n\t"
        "ds_read2_b32 v[28:29], v99 offset0:0 offset1:8\n\t"
        "ds_read2_b32 v[30:31], v99 offset0:16 offset1:24\n\t"
        "ds_read2_b32 v[32:33], v100 offset0:0 offset1:8\n\t"
        "ds_read2_b32 v[34:35], v100 offset0:16 offset1:24\n\t"
        "ds_read2_b32 v[36:37], v101 offset0:0 offset1:8\n\t"
        "ds_read2_b32 v[38:39], v101 offset0:16 offset1:24\n\t"
        "s_waitcnt lgkmcnt(10)\n\t"
        "buffer_store_dwordx4 v[16:19], v12, s[20:23], s48 offen offset:0 sc1\n\t"
        "ds_read2_b32 v[40:41], v102 offset0:0 offset1:8\n\t"
        "ds_read2_b32 v[42:43], v102 offset0:16 offset1:24\n\t"
        "s_waitcnt lgkmcnt(10)\n\t"
        "buffer_store_dwordx4 v[20:23], v12, s[20:23], s48 offen offset:1024 sc1\n\t"
        "ds_read2_b32 v[44:45], v103 offset0:0 offset1:8\n\t"
        "ds_read2_b32 v[46:47], v103 offset0:16 offset1:24\n\t"
        "s_mov_b64 exec, 1\n\t"
        "ds_add_u32 v2, v3 offset:72\n\t"
        "s_mov_b64 exec, -1\n\t"
        "s_waitcnt lgkmcnt(11)\n\t"
        "buffer_store_dwordx4 v[24:27], v12, s[20:23], s48 offen offset:2048 sc1\n\t"
        "s_waitcnt lgkmcnt(9)\n\t"
        "buffer_store_dwordx4 v[28:31], v12, s[20:23], s48 offen offset:3072 sc1\n\t"
        "s_waitcnt lgkmcnt(7)\n\t"
        "buffer_store_dwordx4 v[32:35], v12, s[20:23], s49 offen offset:0 sc1\n\t"
        "s_waitcnt lgkmcnt(5)\n\t"
        "buffer_store_dwordx4 v[36:39], v12, s[20:23], s49 offen offset:1024 sc1\n\t"
        "s_waitcnt lgkmcnt(3)\n\t"
        "buffer_store_dwordx4 v[40:43], v12, s[20:23], s49 offen offset:2048 sc1\n\t"
        "s_waitcnt lgkmcnt(1)\n\t"
        "buffer_store_dwordx4 v[44:47], v12, s[20:23], s49 offen offset:3072 sc1\n\t"
        "s_mov_b64 exec, 1\n\t"
        "ds_add_u32 v2, v3 offset:44\n\t"
        "s_mov_b64 exec, -1\n\t"
        "s_branch .L7_Q\n"
        ".L7_ov:\n\t"
        "s_mov_b32 s78, 68828\n\t"
        "v_mov_b32 v9, 40\n\t"
        "v_mad_u32_u24 v8, v56, v9, s78\n\t"
        "v_min_u32 v76, v76, v8\n\t"
        "v_mad_u32_u24 v8, v57, v9, s78\n\t"
        "v_min_u32 v77, v77, v8\n\t"
        "v_mad_u32_u24 v8, v58, v9, s78\n\t"
        "v_min_u32 v78, v78, v8\n\t"
        "v_mad_u32_u24 v8, v59, v9, s78\n\t"
        "v_min_u32 v79, v79, v8\n\t"
        "v_mad_u32_u24 v8, v60, v9, s78\n\t"
        "v_min_u32 v80, v80, v8\n\t"
        "v_mad_u32_u24 v8, v61, v9, s78\n\t"
        "v_min_u32 v81, v81, v8\n\t"
        "v_mad_u32_u24 v8, v62, v9, s78\n\t"
        "v_min_u32 v82, v82, v8\n\t"
        "v_mad_u32_u24 v8, v63, v9, s78\n\t"
        "v_min_u32 v83, v83, v8\n\t"
        "v_mad_u32_u24 v8, v64, v9, s78\n\t"
        "v_min_u32 v84, v84, v8\n\t"
        "v_mad_u32_u24 v8, v65, v9, s78\n\t"
        "v_min_u32 v85, v85, v8\n\t"
        "v_mad_u32_u24 v8, v66, v9, s78\n\t"
        "v_min_u32 v86, v86, v8\n\t"
        "v_mad_u32_u24 v8, v67, v9, s78\n\t"
        "v_min_u32 v87, v87, v8\n\t"
        "v_mad_u32_u24 v8, v68, v9, s78\n\t"
        "v_min_u32 v88, v88, v8\n\t"
        "v_mad_u32_u24 v8, v69, v9, s78\n\t"
        "v_min_u32 v89, v89, v8\n\t"
        "v_mad_u32_u24 v8, v70, v9, s78\n\t"
        "v_min_u32 v90, v90, v8\n\t"
        "v_mad_u32_u24 v8, v71, v9, s78\n\t"
        "v_min_u32 v91, v91, v8\n\t"
        "v_mad_u32_u24 v8, v72, v9, s78\n\t"
        "v_min_u32 v92, v92, v8\n\t"
        "v_mad_u32_u24 v8, v73, v9, s78\n\t"
        "v_min_u32 v93, v93, v8\n\t"
        "v_mad_u32_u24 v8, v74, v9, s78\n\t"
        "v_min_u32 v94, v94, v8\n\t"
        "v_mad_u32_u24 v8, v75, v9, s78\n\t"
        "v_min_u32 v95, v95, v8\n\t"
        "s_branch .L7_ov1\n"
        ".L7_mk:\n\t"
        "s_mul_i32 s78, s68, 0x02000200\n\t"
        "s_or_b32 s78, s78, 0xffffff\n\t"
        "v_mov_b32 v6, s78\n\t"
        "v_add_u32 v112, -4, v112\n\t"
        "v_add_u32 v113, -4, v113\n\t"
        "s_mov_b64 exec, s[34:35]\n\t"
        "ds_write_b32 v112, v6\n\t"
        "s_mov_b64 exec, s[36:37]\n\t"
        "ds_write_b32 v113, v6\n\t"
        "s_mov_b64 exec, -1\n\t"
        "s_and_b32 s78, s67, 1\n\t"
        "s_lshl_b32 s78, s78, 24\n\t"
        "v_mov_b32 v6, s78\n\t"
        "ds_xor_b32 v112, v6\n\t"
        "ds_xor_b32 v113, v6\n\t"
        "s_branch .L7_mk1\n"
        ".L7_P2:\n\t"
        "s_setprio 3\n\t"
        "v_lshrrev_b32 v15, 3, v1\n\t"
        "v_mul_u32_u24 v15, 0xa0, v15\n\t"
        "v_add_u32 v15, 68864, v15\n\t"
        "v_and_b32 v4, 7, v1\n\t"
        "v_lshl_add_u32 v5, v4, 4, v15\n\t"
        "v_lshl_add_u32 v15, v4, 2, v15\n\t"
        "s_mov_b32 s34, 0x0c0c0c0c\n\t"
        "s_mov_b32 s35, s34\n\t"
        "s_lshl_b64 s[36:37], s[34:35], 2\n\t"
        "s_lshl_b64 s[38:39], s[34:35], 4\n\t"
        "s_waitcnt lgkmcnt(0)\n\t"
        "s_add_u32 s68, s68, 1\n\t"
        "v_mov_b32 v4, s68\n\t"
        "ds_write_b32 v2, v4 offset:76\n\t"
        "s_cmp_eq_u32 s70, 0\n\t"
        "s_cbranch_scc0 .L7_P3\n\t"
        "s_lshl_b32 s78, s68, 24\n\t"
        "v_mov_b32 v8, s78\n\t"
        "s_xor_b32 s78, s78, 0x5a000000\n\t"
        "v_mov_b32 v9, s78\n\t"
        "v_cndmask_b32 v8, v8, v9, s[34:35]\n\t"
        "s_mul_i32 s78, s68, 0x01010000\n\t"
        "v_mov_b32 v9, s78\n\t"
        "v_cndmask_b32 v8, v8, v9, s[36:37]\n\t"
        "s_mul_i32 s55, s68, 0x02000200\n\t"
        "v_mov_b32 v9, s55\n\t"
        "v_cndmask_b32 v8, v8, v9, s[38:39]\n\t"
        "v_and_b32 v8, 0xff000000, v8\n\t"
        "v_or_b32 v8, 0xff0000, v8\n\t"
        "v_mov_b32 v9, v8\n\t"
        "v_mov_b64 v[10:11], v[8:9]\n\t"
        "ds_write_b128 v5, v[8:11] offset:0\n\t"
        "ds_write_b128 v5, v[8:11] offset:1280\n\t"
        "ds_write_b128 v5, v[8:11] offset:2560\n\t"
        "ds_write_b128 v5, v[8:11] offset:3840\n\t"
        "ds_write_b128 v5, v[8:11] offset:5120\n\t"
        "ds_write_b128 v5, v[8:11] offset:6400\n\t"
        "ds_write_b128 v5, v[8:11] offset:7680\n\t"
        "ds_write_b128 v5, v[8:11] offset:8960\n\t"
        "ds_write_b128 v5, v[8:11] offset:10240\n\t"
        "ds_write_b128 v5, v[8:11] offset:11520\n\t"
        "ds_write_b128 v5, v[8:11] offset:12800\n\t"
        "ds_write_b128 v5, v[8:11] offset:14080\n\t"
        "s_waitcnt lgkmcnt(6)\n\t"
        "ds_write_b128 v5, v[8:11] offset:15360\n\t"
        "ds_write_b128 v5, v[8:11] offset:16640\n\t"
        "ds_write_b128 v5, v[8:11] offset:17920\n\t"
        "ds_write_b128 v5, v[8:11] offset:19200\n"
        ".L7_P3:\n\t"
        "s_mov_b64 exec, 1\n\t"
        "ds_add_u32 v2, v3 offset:104\n\t"
        "s_mov_b64 exec, -1\n\t"
        "v_lshlrev_b32 v12, 4, v1\n\t"
        "v_add_u32 v4, 148160, v12\n\t"
        "v_mul_u32_u24 v5, 196, v1\n\t"
        "v_add_u32 v5, 148160, v5\n\t"
        "v_mov_b32 v8, 0xffff\n\t"
        "v_mov_b32 v9, v8\n\t"
        "v_mov_b64 v[10:11], v[8:9]\n\t"
        "s_cmp_eq_u32 s70, 0\n\t"
        "s_cbranch_scc0 .L7_P4\n\t"
        "v_add_u32 v15, 10240, v15\n\t"
        "v_add_u32 v97, 1280, v15\n\t"
        "v_add_u32 v98, 2560, v15\n\t"
        "v_add_u32 v99, 3840, v15\n\t"
        "v_add_u32 v100, 5120, v15\n\t"
        "v_add_u32 v101, 6400, v15\n\t"
        "v_add_u32 v102, 7680, v15\n\t"
        "v_add_u32 v103, 8960, v15\n\t"
        "s_lshl_b32 s48, s71, 14\n\t"
        "s_add_u32 s48, s48, 1056768\n\t"
        "s_add_u32 s49, s48, 4096\n"
        ".L7_w44:\n\t"
        "ds_read_b32 v0, v2 offset:112\n\t"
        "s_waitcnt lgkmcnt(0)\n\t"
        "v_cmp_gt_u16 vcc, 1, v0\n\t"
        "s_cbranch_vccz .L7_d45\n\t"
        "s_sleep 0\n\t"
        "s_sub_i32 s83, s83, 1\n\t"
        "s_cmp_gt_i32 s83, 0\n\t"
        "s_cbranch_scc1 .L7_w44\n"
        ".L7_d45:\n\t"
        "ds_read2_b32 v[16:17], v15 offset0:0 offset1:8\n\t"
        "ds_read2_b32 v[18:19], v15 offset0:16 offset1:24\n\t"
        "ds_read2_b32 v[20:21], v97 offset0:0 offset1:8\n\t"
        "ds_read2_b32 v[22:23], v97 offset0:16 offset1:24\n\t"
        "ds_read2_b32 v[24:25], v98 offset0:0 offset1:8\n\t"
        "ds_read2_b32 v[26:27], v98 offset0:16 offset1:24\n\t"
        "ds_read2_b32 v[28:29], v99 offset0:0 offset1:8\n\t"
        "ds_read2_b32 v[30:31], v99 offset0:16 offset1:24\n\t"
        "ds_read2_b32 v[32:33], v100 offset0:0 offset1:8\n\t"
        "ds_read2_b32 v[34:35], v100 offset0:16 offset1:24\n\t"
        "ds_read2_b32 v[36:37], v101 offset0:0 offset1:8\n\t"
        "ds_read2_b32 v[38:39], v101 offset0:16 offset1:24\n\t"
        "s_waitcnt lgkmcnt(10)\n\t"
        "buffer_store_dwordx4 v[16:19], v12, s[20:23], s48 offen offset:0 sc1\n\t"
        "ds_read2_b32 v[40:41], v102 offset0:0 offset1:8\n\t"
        "ds_read2_b32 v[42:43], v102 offset0:16 offset1:24\n\t"
        "s_waitcnt lgkmcnt(10)\n\t"
        "buffer_store_dwordx4 v[20:23], v12, s[20:23], s48 offen offset:1024 sc1\n\t"
        "ds_read2_b32 v[44:45], v103 offset0:0 offset1:8\n\t"
        "ds_read2_b32 v[46:47], v103 offset0:16 offset1:24\n\t"
        "s_mov_b64 exec, 1\n\t"
        "ds_add_u32 v2, v3 offset:72\n\t"
        "s_mov_b64 exec, -1\n\t"
        "s_waitcnt lgkmcnt(11)\n\t"
        "buffer_store_dwordx4 v[24:27], v12, s[20:23], s48 offen offset:2048 sc1\n\t"
        "s_waitcnt lgkmcnt(9)\n\t"
        "buffer_store_dwordx4 v[28:31], v12, s[20:23], s48 offen offset:3072 sc1\n\t"
        "s_waitcnt lgkmcnt(7)\n\t"
        "buffer_store_dwordx4 v[32:35], v12, s[20:23], s49 offen offset:0 sc1\n\t"
        "s_waitcnt lgkmcnt(5)\n\t"
        "buffer_store_dwordx4 v[36:39], v12, s[20:23], s49 offen offset:1024 sc1\n\t"
        "s_waitcnt lgkmcnt(3)\n\t"
        "buffer_store_dwordx4 v[40:43], v12, s[20:23], s49 offen offset:2048 sc1\n\t"
        "s_waitcnt lgkmcnt(1)\n\t"
        "buffer_store_dwordx4 v[44:47], v12, s[20:23], s49 offen offset:3072 sc1\n\t"
        "s_mov_b64 exec, 1\n\t"
        "ds_add_u32 v2, v3 offset:44\n\t"
        "s_mov_b64 exec, -1\n"
        ".L7_P4:\n\t"
        "s_setprio 0\n\t"
        "s_waitcnt lgkmcnt(0)\n\t"
        "ds_write_b128 v4, v[8:11] offset:0\n\t"
        "ds_write_b128 v4, v[8:11] offset:1024\n\t"
        "ds_write_b128 v4, v[8:11] offset:2048\n\t"
        "ds_write_b128 v4, v[8:11] offset:3072\n\t"
        "ds_write_b128 v4, v[8:11] offset:4096\n\t"
        "ds_write_b128 v4, v[8:11] offset:5120\n\t"
        "ds_write_b128 v4, v[8:11] offset:6144\n\t"
        "ds_write_b128 v4, v[8:11] offset:7168\n\t"
        "ds_write_b128 v4, v[8:11] offset:8192\n\t"
        "ds_write_b128 v4, v[8:11] offset:9216\n\t"
        "ds_write_b128 v4, v[8:11] offset:10240\n\t"
        "ds_write_b128 v4, v[8:11] offset:11264\n\t"
        "s_waitcnt lgkmcnt(6)\n\t"
        "ds_write_b128 v4, v[8:11] offset:12288\n\t"
        "ds_write_b128 v4, v[8:11] offset:13312\n\t"
        "ds_write_b128 v4, v[8:11] offset:14336\n\t"
        "ds_write_b128 v4, v[8:11] offset:15360\n\t"
        "ds_write_b32 v5, v2 offset:192\n\t"
        "s_mov_b64 exec, 0xffff\n\t"
        "ds_write_b32 v5, v2 offset:12736\n\t"
        "s_mov_b64 exec, 1\n\t"
        "ds_add_u32 v2, v3 offset:8\n\t"
        "s_mov_b64 exec, -1\n\t"
        "s_branch .L7_Q\n"
        ".L7_V2:\n\t"
        "v_and_b32 v5, 7, v1\n\t"
        "v_mul_u32_u24 v5, 0x420, v5\n\t"
        "v_and_b32 v6, 8, v1\n\t"
        "v_lshl_add_u32 v5, v6, 6, v5\n\t"
        "v_and_b32 v6, 48, v1\n\t"
        "v_add_u32 v5, v5, v6\n\t"
        "v_add_u32 v5, 68864, v5\n\t"
        "s_lshr_b32 s78, s81, 1\n\t"
        "s_lshl3_add_u32 s78, s78, s80\n\t"
        "s_and_b32 s79, s81, 1\n\t"
        "s_lshl1_add_u32 s78, s78, s79\n\t"
        "s_lshl_b32 s82, s78, 10\n\t"
        "s_add_u32 s82, s82, 131072\n"
        ".L7_w40:\n\t"
        "ds_read_b32 v0, v2 offset:104\n\t"
        "ds_read_b32 v6, v2 offset:76\n\t"
        "s_waitcnt lgkmcnt(1)\n\t"
        "v_cmp_gt_u16 vcc, 1, v0\n\t"
        "s_cbranch_vccz .L7_d41\n\t"
        "s_waitcnt lgkmcnt(0)\n\t"
        "s_sleep 1\n\t"
        "s_sub_i32 s83, s83, 1\n\t"
        "s_cmp_gt_i32 s83, 0\n\t"
        "s_cbranch_scc1 .L7_w40\n"
        ".L7_d41:\n\t"
        "s_waitcnt lgkmcnt(0)\n\t"
        "v_readfirstlane_b32 s68, v6\n\t"
        "v_mov_b32 v7, 2\n\t"
        "s_and_b32 s73, s68, 1\n\t"
        "s_cselect_b32 s73, 1, 0x10000\n\t"
        "v_mov_b32 v16, s73\n\t"
        "v_and_b32 v17, 7, v1\n\t"
        "v_add_u32 v17, 1, v17\n\t"
        "v_lshrrev_b32 v17, v17, s68\n\t"
        "s_mov_b32 s49, 0x10001\n\t"
        "v_and_b32 v17, s49, v17\n\t"
        "s_mov_b32 s48, 0x3db8aa3b\n\t"
        "s_waitcnt vmcnt(0)\n\t"
        "ds_read_b128 v[32:35], v5 offset:0\n\t"
        "ds_read_b128 v[36:39], v5 offset:8448\n\t"
        "ds_read_b128 v[40:43], v5 offset:16896\n\t"
        "ds_read_b128 v[44:47], v5 offset:64\n\t"
        "ds_read_b128 v[48:51], v5 offset:8512\n\t"
        "ds_read_b128 v[52:55], v5 offset:16960\n\t"
        "ds_read_b128 v[56:59], v5 offset:128\n\t"
        "ds_read_b128 v[60:63], v5 offset:8576\n\t"
        "ds_read_b128 v[64:67], v5 offset:17024\n\t"
        "ds_read_b128 v[68:71], v5 offset:192\n\t"
        "ds_read_b128 v[72:75], v5 offset:8640\n\t"
        "ds_read_b128 v[76:79], v5 offset:17088\n\t"
        "s_waitcnt lgkmcnt(9)\n\t"
        "v_mfma_f32_16x16x32_bf16 v[20:23], v[36:39], v[32:35], 0\n\t"
        "v_mfma_f32_16x16x32_bf16 v[24:27], v[40:43], v[32:35], 0\n\t"
        "ds_read_b128 v[80:83], v5 offset:256\n\t"
        "ds_read_b128 v[84:87], v5 offset:8704\n\t"
        "ds_read_b128 v[88:91], v5 offset:17152\n\t"
        "s_waitcnt lgkmcnt(9)\n\t"
        "v_mfma_f32_16x16x32_bf16 v[20:23], v[48:51], v[44:47], v[20:23]\n\t"
        "v_mfma_f32_16x16x32_bf16 v[24:27], v[52:55], v[44:47], v[24:27]\n\t"
        "ds_read_b128 v[92:95], v5 offset:320\n\t"
        "ds_read_b128 v[96:99], v5 offset:8768\n\t"
        "ds_read_b128 v[100:103], v5 offset:17216\n\t"
        "s_waitcnt lgkmcnt(9)\n\t"
        "v_mfma_f32_16x16x32_bf16 v[20:23], v[60:63], v[56:59], v[20:23]\n\t"
        "v_mfma_f32_16x16x32_bf16 v[24:27], v[64:67], v[56:59], v[24:27]\n\t"
        "ds_read_b128 v[104:107], v5 offset:384\n\t"
        "ds_read_b128 v[108:111], v5 offset:8832\n\t"
        "ds_read_b128 v[112:115], v5 offset:17280\n\t"
        "s_waitcnt lgkmcnt(9)\n\t"
        "v_mfma_f32_16x16x32_bf16 v[20:23], v[72:75], v[68:71], v[20:23]\n\t"
        "v_mfma_f32_16x16x32_bf16 v[24:27], v[76:79], v[68:71], v[24:27]\n\t"
        "ds_read_b128 v[116:119], v5 offset:448\n\t"
        "ds_read_b128 v[120:123], v5 offset:8896\n\t"
        "ds_read_b128 v[124:127], v5 offset:17344\n\t"
        "s_waitcnt lgkmcnt(9)\n\t"
        "v_mfma_f32_16x16x32_bf16 v[20:23], v[84:87], v[80:83], v[20:23]\n\t"
        "v_mfma_f32_16x16x32_bf16 v[24:27], v[88:91], v[80:83], v[24:27]\n\t"
        "s_mov_b64 exec, 1\n\t"
        "ds_add_u32 v2, v7 offset:72\n\t"
        "s_mov_b64 exec, -1\n\t"
        "s_waitcnt lgkmcnt(7)\n\t"
        "v_mfma_f32_16x16x32_bf16 v[20:23], v[96:99], v[92:95], v[20:23]\n\t"
        "v_mfma_f32_16x16x32_bf16 v[24:27], v[100:103], v[92:95], v[24:27]\n\t"
        "s_waitcnt lgkmcnt(4)\n\t"
        "v_mfma_f32_16x16x32_bf16 v[20:23], v[108:111], v[104:107], v[20:23]\n\t"
        "v_mfma_f32_16x16x32_bf16 v[24:27], v[112:115], v[104:107], v[24:27]\n\t"
        "s_waitcnt lgkmcnt(1)\n\t"
        "v_mfma_f32_16x16x32_bf16 v[20:23], v[120:123], v[116:119], v[20:23]\n\t"
        "v_mfma_f32_16x16x32_bf16 v[24:27], v[124:127], v[116:119], v[24:27]\n\t"
        "s_nop 6\n\t"
        "v_mul_f32 v20, s48, v20\n\t"
        "v_mul_f32 v21, s48, v21\n\t"
        "v_mul_f32 v22, s48, v22\n\t"
        "v_mul_f32 v23, s48, v23\n\t"
        "v_mul_f32 v24, s48, v24\n\t"
        "v_mul_f32 v25, s48, v25\n\t"
        "v_mul_f32 v26, s48, v26\n\t"
        "v_mul_f32 v27, s48, v27\n\t"
        "s_mov_b32 s48, 0x07060302\n\t"
        "v_add_u32 v20, 0x10000, v20\n\t"
        "v_add_u32 v21, 0x10000, v21\n\t"
        "v_add_u32 v22, 0x10000, v22\n\t"
        "v_add_u32 v23, 0x10000, v23\n\t"
        "v_perm_b32 v20, v21, v20, s48\n\t"
        "v_perm_b32 v21, v23, v22, s48\n\t"
        "v_cvt_pk_bf16_f32 v22, v24, v25\n\t"
        "v_cvt_pk_bf16_f32 v23, v26, v27\n\t"
        "v_bfi_b32 v20, s49, v16, v20\n\t"
        "v_bfi_b32 v21, s49, v17, v21\n\t"
        "buffer_store_dwordx4 v[20:23], v12, s[20:23], s82 offen sc1\n\t"
        "s_mov_b64 exec, 1\n\t"
        "ds_add_u32 v2, v7 offset:44\n\t"
        "s_mov_b64 exec, -1\n\t"
        "s_branch .L7_Q\n"
        ".L7_T2:\n\t"
        "s_and_b32 s82, s82, -1024\n\t"
        "v_mov_b32 v9, 4\n"
        ".L7_g42:\n\t"
        "s_memrealtime s[78:79]\n\t"
        "s_waitcnt lgkmcnt(0)\n\t"
        "s_sub_u32 s78, s78, s74\n\t"
        "s_cmp_ge_i32 s78, 150\n\t"
        "s_cbranch_scc1 .L7_h43\n\t"
        "s_sleep 1\n\t"
        "s_branch .L7_g42\n"
        ".L7_h43:\n"
        ".L7_b1:\n\t"
        "s_buffer_load_dword s40, s[20:23], s82 offset:0 glc\n\t"
        "s_buffer_load_dword s41, s[20:23], s82 offset:128 glc\n\t"
        "s_buffer_load_dword s42, s[20:23], s82 offset:256 glc\n\t"
        "s_buffer_load_dword s43, s[20:23], s82 offset:384 glc\n\t"
        "s_buffer_load_dword s44, s[20:23], s82 offset:512 glc\n\t"
        "s_buffer_load_dword s45, s[20:23], s82 offset:640 glc\n\t"
        "s_buffer_load_dword s46, s[20:23], s82 offset:768 glc\n\t"
        "s_buffer_load_dword s47, s[20:23], s82 offset:896 glc\n\t"
        "s_waitcnt lgkmcnt(0)\n\t"
        "s_mov_b32 s80, 0\n\t"
        "s_mov_b32 s81, 0\n\t"
        "s_sub_u32 s40, s40, s74\n\t"
        "s_max_i32 s80, s80, s40\n\t"
        "s_min_i32 s81, s81, s40\n\t"
        "s_sub_u32 s41, s41, s74\n\t"
        "s_max_i32 s80, s80, s41\n\t"
        "s_min_i32 s81, s81, s41\n\t"
        "s_sub_u32 s42, s42, s74\n\t"
        "s_max_i32 s80, s80, s42\n\t"
        "s_min_i32 s81, s81, s42\n\t"
        "s_sub_u32 s43, s43, s74\n\t"
        "s_max_i32 s80, s80, s43\n\t"
        "s_min_i32 s81, s81, s43\n\t"
        "s_sub_u32 s44, s44, s74\n\t"
        "s_max_i32 s80, s80, s44\n\t"
        "s_min_i32 s81, s81, s44\n\t"
        "s_sub_u32 s45, s45, s74\n\t"
        "s_max_i32 s80, s80, s45\n\t"
        "s_min_i32 s81, s81, s45\n\t"
        "s_sub_u32 s46, s46, s74\n\t"
        "s_max_i32 s80, s80, s46\n\t"
        "s_min_i32 s81, s81, s46\n\t"
        "s_sub_u32 s47, s47, s74\n\t"
        "s_max_i32 s80, s80, s47\n\t"
        "s_min_i32 s81, s81, s47\n\t"
        "s_sub_u32 s81, s80, s81\n\t"
        "s_cmp_lt_u32 s81, 0x3e8\n\t"
        "s_cbranch_scc1 .L7_b2\n\t"
        "s_mov_b64 exec, 1\n\t"
        "ds_add_u32 v2, v9 offset:84\n\t"
        "s_mov_b64 exec, -1\n\t"
        "s_mov_b64 exec, 1\n\t"
        "ds_add_u32 v2, v9 offset:108\n\t"
        "s_mov_b64 exec, -1\n\t"
        "v_mov_b32 v9, 0\n\t"
        "s_mov_b32 s80, 0\n\t"
        "s_sleep 4\n\t"
        "s_memrealtime s[78:79]\n\t"
        "s_waitcnt lgkmcnt(0)\n\t"
        "s_sub_u32 s78, s78, s74\n\t"
        "s_cmp_lt_u32 s78, 0x4b0\n\t"
        "s_cbranch_scc1 .L7_b1\n"
        ".L7_b2:\n\t"
        "s_add_u32 s80, s80, 1\n\t"
        "v_mov_b32 v7, s80\n\t"
        "s_mov_b64 exec, 1\n\t"
        "ds_add_u32 v2, v7 offset:92\n\t"
        "s_mov_b64 exec, -1\n\t"
        "s_branch .L7_Q0\n"
        ".L7_R2:\n\t"
        "s_sub_u32 s79, s64, s72\n\t"
        "s_min_i32 s79, s79, s65\n\t"
        "s_max_i32 s79, s79, 0\n\t"
        "v_mov_b32 v32, v1\n\t"
        "v_add_u32 v33, 64, v1\n\t"
        "v_cmp_gt_u32 s[32:33], s79, v32\n\t"
        "v_cmp_gt_u32 s[34:35], s79, v33\n\t"
        "v_cmp_gt_u32 s[36:37], 40, v1\n\t"
        "s_sub_u32 s78, 0xff, s65\n\t"
        "v_add_u32 v34, s78, v32\n\t"
        "v_add_u32 v6, 0xffffffb0, v34\n\t"
        "v_cmp_gt_u32 vcc, 0xff, v34\n\t"
        "s_nop 1\n\t"
        "v_cndmask_b32 v34, v6, v34, vcc\n\t"
        "v_add_u32 v35, s78, v33\n\t"
        "v_add_u32 v6, 0xffffffb0, v35\n\t"
        "v_cmp_gt_u32 vcc, 0xff, v35\n\t"
        "s_nop 1\n\t"
        "v_cndmask_b32 v35, v6, v35, vcc\n\t"
        "v_mov_b32 v12, 0x8000\n\t"
        "v_cndmask_b32 v44, v12, 0, s[32:33]\n\t"
        "v_cndmask_b32 v45, v12, 0, s[34:35]\n\t"
        "v_cndmask_b32 v46, 63, 31, s[36:37]\n\t"
        "v_mov_b32 v47, 63\n\t"
        "s_waitcnt vmcnt(0)\n\t"
        "v_lshlrev_b32 v16, 16, v16\n\t"
        "v_lshlrev_b32 v17, 16, v17\n\t"
        "v_mul_f32 v36, v16, v16\n\t"
        "v_mul_f32 v37, v17, v17\n\t"
        "v_rcp_f32 v36, v36\n\t"
        "v_rcp_f32 v37, v37\n\t"
        "v_or3_b32 v16, v16, v34, v44\n\t"
        "v_or3_b32 v17, v17, v35, v45\n\t"
        "v_add_f32 v36, 0xc0600000, v36\n\t"
        "v_add_f32 v37, 0xc0600000, v37\n\t"
        "v_cvt_u32_f32 v36, v36\n\t"
        "v_cvt_u32_f32 v37, v37\n\t"
        "v_min_u32 v36, 31, v36\n\t"
        "v_min_u32 v37, 31, v37\n\t"
        "v_cndmask_b32 v36, 0, v36, s[32:33]\n\t"
        "v_cndmask_b32 v37, 0, v37, s[34:35]\n\t"
        "v_sub_u32 v36, v46, v36\n\t"
        "v_sub_u32 v37, v47, v37\n\t"
        "v_lshlrev_b32 v36, 2, v36\n\t"
        "v_lshlrev_b32 v37, 2, v37\n\t"
        "ds_add_rtn_u32 v40, v36, v3 offset:256\n\t"
        "s_mov_b64 exec, 0xffff\n\t"
        "ds_add_rtn_u32 v41, v37, v3 offset:256\n\t"
        "s_mov_b64 exec, -1\n\t"
        "v_lshlrev_b32 v9, 2, v1\n\t"
        "ds_read_b32 v10, v9 offset:256\n\t"
        "s_cmp_eq_u32 s70, 0\n\t"
        "s_cselect_b32 s78, 56, 0\n\t"
        "s_cselect_b32 s79, 0, 56\n\t"
        "v_mov_b32 v49, -40\n\t"
        "v_cndmask_b32 v48, v49, 0, s[36:37]\n\t"
        "s_mov_b32 s40, 0x23108954\n\t"
        "s_mov_b32 s41, 0x67\n\t"
        "s_mov_b32 s42, 0x1986732\n\t"
        "s_mov_b32 s43, 0x45\n\t"
        "v_mov_b32 v13, s78\n\t"
        "v_mov_b32 v14, s79\n\t"
        "v_cndmask_b32 v13, v14, v13, s[36:37]\n\t"
        "s_waitcnt lgkmcnt(0)\n\t"
        "v_mov_b32 v11, v10\n\t"
        "s_nop 1\n\t"
        "v_add_u32_dpp v11, v11, v11 row_shr:1 row_mask:0xf bank_mask:0xf bound_ctrl:1\n\t"
        "s_nop 1\n\t"
        "v_add_u32_dpp v11, v11, v11 row_shr:2 row_mask:0xf bank_mask:0xf bound_ctrl:1\n\t"
        "s_nop 1\n\t"
        "v_add_u32_dpp v11, v11, v11 row_shr:4 row_mask:0xf bank_mask:0xf bound_ctrl:1\n\t"
        "s_nop 1\n\t"
        "v_add_u32_dpp v11, v11, v11 row_shr:8 row_mask:0xf bank_mask:0xf bound_ctrl:1\n\t"
        "s_nop 1\n\t"
        "v_add_u32_dpp v11, v11, v11 row_bcast:15 row_mask:0xa bank_mask:0xf\n\t"
        "s_nop 1\n\t"
        "v_add_u32_dpp v11, v11, v11 row_bcast:31 row_mask:0xc bank_mask:0xf\n\t"
        "v_sub_u32 v11, v11, v10\n\t"
        "ds_bpermute_b32 v36, v36, v11\n\t"
        "ds_bpermute_b32 v37, v37, v11\n\t"
        "v_lshlrev_b32 v9, 1, v1\n\t"
        "s_waitcnt lgkmcnt(0)\n\t"
        "v_add3_u32 v36, v36, v40, v48\n\t"
        "v_and_b32 v6, -4, v36\n\t"
        "v_lshrrev_b64 v[10:11], v6, s[40:41]\n\t"
        "v_lshrrev_b64 v[50:51], v6, s[42:43]\n\t"
        "v_cmp_eq_u32 vcc, 0, v13\n\t"
        "s_nop 1\n\t"
        "v_cndmask_b32 v10, v10, v50, vcc\n\t"
        "v_and_b32 v10, 15, v10\n\t"
        "v_and_b32 v6, 3, v36\n\t"
        "v_lshl_add_u32 v6, v10, 2, v6\n\t"
        "v_add_u32 v36, v6, v13\n\t"
        "v_and_b32 v6, 15, v36\n\t"
        "v_lshlrev_b32 v6, 4, v6\n\t"
        "v_lshl_add_u32 v6, v36, 8, v6\n\t"
        "v_and_b32 v10, 8, v13\n\t"
        "v_lshlrev_b32 v10, 8, v10\n\t"
        "v_sub_u32 v6, v6, v10\n\t"
        "v_lshlrev_b32 v36, 2, v36\n\t"
        "ds_write_b32 v36, v16 offset:512\n\t"
        "ds_write_b16 v9, v6 offset:1792\n\t"
        "v_add3_u32 v37, v37, v41, v49\n\t"
        "v_and_b32 v6, -4, v37\n\t"
        "v_lshrrev_b64 v[10:11], v6, s[40:41]\n\t"
        "v_lshrrev_b64 v[50:51], v6, s[42:43]\n\t"
        "v_cmp_eq_u32 vcc, 0, v14\n\t"
        "s_nop 1\n\t"
        "v_cndmask_b32 v10, v10, v50, vcc\n\t"
        "v_and_b32 v10, 15, v10\n\t"
        "v_and_b32 v6, 3, v37\n\t"
        "v_lshl_add_u32 v6, v10, 2, v6\n\t"
        "v_add_u32 v37, v6, v14\n\t"
        "v_and_b32 v6, 15, v37\n\t"
        "v_lshlrev_b32 v6, 4, v6\n\t"
        "v_lshl_add_u32 v6, v37, 8, v6\n\t"
        "v_and_b32 v10, 8, v14\n\t"
        "v_lshlrev_b32 v10, 8, v10\n\t"
        "v_sub_u32 v6, v6, v10\n\t"
        "v_lshlrev_b32 v37, 2, v37\n\t"
        "s_mov_b64 exec, 0xffff\n\t"
        "ds_write_b32 v37, v17 offset:512\n\t"
        "ds_write_b16 v9, v6 offset:1920\n\t"
        "s_mov_b64 exec, 1\n\t"
        "ds_add_u32 v2, v3 offset:8\n\t"
        "s_mov_b64 exec, -1\n"
        ".L7_w46:\n\t"
        "ds_read_b64 v[4:5], v2 offset:72\n\t"
        "s_waitcnt lgkmcnt(0)\n\t"
        "v_cmp_gt_u16 vcc, 2, v4\n\t"
        "s_cbranch_vccz .L7_d47\n\t"
        "s_sleep 2\n\t"
        "s_sub_i32 s83, s83, 1\n\t"
        "s_cmp_gt_i32 s83, 0\n\t"
        "s_cbranch_scc1 .L7_w46\n"
        ".L7_d47:\n\t"
        "v_readfirstlane_b32 s68, v5\n\t"
        "v_lshlrev_b32 v22, 4, v1\n\t"
        "v_add_u32 v9, 142848, v22\n\t"
        "v_and_b32 v10, 60, v1\n\t"
        "ds_read_b32 v24, v10 offset:704\n\t"
        "ds_read_b32 v25, v10 offset:768\n\t"
        "ds_read_b32 v26, v10 offset:832\n\t"
        "v_and_b32 v11, 15, v1\n\t"
        "v_lshrrev_b32 v12, 4, v1\n\t"
        "v_xor_b32 v16, v11, v12\n\t"
        "v_lshlrev_b32 v16, 4, v16\n\t"
        "v_lshl_add_u32 v16, v11, 8, v16\n\t"
        "v_xor_b32 v17, 64, v16\n\t"
        "v_xor_b32 v18, 128, v16\n\t"
        "v_xor_b32 v19, 192, v16\n\t"
        "ds_write_b128 v9, v[16:19]\n\t"
        "s_and_b32 s73, s68, 1\n\t"
        "s_cselect_b32 s73, 1, 0x10000\n\t"
        "v_mov_b32 v20, s73\n\t"
        "v_and_b32 v21, 7, v1\n\t"
        "v_add_u32 v21, 1, v21\n\t"
        "v_lshrrev_b32 v21, v21, s68\n\t"
        "s_mov_b32 s49, 0x10001\n\t"
        "v_and_b32 v21, s49, v21\n\t"
        "v_and_b32 v23, 0x37f, v22\n\t"
        "ds_write_b128 v9, v[20:23] offset:1024\n\t"
        "s_add_u32 s78, s72, s65\n\t"
        "s_sub_u32 s78, s78, 0xff\n\t"
        "s_lshl_b32 s78, s78, 9\n\t"
        "v_and_b32 v13, 48, v22\n\t"
        "v_add_u32 v14, s78, v13\n\t"
        "s_movk_i32 s78, 0x200\n\t"
        "v_bfe_u32 v27, v1, 1, 2\n\t"
        "v_xor_b32 v27, v27, v12\n\t"
        "v_lshlrev_b32 v27, 4, v27\n\t"
        "v_lshl_add_u32 v27, v11, 6, v27\n\t"
        "v_lshlrev_b32 v28, 1, v1\n\t"
        "v_and_b32 v28, 48, v28\n\t"
        "v_xor_b32 v28, v28, v22\n\t"
        "s_waitcnt lgkmcnt(2)\n\t"
        "v_mad_u32_u16 v24, v24, s78, v14\n\t"
        "v_mad_u32_u16 v25, v25, s78, v14\n\t"
        "v_mad_u32_u16 v26, v26, s78, v14\n\t"
        "s_mov_b32 exec_hi, 0\n\t"
        "v_mov_b32 v24, -2.0\n\t"
        "s_mov_b64 exec, -1\n\t"
        "ds_write_b128 v9, v[24:27] offset:2048\n\t"
        "s_mov_b32 s78, 0x3f80\n\t"
        "s_mov_b32 s79, 0\n\t"
        "v_lshlrev_b64 v[30:31], v13, s[78:79]\n\t"
        "s_mov_b32 s78, 0x00f0000f\n\t"
        "s_mov_b32 s79, 0xf0000f00\n\t"
        "v_cndmask_b32 v30, 0, v30, s[78:79]\n\t"
        "v_cndmask_b32 v31, 0, v31, s[78:79]\n\t"
        "ds_write_b128 v9, v[28:31] offset:3072\n\t"
        "s_branch .L7_Q\n"
        ".L7_Q0:\n"
        ".L7_w48:\n\t"
        "ds_read_b64 v[4:5], v2 offset:72\n\t"
        "s_waitcnt lgkmcnt(0)\n\t"
        "v_cmp_gt_u16 vcc, 2, v4\n\t"
        "s_cbranch_vccz .L7_d49\n\t"
        "s_sleep 2\n\t"
        "s_sub_i32 s83, s83, 1\n\t"
        "s_cmp_gt_i32 s83, 0\n\t"
        "s_cbranch_scc1 .L7_w48\n"
        ".L7_d49:\n\t"
        "v_readfirstlane_b32 s68, v5\n"
        ".L7_Q:\n\t"
        "s_setprio 3\n\t"
        "v_and_b32 v5, 7, v1\n\t"
        "v_mul_u32_u24 v5, 0x420, v5\n\t"
        "v_and_b32 v6, 8, v1\n\t"
        "v_lshl_add_u32 v5, v6, 6, v5\n\t"
        "v_and_b32 v6, 48, v1\n\t"
        "v_add_u32 v5, v5, v6\n\t"
        "s_and_b32 s73, s68, 1\n\t"
        "s_cselect_b32 s73, 1, 0x10000\n\t"
        "v_and_b32 v6, 7, v1\n\t"
        "v_add_u32 v6, 1, v6\n\t"
        "v_lshrrev_b32 v6, v6, s68\n\t"
        "s_mov_b32 s49, 0x10001\n\t"
        "v_and_b32 v6, s49, v6\n\t"
        "s_add_u32 s80, s69, s76\n\t"
        "s_and_b32 s80, s80, 3\n\t"
        "s_lshl2_add_u32 s78, s70, s80\n\t"
        "s_lshl_b32 s78, s78, 14\n\t"
        "s_add_u32 s78, s78, 131072\n\t"
        "v_lshl_add_u32 v4, v1, 4, s78\n\t"
        "s_mov_b32 s24, 4096\n\t"
        "s_mov_b32 s25, 8192\n\t"
        "s_mov_b32 s26, 12288\n"
        ".L7_w50:\n\t"
        "ds_read_b32 v0, v2 offset:92\n\t"
        "s_waitcnt lgkmcnt(0)\n\t"
        "v_cmp_gt_u16 vcc, 1, v0\n\t"
        "s_cbranch_vccz .L7_d51\n\t"
        "s_sleep 1\n\t"
        "s_sub_i32 s83, s83, 1\n\t"
        "s_cmp_gt_i32 s83, 0\n\t"
        "s_cbranch_scc1 .L7_w50\n"
        ".L7_d51:\n\t"
        "v_readfirstlane_b32 s78, v0\n\t"
        "s_sub_u32 s78, s78, 1\n\t"
        "s_add_u32 s77, s74, s78\n"
        ".L7_g52:\n\t"
        "s_memrealtime s[78:79]\n\t"
        "s_waitcnt lgkmcnt(0)\n\t"
        "s_sub_u32 s78, s78, s77\n\t"
        "s_cmp_ge_i32 s78, 280\n\t"
        "s_cbranch_scc1 .L7_h53\n\t"
        "s_sleep 1\n\t"
        "s_branch .L7_g52\n"
        ".L7_h53:\n"
        ".L7_w54:\n\t"
        "ds_read_b32 v0, v2 offset:88\n\t"
        "s_waitcnt lgkmcnt(0)\n\t"
        "v_cmp_gt_u16 vcc, 8, v0\n\t"
        "s_cbranch_vccz .L7_d55\n\t"
        "s_sleep 1\n\t"
        "s_sub_i32 s83, s83, 1\n\t"
        "s_cmp_gt_i32 s83, 0\n\t"
        "s_cbranch_scc1 .L7_w54\n"
        ".L7_d55:\n\t"
        "buffer_load_dwordx4 v[64:67], v4, s[20:23], 0 offen offset:0 sc1\n\t"
        "buffer_load_dwordx4 v[68:71], v4, s[20:23], 0 offen offset:1024 sc1\n\t"
        "buffer_load_dwordx4 v[72:75], v4, s[20:23], 0 offen offset:2048 sc1\n\t"
        "buffer_load_dwordx4 v[76:79], v4, s[20:23], 0 offen offset:3072 sc1\n\t"
        "buffer_load_dwordx4 v[80:83], v4, s[20:23], s24 offen offset:0 sc1\n\t"
        "buffer_load_dwordx4 v[84:87], v4, s[20:23], s24 offen offset:1024 sc1\n\t"
        "buffer_load_dwordx4 v[88:91], v4, s[20:23], s24 offen offset:2048 sc1\n\t"
        "buffer_load_dwordx4 v[92:95], v4, s[20:23], s24 offen offset:3072 sc1\n\t"
        "buffer_load_dwordx4 v[96:99], v4, s[20:23], s25 offen offset:0 sc1\n\t"
        "buffer_load_dwordx4 v[100:103], v4, s[20:23], s25 offen offset:1024 sc1\n\t"
        "buffer_load_dwordx4 v[104:107], v4, s[20:23], s25 offen offset:2048 sc1\n\t"
        "buffer_load_dwordx4 v[108:111], v4, s[20:23], s25 offen offset:3072 sc1\n\t"
        "buffer_load_dwordx4 v[112:115], v4, s[20:23], s26 offen offset:0 sc1\n\t"
        "buffer_load_dwordx4 v[116:119], v4, s[20:23], s26 offen offset:1024 sc1\n\t"
        "buffer_load_dwordx4 v[120:123], v4, s[20:23], s26 offen offset:2048 sc1\n\t"
        "buffer_load_dwordx4 v[124:127], v4, s[20:23], s26 offen offset:3072 sc1\n\t"
        "s_mov_b64 exec, 1\n\t"
        "ds_add_u32 v2, v3 offset:84\n\t"
        "s_mov_b64 exec, -1\n"
        ".L7_w56:\n\t"
        "ds_read_b32 v0, v2 offset:0\n\t"
        "s_waitcnt lgkmcnt(0)\n\t"
        "v_cmp_gt_u16 vcc, 4, v0\n\t"
        "s_cbranch_vccz .L7_d57\n\t"
        "s_sleep 1\n\t"
        "s_sub_i32 s83, s83, 1\n\t"
        "s_cmp_gt_i32 s83, 0\n\t"
        "s_cbranch_scc1 .L7_w56\n"
        ".L7_d57:\n\t"
        "s_branch .L7_qg\n"
        ".L7_qr:\n\t"
        "s_waitcnt lgkmcnt(0)\n\t"
        "s_sleep 8\n\t"
        "s_sub_i32 s83, s83, 64\n\t"
        "s_cmp_gt_i32 s83, 0\n\t"
        "s_cbranch_scc0 .L7_qd\n\t"
        "buffer_load_dwordx4 v[64:67], v4, s[20:23], 0 offen offset:0 sc1\n\t"
        "buffer_load_dwordx4 v[68:71], v4, s[20:23], 0 offen offset:1024 sc1\n\t"
        "buffer_load_dwordx4 v[72:75], v4, s[20:23], 0 offen offset:2048 sc1\n\t"
        "buffer_load_dwordx4 v[76:79], v4, s[20:23], 0 offen offset:3072 sc1\n\t"
        "buffer_load_dwordx4 v[80:83], v4, s[20:23], s24 offen offset:0 sc1\n\t"
        "buffer_load_dwordx4 v[84:87], v4, s[20:23], s24 offen offset:1024 sc1\n\t"
        "buffer_load_dwordx4 v[88:91], v4, s[20:23], s24 offen offset:2048 sc1\n\t"
        "buffer_load_dwordx4 v[92:95], v4, s[20:23], s24 offen offset:3072 sc1\n\t"
        "buffer_load_dwordx4 v[96:99], v4, s[20:23], s25 offen offset:0 sc1\n\t"
        "buffer_load_dwordx4 v[100:103], v4, s[20:23], s25 offen offset:1024 sc1\n\t"
        "buffer_load_dwordx4 v[104:107], v4, s[20:23], s25 offen offset:2048 sc1\n\t"
        "buffer_load_dwordx4 v[108:111], v4, s[20:23], s25 offen offset:3072 sc1\n\t"
        "buffer_load_dwordx4 v[112:115], v4, s[20:23], s26 offen offset:0 sc1\n\t"
        "buffer_load_dwordx4 v[116:119], v4, s[20:23], s26 offen offset:1024 sc1\n\t"
        "buffer_load_dwordx4 v[120:123], v4, s[20:23], s26 offen offset:2048 sc1\n\t"
        "buffer_load_dwordx4 v[124:127], v4, s[20:23], s26 offen offset:3072 sc1\n"
        ".L7_qg:\n\t"
        "v_mov_b32 v7, 0\n\t"
        "ds_read_b128 v[8:11], v5 offset:2048\n\t"
        "ds_read_b128 v[12:15], v5 offset:10496\n\t"
        "ds_read_b128 v[16:19], v5 offset:18944\n\t"
        "ds_read_b128 v[20:23], v5 offset:27392\n\t"
        "s_waitcnt vmcnt(14) lgkmcnt(3)\n\t"
        "v_xor_b32 v64, s73, v64\n\t"
        "v_xor_b32 v65, v6, v65\n\t"
        "v_xor_b32 v68, s73, v68\n\t"
        "v_xor_b32 v69, v6, v69\n\t"
        "v_mfma_f32_16x16x32_bf16 v[24:27], v[64:67], v[8:11], 0\n\t"
        "s_nop 0\n\t"
        "v_mfma_f32_16x16x32_bf16 v[28:31], v[68:71], v[8:11], 0\n\t"
        "v_or3_b32 v7, v7, v64, v65\n\t"
        "ds_read_b128 v[8:11], v5 offset:35840\n\t"
        "s_waitcnt lgkmcnt(3)\n\t"
        "v_mfma_f32_16x16x32_bf16 v[32:35], v[64:67], v[12:15], 0\n\t"
        "v_mfma_f32_16x16x32_bf16 v[36:39], v[68:71], v[12:15], 0\n\t"
        "v_or3_b32 v7, v7, v68, v69\n\t"
        "ds_read_b128 v[12:15], v5 offset:2112\n\t"
        "s_waitcnt lgkmcnt(3)\n\t"
        "v_mfma_f32_16x16x32_bf16 v[40:43], v[64:67], v[16:19], 0\n\t"
        "v_mfma_f32_16x16x32_bf16 v[44:47], v[68:71], v[16:19], 0\n\t"
        "ds_read_b128 v[16:19], v5 offset:10560\n\t"
        "s_waitcnt lgkmcnt(3)\n\t"
        "v_mfma_f32_16x16x32_bf16 v[48:51], v[64:67], v[20:23], 0\n\t"
        "v_mfma_f32_16x16x32_bf16 v[52:55], v[68:71], v[20:23], 0\n\t"
        "ds_read_b128 v[20:23], v5 offset:19008\n\t"
        "s_waitcnt vmcnt(12) lgkmcnt(3)\n\t"
        "v_xor_b32 v72, s73, v72\n\t"
        "v_xor_b32 v73, v6, v73\n\t"
        "v_xor_b32 v76, s73, v76\n\t"
        "v_xor_b32 v77, v6, v77\n\t"
        "v_mfma_f32_16x16x32_bf16 v[56:59], v[64:67], v[8:11], 0\n\t"
        "v_mfma_f32_16x16x32_bf16 v[60:63], v[68:71], v[8:11], 0\n\t"
        "ds_read_b128 v[8:11], v5 offset:27456\n\t"
        "ds_read_b128 v[64:67], v5 offset:35904\n\t"
        "ds_read_b128 v[68:71], v5 offset:2176\n\t"
        "s_waitcnt lgkmcnt(4)\n\t"
        "v_mfma_f32_16x16x32_bf16 v[24:27], v[72:75], v[12:15], v[24:27]\n\t"
        "v_mfma_f32_16x16x32_bf16 v[28:31], v[76:79], v[12:15], v[28:31]\n\t"
        "v_or3_b32 v7, v7, v72, v73\n\t"
        "ds_read_b128 v[12:15], v5 offset:10624\n\t"
        "v_mfma_f32_16x16x32_bf16 v[32:35], v[72:75], v[16:19], v[32:35]\n\t"
        "v_mfma_f32_16x16x32_bf16 v[36:39], v[76:79], v[16:19], v[36:39]\n\t"
        "v_or3_b32 v7, v7, v76, v77\n\t"
        "ds_read_b128 v[16:19], v5 offset:19072\n\t"
        "s_waitcnt lgkmcnt(4)\n\t"
        "v_mfma_f32_16x16x32_bf16 v[40:43], v[72:75], v[20:23], v[40:43]\n\t"
        "v_mfma_f32_16x16x32_bf16 v[44:47], v[76:79], v[20:23], v[44:47]\n\t"
        "ds_read_b128 v[20:23], v5 offset:27520\n\t"
        "v_mfma_f32_16x16x32_bf16 v[48:51], v[72:75], v[8:11], v[48:51]\n\t"
        "v_mfma_f32_16x16x32_bf16 v[52:55], v[76:79], v[8:11], v[52:55]\n\t"
        "ds_read_b128 v[8:11], v5 offset:35968\n\t"
        "s_waitcnt vmcnt(10) lgkmcnt(4)\n\t"
        "v_xor_b32 v80, s73, v80\n\t"
        "v_xor_b32 v81, v6, v81\n\t"
        "v_xor_b32 v84, s73, v84\n\t"
        "v_xor_b32 v85, v6, v85\n\t"
        "v_mfma_f32_16x16x32_bf16 v[56:59], v[72:75], v[64:67], v[56:59]\n\t"
        "v_mfma_f32_16x16x32_bf16 v[60:63], v[76:79], v[64:67], v[60:63]\n\t"
        "ds_read_b128 v[64:67], v5 offset:2240\n\t"
        "ds_read_b128 v[72:75], v5 offset:10688\n\t"
        "ds_read_b128 v[76:79], v5 offset:19136\n\t"
        "v_mfma_f32_16x16x32_bf16 v[24:27], v[80:83], v[68:71], v[24:27]\n\t"
        "v_mfma_f32_16x16x32_bf16 v[28:31], v[84:87], v[68:71], v[28:31]\n\t"
        "v_or3_b32 v7, v7, v80, v81\n\t"
        "ds_read_b128 v[68:71], v5 offset:27584\n\t"
        "s_waitcnt lgkmcnt(6)\n\t"
        "v_mfma_f32_16x16x32_bf16 v[32:35], v[80:83], v[12:15], v[32:35]\n\t"
        "v_mfma_f32_16x16x32_bf16 v[36:39], v[84:87], v[12:15], v[36:39]\n\t"
        "v_or3_b32 v7, v7, v84, v85\n\t"
        "ds_read_b128 v[12:15], v5 offset:36032\n\t"
        "v_mfma_f32_16x16x32_bf16 v[40:43], v[80:83], v[16:19], v[40:43]\n\t"
        "v_mfma_f32_16x16x32_bf16 v[44:47], v[84:87], v[16:19], v[44:47]\n\t"
        "ds_read_b128 v[16:19], v5 offset:2304\n\t"
        "s_waitcnt lgkmcnt(6)\n\t"
        "v_mfma_f32_16x16x32_bf16 v[48:51], v[80:83], v[20:23], v[48:51]\n\t"
        "v_mfma_f32_16x16x32_bf16 v[52:55], v[84:87], v[20:23], v[52:55]\n\t"
        "ds_read_b128 v[20:23], v5 offset:10752\n\t"
        "s_waitcnt vmcnt(8) lgkmcnt(6)\n\t"
        "v_xor_b32 v88, s73, v88\n\t"
        "v_xor_b32 v89, v6, v89\n\t"
        "v_xor_b32 v92, s73, v92\n\t"
        "v_xor_b32 v93, v6, v93\n\t"
        "v_mfma_f32_16x16x32_bf16 v[56:59], v[80:83], v[8:11], v[56:59]\n\t"
        "v_mfma_f32_16x16x32_bf16 v[60:63], v[84:87], v[8:11], v[60:63]\n\t"
        "ds_read_b128 v[8:11], v5 offset:19200\n\t"
        "ds_read_b128 v[80:83], v5 offset:27648\n\t"
        "v_mfma_f32_16x16x32_bf16 v[24:27], v[88:91], v[64:67], v[24:27]\n\t"
        "v_mfma_f32_16x16x32_bf16 v[28:31], v[92:95], v[64:67], v[28:31]\n\t"
        "v_or3_b32 v7, v7, v88, v89\n\t"
        "ds_read_b128 v[84:87], v5 offset:36096\n\t"
        "s_waitcnt lgkmcnt(7)\n\t"
        "v_mfma_f32_16x16x32_bf16 v[32:35], v[88:91], v[72:75], v[32:35]\n\t"
        "v_mfma_f32_16x16x32_bf16 v[36:39], v[92:95], v[72:75], v[36:39]\n\t"
        "v_or3_b32 v7, v7, v92, v93\n\t"
        "ds_read_b128 v[64:67], v5 offset:2368\n\t"
        "v_mfma_f32_16x16x32_bf16 v[40:43], v[88:91], v[76:79], v[40:43]\n\t"
        "v_mfma_f32_16x16x32_bf16 v[44:47], v[92:95], v[76:79], v[44:47]\n\t"
        "ds_read_b128 v[72:75], v5 offset:10816\n\t"
        "s_waitcnt lgkmcnt(7)\n\t"
        "v_mfma_f32_16x16x32_bf16 v[48:51], v[88:91], v[68:71], v[48:51]\n\t"
        "v_mfma_f32_16x16x32_bf16 v[52:55], v[92:95], v[68:71], v[52:55]\n\t"
        "ds_read_b128 v[76:79], v5 offset:19264\n\t"
        "s_waitcnt vmcnt(6) lgkmcnt(7)\n\t"
        "v_xor_b32 v96, s73, v96\n\t"
        "v_xor_b32 v97, v6, v97\n\t"
        "v_xor_b32 v100, s73, v100\n\t"
        "v_xor_b32 v101, v6, v101\n\t"
        "v_mfma_f32_16x16x32_bf16 v[56:59], v[88:91], v[12:15], v[56:59]\n\t"
        "v_mfma_f32_16x16x32_bf16 v[60:63], v[92:95], v[12:15], v[60:63]\n\t"
        "ds_read_b128 v[68:71], v5 offset:27712\n\t"
        "v_mfma_f32_16x16x32_bf16 v[24:27], v[96:99], v[16:19], v[24:27]\n\t"
        "v_mfma_f32_16x16x32_bf16 v[28:31], v[100:103], v[16:19], v[28:31]\n\t"
        "v_or3_b32 v7, v7, v96, v97\n\t"
        "ds_read_b128 v[12:15], v5 offset:36160\n\t"
        "s_waitcnt lgkmcnt(7)\n\t"
        "v_mfma_f32_16x16x32_bf16 v[32:35], v[96:99], v[20:23], v[32:35]\n\t"
        "v_mfma_f32_16x16x32_bf16 v[36:39], v[100:103], v[20:23], v[36:39]\n\t"
        "v_or3_b32 v7, v7, v100, v101\n\t"
        "ds_read_b128 v[88:91], v5 offset:2432\n\t"
        "v_mfma_f32_16x16x32_bf16 v[40:43], v[96:99], v[8:11], v[40:43]\n\t"
        "v_mfma_f32_16x16x32_bf16 v[44:47], v[100:103], v[8:11], v[44:47]\n\t"
        "ds_read_b128 v[92:95], v5 offset:10880\n\t"
        "s_waitcnt lgkmcnt(7)\n\t"
        "v_mfma_f32_16x16x32_bf16 v[48:51], v[96:99], v[80:83], v[48:51]\n\t"
        "v_mfma_f32_16x16x32_bf16 v[52:55], v[100:103], v[80:83], v[52:55]\n\t"
        "ds_read_b128 v[16:19], v5 offset:19328\n\t"
        "s_waitcnt vmcnt(4) lgkmcnt(7)\n\t"
        "v_xor_b32 v104, s73, v104\n\t"
        "v_xor_b32 v105, v6, v105\n\t"
        "v_xor_b32 v108, s73, v108\n\t"
        "v_xor_b32 v109, v6, v109\n\t"
        "v_mfma_f32_16x16x32_bf16 v[56:59], v[96:99], v[84:87], v[56:59]\n\t"
        "v_mfma_f32_16x16x32_bf16 v[60:63], v[100:103], v[84:87], v[60:63]\n\t"
        "ds_read_b128 v[20:23], v5 offset:27776\n\t"
        "v_mfma_f32_16x16x32_bf16 v[24:27], v[104:107], v[64:67], v[24:27]\n\t"
        "v_mfma_f32_16x16x32_bf16 v[28:31], v[108:111], v[64:67], v[28:31]\n\t"
        "v_or3_b32 v7, v7, v104, v105\n\t"
        "ds_read_b128 v[8:11], v5 offset:36224\n\t"
        "s_waitcnt lgkmcnt(7)\n\t"
        "v_mfma_f32_16x16x32_bf16 v[32:35], v[104:107], v[72:75], v[32:35]\n\t"
        "v_mfma_f32_16x16x32_bf16 v[36:39], v[108:111], v[72:75], v[36:39]\n\t"
        "v_or3_b32 v7, v7, v108, v109\n\t"
        "ds_read_b128 v[80:83], v5 offset:2496\n\t"
        "v_mfma_f32_16x16x32_bf16 v[40:43], v[104:107], v[76:79], v[40:43]\n\t"
        "v_mfma_f32_16x16x32_bf16 v[44:47], v[108:111], v[76:79], v[44:47]\n\t"
        "ds_read_b128 v[84:87], v5 offset:10944\n\t"
        "s_waitcnt lgkmcnt(7)\n\t"
        "v_mfma_f32_16x16x32_bf16 v[48:51], v[104:107], v[68:71], v[48:51]\n\t"
        "v_mfma_f32_16x16x32_bf16 v[52:55], v[108:111], v[68:71], v[52:55]\n\t"
        "ds_read_b128 v[96:99], v5 offset:19392\n\t"
        "s_waitcnt vmcnt(2) lgkmcnt(7)\n\t"
        "v_xor_b32 v112, s73, v112\n\t"
        "v_xor_b32 v113, v6, v113\n\t"
        "v_xor_b32 v116, s73, v116\n\t"
        "v_xor_b32 v117, v6, v117\n\t"
        "v_mfma_f32_16x16x32_bf16 v[56:59], v[104:107], v[12:15], v[56:59]\n\t"
        "v_mfma_f32_16x16x32_bf16 v[60:63], v[108:111], v[12:15], v[60:63]\n\t"
        "ds_read_b128 v[100:103], v5 offset:27840\n\t"
        "v_mfma_f32_16x16x32_bf16 v[24:27], v[112:115], v[88:91], v[24:27]\n\t"
        "v_mfma_f32_16x16x32_bf16 v[28:31], v[116:119], v[88:91], v[28:31]\n\t"
        "v_or3_b32 v7, v7, v112, v113\n\t"
        "ds_read_b128 v[64:67], v5 offset:36288\n\t"
        "s_waitcnt lgkmcnt(7)\n\t"
        "v_mfma_f32_16x16x32_bf16 v[32:35], v[112:115], v[92:95], v[32:35]\n\t"
        "v_mfma_f32_16x16x32_bf16 v[36:39], v[116:119], v[92:95], v[36:39]\n\t"
        "v_or3_b32 v7, v7, v116, v117\n\t"
        "v_mfma_f32_16x16x32_bf16 v[40:43], v[112:115], v[16:19], v[40:43]\n\t"
        "v_mfma_f32_16x16x32_bf16 v[44:47], v[116:119], v[16:19], v[44:47]\n\t"
        "s_waitcnt lgkmcnt(5)\n\t"
        "v_mfma_f32_16x16x32_bf16 v[48:51], v[112:115], v[20:23], v[48:51]\n\t"
        "v_mfma_f32_16x16x32_bf16 v[52:55], v[116:119], v[20:23], v[52:55]\n\t"
        "s_waitcnt vmcnt(0) lgkmcnt(4)\n\t"
        "v_xor_b32 v120, s73, v120\n\t"
        "v_xor_b32 v121, v6, v121\n\t"
        "v_xor_b32 v124, s73, v124\n\t"
        "v_xor_b32 v125, v6, v125\n\t"
        "v_mfma_f32_16x16x32_bf16 v[56:59], v[112:115], v[8:11], v[56:59]\n\t"
        "v_mfma_f32_16x16x32_bf16 v[60:63], v[116:119], v[8:11], v[60:63]\n\t"
        "v_mfma_f32_16x16x32_bf16 v[24:27], v[120:123], v[80:83], v[24:27]\n\t"
        "v_mfma_f32_16x16x32_bf16 v[28:31], v[124:127], v[80:83], v[28:31]\n\t"
        "v_or3_b32 v7, v7, v120, v121\n\t"
        "s_waitcnt lgkmcnt(3)\n\t"
        "v_mfma_f32_16x16x32_bf16 v[32:35], v[120:123], v[84:87], v[32:35]\n\t"
        "v_mfma_f32_16x16x32_bf16 v[36:39], v[124:127], v[84:87], v[36:39]\n\t"
        "v_or3_b32 v7, v7, v124, v125\n\t"
        "v_and_b32 v7, s49, v7\n\t"
        "v_cmp_ne_u32 vcc, 0, v7\n\t"
        "s_cbranch_vccnz .L7_qr\n"
        ".L7_qd:\n\t"
        "s_mov_b64 exec, 1\n\t"
        "ds_add_u32 v2, v3 offset:12\n\t"
        "s_mov_b64 exec, -1\n\t"
        "v_and_b32 v6, 15, v1\n\t"
        "v_mul_u32_u24 v6, 272, v6\n\t"
        "v_and_b32 v7, 48, v1\n\t"
        "s_lshl_b32 s78, s80, 6\n\t"
        "v_add3_u32 v6, v6, v7, s78\n\t"
        "s_waitcnt lgkmcnt(2)\n\t"
        "v_mfma_f32_16x16x32_bf16 v[40:43], v[120:123], v[96:99], v[40:43]\n\t"
        "v_mfma_f32_16x16x32_bf16 v[44:47], v[124:127], v[96:99], v[44:47]\n\t"
        "v_cvt_pk_bf16_f32 v24, v24, v25\n\t"
        "v_cvt_pk_bf16_f32 v25, v26, v27\n\t"
        "v_cvt_pk_bf16_f32 v26, v28, v29\n\t"
        "v_cvt_pk_bf16_f32 v27, v30, v31\n\t"
        "s_waitcnt lgkmcnt(0)\n\t"
        "v_mfma_f32_16x16x32_bf16 v[48:51], v[120:123], v[100:103], v[48:51]\n\t"
        "v_mfma_f32_16x16x32_bf16 v[52:55], v[124:127], v[100:103], v[52:55]\n\t"
        "v_cvt_pk_bf16_f32 v32, v32, v33\n\t"
        "v_cvt_pk_bf16_f32 v33, v34, v35\n\t"
        "v_cvt_pk_bf16_f32 v34, v36, v37\n\t"
        "v_cvt_pk_bf16_f32 v35, v38, v39\n\t"
        "v_mfma_f32_16x16x32_bf16 v[56:59], v[120:123], v[64:67], v[56:59]\n\t"
        "v_mfma_f32_16x16x32_bf16 v[60:63], v[124:127], v[64:67], v[60:63]\n\t"
        "v_cvt_pk_bf16_f32 v40, v40, v41\n\t"
        "v_cvt_pk_bf16_f32 v41, v42, v43\n\t"
        "v_cvt_pk_bf16_f32 v42, v44, v45\n\t"
        "v_cvt_pk_bf16_f32 v43, v46, v47\n\t"
        "ds_read_b32 v0, v2 offset:12\n\t"
        "v_cvt_pk_bf16_f32 v48, v48, v49\n\t"
        "v_cvt_pk_bf16_f32 v49, v50, v51\n\t"
        "v_cvt_pk_bf16_f32 v50, v52, v53\n\t"
        "v_cvt_pk_bf16_f32 v51, v54, v55\n\t"
        "v_cvt_pk_bf16_f32 v56, v56, v57\n\t"
        "v_cvt_pk_bf16_f32 v57, v58, v59\n\t"
        "v_cvt_pk_bf16_f32 v58, v60, v61\n\t"
        "v_cvt_pk_bf16_f32 v59, v62, v63\n\t"
        "s_waitcnt lgkmcnt(0)\n\t"
        "v_cmp_gt_u16 vcc, 4, v0\n\t"
        "s_cbranch_vccz .L7_qx\n"
        ".L7_w58:\n\t"
        "ds_read_b32 v0, v2 offset:12\n\t"
        "s_waitcnt lgkmcnt(0)\n\t"
        "v_cmp_gt_u16 vcc, 4, v0\n\t"
        "s_cbranch_vccz .L7_d59\n\t"
        "s_sleep 0\n\t"
        "s_sub_i32 s83, s83, 1\n\t"
        "s_cmp_gt_i32 s83, 0\n\t"
        "s_cbranch_scc1 .L7_w58\n"
        ".L7_d59:\n"
        ".L7_qx:\n\t"
        "ds_write_b128 v6, v[24:27] offset:2048\n\t"
        "ds_write_b128 v6, v[32:35] offset:6400\n\t"
        "ds_write_b128 v6, v[40:43] offset:10752\n\t"
        "ds_write_b128 v6, v[48:51] offset:15104\n\t"
        "ds_write_b128 v6, v[56:59] offset:19456\n\t"
        "s_lshl_b32 s78, s69, 9\n\t"
        "v_lshl_add_u32 v7, v1, 3, s78\n\t"
        "v_mov_b64 v[8:9], 0\n\t"
        "ds_write_b64 v7, v[8:9] offset:34048\n\t"
        "s_mov_b64 exec, 1\n\t"
        "ds_add_u32 v2, v3 offset:16\n\t"
        "s_mov_b64 exec, -1\n\t"
        "v_lshrrev_b32 v10, 3, v1\n\t"
        "v_lshlrev_b32 v10, 9, v10\n\t"
        "v_and_b32 v11, 7, v1\n\t"
        "v_lshl_add_u32 v10, v11, 4, v10\n\t"
        "v_and_b32 v11, 16, v1\n\t"
        "v_lshlrev_b32 v11, 2, v11\n\t"
        "v_xor_b32 v10, v10, v11\n\t"
        "s_load_dwordx4 s[24:27], %2, 32\n\t"
        "s_lshl_b32 s78, s80, 14\n\t"
        "s_add_u32 m0, s78, 68864\n\t"
        "s_lshl_b32 s78, s70, 9\n\t"
        "s_add_u32 s78, s78, s80\n\t"
        "s_lshl_b32 s78, s78, 7\n"
        ".L7_w60:\n\t"
        "ds_read_b32 v0, v2 offset:24\n\t"
        "s_waitcnt lgkmcnt(0)\n\t"
        "v_cmp_gt_u16 vcc, 8, v0\n\t"
        "s_cbranch_vccz .L7_d61\n\t"
        "s_sleep 1\n\t"
        "s_sub_i32 s83, s83, 1\n\t"
        "s_cmp_gt_i32 s83, 0\n\t"
        "s_cbranch_scc1 .L7_w60\n"
        ".L7_d61:\n"
        ".L7_w62:\n\t"
        "ds_read_b32 v0, v2 offset:88\n\t"
        "s_waitcnt lgkmcnt(0)\n\t"
        "v_cmp_gt_u16 vcc, 16, v0\n\t"
        "s_cbranch_vccz .L7_d63\n\t"
        "s_sleep 1\n\t"
        "s_sub_i32 s83, s83, 1\n\t"
        "s_cmp_gt_i32 s83, 0\n\t"
        "s_cbranch_scc1 .L7_w62\n"
        ".L7_d63:\n\t"
        "s_setprio 3\n\t"
        "buffer_load_dwordx4 v10, s[24:27], s78 offen lds\n\t"
        "s_add_u32 m0, m0, 0x400\n\t"
        "s_add_u32 s78, s78, 0x1000\n\t"
        "buffer_load_dwordx4 v10, s[24:27], s78 offen lds\n\t"
        "s_add_u32 m0, m0, 0x400\n\t"
        "s_add_u32 s78, s78, 0x1000\n\t"
        "buffer_load_dwordx4 v10, s[24:27], s78 offen lds\n\t"
        "s_add_u32 m0, m0, 0x400\n\t"
        "s_add_u32 s78, s78, 0x1000\n\t"
        "buffer_load_dwordx4 v10, s[24:27], s78 offen lds\n\t"
        "s_add_u32 m0, m0, 0x400\n\t"
        "s_add_u32 s78, s78, 0x1000\n\t"
        "buffer_load_dwordx4 v10, s[24:27], s78 offen lds\n\t"
        "s_add_u32 m0, m0, 0x400\n\t"
        "s_add_u32 s78, s78, 0x1000\n\t"
        "buffer_load_dwordx4 v10, s[24:27], s78 offen lds\n\t"
        "s_add_u32 m0, m0, 0x400\n\t"
        "s_add_u32 s78, s78, 0x1000\n\t"
        "buffer_load_dwordx4 v10, s[24:27], s78 offen lds\n\t"
        "s_add_u32 m0, m0, 0x400\n\t"
        "s_add_u32 s78, s78, 0x1000\n\t"
        "buffer_load_dwordx4 v10, s[24:27], s78 offen lds\n\t"
        "s_add_u32 m0, m0, 0x400\n\t"
        "s_add_u32 s78, s78, 0x1000\n\t"
        "buffer_load_dwordx4 v10, s[24:27], s78 offen lds\n\t"
        "s_add_u32 m0, m0, 0x400\n\t"
        "s_add_u32 s78, s78, 0x1000\n\t"
        "buffer_load_dwordx4 v10, s[24:27], s78 offen lds\n\t"
        "s_add_u32 m0, m0, 0x400\n\t"
        "s_add_u32 s78, s78, 0x1000\n\t"
        "buffer_load_dwordx4 v10, s[24:27], s78 offen lds\n\t"
        "s_add_u32 m0, m0, 0x400\n\t"
        "s_add_u32 s78, s78, 0x1000\n\t"
        "buffer_load_dwordx4 v10, s[24:27], s78 offen lds\n\t"
        "s_add_u32 m0, m0, 0x400\n\t"
        "s_add_u32 s78, s78, 0x1000\n\t"
        "buffer_load_dwordx4 v10, s[24:27], s78 offen lds\n\t"
        "s_add_u32 m0, m0, 0x400\n\t"
        "s_add_u32 s78, s78, 0x1000\n\t"
        "buffer_load_dwordx4 v10, s[24:27], s78 offen lds\n\t"
        "s_add_u32 m0, m0, 0x400\n\t"
        "s_add_u32 s78, s78, 0x1000\n\t"
        "buffer_load_dwordx4 v10, s[24:27], s78 offen lds\n\t"
        "s_add_u32 m0, m0, 0x400\n\t"
        "s_add_u32 s78, s78, 0x1000\n\t"
        "buffer_load_dwordx4 v10, s[24:27], s78 offen lds\n\t"
        "s_waitcnt vmcnt(0)\n"
        ".L7_vw:\n\t"
        "s_mov_b64 exec, 1\n\t"
        "ds_add_u32 v2, v3 offset:28\n\t"
        "s_mov_b64 exec, -1\n\t"
        "s_setprio 0\n"
        ".L7_w64:\n\t"
        "ds_read_b32 v0, v2 offset:36\n\t"
        "s_waitcnt lgkmcnt(0)\n\t"
        "v_cmp_gt_u16 vcc, 4, v0\n\t"
        "s_cbranch_vccz .L7_d65\n\t"
        "s_sleep 2\n\t"
        "s_sub_i32 s83, s83, 1\n\t"
        "s_cmp_gt_i32 s83, 0\n\t"
        "s_cbranch_scc1 .L7_w64\n"
        ".L7_d65:\n\t"
        "v_cmp_lt_u32 vcc, 0xfffff, v0\n\t"
        "s_cbranch_vccnz .L7_exh\n\t"
        "s_cmp_eq_u32 s69, 0\n\t"
        "s_cbranch_scc0 .L7_hz\n\t"
        "s_lshr_b32 s47, s68, 20\n\t"
        "s_add_u32 s47, s47, s68\n\t"
        "s_and_b32 s47, s47, 0xfff\n\t"
        "s_mul_i32 s46, s68, 0x9e377000\n\t"
        "s_or_b32 s46, s46, s47\n\t"
        "v_mov_b32 v4, s46\n\t"
        "s_xor_b32 s44, s70, 1\n\t"
        "s_lshl1_add_u32 s44, %3, s44\n\t"
        "s_lshl_b32 s44, s44, 10\n\t"
        "s_add_u32 s44, s44, 262144\n\t"
        "s_xor_b32 s45, s44, 0xc00\n"
        ".L7_w66:\n\t"
        "ds_read_b32 v0, v2 offset:56\n\t"
        "s_waitcnt lgkmcnt(0)\n\t"
        "v_cmp_gt_u16 vcc, 3, v0\n\t"
        "s_cbranch_vccz .L7_d67\n\t"
        "s_sleep 8\n\t"
        "s_sub_i32 s83, s83, 1\n\t"
        "s_cmp_gt_i32 s83, 0\n\t"
        "s_cbranch_scc1 .L7_w66\n"
        ".L7_d67:\n\t"
        "s_setprio 3\n"
        ".L7_w68:\n\t"
        "ds_read_b32 v0, v2 offset:96\n\t"
        "s_waitcnt lgkmcnt(0)\n\t"
        "v_cmp_gt_u16 vcc, 7, v0\n\t"
        "s_cbranch_vccz .L7_d69\n\t"
        "s_sleep 1\n\t"
        "s_sub_i32 s83, s83, 1\n\t"
        "s_cmp_gt_i32 s83, 0\n\t"
        "s_cbranch_scc1 .L7_w68\n"
        ".L7_d69:\n\t"
        "s_mov_b64 exec, 1\n\t"
        "buffer_store_dword v4, off, s[20:23], s44 offset:12 sc1\n\t"
        "s_mov_b32 s83, 64\n\t"
        "s_sleep 4\n"
        ".L7_h1:\n\t"
        "s_buffer_load_dword s47, s[20:23], s45 offset:12 glc\n\t"
        "s_waitcnt lgkmcnt(0)\n\t"
        "s_cmp_eq_u32 s47, s46\n\t"
        "s_cbranch_scc1 .L7_h2\n\t"
        "s_sleep 3\n\t"
        "s_sub_i32 s83, s83, 1\n\t"
        "s_cmp_gt_i32 s83, 0\n\t"
        "s_cbranch_scc1 .L7_h1\n"
        ".L7_h2:\n\t"
        "s_mov_b64 exec, 1\n\t"
        "ds_add_u32 v2, v3 offset:100\n\t"
        "s_mov_b64 exec, -1\n"
        ".L7_hz:\n\t"
        "s_endpgm\n"
        ".L7_exh:\n\t"
        "s_mov_b64 exec, 1\n\t"
        "ds_add_u32 v2, v3 offset:100\n\t"
        "s_mov_b64 exec, -1\n\t"
        "s_branch .L7_exit\n"
        ".L7_O2:\n\t"
        "s_load_dwordx8 s[24:31], %2, 32\n\t"
        "s_load_dwordx4 s[60:63], %2, 176\n\t"
        "v_lshrrev_b32 v6, 4, v1\n\t"
        "v_lshlrev_b32 v7, 2, v6\n\t"
        "v_and_b32 v8, 15, v1\n\t"
        "s_lshl_b32 s78, s70, 8\n\t"
        "v_lshl_add_u32 v9, v8, 4, s78\n\t"
        "v_lshrrev_b32 v10, 3, v1\n\t"
        "v_lshlrev_b32 v10, 9, v10\n\t"
        "v_and_b32 v11, 7, v1\n\t"
        "v_lshl_add_u32 v10, v11, 4, v10\n\t"
        "v_and_b32 v11, 16, v1\n\t"
        "v_lshlrev_b32 v11, 2, v11\n\t"
        "v_xor_b32 v10, v10, v11\n\t"
        "s_add_u32 s81, s69, s76\n\t"
        "s_and_b32 s81, s81, 7\n\t"
        "s_lshr_b32 s78, s81, 1\n\t"
        "s_lshl_b32 s78, s78, 7\n\t"
        "s_and_b32 s79, s81, 1\n\t"
        "s_lshl_b32 s79, s79, 15\n\t"
        "s_add_u32 s78, s78, s79\n\t"
        "s_lshl_b32 s79, s70, 16\n\t"
        "s_add_u32 s78, s78, s79\n\t"
        "v_add_u32 v10, s78, v10\n\t"
        "s_lshl_b32 s82, s81, 13\n\t"
        "s_add_u32 s82, s82, 68864\n\t"
        "v_bfe_u32 v12, v1, 2, 2\n\t"
        "v_lshlrev_b32 v11, 10, v6\n\t"
        "v_lshl_add_u32 v11, v12, 7, v11\n\t"
        "v_lshrrev_b32 v12, 1, v12\n\t"
        "s_and_b32 s78, s81, 1\n\t"
        "v_xor_b32 v12, s78, v12\n\t"
        "v_lshl_add_u32 v11, v12, 6, v11\n\t"
        "v_and_b32 v12, 3, v1\n\t"
        "v_lshl_add_u32 v11, v12, 4, v11\n\t"
        "s_lshr_b32 s78, s81, 1\n\t"
        "s_lshl_b32 s78, s78, 14\n\t"
        "s_add_u32 s78, s78, 68864\n\t"
        "v_add_u32 v23, s78, v11\n\t"
        "s_waitcnt vmcnt(0)\n\t"
        "ds_bpermute_b32 v12, v7, v5 offset:0\n\t"
        "ds_bpermute_b32 v13, v7, v5 offset:16\n\t"
        "ds_bpermute_b32 v14, v7, v5 offset:32\n\t"
        "ds_bpermute_b32 v15, v7, v5 offset:48\n\t"
        "ds_bpermute_b32 v16, v7, v5 offset:64\n\t"
        "ds_bpermute_b32 v17, v7, v5 offset:80\n\t"
        "ds_bpermute_b32 v18, v7, v5 offset:96\n\t"
        "ds_bpermute_b32 v19, v7, v5 offset:112\n\t"
        "ds_bpermute_b32 v20, v7, v5 offset:128\n\t"
        "ds_bpermute_b32 v21, v7, v5 offset:144\n\t"
        "s_waitcnt lgkmcnt(0)\n\t"
        "v_lshl_add_u32 v12, v12, 9, v9\n\t"
        "v_lshl_add_u32 v13, v13, 9, v9\n\t"
        "v_lshl_add_u32 v14, v14, 9, v9\n\t"
        "v_lshl_add_u32 v15, v15, 9, v9\n\t"
        "v_lshl_add_u32 v16, v16, 9, v9\n\t"
        "v_lshl_add_u32 v17, v17, 9, v9\n\t"
        "v_lshl_add_u32 v18, v18, 9, v9\n\t"
        "v_lshl_add_u32 v19, v19, 9, v9\n\t"
        "v_lshl_add_u32 v20, v20, 9, v9\n\t"
        "v_lshl_add_u32 v21, v21, 9, v9\n\t"
        "s_setprio 0\n"
        ".L7_w1:\n\t"
        "ds_read_b32 v0, v2 offset:44\n\t"
        "s_waitcnt lgkmcnt(0)\n\t"
        "v_cmp_gt_u16 vcc, 2, v0\n\t"
        "s_cbranch_vccz .L7_d2\n\t"
        "s_sleep 1\n\t"
        "s_sub_i32 s83, s83, 1\n\t"
        "s_cmp_gt_i32 s83, 0\n\t"
        "s_cbranch_scc1 .L7_w1\n"
        ".L7_d2:\n"
        ".L7_w3:\n\t"
        "ds_read_b64 v[4:5], v2 offset:72\n\t"
        "s_waitcnt lgkmcnt(0)\n\t"
        "v_cmp_gt_u16 vcc, 2, v4\n\t"
        "s_cbranch_vccz .L7_d4\n\t"
        "s_sleep 1\n\t"
        "s_sub_i32 s83, s83, 1\n\t"
        "s_cmp_gt_i32 s83, 0\n\t"
        "s_cbranch_scc1 .L7_w3\n"
        ".L7_d4:\n\t"
        "v_readfirstlane_b32 s68, v5\n\t"
        "s_add_u32 m0, s82, 0\n\t"
        "s_mov_b32 s78, 0\n\t"
        "buffer_load_dwordx4 v10, s[28:31], s78 offen lds\n\t"
        "s_add_u32 m0, m0, 0x400\n\t"
        "s_add_u32 s78, s78, 0x1000\n\t"
        "buffer_load_dwordx4 v10, s[28:31], s78 offen lds\n\t"
        "s_add_u32 m0, m0, 0x400\n\t"
        "s_add_u32 s78, s78, 0x1000\n\t"
        "buffer_load_dwordx4 v10, s[28:31], s78 offen lds\n\t"
        "s_add_u32 m0, m0, 0x400\n\t"
        "s_add_u32 s78, s78, 0x1000\n\t"
        "buffer_load_dwordx4 v10, s[28:31], s78 offen lds\n\t"
        "s_add_u32 m0, m0, 0x400\n\t"
        "s_add_u32 s78, s78, 0x1000\n\t"
        "buffer_load_dwordx4 v10, s[28:31], s78 offen lds\n\t"
        "s_add_u32 m0, m0, 0x400\n\t"
        "s_add_u32 s78, s78, 0x1000\n\t"
        "buffer_load_dwordx4 v10, s[28:31], s78 offen lds\n\t"
        "s_add_u32 m0, m0, 0x400\n\t"
        "s_add_u32 s78, s78, 0x1000\n\t"
        "buffer_load_dwordx4 v10, s[28:31], s78 offen lds\n\t"
        "s_add_u32 m0, m0, 0x400\n\t"
        "s_add_u32 s78, s78, 0x1000\n\t"
        "buffer_load_dwordx4 v10, s[28:31], s78 offen lds\n\t"
        "s_mov_b64 exec, 1\n\t"
        "ds_add_u32 v2, v3 offset:88\n\t"
        "s_mov_b64 exec, -1\n\t"
        "s_mov_b32 s36, 0x22222222\n\t"
        "s_mov_b32 s37, s36\n\t"
        "s_lshl_b64 s[38:39], s[36:37], 1\n\t"
        "s_lshl_b64 s[40:41], s[36:37], 2\n\t"
        "s_and_b32 s44, s68, 0xfff\n\t"
        "s_xor_b32 s45, s44, 0x5a5\n\t"
        "s_lshr_b32 s46, s68, 12\n\t"
        "s_lshr_b32 s47, s68, 20\n\t"
        "s_add_u32 s46, s46, s68\n\t"
        "s_and_b32 s46, s46, 0xfff\n\t"
        "s_add_u32 s47, s47, s68\n\t"
        "s_and_b32 s47, s47, 0xfff\n\t"
        "v_mov_b32 v22, s44\n\t"
        "v_mov_b32 v0, s45\n\t"
        "v_cndmask_b32 v22, v22, v0, s[36:37]\n\t"
        "v_mov_b32 v0, s46\n\t"
        "v_cndmask_b32 v22, v22, v0, s[38:39]\n\t"
        "v_mov_b32 v0, s47\n\t"
        "v_cndmask_b32 v22, v22, v0, s[40:41]\n\t"
        "s_lshl3_add_u32 s42, %3, s77\n\t"
        "s_lshl_b32 s42, s42, 8\n\t"
        "s_add_u32 s42, s42, 262144\n\t"
        "s_xor_b32 s43, s42, 0x800\n\t"
        "s_movk_i32 s48, 0xfff\n\t"
        "s_waitcnt vmcnt(0)\n\t"
        "s_mov_b64 exec, 1\n\t"
        "ds_add_u32 v2, v3 offset:20\n\t"
        "s_mov_b64 exec, -1\n"
        ".L7_w5:\n\t"
        "ds_read_b32 v0, v2 offset:20\n\t"
        "s_waitcnt lgkmcnt(0)\n\t"
        "v_cmp_gt_u16 vcc, 8, v0\n\t"
        "s_cbranch_vccz .L7_d6\n\t"
        "s_sleep 1\n\t"
        "s_sub_i32 s83, s83, 1\n\t"
        "s_cmp_gt_i32 s83, 0\n\t"
        "s_cbranch_scc1 .L7_w5\n"
        ".L7_d6:\n\t"
        "ds_read_b64_tr_b16 v[64:65], v23 offset:0\n\t"
        "ds_read_b64_tr_b16 v[66:67], v23 offset:512\n\t"
        "ds_read_b64_tr_b16 v[68:69], v23 offset:8\n\t"
        "ds_read_b64_tr_b16 v[70:71], v23 offset:520\n\t"
        "ds_read_b64_tr_b16 v[72:73], v23 offset:4096\n\t"
        "ds_read_b64_tr_b16 v[74:75], v23 offset:4608\n\t"
        "ds_read_b64_tr_b16 v[76:77], v23 offset:4104\n\t"
        "ds_read_b64_tr_b16 v[78:79], v23 offset:4616\n\t"
        "ds_read_b64_tr_b16 v[80:81], v23 offset:8192\n\t"
        "ds_read_b64_tr_b16 v[82:83], v23 offset:8704\n\t"
        "ds_read_b64_tr_b16 v[84:85], v23 offset:8200\n\t"
        "ds_read_b64_tr_b16 v[86:87], v23 offset:8712\n\t"
        "ds_read_b64_tr_b16 v[88:89], v23 offset:12288\n\t"
        "ds_read_b64_tr_b16 v[90:91], v23 offset:12800\n\t"
        "s_waitcnt lgkmcnt(10)\n\t"
        "ds_read_b64_tr_b16 v[92:93], v23 offset:12296\n\t"
        "ds_read_b64_tr_b16 v[94:95], v23 offset:12808\n\t"
        "s_mov_b64 exec, 1\n\t"
        "ds_add_u32 v2, v3 offset:24\n\t"
        "s_mov_b64 exec, -1\n\t"
        "buffer_load_dwordx4 v[24:27], v12, s[16:19], 0 offen\n\t"
        "buffer_load_dwordx4 v[28:31], v13, s[16:19], 0 offen\n\t"
        "buffer_load_dwordx4 v[32:35], v14, s[16:19], 0 offen\n\t"
        "buffer_load_dwordx4 v[36:39], v15, s[16:19], 0 offen\n\t"
        "buffer_load_dwordx4 v[40:43], v16, s[16:19], 0 offen\n\t"
        "buffer_load_dwordx4 v[44:47], v17, s[16:19], 0 offen\n\t"
        "buffer_load_dwordx4 v[48:51], v18, s[16:19], 0 offen\n\t"
        "buffer_load_dwordx4 v[52:55], v19, s[16:19], 0 offen\n\t"
        "buffer_load_dwordx4 v[56:59], v20, s[16:19], 0 offen\n\t"
        "buffer_load_dwordx4 v[60:63], v21, s[16:19], 0 offen\n\t"
        "s_mov_b64 exec, 1\n\t"
        "ds_add_u32 v2, v3 offset:88\n\t"
        "s_mov_b64 exec, -1\n"
        ".L7_o2:\n\t"
        "s_setprio 2\n\t"
        "s_mul_i32 s78, s80, 272\n\t"
        "v_lshl_add_u32 v21, v8, 4, s78\n"
        ".L7_w7:\n\t"
        "ds_read_b32 v0, v2 offset:16\n\t"
        "s_waitcnt lgkmcnt(0)\n\t"
        "v_cmp_gt_u16 vcc, 4, v0\n\t"
        "s_cbranch_vccz .L7_d8\n\t"
        "s_sleep 1\n\t"
        "s_sub_i32 s83, s83, 1\n\t"
        "s_cmp_gt_i32 s83, 0\n\t"
        "s_cbranch_scc1 .L7_w7\n"
        ".L7_d8:\n\t"
        "ds_read_b128 v[96:99], v21 offset:2048\n\t"
        "ds_read_b128 v[100:103], v21 offset:2320\n\t"
        "ds_read_b128 v[104:107], v21 offset:2592\n\t"
        "ds_read_b128 v[108:111], v21 offset:2864\n\t"
        "ds_read_b128 v[112:115], v21 offset:3136\n\t"
        "ds_read_b128 v[116:119], v21 offset:3408\n\t"
        "ds_read_b128 v[120:123], v21 offset:3680\n\t"
        "ds_read_b128 v[124:127], v21 offset:3952\n\t"
        "ds_read_b128 v[4:7], v21 offset:4224\n\t"
        "ds_read_b128 v[8:11], v21 offset:4496\n\t"
        "s_waitcnt vmcnt(0)\n\t"
        "s_waitcnt lgkmcnt(8)\n\t"
        "v_dot2_f32_bf16 v12, v24, v96, 0\n\t"
        "v_dot2_f32_bf16 v13, v28, v100, 0\n\t"
        "v_dot2_f32_bf16 v12, v25, v97, v12\n\t"
        "v_dot2_f32_bf16 v13, v29, v101, v13\n\t"
        "v_dot2_f32_bf16 v12, v26, v98, v12\n\t"
        "v_dot2_f32_bf16 v13, v30, v102, v13\n\t"
        "v_dot2_f32_bf16 v12, v27, v99, v12\n\t"
        "v_dot2_f32_bf16 v13, v31, v103, v13\n\t"
        "s_waitcnt lgkmcnt(6)\n\t"
        "v_dot2_f32_bf16 v14, v32, v104, 0\n\t"
        "v_dot2_f32_bf16 v15, v36, v108, 0\n\t"
        "v_dot2_f32_bf16 v14, v33, v105, v14\n\t"
        "v_dot2_f32_bf16 v15, v37, v109, v15\n\t"
        "v_dot2_f32_bf16 v14, v34, v106, v14\n\t"
        "v_dot2_f32_bf16 v15, v38, v110, v15\n\t"
        "v_dot2_f32_bf16 v14, v35, v107, v14\n\t"
        "v_dot2_f32_bf16 v15, v39, v111, v15\n\t"
        "s_waitcnt lgkmcnt(4)\n\t"
        "v_dot2_f32_bf16 v16, v40, v112, 0\n\t"
        "v_dot2_f32_bf16 v17, v44, v116, 0\n\t"
        "v_dot2_f32_bf16 v16, v41, v113, v16\n\t"
        "v_dot2_f32_bf16 v17, v45, v117, v17\n\t"
        "v_dot2_f32_bf16 v16, v42, v114, v16\n\t"
        "v_dot2_f32_bf16 v17, v46, v118, v17\n\t"
        "v_dot2_f32_bf16 v16, v43, v115, v16\n\t"
        "v_dot2_f32_bf16 v17, v47, v119, v17\n\t"
        "s_waitcnt lgkmcnt(2)\n\t"
        "v_dot2_f32_bf16 v18, v48, v120, 0\n\t"
        "v_dot2_f32_bf16 v19, v52, v124, 0\n\t"
        "v_dot2_f32_bf16 v18, v49, v121, v18\n\t"
        "v_dot2_f32_bf16 v19, v53, v125, v19\n\t"
        "v_dot2_f32_bf16 v18, v50, v122, v18\n\t"
        "v_dot2_f32_bf16 v19, v54, v126, v19\n\t"
        "v_dot2_f32_bf16 v18, v51, v123, v18\n\t"
        "v_dot2_f32_bf16 v19, v55, v127, v19\n\t"
        "s_waitcnt lgkmcnt(0)\n\t"
        "v_dot2_f32_bf16 v20, v56, v4, 0\n\t"
        "v_dot2_f32_bf16 v21, v60, v8, 0\n\t"
        "v_dot2_f32_bf16 v20, v57, v5, v20\n\t"
        "v_dot2_f32_bf16 v21, v61, v9, v21\n\t"
        "v_dot2_f32_bf16 v20, v58, v6, v20\n\t"
        "v_dot2_f32_bf16 v21, v62, v10, v21\n\t"
        "v_dot2_f32_bf16 v20, v59, v7, v20\n\t"
        "v_dot2_f32_bf16 v21, v63, v11, v21\n\t"
        "v_add_f32_dpp v12, v12, v12 row_ror:8 row_mask:0xf bank_mask:0x3\n\t"
        "v_add_f32_dpp v14, v14, v14 row_ror:8 row_mask:0xf bank_mask:0x3\n\t"
        "v_add_f32_dpp v16, v16, v16 row_ror:8 row_mask:0xf bank_mask:0x3\n\t"
        "v_add_f32_dpp v18, v18, v18 row_ror:8 row_mask:0xf bank_mask:0x3\n\t"
        "v_add_f32_dpp v20, v20, v20 row_ror:8 row_mask:0xf bank_mask:0x3\n\t"
        "v_add_f32_dpp v12, v13, v13 row_ror:8 row_mask:0xf bank_mask:0xc\n\t"
        "v_add_f32_dpp v14, v15, v15 row_ror:8 row_mask:0xf bank_mask:0xc\n\t"
        "v_add_f32_dpp v16, v17, v17 row_ror:8 row_mask:0xf bank_mask:0xc\n\t"
        "v_add_f32_dpp v18, v19, v19 row_ror:8 row_mask:0xf bank_mask:0xc\n\t"
        "v_add_f32_dpp v20, v21, v21 row_ror:8 row_mask:0xf bank_mask:0xc\n\t"
        "v_add_f32_dpp v12, v12, v12 row_half_mirror row_mask:0xf bank_mask:0x5\n\t"
        "v_add_f32_dpp v16, v16, v16 row_half_mirror row_mask:0xf bank_mask:0x5\n\t"
        "v_add_f32_dpp v20, v20, v20 row_half_mirror row_mask:0xf bank_mask:0xf\n\t"
        "v_add_f32_dpp v12, v14, v14 row_half_mirror row_mask:0xf bank_mask:0xa\n\t"
        "v_add_f32_dpp v16, v18, v18 row_half_mirror row_mask:0xf bank_mask:0xa\n\t"
        "v_add_f32_dpp v20, v20, v20 quad_perm:[1,0,3,2] row_mask:0xf bank_mask:0xf\n\t"
        "v_add_f32_dpp v12, v12, v12 quad_perm:[1,0,3,2] row_mask:0xf bank_mask:0xf\n\t"
        "v_add_f32_dpp v16, v16, v16 quad_perm:[1,0,3,2] row_mask:0xf bank_mask:0xf\n\t"
        "v_add_f32_dpp v20, v20, v20 quad_perm:[2,3,0,1] row_mask:0xf bank_mask:0xf\n\t"
        "v_add_f32_dpp v12, v12, v12 quad_perm:[2,3,0,1] row_mask:0xf bank_mask:0xf\n\t"
        "v_add_f32_dpp v16, v16, v16 quad_perm:[2,3,0,1] row_mask:0xf bank_mask:0xf\n\t"
        "v_cndmask_b32 v12, v12, v16, s[36:37]\n\t"
        "v_cndmask_b32 v12, v12, v20, s[38:39]\n\t"
        "v_add_u32 v12, 0x800, v12\n\t"
        "v_bfi_b32 v21, s48, v22, v12\n\t"
        "v_lshlrev_b32 v13, 2, v1\n\t"
        "s_setprio 3\n\t"
        "buffer_store_dword v21, v13, s[20:23], s42 offen sc1\n\t"
        "s_setprio 2\n\t"
        "v_and_b32 v14, 3, v1\n\t"
        "v_lshlrev_b32 v14, 4, v14\n\t"
        "s_mov_b32 s78, 0xffff\n\t"
        "s_mov_b32 s79, 0\n\t"
        "v_lshlrev_b64 v[4:5], v14, s[78:79]\n\t"
        "s_mov_b32 s50, 0x0f0f0f0f\n\t"
        "s_mov_b32 s51, s50\n\t"
        "v_cndmask_b32 v6, v4, 0, s[50:51]\n\t"
        "v_cndmask_b32 v7, v5, 0, s[50:51]\n\t"
        "v_cndmask_b32 v4, 0, v4, s[50:51]\n\t"
        "v_cndmask_b32 v5, 0, v5, s[50:51]\n\t"
        "v_lshrrev_b32 v14, 5, v1\n\t"
        "v_add_lshl_u32 v14, v14, s80, 1\n"
        ".L7_w9:\n\t"
        "ds_read_b32 v0, v2 offset:8\n\t"
        "ds_read_u16 v16, v14 offset:1792\n\t"
        "ds_read_u16 v17, v14 offset:1796\n\t"
        "ds_read_u16 v18, v14 offset:1800\n\t"
        "ds_read_u16 v19, v14 offset:1804\n\t"
        "ds_read_u16 v20, v14 offset:1808\n\t"
        "s_waitcnt lgkmcnt(5)\n\t"
        "v_cmp_gt_u16 vcc, 2, v0\n\t"
        "s_cbranch_vccz .L7_d10\n\t"
        "s_waitcnt lgkmcnt(0)\n\t"
        "s_sleep 1\n\t"
        "s_sub_i32 s83, s83, 1\n\t"
        "s_cmp_gt_i32 s83, 0\n\t"
        "s_cbranch_scc1 .L7_w9\n"
        ".L7_d10:\n\t"
        "v_and_b32 v14, 15, v1\n\t"
        "v_lshlrev_b32 v14, 4, v14\n\t"
        "v_bfe_u32 v15, v1, 4, 1\n\t"
        "v_lshl_add_u32 v14, v15, 3, v14\n\t"
        "s_waitcnt lgkmcnt(0)\n\t"
        "v_xor_b32 v16, v16, v14\n\t"
        "v_xor_b32 v17, v17, v14\n\t"
        "v_xor_b32 v18, v18, v14\n\t"
        "v_xor_b32 v19, v19, v14\n\t"
        "v_xor_b32 v20, v20, v14\n\t"
        "v_xor_b32 v21, v21, v22\n\t"
        "v_mov_b64 v[8:9], 0\n\t"
        "v_mov_b64 v[10:11], 0\n"
        ".L7_w11:\n\t"
        "ds_read_b32 v0, v2 offset:28\n\t"
        "s_waitcnt lgkmcnt(0)\n\t"
        "v_cmp_gt_u16 vcc, 4, v0\n\t"
        "s_cbranch_vccz .L7_d12\n\t"
        "s_sleep 1\n\t"
        "s_sub_i32 s83, s83, 1\n\t"
        "s_cmp_gt_i32 s83, 0\n\t"
        "s_cbranch_scc1 .L7_w11\n"
        ".L7_d12:\n\t"
        "ds_read_b64_tr_b16 v[96:97], v23 offset:0\n\t"
        "ds_read_b64_tr_b16 v[98:99], v23 offset:512\n\t"
        "ds_read_b64_tr_b16 v[100:101], v23 offset:8\n\t"
        "ds_read_b64_tr_b16 v[102:103], v23 offset:520\n\t"
        "ds_read_b64_tr_b16 v[104:105], v23 offset:4096\n\t"
        "ds_read_b64_tr_b16 v[106:107], v23 offset:4608\n\t"
        "ds_read_b64_tr_b16 v[108:109], v23 offset:4104\n\t"
        "ds_read_b64_tr_b16 v[110:111], v23 offset:4616\n\t"
        "ds_read_b64_tr_b16 v[112:113], v23 offset:8192\n\t"
        "ds_read_b64_tr_b16 v[114:115], v23 offset:8704\n\t"
        "ds_read_b64_tr_b16 v[116:117], v23 offset:8200\n\t"
        "ds_read_b64_tr_b16 v[118:119], v23 offset:8712\n\t"
        "ds_read_b64_tr_b16 v[120:121], v23 offset:12288\n\t"
        "ds_read_b64_tr_b16 v[122:123], v23 offset:12800\n"
        ".L7_sp:\n\t"
        "buffer_load_dword v12, v13, s[20:23], s43 offen sc1\n"
        ".L7_sw:\n\t"
        "s_waitcnt vmcnt(0)\n\t"
        "v_xor_b32 v12, v12, v22\n\t"
        "v_and_b32 v0, s48, v12\n\t"
        "v_cmp_ne_u32 vcc, 0, v0\n\t"
        "s_cbranch_vccz .L7_sq\n\t"
        "s_sleep 8\n\t"
        "s_sub_i32 s83, s83, 1\n\t"
        "s_cmp_gt_i32 s83, 0\n\t"
        "s_cbranch_scc1 .L7_sp\n"
        ".L7_sq:\n\t"
        "v_add_f32 v21, v21, v12\n\t"
        "v_exp_f32 v21, v21\n\t"
        "v_mov_b64 v[12:13], 0\n\t"
        "v_mov_b64 v[14:15], 0\n\t"
        "v_mov_b32 v22, v21\n\t"
        "v_mov_b32 v0, v21\n\t"
        "s_nop 1\n\t"
        "v_permlane16_swap_b32 v22, v0\n\t"
        "v_add_f32 v22, v22, v0\n\t"
        "v_mov_b32 v0, v22\n\t"
        "s_nop 1\n\t"
        "v_permlane32_swap_b32 v22, v0\n\t"
        "v_add_f32 v22, v22, v0\n\t"
        "v_add_f32 v22, 0x3089705f, v22\n\t"
        "v_rcp_f32 v22, v22\n\t"
        "s_nop 0\n\t"
        "v_mul_f32 v21, v21, v22\n\t"
        "v_cvt_pk_bf16_f32 v21, v21, v21\n\t"
        "v_lshlrev_b32 v22, 4, v1\n\t"
        "v_add_u32 v22, 142848, v22\n"
        ".L7_w13:\n\t"
        "ds_read_b32 v0, v2 offset:36\n\t"
        "s_waitcnt lgkmcnt(0)\n\t"
        "v_cmp_gt_u16 vcc, 4, v0\n\t"
        "s_cbranch_vccz .L7_d14\n\t"
        "s_sleep 1\n\t"
        "s_sub_i32 s83, s83, 1\n\t"
        "s_cmp_gt_i32 s83, 0\n\t"
        "s_cbranch_scc1 .L7_w13\n"
        ".L7_d14:\n\t"
        "v_cmp_lt_u32 vcc, 0xfffff, v0\n\t"
        "s_cbranch_vccnz .L7_na\n\t"
        "v_and_b32_dpp v8, v21, v4 row_newbcast:0 row_mask:0xf bank_mask:0x3\n\t"
        "v_and_b32_dpp v9, v21, v5 row_newbcast:0 row_mask:0xf bank_mask:0x3\n\t"
        "v_and_b32_dpp v10, v21, v6 row_newbcast:0 row_mask:0xf bank_mask:0x3\n\t"
        "v_and_b32_dpp v11, v21, v7 row_newbcast:0 row_mask:0xf bank_mask:0x3\n\t"
        "v_and_b32_dpp v12, v21, v4 row_newbcast:8 row_mask:0xf bank_mask:0xc\n\t"
        "v_and_b32_dpp v13, v21, v5 row_newbcast:8 row_mask:0xf bank_mask:0xc\n\t"
        "v_and_b32_dpp v14, v21, v6 row_newbcast:8 row_mask:0xf bank_mask:0xc\n\t"
        "v_and_b32_dpp v15, v21, v7 row_newbcast:8 row_mask:0xf bank_mask:0xc\n\t"
        "v_mfma_f32_16x16x32_bf16 v[124:127], v[8:11], v[24:27], 0\n\t"
        "s_nop 0\n\t"
        "v_mfma_f32_16x16x32_bf16 v[124:127], v[12:15], v[28:31], v[124:127]\n\t"
        "v_and_b32_dpp v8, v21, v4 row_newbcast:4 row_mask:0xf bank_mask:0x3\n\t"
        "v_and_b32_dpp v9, v21, v5 row_newbcast:4 row_mask:0xf bank_mask:0x3\n\t"
        "v_and_b32_dpp v10, v21, v6 row_newbcast:4 row_mask:0xf bank_mask:0x3\n\t"
        "v_and_b32_dpp v11, v21, v7 row_newbcast:4 row_mask:0xf bank_mask:0x3\n\t"
        "v_and_b32_dpp v12, v21, v4 row_newbcast:12 row_mask:0xf bank_mask:0xc\n\t"
        "v_and_b32_dpp v13, v21, v5 row_newbcast:12 row_mask:0xf bank_mask:0xc\n\t"
        "v_and_b32_dpp v14, v21, v6 row_newbcast:12 row_mask:0xf bank_mask:0xc\n\t"
        "v_and_b32_dpp v15, v21, v7 row_newbcast:12 row_mask:0xf bank_mask:0xc\n\t"
        "v_mfma_f32_16x16x32_bf16 v[24:27], v[8:11], v[32:35], 0\n\t"
        "s_nop 0\n\t"
        "v_mfma_f32_16x16x32_bf16 v[24:27], v[12:15], v[36:39], v[24:27]\n\t"
        "v_cvt_pk_bf16_f32 v124, v124, v125\n\t"
        "v_cvt_pk_bf16_f32 v125, v126, v127\n\t"
        "ds_write_b64 v16, v[124:125] offset:46336\n\t"
        "v_and_b32_dpp v8, v21, v4 row_newbcast:1 row_mask:0xf bank_mask:0x3\n\t"
        "v_and_b32_dpp v9, v21, v5 row_newbcast:1 row_mask:0xf bank_mask:0x3\n\t"
        "v_and_b32_dpp v10, v21, v6 row_newbcast:1 row_mask:0xf bank_mask:0x3\n\t"
        "v_and_b32_dpp v11, v21, v7 row_newbcast:1 row_mask:0xf bank_mask:0x3\n\t"
        "v_and_b32_dpp v12, v21, v4 row_newbcast:9 row_mask:0xf bank_mask:0xc\n\t"
        "v_and_b32_dpp v13, v21, v5 row_newbcast:9 row_mask:0xf bank_mask:0xc\n\t"
        "v_and_b32_dpp v14, v21, v6 row_newbcast:9 row_mask:0xf bank_mask:0xc\n\t"
        "v_and_b32_dpp v15, v21, v7 row_newbcast:9 row_mask:0xf bank_mask:0xc\n\t"
        "v_mfma_f32_16x16x32_bf16 v[32:35], v[8:11], v[40:43], 0\n\t"
        "s_nop 0\n\t"
        "v_mfma_f32_16x16x32_bf16 v[32:35], v[12:15], v[44:47], v[32:35]\n\t"
        "v_cvt_pk_bf16_f32 v24, v24, v25\n\t"
        "v_cvt_pk_bf16_f32 v25, v26, v27\n\t"
        "ds_write_b64 v17, v[24:25] offset:46336\n\t"
        "v_and_b32_dpp v8, v21, v4 row_newbcast:5 row_mask:0xf bank_mask:0x3\n\t"
        "v_and_b32_dpp v9, v21, v5 row_newbcast:5 row_mask:0xf bank_mask:0x3\n\t"
        "v_and_b32_dpp v10, v21, v6 row_newbcast:5 row_mask:0xf bank_mask:0x3\n\t"
        "v_and_b32_dpp v11, v21, v7 row_newbcast:5 row_mask:0xf bank_mask:0x3\n\t"
        "v_and_b32_dpp v12, v21, v4 row_newbcast:13 row_mask:0xf bank_mask:0xc\n\t"
        "v_and_b32_dpp v13, v21, v5 row_newbcast:13 row_mask:0xf bank_mask:0xc\n\t"
        "v_and_b32_dpp v14, v21, v6 row_newbcast:13 row_mask:0xf bank_mask:0xc\n\t"
        "v_and_b32_dpp v15, v21, v7 row_newbcast:13 row_mask:0xf bank_mask:0xc\n\t"
        "v_mfma_f32_16x16x32_bf16 v[40:43], v[8:11], v[48:51], 0\n\t"
        "s_nop 0\n\t"
        "v_mfma_f32_16x16x32_bf16 v[40:43], v[12:15], v[52:55], v[40:43]\n\t"
        "v_cvt_pk_bf16_f32 v32, v32, v33\n\t"
        "v_cvt_pk_bf16_f32 v33, v34, v35\n\t"
        "ds_write_b64 v18, v[32:33] offset:46336\n\t"
        "v_and_b32_dpp v8, v21, v4 row_newbcast:2 row_mask:0xf bank_mask:0x3\n\t"
        "v_and_b32_dpp v9, v21, v5 row_newbcast:2 row_mask:0xf bank_mask:0x3\n\t"
        "v_and_b32_dpp v10, v21, v6 row_newbcast:2 row_mask:0xf bank_mask:0x3\n\t"
        "v_and_b32_dpp v11, v21, v7 row_newbcast:2 row_mask:0xf bank_mask:0x3\n\t"
        "v_and_b32_dpp v12, v21, v4 row_newbcast:10 row_mask:0xf bank_mask:0xc\n\t"
        "v_and_b32_dpp v13, v21, v5 row_newbcast:10 row_mask:0xf bank_mask:0xc\n\t"
        "v_and_b32_dpp v14, v21, v6 row_newbcast:10 row_mask:0xf bank_mask:0xc\n\t"
        "v_and_b32_dpp v15, v21, v7 row_newbcast:10 row_mask:0xf bank_mask:0xc\n\t"
        "v_mfma_f32_16x16x32_bf16 v[48:51], v[8:11], v[56:59], 0\n\t"
        "s_nop 0\n\t"
        "v_mfma_f32_16x16x32_bf16 v[48:51], v[12:15], v[60:63], v[48:51]\n\t"
        "v_cvt_pk_bf16_f32 v40, v40, v41\n\t"
        "v_cvt_pk_bf16_f32 v41, v42, v43\n\t"
        "ds_write_b64 v19, v[40:41] offset:46336\n\t"
        "s_cmp_lt_u32 s69, 4\n\t"
        "s_cselect_b32 s78, 16, 1\n\t"
        "v_mov_b32 v0, s78\n\t"
        "s_nop 1\n\t"
        "v_cvt_pk_bf16_f32 v48, v48, v49\n\t"
        "v_cvt_pk_bf16_f32 v49, v50, v51\n\t"
        "ds_write_b64 v20, v[48:49] offset:46336\n\t"
        "s_mov_b64 exec, 1\n\t"
        "ds_add_u32 v2, v0 offset:40\n\t"
        "s_mov_b64 exec, -1\n"
        ".L7_na:\n\t"
        "ds_read_b64_tr_b16 v[124:125], v23 offset:12296\n\t"
        "ds_read_b64_tr_b16 v[126:127], v23 offset:12808\n\t"
        "ds_read_b128 v[4:7], v22\n\t"
        "s_lshl3_add_u32 s42, %3, s81\n\t"
        "s_mul_i32 s42, s42, 0xc00\n\t"
        "s_add_u32 s42, s42, 3145728\n\t"
        "s_xor_b32 s78, %3, 1\n\t"
        "s_lshl3_add_u32 s43, s78, s81\n\t"
        "s_mul_i32 s43, s43, 0xc00\n\t"
        "s_add_u32 s43, s43, 3145728\n\t"
        "s_lshl_b32 s50, s81, 6\n\t"
        "s_lshl_b32 s51, s69, 10\n\t"
        "s_add_u32 s51, s51, 134656\n\t"
        "s_mov_b32 s49, 0x10001\n\t"
        "s_mov_b32 s48, 0x07060302\n\t"
        "s_mov_b32 s55, 142848\n"
        ".L7_w15:\n\t"
        "ds_read_b32 v0, v2 offset:40\n\t"
        "s_waitcnt lgkmcnt(0)\n\t"
        "v_cmp_gt_u16 vcc, 64, v0\n\t"
        "s_cbranch_vccz .L7_d16\n\t"
        "s_setprio 0\n\t"
        "s_sleep 1\n\t"
        "s_sub_i32 s83, s83, 1\n\t"
        "s_cmp_gt_i32 s83, 0\n\t"
        "s_cbranch_scc1 .L7_w15\n"
        ".L7_d16:\n\t"
        "s_setprio 2\n\t"
        "ds_read_b128 v[8:11], v4 offset:46336\n\t"
        "ds_read_b128 v[12:15], v5 offset:46336\n\t"
        "ds_read_b128 v[16:19], v6 offset:46336\n\t"
        "ds_read_b128 v[20:23], v7 offset:46336\n\t"
        "ds_read_b128 v[48:51], v4 offset:50432\n\t"
        "ds_read_b128 v[52:55], v5 offset:50432\n\t"
        "ds_read_b128 v[56:59], v6 offset:50432\n\t"
        "ds_read_b128 v[60:63], v7 offset:50432\n\t"
        "v_and_b32 v3, 8, v1\n\t"
        "s_waitcnt lgkmcnt(6)\n\t"
        "v_mfma_f32_16x16x32_bf16 v[32:35], v[64:67], v[8:11], 0\n\t"
        "v_mfma_f32_16x16x32_bf16 v[36:39], v[68:71], v[8:11], 0\n\t"
        "v_mfma_f32_16x16x32_bf16 v[32:35], v[72:75], v[12:15], v[32:35]\n\t"
        "v_mfma_f32_16x16x32_bf16 v[36:39], v[76:79], v[12:15], v[36:39]\n\t"
        "s_waitcnt lgkmcnt(4)\n\t"
        "v_mfma_f32_16x16x32_bf16 v[32:35], v[80:83], v[16:19], v[32:35]\n\t"
        "v_mfma_f32_16x16x32_bf16 v[36:39], v[84:87], v[16:19], v[36:39]\n\t"
        "v_mfma_f32_16x16x32_bf16 v[32:35], v[88:91], v[20:23], v[32:35]\n\t"
        "v_mfma_f32_16x16x32_bf16 v[36:39], v[92:95], v[20:23], v[36:39]\n\t"
        "ds_read_b32 v0, v2 offset:40\n\t"
        "v_lshl_add_u32 v8, v3, 8, v4\n\t"
        "v_lshl_add_u32 v12, v3, 8, v5\n\t"
        "v_lshl_add_u32 v16, v3, 8, v6\n\t"
        "v_lshl_add_u32 v20, v3, 8, v7\n\t"
        "ds_read_b128 v[8:11], v8 offset:54528\n\t"
        "ds_read_b128 v[12:15], v12 offset:54528\n\t"
        "ds_read_b128 v[16:19], v16 offset:54528\n\t"
        "ds_read_b128 v[20:23], v20 offset:54528\n\t"
        "s_waitcnt lgkmcnt(7)\n\t"
        "v_mfma_f32_16x16x32_bf16 v[40:43], v[64:67], v[48:51], 0\n\t"
        "v_mfma_f32_16x16x32_bf16 v[44:47], v[68:71], v[48:51], 0\n\t"
        "v_mfma_f32_16x16x32_bf16 v[40:43], v[72:75], v[52:55], v[40:43]\n\t"
        "v_mfma_f32_16x16x32_bf16 v[44:47], v[76:79], v[52:55], v[44:47]\n\t"
        "s_waitcnt lgkmcnt(5)\n\t"
        "v_mfma_f32_16x16x32_bf16 v[40:43], v[80:83], v[56:59], v[40:43]\n\t"
        "v_mfma_f32_16x16x32_bf16 v[44:47], v[84:87], v[56:59], v[44:47]\n\t"
        "v_mfma_f32_16x16x32_bf16 v[40:43], v[88:91], v[60:63], v[40:43]\n\t"
        "v_mfma_f32_16x16x32_bf16 v[44:47], v[92:95], v[60:63], v[44:47]\n\t"
        "s_waitcnt lgkmcnt(4)\n\t"
        "v_cmp_gt_u16 vcc, 68, v0\n\t"
        "s_cbranch_vccz .L7_t0\n"
        ".L7_w17:\n\t"
        "ds_read_b32 v0, v2 offset:40\n\t"
        "s_waitcnt lgkmcnt(0)\n\t"
        "v_cmp_gt_u16 vcc, 68, v0\n\t"
        "s_cbranch_vccz .L7_d18\n\t"
        "s_setprio 0\n\t"
        "s_sleep 1\n\t"
        "s_sub_i32 s83, s83, 1\n\t"
        "s_cmp_gt_i32 s83, 0\n\t"
        "s_cbranch_scc1 .L7_w17\n"
        ".L7_d18:\n\t"
        "s_setprio 2\n\t"
        "v_lshl_add_u32 v8, v3, 8, v4\n\t"
        "v_lshl_add_u32 v12, v3, 8, v5\n\t"
        "v_lshl_add_u32 v16, v3, 8, v6\n\t"
        "v_lshl_add_u32 v20, v3, 8, v7\n\t"
        "ds_read_b128 v[8:11], v8 offset:54528\n\t"
        "ds_read_b128 v[12:15], v12 offset:54528\n\t"
        "ds_read_b128 v[16:19], v16 offset:54528\n\t"
        "ds_read_b128 v[20:23], v20 offset:54528\n"
        ".L7_t0:\n\t"
        "ds_read_b32 v0, v2 offset:52\n\t"
        "ds_read_b128 v[48:51], v4 offset:23808\n\t"
        "ds_read_b128 v[52:55], v5 offset:23808\n\t"
        "ds_read_b128 v[56:59], v6 offset:23808\n\t"
        "ds_read_b128 v[60:63], v7 offset:23808\n\t"
        "s_waitcnt lgkmcnt(7)\n\t"
        "v_mfma_f32_16x16x32_bf16 v[24:27], v[64:67], v[8:11], 0\n\t"
        "v_mfma_f32_16x16x32_bf16 v[28:31], v[68:71], v[8:11], 0\n\t"
        "v_mfma_f32_16x16x32_bf16 v[24:27], v[72:75], v[12:15], v[24:27]\n\t"
        "v_mfma_f32_16x16x32_bf16 v[28:31], v[76:79], v[12:15], v[28:31]\n\t"
        "s_waitcnt lgkmcnt(5)\n\t"
        "v_mfma_f32_16x16x32_bf16 v[24:27], v[80:83], v[16:19], v[24:27]\n\t"
        "v_mfma_f32_16x16x32_bf16 v[28:31], v[84:87], v[16:19], v[28:31]\n\t"
        "v_mfma_f32_16x16x32_bf16 v[24:27], v[88:91], v[20:23], v[24:27]\n\t"
        "v_mfma_f32_16x16x32_bf16 v[28:31], v[92:95], v[20:23], v[28:31]\n\t"
        "ds_read_b128 v[8:11], v4 offset:32000\n\t"
        "ds_read_b128 v[12:15], v5 offset:32000\n\t"
        "ds_read_b128 v[16:19], v6 offset:32000\n\t"
        "ds_read_b128 v[20:23], v7 offset:32000\n\t"
        "s_waitcnt lgkmcnt(8)\n\t"
        "v_cmp_gt_u16 vcc, 6, v0\n\t"
        "s_cbranch_vccz .L7_t1\n"
        ".L7_w19:\n\t"
        "ds_read_b32 v0, v2 offset:52\n\t"
        "s_waitcnt lgkmcnt(0)\n\t"
        "v_cmp_gt_u16 vcc, 6, v0\n\t"
        "s_cbranch_vccz .L7_d20\n\t"
        "s_setprio 0\n\t"
        "s_sleep 1\n\t"
        "s_sub_i32 s83, s83, 1\n\t"
        "s_cmp_gt_i32 s83, 0\n\t"
        "s_cbranch_scc1 .L7_w19\n"
        ".L7_d20:\n\t"
        "s_setprio 2\n\t"
        "ds_read_b128 v[48:51], v4 offset:23808\n\t"
        "ds_read_b128 v[52:55], v5 offset:23808\n\t"
        "ds_read_b128 v[56:59], v6 offset:23808\n\t"
        "ds_read_b128 v[60:63], v7 offset:23808\n\t"
        "ds_read_b128 v[8:11], v4 offset:32000\n\t"
        "ds_read_b128 v[12:15], v5 offset:32000\n\t"
        "ds_read_b128 v[16:19], v6 offset:32000\n\t"
        "ds_read_b128 v[20:23], v7 offset:32000\n"
        ".L7_t1:\n\t"
        "v_lshl_add_u32 v3, v1, 4, s55\n\t"
        "s_waitcnt lgkmcnt(6)\n\t"
        "v_mfma_f32_16x16x32_bf16 v[32:35], v[96:99], v[48:51], v[32:35]\n\t"
        "v_mfma_f32_16x16x32_bf16 v[36:39], v[100:103], v[48:51], v[36:39]\n\t"
        "v_mfma_f32_16x16x32_bf16 v[32:35], v[104:107], v[52:55], v[32:35]\n\t"
        "v_mfma_f32_16x16x32_bf16 v[36:39], v[108:111], v[52:55], v[36:39]\n\t"
        "s_waitcnt lgkmcnt(4)\n\t"
        "v_mfma_f32_16x16x32_bf16 v[32:35], v[112:115], v[56:59], v[32:35]\n\t"
        "v_mfma_f32_16x16x32_bf16 v[36:39], v[116:119], v[56:59], v[36:39]\n\t"
        "v_mfma_f32_16x16x32_bf16 v[32:35], v[120:123], v[60:63], v[32:35]\n\t"
        "v_mfma_f32_16x16x32_bf16 v[36:39], v[124:127], v[60:63], v[36:39]\n\t"
        "ds_read_b128 v[60:63], v3 offset:1024\n\t"
        "s_waitcnt lgkmcnt(3)\n\t"
        "v_mfma_f32_16x16x32_bf16 v[24:27], v[96:99], v[8:11], v[24:27]\n\t"
        "v_mfma_f32_16x16x32_bf16 v[28:31], v[100:103], v[8:11], v[28:31]\n\t"
        "v_mfma_f32_16x16x32_bf16 v[24:27], v[104:107], v[12:15], v[24:27]\n\t"
        "v_mfma_f32_16x16x32_bf16 v[28:31], v[108:111], v[12:15], v[28:31]\n\t"
        "s_waitcnt lgkmcnt(1)\n\t"
        "v_mfma_f32_16x16x32_bf16 v[24:27], v[112:115], v[16:19], v[24:27]\n\t"
        "v_mfma_f32_16x16x32_bf16 v[28:31], v[116:119], v[16:19], v[28:31]\n\t"
        "v_mfma_f32_16x16x32_bf16 v[24:27], v[120:123], v[20:23], v[24:27]\n\t"
        "v_mfma_f32_16x16x32_bf16 v[28:31], v[124:127], v[20:23], v[28:31]\n\t"
        "ds_read_b32 v0, v2 offset:56\n\t"
        "ds_read_b128 v[8:11], v4 offset:27904\n\t"
        "ds_read_b128 v[12:15], v5 offset:27904\n\t"
        "ds_read_b128 v[16:19], v6 offset:27904\n\t"
        "ds_read_b128 v[20:23], v7 offset:27904\n\t"
        "s_waitcnt lgkmcnt(5)\n\t"
        "v_add_u32 v32, 0x10000, v32\n\t"
        "v_add_u32 v33, 0x10000, v33\n\t"
        "v_add_u32 v34, 0x10000, v34\n\t"
        "v_add_u32 v35, 0x10000, v35\n\t"
        "v_perm_b32 v52, v33, v32, s48\n\t"
        "v_perm_b32 v53, v35, v34, s48\n\t"
        "v_cvt_pk_bf16_f32 v54, v36, v37\n\t"
        "v_cvt_pk_bf16_f32 v55, v38, v39\n\t"
        "v_bfi_b32 v52, s49, v60, v52\n\t"
        "v_bfi_b32 v53, s49, v61, v53\n\t"
        "buffer_store_dwordx4 v[52:55], v62, s[20:23], s42 offen offset:1024 sc1\n\t"
        "s_mov_b32 exec_lo, 0x00ff00ff\n\t"
        "s_mov_b32 exec_hi, 0x00ff00ff\n\t"
        "v_add_u32 v24, 0x10000, v24\n\t"
        "v_add_u32 v25, 0x10000, v25\n\t"
        "v_add_u32 v26, 0x10000, v26\n\t"
        "v_add_u32 v27, 0x10000, v27\n\t"
        "v_perm_b32 v48, v25, v24, s48\n\t"
        "v_perm_b32 v49, v27, v26, s48\n\t"
        "v_cvt_pk_bf16_f32 v50, v28, v29\n\t"
        "v_cvt_pk_bf16_f32 v51, v30, v31\n\t"
        "v_bfi_b32 v48, s49, v60, v48\n\t"
        "v_bfi_b32 v49, s49, v61, v49\n\t"
        "buffer_store_dwordx4 v[48:51], v62, s[20:23], s42 offen offset:0 sc1\n\t"
        "s_mov_b64 exec, -1\n\t"
        "s_waitcnt lgkmcnt(4)\n\t"
        "v_cmp_gt_u16 vcc, 4, v0\n\t"
        "s_cbranch_vccz .L7_t2\n"
        ".L7_w21:\n\t"
        "ds_read_b32 v0, v2 offset:56\n\t"
        "s_waitcnt lgkmcnt(0)\n\t"
        "v_cmp_gt_u16 vcc, 3, v0\n\t"
        "s_cbranch_vccz .L7_d22\n\t"
        "s_setprio 0\n\t"
        "s_sleep 1\n\t"
        "s_sub_i32 s83, s83, 1\n\t"
        "s_cmp_gt_i32 s83, 0\n\t"
        "s_cbranch_scc1 .L7_w21\n"
        ".L7_d22:\n\t"
        "s_setprio 2\n"
        ".L7_w23:\n\t"
        "ds_read_b32 v0, v2 offset:56\n\t"
        "ds_read_b128 v[8:11], v4 offset:27904\n\t"
        "ds_read_b128 v[12:15], v5 offset:27904\n\t"
        "ds_read_b128 v[16:19], v6 offset:27904\n\t"
        "ds_read_b128 v[20:23], v7 offset:27904\n\t"
        "s_waitcnt lgkmcnt(4)\n\t"
        "v_cmp_gt_u16 vcc, 4, v0\n\t"
        "s_cbranch_vccz .L7_d24\n\t"
        "s_waitcnt lgkmcnt(0)\n\t"
        "s_setprio 0\n\t"
        "s_sleep 1\n\t"
        "s_sub_i32 s83, s83, 1\n\t"
        "s_cmp_gt_i32 s83, 0\n\t"
        "s_cbranch_scc1 .L7_w23\n"
        ".L7_d24:\n\t"
        "s_setprio 2\n"
        ".L7_t2:\n\t"
        "s_waitcnt lgkmcnt(2)\n\t"
        "v_mfma_f32_16x16x32_bf16 v[40:43], v[96:99], v[8:11], v[40:43]\n\t"
        "v_mfma_f32_16x16x32_bf16 v[44:47], v[100:103], v[8:11], v[44:47]\n\t"
        "v_mfma_f32_16x16x32_bf16 v[40:43], v[104:107], v[12:15], v[40:43]\n\t"
        "v_mfma_f32_16x16x32_bf16 v[44:47], v[108:111], v[12:15], v[44:47]\n\t"
        "s_waitcnt lgkmcnt(0)\n\t"
        "v_mfma_f32_16x16x32_bf16 v[40:43], v[112:115], v[16:19], v[40:43]\n\t"
        "v_mfma_f32_16x16x32_bf16 v[44:47], v[116:119], v[16:19], v[44:47]\n\t"
        "v_mfma_f32_16x16x32_bf16 v[40:43], v[120:123], v[20:23], v[40:43]\n\t"
        "v_mfma_f32_16x16x32_bf16 v[44:47], v[124:127], v[20:23], v[44:47]\n\t"
        "ds_read_b128 v[8:11], v4 offset:60672\n\t"
        "ds_read_b128 v[12:15], v5 offset:60672\n\t"
        "ds_read_b128 v[16:19], v6 offset:60672\n\t"
        "ds_read_b128 v[20:23], v7 offset:60672\n\t"
        "s_nop 2\n\t"
        "v_add_u32 v40, 0x10000, v40\n\t"
        "v_add_u32 v41, 0x10000, v41\n\t"
        "v_add_u32 v42, 0x10000, v42\n\t"
        "v_add_u32 v43, 0x10000, v43\n\t"
        "v_perm_b32 v56, v41, v40, s48\n\t"
        "v_perm_b32 v57, v43, v42, s48\n\t"
        "v_cvt_pk_bf16_f32 v58, v44, v45\n\t"
        "v_cvt_pk_bf16_f32 v59, v46, v47\n\t"
        "v_bfi_b32 v56, s49, v60, v56\n\t"
        "v_bfi_b32 v57, s49, v61, v57\n\t"
        "buffer_store_dwordx4 v[56:59], v62, s[20:23], s42 offen offset:2048 sc1\n\t"
        "v_mov_b32 v0, 1\n\t"
        "s_mov_b64 exec, 1\n\t"
        "ds_add_u32 v2, v0 offset:96\n\t"
        "s_mov_b64 exec, -1\n\t"
        "s_waitcnt lgkmcnt(3)\n\t"
        "v_mfma_f32_16x16x32_bf16 v[32:35], v[64:67], v[8:11], 0\n\t"
        "v_mfma_f32_16x16x32_bf16 v[36:39], v[68:71], v[8:11], 0\n\t"
        "v_mfma_f32_16x16x32_bf16 v[32:35], v[72:75], v[12:15], v[32:35]\n\t"
        "v_mfma_f32_16x16x32_bf16 v[36:39], v[76:79], v[12:15], v[36:39]\n\t"
        "s_waitcnt lgkmcnt(1)\n\t"
        "v_mfma_f32_16x16x32_bf16 v[32:35], v[80:83], v[16:19], v[32:35]\n\t"
        "v_mfma_f32_16x16x32_bf16 v[36:39], v[84:87], v[16:19], v[36:39]\n\t"
        "v_mfma_f32_16x16x32_bf16 v[32:35], v[88:91], v[20:23], v[32:35]\n\t"
        "v_mfma_f32_16x16x32_bf16 v[36:39], v[92:95], v[20:23], v[36:39]\n\t"
        "ds_read_b128 v[8:11], v4 offset:64768\n\t"
        "ds_read_b128 v[12:15], v5 offset:64768\n\t"
        "ds_read_b128 v[16:19], v6 offset:64768\n\t"
        "ds_read_b128 v[20:23], v7 offset:64768\n\t"
        "s_waitcnt lgkmcnt(2)\n\t"
        "v_mfma_f32_16x16x32_bf16 v[40:43], v[64:67], v[8:11], 0\n\t"
        "v_mfma_f32_16x16x32_bf16 v[44:47], v[68:71], v[8:11], 0\n\t"
        "v_mfma_f32_16x16x32_bf16 v[40:43], v[72:75], v[12:15], v[40:43]\n\t"
        "v_mfma_f32_16x16x32_bf16 v[44:47], v[76:79], v[12:15], v[44:47]\n\t"
        "s_waitcnt lgkmcnt(0)\n\t"
        "v_mfma_f32_16x16x32_bf16 v[40:43], v[80:83], v[16:19], v[40:43]\n\t"
        "v_mfma_f32_16x16x32_bf16 v[44:47], v[84:87], v[16:19], v[44:47]\n\t"
        "v_mfma_f32_16x16x32_bf16 v[40:43], v[88:91], v[20:23], v[40:43]\n\t"
        "v_mfma_f32_16x16x32_bf16 v[44:47], v[92:95], v[20:23], v[44:47]\n\t"
        "ds_read_b128 v[68:71], v3 offset:2048\n\t"
        "ds_read_b128 v[74:77], v3 offset:3072\n"
        ".L7_w25:\n\t"
        "ds_read_b32 v0, v2 offset:60\n\t"
        "s_waitcnt lgkmcnt(0)\n\t"
        "v_cmp_gt_u16 vcc, 2, v0\n\t"
        "s_cbranch_vccz .L7_d26\n\t"
        "s_setprio 0\n\t"
        "s_sleep 1\n\t"
        "s_sub_i32 s83, s83, 1\n\t"
        "s_cmp_gt_i32 s83, 0\n\t"
        "s_cbranch_scc1 .L7_w25\n"
        ".L7_d26:\n\t"
        "s_setprio 2\n\t"
        "ds_read_b128 v[8:11], v4 offset:34048\n\t"
        "ds_read_b128 v[12:15], v5 offset:34048\n\t"
        "ds_read_b128 v[16:19], v6 offset:34048\n\t"
        "ds_read_b128 v[20:23], v7 offset:34048\n\t"
        "v_add_u32 v71, s51, v71\n\t"
        "v_add_u32 v72, s51, v74\n\t"
        "v_mov_b64 v[74:75], 0\n\t"
        "v_mov_b64 v[78:79], 0\n\t"
        "s_waitcnt lgkmcnt(2)\n\t"
        "v_mfma_f32_16x16x32_bf16 v[24:27], v[96:99], v[8:11], v[24:27]\n\t"
        "v_mfma_f32_16x16x32_bf16 v[28:31], v[100:103], v[8:11], v[28:31]\n\t"
        "v_mfma_f32_16x16x32_bf16 v[24:27], v[104:107], v[12:15], v[24:27]\n\t"
        "v_mfma_f32_16x16x32_bf16 v[28:31], v[108:111], v[12:15], v[28:31]\n\t"
        "s_waitcnt lgkmcnt(0)\n\t"
        "v_mfma_f32_16x16x32_bf16 v[24:27], v[112:115], v[16:19], v[24:27]\n\t"
        "v_mfma_f32_16x16x32_bf16 v[28:31], v[116:119], v[16:19], v[28:31]\n\t"
        "v_mfma_f32_16x16x32_bf16 v[24:27], v[120:123], v[20:23], v[24:27]\n\t"
        "v_mfma_f32_16x16x32_bf16 v[28:31], v[124:127], v[20:23], v[28:31]\n"
        ".L7_w27:\n\t"
        "ds_read_b32 v0, v2 offset:100\n\t"
        "s_waitcnt lgkmcnt(0)\n\t"
        "v_cmp_gt_u16 vcc, 1, v0\n\t"
        "s_cbranch_vccz .L7_d28\n\t"
        "s_setprio 0\n\t"
        "s_sleep 1\n\t"
        "s_sub_i32 s83, s83, 1\n\t"
        "s_cmp_gt_i32 s83, 0\n\t"
        "s_cbranch_scc1 .L7_w27\n"
        ".L7_d28:\n\t"
        "s_setprio 2\n\t"
        "buffer_load_dwordx4 v[48:51], v63, s[20:23], s43 offen offset:0 sc1\n\t"
        "buffer_load_dwordx4 v[52:55], v62, s[20:23], s43 offen offset:1024 sc1\n\t"
        "buffer_load_dwordx4 v[56:59], v62, s[20:23], s43 offen offset:2048 sc1\n"
        ".L7_w29:\n\t"
        "ds_read_b32 v0, v2 offset:64\n\t"
        "s_waitcnt lgkmcnt(0)\n\t"
        "v_cmp_gt_u16 vcc, 4, v0\n\t"
        "s_cbranch_vccz .L7_d30\n\t"
        "s_setprio 0\n\t"
        "s_sleep 1\n\t"
        "s_sub_i32 s83, s83, 1\n\t"
        "s_cmp_gt_i32 s83, 0\n\t"
        "s_cbranch_scc1 .L7_w29\n"
        ".L7_d30:\n\t"
        "s_setprio 2\n\t"
        "ds_read_b128 v[8:11], v4 offset:38144\n\t"
        "ds_read_b128 v[12:15], v5 offset:38144\n\t"
        "ds_read_b128 v[16:19], v6 offset:38144\n\t"
        "ds_read_b128 v[20:23], v7 offset:38144\n\t"
        "s_waitcnt lgkmcnt(2)\n\t"
        "v_mfma_f32_16x16x32_bf16 v[32:35], v[96:99], v[8:11], v[32:35]\n\t"
        "v_mfma_f32_16x16x32_bf16 v[36:39], v[100:103], v[8:11], v[36:39]\n\t"
        "v_mfma_f32_16x16x32_bf16 v[32:35], v[104:107], v[12:15], v[32:35]\n\t"
        "v_mfma_f32_16x16x32_bf16 v[36:39], v[108:111], v[12:15], v[36:39]\n\t"
        "s_waitcnt lgkmcnt(0)\n\t"
        "v_mfma_f32_16x16x32_bf16 v[32:35], v[112:115], v[16:19], v[32:35]\n\t"
        "v_mfma_f32_16x16x32_bf16 v[36:39], v[116:119], v[16:19], v[36:39]\n\t"
        "v_mfma_f32_16x16x32_bf16 v[32:35], v[120:123], v[20:23], v[32:35]\n\t"
        "v_mfma_f32_16x16x32_bf16 v[36:39], v[124:127], v[20:23], v[36:39]\n\t"
        "s_waitcnt vmcnt(0)\n\t"
        "s_branch .L7_s32\n"
        ".L7_r31:\n\t"
        "buffer_load_dwordx4 v[48:51], v63, s[20:23], s43 offen offset:0 sc1\n\t"
        "buffer_load_dwordx4 v[52:55], v62, s[20:23], s43 offen offset:1024 sc1\n\t"
        "buffer_load_dwordx4 v[56:59], v62, s[20:23], s43 offen offset:2048 sc1\n\t"
        "s_waitcnt vmcnt(0)\n"
        ".L7_s32:\n\t"
        "v_xor_b32 v48, v48, v60\n\t"
        "v_xor_b32 v49, v49, v61\n\t"
        "v_xor_b32 v52, v52, v60\n\t"
        "v_xor_b32 v53, v53, v61\n\t"
        "v_xor_b32 v56, v56, v60\n\t"
        "v_xor_b32 v57, v57, v61\n\t"
        "v_or_b32 v0, v48, v49\n\t"
        "v_or3_b32 v0, v0, v52, v53\n\t"
        "v_or3_b32 v0, v0, v56, v57\n\t"
        "v_and_b32 v0, s49, v0\n\t"
        "v_cmp_ne_u32 vcc, 0, v0\n\t"
        "s_cbranch_vccz .L7_t33\n\t"
        "s_sleep 8\n\t"
        "s_sub_i32 s83, s83, 1\n\t"
        "s_cmp_gt_i32 s83, 0\n\t"
        "s_cbranch_scc1 .L7_r31\n"
        ".L7_t33:\n\t"
        "v_mfma_f32_16x16x32_bf16 v[24:27], v[76:79], v[48:51], v[24:27]\n\t"
        "v_mfma_f32_16x16x32_bf16 v[28:31], v[74:77], v[48:51], v[28:31]\n\t"
        "v_mfma_f32_16x16x32_bf16 v[32:35], v[76:79], v[52:55], v[32:35]\n\t"
        "v_mfma_f32_16x16x32_bf16 v[36:39], v[74:77], v[52:55], v[36:39]\n\t"
        "v_mfma_f32_16x16x32_bf16 v[40:43], v[76:79], v[56:59], v[40:43]\n\t"
        "v_mfma_f32_16x16x32_bf16 v[44:47], v[74:77], v[56:59], v[44:47]\n\t"
        "s_nop 2\n\t"
        "v_cvt_pk_bf16_f32 v48, v24, v25\n\t"
        "v_cvt_pk_bf16_f32 v49, v26, v27\n\t"
        "v_cvt_pk_bf16_f32 v50, v28, v29\n\t"
        "v_cvt_pk_bf16_f32 v51, v30, v31\n\t"
        "ds_write_b128 v71, v[48:51]\n\t"
        "ds_read_b128 v[48:51], v72\n\t"
        "v_cvt_pk_bf16_f32 v52, v32, v33\n\t"
        "v_cvt_pk_bf16_f32 v53, v34, v35\n\t"
        "v_cvt_pk_bf16_f32 v54, v36, v37\n\t"
        "v_cvt_pk_bf16_f32 v55, v38, v39\n\t"
        "ds_write_b128 v71, v[52:55]\n\t"
        "ds_read_b128 v[52:55], v72\n\t"
        "ds_read_b32 v0, v2 offset:68\n\t"
        "ds_read_b128 v[8:11], v4 offset:42240\n\t"
        "ds_read_b128 v[12:15], v5 offset:42240\n\t"
        "ds_read_b128 v[16:19], v6 offset:42240\n\t"
        "ds_read_b128 v[20:23], v7 offset:42240\n\t"
        "s_waitcnt lgkmcnt(4)\n\t"
        "buffer_store_dwordx4 v[48:51], v68, s[60:63], s50 offen sc1\n\t"
        "buffer_store_dwordx4 v[52:55], v69, s[60:63], s50 offen sc1\n\t"
        "v_cmp_gt_u16 vcc, 4, v0\n\t"
        "s_cbranch_vccz .L7_t5\n"
        ".L7_w34:\n\t"
        "ds_read_b32 v0, v2 offset:68\n\t"
        "s_waitcnt lgkmcnt(0)\n\t"
        "v_cmp_gt_u16 vcc, 3, v0\n\t"
        "s_cbranch_vccz .L7_d35\n\t"
        "s_setprio 0\n\t"
        "s_sleep 1\n\t"
        "s_sub_i32 s83, s83, 1\n\t"
        "s_cmp_gt_i32 s83, 0\n\t"
        "s_cbranch_scc1 .L7_w34\n"
        ".L7_d35:\n\t"
        "s_setprio 2\n"
        ".L7_w36:\n\t"
        "ds_read_b32 v0, v2 offset:68\n\t"
        "ds_read_b128 v[8:11], v4 offset:42240\n\t"
        "ds_read_b128 v[12:15], v5 offset:42240\n\t"
        "ds_read_b128 v[16:19], v6 offset:42240\n\t"
        "ds_read_b128 v[20:23], v7 offset:42240\n\t"
        "s_waitcnt lgkmcnt(4)\n\t"
        "v_cmp_gt_u16 vcc, 4, v0\n\t"
        "s_cbranch_vccz .L7_d37\n\t"
        "s_waitcnt lgkmcnt(0)\n\t"
        "s_setprio 0\n\t"
        "s_sleep 0\n\t"
        "s_sub_i32 s83, s83, 1\n\t"
        "s_cmp_gt_i32 s83, 0\n\t"
        "s_cbranch_scc1 .L7_w36\n"
        ".L7_d37:\n\t"
        "s_setprio 2\n"
        ".L7_t5:\n\t"
        "s_waitcnt lgkmcnt(2)\n\t"
        "v_mfma_f32_16x16x32_bf16 v[40:43], v[96:99], v[8:11], v[40:43]\n\t"
        "v_mfma_f32_16x16x32_bf16 v[44:47], v[100:103], v[8:11], v[44:47]\n\t"
        "v_mfma_f32_16x16x32_bf16 v[40:43], v[104:107], v[12:15], v[40:43]\n\t"
        "v_mfma_f32_16x16x32_bf16 v[44:47], v[108:111], v[12:15], v[44:47]\n\t"
        "s_waitcnt lgkmcnt(0)\n\t"
        "v_mfma_f32_16x16x32_bf16 v[40:43], v[112:115], v[16:19], v[40:43]\n\t"
        "v_mfma_f32_16x16x32_bf16 v[44:47], v[116:119], v[16:19], v[44:47]\n\t"
        "v_mfma_f32_16x16x32_bf16 v[40:43], v[120:123], v[20:23], v[40:43]\n\t"
        "v_mfma_f32_16x16x32_bf16 v[44:47], v[124:127], v[20:23], v[44:47]\n\t"
        "s_nop 6\n\t"
        "v_cvt_pk_bf16_f32 v56, v40, v41\n\t"
        "v_cvt_pk_bf16_f32 v57, v42, v43\n\t"
        "v_cvt_pk_bf16_f32 v58, v44, v45\n\t"
        "v_cvt_pk_bf16_f32 v59, v46, v47\n\t"
        "ds_write_b128 v71, v[56:59]\n\t"
        "ds_read_b128 v[56:59], v72\n\t"
        "s_waitcnt lgkmcnt(0)\n\t"
        "buffer_store_dwordx4 v[56:59], v70, s[60:63], s50 offen sc1\n\t"
        "s_or_b32 s78, %3, s69\n\t"
        "s_cbranch_scc1 .L7_ng\n\t"
        "v_mov_b32 v0, s68\n\t"
        "buffer_store_dword v0, off, s[20:23], 0 sc1\n"
        ".L7_ng:\n\t"
        "s_endpgm\n"
        ".L7_G2:\n\t"
        "s_setprio 1\n\t"
        "v_lshlrev_b32 v5, 4, v1\n\t"
        "s_mul_i32 s82, s76, 13\n\t"
        "s_lshr_b32 s82, s82, 6\n\t"
        "s_mul_i32 s82, s82, 5\n\t"
        "s_sub_u32 s82, s76, s82\n\t"
        "s_waitcnt lgkmcnt(0)\n\t"
        "s_lshl2_add_u32 s78, s82, s69\n\t"
        "s_lshl_b32 s78, s78, 10\n\t"
        "s_add_u32 m0, s78, 46336\n\t"
        "s_add_u32 s82, s82, 1\n\t"
        "s_cmp_eq_u32 s82, 5\n\t"
        "buffer_load_dwordx4 v5, s[56:59], s78 offen lds\n\t"
        "s_cselect_b32 s82, 0, s82\n\t"
        "v_lshrrev_b32 v18, 3, v1\n\t"
        "v_lshlrev_b32 v18, 14, v18\n\t"
        "v_and_b32 v6, 0x70, v5\n\t"
        "s_lshl_b32 s78, s69, 19\n\t"
        "s_lshl_b32 s79, s71, 7\n\t"
        "s_add_u32 s78, s78, s79\n\t"
        "v_add3_u32 v18, v18, v6, s78\n\t"
        "s_mov_b32 s24, 1048576\n\t"
        "s_mov_b32 s25, 1179648\n\t"
        "s_mov_b32 s26, 1310720\n\t"
        "s_mov_b32 s27, 1441792\n\t"
        "v_and_b32 v6, 0xf0, v5\n\t"
        "s_lshl_b32 s78, s70, 8\n\t"
        "v_add_u32 v62, s78, v6\n\t"
        "v_mov_b32 v7, 113860\n\t"
        "s_movk_i32 s38, 196\n\t"
        "s_mov_b32 s39, 12845056\n\t"
        "s_lshl_b32 s33, s69, 3\n\t"
        "s_lshl_b32 s78, 1, s33\n\t"
        "v_mov_b32 v29, s78\n\t"
        "s_sub_u32 s32, s78, 1\n\t"
        "s_and_b32 s32, s32, 0x01010101\n\t"
        "s_mov_b32 s35, 0xffffff\n\t"
        "s_mov_b32 s40, 0x13050109\n\t"
        "s_mov_b32 s41, 23\n\t"
        "s_cmp_eq_u32 s69, 1\n\t"
        "s_cselect_b32 s40, 0x12040008, s40\n\t"
        "s_cselect_b32 s41, 22, s41\n\t"
        "s_cmp_eq_u32 s69, 2\n\t"
        "s_cselect_b32 s40, 0x100e0602, s40\n\t"
        "s_cselect_b32 s41, 20, s41\n\t"
        "s_cmp_eq_u32 s69, 3\n\t"
        "s_cselect_b32 s40, 0x110f0703, s40\n\t"
        "s_cselect_b32 s41, 21, s41\n\t"
        "s_mul_i32 s41, s41, 0x01010101\n\t"
        "s_bitcmp1_b32 s40, 3\n\t"
        "s_cselect_b32 s78, 0, 4\n\t"
        "v_mov_b32 v61, s78\n\t"
        "v_lshrrev_b32 v12, 4, v1\n\t"
        "s_bfe_u32 s78, s40, 0x80000\n\t"
        "s_lshl_b32 s78, s78, 2\n\t"
        "v_add_u32 v13, s78, v12\n\t"
        "v_lshlrev_b32 v24, 2, v13\n\t"
        "v_lshlrev_b32 v15, 4, v13\n\t"
        "v_and_b32 v15, 0xf0, v15\n\t"
        "v_xor_b32 v15, v15, v6\n\t"
        "v_lshl_add_u32 v56, v13, 8, v15\n\t"
        "s_bfe_u32 s78, s40, 0x80008\n\t"
        "s_lshl_b32 s78, s78, 2\n\t"
        "v_add_u32 v13, s78, v12\n\t"
        "v_lshlrev_b32 v25, 2, v13\n\t"
        "v_lshlrev_b32 v15, 4, v13\n\t"
        "v_and_b32 v15, 0xf0, v15\n\t"
        "v_xor_b32 v15, v15, v6\n\t"
        "v_lshl_add_u32 v57, v13, 8, v15\n\t"
        "s_bfe_u32 s78, s40, 0x80010\n\t"
        "s_lshl_b32 s78, s78, 2\n\t"
        "v_add_u32 v13, s78, v12\n\t"
        "v_lshlrev_b32 v26, 2, v13\n\t"
        "v_lshlrev_b32 v15, 4, v13\n\t"
        "v_and_b32 v15, 0xf0, v15\n\t"
        "v_xor_b32 v15, v15, v6\n\t"
        "v_lshl_add_u32 v58, v13, 8, v15\n\t"
        "v_lshlrev_b32 v15, 9, v61\n\t"
        "v_sub_u32 v58, v58, v15\n\t"
        "s_bfe_u32 s78, s40, 0x80018\n\t"
        "s_lshl_b32 s78, s78, 2\n\t"
        "v_add_u32 v13, s78, v12\n\t"
        "v_lshlrev_b32 v27, 2, v13\n\t"
        "v_lshlrev_b32 v15, 4, v13\n\t"
        "v_and_b32 v15, 0xf0, v15\n\t"
        "v_xor_b32 v15, v15, v6\n\t"
        "v_lshl_add_u32 v59, v13, 8, v15\n\t"
        "s_bfe_u32 s78, s41, 0x80000\n\t"
        "s_lshl_b32 s78, s78, 2\n\t"
        "v_add_u32 v13, s78, v12\n\t"
        "v_lshlrev_b32 v28, 2, v13\n\t"
        "v_lshlrev_b32 v15, 4, v13\n\t"
        "v_and_b32 v15, 0xf0, v15\n\t"
        "v_xor_b32 v15, v15, v6\n\t"
        "v_lshl_add_u32 v60, v13, 8, v15\n\t"
        "v_and_b32 v20, 28, v1\n\t"
        "v_lshlrev_b32 v20, 1, v20\n\t"
        "v_lshrrev_b64 v[12:13], v20, s[40:41]\n\t"
        "v_and_b32 v13, 0xff, v12\n\t"
        "v_and_b32 v12, 3, v1\n\t"
        "v_lshl_add_u32 v13, v13, 2, v12\n\t"
        "v_lshlrev_b32 v31, 2, v13\n\t"
        "s_and_b32 s78, s67, 0x10000\n\t"
        "s_or_b32 s78, s78, 1\n\t"
        "v_mov_b32 v30, s78\n\t"
        "s_mov_b32 s36, 0\n\t"
        "v_and_b32 v0, 48, v5\n\t"
        "s_mov_b32 s78, 0x0f0e\n\t"
        "s_mov_b32 s79, 0\n\t"
        "v_lshlrev_b64 v[8:9], v0, s[78:79]\n\t"
        "s_mov_b32 s78, 0x0b0a\n\t"
        "v_lshlrev_b64 v[12:13], v0, s[78:79]\n\t"
        "s_mov_b32 s78, 0xf0f0f0f0\n\t"
        "s_mov_b32 s79, s78\n\t"
        "v_cndmask_b32 v8, v8, v12, s[78:79]\n\t"
        "v_cndmask_b32 v9, v9, v13, s[78:79]\n\t"
        "v_xor_b32 v8, 0x0c0c0c0c, v8\n\t"
        "v_xor_b32 v9, 0x0c0c0c0c, v9\n\t"
        "s_movk_i32 s48, 0x200\n\t"
        "s_mov_b32 s37, 0x01010101\n\t"
        "s_mov_b32 s49, 0x05040100\n\t"
        "s_getpc_b64 s[50:51]\n"
        ".L7_pc:\n\t"
        "s_add_u32 s52, s50, .L7_ce-.L7_pc\n\t"
        "s_addc_u32 s53, s51, 0\n\t"
        "s_add_u32 s42, s50, .L7_ev-.L7_pc\n\t"
        "s_addc_u32 s43, s51, 0\n\t"
        "s_add_u32 s50, s50, .L7_ie-.L7_pc\n\t"
        "s_addc_u32 s51, s51, 0\n\t"
        "s_cmp_eq_u32 s80, 10\n\t"
        "s_cselect_b32 s80, 0, s80\n\t"
        "s_mul_i32 s78, s80, 0x1080\n\t"
        "s_add_u32 m0, s78, s81\n\t"
        "s_lshr_b32 s78, s80, 1\n\t"
        "s_lshl1_add_u32 s78, s78, s80\n\t"
        "s_lshl_b32 s78, s78, 11\n\t"
        "buffer_load_dwordx4 v4, s[16:19], s78 offen lds\n\t"
        "s_add_u32 s80, s80, 1\n\t"
        "s_mul_i32 s78, s80, 0x1080\n\t"
        "s_add_u32 m0, s78, s81\n\t"
        "s_lshr_b32 s78, s80, 1\n\t"
        "s_lshl1_add_u32 s78, s78, s80\n\t"
        "s_lshl_b32 s78, s78, 11\n\t"
        "buffer_load_dwordx4 v4, s[16:19], s78 offen lds\n\t"
        "s_add_u32 s80, s80, 1\n\t"
        "s_mul_i32 s78, s80, 0x1080\n\t"
        "s_add_u32 m0, s78, s81\n\t"
        "s_lshr_b32 s78, s80, 1\n\t"
        "s_lshl1_add_u32 s78, s78, s80\n\t"
        "s_lshl_b32 s78, s78, 11\n\t"
        "buffer_load_dwordx4 v4, s[16:19], s78 offen lds\n\t"
        "s_add_u32 s80, s80, 1\n\t"
        "s_mul_i32 s78, s80, 0x1080\n\t"
        "s_add_u32 m0, s78, s81\n\t"
        "s_lshr_b32 s78, s80, 1\n\t"
        "s_lshl1_add_u32 s78, s78, s80\n\t"
        "s_lshl_b32 s78, s78, 11\n\t"
        "buffer_load_dwordx4 v4, s[16:19], s78 offen lds\n\t"
        "s_add_u32 s80, s80, 1\n\t"
        "s_mul_i32 s78, s80, 0x1080\n\t"
        "s_add_u32 m0, s78, s81\n\t"
        "s_lshr_b32 s78, s80, 1\n\t"
        "s_lshl1_add_u32 s78, s78, s80\n\t"
        "s_lshl_b32 s78, s78, 11\n\t"
        "buffer_load_dwordx4 v4, s[16:19], s78 offen lds\n\t"
        "s_add_u32 s80, s80, 1\n\t"
        "s_lshl2_add_u32 s78, s82, s69\n\t"
        "s_lshl_b32 s78, s78, 10\n\t"
        "s_add_u32 m0, s78, 46336\n\t"
        "s_add_u32 s82, s82, 1\n\t"
        "s_cmp_eq_u32 s82, 5\n\t"
        "buffer_load_dwordx4 v5, s[56:59], s78 offen lds\n\t"
        "s_cselect_b32 s82, 0, s82\n\t"
        "s_lshl2_add_u32 s78, s82, s69\n\t"
        "s_lshl_b32 s78, s78, 10\n\t"
        "s_add_u32 m0, s78, 46336\n\t"
        "s_add_u32 s82, s82, 1\n\t"
        "s_cmp_eq_u32 s82, 5\n\t"
        "buffer_load_dwordx4 v5, s[56:59], s78 offen lds\n\t"
        "s_cselect_b32 s82, 0, s82\n\t"
        "s_lshl2_add_u32 s78, s82, s69\n\t"
        "s_lshl_b32 s78, s78, 10\n\t"
        "s_add_u32 m0, s78, 46336\n\t"
        "s_add_u32 s82, s82, 1\n\t"
        "s_cmp_eq_u32 s82, 5\n\t"
        "buffer_load_dwordx4 v5, s[56:59], s78 offen lds\n\t"
        "s_cselect_b32 s82, 0, s82\n\t"
        "s_lshl2_add_u32 s78, s82, s69\n\t"
        "s_lshl_b32 s78, s78, 10\n\t"
        "s_add_u32 m0, s78, 46336\n\t"
        "s_add_u32 s82, s82, 1\n\t"
        "s_cmp_eq_u32 s82, 5\n\t"
        "buffer_load_dwordx4 v5, s[56:59], s78 offen lds\n\t"
        "s_cselect_b32 s82, 0, s82\n\t"
        "s_mov_b64 exec, 1\n\t"
        "ds_add_u32 v2, v3 offset:80\n\t"
        "s_mov_b64 exec, -1\n\t"
        "s_waitcnt vmcnt(4)\n\t"
        "s_mov_b64 exec, 1\n\t"
        "ds_add_u32 v2, v3 offset:0\n\t"
        "s_mov_b64 exec, -1\n"
        ".L7_w70:\n\t"
        "ds_read_b64 v[12:13], v2 offset:72\n\t"
        "s_waitcnt lgkmcnt(0)\n\t"
        "v_cmp_gt_u16 vcc, 2, v12\n\t"
        "s_cbranch_vccz .L7_d71\n\t"
        "s_sleep 1\n\t"
        "s_sub_i32 s83, s83, 1\n\t"
        "s_cmp_gt_i32 s83, 0\n\t"
        "s_cbranch_scc1 .L7_w70\n"
        ".L7_d71:\n\t"
        "v_readfirstlane_b32 s68, v13\n"
        ".L7_w72:\n\t"
        "ds_read_b32 v0, v2 offset:8\n\t"
        "s_waitcnt lgkmcnt(0)\n\t"
        "v_cmp_gt_u16 vcc, 2, v0\n\t"
        "s_cbranch_vccz .L7_d73\n\t"
        "s_sleep 1\n\t"
        "s_sub_i32 s83, s83, 1\n\t"
        "s_cmp_gt_i32 s83, 0\n\t"
        "s_cbranch_scc1 .L7_w72\n"
        ".L7_d73:\n\t"
        "ds_read_b32 v5, v31 offset:512\n\t"
        "ds_read_b32 v24, v24 offset:512\n\t"
        "ds_read_b32 v25, v25 offset:512\n\t"
        "ds_read_b32 v26, v26 offset:512\n\t"
        "ds_read_b32 v27, v27 offset:512\n\t"
        "ds_read_b32 v28, v28 offset:512\n\t"
        "s_waitcnt lgkmcnt(0)\n\t"
        "v_dot4_u32_u8 v31, v5, s38, v7\n\t"
        "s_waitcnt vmcnt(0)\n\t"
        "s_mov_b64 exec, 1\n\t"
        "ds_add_u32 v2, v3 offset:4\n\t"
        "s_mov_b64 exec, -1\n\t"
        "s_setprio 3\n\t"
        "s_lshl_b32 s44, s68, 24\n\t"
        "s_xor_b32 s45, s44, 0x5a000000\n\t"
        "s_mul_i32 s46, s68, 0x01010000\n\t"
        "s_mul_i32 s47, s68, 0x02000200\n\t"
        "s_and_b32 s47, s47, 0xff000000\n\t"
        "s_or_b32 s47, s47, 0xff0000\n"
        ".L7_w74:\n\t"
        "ds_read_b32 v0, v2 offset:92\n\t"
        "s_waitcnt lgkmcnt(0)\n\t"
        "v_cmp_gt_u16 vcc, 1, v0\n\t"
        "s_cbranch_vccz .L7_d75\n\t"
        "s_sleep 1\n\t"
        "s_sub_i32 s83, s83, 1\n\t"
        "s_cmp_gt_i32 s83, 0\n\t"
        "s_cbranch_scc1 .L7_w74\n"
        ".L7_d75:\n\t"
        "v_readfirstlane_b32 s78, v0\n\t"
        "s_sub_u32 s78, s78, 1\n\t"
        "s_add_u32 s77, s74, s78\n"
        ".L7_g76:\n\t"
        "s_memrealtime s[78:79]\n\t"
        "s_waitcnt lgkmcnt(0)\n\t"
        "s_sub_u32 s78, s78, s77\n\t"
        "s_cmp_ge_i32 s78, 315\n\t"
        "s_cbranch_scc1 .L7_h77\n\t"
        "s_sleep 1\n\t"
        "s_branch .L7_g76\n"
        ".L7_h77:\n\t"
        "s_branch .L7_ll\n"
        ".L7_again:\n\t"
        "s_waitcnt lgkmcnt(0)\n\t"
        "s_sleep 8\n\t"
        "s_sub_i32 s83, s83, 16\n\t"
        "s_cmp_gt_i32 s83, 0\n\t"
        "s_cbranch_scc0 .L7_got\n"
        ".L7_ll:\n\t"
        "buffer_load_dwordx4 v[64:67], v18, s[20:23], s24 offen sc1\n\t"
        "buffer_load_dwordx4 v[68:71], v18, s[20:23], s25 offen sc1\n\t"
        "buffer_load_dwordx4 v[72:75], v18, s[20:23], s26 offen sc1\n\t"
        "buffer_load_dwordx4 v[76:79], v18, s[20:23], s27 offen sc1\n\t"
        "s_mov_b64 exec, 1\n\t"
        "ds_add_u32 v2, v3 offset:108\n\t"
        "s_mov_b64 exec, -1\n"
        ".L7_w78:\n\t"
        "ds_read_b32 v0, v2 offset:4\n\t"
        "s_waitcnt lgkmcnt(0)\n\t"
        "v_cmp_gt_u16 vcc, 4, v0\n\t"
        "s_cbranch_vccz .L7_d79\n\t"
        "s_sleep 1\n\t"
        "s_sub_i32 s83, s83, 1\n\t"
        "s_cmp_gt_i32 s83, 0\n\t"
        "s_cbranch_scc1 .L7_w78\n"
        ".L7_d79:\n\t"
        "s_waitcnt vmcnt(3)\n\t"
        "v_xor_b32 v5, s44, v64\n\t"
        "v_bitop3_b32 v5, v65, v5, s45 bitop3:0xde\n\t"
        "v_bitop3_b32 v5, v66, v5, s46 bitop3:0xde\n\t"
        "v_xor_b32 v4, s47, v67\n\t"
        "v_lshlrev_b32_sdwa v112, 1, v64 dst_sel:DWORD dst_unused:UNUSED_PAD src0_sel:DWORD src1_sel:WORD_0\n\t"
        "ds_read_u16 v112, v112 offset:46336\n\t"
        "v_lshlrev_b32_sdwa v113, 1, v65 dst_sel:DWORD dst_unused:UNUSED_PAD src0_sel:DWORD src1_sel:WORD_0\n\t"
        "ds_read_u16 v113, v113 offset:46336\n\t"
        "v_lshlrev_b32_sdwa v114, 1, v66 dst_sel:DWORD dst_unused:UNUSED_PAD src0_sel:DWORD src1_sel:WORD_0\n\t"
        "ds_read_u16 v114, v114 offset:46336\n\t"
        "v_dot4_u32_u8 v80, v64, s39, v7\n\t"
        "v_dot4_u32_u8 v81, v65, s39, v7\n\t"
        "v_dot4_u32_u8 v82, v66, s39, v7\n\t"
        "s_waitcnt vmcnt(2)\n\t"
        "v_bitop3_b32 v5, v68, v5, s44 bitop3:0xde\n\t"
        "v_bitop3_b32 v5, v69, v5, s45 bitop3:0xde\n\t"
        "v_bitop3_b32 v5, v70, v5, s46 bitop3:0xde\n\t"
        "v_bitop3_b32 v4, v71, v4, s47 bitop3:0xde\n\t"
        "v_lshlrev_b32_sdwa v116, 1, v68 dst_sel:DWORD dst_unused:UNUSED_PAD src0_sel:DWORD src1_sel:WORD_0\n\t"
        "ds_read_u16 v116, v116 offset:46336\n\t"
        "v_lshlrev_b32_sdwa v117, 1, v69 dst_sel:DWORD dst_unused:UNUSED_PAD src0_sel:DWORD src1_sel:WORD_0\n\t"
        "ds_read_u16 v117, v117 offset:46336\n\t"
        "v_lshlrev_b32_sdwa v118, 1, v70 dst_sel:DWORD dst_unused:UNUSED_PAD src0_sel:DWORD src1_sel:WORD_0\n\t"
        "ds_read_u16 v118, v118 offset:46336\n\t"
        "v_dot4_u32_u8 v84, v68, s39, v7\n\t"
        "v_dot4_u32_u8 v85, v69, s39, v7\n\t"
        "v_dot4_u32_u8 v86, v70, s39, v7\n\t"
        "s_waitcnt vmcnt(1)\n\t"
        "v_bitop3_b32 v5, v72, v5, s44 bitop3:0xde\n\t"
        "v_bitop3_b32 v5, v73, v5, s45 bitop3:0xde\n\t"
        "v_bitop3_b32 v5, v74, v5, s46 bitop3:0xde\n\t"
        "v_bitop3_b32 v4, v75, v4, s47 bitop3:0xde\n\t"
        "v_lshlrev_b32_sdwa v120, 1, v72 dst_sel:DWORD dst_unused:UNUSED_PAD src0_sel:DWORD src1_sel:WORD_0\n\t"
        "ds_read_u16 v120, v120 offset:46336\n\t"
        "v_lshlrev_b32_sdwa v121, 1, v73 dst_sel:DWORD dst_unused:UNUSED_PAD src0_sel:DWORD src1_sel:WORD_0\n\t"
        "ds_read_u16 v121, v121 offset:46336\n\t"
        "v_lshlrev_b32_sdwa v122, 1, v74 dst_sel:DWORD dst_unused:UNUSED_PAD src0_sel:DWORD src1_sel:WORD_0\n\t"
        "ds_read_u16 v122, v122 offset:46336\n\t"
        "v_dot4_u32_u8 v88, v72, s39, v7\n\t"
        "v_dot4_u32_u8 v89, v73, s39, v7\n\t"
        "v_dot4_u32_u8 v90, v74, s39, v7\n\t"
        "s_waitcnt vmcnt(0)\n\t"
        "v_bitop3_b32 v5, v76, v5, s44 bitop3:0xde\n\t"
        "v_bitop3_b32 v5, v77, v5, s45 bitop3:0xde\n\t"
        "v_bitop3_b32 v5, v78, v5, s46 bitop3:0xde\n\t"
        "v_bitop3_b32 v4, v79, v4, s47 bitop3:0xde\n\t"
        "v_lshlrev_b32_sdwa v124, 1, v76 dst_sel:DWORD dst_unused:UNUSED_PAD src0_sel:DWORD src1_sel:WORD_0\n\t"
        "ds_read_u16 v124, v124 offset:46336\n\t"
        "v_lshlrev_b32_sdwa v125, 1, v77 dst_sel:DWORD dst_unused:UNUSED_PAD src0_sel:DWORD src1_sel:WORD_0\n\t"
        "ds_read_u16 v125, v125 offset:46336\n\t"
        "v_lshlrev_b32_sdwa v126, 1, v78 dst_sel:DWORD dst_unused:UNUSED_PAD src0_sel:DWORD src1_sel:WORD_0\n\t"
        "ds_read_u16 v126, v126 offset:46336\n\t"
        "v_dot4_u32_u8 v92, v76, s39, v7\n\t"
        "v_dot4_u32_u8 v93, v77, s39, v7\n\t"
        "v_dot4_u32_u8 v94, v78, s39, v7\n\t"
        "v_and_b32 v6, 0xfe000000, v4\n\t"
        "v_or_b32 v5, v5, v6\n\t"
        "v_cmp_lt_u32 vcc, s35, v5\n\t"
        "s_cbranch_vccnz .L7_again\n"
        ".L7_got:\n\t"
        "v_and_b32 v6, s35, v4\n\t"
        "s_waitcnt lgkmcnt(2)\n\t"
        "ds_add_rtn_u32 v96, v80, v29 offset:192\n\t"
        "ds_add_rtn_u32 v97, v81, v29 offset:192\n\t"
        "ds_add_rtn_u32 v98, v82, v29 offset:192\n\t"
        "ds_add_rtn_u32 v100, v84, v29 offset:192\n\t"
        "ds_add_rtn_u32 v101, v85, v29 offset:192\n\t"
        "ds_add_rtn_u32 v102, v86, v29 offset:192\n\t"
        "ds_add_rtn_u32 v104, v88, v29 offset:192\n\t"
        "ds_add_rtn_u32 v105, v89, v29 offset:192\n\t"
        "ds_add_rtn_u32 v106, v90, v29 offset:192\n\t"
        "ds_add_rtn_u32 v108, v92, v29 offset:192\n\t"
        "ds_add_rtn_u32 v109, v93, v29 offset:192\n\t"
        "ds_add_rtn_u32 v110, v94, v29 offset:192\n\t"
        "v_cmp_ne_u32 vcc, 0, v6\n\t"
        "s_cbranch_vccnz .L7_x\n"
        ".L7_y:\n\t"
        "s_mov_b64 exec, 1\n\t"
        "ds_add_u32 v2, v3 offset:32\n\t"
        "s_mov_b64 exec, -1\n\t"
        "v_perm_b32 v112, v112, v64, s49\n\t"
        "v_perm_b32 v113, v113, v65, s49\n\t"
        "v_perm_b32 v114, v114, v66, s49\n\t"
        "v_perm_b32 v116, v116, v68, s49\n\t"
        "v_perm_b32 v117, v117, v69, s49\n\t"
        "v_perm_b32 v118, v118, v70, s49\n\t"
        "v_perm_b32 v120, v120, v72, s49\n\t"
        "v_perm_b32 v121, v121, v73, s49\n\t"
        "v_perm_b32 v122, v122, v74, s49\n\t"
        "v_perm_b32 v124, v124, v76, s49\n\t"
        "v_cmp_lt_u32 vcc, s35, v4\n\t"
        "s_cmp_lg_u64 vcc, 0\n\t"
        "s_cselect_b32 s78, 0x100000, 0\n\t"
        "v_or_b32 v30, s78, v30\n\t"
        "s_waitcnt lgkmcnt(1)\n\t"
        "v_perm_b32 v125, v125, v77, s49\n\t"
        "v_perm_b32 v126, v126, v78, s49\n\t"
        "v_bfe_u32 v96, v96, s33, 8\n\t"
        "v_bfe_u32 v97, v97, s33, 8\n\t"
        "v_bfe_u32 v98, v98, s33, 8\n\t"
        "v_bfe_u32 v100, v100, s33, 8\n\t"
        "v_bfe_u32 v101, v101, s33, 8\n\t"
        "v_bfe_u32 v102, v102, s33, 8\n\t"
        "v_bfe_u32 v104, v104, s33, 8\n\t"
        "v_bfe_u32 v105, v105, s33, 8\n\t"
        "v_bfe_u32 v106, v106, s33, 8\n\t"
        "v_bfe_u32 v108, v108, s33, 8\n\t"
        "v_bfe_u32 v109, v109, s33, 8\n\t"
        "v_bfe_u32 v110, v110, s33, 8\n"
        ".L7_w80:\n\t"
        "ds_read_b32 v0, v2 offset:32\n\t"
        "s_waitcnt lgkmcnt(0)\n\t"
        "v_cmp_gt_u16 vcc, 4, v0\n\t"
        "s_cbranch_vccz .L7_d81\n\t"
        "s_sleep 1\n\t"
        "s_sub_i32 s83, s83, 1\n\t"
        "s_cmp_gt_i32 s83, 0\n\t"
        "s_cbranch_scc1 .L7_w80\n"
        ".L7_d81:\n\t"
        "ds_read_b32 v32, v80 offset:192\n\t"
        "ds_read_b32 v33, v81 offset:192\n\t"
        "ds_read_b32 v34, v82 offset:192\n\t"
        "ds_read_b32 v35, v84 offset:192\n\t"
        "ds_read_b32 v36, v85 offset:192\n\t"
        "ds_read_b32 v37, v86 offset:192\n\t"
        "ds_read_b32 v38, v88 offset:192\n\t"
        "ds_read_b32 v39, v89 offset:192\n\t"
        "ds_read_b32 v40, v90 offset:192\n\t"
        "ds_read_b32 v41, v92 offset:192\n\t"
        "ds_read_b32 v42, v93 offset:192\n\t"
        "ds_read_b32 v43, v94 offset:192\n\t"
        "ds_read_b32 v6, v31 offset:192\n\t"
        "s_waitcnt lgkmcnt(10)\n\t"
        "v_dot4_u32_u8 v96, v32, s32, v96\n\t"
        "v_dot4_u32_u8 v97, v33, s32, v97\n\t"
        "v_dot4_u32_u8 v98, v34, s32, v98\n\t"
        "s_waitcnt lgkmcnt(7)\n\t"
        "v_dot4_u32_u8 v100, v35, s32, v100\n\t"
        "v_dot4_u32_u8 v101, v36, s32, v101\n\t"
        "v_dot4_u32_u8 v102, v37, s32, v102\n\t"
        "s_waitcnt lgkmcnt(4)\n\t"
        "v_dot4_u32_u8 v104, v38, s32, v104\n\t"
        "v_dot4_u32_u8 v105, v39, s32, v105\n\t"
        "v_dot4_u32_u8 v106, v40, s32, v106\n\t"
        "s_waitcnt lgkmcnt(1)\n\t"
        "v_dot4_u32_u8 v108, v41, s32, v108\n\t"
        "v_dot4_u32_u8 v109, v42, s32, v109\n\t"
        "v_dot4_u32_u8 v110, v43, s32, v110\n\t"
        "v_min_u32 v96, 47, v96\n\t"
        "v_min_u32 v97, 47, v97\n\t"
        "v_min_u32 v98, 47, v98\n\t"
        "v_min_u32 v100, 47, v100\n\t"
        "v_min_u32 v101, 47, v101\n\t"
        "v_min_u32 v102, 47, v102\n\t"
        "v_min_u32 v104, 47, v104\n\t"
        "v_min_u32 v105, 47, v105\n\t"
        "v_min_u32 v106, 47, v106\n\t"
        "v_min_u32 v108, 47, v108\n\t"
        "v_min_u32 v109, 47, v109\n\t"
        "v_min_u32 v110, 47, v110\n\t"
        "v_lshl_add_u32 v96, v96, 2, v80\n\t"
        "ds_write_b32 v96, v112\n\t"
        "v_lshl_add_u32 v97, v97, 2, v81\n\t"
        "ds_write_b32 v97, v113\n\t"
        "v_lshl_add_u32 v98, v98, 2, v82\n\t"
        "ds_write_b32 v98, v114\n\t"
        "v_lshl_add_u32 v100, v100, 2, v84\n\t"
        "ds_write_b32 v100, v116\n\t"
        "v_lshl_add_u32 v101, v101, 2, v85\n\t"
        "ds_write_b32 v101, v117\n\t"
        "v_lshl_add_u32 v102, v102, 2, v86\n\t"
        "ds_write_b32 v102, v118\n\t"
        "v_lshl_add_u32 v104, v104, 2, v88\n\t"
        "ds_write_b32 v104, v120\n\t"
        "v_lshl_add_u32 v105, v105, 2, v89\n\t"
        "ds_write_b32 v105, v121\n\t"
        "v_lshl_add_u32 v106, v106, 2, v90\n\t"
        "ds_write_b32 v106, v122\n\t"
        "v_lshl_add_u32 v108, v108, 2, v92\n\t"
        "ds_write_b32 v108, v124\n\t"
        "v_lshl_add_u32 v109, v109, 2, v93\n\t"
        "ds_write_b32 v109, v125\n\t"
        "v_lshl_add_u32 v110, v110, 2, v94\n\t"
        "ds_write_b32 v110, v126\n\t"
        "s_cmp_lg_u32 s36, 0\n\t"
        "s_cbranch_scc1 .L7_z\n"
        ".L7_zr:\n\t"
        "s_mov_b64 exec, 1\n\t"
        "ds_add_u32 v2, v30 offset:36\n\t"
        "s_mov_b64 exec, -1\n\t"
        "s_waitcnt lgkmcnt(13)\n\t"
        "v_dot4_u32_u8 v6, v6, s37, -1\n\t"
        "v_mov_b64 v[32:33], 0\n\t"
        "v_mov_b64 v[34:35], 0\n\t"
        "v_mov_b64 v[36:37], 0\n\t"
        "v_med3_i32 v6, v6, 0, 47\n\t"
        "v_lshrrev_b32 v6, 1, v6\n\t"
        "v_mov_b64 v[38:39], 0\n\t"
        "v_dot4_u32_u8 v63, v24, s38, v7\n\t"
        "v_max_u32_dpp v6, v6, v6 quad_perm:[1,0,3,2] row_mask:0xf bank_mask:0xf\n\t"
        "s_nop 1\n\t"
        "v_max_u32_dpp v6, v6, v6 quad_perm:[2,3,0,1] row_mask:0xf bank_mask:0xf\n\t"
        "s_nop 0\n\t"
        "v_readlane_b32 s24, v6, 0\n\t"
        "v_readlane_b32 s25, v6, 4\n\t"
        "v_readlane_b32 s26, v6, 8\n\t"
        "v_readlane_b32 s27, v6, 12\n\t"
        "v_readlane_b32 s28, v6, 16\n\t"
        "s_max_u32 s28, s28, 5\n\t"
        "s_mov_b32 s56, s24\n\t"
        "s_mov_b32 s57, s24\n\t"
        "s_mov_b32 s78, 0xf0000\n"
        ".L7_w82:\n\t"
        "ds_read_b32 v4, v2 offset:36\n\t"
        "ds_read_b32 v12, v31 offset:188\n\t"
        "s_waitcnt lgkmcnt(1)\n\t"
        "v_cmp_gt_u16 vcc, 4, v4\n\t"
        "s_cbranch_vccz .L7_d83\n\t"
        "s_waitcnt lgkmcnt(0)\n\t"
        "s_sleep 1\n\t"
        "s_sub_i32 s83, s83, 1\n\t"
        "s_cmp_gt_i32 s83, 0\n\t"
        "s_cbranch_scc1 .L7_w82\n"
        ".L7_d83:\n\t"
        "ds_read2_b32 v[40:41], v63 offset0:0 offset1:1\n\t"
        "s_sub_u32 s56, s56, 1\n\t"
        "s_cbranch_scc1 .L7_i84\n"
        ".L7_j85:\n\t"
        "ds_read2_b32 v[42:43], v63 offset0:2 offset1:3\n\t"
        "s_sub_u32 s56, s56, 1\n\t"
        "s_cbranch_scc1 .L7_i86\n"
        ".L7_j87:\n\t"
        "ds_read2_b32 v[44:45], v63 offset0:4 offset1:5\n\t"
        "s_sub_u32 s56, s56, 1\n\t"
        "s_cbranch_scc1 .L7_i88\n"
        ".L7_j89:\n\t"
        "ds_read2_b32 v[46:47], v63 offset0:6 offset1:7\n\t"
        "s_sub_u32 s56, s56, 1\n\t"
        "s_cbranch_scc1 .L7_i90\n"
        ".L7_j91:\n\t"
        "ds_read2_b32 v[48:49], v63 offset0:8 offset1:9\n\t"
        "s_sub_u32 s56, s56, 1\n\t"
        "s_cbranch_scc1 .L7_i92\n"
        ".L7_j93:\n\t"
        "ds_read2_b32 v[50:51], v63 offset0:10 offset1:11\n\t"
        "s_sub_u32 s56, s56, 1\n\t"
        "s_cbranch_scc1 .L7_i94\n"
        ".L7_j95:\n\t"
        "ds_read2_b32 v[52:53], v63 offset0:12 offset1:13\n\t"
        "s_sub_u32 s56, s56, 1\n\t"
        "s_cbranch_scc1 .L7_i96\n"
        ".L7_j97:\n\t"
        "ds_read2_b32 v[54:55], v63 offset0:14 offset1:15\n\t"
        "s_sub_u32 s56, s56, 1\n\t"
        "s_cbranch_scc1 .L7_i98\n"
        ".L7_j99:\n\t"
        "s_waitcnt lgkmcnt(8)\n\t"
        "v_and_or_b32 v12, v4, s78, v12\n\t"
        "v_cmp_ne_u32 vcc, 0xffff, v12\n\t"
        "s_cbranch_vccnz .L7_exit4\n\t"
        "s_waitcnt lgkmcnt(7)\n\t"
        "v_mad_u32_u16 v10, v40, s48, v62\n\t"
        "v_mad_u32_u16 v11, v41, s48, v62\n\t"
        "buffer_load_dwordx4 v[64:67], v10, s[16:19], 0 offen\n\t"
        "buffer_load_dwordx4 v[68:71], v11, s[16:19], 0 offen\n\t"
        "s_waitcnt lgkmcnt(6)\n\t"
        "v_mad_u32_u16 v10, v42, s48, v62\n\t"
        "v_mad_u32_u16 v11, v43, s48, v62\n\t"
        "buffer_load_dwordx4 v[72:75], v10, s[16:19], 0 offen\n\t"
        "buffer_load_dwordx4 v[76:79], v11, s[16:19], 0 offen\n\t"
        "s_waitcnt lgkmcnt(5)\n\t"
        "v_mad_u32_u16 v10, v44, s48, v62\n\t"
        "v_mad_u32_u16 v11, v45, s48, v62\n\t"
        "buffer_load_dwordx4 v[80:83], v10, s[16:19], 0 offen\n\t"
        "buffer_load_dwordx4 v[84:87], v11, s[16:19], 0 offen\n\t"
        "s_waitcnt lgkmcnt(4)\n\t"
        "v_mad_u32_u16 v10, v46, s48, v62\n\t"
        "v_mad_u32_u16 v11, v47, s48, v62\n\t"
        "buffer_load_dwordx4 v[88:91], v10, s[16:19], 0 offen\n\t"
        "buffer_load_dwordx4 v[92:95], v11, s[16:19], 0 offen\n\t"
        "s_waitcnt lgkmcnt(3)\n\t"
        "v_mad_u32_u16 v10, v48, s48, v62\n\t"
        "v_mad_u32_u16 v11, v49, s48, v62\n\t"
        "buffer_load_dwordx4 v[96:99], v10, s[16:19], 0 offen\n\t"
        "buffer_load_dwordx4 v[100:103], v11, s[16:19], 0 offen\n\t"
        "s_waitcnt lgkmcnt(2)\n\t"
        "v_mad_u32_u16 v10, v50, s48, v62\n\t"
        "v_mad_u32_u16 v11, v51, s48, v62\n\t"
        "buffer_load_dwordx4 v[104:107], v10, s[16:19], 0 offen\n\t"
        "buffer_load_dwordx4 v[108:111], v11, s[16:19], 0 offen\n\t"
        "s_min_u32 s59, s56, s57\n\t"
        "s_add_u32 s58, s59, 1\n\t"
        "s_setprio 3\n"
        ".L7_loop:\n\t"
        "v_add_u32 v63, 64, v63\n\t"
        "v_perm_b32 v12, v41, v40, v8\n\t"
        "v_perm_b32 v13, v41, v40, v9\n\t"
        "s_waitcnt vmcnt(10) lgkmcnt(1)\n\t"
        "v_mad_u32_u16 v10, v52, s48, v62\n\t"
        "v_mad_u32_u16 v11, v53, s48, v62\n\t"
        "buffer_load_dwordx4 v[112:115], v10, s[16:19], 0 offen\n\t"
        "buffer_load_dwordx4 v[116:119], v11, s[16:19], 0 offen\n\t"
        "v_mfma_f32_4x4x4_16b_bf16 v[32:35], v[12:13], v[64:65], v[32:35] cbsz:1 abid:0\n\t"
        "v_mfma_f32_4x4x4_16b_bf16 v[36:39], v[12:13], v[66:67], v[36:39] cbsz:1 abid:0\n\t"
        "ds_read2_b32 v[40:41], v63 offset0:0 offset1:1\n\t"
        "v_mfma_f32_4x4x4_16b_bf16 v[32:35], v[12:13], v[68:69], v[32:35] cbsz:1 abid:1\n\t"
        "v_mfma_f32_4x4x4_16b_bf16 v[36:39], v[12:13], v[70:71], v[36:39] cbsz:1 abid:1\n\t"
        "s_sub_u32 s59, s59, 1\n\t"
        "s_cbranch_scc1 .L7_i100\n"
        ".L7_j101:\n\t"
        "v_perm_b32 v12, v43, v42, v8\n\t"
        "v_perm_b32 v13, v43, v42, v9\n\t"
        "s_waitcnt vmcnt(10) lgkmcnt(1)\n\t"
        "v_mad_u32_u16 v10, v54, s48, v62\n\t"
        "v_mad_u32_u16 v11, v55, s48, v62\n\t"
        "buffer_load_dwordx4 v[120:123], v10, s[16:19], 0 offen\n\t"
        "buffer_load_dwordx4 v[124:127], v11, s[16:19], 0 offen\n\t"
        "v_mfma_f32_4x4x4_16b_bf16 v[32:35], v[12:13], v[72:73], v[32:35] cbsz:1 abid:0\n\t"
        "v_mfma_f32_4x4x4_16b_bf16 v[36:39], v[12:13], v[74:75], v[36:39] cbsz:1 abid:0\n\t"
        "ds_read2_b32 v[42:43], v63 offset0:2 offset1:3\n\t"
        "v_mfma_f32_4x4x4_16b_bf16 v[32:35], v[12:13], v[76:77], v[32:35] cbsz:1 abid:1\n\t"
        "v_mfma_f32_4x4x4_16b_bf16 v[36:39], v[12:13], v[78:79], v[36:39] cbsz:1 abid:1\n\t"
        "s_sub_u32 s59, s59, 1\n\t"
        "s_cbranch_scc1 .L7_i102\n"
        ".L7_j103:\n\t"
        "v_perm_b32 v12, v45, v44, v8\n\t"
        "v_perm_b32 v13, v45, v44, v9\n\t"
        "s_waitcnt vmcnt(10) lgkmcnt(1)\n\t"
        "v_mad_u32_u16 v10, v40, s48, v62\n\t"
        "v_mad_u32_u16 v11, v41, s48, v62\n\t"
        "buffer_load_dwordx4 v[64:67], v10, s[16:19], 0 offen\n\t"
        "buffer_load_dwordx4 v[68:71], v11, s[16:19], 0 offen\n\t"
        "v_mfma_f32_4x4x4_16b_bf16 v[32:35], v[12:13], v[80:81], v[32:35] cbsz:1 abid:0\n\t"
        "v_mfma_f32_4x4x4_16b_bf16 v[36:39], v[12:13], v[82:83], v[36:39] cbsz:1 abid:0\n\t"
        "ds_read2_b32 v[44:45], v63 offset0:4 offset1:5\n\t"
        "v_mfma_f32_4x4x4_16b_bf16 v[32:35], v[12:13], v[84:85], v[32:35] cbsz:1 abid:1\n\t"
        "v_mfma_f32_4x4x4_16b_bf16 v[36:39], v[12:13], v[86:87], v[36:39] cbsz:1 abid:1\n\t"
        "s_sub_u32 s59, s59, 1\n\t"
        "s_cbranch_scc1 .L7_i104\n"
        ".L7_j105:\n\t"
        "v_perm_b32 v12, v47, v46, v8\n\t"
        "v_perm_b32 v13, v47, v46, v9\n\t"
        "s_waitcnt vmcnt(10) lgkmcnt(1)\n\t"
        "v_mad_u32_u16 v10, v42, s48, v62\n\t"
        "v_mad_u32_u16 v11, v43, s48, v62\n\t"
        "buffer_load_dwordx4 v[72:75], v10, s[16:19], 0 offen\n\t"
        "buffer_load_dwordx4 v[76:79], v11, s[16:19], 0 offen\n\t"
        "v_mfma_f32_4x4x4_16b_bf16 v[32:35], v[12:13], v[88:89], v[32:35] cbsz:1 abid:0\n\t"
        "v_mfma_f32_4x4x4_16b_bf16 v[36:39], v[12:13], v[90:91], v[36:39] cbsz:1 abid:0\n\t"
        "ds_read2_b32 v[46:47], v63 offset0:6 offset1:7\n\t"
        "v_mfma_f32_4x4x4_16b_bf16 v[32:35], v[12:13], v[92:93], v[32:35] cbsz:1 abid:1\n\t"
        "v_mfma_f32_4x4x4_16b_bf16 v[36:39], v[12:13], v[94:95], v[36:39] cbsz:1 abid:1\n\t"
        "s_sub_u32 s59, s59, 1\n\t"
        "s_cbranch_scc1 .L7_i106\n"
        ".L7_j107:\n\t"
        "v_perm_b32 v12, v49, v48, v8\n\t"
        "v_perm_b32 v13, v49, v48, v9\n\t"
        "s_waitcnt vmcnt(10) lgkmcnt(1)\n\t"
        "v_mad_u32_u16 v10, v44, s48, v62\n\t"
        "v_mad_u32_u16 v11, v45, s48, v62\n\t"
        "buffer_load_dwordx4 v[80:83], v10, s[16:19], 0 offen\n\t"
        "buffer_load_dwordx4 v[84:87], v11, s[16:19], 0 offen\n\t"
        "v_mfma_f32_4x4x4_16b_bf16 v[32:35], v[12:13], v[96:97], v[32:35] cbsz:1 abid:0\n\t"
        "v_mfma_f32_4x4x4_16b_bf16 v[36:39], v[12:13], v[98:99], v[36:39] cbsz:1 abid:0\n\t"
        "ds_read2_b32 v[48:49], v63 offset0:8 offset1:9\n\t"
        "v_mfma_f32_4x4x4_16b_bf16 v[32:35], v[12:13], v[100:101], v[32:35] cbsz:1 abid:1\n\t"
        "v_mfma_f32_4x4x4_16b_bf16 v[36:39], v[12:13], v[102:103], v[36:39] cbsz:1 abid:1\n\t"
        "s_sub_u32 s59, s59, 1\n\t"
        "s_cbranch_scc1 .L7_i108\n"
        ".L7_j109:\n\t"
        "v_perm_b32 v12, v51, v50, v8\n\t"
        "v_perm_b32 v13, v51, v50, v9\n\t"
        "s_waitcnt vmcnt(10) lgkmcnt(1)\n\t"
        "v_mad_u32_u16 v10, v46, s48, v62\n\t"
        "v_mad_u32_u16 v11, v47, s48, v62\n\t"
        "buffer_load_dwordx4 v[88:91], v10, s[16:19], 0 offen\n\t"
        "buffer_load_dwordx4 v[92:95], v11, s[16:19], 0 offen\n\t"
        "v_mfma_f32_4x4x4_16b_bf16 v[32:35], v[12:13], v[104:105], v[32:35] cbsz:1 abid:0\n\t"
        "v_mfma_f32_4x4x4_16b_bf16 v[36:39], v[12:13], v[106:107], v[36:39] cbsz:1 abid:0\n\t"
        "ds_read2_b32 v[50:51], v63 offset0:10 offset1:11\n\t"
        "v_mfma_f32_4x4x4_16b_bf16 v[32:35], v[12:13], v[108:109], v[32:35] cbsz:1 abid:1\n\t"
        "v_mfma_f32_4x4x4_16b_bf16 v[36:39], v[12:13], v[110:111], v[36:39] cbsz:1 abid:1\n\t"
        "s_sub_u32 s59, s59, 1\n\t"
        "s_cbranch_scc1 .L7_i110\n"
        ".L7_j111:\n\t"
        "v_perm_b32 v12, v53, v52, v8\n\t"
        "v_perm_b32 v13, v53, v52, v9\n\t"
        "s_waitcnt vmcnt(10) lgkmcnt(1)\n\t"
        "v_mad_u32_u16 v10, v48, s48, v62\n\t"
        "v_mad_u32_u16 v11, v49, s48, v62\n\t"
        "buffer_load_dwordx4 v[96:99], v10, s[16:19], 0 offen\n\t"
        "buffer_load_dwordx4 v[100:103], v11, s[16:19], 0 offen\n\t"
        "v_mfma_f32_4x4x4_16b_bf16 v[32:35], v[12:13], v[112:113], v[32:35] cbsz:1 abid:0\n\t"
        "v_mfma_f32_4x4x4_16b_bf16 v[36:39], v[12:13], v[114:115], v[36:39] cbsz:1 abid:0\n\t"
        "ds_read2_b32 v[52:53], v63 offset0:12 offset1:13\n\t"
        "v_mfma_f32_4x4x4_16b_bf16 v[32:35], v[12:13], v[116:117], v[32:35] cbsz:1 abid:1\n\t"
        "v_mfma_f32_4x4x4_16b_bf16 v[36:39], v[12:13], v[118:119], v[36:39] cbsz:1 abid:1\n\t"
        "s_sub_u32 s59, s59, 1\n\t"
        "s_cbranch_scc1 .L7_i112\n"
        ".L7_j113:\n\t"
        "v_perm_b32 v12, v55, v54, v8\n\t"
        "v_perm_b32 v13, v55, v54, v9\n\t"
        "s_waitcnt vmcnt(10) lgkmcnt(1)\n\t"
        "v_mad_u32_u16 v10, v50, s48, v62\n\t"
        "v_mad_u32_u16 v11, v51, s48, v62\n\t"
        "buffer_load_dwordx4 v[104:107], v10, s[16:19], 0 offen\n\t"
        "buffer_load_dwordx4 v[108:111], v11, s[16:19], 0 offen\n\t"
        "v_mfma_f32_4x4x4_16b_bf16 v[32:35], v[12:13], v[120:121], v[32:35] cbsz:1 abid:0\n\t"
        "v_mfma_f32_4x4x4_16b_bf16 v[36:39], v[12:13], v[122:123], v[36:39] cbsz:1 abid:0\n\t"
        "ds_read2_b32 v[54:55], v63 offset0:14 offset1:15\n\t"
        "v_mfma_f32_4x4x4_16b_bf16 v[32:35], v[12:13], v[124:125], v[32:35] cbsz:1 abid:1\n\t"
        "v_mfma_f32_4x4x4_16b_bf16 v[36:39], v[12:13], v[126:127], v[36:39] cbsz:1 abid:1\n\t"
        "s_sub_u32 s59, s59, 1\n\t"
        "s_cbranch_scc0 .L7_loop\n\t"
        "s_branch .L7_i114\n"
        ".L7_i84:\n\t"
        "s_mov_b32 s60, 8\n\t"
        "s_swappc_b64 s[54:55], s[50:51]\n\t"
        "s_branch .L7_j85\n"
        ".L7_i86:\n\t"
        "s_mov_b32 s60, 16\n\t"
        "s_swappc_b64 s[54:55], s[50:51]\n\t"
        "s_branch .L7_j87\n"
        ".L7_i88:\n\t"
        "s_mov_b32 s60, 24\n\t"
        "s_swappc_b64 s[54:55], s[50:51]\n\t"
        "s_branch .L7_j89\n"
        ".L7_i90:\n\t"
        "s_mov_b32 s60, 32\n\t"
        "s_swappc_b64 s[54:55], s[50:51]\n\t"
        "s_branch .L7_j91\n"
        ".L7_i92:\n\t"
        "s_mov_b32 s60, 40\n\t"
        "s_swappc_b64 s[54:55], s[50:51]\n\t"
        "s_branch .L7_j93\n"
        ".L7_i94:\n\t"
        "s_mov_b32 s60, 48\n\t"
        "s_swappc_b64 s[54:55], s[50:51]\n\t"
        "s_branch .L7_j95\n"
        ".L7_i96:\n\t"
        "s_mov_b32 s60, 56\n\t"
        "s_swappc_b64 s[54:55], s[50:51]\n\t"
        "s_branch .L7_j97\n"
        ".L7_i98:\n\t"
        "s_mov_b32 s60, 64\n\t"
        "s_swappc_b64 s[54:55], s[50:51]\n\t"
        "s_branch .L7_j99\n"
        ".L7_i100:\n\t"
        "s_mov_b32 s60, 8\n\t"
        "s_swappc_b64 s[62:63], s[42:43]\n\t"
        "s_cbranch_vccnz .L7_dt1\n\t"
        "s_branch .L7_j101\n"
        ".L7_i102:\n\t"
        "s_mov_b32 s60, 16\n\t"
        "s_swappc_b64 s[62:63], s[42:43]\n\t"
        "s_cbranch_vccnz .L7_dt2\n\t"
        "s_branch .L7_j103\n"
        ".L7_i104:\n\t"
        "s_mov_b32 s60, 24\n\t"
        "s_swappc_b64 s[62:63], s[42:43]\n\t"
        "s_cbranch_vccnz .L7_dt3\n\t"
        "s_branch .L7_j105\n"
        ".L7_i106:\n\t"
        "s_mov_b32 s60, 32\n\t"
        "s_swappc_b64 s[62:63], s[42:43]\n\t"
        "s_cbranch_vccnz .L7_dt4\n\t"
        "s_branch .L7_j107\n"
        ".L7_i108:\n\t"
        "s_mov_b32 s60, 40\n\t"
        "s_swappc_b64 s[62:63], s[42:43]\n\t"
        "s_cbranch_vccnz .L7_dt5\n\t"
        "s_branch .L7_j109\n"
        ".L7_i110:\n\t"
        "s_mov_b32 s60, 48\n\t"
        "s_swappc_b64 s[62:63], s[42:43]\n\t"
        "s_cbranch_vccnz .L7_dt6\n\t"
        "s_branch .L7_j111\n"
        ".L7_i112:\n\t"
        "s_mov_b32 s60, 56\n\t"
        "s_swappc_b64 s[62:63], s[42:43]\n\t"
        "s_cbranch_vccnz .L7_dt7\n\t"
        "s_branch .L7_j113\n"
        ".L7_i114:\n\t"
        "s_mov_b32 s60, 64\n\t"
        "s_swappc_b64 s[62:63], s[42:43]\n\t"
        "s_cbranch_vccnz .L7_dt0\n\t"
        "s_branch .L7_loop\n"
        ".L7_ev:\n\t"
        "s_sub_u32 s56, s56, s58\n\t"
        "s_cbranch_scc0 .L7_e1\n\t"
        "s_swappc_b64 s[54:55], s[50:51]\n"
        ".L7_e1:\n\t"
        "s_sub_u32 s57, s57, s58\n\t"
        "s_cbranch_scc0 .L7_e2\n\t"
        "s_swappc_b64 s[54:55], s[52:53]\n"
        ".L7_e2:\n\t"
        "s_min_u32 s59, s56, s57\n\t"
        "s_add_u32 s58, s59, 1\n\t"
        "s_setpc_b64 s[62:63]\n"
        ".L7_dt0:\n\t"
        "v_perm_b32 v12, v41, v40, v8\n\t"
        "v_perm_b32 v13, v41, v40, v9\n\t"
        "s_waitcnt vmcnt(6)\n\t"
        "s_cmp_lt_u32 s57, 3\n\t"
        "s_cbranch_scc0 .L7_du0\n\t"
        "s_waitcnt vmcnt(2)\n\t"
        "s_cmp_eq_u32 s57, 0\n\t"
        "s_cbranch_scc0 .L7_du0\n\t"
        "s_waitcnt vmcnt(0)\n"
        ".L7_du0:\n\t"
        "v_mfma_f32_4x4x4_16b_bf16 v[32:35], v[12:13], v[64:65], v[32:35] cbsz:1 abid:0\n\t"
        "v_mfma_f32_4x4x4_16b_bf16 v[36:39], v[12:13], v[66:67], v[36:39] cbsz:1 abid:0\n\t"
        "s_sub_u32 s57, s57, 1\n\t"
        "v_mfma_f32_4x4x4_16b_bf16 v[32:35], v[12:13], v[68:69], v[32:35] cbsz:1 abid:1\n\t"
        "v_mfma_f32_4x4x4_16b_bf16 v[36:39], v[12:13], v[70:71], v[36:39] cbsz:1 abid:1\n\t"
        "s_cbranch_scc1 .L7_last\n"
        ".L7_dt1:\n\t"
        "v_perm_b32 v12, v43, v42, v8\n\t"
        "v_perm_b32 v13, v43, v42, v9\n\t"
        "s_waitcnt vmcnt(6)\n\t"
        "s_cmp_lt_u32 s57, 3\n\t"
        "s_cbranch_scc0 .L7_du1\n\t"
        "s_waitcnt vmcnt(2)\n\t"
        "s_cmp_eq_u32 s57, 0\n\t"
        "s_cbranch_scc0 .L7_du1\n\t"
        "s_waitcnt vmcnt(0)\n"
        ".L7_du1:\n\t"
        "v_mfma_f32_4x4x4_16b_bf16 v[32:35], v[12:13], v[72:73], v[32:35] cbsz:1 abid:0\n\t"
        "v_mfma_f32_4x4x4_16b_bf16 v[36:39], v[12:13], v[74:75], v[36:39] cbsz:1 abid:0\n\t"
        "s_sub_u32 s57, s57, 1\n\t"
        "v_mfma_f32_4x4x4_16b_bf16 v[32:35], v[12:13], v[76:77], v[32:35] cbsz:1 abid:1\n\t"
        "v_mfma_f32_4x4x4_16b_bf16 v[36:39], v[12:13], v[78:79], v[36:39] cbsz:1 abid:1\n\t"
        "s_cbranch_scc1 .L7_last\n"
        ".L7_dt2:\n\t"
        "v_perm_b32 v12, v45, v44, v8\n\t"
        "v_perm_b32 v13, v45, v44, v9\n\t"
        "s_waitcnt vmcnt(6)\n\t"
        "s_cmp_lt_u32 s57, 3\n\t"
        "s_cbranch_scc0 .L7_du2\n\t"
        "s_waitcnt vmcnt(2)\n\t"
        "s_cmp_eq_u32 s57, 0\n\t"
        "s_cbranch_scc0 .L7_du2\n\t"
        "s_waitcnt vmcnt(0)\n"
        ".L7_du2:\n\t"
        "v_mfma_f32_4x4x4_16b_bf16 v[32:35], v[12:13], v[80:81], v[32:35] cbsz:1 abid:0\n\t"
        "v_mfma_f32_4x4x4_16b_bf16 v[36:39], v[12:13], v[82:83], v[36:39] cbsz:1 abid:0\n\t"
        "s_sub_u32 s57, s57, 1\n\t"
        "v_mfma_f32_4x4x4_16b_bf16 v[32:35], v[12:13], v[84:85], v[32:35] cbsz:1 abid:1\n\t"
        "v_mfma_f32_4x4x4_16b_bf16 v[36:39], v[12:13], v[86:87], v[36:39] cbsz:1 abid:1\n\t"
        "s_cbranch_scc1 .L7_last\n"
        ".L7_dt3:\n\t"
        "v_perm_b32 v12, v47, v46, v8\n\t"
        "v_perm_b32 v13, v47, v46, v9\n\t"
        "s_waitcnt vmcnt(6)\n\t"
        "s_cmp_lt_u32 s57, 3\n\t"
        "s_cbranch_scc0 .L7_du3\n\t"
        "s_waitcnt vmcnt(2)\n\t"
        "s_cmp_eq_u32 s57, 0\n\t"
        "s_cbranch_scc0 .L7_du3\n\t"
        "s_waitcnt vmcnt(0)\n"
        ".L7_du3:\n\t"
        "v_mfma_f32_4x4x4_16b_bf16 v[32:35], v[12:13], v[88:89], v[32:35] cbsz:1 abid:0\n\t"
        "v_mfma_f32_4x4x4_16b_bf16 v[36:39], v[12:13], v[90:91], v[36:39] cbsz:1 abid:0\n\t"
        "s_sub_u32 s57, s57, 1\n\t"
        "v_mfma_f32_4x4x4_16b_bf16 v[32:35], v[12:13], v[92:93], v[32:35] cbsz:1 abid:1\n\t"
        "v_mfma_f32_4x4x4_16b_bf16 v[36:39], v[12:13], v[94:95], v[36:39] cbsz:1 abid:1\n\t"
        "s_cbranch_scc1 .L7_last\n"
        ".L7_dt4:\n\t"
        "v_perm_b32 v12, v49, v48, v8\n\t"
        "v_perm_b32 v13, v49, v48, v9\n\t"
        "s_waitcnt vmcnt(6)\n\t"
        "s_cmp_lt_u32 s57, 3\n\t"
        "s_cbranch_scc0 .L7_du4\n\t"
        "s_waitcnt vmcnt(2)\n\t"
        "s_cmp_eq_u32 s57, 0\n\t"
        "s_cbranch_scc0 .L7_du4\n\t"
        "s_waitcnt vmcnt(0)\n"
        ".L7_du4:\n\t"
        "v_mfma_f32_4x4x4_16b_bf16 v[32:35], v[12:13], v[96:97], v[32:35] cbsz:1 abid:0\n\t"
        "v_mfma_f32_4x4x4_16b_bf16 v[36:39], v[12:13], v[98:99], v[36:39] cbsz:1 abid:0\n\t"
        "s_sub_u32 s57, s57, 1\n\t"
        "v_mfma_f32_4x4x4_16b_bf16 v[32:35], v[12:13], v[100:101], v[32:35] cbsz:1 abid:1\n\t"
        "v_mfma_f32_4x4x4_16b_bf16 v[36:39], v[12:13], v[102:103], v[36:39] cbsz:1 abid:1\n\t"
        "s_cbranch_scc1 .L7_last\n"
        ".L7_dt5:\n\t"
        "v_perm_b32 v12, v51, v50, v8\n\t"
        "v_perm_b32 v13, v51, v50, v9\n\t"
        "s_waitcnt vmcnt(6)\n\t"
        "s_cmp_lt_u32 s57, 3\n\t"
        "s_cbranch_scc0 .L7_du5\n\t"
        "s_waitcnt vmcnt(2)\n\t"
        "s_cmp_eq_u32 s57, 0\n\t"
        "s_cbranch_scc0 .L7_du5\n\t"
        "s_waitcnt vmcnt(0)\n"
        ".L7_du5:\n\t"
        "v_mfma_f32_4x4x4_16b_bf16 v[32:35], v[12:13], v[104:105], v[32:35] cbsz:1 abid:0\n\t"
        "v_mfma_f32_4x4x4_16b_bf16 v[36:39], v[12:13], v[106:107], v[36:39] cbsz:1 abid:0\n\t"
        "s_sub_u32 s57, s57, 1\n\t"
        "v_mfma_f32_4x4x4_16b_bf16 v[32:35], v[12:13], v[108:109], v[32:35] cbsz:1 abid:1\n\t"
        "v_mfma_f32_4x4x4_16b_bf16 v[36:39], v[12:13], v[110:111], v[36:39] cbsz:1 abid:1\n\t"
        "s_cbranch_scc1 .L7_last\n"
        ".L7_dt6:\n\t"
        "v_perm_b32 v12, v53, v52, v8\n\t"
        "v_perm_b32 v13, v53, v52, v9\n\t"
        "s_waitcnt vmcnt(6)\n\t"
        "s_cmp_lt_u32 s57, 3\n\t"
        "s_cbranch_scc0 .L7_du6\n\t"
        "s_waitcnt vmcnt(2)\n\t"
        "s_cmp_eq_u32 s57, 0\n\t"
        "s_cbranch_scc0 .L7_du6\n\t"
        "s_waitcnt vmcnt(0)\n"
        ".L7_du6:\n\t"
        "v_mfma_f32_4x4x4_16b_bf16 v[32:35], v[12:13], v[112:113], v[32:35] cbsz:1 abid:0\n\t"
        "v_mfma_f32_4x4x4_16b_bf16 v[36:39], v[12:13], v[114:115], v[36:39] cbsz:1 abid:0\n\t"
        "s_sub_u32 s57, s57, 1\n\t"
        "v_mfma_f32_4x4x4_16b_bf16 v[32:35], v[12:13], v[116:117], v[32:35] cbsz:1 abid:1\n\t"
        "v_mfma_f32_4x4x4_16b_bf16 v[36:39], v[12:13], v[118:119], v[36:39] cbsz:1 abid:1\n\t"
        "s_cbranch_scc1 .L7_last\n"
        ".L7_dt7:\n\t"
        "v_perm_b32 v12, v55, v54, v8\n\t"
        "v_perm_b32 v13, v55, v54, v9\n\t"
        "s_waitcnt vmcnt(6)\n\t"
        "s_cmp_lt_u32 s57, 3\n\t"
        "s_cbranch_scc0 .L7_du7\n\t"
        "s_waitcnt vmcnt(2)\n\t"
        "s_cmp_eq_u32 s57, 0\n\t"
        "s_cbranch_scc0 .L7_du7\n\t"
        "s_waitcnt vmcnt(0)\n"
        ".L7_du7:\n\t"
        "v_mfma_f32_4x4x4_16b_bf16 v[32:35], v[12:13], v[120:121], v[32:35] cbsz:1 abid:0\n\t"
        "v_mfma_f32_4x4x4_16b_bf16 v[36:39], v[12:13], v[122:123], v[36:39] cbsz:1 abid:0\n\t"
        "s_sub_u32 s57, s57, 1\n\t"
        "v_mfma_f32_4x4x4_16b_bf16 v[32:35], v[12:13], v[124:125], v[32:35] cbsz:1 abid:1\n\t"
        "v_mfma_f32_4x4x4_16b_bf16 v[36:39], v[12:13], v[126:127], v[36:39] cbsz:1 abid:1\n\t"
        "s_cbranch_scc1 .L7_last\n\t"
        "s_branch .L7_dt0\n"
        ".L7_ie:\n\t"
        "v_dot4_u32_u8 v63, v25, s38, v7\n\t"
        "s_nop 2\n\t"
        "v_subrev_u32 v63, s60, v63\n\t"
        "s_mov_b32 s56, s25\n\t"
        "s_swappc_b64 s[50:51], s[54:55]\n\t"
        "v_dot4_u32_u8 v63, v26, s38, v7\n\t"
        "s_nop 2\n\t"
        "v_subrev_u32 v63, s60, v63\n\t"
        "s_mov_b32 s56, s26\n\t"
        "s_swappc_b64 s[50:51], s[54:55]\n\t"
        "v_dot4_u32_u8 v63, v27, s38, v7\n\t"
        "s_nop 2\n\t"
        "v_subrev_u32 v63, s60, v63\n\t"
        "s_mov_b32 s56, s27\n\t"
        "s_swappc_b64 s[50:51], s[54:55]\n\t"
        "v_dot4_u32_u8 v63, v28, s38, v7\n\t"
        "s_nop 2\n\t"
        "v_subrev_u32 v63, s60, v63\n\t"
        "s_add_u32 s56, s28, 2\n\t"
        "s_swappc_b64 s[50:51], s[54:55]\n\t"
        "s_mov_b64 vcc, -1\n\t"
        "s_setpc_b64 s[54:55]\n"
        ".L7_ce:\n\t"
        "v_and_b32 v4, 0xffff0000, v24\n\t"
        "s_nop 2\n\t"
        "v_pk_mul_f32 v[32:33], v[32:33], v[4:5] op_sel_hi:[1,0]\n\t"
        "v_pk_mul_f32 v[34:35], v[34:35], v[4:5] op_sel_hi:[1,0]\n\t"
        "v_pk_mul_f32 v[36:37], v[36:37], v[4:5] op_sel_hi:[1,0]\n\t"
        "v_pk_mul_f32 v[38:39], v[38:39], v[4:5] op_sel_hi:[1,0]\n\t"
        "v_cvt_pk_bf16_f32 v12, v32, v33\n\t"
        "v_cvt_pk_bf16_f32 v13, v34, v35\n\t"
        "v_cvt_pk_bf16_f32 v14, v36, v37\n\t"
        "v_cvt_pk_bf16_f32 v15, v38, v39\n"
        ".L7_w115:\n\t"
        "ds_read_b32 v4, v2 offset:12\n\t"
        "s_waitcnt lgkmcnt(0)\n\t"
        "v_cmp_gt_u16 vcc, 4, v4\n\t"
        "s_cbranch_vccz .L7_d116\n\t"
        "s_sleep 1\n\t"
        "s_sub_i32 s83, s83, 1\n\t"
        "s_cmp_gt_i32 s83, 0\n\t"
        "s_cbranch_scc1 .L7_w115\n"
        ".L7_d116:\n\t"
        "s_mov_b64 vcc, 0\n\t"
        "ds_write_b128 v56, v[12:15] offset:23808\n\t"
        "s_mov_b64 exec, 1\n\t"
        "ds_add_u32 v2, v3 offset:52\n\t"
        "s_mov_b64 exec, -1\n\t"
        "v_mov_b64 v[32:33], 0\n\t"
        "v_mov_b64 v[34:35], 0\n\t"
        "v_mov_b64 v[36:37], 0\n\t"
        "v_mov_b64 v[38:39], 0\n\t"
        "s_mov_b32 s57, s25\n\t"
        "s_swappc_b64 s[52:53], s[54:55]\n\t"
        "v_and_b32 v4, 0xffff0000, v25\n\t"
        "s_nop 2\n\t"
        "v_pk_mul_f32 v[32:33], v[32:33], v[4:5] op_sel_hi:[1,0]\n\t"
        "v_pk_mul_f32 v[34:35], v[34:35], v[4:5] op_sel_hi:[1,0]\n\t"
        "v_pk_mul_f32 v[36:37], v[36:37], v[4:5] op_sel_hi:[1,0]\n\t"
        "v_pk_mul_f32 v[38:39], v[38:39], v[4:5] op_sel_hi:[1,0]\n\t"
        "v_cvt_pk_bf16_f32 v12, v32, v33\n\t"
        "v_cvt_pk_bf16_f32 v13, v34, v35\n\t"
        "v_cvt_pk_bf16_f32 v14, v36, v37\n\t"
        "v_cvt_pk_bf16_f32 v15, v38, v39\n\t"
        "ds_write_b128 v57, v[12:15] offset:23808\n\t"
        "s_mov_b64 exec, 1\n\t"
        "ds_add_u32 v61, v3 offset:52\n\t"
        "s_mov_b64 exec, -1\n\t"
        "v_mov_b64 v[32:33], 0\n\t"
        "v_mov_b64 v[34:35], 0\n\t"
        "v_mov_b64 v[36:37], 0\n\t"
        "v_mov_b64 v[38:39], 0\n\t"
        "s_mov_b32 s57, s26\n\t"
        "s_swappc_b64 s[52:53], s[54:55]\n\t"
        "v_and_b32 v4, 0xffff0000, v26\n\t"
        "s_nop 2\n\t"
        "v_pk_mul_f32 v[32:33], v[32:33], v[4:5] op_sel_hi:[1,0]\n\t"
        "v_pk_mul_f32 v[34:35], v[34:35], v[4:5] op_sel_hi:[1,0]\n\t"
        "v_pk_mul_f32 v[36:37], v[36:37], v[4:5] op_sel_hi:[1,0]\n\t"
        "v_pk_mul_f32 v[38:39], v[38:39], v[4:5] op_sel_hi:[1,0]\n\t"
        "v_cvt_pk_bf16_f32 v12, v32, v33\n\t"
        "v_cvt_pk_bf16_f32 v13, v34, v35\n\t"
        "v_cvt_pk_bf16_f32 v14, v36, v37\n\t"
        "v_cvt_pk_bf16_f32 v15, v38, v39\n\t"
        "ds_write_b128 v58, v[12:15] offset:23808\n\t"
        "s_mov_b64 exec, 1\n\t"
        "ds_add_u32 v61, v3 offset:56\n\t"
        "s_mov_b64 exec, -1\n\t"
        "v_mov_b64 v[32:33], 0\n\t"
        "v_mov_b64 v[34:35], 0\n\t"
        "v_mov_b64 v[36:37], 0\n\t"
        "v_mov_b64 v[38:39], 0\n\t"
        "s_mov_b32 s57, s27\n\t"
        "s_swappc_b64 s[52:53], s[54:55]\n\t"
        "v_and_b32 v4, 0xffff0000, v27\n\t"
        "s_nop 2\n\t"
        "v_pk_mul_f32 v[32:33], v[32:33], v[4:5] op_sel_hi:[1,0]\n\t"
        "v_pk_mul_f32 v[34:35], v[34:35], v[4:5] op_sel_hi:[1,0]\n\t"
        "v_pk_mul_f32 v[36:37], v[36:37], v[4:5] op_sel_hi:[1,0]\n\t"
        "v_pk_mul_f32 v[38:39], v[38:39], v[4:5] op_sel_hi:[1,0]\n\t"
        "v_cvt_pk_bf16_f32 v12, v32, v33\n\t"
        "v_cvt_pk_bf16_f32 v13, v34, v35\n\t"
        "v_cvt_pk_bf16_f32 v14, v36, v37\n\t"
        "v_cvt_pk_bf16_f32 v15, v38, v39\n\t"
        "ds_write_b128 v59, v[12:15] offset:21760\n\t"
        "s_mov_b64 exec, 1\n\t"
        "ds_add_u32 v2, v3 offset:64\n\t"
        "s_mov_b64 exec, -1\n\t"
        "v_mov_b64 v[32:33], 0\n\t"
        "v_mov_b64 v[34:35], 0\n\t"
        "v_mov_b64 v[36:37], 0\n\t"
        "v_mov_b64 v[38:39], 0\n\t"
        "s_mov_b32 s57, s28\n\t"
        "s_swappc_b64 s[52:53], s[54:55]\n"
        ".L7_last:\n\t"
        "v_and_b32 v4, 0xffff0000, v28\n\t"
        "s_nop 2\n\t"
        "v_pk_mul_f32 v[32:33], v[32:33], v[4:5] op_sel_hi:[1,0]\n\t"
        "v_pk_mul_f32 v[34:35], v[34:35], v[4:5] op_sel_hi:[1,0]\n\t"
        "v_pk_mul_f32 v[36:37], v[36:37], v[4:5] op_sel_hi:[1,0]\n\t"
        "v_pk_mul_f32 v[38:39], v[38:39], v[4:5] op_sel_hi:[1,0]\n\t"
        "v_cvt_pk_bf16_f32 v12, v32, v33\n\t"
        "v_cvt_pk_bf16_f32 v13, v34, v35\n\t"
        "v_cvt_pk_bf16_f32 v14, v36, v37\n\t"
        "v_cvt_pk_bf16_f32 v15, v38, v39\n\t"
        "ds_write_b128 v60, v[12:15] offset:21760\n\t"
        "s_mov_b64 exec, 1\n\t"
        "ds_add_u32 v2, v3 offset:68\n\t"
        "s_mov_b64 exec, -1\n\t"
        "s_endpgm\n"
        ".L7_x:\n\t"
        "s_mov_b32 s36, 1\n\t"
        "s_waitcnt lgkmcnt(0)\n\t"
        "v_dot4_u32_u8 v83, v67, s39, v7\n\t"
        "v_lshlrev_b32_sdwa v115, 1, v67 dst_sel:DWORD dst_unused:UNUSED_PAD src0_sel:DWORD src1_sel:WORD_0\n\t"
        "ds_read_u16 v115, v115 offset:46336\n\t"
        "v_cmp_eq_u16 vcc, -1, v67\n\t"
        "s_cmp_lg_u64 vcc, 0\n\t"
        "s_cselect_b32 s78, 0x10000, 0\n\t"
        "v_or_b32 v30, s78, v30\n\t"
        "ds_add_rtn_u32 v99, v83, v29 offset:192\n\t"
        "v_dot4_u32_u8 v87, v71, s39, v7\n\t"
        "v_lshlrev_b32_sdwa v119, 1, v71 dst_sel:DWORD dst_unused:UNUSED_PAD src0_sel:DWORD src1_sel:WORD_0\n\t"
        "ds_read_u16 v119, v119 offset:46336\n\t"
        "v_cmp_eq_u16 vcc, -1, v71\n\t"
        "s_cmp_lg_u64 vcc, 0\n\t"
        "s_cselect_b32 s78, 0x10000, 0\n\t"
        "v_or_b32 v30, s78, v30\n\t"
        "ds_add_rtn_u32 v103, v87, v29 offset:192\n\t"
        "v_dot4_u32_u8 v91, v75, s39, v7\n\t"
        "v_lshlrev_b32_sdwa v123, 1, v75 dst_sel:DWORD dst_unused:UNUSED_PAD src0_sel:DWORD src1_sel:WORD_0\n\t"
        "ds_read_u16 v123, v123 offset:46336\n\t"
        "v_cmp_eq_u16 vcc, -1, v75\n\t"
        "s_cmp_lg_u64 vcc, 0\n\t"
        "s_cselect_b32 s78, 0x10000, 0\n\t"
        "v_or_b32 v30, s78, v30\n\t"
        "ds_add_rtn_u32 v107, v91, v29 offset:192\n\t"
        "v_dot4_u32_u8 v95, v79, s39, v7\n\t"
        "v_lshlrev_b32_sdwa v127, 1, v79 dst_sel:DWORD dst_unused:UNUSED_PAD src0_sel:DWORD src1_sel:WORD_0\n\t"
        "ds_read_u16 v127, v127 offset:46336\n\t"
        "v_cmp_eq_u16 vcc, -1, v79\n\t"
        "s_cmp_lg_u64 vcc, 0\n\t"
        "s_cselect_b32 s78, 0x10000, 0\n\t"
        "v_or_b32 v30, s78, v30\n\t"
        "ds_add_rtn_u32 v111, v95, v29 offset:192\n\t"
        "s_branch .L7_y\n"
        ".L7_z:\n\t"
        "s_waitcnt lgkmcnt(0)\n\t"
        "ds_read_b32 v32, v83 offset:192\n\t"
        "ds_read_b32 v33, v87 offset:192\n\t"
        "ds_read_b32 v34, v91 offset:192\n\t"
        "ds_read_b32 v35, v95 offset:192\n\t"
        "v_bfe_u32 v99, v99, s33, 8\n\t"
        "v_perm_b32 v115, v115, v67, s49\n\t"
        "v_bfe_u32 v103, v103, s33, 8\n\t"
        "v_perm_b32 v119, v119, v71, s49\n\t"
        "v_bfe_u32 v107, v107, s33, 8\n\t"
        "v_perm_b32 v123, v123, v75, s49\n\t"
        "v_bfe_u32 v111, v111, s33, 8\n\t"
        "v_perm_b32 v127, v127, v79, s49\n\t"
        "s_waitcnt lgkmcnt(0)\n\t"
        "v_dot4_u32_u8 v99, v32, s32, v99\n\t"
        "v_dot4_u32_u8 v103, v33, s32, v103\n\t"
        "v_dot4_u32_u8 v107, v34, s32, v107\n\t"
        "v_dot4_u32_u8 v111, v35, s32, v111\n\t"
        "v_min_u32 v99, 47, v99\n\t"
        "v_min_u32 v103, 47, v103\n\t"
        "v_min_u32 v107, 47, v107\n\t"
        "v_min_u32 v111, 47, v111\n\t"
        "v_lshl_add_u32 v99, v99, 2, v83\n\t"
        "ds_write_b32 v99, v115\n\t"
        "v_lshl_add_u32 v103, v103, 2, v87\n\t"
        "ds_write_b32 v103, v119\n\t"
        "v_lshl_add_u32 v107, v107, 2, v91\n\t"
        "ds_write_b32 v107, v123\n\t"
        "v_lshl_add_u32 v111, v111, 2, v95\n\t"
        "ds_write_b32 v111, v127\n\t"
        "s_branch .L7_zr\n"
        ".L7_exit4:\n\t"
        "s_add_u32 s69, s69, 4\n"
        ".L7_exit:\n\t"
        "s_waitcnt vmcnt(0) lgkmcnt(0)\n\t"
        "s_setprio 0\n\t"
        : "+{v0}"(tid), "=&{s69}"(wv)
        : "s"(__builtin_amdgcn_kernarg_segment_ptr()), "s"(blockIdx.x)
        : "scc", "vcc", "memory", "s16", "s17", "s18", "s19", "s20", "s21", "s22", "s23", "s24", "s25", "s26", "s27", "s28", "s29", "s30", "s31", "s32", "s33", "s34", "s35",
          "s36", "s37", "s38", "s39", "s40", "s41", "s42", "s43", "s44", "s45", "s46", "s47", "s48", "s49", "s50", "s51", "s52", "s53", "s54", "s55",
          "s56", "s57", "s58", "s59", "s60", "s61", "s62", "s63", "s64", "s65", "s66", "s67", "s68", "s70", "s71", "s72", "s73", "s74", "s75", "s76",
          "s77", "s78", "s79", "s80", "s81", "s82", "s83", "s84", "s85", "s86", "s87", "s88", "s89", "s90", "s91", "s92", "s93", "s94", "s95", "s96",
          "s97", "s98", "s99", "s100", "s101", "v1", "v2", "v3", "v4", "v5", "v6", "v7", "v8", "v9", "v10", "v11", "v12", "v13", "v14", "v15",
          "v16", "v17", "v18", "v19", "v20", "v21", "v22", "v23", "v24", "v25", "v26", "v27", "v28", "v29", "v30", "v31", "v32", "v33", "v34", "v35",
          "v36", "v37", "v38", "v39", "v40", "v41", "v42", "v43", "v44", "v45", "v46", "v47", "v48", "v49", "v50", "v51", "v52", "v53", "v54", "v55",
          "v56", "v57", "v58", "v59", "v60", "v61", "v62", "v63", "v64", "v65", "v66", "v67", "v68", "v69", "v70", "v71", "v72", "v73", "v74", "v75",
          "v76", "v77", "v78", "v79", "v80", "v81", "v82", "v83", "v84", "v85", "v86", "v87", "v88", "v89", "v90", "v91", "v92", "v93", "v94", "v95",
          "v96", "v97", "v98", "v99", "v100", "v101", "v102", "v103", "v104", "v105", "v106", "v107", "v108", "v109", "v110", "v111", "v112", "v113", "v114", "v115",
          "v116", "v117", "v118", "v119", "v120", "v121", "v122", "v123", "v124", "v125", "v126", "v127");
    const __attribute__((address_space(4))) Args* q = (const __attribute__((address_space(4))) Args*)__builtin_amdgcn_kernarg_segment_ptr();
    asm volatile("" : "+s"(q));
    const u32 wave = __builtin_amdgcn_readfirstlane(wv);
    u32 lane;
    asm volatile("v_mbcnt_lo_u32_b32 %0, -1, 0\n\tv_mbcnt_hi_u32_b32 %0, -1, %0" : "=v"(lane));
    const u32 half = blockIdx.x & 1u, TN = q->TN, r0 = (blockIdx.x >> 1) * TN;
    const u16* x = q->xall;
    _Pragma("nounroll") for (u32 n = 0; n < (1u << 26) && *(volatile __attribute__((address_space(3))) u32*)S_QK < 4u; ++n) __builtin_amdgcn_s_sleep(2);
    if (wave >= 4u) {
        const int E = q->E, *dst = (const int*)q->dst.p, *src = (const int*)q->src.p;
        const u16* dis = q->disall;
        const u32 w = wave - 4u;
        _Pragma("nounroll") for (u32 k = 0; k < 5u; ++k) {
            const u32 grp = (u32)(((w == 0u ? 0x1713050109ull : w == 1u ? 0x1612040008ull : w == 2u ? 0x14100e0602ull : 0x15110f0703ull) >> (8u * k)) & 255u);
            const u32 row = grp * 4u + (lane >> 4), iv = *(const u32*)(smem + L_INV + row * 4u);
            const int me = (iv & 0x8000u) ? -1 : q->nbase + (int)(r0 + (iv & 255u) + TN - 255u);
            const int m0 = __builtin_amdgcn_readlane(me, 0), m1 = __builtin_amdgcn_readlane(me, 16), m2 = __builtin_amdgcn_readlane(me, 32), m3 = __builtin_amdgcn_readlane(me, 48);
            float s[8] = {0.f, 0.f, 0.f, 0.f, 0.f, 0.f, 0.f, 0.f};
            for (int e0 = 0; e0 < E; e0 += 64) {
                const int e = min(e0 + (int)lane, E - 1), d = dst[e], si = src[e];
                unsigned long long m = __ballot(e0 + (int)lane < E && (d == m0 || d == m1 || d == m2 || d == m3));
                while (m) {
                    const int l = __builtin_ctzll(m);
                    m &= m - 1;
                    const int sl = __builtin_amdgcn_readlane(si, l);
                    if (__builtin_amdgcn_readlane(d, l) == me) {
                        const u32x4 r = *(const u32x4*)(x + (size_t)sl * 256 + half * 128u + (lane & 15u) * 8u);
                        const float wt = bf2f(dis[sl]);
#pragma unroll
                        for (int j = 0; j < 4; ++j) { s[2 * j] += wt * lo(r[j]); s[2 * j + 1] += wt * hi(r[j]); }
                    }
                }
            }
            const float sc = bf2f(dis[max(me, 0)]);
            *(u32x4*)(smem + L_AL + (row < 48u ? row : row - 8u) * 256u + (((lane ^ row) & 15u) << 4)) =
                (u32x4){pack2(s[0] * sc, s[1] * sc), pack2(s[2] * sc, s[3] * sc), pack2(s[4] * sc, s[5] * sc), pack2(s[6] * sc, s[7] * sc)};
            lds_signal(S_T0 + 4u * (grp < 12u ? 1u + ((grp >> 2) & 1u) : grp >> 2), 1u);
        }
        return;
    }
    const int Ee = q->Ee, *edst = (const int*)q->edst.p, *esrc = (const int*)q->esrc.p;
    const u16 *wq = (const u16*)q->wq.p, *wk = (const u16*)q->wk.p;
    float* qs = (float*)(smem + L_QK + wave * 1024u);
    _Pragma("nounroll") for (u32 n = wave; n < TN && r0 + n < q->N; n += 4u) {
        const int me = q->nbase + (int)(r0 + n);
        float c0 = 0.f, c1 = 0.f, c2 = 0.f, c3 = 0.f;
        _Pragma("nounroll") for (int i = 0; i < 256; ++i) {
            const float xv = bf2f(x[(size_t)me * 256 + i]);
            const u32x2 r = *(const u32x2*)(wq + i * 256 + lane * 4u);
            c0 += xv * lo(r[0]); c1 += xv * hi(r[0]); c2 += xv * lo(r[1]); c3 += xv * hi(r[1]);
        }
        qs[lane * 4u] = c0; qs[lane * 4u + 1u] = c1; qs[lane * 4u + 2u] = c2; qs[lane * 4u + 3u] = c3;
        float k[4] = {0.f, 0.f, 0.f, 0.f};
        _Pragma("nounroll") for (int c = 0; c < 256; c += 2) {
            const float qa = qs[c], qb = qs[c + 1];
#pragma unroll
            for (int j = 0; j < 4; ++j) { const u32 r = *(const u32*)(wk + (lane * 4u + j) * 256u + c); k[j] += qa * lo(r) + qb * hi(r); }
        }
        float s0 = 0.f, s1 = 0.f, s2 = 0.f, s3 = 0.f, dn = 0.f;
        for (int e0 = 0; e0 < Ee; e0 += 64) {
            const int e = min(e0 + (int)lane, Ee - 1), si = esrc[e];
            unsigned long long m = __ballot(e0 + (int)lane < Ee && edst[e] == me);
            while (m) {
                const int l = __builtin_ctzll(m);
                m &= m - 1;
                const u32x2 r = *(const u32x2*)(x + (size_t)__builtin_amdgcn_readlane(si, l) * 256 + lane * 4u);
                float d = k[0] * lo(r[0]) + k[1] * hi(r[0]) + k[2] * lo(r[1]) + k[3] * hi(r[1]);
#pragma unroll
                for (int o = 1; o < 64; o <<= 1)
                    d += __uint_as_float((u32)__builtin_amdgcn_ds_bpermute((int)((lane ^ (u32)o) * 4u), (int)__float_as_uint(d)));
                const float wt = __builtin_amdgcn_exp2f(d * 0.09016844f);
                s0 += wt * lo(r[0]); s1 += wt * hi(r[0]); s2 += wt * lo(r[1]); s3 += wt * hi(r[1]);
                dn += wt;
            }
        }
        const float sc = __builtin_amdgcn_rcpf(dn + 1e-9f);
        if ((lane >> 5) == half)
            *(u32x2*)(smem + L_AA + ((u32)*(const u16*)(smem + L_MAP + n * 2u) ^ ((lane & 31u) * 8u))) =
                (u32x2){pack2(s0 * sc, s1 * sc), pack2(s2 * sc, s3 * sc)};
    }
    lds_signal(S_Z, 17u);
}

extern "C" void kernel_launch(void* const* d_in, const int* in_sizes, int n_in,
                              void* d_out, int out_size, void* d_ws, size_t ws_size,
                              hipStream_t stream) {
    const u32 N = in_sizes[5], E = in_sizes[1], Ee = in_sizes[3];
    const u16* x = (const u16*)d_in[0];
    const u16* dis = (const u16*)d_in[5];
    const u32 FL = 0x00020000u;
    const bool generic = N > NMAX || E > EMAX || (E & 3u) || (N & 1u);
    const bool irr = N > NMAX || Ee != 4u * N;
    (void)hipFuncSetAttribute((const void*)agg_kernel, hipFuncAttributeMaxDynamicSharedMemorySize, LDS_BYTES);
    for (u32 nbase = 0; nbase < N; nbase += NMAX) {
        const u32 n = N - nbase < NMAX ? N - nbase : NMAX;
        Args a;
        a.x = Desc{x + (size_t)nbase * 256, n * 512u, FL};
        a.ws = Desc{d_ws, WS_END, FL};
        a.wl = Desc{d_in[6], 131072u, FL}; a.wq = Desc{d_in[7], 131072u, FL}; a.wk = Desc{d_in[8], 131072u, FL}; a.wv = Desc{d_in[9], 131072u, FL};
        a.src = Desc{d_in[1], generic ? 0u : E * 4u, FL}; a.dst = Desc{d_in[2], generic ? 0u : E * 4u, FL};
        a.esrc = Desc{d_in[3], irr ? 0u : Ee * 4u, FL}; a.edst = Desc{d_in[4], irr ? 0u : Ee * 4u, FL};
        a.dis = Desc{dis + nbase, n * 2u, FL};
        a.out = Desc{(u16*)d_out + (size_t)nbase * 256, n * 512u, FL};
        a.N = n; a.TN = n > 256u ? (n + 127u) / 128u : 2u; a.M = ((1u << 24) + a.TN - 1u) / a.TN;
        a.irr = (irr ? 1u : 0u) | (generic ? 0x10000u : 0u);
        a.xall = x; a.disall = dis;
        a.E = (int)E; a.Ee = (int)Ee; a.nbase = (int)nbase; a.pad = 0;
        agg_kernel<<<256, 1024, LDS_BYTES, stream>>>(a);
    }
}
```

```cpp
#include <hip/hip_runtime.h>
#include <hip/hip_bf16.h>
#include <stdint.h>

typedef unsigned short u16;
typedef unsigned int u32;
typedef __attribute__((ext_vector_type(4))) u32 u32x4;
typedef __attribute__((ext_vector_type(2))) u32 u32x2;
struct Desc { const void* p; u32 bytes, flags; };
struct Args {
    Desc x, ws, wl, wv, wq, wk, src, dst, esrc, edst, dis, out;
    u32 N, TN, M, irr;
    const u16* xall;
    const u16* disall;
    int E, Ee, nbase, pad;
};
constexpr u32 NMAX = 10240, EMAX = 163840;
constexpr u32 WS_END = 9437184;
constexpr int LDS_BYTES = 163840;
constexpr u32 L_INV = 512, L_MAP = 1792, L_QK = 2048, L_AL = 23808, L_AA = 46336, S_QK = 16, S_Z = 40, S_T0 = 48;

__device__ __forceinline__ float bf2f(u32 h) { return __uint_as_float(h << 16); }
__device__ __forceinline__ float lo(u32 v) { return __uint_as_float(v << 16); }
__device__ __forceinline__ float hi(u32 v) { return __uint_as_float(v & 0xFFFF0000u); }
__device__ __forceinline__ u32 pack2(float a, float b) {
    return (u32)__builtin_bit_cast(u16, (__bf16)a) | ((u32)__builtin_bit_cast(u16, (__bf16)b) << 16);
}
__device__ __forceinline__ void lds_signal(const u32 addr, const u32 v) {
    asm volatile("s_mov_b64 exec, 1\n\tds_add_u32 %0, %1\n\ts_mov_b64 exec, -1" :: "v"(addr), "v"(v) : "memory");
}

extern "C" __global__ void __launch_bounds__(1024, 4)
agg_kernel(const Args a) {
    extern __shared__ __attribute__((aligned(16))) char smem[];
    u32 tid = threadIdx.x, wv;
    asm volatile(
        "s_memrealtime s[74:75]\n\t"
        "s_load_dwordx8 s[16:23], %2, 0\n\t"
        "s_load_dwordx4 s[64:67], %2, 192\n\t"
        "v_readfirstlane_b32 s69, v0\n\t"
        "v_and_b32 v1, 63, v0\n\t"
        "v_mov_b32 v2, 0\n\t"
        "v_mov_b32 v3, 1\n\t"
        "s_lshr_b32 s76, %3, 3\n\t"
        "s_and_b32 s70, %3, 1\n\t"
        "s_lshr_b32 s71, %3, 1\n\t"
        "s_mov_b32 s83, 0x2000000\n\t"
        "s_lshr_b32 s69, s69, 6\n\t"
        "s_cbranch_scc0 .L7_U\n\t"
        "s_cmp_lt_u32 s69, 8\n\t"
        "s_cbranch_scc1 .L7_G0\n\t"
        "s_load_dwordx4 s[48:51], %2, 128\n\t"
        "s_sub_u32 s69, s69, 8\n\t"
        "v_lshlrev_b32 v4, 2, v1\n\t"
        "s_lshl_b32 s77, s70, 2\n\t"
        "s_xor_b32 s77, s77, s69\n\t"
        "s_xor_b32 s77, s77, 4\n\t"
        "s_mul_i32 s80, s77, 10\n\t"
        "s_waitcnt lgkmcnt(0)\n\t"
        "s_mul_i32 s72, s71, s65\n\t"
        "s_add_u32 s78, s72, s80\n\t"
        "s_lshl_b32 s78, s78, 4\n\t"
        "s_mov_b32 exec_hi, 0xff\n\t"
        "buffer_load_dword v5, v4, s[48:51], s78 offen\n\t"
        "s_mov_b64 exec, -1\n\t"
        "s_barrier\n\t"
        "s_branch .L7_O2\n"
        ".L7_U:\n\t"
        "s_load_dwordx16 s[32:47], %2, 64\n\t"
        "s_load_dwordx16 s[48:63], %2, 128\n\t"
        "s_setprio 3\n\t"
        "s_getpc_b64 s[24:25]\n\t"
        "s_add_u32 s24, s24, agg_kernel@rel32@lo+4\n\t"
        "s_addc_u32 s25, s25, agg_kernel@rel32@hi+12\n\t"
        "s_and_b32 s25, s25, 0xffff\n\t"
        "s_mov_b32 s26, .L7_x-agg_kernel\n\t"
        "s_mov_b32 s27, 0x20000\n\t"
        "v_lshlrev_b32 v6, 12, v1\n\t"
        "s_lshl_b32 s78, s76, 7\n\t"
        "s_mov_b32 m0, 134400\n\t"
        "s_mov_b64 exec, 63\n\t"
        "buffer_load_dword v6, s[24:27], s78 offen lds\n\t"
        "s_mov_b64 exec, -1\n\t"
        "s_cmp_eq_u32 s70, 0\n\t"
        "s_cbranch_scc0 .L7_V\n\t"
        "v_lshrrev_b32 v4, 3, v1\n\t"
        "v_and_b32 v5, 7, v1\n\t"
        "v_lshlrev_b32 v4, 14, v4\n\t"
        "v_lshl_add_u32 v4, v5, 4, v4\n\t"
        "s_lshl_b32 s78, s71, 7\n\t"
        "v_add_u32 v4, s78, v4\n\t"
        "v_add_u32 v5, 0x20000, v4\n\t"
        "v_add_u32 v6, 0x40000, v4\n\t"
        "v_add_u32 v7, 0x60000, v4\n\t"
        "v_add_u32 v8, 0x80000, v4\n\t"
        "s_waitcnt lgkmcnt(0)\n\t"
        "s_mul_i32 s72, s71, s65\n\t"
        "buffer_load_dwordx4 v[16:19], v4, s[44:47], 0 offen\n\t"
        "buffer_load_dwordx4 v[20:23], v5, s[44:47], 0 offen\n\t"
        "buffer_load_dwordx4 v[24:27], v6, s[44:47], 0 offen\n\t"
        "buffer_load_dwordx4 v[28:31], v7, s[44:47], 0 offen\n\t"
        "buffer_load_dwordx4 v[32:35], v8, s[44:47], 0 offen\n\t"
        "s_mul_i32 s80, s71, 0x50\n\t"
        "s_lshl_b32 s78, s80, 4\n\t"
        "v_lshlrev_b32 v12, 4, v1\n\t"
        "buffer_load_dwordx4 v[116:119], v12, s[52:55], s78 offen\n\t"
        "s_mov_b64 exec, 0xffff\n\t"
        "buffer_load_dwordx4 v[120:123], v12, s[52:55], s78 offen offset:1024\n\t"
        "s_mov_b64 exec, -1\n\t"
        "buffer_load_dwordx4 v[36:39], v4, s[40:43], 0 offen\n\t"
        "buffer_load_dwordx4 v[40:43], v5, s[40:43], 0 offen\n\t"
        "buffer_load_dwordx4 v[44:47], v6, s[40:43], 0 offen\n\t"
        "buffer_load_dwordx4 v[48:51], v7, s[40:43], 0 offen\n\t"
        "buffer_load_dwordx4 v[52:55], v8, s[40:43], 0 offen\n\t"
        "s_barrier\n\t"
        "s_branch .L7_U2\n"
        ".L7_G0:\n\t"
        "s_cmp_lt_u32 s69, 4\n\t"
        "s_cbranch_scc1 .L7_L\n\t"
        "s_sub_u32 s69, s69, 4\n\t"
        "v_lshlrev_b32 v4, 4, v1\n\t"
        "v_and_b32 v5, 0x200, v4\n\t"
        "v_mad_u32_u24 v4, v5, 7, v4\n\t"
        "s_mul_i32 s80, s70, 5\n\t"
        "s_mul_i32 s81, s69, 0x420\n\t"
        "s_add_u32 s81, s81, 2048\n\t"
        "s_mul_i32 s78, s80, 0x1080\n\t"
        "s_add_u32 m0, s78, s81\n\t"
        "s_lshr_b32 s78, s80, 1\n\t"
        "s_lshl1_add_u32 s78, s78, s80\n\t"
        "s_lshl_b32 s78, s78, 11\n\t"
        "s_waitcnt lgkmcnt(0)\n\t"
        "s_mul_i32 s72, s71, s65\n\t"
        "s_add_u32 s79, s72, s69\n\t"
        "s_lshl_b32 s79, s79, 9\n\t"
        "v_add_u32 v4, s79, v4\n\t"
        "s_sleep 3\n\t"
        "s_cmp_eq_u32 s70, 0\n\t"
        "s_cbranch_scc1 .L7_gs\n\t"
        "s_sleep 4\n"
        ".L7_gs:\n\t"
        "buffer_load_dwordx4 v4, s[16:19], s78 offen lds\n\t"
        "s_add_u32 s80, s80, 1\n\t"
        "s_load_dwordx4 s[56:59], %2, 160\n\t"
        "s_mul_i32 s78, s80, 0x1080\n\t"
        "s_add_u32 m0, s78, s81\n\t"
        "s_lshr_b32 s78, s80, 1\n\t"
        "s_lshl1_add_u32 s78, s78, s80\n\t"
        "s_lshl_b32 s78, s78, 11\n\t"
        "buffer_load_dwordx4 v4, s[16:19], s78 offen lds\n\t"
        "s_add_u32 s80, s80, 1\n\t"
        "s_mul_i32 s78, s80, 0x1080\n\t"
        "s_add_u32 m0, s78, s81\n\t"
        "s_lshr_b32 s78, s80, 1\n\t"
        "s_lshl1_add_u32 s78, s78, s80\n\t"
        "s_lshl_b32 s78, s78, 11\n\t"
        "buffer_load_dwordx4 v4, s[16:19], s78 offen lds\n\t"
        "s_add_u32 s80, s80, 1\n\t"
        "s_mul_i32 s78, s80, 0x1080\n\t"
        "s_add_u32 m0, s78, s81\n\t"
        "s_lshr_b32 s78, s80, 1\n\t"
        "s_lshl1_add_u32 s78, s78, s80\n\t"
        "s_lshl_b32 s78, s78, 11\n\t"
        "buffer_load_dwordx4 v4, s[16:19], s78 offen lds\n\t"
        "s_add_u32 s80, s80, 1\n\t"
        "s_mul_i32 s78, s80, 0x1080\n\t"
        "s_add_u32 m0, s78, s81\n\t"
        "s_lshr_b32 s78, s80, 1\n\t"
        "s_lshl1_add_u32 s78, s78, s80\n\t"
        "s_lshl_b32 s78, s78, 11\n\t"
        "buffer_load_dwordx4 v4, s[16:19], s78 offen lds\n\t"
        "s_add_u32 s80, s80, 1\n\t"
        "s_barrier\n\t"
        "s_branch .L7_G2\n"
        ".L7_L:\n\t"
        "s_cmp_eq_u32 s69, 2\n\t"
        "s_cbranch_scc0 .L7_L1\n\t"
        "s_waitcnt lgkmcnt(0)\n\t"
        "s_mul_i32 s72, s71, s65\n\t"
        "s_buffer_load_dword s68, s[20:23], 0\n\t"
        "s_barrier\n\t"
        "s_branch .L7_P2\n"
        ".L7_L1:\n\t"
        "s_cmp_eq_u32 s69, 1\n\t"
        "s_cbranch_scc0 .L7_T\n\t"
        "s_load_dwordx4 s[56:59], %2, 160\n\t"
        "v_lshlrev_b32 v4, 3, v1\n\t"
        "v_mov_b64 v[8:9], 0\n\t"
        "ds_write_b64 v4, v[8:9]\n\t"
        "s_waitcnt lgkmcnt(0)\n\t"
        "s_mul_i32 s72, s71, s65\n\t"
        "v_add_lshl_u32 v4, v1, s72, 1\n\t"
        "buffer_load_ushort v16, v4, s[56:59], 0 offen\n\t"
        "buffer_load_ushort v17, v4, s[56:59], 0 offen offset:128\n\t"
        "s_barrier\n\t"
        "s_branch .L7_R2\n"
        ".L7_T:\n\t"
        "s_load_dwordx8 s[24:31], %2, 32\n\t"
        "v_lshlrev_b32 v6, 7, v1\n\t"
        "s_mov_b32 m0, 134400\n\t"
        "s_lshl_b32 s78, s70, 16\n\t"
        "s_lshl_b32 s79, s76, 11\n\t"
        "s_add_u32 s80, s78, s79\n\t"
        "s_mul_i32 s81, s76, 0x300\n\t"
        "s_lshl_b32 s82, %3, 7\n\t"
        "s_add_u32 s82, s82, 4096\n\t"
        "v_lshlrev_b32 v8, 2, v1\n\t"
        "s_and_b32 s78, s71, 63\n\t"
        "s_lshl_b32 s78, s78, 7\n\t"
        "s_and_b32 s79, s71, 64\n\t"
        "s_lshl_b32 s79, s79, 8\n\t"
        "s_add_u32 s42, s78, s79\n\t"
        "s_and_b32 s44, s76, 1\n\t"
        "s_lshl_b32 s78, s44, 13\n\t"
        "s_add_u32 s42, s42, s78\n\t"
        "s_lshl_b32 s78, s70, 15\n\t"
        "s_add_u32 s42, s42, s78\n\t"
        "s_add_u32 s42, s42, 65536\n\t"
        "s_xor_b32 s43, s42, 0x2000\n\t"
        "s_waitcnt lgkmcnt(0)\n\t"
        "s_mul_i32 s72, s71, s65\n\t"
        "s_buffer_load_dword s40, s[20:23], s42 glc\n\t"
        "s_buffer_load_dword s41, s[20:23], s43 glc\n\t"
        "v_mov_b32 v7, s74\n\t"
        "s_mov_b32 exec_hi, 0\n\t"
        "buffer_store_dword v7, v8, s[20:23], s82 offen sc1\n\t"
        "s_lshr_b32 s78, %3, 2\n\t"
        "s_xor_b32 s44, s44, s78\n\t"
        "s_waitcnt lgkmcnt(1)\n\t"
        "s_nop 8\n\t"
        "s_getreg_b32 s45, hwreg(HW_REG_IB_STS, 8, 4)\n\t"
        "s_xnor_b32 s44, s44, s45\n\t"
        "s_and_b32 s44, s44, 1\n\t"
        "s_lshl_b32 s44, s44, 25\n\t"
        "v_mov_b32 v7, s44\n\t"
        "ds_write_b32 v2, v7 offset:1984\n\t"
        "s_waitcnt lgkmcnt(0)\n\t"
        "s_barrier\n\t"
        "s_mov_b64 exec, -1\n\t"
        "s_mov_b64 exec, 0xffff\n\t"
        "buffer_load_dword v6, s[28:31], s80 offen lds\n\t"
        "buffer_load_dword v6, s[24:27], s80 offen lds\n\t"
        "s_mov_b64 exec, -1\n\t"
        "s_branch .L7_T2\n"
        ".L7_V:\n\t"
        "s_and_b32 s78, s71, 1\n\t"
        "s_and_b32 s79, s76, 3\n\t"
        "s_lshl2_add_u32 s80, s78, s79\n\t"
        "s_and_b32 s78, s71, 2\n\t"
        "s_lshr_b32 s79, s76, 2\n\t"
        "s_lshl2_add_u32 s81, s78, s79\n\t"
        "v_lshlrev_b32 v12, 4, v1\n\t"
        "v_and_b32 v5, 0x200, v12\n\t"
        "v_mad_u32_u24 v4, v5, 15, v12\n\t"
        "s_lshl_b32 s82, s80, 14\n\t"
        "s_lshr_b32 s78, s81, 1\n\t"
        "s_lshl_b32 s78, s78, 14\n\t"
        "s_and_b32 s79, s81, 1\n\t"
        "s_lshl_b32 s79, s79, 11\n\t"
        "s_add_u32 s77, s78, s79\n\t"
        "s_mov_b32 m0, 68864\n\t"
        "s_waitcnt lgkmcnt(0)\n\t"
        "s_mul_i32 s72, s71, s65\n\t"
        "buffer_load_dwordx4 v4, s[36:39], s77 offen offset:0 lds\n\t"
        "s_add_u32 m0, m0, 544\n\t"
        "s_nop 0\n\t"
        "buffer_load_dwordx4 v4, s[36:39], s77 offen offset:512 lds\n\t"
        "s_add_u32 m0, m0, 544\n\t"
        "s_nop 0\n\t"
        "buffer_load_dwordx4 v4, s[36:39], s77 offen offset:1024 lds\n\t"
        "s_add_u32 m0, m0, 544\n\t"
        "s_nop 0\n\t"
        "buffer_load_dwordx4 v4, s[36:39], s77 offen offset:1536 lds\n\t"
        "s_add_u32 m0, m0, 2592\n\t"
        "s_add_u32 s77, s77, 0x1000\n\t"
        "buffer_load_dwordx4 v4, s[36:39], s77 offen offset:0 lds\n\t"
        "s_add_u32 m0, m0, 544\n\t"
        "s_nop 0\n\t"
        "buffer_load_dwordx4 v4, s[36:39], s77 offen offset:512 lds\n\t"
        "s_add_u32 m0, m0, 544\n\t"
        "s_nop 0\n\t"
        "buffer_load_dwordx4 v4, s[36:39], s77 offen offset:1024 lds\n\t"
        "s_add_u32 m0, m0, 544\n\t"
        "s_nop 0\n\t"
        "buffer_load_dwordx4 v4, s[36:39], s77 offen offset:1536 lds\n\t"
        "s_add_u32 m0, m0, 2592\n\t"
        "s_nop 0\n\t"
        "buffer_load_dwordx4 v4, s[32:35], s82 offen offset:0 lds\n\t"
        "s_add_u32 m0, m0, 544\n\t"
        "s_nop 0\n\t"
        "buffer_load_dwordx4 v4, s[32:35], s82 offen offset:512 lds\n\t"
        "s_add_u32 m0, m0, 544\n\t"
        "s_nop 0\n\t"
        "buffer_load_dwordx4 v4, s[32:35], s82 offen offset:1024 lds\n\t"
        "s_add_u32 m0, m0, 544\n\t"
        "s_nop 0\n\t"
        "buffer_load_dwordx4 v4, s[32:35], s82 offen offset:1536 lds\n\t"
        "s_add_u32 m0, m0, 2592\n\t"
        "s_add_u32 s82, s82, 0x1000\n\t"
        "buffer_load_dwordx4 v4, s[32:35], s82 offen offset:0 lds\n\t"
        "s_add_u32 m0, m0, 544\n\t"
        "s_nop 0\n\t"
        "buffer_load_dwordx4 v4, s[32:35], s82 offen offset:512 lds\n\t"
        "s_add_u32 m0, m0, 544\n\t"
        "s_nop 0\n\t"
        "buffer_load_dwordx4 v4, s[32:35], s82 offen offset:1024 lds\n\t"
        "s_add_u32 m0, m0, 544\n\t"
        "s_nop 0\n\t"
        "buffer_load_dwordx4 v4, s[32:35], s82 offen offset:1536 lds\n\t"
        "s_add_u32 m0, m0, 2592\n\t"
        "s_sub_u32 s82, s82, 0x800\n\t"
        "buffer_load_dwordx4 v4, s[32:35], s82 offen offset:0 lds\n\t"
        "s_add_u32 m0, m0, 544\n\t"
        "s_nop 0\n\t"
        "buffer_load_dwordx4 v4, s[32:35], s82 offen offset:512 lds\n\t"
        "s_add_u32 m0, m0, 544\n\t"
        "s_nop 0\n\t"
        "buffer_load_dwordx4 v4, s[32:35], s82 offen offset:1024 lds\n\t"
        "s_add_u32 m0, m0, 544\n\t"
        "s_nop 0\n\t"
        "buffer_load_dwordx4 v4, s[32:35], s82 offen offset:1536 lds\n\t"
        "s_add_u32 m0, m0, 2592\n\t"
        "s_add_u32 s82, s82, 0x1000\n\t"
        "buffer_load_dwordx4 v4, s[32:35], s82 offen offset:0 lds\n\t"
        "s_add_u32 m0, m0, 544\n\t"
        "s_nop 0\n\t"
        "buffer_load_dwordx4 v4, s[32:35], s82 offen offset:512 lds\n\t"
        "s_add_u32 m0, m0, 544\n\t"
        "s_nop 0\n\t"
        "buffer_load_dwordx4 v4, s[32:35], s82 offen offset:1024 lds\n\t"
        "s_add_u32 m0, m0, 544\n\t"
        "s_nop 0\n\t"
        "buffer_load_dwordx4 v4, s[32:35], s82 offen offset:1536 lds\n\t"
        "s_barrier\n\t"
        "s_branch .L7_V2\n"
        ".L7_U2:\n\t"
        "v_cmp_gt_u32 s[24:25], s46, v4\n\t"
        "v_cmp_gt_u32 s[26:27], s46, v5\n\t"
        "v_cmp_gt_u32 s[28:29], s46, v6\n\t"
        "v_cmp_gt_u32 s[30:31], s46, v7\n\t"
        "v_cmp_gt_u32 s[32:33], s46, v8\n\t"
        "v_mul_u32_u24 v112, 0xa0, v1\n\t"
        "v_add_u32 v112, 68864, v112\n\t"
        "v_add_u32 v113, 10240, v112\n\t"
        "v_lshlrev_b32 v7, 2, v1\n\t"
        "ds_write_b32 v7, v112 offset:1028\n\t"
        "ds_write_b32 v7, v113 offset:1284\n\t"
        "v_lshrrev_b32 v15, 3, v1\n\t"
        "v_mul_u32_u24 v15, 0xa0, v15\n\t"
        "v_add_u32 v15, 68864, v15\n\t"
        "v_and_b32 v4, 7, v1\n\t"
        "v_lshl_add_u32 v15, v4, 2, v15\n\t"
        "v_mov_b32 v13, 0x1000000\n\t"
        "v_mov_b32 v14, 4\n\t"
        "s_lshl_b32 s54, s65, 14\n\t"
        "s_sub_u32 s54, 0, s54\n\t"
        "s_lshl_b32 s48, s71, 14\n\t"
        "s_add_u32 s48, s48, 1048576\n\t"
        "s_add_u32 s49, s48, 4096\n\t"
        "v_add_u32 v97, 1280, v15\n\t"
        "v_add_u32 v98, 2560, v15\n\t"
        "v_add_u32 v99, 3840, v15\n\t"
        "v_add_u32 v100, 5120, v15\n\t"
        "v_add_u32 v101, 6400, v15\n\t"
        "v_add_u32 v102, 7680, v15\n\t"
        "v_add_u32 v103, 8960, v15\n\t"
        "v_add_u32 v112, 0x80, v112\n\t"
        "v_add_u32 v113, 0x80, v113\n\t"
        "v_add_u32 v114, s80, v1\n\t"
        "v_add_u32 v115, 64, v114\n\t"
        "v_cmp_gt_u32 s[36:37], s64, v114\n\t"
        "v_cmp_gt_u32 s[38:39], s64, v115\n\t"
        "s_waitcnt vmcnt(11) lgkmcnt(8)\n\t"
        "s_mov_b64 exec, s[24:25]\n\t"
        "v_mad_u32_u24 v56, v16, s66, v13\n\t"
        "v_mad_u32_u24 v57, v17, s66, v13\n\t"
        "v_mad_u32_u24 v58, v18, s66, v13\n\t"
        "v_mad_u32_u24 v59, v19, s66, v13\n\t"
        "v_lshlrev_b32_sdwa v56, 2, v56 dst_sel:DWORD dst_unused:UNUSED_PAD src0_sel:DWORD src1_sel:BYTE_3\n\t"
        "ds_add_rtn_u32 v76, v56, v14 offset:1024\n\t"
        "v_lshlrev_b32_sdwa v57, 2, v57 dst_sel:DWORD dst_unused:UNUSED_PAD src0_sel:DWORD src1_sel:BYTE_3\n\t"
        "ds_add_rtn_u32 v77, v57, v14 offset:1024\n\t"
        "v_lshlrev_b32_sdwa v58, 2, v58 dst_sel:DWORD dst_unused:UNUSED_PAD src0_sel:DWORD src1_sel:BYTE_3\n\t"
        "ds_add_rtn_u32 v78, v58, v14 offset:1024\n\t"
        "v_lshlrev_b32_sdwa v59, 2, v59 dst_sel:DWORD dst_unused:UNUSED_PAD src0_sel:DWORD src1_sel:BYTE_3\n\t"
        "ds_add_rtn_u32 v79, v59, v14 offset:1024\n\t"
        "s_waitcnt vmcnt(10) lgkmcnt(8)\n\t"
        "s_mov_b64 exec, s[26:27]\n\t"
        "v_mad_u32_u24 v60, v20, s66, v13\n\t"
        "v_mad_u32_u24 v61, v21, s66, v13\n\t"
        "v_mad_u32_u24 v62, v22, s66, v13\n\t"
        "v_mad_u32_u24 v63, v23, s66, v13\n\t"
        "v_lshlrev_b32_sdwa v60, 2, v60 dst_sel:DWORD dst_unused:UNUSED_PAD src0_sel:DWORD src1_sel:BYTE_3\n\t"
        "ds_add_rtn_u32 v80, v60, v14 offset:1024\n\t"
        "v_lshlrev_b32_sdwa v61, 2, v61 dst_sel:DWORD dst_unused:UNUSED_PAD src0_sel:DWORD src1_sel:BYTE_3\n\t"
        "ds_add_rtn_u32 v81, v61, v14 offset:1024\n\t"
        "v_lshlrev_b32_sdwa v62, 2, v62 dst_sel:DWORD dst_unused:UNUSED_PAD src0_sel:DWORD src1_sel:BYTE_3\n\t"
        "ds_add_rtn_u32 v82, v62, v14 offset:1024\n\t"
        "v_lshlrev_b32_sdwa v63, 2, v63 dst_sel:DWORD dst_unused:UNUSED_PAD src0_sel:DWORD src1_sel:BYTE_3\n\t"
        "ds_add_rtn_u32 v83, v63, v14 offset:1024\n\t"
        "s_waitcnt vmcnt(9) lgkmcnt(8)\n\t"
        "s_mov_b64 exec, s[28:29]\n\t"
        "v_mad_u32_u24 v64, v24, s66, v13\n\t"
        "v_mad_u32_u24 v65, v25, s66, v13\n\t"
        "v_mad_u32_u24 v66, v26, s66, v13\n\t"
        "v_mad_u32_u24 v67, v27, s66, v13\n\t"
        "v_lshlrev_b32_sdwa v64, 2, v64 dst_sel:DWORD dst_unused:UNUSED_PAD src0_sel:DWORD src1_sel:BYTE_3\n\t"
        "ds_add_rtn_u32 v84, v64, v14 offset:1024\n\t"
        "v_lshlrev_b32_sdwa v65, 2, v65 dst_sel:DWORD dst_unused:UNUSED_PAD src0_sel:DWORD src1_sel:BYTE_3\n\t"
        "ds_add_rtn_u32 v85, v65, v14 offset:1024\n\t"
        "v_lshlrev_b32_sdwa v66, 2, v66 dst_sel:DWORD dst_unused:UNUSED_PAD src0_sel:DWORD src1_sel:BYTE_3\n\t"
        "ds_add_rtn_u32 v86, v66, v14 offset:1024\n\t"
        "v_lshlrev_b32_sdwa v67, 2, v67 dst_sel:DWORD dst_unused:UNUSED_PAD src0_sel:DWORD src1_sel:BYTE_3\n\t"
        "ds_add_rtn_u32 v87, v67, v14 offset:1024\n\t"
        "s_waitcnt vmcnt(8) lgkmcnt(8)\n\t"
        "s_mov_b64 exec, s[30:31]\n\t"
        "v_mad_u32_u24 v68, v28, s66, v13\n\t"
        "v_mad_u32_u24 v69, v29, s66, v13\n\t"
        "v_mad_u32_u24 v70, v30, s66, v13\n\t"
        "v_mad_u32_u24 v71, v31, s66, v13\n\t"
        "v_lshlrev_b32_sdwa v68, 2, v68 dst_sel:DWORD dst_unused:UNUSED_PAD src0_sel:DWORD src1_sel:BYTE_3\n\t"
        "ds_add_rtn_u32 v88, v68, v14 offset:1024\n\t"
        "v_lshlrev_b32_sdwa v69, 2, v69 dst_sel:DWORD dst_unused:UNUSED_PAD src0_sel:DWORD src1_sel:BYTE_3\n\t"
        "ds_add_rtn_u32 v89, v69, v14 offset:1024\n\t"
        "v_lshlrev_b32_sdwa v70, 2, v70 dst_sel:DWORD dst_unused:UNUSED_PAD src0_sel:DWORD src1_sel:BYTE_3\n\t"
        "ds_add_rtn_u32 v90, v70, v14 offset:1024\n\t"
        "v_lshlrev_b32_sdwa v71, 2, v71 dst_sel:DWORD dst_unused:UNUSED_PAD src0_sel:DWORD src1_sel:BYTE_3\n\t"
        "ds_add_rtn_u32 v91, v71, v14 offset:1024\n\t"
        "s_waitcnt vmcnt(7) lgkmcnt(8)\n\t"
        "s_mov_b64 exec, s[32:33]\n\t"
        "v_mad_u32_u24 v72, v32, s66, v13\n\t"
        "v_mad_u32_u24 v73, v33, s66, v13\n\t"
        "v_mad_u32_u24 v74, v34, s66, v13\n\t"
        "v_mad_u32_u24 v75, v35, s66, v13\n\t"
        "v_lshlrev_b32_sdwa v72, 2, v72 dst_sel:DWORD dst_unused:UNUSED_PAD src0_sel:DWORD src1_sel:BYTE_3\n\t"
        "ds_add_rtn_u32 v92, v72, v14 offset:1024\n\t"
        "v_lshlrev_b32_sdwa v73, 2, v73 dst_sel:DWORD dst_unused:UNUSED_PAD src0_sel:DWORD src1_sel:BYTE_3\n\t"
        "ds_add_rtn_u32 v93, v73, v14 offset:1024\n\t"
        "v_lshlrev_b32_sdwa v74, 2, v74 dst_sel:DWORD dst_unused:UNUSED_PAD src0_sel:DWORD src1_sel:BYTE_3\n\t"
        "ds_add_rtn_u32 v94, v74, v14 offset:1024\n\t"
        "v_lshlrev_b32_sdwa v75, 2, v75 dst_sel:DWORD dst_unused:UNUSED_PAD src0_sel:DWORD src1_sel:BYTE_3\n\t"
        "ds_add_rtn_u32 v95, v75, v14 offset:1024\n\t"
        "s_mov_b64 exec, -1\n\t"
        "ds_read_b32 v4, v7 offset:1028\n\t"
        "ds_read_b32 v5, v7 offset:1284\n\t"
        "s_waitcnt vmcnt(5)\n\t"
        "v_xor_b32 v116, v116, v114\n\t"
        "v_bitop3_b32 v116, v117, v116, v114 bitop3:0xde\n\t"
        "v_bitop3_b32 v116, v118, v116, v114 bitop3:0xde\n\t"
        "v_bitop3_b32 v116, v119, v116, v114 bitop3:0xde\n\t"
        "v_xor_b32 v120, v120, v115\n\t"
        "v_bitop3_b32 v120, v121, v120, v115 bitop3:0xde\n\t"
        "v_bitop3_b32 v120, v122, v120, v115 bitop3:0xde\n\t"
        "v_bitop3_b32 v120, v123, v120, v115 bitop3:0xde\n\t"
        "v_cmp_ne_u32 s[40:41], 0, v116\n\t"
        "v_cmp_ne_u32 s[42:43], 0, v120\n\t"
        "s_and_b64 s[38:39], s[38:39], 0xffff\n\t"
        "s_and_b64 s[36:37], s[36:37], s[40:41]\n\t"
        "s_and_b64 s[38:39], s[38:39], s[42:43]\n\t"
        "s_or_b64 s[36:37], s[36:37], s[38:39]\n\t"
        "s_cselect_b32 s67, 1, s67\n"
        ".L7_w40:\n\t"
        "ds_read_b32 v0, v2 offset:104\n\t"
        "ds_read_b32 v6, v2 offset:76\n\t"
        "s_waitcnt lgkmcnt(1)\n\t"
        "v_cmp_gt_u16 vcc, 1, v0\n\t"
        "s_cbranch_vccz .L7_d41\n\t"
        "s_waitcnt lgkmcnt(0)\n\t"
        "s_sleep 0\n\t"
        "s_sub_i32 s83, s83, 1\n\t"
        "s_cmp_gt_i32 s83, 0\n\t"
        "s_cbranch_scc1 .L7_w40\n"
        ".L7_d41:\n\t"
        "v_cmp_gt_u32 s[34:35], v4, v112\n\t"
        "v_cmp_gt_u32 s[36:37], v5, v113\n\t"
        "s_or_b64 s[38:39], s[34:35], s[36:37]\n\t"
        "s_cbranch_scc1 .L7_ov\n"
        ".L7_ov1:\n\t"
        "s_waitcnt lgkmcnt(0)\n\t"
        "v_readfirstlane_b32 s68, v6\n\t"
        "s_waitcnt vmcnt(4) lgkmcnt(8)\n\t"
        "s_mov_b64 exec, s[24:25]\n\t"
        "v_lshl_add_u32 v16, v16, 16, v36\n\t"
        "v_lshl_add_u32 v17, v17, 16, v37\n\t"
        "v_lshl_add_u32 v18, v18, 16, v38\n\t"
        "v_lshl_add_u32 v19, v19, 16, v39\n\t"
        "v_mad_i32_i24 v16, v56, s54, v16\n\t"
        "ds_add_u32 v76, v16\n\t"
        "v_mad_i32_i24 v17, v57, s54, v17\n\t"
        "ds_add_u32 v77, v17\n\t"
        "v_mad_i32_i24 v18, v58, s54, v18\n\t"
        "ds_add_u32 v78, v18\n\t"
        "v_mad_i32_i24 v19, v59, s54, v19\n\t"
        "ds_add_u32 v79, v19\n\t"
        "s_waitcnt vmcnt(3) lgkmcnt(8)\n\t"
        "s_mov_b64 exec, s[26:27]\n\t"
        "v_lshl_add_u32 v20, v20, 16, v40\n\t"
        "v_lshl_add_u32 v21, v21, 16, v41\n\t"
        "v_lshl_add_u32 v22, v22, 16, v42\n\t"
        "v_lshl_add_u32 v23, v23, 16, v43\n\t"
        "v_mad_i32_i24 v20, v60, s54, v20\n\t"
        "ds_add_u32 v80, v20\n\t"
        "v_mad_i32_i24 v21, v61, s54, v21\n\t"
        "ds_add_u32 v81, v21\n\t"
        "v_mad_i32_i24 v22, v62, s54, v22\n\t"
        "ds_add_u32 v82, v22\n\t"
        "v_mad_i32_i24 v23, v63, s54, v23\n\t"
        "ds_add_u32 v83, v23\n\t"
        "s_waitcnt vmcnt(2) lgkmcnt(8)\n\t"
        "s_mov_b64 exec, s[28:29]\n\t"
        "v_lshl_add_u32 v24, v24, 16, v44\n\t"
        "v_lshl_add_u32 v25, v25, 16, v45\n\t"
        "v_lshl_add_u32 v26, v26, 16, v46\n\t"
        "v_lshl_add_u32 v27, v27, 16, v47\n\t"
        "v_mad_i32_i24 v24, v64, s54, v24\n\t"
        "ds_add_u32 v84, v24\n\t"
        "v_mad_i32_i24 v25, v65, s54, v25\n\t"
        "ds_add_u32 v85, v25\n\t"
        "v_mad_i32_i24 v26, v66, s54, v26\n\t"
        "ds_add_u32 v86, v26\n\t"
        "v_mad_i32_i24 v27, v67, s54, v27\n\t"
        "ds_add_u32 v87, v27\n\t"
        "s_waitcnt vmcnt(1) lgkmcnt(8)\n\t"
        "s_mov_b64 exec, s[30:31]\n\t"
        "v_lshl_add_u32 v28, v28, 16, v48\n\t"
        "v_lshl_add_u32 v29, v29, 16, v49\n\t"
        "v_lshl_add_u32 v30, v30, 16, v50\n\t"
        "v_lshl_add_u32 v31, v31, 16, v51\n\t"
        "v_mad_i32_i24 v28, v68, s54, v28\n\t"
        "ds_add_u32 v88, v28\n\t"
        "v_mad_i32_i24 v29, v69, s54, v29\n\t"
        "ds_add_u32 v89, v29\n\t"
        "v_mad_i32_i24 v30, v70, s54, v30\n\t"
        "ds_add_u32 v90, v30\n\t"
        "v_mad_i32_i24 v31, v71, s54, v31\n\t"
        "ds_add_u32 v91, v31\n\t"
        "s_waitcnt vmcnt(0) lgkmcnt(8)\n\t"
        "s_mov_b64 exec, s[32:33]\n\t"
        "v_lshl_add_u32 v32, v32, 16, v52\n\t"
        "v_lshl_add_u32 v33, v33, 16, v53\n\t"
        "v_lshl_add_u32 v34, v34, 16, v54\n\t"
        "v_lshl_add_u32 v35, v35, 16, v55\n\t"
        "v_mad_i32_i24 v32, v72, s54, v32\n\t"
        "ds_add_u32 v92, v32\n\t"
        "v_mad_i32_i24 v33, v73, s54, v33\n\t"
        "ds_add_u32 v93, v33\n\t"
        "v_mad_i32_i24 v34, v74, s54, v34\n\t"
        "ds_add_u32 v94, v34\n\t"
        "v_mad_i32_i24 v35, v75, s54, v35\n\t"
        "ds_add_u32 v95, v35\n\t"
        "s_mov_b64 exec, -1\n\t"
        "s_and_b32 s78, s67, 1\n\t"
        "s_or_b32 s78, s78, s38\n\t"
        "s_or_b32 s78, s78, s39\n\t"
        "s_cbranch_scc1 .L7_mk\n"
        ".L7_mk1:\n\t"
        "s_mov_b64 exec, 1\n\t"
        "ds_add_u32 v2, v3 offset:112\n\t"
        "s_mov_b64 exec, -1\n\t"
        "s_waitcnt lgkmcnt(3)\n\t"
        "ds_read2_b32 v[16:17], v15 offset0:0 offset1:8\n\t"
        "ds_read2_b32 v[18:19], v15 offset0:16 offset1:24\n\t"
        "ds_read2_b32 v[20:21], v97 offset0:0 offset1:8\n\t"
        "ds_read2_b32 v[22:23], v97 offset0:16 offset1:24\n\t"
        "ds_read2_b32 v[24:25], v98 offset0:0 offset1:8\n\t"
        "ds_read2_b32 v[26:27], v98 offset0:16 offset1:24\n\t"
        "ds_read2_b32 v[28:29], v99 offset0:0 offset1:8\n\t"
        "ds_read2_b32 v[30:31], v99 offset0:16 offset1:24\n\t"
        "ds_read2_b32 v[32:33], v100 offset0:0 offset1:8\n\t"
        "ds_read2_b32 v[34:35], v100 offset0:16 offset1:24\n\t"
        "ds_read2_b32 v[36:37], v101 offset0:0 offset1:8\n\t"
        "ds_read2_b32 v[38:39], v101 offset0:16 offset1:24\n\t"
        "s_waitcnt lgkmcnt(10)\n\t"
        "buffer_store_dwordx4 v[16:19], v12, s[20:23], s48 offen offset:0 sc1\n\t"
        "ds_read2_b32 v[40:41], v102 offset0:0 offset1:8\n\t"
        "ds_read2_b32 v[42:43], v102 offset0:16 offset1:24\n\t"
        "s_waitcnt lgkmcnt(10)\n\t"
        "buffer_store_dwordx4 v[20:23], v12, s[20:23], s48 offen offset:1024 sc1\n\t"
        "ds_read2_b32 v[44:45], v103 offset0:0 offset1:8\n\t"
        "ds_read2_b32 v[46:47], v103 offset0:16 offset1:24\n\t"
        "s_mov_b64 exec, 1\n\t"
        "ds_add_u32 v2, v3 offset:72\n\t"
        "s_mov_b64 exec, -1\n\t"
        "s_waitcnt lgkmcnt(11)\n\t"
        "buffer_store_dwordx4 v[24:27], v12, s[20:23], s48 offen offset:2048 sc1\n\t"
        "s_waitcnt lgkmcnt(9)\n\t"
        "buffer_store_dwordx4 v[28:31], v12, s[20:23], s48 offen offset:3072 sc1\n\t"
        "s_waitcnt lgkmcnt(7)\n\t"
        "buffer_store_dwordx4 v[32:35], v12, s[20:23], s49 offen offset:0 sc1\n\t"
        "s_waitcnt lgkmcnt(5)\n\t"
        "buffer_store_dwordx4 v[36:39], v12, s[20:23], s49 offen offset:1024 sc1\n\t"
        "s_waitcnt lgkmcnt(3)\n\t"
        "buffer_store_dwordx4 v[40:43], v12, s[20:23], s49 offen offset:2048 sc1\n\t"
        "s_waitcnt lgkmcnt(1)\n\t"
        "buffer_store_dwordx4 v[44:47], v12, s[20:23], s49 offen offset:3072 sc1\n\t"
        "s_mov_b64 exec, 1\n\t"
        "ds_add_u32 v2, v3 offset:44\n\t"
        "s_mov_b64 exec, -1\n\t"
        "s_branch .L7_Q\n"
        ".L7_ov:\n\t"
        "s_mov_b32 s78, 68828\n\t"
        "v_mov_b32 v9, 40\n\t"
        "v_mad_u32_u24 v8, v56, v9, s78\n\t"
        "v_min_u32 v76, v76, v8\n\t"
        "v_mad_u32_u24 v8, v57, v9, s78\n\t"
        "v_min_u32 v77, v77, v8\n\t"
        "v_mad_u32_u24 v8, v58, v9, s78\n\t"
        "v_min_u32 v78, v78, v8\n\t"
        "v_mad_u32_u24 v8, v59, v9, s78\n\t"
        "v_min_u32 v79, v79, v8\n\t"
        "v_mad_u32_u24 v8, v60, v9, s78\n\t"
        "v_min_u32 v80, v80, v8\n\t"
        "v_mad_u32_u24 v8, v61, v9, s78\n\t"
        "v_min_u32 v81, v81, v8\n\t"
        "v_mad_u32_u24 v8, v62, v9, s78\n\t"
        "v_min_u32 v82, v82, v8\n\t"
        "v_mad_u32_u24 v8, v63, v9, s78\n\t"
        "v_min_u32 v83, v83, v8\n\t"
        "v_mad_u32_u24 v8, v64, v9, s78\n\t"
        "v_min_u32 v84, v84, v8\n\t"
        "v_mad_u32_u24 v8, v65, v9, s78\n\t"
        "v_min_u32 v85, v85, v8\n\t"
        "v_mad_u32_u24 v8, v66, v9, s78\n\t"
        "v_min_u32 v86, v86, v8\n\t"
        "v_mad_u32_u24 v8, v67, v9, s78\n\t"
        "v_min_u32 v87, v87, v8\n\t"
        "v_mad_u32_u24 v8, v68, v9, s78\n\t"
        "v_min_u32 v88, v88, v8\n\t"
        "v_mad_u32_u24 v8, v69, v9, s78\n\t"
        "v_min_u32 v89, v89, v8\n\t"
        "v_mad_u32_u24 v8, v70, v9, s78\n\t"
        "v_min_u32 v90, v90, v8\n\t"
        "v_mad_u32_u24 v8, v71, v9, s78\n\t"
        "v_min_u32 v91, v91, v8\n\t"
        "v_mad_u32_u24 v8, v72, v9, s78\n\t"
        "v_min_u32 v92, v92, v8\n\t"
        "v_mad_u32_u24 v8, v73, v9, s78\n\t"
        "v_min_u32 v93, v93, v8\n\t"
        "v_mad_u32_u24 v8, v74, v9, s78\n\t"
        "v_min_u32 v94, v94, v8\n\t"
        "v_mad_u32_u24 v8, v75, v9, s78\n\t"
        "v_min_u32 v95, v95, v8\n\t"
        "s_branch .L7_ov1\n"
        ".L7_mk:\n\t"
        "s_mul_i32 s78, s68, 0x4000400\n\t"
        "s_or_b32 s78, s78, 0xffffff\n\t"
        "v_mov_b32 v6, s78\n\t"
        "v_add_u32 v112, -4, v112\n\t"
        "v_add_u32 v113, -4, v113\n\t"
        "s_mov_b64 exec, s[34:35]\n\t"
        "ds_write_b32 v112, v6\n\t"
        "s_mov_b64 exec, s[36:37]\n\t"
        "ds_write_b32 v113, v6\n\t"
        "s_mov_b64 exec, -1\n\t"
        "s_and_b32 s78, s67, 1\n\t"
        "s_lshl_b32 s78, s78, 24\n\t"
        "v_mov_b32 v6, s78\n\t"
        "ds_xor_b32 v112, v6\n\t"
        "ds_xor_b32 v113, v6\n\t"
        "s_branch .L7_mk1\n"
        ".L7_P2:\n\t"
        "s_setprio 3\n\t"
        "ds_read_b32 v6, v2 offset:1984\n\t"
        "v_lshrrev_b32 v15, 3, v1\n\t"
        "v_mul_u32_u24 v15, 0xa0, v15\n\t"
        "v_add_u32 v15, 68864, v15\n\t"
        "v_and_b32 v4, 7, v1\n\t"
        "v_lshl_add_u32 v5, v4, 4, v15\n\t"
        "v_lshl_add_u32 v15, v4, 2, v15\n\t"
        "s_mov_b32 s34, 0x0c0c0c0c\n\t"
        "s_mov_b32 s35, s34\n\t"
        "s_lshl_b64 s[36:37], s[34:35], 2\n\t"
        "s_lshl_b64 s[38:39], s[34:35], 4\n\t"
        "s_waitcnt lgkmcnt(0)\n\t"
        "s_add_u32 s68, s68, 1\n\t"
        "v_mov_b32 v4, s68\n\t"
        "ds_write_b32 v2, v4 offset:76\n\t"
        "s_cmp_eq_u32 s70, 0\n\t"
        "s_cbranch_scc0 .L7_P3\n\t"
        "s_lshl_b32 s78, s68, 24\n\t"
        "v_mov_b32 v8, s78\n\t"
        "s_xor_b32 s78, s78, 0x5a000000\n\t"
        "v_mov_b32 v9, s78\n\t"
        "v_cndmask_b32 v8, v8, v9, s[34:35]\n\t"
        "s_mul_i32 s78, s68, 0x01010000\n\t"
        "v_mov_b32 v9, s78\n\t"
        "v_cndmask_b32 v8, v8, v9, s[36:37]\n\t"
        "s_mul_i32 s55, s68, 0x4000400\n\t"
        "v_xor_b32 v9, s55, v6\n\t"
        "v_cndmask_b32 v8, v8, v9, s[38:39]\n\t"
        "v_and_b32 v8, 0xff000000, v8\n\t"
        "v_or_b32 v8, 0xff0000, v8\n\t"
        "v_mov_b32 v9, v8\n\t"
        "v_mov_b64 v[10:11], v[8:9]\n\t"
        "ds_write_b128 v5, v[8:11] offset:0\n\t"
        "ds_write_b128 v5, v[8:11] offset:1280\n\t"
        "ds_write_b128 v5, v[8:11] offset:2560\n\t"
        "ds_write_b128 v5, v[8:11] offset:3840\n\t"
        "ds_write_b128 v5, v[8:11] offset:5120\n\t"
        "ds_write_b128 v5, v[8:11] offset:6400\n\t"
        "ds_write_b128 v5, v[8:11] offset:7680\n\t"
        "ds_write_b128 v5, v[8:11] offset:8960\n\t"
        "ds_write_b128 v5, v[8:11] offset:10240\n\t"
        "ds_write_b128 v5, v[8:11] offset:11520\n\t"
        "ds_write_b128 v5, v[8:11] offset:12800\n\t"
        "ds_write_b128 v5, v[8:11] offset:14080\n\t"
        "s_waitcnt lgkmcnt(6)\n\t"
        "ds_write_b128 v5, v[8:11] offset:15360\n\t"
        "ds_write_b128 v5, v[8:11] offset:16640\n\t"
        "ds_write_b128 v5, v[8:11] offset:17920\n\t"
        "ds_write_b128 v5, v[8:11] offset:19200\n"
        ".L7_P3:\n\t"
        "s_mov_b64 exec, 1\n\t"
        "ds_add_u32 v2, v3 offset:104\n\t"
        "s_mov_b64 exec, -1\n\t"
        "v_lshlrev_b32 v12, 4, v1\n\t"
        "v_add_u32 v4, 148160, v12\n\t"
        "v_mul_u32_u24 v5, 196, v1\n\t"
        "v_add_u32 v5, 148160, v5\n\t"
        "v_mov_b32 v8, 0xffff\n\t"
        "v_mov_b32 v9, v8\n\t"
        "v_mov_b64 v[10:11], v[8:9]\n\t"
        "s_cmp_eq_u32 s70, 0\n\t"
        "s_cbranch_scc0 .L7_P4\n\t"
        "v_add_u32 v15, 10240, v15\n\t"
        "v_add_u32 v97, 1280, v15\n\t"
        "v_add_u32 v98, 2560, v15\n\t"
        "v_add_u32 v99, 3840, v15\n\t"
        "v_add_u32 v100, 5120, v15\n\t"
        "v_add_u32 v101, 6400, v15\n\t"
        "v_add_u32 v102, 7680, v15\n\t"
        "v_add_u32 v103, 8960, v15\n\t"
        "s_lshl_b32 s48, s71, 14\n\t"
        "s_add_u32 s48, s48, 1056768\n\t"
        "s_add_u32 s49, s48, 4096\n"
        ".L7_w46:\n\t"
        "ds_read_b32 v0, v2 offset:112\n\t"
        "s_waitcnt lgkmcnt(0)\n\t"
        "v_cmp_gt_u16 vcc, 1, v0\n\t"
        "s_cbranch_vccz .L7_d47\n\t"
        "s_sleep 0\n\t"
        "s_sub_i32 s83, s83, 1\n\t"
        "s_cmp_gt_i32 s83, 0\n\t"
        "s_cbranch_scc1 .L7_w46\n"
        ".L7_d47:\n\t"
        "ds_read2_b32 v[16:17], v15 offset0:0 offset1:8\n\t"
        "ds_read2_b32 v[18:19], v15 offset0:16 offset1:24\n\t"
        "ds_read2_b32 v[20:21], v97 offset0:0 offset1:8\n\t"
        "ds_read2_b32 v[22:23], v97 offset0:16 offset1:24\n\t"
        "ds_read2_b32 v[24:25], v98 offset0:0 offset1:8\n\t"
        "ds_read2_b32 v[26:27], v98 offset0:16 offset1:24\n\t"
        "ds_read2_b32 v[28:29], v99 offset0:0 offset1:8\n\t"
        "ds_read2_b32 v[30:31], v99 offset0:16 offset1:24\n\t"
        "ds_read2_b32 v[32:33], v100 offset0:0 offset1:8\n\t"
        "ds_read2_b32 v[34:35], v100 offset0:16 offset1:24\n\t"
        "ds_read2_b32 v[36:37], v101 offset0:0 offset1:8\n\t"
        "ds_read2_b32 v[38:39], v101 offset0:16 offset1:24\n\t"
        "s_waitcnt lgkmcnt(10)\n\t"
        "buffer_store_dwordx4 v[16:19], v12, s[20:23], s48 offen offset:0 sc1\n\t"
        "ds_read2_b32 v[40:41], v102 offset0:0 offset1:8\n\t"
        "ds_read2_b32 v[42:43], v102 offset0:16 offset1:24\n\t"
        "s_waitcnt lgkmcnt(10)\n\t"
        "buffer_store_dwordx4 v[20:23], v12, s[20:23], s48 offen offset:1024 sc1\n\t"
        "ds_read2_b32 v[44:45], v103 offset0:0 offset1:8\n\t"
        "ds_read2_b32 v[46:47], v103 offset0:16 offset1:24\n\t"
        "s_mov_b64 exec, 1\n\t"
        "ds_add_u32 v2, v3 offset:72\n\t"
        "s_mov_b64 exec, -1\n\t"
        "s_waitcnt lgkmcnt(11)\n\t"
        "buffer_store_dwordx4 v[24:27], v12, s[20:23], s48 offen offset:2048 sc1\n\t"
        "s_waitcnt lgkmcnt(9)\n\t"
        "buffer_store_dwordx4 v[28:31], v12, s[20:23], s48 offen offset:3072 sc1\n\t"
        "s_waitcnt lgkmcnt(7)\n\t"
        "buffer_store_dwordx4 v[32:35], v12, s[20:23], s49 offen offset:0 sc1\n\t"
        "s_waitcnt lgkmcnt(5)\n\t"
        "buffer_store_dwordx4 v[36:39], v12, s[20:23], s49 offen offset:1024 sc1\n\t"
        "s_waitcnt lgkmcnt(3)\n\t"
        "buffer_store_dwordx4 v[40:43], v12, s[20:23], s49 offen offset:2048 sc1\n\t"
        "s_waitcnt lgkmcnt(1)\n\t"
        "buffer_store_dwordx4 v[44:47], v12, s[20:23], s49 offen offset:3072 sc1\n\t"
        "s_mov_b64 exec, 1\n\t"
        "ds_add_u32 v2, v3 offset:44\n\t"
        "s_mov_b64 exec, -1\n"
        ".L7_P4:\n\t"
        "s_setprio 0\n\t"
        "s_waitcnt lgkmcnt(0)\n\t"
        "ds_write_b128 v4, v[8:11] offset:0\n\t"
        "ds_write_b128 v4, v[8:11] offset:1024\n\t"
        "ds_write_b128 v4, v[8:11] offset:2048\n\t"
        "ds_write_b128 v4, v[8:11] offset:3072\n\t"
        "ds_write_b128 v4, v[8:11] offset:4096\n\t"
        "ds_write_b128 v4, v[8:11] offset:5120\n\t"
        "ds_write_b128 v4, v[8:11] offset:6144\n\t"
        "ds_write_b128 v4, v[8:11] offset:7168\n\t"
        "ds_write_b128 v4, v[8:11] offset:8192\n\t"
        "ds_write_b128 v4, v[8:11] offset:9216\n\t"
        "ds_write_b128 v4, v[8:11] offset:10240\n\t"
        "ds_write_b128 v4, v[8:11] offset:11264\n\t"
        "s_waitcnt lgkmcnt(6)\n\t"
        "ds_write_b128 v4, v[8:11] offset:12288\n\t"
        "ds_write_b128 v4, v[8:11] offset:13312\n\t"
        "ds_write_b128 v4, v[8:11] offset:14336\n\t"
        "ds_write_b128 v4, v[8:11] offset:15360\n\t"
        "ds_write_b32 v5, v2 offset:192\n\t"
        "s_mov_b64 exec, 0xffff\n\t"
        "ds_write_b32 v5, v2 offset:12736\n\t"
        "s_mov_b64 exec, 1\n\t"
        "ds_add_u32 v2, v3 offset:8\n\t"
        "s_mov_b64 exec, -1\n\t"
        "s_branch .L7_Q\n"
        ".L7_V2:\n\t"
        "v_and_b32 v5, 7, v1\n\t"
        "v_mul_u32_u24 v5, 0x420, v5\n\t"
        "v_and_b32 v6, 8, v1\n\t"
        "v_lshl_add_u32 v5, v6, 6, v5\n\t"
        "v_and_b32 v6, 48, v1\n\t"
        "v_add_u32 v5, v5, v6\n\t"
        "v_add_u32 v5, 68864, v5\n\t"
        "s_lshr_b32 s78, s81, 1\n\t"
        "s_lshl3_add_u32 s78, s78, s80\n\t"
        "s_and_b32 s79, s81, 1\n\t"
        "s_lshl1_add_u32 s78, s78, s79\n\t"
        "s_lshl_b32 s82, s78, 10\n\t"
        "s_add_u32 s82, s82, 131072\n"
        ".L7_w42:\n\t"
        "ds_read_b32 v0, v2 offset:104\n\t"
        "ds_read_b32 v6, v2 offset:76\n\t"
        "s_waitcnt lgkmcnt(1)\n\t"
        "v_cmp_gt_u16 vcc, 1, v0\n\t"
        "s_cbranch_vccz .L7_d43\n\t"
        "s_waitcnt lgkmcnt(0)\n\t"
        "s_sleep 1\n\t"
        "s_sub_i32 s83, s83, 1\n\t"
        "s_cmp_gt_i32 s83, 0\n\t"
        "s_cbranch_scc1 .L7_w42\n"
        ".L7_d43:\n\t"
        "s_waitcnt lgkmcnt(0)\n\t"
        "v_readfirstlane_b32 s68, v6\n\t"
        "v_mov_b32 v7, 2\n\t"
        "s_and_b32 s73, s68, 1\n\t"
        "s_cselect_b32 s73, 1, 0x10000\n\t"
        "v_mov_b32 v16, s73\n\t"
        "v_and_b32 v17, 7, v1\n\t"
        "v_add_u32 v17, 1, v17\n\t"
        "v_lshrrev_b32 v17, v17, s68\n\t"
        "s_mov_b32 s49, 0x10001\n\t"
        "v_and_b32 v17, s49, v17\n\t"
        "s_mov_b32 s48, 0x3db8aa3b\n\t"
        "s_waitcnt vmcnt(0)\n\t"
        "ds_read_b128 v[32:35], v5 offset:0\n\t"
        "ds_read_b128 v[36:39], v5 offset:8448\n\t"
        "ds_read_b128 v[40:43], v5 offset:16896\n\t"
        "ds_read_b128 v[44:47], v5 offset:64\n\t"
        "ds_read_b128 v[48:51], v5 offset:8512\n\t"
        "ds_read_b128 v[52:55], v5 offset:16960\n\t"
        "ds_read_b128 v[56:59], v5 offset:128\n\t"
        "ds_read_b128 v[60:63], v5 offset:8576\n\t"
        "ds_read_b128 v[64:67], v5 offset:17024\n\t"
        "ds_read_b128 v[68:71], v5 offset:192\n\t"
        "ds_read_b128 v[72:75], v5 offset:8640\n\t"
        "ds_read_b128 v[76:79], v5 offset:17088\n\t"
        "s_waitcnt lgkmcnt(9)\n\t"
        "v_mfma_f32_16x16x32_bf16 v[20:23], v[36:39], v[32:35], 0\n\t"
        "v_mfma_f32_16x16x32_bf16 v[24:27], v[40:43], v[32:35], 0\n\t"
        "ds_read_b128 v[80:83], v5 offset:256\n\t"
        "ds_read_b128 v[84:87], v5 offset:8704\n\t"
        "ds_read_b128 v[88:91], v5 offset:17152\n\t"
        "s_waitcnt lgkmcnt(9)\n\t"
        "v_mfma_f32_16x16x32_bf16 v[20:23], v[48:51], v[44:47], v[20:23]\n\t"
        "v_mfma_f32_16x16x32_bf16 v[24:27], v[52:55], v[44:47], v[24:27]\n\t"
        "ds_read_b128 v[92:95], v5 offset:320\n\t"
        "ds_read_b128 v[96:99], v5 offset:8768\n\t"
        "ds_read_b128 v[100:103], v5 offset:17216\n\t"
        "s_waitcnt lgkmcnt(9)\n\t"
        "v_mfma_f32_16x16x32_bf16 v[20:23], v[60:63], v[56:59], v[20:23]\n\t"
        "v_mfma_f32_16x16x32_bf16 v[24:27], v[64:67], v[56:59], v[24:27]\n\t"
        "ds_read_b128 v[104:107], v5 offset:384\n\t"
        "ds_read_b128 v[108:111], v5 offset:8832\n\t"
        "ds_read_b128 v[112:115], v5 offset:17280\n\t"
        "s_waitcnt lgkmcnt(9)\n\t"
        "v_mfma_f32_16x16x32_bf16 v[20:23], v[72:75], v[68:71], v[20:23]\n\t"
        "v_mfma_f32_16x16x32_bf16 v[24:27], v[76:79], v[68:71], v[24:27]\n\t"
        "ds_read_b128 v[116:119], v5 offset:448\n\t"
        "ds_read_b128 v[120:123], v5 offset:8896\n\t"
        "ds_read_b128 v[124:127], v5 offset:17344\n\t"
        "s_waitcnt lgkmcnt(9)\n\t"
        "v_mfma_f32_16x16x32_bf16 v[20:23], v[84:87], v[80:83], v[20:23]\n\t"
        "v_mfma_f32_16x16x32_bf16 v[24:27], v[88:91], v[80:83], v[24:27]\n\t"
        "s_mov_b64 exec, 1\n\t"
        "ds_add_u32 v2, v7 offset:72\n\t"
        "s_mov_b64 exec, -1\n\t"
        "s_waitcnt lgkmcnt(7)\n\t"
        "v_mfma_f32_16x16x32_bf16 v[20:23], v[96:99], v[92:95], v[20:23]\n\t"
        "v_mfma_f32_16x16x32_bf16 v[24:27], v[100:103], v[92:95], v[24:27]\n\t"
        "s_waitcnt lgkmcnt(4)\n\t"
        "v_mfma_f32_16x16x32_bf16 v[20:23], v[108:111], v[104:107], v[20:23]\n\t"
        "v_mfma_f32_16x16x32_bf16 v[24:27], v[112:115], v[104:107], v[24:27]\n\t"
        "s_waitcnt lgkmcnt(1)\n\t"
        "v_mfma_f32_16x16x32_bf16 v[20:23], v[120:123], v[116:119], v[20:23]\n\t"
        "v_mfma_f32_16x16x32_bf16 v[24:27], v[124:127], v[116:119], v[24:27]\n\t"
        "s_nop 6\n\t"
        "v_mul_f32 v20, s48, v20\n\t"
        "v_mul_f32 v21, s48, v21\n\t"
        "v_mul_f32 v22, s48, v22\n\t"
        "v_mul_f32 v23, s48, v23\n\t"
        "v_mul_f32 v24, s48, v24\n\t"
        "v_mul_f32 v25, s48, v25\n\t"
        "v_mul_f32 v26, s48, v26\n\t"
        "v_mul_f32 v27, s48, v27\n\t"
        "s_mov_b32 s48, 0x07060302\n\t"
        "v_add_u32 v20, 0x10000, v20\n\t"
        "v_add_u32 v21, 0x10000, v21\n\t"
        "v_add_u32 v22, 0x10000, v22\n\t"
        "v_add_u32 v23, 0x10000, v23\n\t"
        "v_perm_b32 v20, v21, v20, s48\n\t"
        "v_perm_b32 v21, v23, v22, s48\n\t"
        "v_cvt_pk_bf16_f32 v22, v24, v25\n\t"
        "v_cvt_pk_bf16_f32 v23, v26, v27\n\t"
        "v_bfi_b32 v20, s49, v16, v20\n\t"
        "v_bfi_b32 v21, s49, v17, v21\n\t"
        "buffer_store_dwordx4 v[20:23], v12, s[20:23], s82 offen sc1\n\t"
        "s_mov_b64 exec, 1\n\t"
        "ds_add_u32 v2, v7 offset:44\n\t"
        "s_mov_b64 exec, -1\n\t"
        "s_branch .L7_Q\n"
        ".L7_T2:\n\t"
        "s_and_b32 s82, s82, -1024\n\t"
        "v_mov_b32 v9, 4\n"
        ".L7_g44:\n\t"
        "s_memrealtime s[78:79]\n\t"
        "s_waitcnt lgkmcnt(0)\n\t"
        "s_sub_u32 s78, s78, s74\n\t"
        "s_cmp_ge_i32 s78, 150\n\t"
        "s_cbranch_scc1 .L7_h45\n\t"
        "s_sleep 1\n\t"
        "s_branch .L7_g44\n"
        ".L7_h45:\n"
        ".L7_b1:\n\t"
        "s_buffer_load_dword s40, s[20:23], s82 offset:0 glc\n\t"
        "s_buffer_load_dword s41, s[20:23], s82 offset:128 glc\n\t"
        "s_buffer_load_dword s42, s[20:23], s82 offset:256 glc\n\t"
        "s_buffer_load_dword s43, s[20:23], s82 offset:384 glc\n\t"
        "s_buffer_load_dword s44, s[20:23], s82 offset:512 glc\n\t"
        "s_buffer_load_dword s45, s[20:23], s82 offset:640 glc\n\t"
        "s_buffer_load_dword s46, s[20:23], s82 offset:768 glc\n\t"
        "s_buffer_load_dword s47, s[20:23], s82 offset:896 glc\n\t"
        "s_waitcnt lgkmcnt(0)\n\t"
        "s_mov_b32 s80, 0\n\t"
        "s_mov_b32 s81, 0\n\t"
        "s_sub_u32 s40, s40, s74\n\t"
        "s_max_i32 s80, s80, s40\n\t"
        "s_min_i32 s81, s81, s40\n\t"
        "s_sub_u32 s41, s41, s74\n\t"
        "s_max_i32 s80, s80, s41\n\t"
        "s_min_i32 s81, s81, s41\n\t"
        "s_sub_u32 s42, s42, s74\n\t"
        "s_max_i32 s80, s80, s42\n\t"
        "s_min_i32 s81, s81, s42\n\t"
        "s_sub_u32 s43, s43, s74\n\t"
        "s_max_i32 s80, s80, s43\n\t"
        "s_min_i32 s81, s81, s43\n\t"
        "s_sub_u32 s44, s44, s74\n\t"
        "s_max_i32 s80, s80, s44\n\t"
        "s_min_i32 s81, s81, s44\n\t"
        "s_sub_u32 s45, s45, s74\n\t"
        "s_max_i32 s80, s80, s45\n\t"
        "s_min_i32 s81, s81, s45\n\t"
        "s_sub_u32 s46, s46, s74\n\t"
        "s_max_i32 s80, s80, s46\n\t"
        "s_min_i32 s81, s81, s46\n\t"
        "s_sub_u32 s47, s47, s74\n\t"
        "s_max_i32 s80, s80, s47\n\t"
        "s_min_i32 s81, s81, s47\n\t"
        "s_sub_u32 s81, s80, s81\n\t"
        "s_cmp_lt_u32 s81, 0x3e8\n\t"
        "s_cbranch_scc1 .L7_b2\n\t"
        "s_mov_b64 exec, 1\n\t"
        "ds_add_u32 v2, v9 offset:84\n\t"
        "s_mov_b64 exec, -1\n\t"
        "s_mov_b64 exec, 1\n\t"
        "ds_add_u32 v2, v9 offset:108\n\t"
        "s_mov_b64 exec, -1\n\t"
        "v_mov_b32 v9, 0\n\t"
        "s_mov_b32 s80, 0\n\t"
        "s_sleep 4\n\t"
        "s_memrealtime s[78:79]\n\t"
        "s_waitcnt lgkmcnt(0)\n\t"
        "s_sub_u32 s78, s78, s74\n\t"
        "s_cmp_lt_u32 s78, 0x4b0\n\t"
        "s_cbranch_scc1 .L7_b1\n"
        ".L7_b2:\n\t"
        "s_add_u32 s80, s80, 1\n\t"
        "v_mov_b32 v7, s80\n\t"
        "s_mov_b64 exec, 1\n\t"
        "ds_add_u32 v2, v7 offset:92\n\t"
        "s_mov_b64 exec, -1\n\t"
        "s_branch .L7_Q0\n"
        ".L7_R2:\n\t"
        "s_sub_u32 s79, s64, s72\n\t"
        "s_min_i32 s79, s79, s65\n\t"
        "s_max_i32 s79, s79, 0\n\t"
        "v_mov_b32 v32, v1\n\t"
        "v_add_u32 v33, 64, v1\n\t"
        "v_cmp_gt_u32 s[32:33], s79, v32\n\t"
        "v_cmp_gt_u32 s[34:35], s79, v33\n\t"
        "v_cmp_gt_u32 s[36:37], 40, v1\n\t"
        "s_sub_u32 s78, 0xff, s65\n\t"
        "v_add_u32 v34, s78, v32\n\t"
        "v_add_u32 v6, 0xffffffb0, v34\n\t"
        "v_cmp_gt_u32 vcc, 0xff, v34\n\t"
        "s_nop 1\n\t"
        "v_cndmask_b32 v34, v6, v34, vcc\n\t"
        "v_add_u32 v35, s78, v33\n\t"
        "v_add_u32 v6, 0xffffffb0, v35\n\t"
        "v_cmp_gt_u32 vcc, 0xff, v35\n\t"
        "s_nop 1\n\t"
        "v_cndmask_b32 v35, v6, v35, vcc\n\t"
        "v_mov_b32 v12, 0x8000\n\t"
        "v_cndmask_b32 v44, v12, 0, s[32:33]\n\t"
        "v_cndmask_b32 v45, v12, 0, s[34:35]\n\t"
        "v_cndmask_b32 v46, 63, 31, s[36:37]\n\t"
        "v_mov_b32 v47, 63\n\t"
        "s_waitcnt vmcnt(0)\n\t"
        "v_lshlrev_b32 v16, 16, v16\n\t"
        "v_lshlrev_b32 v17, 16, v17\n\t"
        "v_mul_f32 v36, v16, v16\n\t"
        "v_mul_f32 v37, v17, v17\n\t"
        "v_rcp_f32 v36, v36\n\t"
        "v_rcp_f32 v37, v37\n\t"
        "v_or3_b32 v16, v16, v34, v44\n\t"
        "v_or3_b32 v17, v17, v35, v45\n\t"
        "v_add_f32 v36, 0xc0600000, v36\n\t"
        "v_add_f32 v37, 0xc0600000, v37\n\t"
        "v_cvt_u32_f32 v36, v36\n\t"
        "v_cvt_u32_f32 v37, v37\n\t"
        "v_min_u32 v36, 31, v36\n\t"
        "v_min_u32 v37, 31, v37\n\t"
        "v_cndmask_b32 v36, 0, v36, s[32:33]\n\t"
        "v_cndmask_b32 v37, 0, v37, s[34:35]\n\t"
        "v_sub_u32 v36, v46, v36\n\t"
        "v_sub_u32 v37, v47, v37\n\t"
        "v_lshlrev_b32 v36, 2, v36\n\t"
        "v_lshlrev_b32 v37, 2, v37\n\t"
        "ds_add_rtn_u32 v40, v36, v3 offset:256\n\t"
        "s_mov_b64 exec, 0xffff\n\t"
        "ds_add_rtn_u32 v41, v37, v3 offset:256\n\t"
        "s_mov_b64 exec, -1\n\t"
        "v_lshlrev_b32 v9, 2, v1\n\t"
        "ds_read_b32 v10, v9 offset:256\n\t"
        "s_cmp_eq_u32 s70, 0\n\t"
        "s_cselect_b32 s78, 56, 0\n\t"
        "s_cselect_b32 s79, 0, 56\n\t"
        "v_mov_b32 v49, -40\n\t"
        "v_cndmask_b32 v48, v49, 0, s[36:37]\n\t"
        "s_mov_b32 s40, 0x23108954\n\t"
        "s_mov_b32 s41, 0x67\n\t"
        "s_mov_b32 s42, 0x1986732\n\t"
        "s_mov_b32 s43, 0x45\n\t"
        "v_mov_b32 v13, s78\n\t"
        "v_mov_b32 v14, s79\n\t"
        "v_cndmask_b32 v13, v14, v13, s[36:37]\n\t"
        "s_waitcnt lgkmcnt(0)\n\t"
        "v_mov_b32 v11, v10\n\t"
        "s_nop 1\n\t"
        "v_add_u32_dpp v11, v11, v11 row_shr:1 row_mask:0xf bank_mask:0xf bound_ctrl:1\n\t"
        "s_nop 1\n\t"
        "v_add_u32_dpp v11, v11, v11 row_shr:2 row_mask:0xf bank_mask:0xf bound_ctrl:1\n\t"
        "s_nop 1\n\t"
        "v_add_u32_dpp v11, v11, v11 row_shr:4 row_mask:0xf bank_mask:0xf bound_ctrl:1\n\t"
        "s_nop 1\n\t"
        "v_add_u32_dpp v11, v11, v11 row_shr:8 row_mask:0xf bank_mask:0xf bound_ctrl:1\n\t"
        "s_nop 1\n\t"
        "v_add_u32_dpp v11, v11, v11 row_bcast:15 row_mask:0xa bank_mask:0xf\n\t"
        "s_nop 1\n\t"
        "v_add_u32_dpp v11, v11, v11 row_bcast:31 row_mask:0xc bank_mask:0xf\n\t"
        "v_sub_u32 v11, v11, v10\n\t"
        "ds_bpermute_b32 v36, v36, v11\n\t"
        "ds_bpermute_b32 v37, v37, v11\n\t"
        "v_lshlrev_b32 v9, 1, v1\n\t"
        "s_waitcnt lgkmcnt(0)\n\t"
        "v_add3_u32 v36, v36, v40, v48\n\t"
        "v_and_b32 v6, -4, v36\n\t"
        "v_lshrrev_b64 v[10:11], v6, s[40:41]\n\t"
        "v_lshrrev_b64 v[50:51], v6, s[42:43]\n\t"
        "v_cmp_eq_u32 vcc, 0, v13\n\t"
        "s_nop 1\n\t"
        "v_cndmask_b32 v10, v10, v50, vcc\n\t"
        "v_and_b32 v10, 15, v10\n\t"
        "v_and_b32 v6, 3, v36\n\t"
        "v_lshl_add_u32 v6, v10, 2, v6\n\t"
        "v_add_u32 v36, v6, v13\n\t"
        "v_and_b32 v6, 15, v36\n\t"
        "v_lshlrev_b32 v6, 4, v6\n\t"
        "v_lshl_add_u32 v6, v36, 8, v6\n\t"
        "v_and_b32 v10, 8, v13\n\t"
        "v_lshlrev_b32 v10, 8, v10\n\t"
        "v_sub_u32 v6, v6, v10\n\t"
        "v_lshlrev_b32 v36, 2, v36\n\t"
        "ds_write_b32 v36, v16 offset:512\n\t"
        "ds_write_b16 v9, v6 offset:1792\n\t"
        "v_add3_u32 v37, v37, v41, v49\n\t"
        "v_and_b32 v6, -4, v37\n\t"
        "v_lshrrev_b64 v[10:11], v6, s[40:41]\n\t"
        "v_lshrrev_b64 v[50:51], v6, s[42:43]\n\t"
        "v_cmp_eq_u32 vcc, 0, v14\n\t"
        "s_nop 1\n\t"
        "v_cndmask_b32 v10, v10, v50, vcc\n\t"
        "v_and_b32 v10, 15, v10\n\t"
        "v_and_b32 v6, 3, v37\n\t"
        "v_lshl_add_u32 v6, v10, 2, v6\n\t"
        "v_add_u32 v37, v6, v14\n\t"
        "v_and_b32 v6, 15, v37\n\t"
        "v_lshlrev_b32 v6, 4, v6\n\t"
        "v_lshl_add_u32 v6, v37, 8, v6\n\t"
        "v_and_b32 v10, 8, v14\n\t"
        "v_lshlrev_b32 v10, 8, v10\n\t"
        "v_sub_u32 v6, v6, v10\n\t"
        "v_lshlrev_b32 v37, 2, v37\n\t"
        "s_mov_b64 exec, 0xffff\n\t"
        "ds_write_b32 v37, v17 offset:512\n\t"
        "ds_write_b16 v9, v6 offset:1920\n\t"
        "s_mov_b64 exec, 1\n\t"
        "ds_add_u32 v2, v3 offset:8\n\t"
        "s_mov_b64 exec, -1\n"
        ".L7_w48:\n\t"
        "ds_read_b64 v[4:5], v2 offset:72\n\t"
        "s_waitcnt lgkmcnt(0)\n\t"
        "v_cmp_gt_u16 vcc, 2, v4\n\t"
        "s_cbranch_vccz .L7_d49\n\t"
        "s_sleep 2\n\t"
        "s_sub_i32 s83, s83, 1\n\t"
        "s_cmp_gt_i32 s83, 0\n\t"
        "s_cbranch_scc1 .L7_w48\n"
        ".L7_d49:\n\t"
        "v_readfirstlane_b32 s68, v5\n\t"
        "v_lshlrev_b32 v22, 4, v1\n\t"
        "v_add_u32 v9, 142848, v22\n\t"
        "v_and_b32 v10, 60, v1\n\t"
        "ds_read_b32 v24, v10 offset:704\n\t"
        "ds_read_b32 v25, v10 offset:768\n\t"
        "ds_read_b32 v26, v10 offset:832\n\t"
        "v_and_b32 v11, 15, v1\n\t"
        "v_lshrrev_b32 v12, 4, v1\n\t"
        "v_xor_b32 v16, v11, v12\n\t"
        "v_lshlrev_b32 v16, 4, v16\n\t"
        "v_lshl_add_u32 v16, v11, 8, v16\n\t"
        "v_xor_b32 v17, 64, v16\n\t"
        "v_xor_b32 v18, 128, v16\n\t"
        "v_xor_b32 v19, 192, v16\n\t"
        "ds_write_b128 v9, v[16:19]\n\t"
        "s_and_b32 s73, s68, 1\n\t"
        "s_cselect_b32 s73, 1, 0x10000\n\t"
        "v_mov_b32 v20, s73\n\t"
        "v_and_b32 v21, 7, v1\n\t"
        "v_add_u32 v21, 1, v21\n\t"
        "v_lshrrev_b32 v21, v21, s68\n\t"
        "s_mov_b32 s49, 0x10001\n\t"
        "v_and_b32 v21, s49, v21\n\t"
        "v_and_b32 v23, 0x37f, v22\n\t"
        "ds_write_b128 v9, v[20:23] offset:1024\n\t"
        "s_add_u32 s78, s72, s65\n\t"
        "s_sub_u32 s78, s78, 0xff\n\t"
        "s_lshl_b32 s78, s78, 9\n\t"
        "v_and_b32 v13, 48, v22\n\t"
        "v_add_u32 v14, s78, v13\n\t"
        "s_movk_i32 s78, 0x200\n\t"
        "v_bfe_u32 v27, v1, 1, 2\n\t"
        "v_xor_b32 v27, v27, v12\n\t"
        "v_lshlrev_b32 v27, 4, v27\n\t"
        "v_lshl_add_u32 v27, v11, 6, v27\n\t"
        "v_lshlrev_b32 v28, 1, v1\n\t"
        "v_and_b32 v28, 48, v28\n\t"
        "v_xor_b32 v28, v28, v22\n\t"
        "s_waitcnt lgkmcnt(2)\n\t"
        "v_mad_u32_u16 v24, v24, s78, v14\n\t"
        "v_mad_u32_u16 v25, v25, s78, v14\n\t"
        "v_mad_u32_u16 v26, v26, s78, v14\n\t"
        "s_mov_b32 exec_hi, 0\n\t"
        "v_mov_b32 v24, -2.0\n\t"
        "s_mov_b64 exec, -1\n\t"
        "ds_write_b128 v9, v[24:27] offset:2048\n\t"
        "s_mov_b32 s78, 0x3f80\n\t"
        "s_mov_b32 s79, 0\n\t"
        "v_lshlrev_b64 v[30:31], v13, s[78:79]\n\t"
        "s_mov_b32 s78, 0x00f0000f\n\t"
        "s_mov_b32 s79, 0xf0000f00\n\t"
        "v_cndmask_b32 v30, 0, v30, s[78:79]\n\t"
        "v_cndmask_b32 v31, 0, v31, s[78:79]\n\t"
        "ds_write_b128 v9, v[28:31] offset:3072\n\t"
        "s_branch .L7_Q\n"
        ".L7_Q0:\n"
        ".L7_w50:\n\t"
        "ds_read_b64 v[4:5], v2 offset:72\n\t"
        "s_waitcnt lgkmcnt(0)\n\t"
        "v_cmp_gt_u16 vcc, 2, v4\n\t"
        "s_cbranch_vccz .L7_d51\n\t"
        "s_sleep 2\n\t"
        "s_sub_i32 s83, s83, 1\n\t"
        "s_cmp_gt_i32 s83, 0\n\t"
        "s_cbranch_scc1 .L7_w50\n"
        ".L7_d51:\n\t"
        "v_readfirstlane_b32 s68, v5\n"
        ".L7_Q:\n\t"
        "s_setprio 3\n\t"
        "v_and_b32 v5, 7, v1\n\t"
        "v_mul_u32_u24 v5, 0x420, v5\n\t"
        "v_and_b32 v6, 8, v1\n\t"
        "v_lshl_add_u32 v5, v6, 6, v5\n\t"
        "v_and_b32 v6, 48, v1\n\t"
        "v_add_u32 v5, v5, v6\n\t"
        "s_and_b32 s73, s68, 1\n\t"
        "s_cselect_b32 s73, 1, 0x10000\n\t"
        "v_and_b32 v6, 7, v1\n\t"
        "v_add_u32 v6, 1, v6\n\t"
        "v_lshrrev_b32 v6, v6, s68\n\t"
        "s_mov_b32 s49, 0x10001\n\t"
        "v_and_b32 v6, s49, v6\n\t"
        "s_add_u32 s80, s69, s76\n\t"
        "s_and_b32 s80, s80, 3\n\t"
        "s_lshl2_add_u32 s78, s70, s80\n\t"
        "s_lshl_b32 s78, s78, 14\n\t"
        "s_add_u32 s78, s78, 131072\n\t"
        "v_lshl_add_u32 v4, v1, 4, s78\n\t"
        "s_mov_b32 s24, 4096\n\t"
        "s_mov_b32 s25, 8192\n\t"
        "s_mov_b32 s26, 12288\n"
        ".L7_w52:\n\t"
        "ds_read_b32 v0, v2 offset:92\n\t"
        "s_waitcnt lgkmcnt(0)\n\t"
        "v_cmp_gt_u16 vcc, 1, v0\n\t"
        "s_cbranch_vccz .L7_d53\n\t"
        "s_sleep 1\n\t"
        "s_sub_i32 s83, s83, 1\n\t"
        "s_cmp_gt_i32 s83, 0\n\t"
        "s_cbranch_scc1 .L7_w52\n"
        ".L7_d53:\n\t"
        "v_readfirstlane_b32 s78, v0\n\t"
        "s_sub_u32 s78, s78, 1\n\t"
        "s_add_u32 s77, s74, s78\n"
        ".L7_g54:\n\t"
        "s_memrealtime s[78:79]\n\t"
        "s_waitcnt lgkmcnt(0)\n\t"
        "s_sub_u32 s78, s78, s77\n\t"
        "s_cmp_ge_i32 s78, 280\n\t"
        "s_cbranch_scc1 .L7_h55\n\t"
        "s_sleep 1\n\t"
        "s_branch .L7_g54\n"
        ".L7_h55:\n"
        ".L7_w56:\n\t"
        "ds_read_b32 v0, v2 offset:88\n\t"
        "s_waitcnt lgkmcnt(0)\n\t"
        "v_cmp_gt_u16 vcc, 8, v0\n\t"
        "s_cbranch_vccz .L7_d57\n\t"
        "s_sleep 1\n\t"
        "s_sub_i32 s83, s83, 1\n\t"
        "s_cmp_gt_i32 s83, 0\n\t"
        "s_cbranch_scc1 .L7_w56\n"
        ".L7_d57:\n\t"
        "buffer_load_dwordx4 v[64:67], v4, s[20:23], 0 offen offset:0 sc1\n\t"
        "buffer_load_dwordx4 v[68:71], v4, s[20:23], 0 offen offset:1024 sc1\n\t"
        "buffer_load_dwordx4 v[72:75], v4, s[20:23], 0 offen offset:2048 sc1\n\t"
        "buffer_load_dwordx4 v[76:79], v4, s[20:23], 0 offen offset:3072 sc1\n\t"
        "buffer_load_dwordx4 v[80:83], v4, s[20:23], s24 offen offset:0 sc1\n\t"
        "buffer_load_dwordx4 v[84:87], v4, s[20:23], s24 offen offset:1024 sc1\n\t"
        "buffer_load_dwordx4 v[88:91], v4, s[20:23], s24 offen offset:2048 sc1\n\t"
        "buffer_load_dwordx4 v[92:95], v4, s[20:23], s24 offen offset:3072 sc1\n\t"
        "buffer_load_dwordx4 v[96:99], v4, s[20:23], s25 offen offset:0 sc1\n\t"
        "buffer_load_dwordx4 v[100:103], v4, s[20:23], s25 offen offset:1024 sc1\n\t"
        "buffer_load_dwordx4 v[104:107], v4, s[20:23], s25 offen offset:2048 sc1\n\t"
        "buffer_load_dwordx4 v[108:111], v4, s[20:23], s25 offen offset:3072 sc1\n\t"
        "buffer_load_dwordx4 v[112:115], v4, s[20:23], s26 offen offset:0 sc1\n\t"
        "buffer_load_dwordx4 v[116:119], v4, s[20:23], s26 offen offset:1024 sc1\n\t"
        "buffer_load_dwordx4 v[120:123], v4, s[20:23], s26 offen offset:2048 sc1\n\t"
        "buffer_load_dwordx4 v[124:127], v4, s[20:23], s26 offen offset:3072 sc1\n\t"
        "s_mov_b64 exec, 1\n\t"
        "ds_add_u32 v2, v3 offset:84\n\t"
        "s_mov_b64 exec, -1\n"
        ".L7_w58:\n\t"
        "ds_read_b32 v0, v2 offset:0\n\t"
        "s_waitcnt lgkmcnt(0)\n\t"
        "v_cmp_gt_u16 vcc, 4, v0\n\t"
        "s_cbranch_vccz .L7_d59\n\t"
        "s_sleep 1\n\t"
        "s_sub_i32 s83, s83, 1\n\t"
        "s_cmp_gt_i32 s83, 0\n\t"
        "s_cbranch_scc1 .L7_w58\n"
        ".L7_d59:\n\t"
        "s_branch .L7_qg\n"
        ".L7_qr:\n\t"
        "s_waitcnt lgkmcnt(0)\n\t"
        "s_sleep 8\n\t"
        "s_sub_i32 s83, s83, 64\n\t"
        "s_cmp_gt_i32 s83, 0\n\t"
        "s_cbranch_scc0 .L7_qd\n\t"
        "buffer_load_dwordx4 v[64:67], v4, s[20:23], 0 offen offset:0 sc1\n\t"
        "buffer_load_dwordx4 v[68:71], v4, s[20:23], 0 offen offset:1024 sc1\n\t"
        "buffer_load_dwordx4 v[72:75], v4, s[20:23], 0 offen offset:2048 sc1\n\t"
        "buffer_load_dwordx4 v[76:79], v4, s[20:23], 0 offen offset:3072 sc1\n\t"
        "buffer_load_dwordx4 v[80:83], v4, s[20:23], s24 offen offset:0 sc1\n\t"
        "buffer_load_dwordx4 v[84:87], v4, s[20:23], s24 offen offset:1024 sc1\n\t"
        "buffer_load_dwordx4 v[88:91], v4, s[20:23], s24 offen offset:2048 sc1\n\t"
        "buffer_load_dwordx4 v[92:95], v4, s[20:23], s24 offen offset:3072 sc1\n\t"
        "buffer_load_dwordx4 v[96:99], v4, s[20:23], s25 offen offset:0 sc1\n\t"
        "buffer_load_dwordx4 v[100:103], v4, s[20:23], s25 offen offset:1024 sc1\n\t"
        "buffer_load_dwordx4 v[104:107], v4, s[20:23], s25 offen offset:2048 sc1\n\t"
        "buffer_load_dwordx4 v[108:111], v4, s[20:23], s25 offen offset:3072 sc1\n\t"
        "buffer_load_dwordx4 v[112:115], v4, s[20:23], s26 offen offset:0 sc1\n\t"
        "buffer_load_dwordx4 v[116:119], v4, s[20:23], s26 offen offset:1024 sc1\n\t"
        "buffer_load_dwordx4 v[120:123], v4, s[20:23], s26 offen offset:2048 sc1\n\t"
        "buffer_load_dwordx4 v[124:127], v4, s[20:23], s26 offen offset:3072 sc1\n"
        ".L7_qg:\n\t"
        "v_mov_b32 v7, 0\n\t"
        "ds_read_b128 v[8:11], v5 offset:2048\n\t"
        "ds_read_b128 v[12:15], v5 offset:10496\n\t"
        "ds_read_b128 v[16:19], v5 offset:18944\n\t"
        "ds_read_b128 v[20:23], v5 offset:27392\n\t"
        "s_waitcnt vmcnt(14) lgkmcnt(3)\n\t"
        "v_xor_b32 v64, s73, v64\n\t"
        "v_xor_b32 v65, v6, v65\n\t"
        "v_xor_b32 v68, s73, v68\n\t"
        "v_xor_b32 v69, v6, v69\n\t"
        "v_mfma_f32_16x16x32_bf16 v[24:27], v[64:67], v[8:11], 0\n\t"
        "s_nop 0\n\t"
        "v_mfma_f32_16x16x32_bf16 v[28:31], v[68:71], v[8:11], 0\n\t"
        "v_or3_b32 v7, v7, v64, v65\n\t"
        "ds_read_b128 v[8:11], v5 offset:35840\n\t"
        "s_waitcnt lgkmcnt(3)\n\t"
        "v_mfma_f32_16x16x32_bf16 v[32:35], v[64:67], v[12:15], 0\n\t"
        "v_mfma_f32_16x16x32_bf16 v[36:39], v[68:71], v[12:15], 0\n\t"
        "v_or3_b32 v7, v7, v68, v69\n\t"
        "ds_read_b128 v[12:15], v5 offset:2112\n\t"
        "s_waitcnt lgkmcnt(3)\n\t"
        "v_mfma_f32_16x16x32_bf16 v[40:43], v[64:67], v[16:19], 0\n\t"
        "v_mfma_f32_16x16x32_bf16 v[44:47], v[68:71], v[16:19], 0\n\t"
        "ds_read_b128 v[16:19], v5 offset:10560\n\t"
        "s_waitcnt lgkmcnt(3)\n\t"
        "v_mfma_f32_16x16x32_bf16 v[48:51], v[64:67], v[20:23], 0\n\t"
        "v_mfma_f32_16x16x32_bf16 v[52:55], v[68:71], v[20:23], 0\n\t"
        "ds_read_b128 v[20:23], v5 offset:19008\n\t"
        "s_waitcnt vmcnt(12) lgkmcnt(3)\n\t"
        "v_xor_b32 v72, s73, v72\n\t"
        "v_xor_b32 v73, v6, v73\n\t"
        "v_xor_b32 v76, s73, v76\n\t"
        "v_xor_b32 v77, v6, v77\n\t"
        "v_mfma_f32_16x16x32_bf16 v[56:59], v[64:67], v[8:11], 0\n\t"
        "v_mfma_f32_16x16x32_bf16 v[60:63], v[68:71], v[8:11], 0\n\t"
        "ds_read_b128 v[8:11], v5 offset:27456\n\t"
        "ds_read_b128 v[64:67], v5 offset:35904\n\t"
        "ds_read_b128 v[68:71], v5 offset:2176\n\t"
        "s_waitcnt lgkmcnt(4)\n\t"
        "v_mfma_f32_16x16x32_bf16 v[24:27], v[72:75], v[12:15], v[24:27]\n\t"
        "v_mfma_f32_16x16x32_bf16 v[28:31], v[76:79], v[12:15], v[28:31]\n\t"
        "v_or3_b32 v7, v7, v72, v73\n\t"
        "ds_read_b128 v[12:15], v5 offset:10624\n\t"
        "v_mfma_f32_16x16x32_bf16 v[32:35], v[72:75], v[16:19], v[32:35]\n\t"
        "v_mfma_f32_16x16x32_bf16 v[36:39], v[76:79], v[16:19], v[36:39]\n\t"
        "v_or3_b32 v7, v7, v76, v77\n\t"
        "ds_read_b128 v[16:19], v5 offset:19072\n\t"
        "s_waitcnt lgkmcnt(4)\n\t"
        "v_mfma_f32_16x16x32_bf16 v[40:43], v[72:75], v[20:23], v[40:43]\n\t"
        "v_mfma_f32_16x16x32_bf16 v[44:47], v[76:79], v[20:23], v[44:47]\n\t"
        "ds_read_b128 v[20:23], v5 offset:27520\n\t"
        "v_mfma_f32_16x16x32_bf16 v[48:51], v[72:75], v[8:11], v[48:51]\n\t"
        "v_mfma_f32_16x16x32_bf16 v[52:55], v[76:79], v[8:11], v[52:55]\n\t"
        "ds_read_b128 v[8:11], v5 offset:35968\n\t"
        "s_waitcnt vmcnt(10) lgkmcnt(4)\n\t"
        "v_xor_b32 v80, s73, v80\n\t"
        "v_xor_b32 v81, v6, v81\n\t"
        "v_xor_b32 v84, s73, v84\n\t"
        "v_xor_b32 v85, v6, v85\n\t"
        "v_mfma_f32_16x16x32_bf16 v[56:59], v[72:75], v[64:67], v[56:59]\n\t"
        "v_mfma_f32_16x16x32_bf16 v[60:63], v[76:79], v[64:67], v[60:63]\n\t"
        "ds_read_b128 v[64:67], v5 offset:2240\n\t"
        "ds_read_b128 v[72:75], v5 offset:10688\n\t"
        "ds_read_b128 v[76:79], v5 offset:19136\n\t"
        "v_mfma_f32_16x16x32_bf16 v[24:27], v[80:83], v[68:71], v[24:27]\n\t"
        "v_mfma_f32_16x16x32_bf16 v[28:31], v[84:87], v[68:71], v[28:31]\n\t"
        "v_or3_b32 v7, v7, v80, v81\n\t"
        "ds_read_b128 v[68:71], v5 offset:27584\n\t"
        "s_waitcnt lgkmcnt(6)\n\t"
        "v_mfma_f32_16x16x32_bf16 v[32:35], v[80:83], v[12:15], v[32:35]\n\t"
        "v_mfma_f32_16x16x32_bf16 v[36:39], v[84:87], v[12:15], v[36:39]\n\t"
        "v_or3_b32 v7, v7, v84, v85\n\t"
        "ds_read_b128 v[12:15], v5 offset:36032\n\t"
        "v_mfma_f32_16x16x32_bf16 v[40:43], v[80:83], v[16:19], v[40:43]\n\t"
        "v_mfma_f32_16x16x32_bf16 v[44:47], v[84:87], v[16:19], v[44:47]\n\t"
        "ds_read_b128 v[16:19], v5 offset:2304\n\t"
        "s_waitcnt lgkmcnt(6)\n\t"
        "v_mfma_f32_16x16x32_bf16 v[48:51], v[80:83], v[20:23], v[48:51]\n\t"
        "v_mfma_f32_16x16x32_bf16 v[52:55], v[84:87], v[20:23], v[52:55]\n\t"
        "ds_read_b128 v[20:23], v5 offset:10752\n\t"
        "s_waitcnt vmcnt(8) lgkmcnt(6)\n\t"
        "v_xor_b32 v88, s73, v88\n\t"
        "v_xor_b32 v89, v6, v89\n\t"
        "v_xor_b32 v92, s73, v92\n\t"
        "v_xor_b32 v93, v6, v93\n\t"
        "v_mfma_f32_16x16x32_bf16 v[56:59], v[80:83], v[8:11], v[56:59]\n\t"
        "v_mfma_f32_16x16x32_bf16 v[60:63], v[84:87], v[8:11], v[60:63]\n\t"
        "ds_read_b128 v[8:11], v5 offset:19200\n\t"
        "ds_read_b128 v[80:83], v5 offset:27648\n\t"
        "v_mfma_f32_16x16x32_bf16 v[24:27], v[88:91], v[64:67], v[24:27]\n\t"
        "v_mfma_f32_16x16x32_bf16 v[28:31], v[92:95], v[64:67], v[28:31]\n\t"
        "v_or3_b32 v7, v7, v88, v89\n\t"
        "ds_read_b128 v[84:87], v5 offset:36096\n\t"
        "s_waitcnt lgkmcnt(7)\n\t"
        "v_mfma_f32_16x16x32_bf16 v[32:35], v[88:91], v[72:75], v[32:35]\n\t"
        "v_mfma_f32_16x16x32_bf16 v[36:39], v[92:95], v[72:75], v[36:39]\n\t"
        "v_or3_b32 v7, v7, v92, v93\n\t"
        "ds_read_b128 v[64:67], v5 offset:2368\n\t"
        "v_mfma_f32_16x16x32_bf16 v[40:43], v[88:91], v[76:79], v[40:43]\n\t"
        "v_mfma_f32_16x16x32_bf16 v[44:47], v[92:95], v[76:79], v[44:47]\n\t"
        "ds_read_b128 v[72:75], v5 offset:10816\n\t"
        "s_waitcnt lgkmcnt(7)\n\t"
        "v_mfma_f32_16x16x32_bf16 v[48:51], v[88:91], v[68:71], v[48:51]\n\t"
        "v_mfma_f32_16x16x32_bf16 v[52:55], v[92:95], v[68:71], v[52:55]\n\t"
        "ds_read_b128 v[76:79], v5 offset:19264\n\t"
        "s_waitcnt vmcnt(6) lgkmcnt(7)\n\t"
        "v_xor_b32 v96, s73, v96\n\t"
        "v_xor_b32 v97, v6, v97\n\t"
        "v_xor_b32 v100, s73, v100\n\t"
        "v_xor_b32 v101, v6, v101\n\t"
        "v_mfma_f32_16x16x32_bf16 v[56:59], v[88:91], v[12:15], v[56:59]\n\t"
        "v_mfma_f32_16x16x32_bf16 v[60:63], v[92:95], v[12:15], v[60:63]\n\t"
        "ds_read_b128 v[68:71], v5 offset:27712\n\t"
        "v_mfma_f32_16x16x32_bf16 v[24:27], v[96:99], v[16:19], v[24:27]\n\t"
        "v_mfma_f32_16x16x32_bf16 v[28:31], v[100:103], v[16:19], v[28:31]\n\t"
        "v_or3_b32 v7, v7, v96, v97\n\t"
        "ds_read_b128 v[12:15], v5 offset:36160\n\t"
        "s_waitcnt lgkmcnt(7)\n\t"
        "v_mfma_f32_16x16x32_bf16 v[32:35], v[96:99], v[20:23], v[32:35]\n\t"
        "v_mfma_f32_16x16x32_bf16 v[36:39], v[100:103], v[20:23], v[36:39]\n\t"
        "v_or3_b32 v7, v7, v100, v101\n\t"
        "ds_read_b128 v[88:91], v5 offset:2432\n\t"
        "v_mfma_f32_16x16x32_bf16 v[40:43], v[96:99], v[8:11], v[40:43]\n\t"
        "v_mfma_f32_16x16x32_bf16 v[44:47], v[100:103], v[8:11], v[44:47]\n\t"
        "ds_read_b128 v[92:95], v5 offset:10880\n\t"
        "s_waitcnt lgkmcnt(7)\n\t"
        "v_mfma_f32_16x16x32_bf16 v[48:51], v[96:99], v[80:83], v[48:51]\n\t"
        "v_mfma_f32_16x16x32_bf16 v[52:55], v[100:103], v[80:83], v[52:55]\n\t"
        "ds_read_b128 v[16:19], v5 offset:19328\n\t"
        "s_waitcnt vmcnt(4) lgkmcnt(7)\n\t"
        "v_xor_b32 v104, s73, v104\n\t"
        "v_xor_b32 v105, v6, v105\n\t"
        "v_xor_b32 v108, s73, v108\n\t"
        "v_xor_b32 v109, v6, v109\n\t"
        "v_mfma_f32_16x16x32_bf16 v[56:59], v[96:99], v[84:87], v[56:59]\n\t"
        "v_mfma_f32_16x16x32_bf16 v[60:63], v[100:103], v[84:87], v[60:63]\n\t"
        "ds_read_b128 v[20:23], v5 offset:27776\n\t"
        "v_mfma_f32_16x16x32_bf16 v[24:27], v[104:107], v[64:67], v[24:27]\n\t"
        "v_mfma_f32_16x16x32_bf16 v[28:31], v[108:111], v[64:67], v[28:31]\n\t"
        "v_or3_b32 v7, v7, v104, v105\n\t"
        "ds_read_b128 v[8:11], v5 offset:36224\n\t"
        "s_waitcnt lgkmcnt(7)\n\t"
        "v_mfma_f32_16x16x32_bf16 v[32:35], v[104:107], v[72:75], v[32:35]\n\t"
        "v_mfma_f32_16x16x32_bf16 v[36:39], v[108:111], v[72:75], v[36:39]\n\t"
        "v_or3_b32 v7, v7, v108, v109\n\t"
        "ds_read_b128 v[80:83], v5 offset:2496\n\t"
        "v_mfma_f32_16x16x32_bf16 v[40:43], v[104:107], v[76:79], v[40:43]\n\t"
        "v_mfma_f32_16x16x32_bf16 v[44:47], v[108:111], v[76:79], v[44:47]\n\t"
        "ds_read_b128 v[84:87], v5 offset:10944\n\t"
        "s_waitcnt lgkmcnt(7)\n\t"
        "v_mfma_f32_16x16x32_bf16 v[48:51], v[104:107], v[68:71], v[48:51]\n\t"
        "v_mfma_f32_16x16x32_bf16 v[52:55], v[108:111], v[68:71], v[52:55]\n\t"
        "ds_read_b128 v[96:99], v5 offset:19392\n\t"
        "s_waitcnt vmcnt(2) lgkmcnt(7)\n\t"
        "v_xor_b32 v112, s73, v112\n\t"
        "v_xor_b32 v113, v6, v113\n\t"
        "v_xor_b32 v116, s73, v116\n\t"
        "v_xor_b32 v117, v6, v117\n\t"
        "v_mfma_f32_16x16x32_bf16 v[56:59], v[104:107], v[12:15], v[56:59]\n\t"
        "v_mfma_f32_16x16x32_bf16 v[60:63], v[108:111], v[12:15], v[60:63]\n\t"
        "ds_read_b128 v[100:103], v5 offset:27840\n\t"
        "v_mfma_f32_16x16x32_bf16 v[24:27], v[112:115], v[88:91], v[24:27]\n\t"
        "v_mfma_f32_16x16x32_bf16 v[28:31], v[116:119], v[88:91], v[28:31]\n\t"
        "v_or3_b32 v7, v7, v112, v113\n\t"
        "ds_read_b128 v[64:67], v5 offset:36288\n\t"
        "s_waitcnt lgkmcnt(7)\n\t"
        "v_mfma_f32_16x16x32_bf16 v[32:35], v[112:115], v[92:95], v[32:35]\n\t"
        "v_mfma_f32_16x16x32_bf16 v[36:39], v[116:119], v[92:95], v[36:39]\n\t"
        "v_or3_b32 v7, v7, v116, v117\n\t"
        "v_mfma_f32_16x16x32_bf16 v[40:43], v[112:115], v[16:19], v[40:43]\n\t"
        "v_mfma_f32_16x16x32_bf16 v[44:47], v[116:119], v[16:19], v[44:47]\n\t"
        "s_waitcnt lgkmcnt(5)\n\t"
        "v_mfma_f32_16x16x32_bf16 v[48:51], v[112:115], v[20:23], v[48:51]\n\t"
        "v_mfma_f32_16x16x32_bf16 v[52:55], v[116:119], v[20:23], v[52:55]\n\t"
        "s_waitcnt vmcnt(0) lgkmcnt(4)\n\t"
        "v_xor_b32 v120, s73, v120\n\t"
        "v_xor_b32 v121, v6, v121\n\t"
        "v_xor_b32 v124, s73, v124\n\t"
        "v_xor_b32 v125, v6, v125\n\t"
        "v_mfma_f32_16x16x32_bf16 v[56:59], v[112:115], v[8:11], v[56:59]\n\t"
        "v_mfma_f32_16x16x32_bf16 v[60:63], v[116:119], v[8:11], v[60:63]\n\t"
        "v_mfma_f32_16x16x32_bf16 v[24:27], v[120:123], v[80:83], v[24:27]\n\t"
        "v_mfma_f32_16x16x32_bf16 v[28:31], v[124:127], v[80:83], v[28:31]\n\t"
        "v_or3_b32 v7, v7, v120, v121\n\t"
        "s_waitcnt lgkmcnt(3)\n\t"
        "v_mfma_f32_16x16x32_bf16 v[32:35], v[120:123], v[84:87], v[32:35]\n\t"
        "v_mfma_f32_16x16x32_bf16 v[36:39], v[124:127], v[84:87], v[36:39]\n\t"
        "v_or3_b32 v7, v7, v124, v125\n\t"
        "v_and_b32 v7, s49, v7\n\t"
        "v_cmp_ne_u32 vcc, 0, v7\n\t"
        "s_cbranch_vccnz .L7_qr\n"
        ".L7_qd:\n\t"
        "s_mov_b64 exec, 1\n\t"
        "ds_add_u32 v2, v3 offset:12\n\t"
        "s_mov_b64 exec, -1\n\t"
        "v_and_b32 v6, 15, v1\n\t"
        "v_mul_u32_u24 v6, 272, v6\n\t"
        "v_and_b32 v7, 48, v1\n\t"
        "s_lshl_b32 s78, s80, 6\n\t"
        "v_add3_u32 v6, v6, v7, s78\n\t"
        "s_waitcnt lgkmcnt(2)\n\t"
        "v_mfma_f32_16x16x32_bf16 v[40:43], v[120:123], v[96:99], v[40:43]\n\t"
        "v_mfma_f32_16x16x32_bf16 v[44:47], v[124:127], v[96:99], v[44:47]\n\t"
        "v_cvt_pk_bf16_f32 v24, v24, v25\n\t"
        "v_cvt_pk_bf16_f32 v25, v26, v27\n\t"
        "v_cvt_pk_bf16_f32 v26, v28, v29\n\t"
        "v_cvt_pk_bf16_f32 v27, v30, v31\n\t"
        "s_waitcnt lgkmcnt(0)\n\t"
        "v_mfma_f32_16x16x32_bf16 v[48:51], v[120:123], v[100:103], v[48:51]\n\t"
        "v_mfma_f32_16x16x32_bf16 v[52:55], v[124:127], v[100:103], v[52:55]\n\t"
        "v_cvt_pk_bf16_f32 v32, v32, v33\n\t"
        "v_cvt_pk_bf16_f32 v33, v34, v35\n\t"
        "v_cvt_pk_bf16_f32 v34, v36, v37\n\t"
        "v_cvt_pk_bf16_f32 v35, v38, v39\n\t"
        "v_mfma_f32_16x16x32_bf16 v[56:59], v[120:123], v[64:67], v[56:59]\n\t"
        "v_mfma_f32_16x16x32_bf16 v[60:63], v[124:127], v[64:67], v[60:63]\n\t"
        "v_cvt_pk_bf16_f32 v40, v40, v41\n\t"
        "v_cvt_pk_bf16_f32 v41, v42, v43\n\t"
        "v_cvt_pk_bf16_f32 v42, v44, v45\n\t"
        "v_cvt_pk_bf16_f32 v43, v46, v47\n\t"
        "ds_read_b32 v0, v2 offset:12\n\t"
        "v_cvt_pk_bf16_f32 v48, v48, v49\n\t"
        "v_cvt_pk_bf16_f32 v49, v50, v51\n\t"
        "v_cvt_pk_bf16_f32 v50, v52, v53\n\t"
        "v_cvt_pk_bf16_f32 v51, v54, v55\n\t"
        "v_cvt_pk_bf16_f32 v56, v56, v57\n\t"
        "v_cvt_pk_bf16_f32 v57, v58, v59\n\t"
        "v_cvt_pk_bf16_f32 v58, v60, v61\n\t"
        "v_cvt_pk_bf16_f32 v59, v62, v63\n\t"
        "s_waitcnt lgkmcnt(0)\n\t"
        "v_cmp_gt_u16 vcc, 4, v0\n\t"
        "s_cbranch_vccz .L7_qx\n"
        ".L7_w60:\n\t"
        "ds_read_b32 v0, v2 offset:12\n\t"
        "s_waitcnt lgkmcnt(0)\n\t"
        "v_cmp_gt_u16 vcc, 4, v0\n\t"
        "s_cbranch_vccz .L7_d61\n\t"
        "s_sleep 0\n\t"
        "s_sub_i32 s83, s83, 1\n\t"
        "s_cmp_gt_i32 s83, 0\n\t"
        "s_cbranch_scc1 .L7_w60\n"
        ".L7_d61:\n"
        ".L7_qx:\n\t"
        "ds_write_b128 v6, v[24:27] offset:2048\n\t"
        "ds_write_b128 v6, v[32:35] offset:6400\n\t"
        "ds_write_b128 v6, v[40:43] offset:10752\n\t"
        "ds_write_b128 v6, v[48:51] offset:15104\n\t"
        "ds_write_b128 v6, v[56:59] offset:19456\n\t"
        "s_lshl_b32 s78, s69, 9\n\t"
        "v_lshl_add_u32 v7, v1, 3, s78\n\t"
        "v_mov_b64 v[8:9], 0\n\t"
        "ds_write_b64 v7, v[8:9] offset:34048\n\t"
        "s_mov_b64 exec, 1\n\t"
        "ds_add_u32 v2, v3 offset:16\n\t"
        "s_mov_b64 exec, -1\n\t"
        "v_lshrrev_b32 v10, 3, v1\n\t"
        "v_lshlrev_b32 v10, 9, v10\n\t"
        "v_and_b32 v11, 7, v1\n\t"
        "v_lshl_add_u32 v10, v11, 4, v10\n\t"
        "v_and_b32 v11, 16, v1\n\t"
        "v_lshlrev_b32 v11, 2, v11\n\t"
        "v_xor_b32 v10, v10, v11\n\t"
        "s_load_dwordx4 s[24:27], %2, 32\n\t"
        "s_lshl_b32 s78, s80, 14\n\t"
        "s_add_u32 m0, s78, 68864\n\t"
        "s_lshl_b32 s78, s70, 9\n\t"
        "s_add_u32 s78, s78, s80\n\t"
        "s_lshl_b32 s78, s78, 7\n"
        ".L7_w62:\n\t"
        "ds_read_b32 v0, v2 offset:24\n\t"
        "s_waitcnt lgkmcnt(0)\n\t"
        "v_cmp_gt_u16 vcc, 8, v0\n\t"
        "s_cbranch_vccz .L7_d63\n\t"
        "s_sleep 1\n\t"
        "s_sub_i32 s83, s83, 1\n\t"
        "s_cmp_gt_i32 s83, 0\n\t"
        "s_cbranch_scc1 .L7_w62\n"
        ".L7_d63:\n"
        ".L7_w64:\n\t"
        "ds_read_b32 v0, v2 offset:88\n\t"
        "s_waitcnt lgkmcnt(0)\n\t"
        "v_cmp_gt_u16 vcc, 16, v0\n\t"
        "s_cbranch_vccz .L7_d65\n\t"
        "s_sleep 1\n\t"
        "s_sub_i32 s83, s83, 1\n\t"
        "s_cmp_gt_i32 s83, 0\n\t"
        "s_cbranch_scc1 .L7_w64\n"
        ".L7_d65:\n\t"
        "s_setprio 3\n\t"
        "buffer_load_dwordx4 v10, s[24:27], s78 offen lds\n\t"
        "s_add_u32 m0, m0, 0x400\n\t"
        "s_add_u32 s78, s78, 0x1000\n\t"
        "buffer_load_dwordx4 v10, s[24:27], s78 offen lds\n\t"
        "s_add_u32 m0, m0, 0x400\n\t"
        "s_add_u32 s78, s78, 0x1000\n\t"
        "buffer_load_dwordx4 v10, s[24:27], s78 offen lds\n\t"
        "s_add_u32 m0, m0, 0x400\n\t"
        "s_add_u32 s78, s78, 0x1000\n\t"
        "buffer_load_dwordx4 v10, s[24:27], s78 offen lds\n\t"
        "s_add_u32 m0, m0, 0x400\n\t"
        "s_add_u32 s78, s78, 0x1000\n\t"
        "buffer_load_dwordx4 v10, s[24:27], s78 offen lds\n\t"
        "s_add_u32 m0, m0, 0x400\n\t"
        "s_add_u32 s78, s78, 0x1000\n\t"
        "buffer_load_dwordx4 v10, s[24:27], s78 offen lds\n\t"
        "s_add_u32 m0, m0, 0x400\n\t"
        "s_add_u32 s78, s78, 0x1000\n\t"
        "buffer_load_dwordx4 v10, s[24:27], s78 offen lds\n\t"
        "s_add_u32 m0, m0, 0x400\n\t"
        "s_add_u32 s78, s78, 0x1000\n\t"
        "buffer_load_dwordx4 v10, s[24:27], s78 offen lds\n\t"
        "s_add_u32 m0, m0, 0x400\n\t"
        "s_add_u32 s78, s78, 0x1000\n\t"
        "buffer_load_dwordx4 v10, s[24:27], s78 offen lds\n\t"
        "s_add_u32 m0, m0, 0x400\n\t"
        "s_add_u32 s78, s78, 0x1000\n\t"
        "buffer_load_dwordx4 v10, s[24:27], s78 offen lds\n\t"
        "s_add_u32 m0, m0, 0x400\n\t"
        "s_add_u32 s78, s78, 0x1000\n\t"
        "buffer_load_dwordx4 v10, s[24:27], s78 offen lds\n\t"
        "s_add_u32 m0, m0, 0x400\n\t"
        "s_add_u32 s78, s78, 0x1000\n\t"
        "buffer_load_dwordx4 v10, s[24:27], s78 offen lds\n\t"
        "s_add_u32 m0, m0, 0x400\n\t"
        "s_add_u32 s78, s78, 0x1000\n\t"
        "buffer_load_dwordx4 v10, s[24:27], s78 offen lds\n\t"
        "s_add_u32 m0, m0, 0x400\n\t"
        "s_add_u32 s78, s78, 0x1000\n\t"
        "buffer_load_dwordx4 v10, s[24:27], s78 offen lds\n\t"
        "s_add_u32 m0, m0, 0x400\n\t"
        "s_add_u32 s78, s78, 0x1000\n\t"
        "buffer_load_dwordx4 v10, s[24:27], s78 offen lds\n\t"
        "s_add_u32 m0, m0, 0x400\n\t"
        "s_add_u32 s78, s78, 0x1000\n\t"
        "buffer_load_dwordx4 v10, s[24:27], s78 offen lds\n\t"
        "s_waitcnt vmcnt(0)\n"
        ".L7_vw:\n\t"
        "s_mov_b64 exec, 1\n\t"
        "ds_add_u32 v2, v3 offset:28\n\t"
        "s_mov_b64 exec, -1\n\t"
        "s_setprio 0\n"
        ".L7_w66:\n\t"
        "ds_read_b32 v0, v2 offset:36\n\t"
        "s_waitcnt lgkmcnt(0)\n\t"
        "v_cmp_gt_u16 vcc, 4, v0\n\t"
        "s_cbranch_vccz .L7_d67\n\t"
        "s_sleep 2\n\t"
        "s_sub_i32 s83, s83, 1\n\t"
        "s_cmp_gt_i32 s83, 0\n\t"
        "s_cbranch_scc1 .L7_w66\n"
        ".L7_d67:\n\t"
        "v_cmp_lt_u32 vcc, 0xfffff, v0\n\t"
        "s_cbranch_vccnz .L7_exh\n\t"
        "s_cmp_eq_u32 s69, 0\n\t"
        "s_cbranch_scc0 .L7_hz\n\t"
        "s_lshr_b32 s47, s68, 20\n\t"
        "s_add_u32 s47, s47, s68\n\t"
        "s_and_b32 s47, s47, 0xfff\n\t"
        "s_mul_i32 s46, s68, 0x9e377000\n\t"
        "s_or_b32 s46, s46, s47\n\t"
        "v_mov_b32 v4, s46\n\t"
        "s_xor_b32 s44, s70, 1\n\t"
        "s_lshl1_add_u32 s44, %3, s44\n\t"
        "s_lshl_b32 s44, s44, 10\n\t"
        "s_add_u32 s44, s44, 262144\n"
        ".L7_w68:\n\t"
        "ds_read_b32 v0, v2 offset:116\n\t"
        "s_waitcnt lgkmcnt(0)\n\t"
        "v_cmp_gt_u16 vcc, 1, v0\n\t"
        "s_cbranch_vccz .L7_d69\n\t"
        "s_sleep 1\n\t"
        "s_sub_i32 s83, s83, 1\n\t"
        "s_cmp_gt_i32 s83, 0\n\t"
        "s_cbranch_scc1 .L7_w68\n"
        ".L7_d69:\n\t"
        "v_readfirstlane_b32 s78, v0\n\t"
        "s_lshr_b32 s78, s78, 16\n\t"
        "s_cmp_gt_u32 s78, 16\n\t"
        "s_cselect_b32 s45, 0x2000, 0\n\t"
        "s_xor_b32 s44, s44, s45\n\t"
        "s_xor_b32 s45, s44, 0xc00\n"
        ".L7_w70:\n\t"
        "ds_read_b32 v0, v2 offset:56\n\t"
        "s_waitcnt lgkmcnt(0)\n\t"
        "v_cmp_gt_u16 vcc, 3, v0\n\t"
        "s_cbranch_vccz .L7_d71\n\t"
        "s_sleep 8\n\t"
        "s_sub_i32 s83, s83, 1\n\t"
        "s_cmp_gt_i32 s83, 0\n\t"
        "s_cbranch_scc1 .L7_w70\n"
        ".L7_d71:\n\t"
        "s_setprio 3\n"
        ".L7_w72:\n\t"
        "ds_read_b32 v0, v2 offset:96\n\t"
        "s_waitcnt lgkmcnt(0)\n\t"
        "v_cmp_gt_u16 vcc, 7, v0\n\t"
        "s_cbranch_vccz .L7_d73\n\t"
        "s_sleep 1\n\t"
        "s_sub_i32 s83, s83, 1\n\t"
        "s_cmp_gt_i32 s83, 0\n\t"
        "s_cbranch_scc1 .L7_w72\n"
        ".L7_d73:\n\t"
        "s_mov_b64 exec, 1\n\t"
        "buffer_store_dword v4, off, s[20:23], s44 offset:12 sc1\n\t"
        "s_mov_b32 s83, 64\n\t"
        "s_sleep 4\n"
        ".L7_h1:\n\t"
        "s_buffer_load_dword s47, s[20:23], s45 offset:12 glc\n\t"
        "s_waitcnt lgkmcnt(0)\n\t"
        "s_cmp_eq_u32 s47, s46\n\t"
        "s_cbranch_scc1 .L7_h2\n\t"
        "s_sleep 3\n\t"
        "s_sub_i32 s83, s83, 1\n\t"
        "s_cmp_gt_i32 s83, 0\n\t"
        "s_cbranch_scc1 .L7_h1\n"
        ".L7_h2:\n\t"
        "s_mov_b64 exec, 1\n\t"
        "ds_add_u32 v2, v3 offset:100\n\t"
        "s_mov_b64 exec, -1\n"
        ".L7_hz:\n\t"
        "s_endpgm\n"
        ".L7_exh:\n\t"
        "s_mov_b64 exec, 1\n\t"
        "ds_add_u32 v2, v3 offset:100\n\t"
        "s_mov_b64 exec, -1\n\t"
        "s_branch .L7_exit\n"
        ".L7_O2:\n\t"
        "s_load_dwordx8 s[24:31], %2, 32\n\t"
        "s_load_dwordx4 s[60:63], %2, 176\n\t"
        "v_lshrrev_b32 v6, 4, v1\n\t"
        "v_lshlrev_b32 v7, 2, v6\n\t"
        "v_and_b32 v8, 15, v1\n\t"
        "s_lshl_b32 s78, s70, 8\n\t"
        "v_lshl_add_u32 v9, v8, 4, s78\n\t"
        "v_lshrrev_b32 v10, 3, v1\n\t"
        "v_lshlrev_b32 v10, 9, v10\n\t"
        "v_and_b32 v11, 7, v1\n\t"
        "v_lshl_add_u32 v10, v11, 4, v10\n\t"
        "v_and_b32 v11, 16, v1\n\t"
        "v_lshlrev_b32 v11, 2, v11\n\t"
        "v_xor_b32 v10, v10, v11\n\t"
        "s_add_u32 s81, s69, s76\n\t"
        "s_and_b32 s81, s81, 7\n\t"
        "s_lshr_b32 s78, s81, 1\n\t"
        "s_lshl_b32 s78, s78, 7\n\t"
        "s_and_b32 s79, s81, 1\n\t"
        "s_lshl_b32 s79, s79, 15\n\t"
        "s_add_u32 s78, s78, s79\n\t"
        "s_lshl_b32 s79, s70, 16\n\t"
        "s_add_u32 s78, s78, s79\n\t"
        "v_add_u32 v10, s78, v10\n\t"
        "s_lshl_b32 s82, s81, 13\n\t"
        "s_add_u32 s82, s82, 68864\n\t"
        "v_bfe_u32 v12, v1, 2, 2\n\t"
        "v_lshlrev_b32 v11, 10, v6\n\t"
        "v_lshl_add_u32 v11, v12, 7, v11\n\t"
        "v_lshrrev_b32 v12, 1, v12\n\t"
        "s_and_b32 s78, s81, 1\n\t"
        "v_xor_b32 v12, s78, v12\n\t"
        "v_lshl_add_u32 v11, v12, 6, v11\n\t"
        "v_and_b32 v12, 3, v1\n\t"
        "v_lshl_add_u32 v11, v12, 4, v11\n\t"
        "s_lshr_b32 s78, s81, 1\n\t"
        "s_lshl_b32 s78, s78, 14\n\t"
        "s_add_u32 s78, s78, 68864\n\t"
        "v_add_u32 v23, s78, v11\n\t"
        "s_waitcnt vmcnt(0)\n\t"
        "ds_bpermute_b32 v12, v7, v5 offset:0\n\t"
        "ds_bpermute_b32 v13, v7, v5 offset:16\n\t"
        "ds_bpermute_b32 v14, v7, v5 offset:32\n\t"
        "ds_bpermute_b32 v15, v7, v5 offset:48\n\t"
        "ds_bpermute_b32 v16, v7, v5 offset:64\n\t"
        "ds_bpermute_b32 v17, v7, v5 offset:80\n\t"
        "ds_bpermute_b32 v18, v7, v5 offset:96\n\t"
        "ds_bpermute_b32 v19, v7, v5 offset:112\n\t"
        "ds_bpermute_b32 v20, v7, v5 offset:128\n\t"
        "ds_bpermute_b32 v21, v7, v5 offset:144\n\t"
        "s_waitcnt lgkmcnt(0)\n\t"
        "v_lshl_add_u32 v12, v12, 9, v9\n\t"
        "v_lshl_add_u32 v13, v13, 9, v9\n\t"
        "v_lshl_add_u32 v14, v14, 9, v9\n\t"
        "v_lshl_add_u32 v15, v15, 9, v9\n\t"
        "v_lshl_add_u32 v16, v16, 9, v9\n\t"
        "v_lshl_add_u32 v17, v17, 9, v9\n\t"
        "v_lshl_add_u32 v18, v18, 9, v9\n\t"
        "v_lshl_add_u32 v19, v19, 9, v9\n\t"
        "v_lshl_add_u32 v20, v20, 9, v9\n\t"
        "v_lshl_add_u32 v21, v21, 9, v9\n\t"
        "s_setprio 0\n"
        ".L7_w1:\n\t"
        "ds_read_b32 v0, v2 offset:44\n\t"
        "s_waitcnt lgkmcnt(0)\n\t"
        "v_cmp_gt_u16 vcc, 2, v0\n\t"
        "s_cbranch_vccz .L7_d2\n\t"
        "s_sleep 1\n\t"
        "s_sub_i32 s83, s83, 1\n\t"
        "s_cmp_gt_i32 s83, 0\n\t"
        "s_cbranch_scc1 .L7_w1\n"
        ".L7_d2:\n"
        ".L7_w3:\n\t"
        "ds_read_b64 v[4:5], v2 offset:72\n\t"
        "s_waitcnt lgkmcnt(0)\n\t"
        "v_cmp_gt_u16 vcc, 2, v4\n\t"
        "s_cbranch_vccz .L7_d4\n\t"
        "s_sleep 1\n\t"
        "s_sub_i32 s83, s83, 1\n\t"
        "s_cmp_gt_i32 s83, 0\n\t"
        "s_cbranch_scc1 .L7_w3\n"
        ".L7_d4:\n\t"
        "v_readfirstlane_b32 s68, v5\n\t"
        "s_add_u32 m0, s82, 0\n\t"
        "s_mov_b32 s78, 0\n\t"
        "buffer_load_dwordx4 v10, s[28:31], s78 offen lds\n\t"
        "s_add_u32 m0, m0, 0x400\n\t"
        "s_add_u32 s78, s78, 0x1000\n\t"
        "buffer_load_dwordx4 v10, s[28:31], s78 offen lds\n\t"
        "s_add_u32 m0, m0, 0x400\n\t"
        "s_add_u32 s78, s78, 0x1000\n\t"
        "buffer_load_dwordx4 v10, s[28:31], s78 offen lds\n\t"
        "s_add_u32 m0, m0, 0x400\n\t"
        "s_add_u32 s78, s78, 0x1000\n\t"
        "buffer_load_dwordx4 v10, s[28:31], s78 offen lds\n\t"
        "s_add_u32 m0, m0, 0x400\n\t"
        "s_add_u32 s78, s78, 0x1000\n\t"
        "buffer_load_dwordx4 v10, s[28:31], s78 offen lds\n\t"
        "s_add_u32 m0, m0, 0x400\n\t"
        "s_add_u32 s78, s78, 0x1000\n\t"
        "buffer_load_dwordx4 v10, s[28:31], s78 offen lds\n\t"
        "s_add_u32 m0, m0, 0x400\n\t"
        "s_add_u32 s78, s78, 0x1000\n\t"
        "buffer_load_dwordx4 v10, s[28:31], s78 offen lds\n\t"
        "s_add_u32 m0, m0, 0x400\n\t"
        "s_add_u32 s78, s78, 0x1000\n\t"
        "buffer_load_dwordx4 v10, s[28:31], s78 offen lds\n\t"
        "s_mov_b64 exec, 1\n\t"
        "ds_add_u32 v2, v3 offset:88\n\t"
        "s_mov_b64 exec, -1\n\t"
        "s_mov_b32 s36, 0x22222222\n\t"
        "s_mov_b32 s37, s36\n\t"
        "s_lshl_b64 s[38:39], s[36:37], 1\n\t"
        "s_lshl_b64 s[40:41], s[36:37], 2\n\t"
        "s_and_b32 s44, s68, 0xfff\n\t"
        "s_xor_b32 s45, s44, 0x5a5\n\t"
        "s_lshr_b32 s46, s68, 12\n\t"
        "s_lshr_b32 s47, s68, 20\n\t"
        "s_add_u32 s46, s46, s68\n\t"
        "s_and_b32 s46, s46, 0xfff\n\t"
        "s_add_u32 s47, s47, s68\n\t"
        "s_and_b32 s47, s47, 0xfff\n\t"
        "v_mov_b32 v22, s44\n\t"
        "v_mov_b32 v0, s45\n\t"
        "v_cndmask_b32 v22, v22, v0, s[36:37]\n\t"
        "v_mov_b32 v0, s46\n\t"
        "v_cndmask_b32 v22, v22, v0, s[38:39]\n\t"
        "v_mov_b32 v0, s47\n\t"
        "v_cndmask_b32 v22, v22, v0, s[40:41]\n\t"
        "s_lshl3_add_u32 s42, %3, s77\n\t"
        "s_lshl_b32 s42, s42, 8\n\t"
        "s_add_u32 s42, s42, 262144\n\t"
        "s_xor_b32 s43, s42, 0x800\n\t"
        "s_movk_i32 s48, 0xfff\n\t"
        "s_waitcnt vmcnt(0)\n\t"
        "s_mov_b64 exec, 1\n\t"
        "ds_add_u32 v2, v3 offset:20\n\t"
        "s_mov_b64 exec, -1\n"
        ".L7_w5:\n\t"
        "ds_read_b32 v0, v2 offset:20\n\t"
        "s_waitcnt lgkmcnt(0)\n\t"
        "v_cmp_gt_u16 vcc, 8, v0\n\t"
        "s_cbranch_vccz .L7_d6\n\t"
        "s_sleep 1\n\t"
        "s_sub_i32 s83, s83, 1\n\t"
        "s_cmp_gt_i32 s83, 0\n\t"
        "s_cbranch_scc1 .L7_w5\n"
        ".L7_d6:\n\t"
        "ds_read_b64_tr_b16 v[64:65], v23 offset:0\n\t"
        "ds_read_b64_tr_b16 v[66:67], v23 offset:512\n\t"
        "ds_read_b64_tr_b16 v[68:69], v23 offset:8\n\t"
        "ds_read_b64_tr_b16 v[70:71], v23 offset:520\n\t"
        "ds_read_b64_tr_b16 v[72:73], v23 offset:4096\n\t"
        "ds_read_b64_tr_b16 v[74:75], v23 offset:4608\n\t"
        "ds_read_b64_tr_b16 v[76:77], v23 offset:4104\n\t"
        "ds_read_b64_tr_b16 v[78:79], v23 offset:4616\n\t"
        "ds_read_b64_tr_b16 v[80:81], v23 offset:8192\n\t"
        "ds_read_b64_tr_b16 v[82:83], v23 offset:8704\n\t"
        "ds_read_b64_tr_b16 v[84:85], v23 offset:8200\n\t"
        "ds_read_b64_tr_b16 v[86:87], v23 offset:8712\n\t"
        "ds_read_b64_tr_b16 v[88:89], v23 offset:12288\n\t"
        "ds_read_b64_tr_b16 v[90:91], v23 offset:12800\n\t"
        "s_waitcnt lgkmcnt(10)\n\t"
        "ds_read_b64_tr_b16 v[92:93], v23 offset:12296\n\t"
        "ds_read_b64_tr_b16 v[94:95], v23 offset:12808\n\t"
        "s_mov_b64 exec, 1\n\t"
        "ds_add_u32 v2, v3 offset:24\n\t"
        "s_mov_b64 exec, -1\n\t"
        "buffer_load_dwordx4 v[24:27], v12, s[16:19], 0 offen\n\t"
        "buffer_load_dwordx4 v[28:31], v13, s[16:19], 0 offen\n\t"
        "buffer_load_dwordx4 v[32:35], v14, s[16:19], 0 offen\n\t"
        "buffer_load_dwordx4 v[36:39], v15, s[16:19], 0 offen\n\t"
        "buffer_load_dwordx4 v[40:43], v16, s[16:19], 0 offen\n\t"
        "buffer_load_dwordx4 v[44:47], v17, s[16:19], 0 offen\n\t"
        "buffer_load_dwordx4 v[48:51], v18, s[16:19], 0 offen\n\t"
        "buffer_load_dwordx4 v[52:55], v19, s[16:19], 0 offen\n\t"
        "buffer_load_dwordx4 v[56:59], v20, s[16:19], 0 offen\n\t"
        "buffer_load_dwordx4 v[60:63], v21, s[16:19], 0 offen\n\t"
        "s_mov_b64 exec, 1\n\t"
        "ds_add_u32 v2, v3 offset:88\n\t"
        "s_mov_b64 exec, -1\n"
        ".L7_o2:\n\t"
        "s_setprio 2\n\t"
        "s_and_b32 s57, s81, 1\n\t"
        "s_lshl_b32 s57, s57, 13\n"
        ".L7_w7:\n\t"
        "ds_read_b32 v0, v2 offset:116\n\t"
        "s_waitcnt lgkmcnt(0)\n\t"
        "v_cmp_gt_u16 vcc, 1, v0\n\t"
        "s_cbranch_vccz .L7_d8\n\t"
        "s_sleep 1\n\t"
        "s_sub_i32 s83, s83, 1\n\t"
        "s_cmp_gt_i32 s83, 0\n\t"
        "s_cbranch_scc1 .L7_w7\n"
        ".L7_d8:\n\t"
        "v_readfirstlane_b32 s78, v0\n\t"
        "s_lshr_b32 s78, s78, 16\n\t"
        "s_cmp_gt_u32 s78, 16\n\t"
        "s_cselect_b32 s56, 0x2000, 0\n\t"
        "s_sub_u32 s78, s78, 13\n\t"
        "s_cmp_le_u32 s78, 6\n\t"
        "s_cselect_b32 s57, s57, s56\n\t"
        "s_xor_b32 s42, s42, s56\n\t"
        "s_xor_b32 s43, s43, s56\n\t"
        "s_lshr_b32 s78, %3, 3\n\t"
        "s_and_b32 s79, %3, 3\n\t"
        "s_lshl2_add_u32 s78, s78, s79\n\t"
        "s_and_b32 s79, %3, 4\n\t"
        "s_lshl_b32 s79, s79, 11\n\t"
        "s_lshl_b32 s30, s81, 10\n\t"
        "s_add_u32 s79, s79, s30\n\t"
        "s_add_u32 s79, s79, 3145728\n\t"
        "s_xor_b32 s79, s79, s57\n\t"
        "s_mul_i32 s24, s78, 0xc000\n\t"
        "s_xor_b32 s78, s78, 1\n\t"
        "s_mul_i32 s27, s78, 0xc000\n\t"
        "s_add_u32 s24, s24, s79\n\t"
        "s_add_u32 s25, s24, 0x4000\n\t"
        "s_add_u32 s26, s24, 0x8000\n\t"
        "s_add_u32 s27, s27, s79\n\t"
        "s_add_u32 s28, s27, 0x4000\n\t"
        "s_add_u32 s29, s27, 0x8000\n\t"
        "s_lshr_b32 s78, s24, 8\n\t"
        "s_and_b32 s78, s78, 0x2000\n\t"
        "s_xor_b32 s24, s24, s78\n\t"
        "s_lshr_b32 s78, s25, 8\n\t"
        "s_and_b32 s78, s78, 0x2000\n\t"
        "s_xor_b32 s25, s25, s78\n\t"
        "s_lshr_b32 s78, s26, 8\n\t"
        "s_and_b32 s78, s78, 0x2000\n\t"
        "s_xor_b32 s26, s26, s78\n\t"
        "s_lshr_b32 s78, s27, 8\n\t"
        "s_and_b32 s78, s78, 0x2000\n\t"
        "s_xor_b32 s27, s27, s78\n\t"
        "s_lshr_b32 s78, s28, 8\n\t"
        "s_and_b32 s78, s78, 0x2000\n\t"
        "s_xor_b32 s28, s28, s78\n\t"
        "s_lshr_b32 s78, s29, 8\n\t"
        "s_and_b32 s78, s78, 0x2000\n\t"
        "s_xor_b32 s29, s29, s78\n\t"
        "s_mul_i32 s78, s80, 272\n\t"
        "v_lshl_add_u32 v21, v8, 4, s78\n"
        ".L7_w9:\n\t"
        "ds_read_b32 v0, v2 offset:16\n\t"
        "s_waitcnt lgkmcnt(0)\n\t"
        "v_cmp_gt_u16 vcc, 4, v0\n\t"
        "s_cbranch_vccz .L7_d10\n\t"
        "s_sleep 1\n\t"
        "s_sub_i32 s83, s83, 1\n\t"
        "s_cmp_gt_i32 s83, 0\n\t"
        "s_cbranch_scc1 .L7_w9\n"
        ".L7_d10:\n\t"
        "ds_read_b128 v[96:99], v21 offset:2048\n\t"
        "ds_read_b128 v[100:103], v21 offset:2320\n\t"
        "ds_read_b128 v[104:107], v21 offset:2592\n\t"
        "ds_read_b128 v[108:111], v21 offset:2864\n\t"
        "ds_read_b128 v[112:115], v21 offset:3136\n\t"
        "ds_read_b128 v[116:119], v21 offset:3408\n\t"
        "ds_read_b128 v[120:123], v21 offset:3680\n\t"
        "ds_read_b128 v[124:127], v21 offset:3952\n\t"
        "ds_read_b128 v[4:7], v21 offset:4224\n\t"
        "ds_read_b128 v[8:11], v21 offset:4496\n\t"
        "s_waitcnt vmcnt(0)\n\t"
        "s_waitcnt lgkmcnt(8)\n\t"
        "v_dot2_f32_bf16 v12, v24, v96, 0\n\t"
        "v_dot2_f32_bf16 v13, v28, v100, 0\n\t"
        "v_dot2_f32_bf16 v12, v25, v97, v12\n\t"
        "v_dot2_f32_bf16 v13, v29, v101, v13\n\t"
        "v_dot2_f32_bf16 v12, v26, v98, v12\n\t"
        "v_dot2_f32_bf16 v13, v30, v102, v13\n\t"
        "v_dot2_f32_bf16 v12, v27, v99, v12\n\t"
        "v_dot2_f32_bf16 v13, v31, v103, v13\n\t"
        "s_waitcnt lgkmcnt(6)\n\t"
        "v_dot2_f32_bf16 v14, v32, v104, 0\n\t"
        "v_dot2_f32_bf16 v15, v36, v108, 0\n\t"
        "v_dot2_f32_bf16 v14, v33, v105, v14\n\t"
        "v_dot2_f32_bf16 v15, v37, v109, v15\n\t"
        "v_dot2_f32_bf16 v14, v34, v106, v14\n\t"
        "v_dot2_f32_bf16 v15, v38, v110, v15\n\t"
        "v_dot2_f32_bf16 v14, v35, v107, v14\n\t"
        "v_dot2_f32_bf16 v15, v39, v111, v15\n\t"
        "s_waitcnt lgkmcnt(4)\n\t"
        "v_dot2_f32_bf16 v16, v40, v112, 0\n\t"
        "v_dot2_f32_bf16 v17, v44, v116, 0\n\t"
        "v_dot2_f32_bf16 v16, v41, v113, v16\n\t"
        "v_dot2_f32_bf16 v17, v45, v117, v17\n\t"
        "v_dot2_f32_bf16 v16, v42, v114, v16\n\t"
        "v_dot2_f32_bf16 v17, v46, v118, v17\n\t"
        "v_dot2_f32_bf16 v16, v43, v115, v16\n\t"
        "v_dot2_f32_bf16 v17, v47, v119, v17\n\t"
        "s_waitcnt lgkmcnt(2)\n\t"
        "v_dot2_f32_bf16 v18, v48, v120, 0\n\t"
        "v_dot2_f32_bf16 v19, v52, v124, 0\n\t"
        "v_dot2_f32_bf16 v18, v49, v121, v18\n\t"
        "v_dot2_f32_bf16 v19, v53, v125, v19\n\t"
        "v_dot2_f32_bf16 v18, v50, v122, v18\n\t"
        "v_dot2_f32_bf16 v19, v54, v126, v19\n\t"
        "v_dot2_f32_bf16 v18, v51, v123, v18\n\t"
        "v_dot2_f32_bf16 v19, v55, v127, v19\n\t"
        "s_waitcnt lgkmcnt(0)\n\t"
        "v_dot2_f32_bf16 v20, v56, v4, 0\n\t"
        "v_dot2_f32_bf16 v21, v60, v8, 0\n\t"
        "v_dot2_f32_bf16 v20, v57, v5, v20\n\t"
        "v_dot2_f32_bf16 v21, v61, v9, v21\n\t"
        "v_dot2_f32_bf16 v20, v58, v6, v20\n\t"
        "v_dot2_f32_bf16 v21, v62, v10, v21\n\t"
        "v_dot2_f32_bf16 v20, v59, v7, v20\n\t"
        "v_dot2_f32_bf16 v21, v63, v11, v21\n\t"
        "v_add_f32_dpp v12, v12, v12 row_ror:8 row_mask:0xf bank_mask:0x3\n\t"
        "v_add_f32_dpp v14, v14, v14 row_ror:8 row_mask:0xf bank_mask:0x3\n\t"
        "v_add_f32_dpp v16, v16, v16 row_ror:8 row_mask:0xf bank_mask:0x3\n\t"
        "v_add_f32_dpp v18, v18, v18 row_ror:8 row_mask:0xf bank_mask:0x3\n\t"
        "v_add_f32_dpp v20, v20, v20 row_ror:8 row_mask:0xf bank_mask:0x3\n\t"
        "v_add_f32_dpp v12, v13, v13 row_ror:8 row_mask:0xf bank_mask:0xc\n\t"
        "v_add_f32_dpp v14, v15, v15 row_ror:8 row_mask:0xf bank_mask:0xc\n\t"
        "v_add_f32_dpp v16, v17, v17 row_ror:8 row_mask:0xf bank_mask:0xc\n\t"
        "v_add_f32_dpp v18, v19, v19 row_ror:8 row_mask:0xf bank_mask:0xc\n\t"
        "v_add_f32_dpp v20, v21, v21 row_ror:8 row_mask:0xf bank_mask:0xc\n\t"
        "v_add_f32_dpp v12, v12, v12 row_half_mirror row_mask:0xf bank_mask:0x5\n\t"
        "v_add_f32_dpp v16, v16, v16 row_half_mirror row_mask:0xf bank_mask:0x5\n\t"
        "v_add_f32_dpp v20, v20, v20 row_half_mirror row_mask:0xf bank_mask:0xf\n\t"
        "v_add_f32_dpp v12, v14, v14 row_half_mirror row_mask:0xf bank_mask:0xa\n\t"
        "v_add_f32_dpp v16, v18, v18 row_half_mirror row_mask:0xf bank_mask:0xa\n\t"
        "v_add_f32_dpp v20, v20, v20 quad_perm:[1,0,3,2] row_mask:0xf bank_mask:0xf\n\t"
        "v_add_f32_dpp v12, v12, v12 quad_perm:[1,0,3,2] row_mask:0xf bank_mask:0xf\n\t"
        "v_add_f32_dpp v16, v16, v16 quad_perm:[1,0,3,2] row_mask:0xf bank_mask:0xf\n\t"
        "v_add_f32_dpp v20, v20, v20 quad_perm:[2,3,0,1] row_mask:0xf bank_mask:0xf\n\t"
        "v_add_f32_dpp v12, v12, v12 quad_perm:[2,3,0,1] row_mask:0xf bank_mask:0xf\n\t"
        "v_add_f32_dpp v16, v16, v16 quad_perm:[2,3,0,1] row_mask:0xf bank_mask:0xf\n\t"
        "v_cndmask_b32 v12, v12, v16, s[36:37]\n\t"
        "v_cndmask_b32 v12, v12, v20, s[38:39]\n\t"
        "v_add_u32 v12, 0x800, v12\n\t"
        "v_bfi_b32 v21, s48, v22, v12\n\t"
        "v_lshlrev_b32 v13, 2, v1\n\t"
        "s_setprio 3\n\t"
        "buffer_store_dword v21, v13, s[20:23], s42 offen sc1\n\t"
        "s_setprio 2\n\t"
        "v_and_b32 v14, 3, v1\n\t"
        "v_lshlrev_b32 v14, 4, v14\n\t"
        "s_mov_b32 s78, 0xffff\n\t"
        "s_mov_b32 s79, 0\n\t"
        "v_lshlrev_b64 v[4:5], v14, s[78:79]\n\t"
        "s_mov_b32 s50, 0x0f0f0f0f\n\t"
        "s_mov_b32 s51, s50\n\t"
        "v_cndmask_b32 v6, v4, 0, s[50:51]\n\t"
        "v_cndmask_b32 v7, v5, 0, s[50:51]\n\t"
        "v_cndmask_b32 v4, 0, v4, s[50:51]\n\t"
        "v_cndmask_b32 v5, 0, v5, s[50:51]\n\t"
        "v_lshrrev_b32 v14, 5, v1\n\t"
        "v_add_lshl_u32 v14, v14, s80, 1\n"
        ".L7_w11:\n\t"
        "ds_read_b32 v0, v2 offset:8\n\t"
        "ds_read_u16 v16, v14 offset:1792\n\t"
        "ds_read_u16 v17, v14 offset:1796\n\t"
        "ds_read_u16 v18, v14 offset:1800\n\t"
        "ds_read_u16 v19, v14 offset:1804\n\t"
        "ds_read_u16 v20, v14 offset:1808\n\t"
        "s_waitcnt lgkmcnt(5)\n\t"
        "v_cmp_gt_u16 vcc, 2, v0\n\t"
        "s_cbranch_vccz .L7_d12\n\t"
        "s_waitcnt lgkmcnt(0)\n\t"
        "s_sleep 1\n\t"
        "s_sub_i32 s83, s83, 1\n\t"
        "s_cmp_gt_i32 s83, 0\n\t"
        "s_cbranch_scc1 .L7_w11\n"
        ".L7_d12:\n\t"
        "v_and_b32 v14, 15, v1\n\t"
        "v_lshlrev_b32 v14, 4, v14\n\t"
        "v_bfe_u32 v15, v1, 4, 1\n\t"
        "v_lshl_add_u32 v14, v15, 3, v14\n\t"
        "s_waitcnt lgkmcnt(0)\n\t"
        "v_xor_b32 v16, v16, v14\n\t"
        "v_xor_b32 v17, v17, v14\n\t"
        "v_xor_b32 v18, v18, v14\n\t"
        "v_xor_b32 v19, v19, v14\n\t"
        "v_xor_b32 v20, v20, v14\n\t"
        "v_xor_b32 v21, v21, v22\n\t"
        "v_mov_b64 v[8:9], 0\n\t"
        "v_mov_b64 v[10:11], 0\n"
        ".L7_w13:\n\t"
        "ds_read_b32 v0, v2 offset:28\n\t"
        "s_waitcnt lgkmcnt(0)\n\t"
        "v_cmp_gt_u16 vcc, 4, v0\n\t"
        "s_cbranch_vccz .L7_d14\n\t"
        "s_sleep 1\n\t"
        "s_sub_i32 s83, s83, 1\n\t"
        "s_cmp_gt_i32 s83, 0\n\t"
        "s_cbranch_scc1 .L7_w13\n"
        ".L7_d14:\n\t"
        "ds_read_b64_tr_b16 v[96:97], v23 offset:0\n\t"
        "ds_read_b64_tr_b16 v[98:99], v23 offset:512\n\t"
        "ds_read_b64_tr_b16 v[100:101], v23 offset:8\n\t"
        "ds_read_b64_tr_b16 v[102:103], v23 offset:520\n\t"
        "ds_read_b64_tr_b16 v[104:105], v23 offset:4096\n\t"
        "ds_read_b64_tr_b16 v[106:107], v23 offset:4608\n\t"
        "ds_read_b64_tr_b16 v[108:109], v23 offset:4104\n\t"
        "ds_read_b64_tr_b16 v[110:111], v23 offset:4616\n\t"
        "ds_read_b64_tr_b16 v[112:113], v23 offset:8192\n\t"
        "ds_read_b64_tr_b16 v[114:115], v23 offset:8704\n\t"
        "ds_read_b64_tr_b16 v[116:117], v23 offset:8200\n\t"
        "ds_read_b64_tr_b16 v[118:119], v23 offset:8712\n\t"
        "ds_read_b64_tr_b16 v[120:121], v23 offset:12288\n\t"
        "ds_read_b64_tr_b16 v[122:123], v23 offset:12800\n"
        ".L7_sp:\n\t"
        "buffer_load_dword v12, v13, s[20:23], s43 offen sc1\n"
        ".L7_sw:\n\t"
        "s_waitcnt vmcnt(0)\n\t"
        "v_xor_b32 v12, v12, v22\n\t"
        "v_and_b32 v0, s48, v12\n\t"
        "v_cmp_ne_u32 vcc, 0, v0\n\t"
        "s_cbranch_vccz .L7_sq\n\t"
        "s_sleep 8\n\t"
        "s_sub_i32 s83, s83, 1\n\t"
        "s_cmp_gt_i32 s83, 0\n\t"
        "s_cbranch_scc1 .L7_sp\n"
        ".L7_sq:\n\t"
        "v_add_f32 v21, v21, v12\n\t"
        "v_exp_f32 v21, v21\n\t"
        "v_mov_b64 v[12:13], 0\n\t"
        "v_mov_b64 v[14:15], 0\n\t"
        "v_mov_b32 v22, v21\n\t"
        "v_mov_b32 v0, v21\n\t"
        "s_nop 1\n\t"
        "v_permlane16_swap_b32 v22, v0\n\t"
        "v_add_f32 v22, v22, v0\n\t"
        "v_mov_b32 v0, v22\n\t"
        "s_nop 1\n\t"
        "v_permlane32_swap_b32 v22, v0\n\t"
        "v_add_f32 v22, v22, v0\n\t"
        "v_add_f32 v22, 0x3089705f, v22\n\t"
        "v_rcp_f32 v22, v22\n\t"
        "s_nop 0\n\t"
        "v_mul_f32 v21, v21, v22\n\t"
        "v_cvt_pk_bf16_f32 v21, v21, v21\n\t"
        "v_lshlrev_b32 v22, 4, v1\n\t"
        "v_add_u32 v22, 142848, v22\n"
        ".L7_w15:\n\t"
        "ds_read_b32 v0, v2 offset:36\n\t"
        "s_waitcnt lgkmcnt(0)\n\t"
        "v_cmp_gt_u16 vcc, 4, v0\n\t"
        "s_cbranch_vccz .L7_d16\n\t"
        "s_sleep 1\n\t"
        "s_sub_i32 s83, s83, 1\n\t"
        "s_cmp_gt_i32 s83, 0\n\t"
        "s_cbranch_scc1 .L7_w15\n"
        ".L7_d16:\n\t"
        "v_cmp_lt_u32 vcc, 0xfffff, v0\n\t"
        "s_cbranch_vccnz .L7_na\n\t"
        "v_and_b32_dpp v8, v21, v4 row_newbcast:0 row_mask:0xf bank_mask:0x3\n\t"
        "v_and_b32_dpp v9, v21, v5 row_newbcast:0 row_mask:0xf bank_mask:0x3\n\t"
        "v_and_b32_dpp v10, v21, v6 row_newbcast:0 row_mask:0xf bank_mask:0x3\n\t"
        "v_and_b32_dpp v11, v21, v7 row_newbcast:0 row_mask:0xf bank_mask:0x3\n\t"
        "v_and_b32_dpp v12, v21, v4 row_newbcast:8 row_mask:0xf bank_mask:0xc\n\t"
        "v_and_b32_dpp v13, v21, v5 row_newbcast:8 row_mask:0xf bank_mask:0xc\n\t"
        "v_and_b32_dpp v14, v21, v6 row_newbcast:8 row_mask:0xf bank_mask:0xc\n\t"
        "v_and_b32_dpp v15, v21, v7 row_newbcast:8 row_mask:0xf bank_mask:0xc\n\t"
        "v_mfma_f32_16x16x32_bf16 v[124:127], v[8:11], v[24:27], 0\n\t"
        "s_nop 0\n\t"
        "v_mfma_f32_16x16x32_bf16 v[124:127], v[12:15], v[28:31], v[124:127]\n\t"
        "v_and_b32_dpp v8, v21, v4 row_newbcast:4 row_mask:0xf bank_mask:0x3\n\t"
        "v_and_b32_dpp v9, v21, v5 row_newbcast:4 row_mask:0xf bank_mask:0x3\n\t"
        "v_and_b32_dpp v10, v21, v6 row_newbcast:4 row_mask:0xf bank_mask:0x3\n\t"
        "v_and_b32_dpp v11, v21, v7 row_newbcast:4 row_mask:0xf bank_mask:0x3\n\t"
        "v_and_b32_dpp v12, v21, v4 row_newbcast:12 row_mask:0xf bank_mask:0xc\n\t"
        "v_and_b32_dpp v13, v21, v5 row_newbcast:12 row_mask:0xf bank_mask:0xc\n\t"
        "v_and_b32_dpp v14, v21, v6 row_newbcast:12 row_mask:0xf bank_mask:0xc\n\t"
        "v_and_b32_dpp v15, v21, v7 row_newbcast:12 row_mask:0xf bank_mask:0xc\n\t"
        "v_mfma_f32_16x16x32_bf16 v[24:27], v[8:11], v[32:35], 0\n\t"
        "s_nop 0\n\t"
        "v_mfma_f32_16x16x32_bf16 v[24:27], v[12:15], v[36:39], v[24:27]\n\t"
        "v_cvt_pk_bf16_f32 v124, v124, v125\n\t"
        "v_cvt_pk_bf16_f32 v125, v126, v127\n\t"
        "ds_write_b64 v16, v[124:125] offset:46336\n\t"
        "v_and_b32_dpp v8, v21, v4 row_newbcast:1 row_mask:0xf bank_mask:0x3\n\t"
        "v_and_b32_dpp v9, v21, v5 row_newbcast:1 row_mask:0xf bank_mask:0x3\n\t"
        "v_and_b32_dpp v10, v21, v6 row_newbcast:1 row_mask:0xf bank_mask:0x3\n\t"
        "v_and_b32_dpp v11, v21, v7 row_newbcast:1 row_mask:0xf bank_mask:0x3\n\t"
        "v_and_b32_dpp v12, v21, v4 row_newbcast:9 row_mask:0xf bank_mask:0xc\n\t"
        "v_and_b32_dpp v13, v21, v5 row_newbcast:9 row_mask:0xf bank_mask:0xc\n\t"
        "v_and_b32_dpp v14, v21, v6 row_newbcast:9 row_mask:0xf bank_mask:0xc\n\t"
        "v_and_b32_dpp v15, v21, v7 row_newbcast:9 row_mask:0xf bank_mask:0xc\n\t"
        "v_mfma_f32_16x16x32_bf16 v[32:35], v[8:11], v[40:43], 0\n\t"
        "s_nop 0\n\t"
        "v_mfma_f32_16x16x32_bf16 v[32:35], v[12:15], v[44:47], v[32:35]\n\t"
        "v_cvt_pk_bf16_f32 v24, v24, v25\n\t"
        "v_cvt_pk_bf16_f32 v25, v26, v27\n\t"
        "ds_write_b64 v17, v[24:25] offset:46336\n\t"
        "v_and_b32_dpp v8, v21, v4 row_newbcast:5 row_mask:0xf bank_mask:0x3\n\t"
        "v_and_b32_dpp v9, v21, v5 row_newbcast:5 row_mask:0xf bank_mask:0x3\n\t"
        "v_and_b32_dpp v10, v21, v6 row_newbcast:5 row_mask:0xf bank_mask:0x3\n\t"
        "v_and_b32_dpp v11, v21, v7 row_newbcast:5 row_mask:0xf bank_mask:0x3\n\t"
        "v_and_b32_dpp v12, v21, v4 row_newbcast:13 row_mask:0xf bank_mask:0xc\n\t"
        "v_and_b32_dpp v13, v21, v5 row_newbcast:13 row_mask:0xf bank_mask:0xc\n\t"
        "v_and_b32_dpp v14, v21, v6 row_newbcast:13 row_mask:0xf bank_mask:0xc\n\t"
        "v_and_b32_dpp v15, v21, v7 row_newbcast:13 row_mask:0xf bank_mask:0xc\n\t"
        "v_mfma_f32_16x16x32_bf16 v[40:43], v[8:11], v[48:51], 0\n\t"
        "s_nop 0\n\t"
        "v_mfma_f32_16x16x32_bf16 v[40:43], v[12:15], v[52:55], v[40:43]\n\t"
        "v_cvt_pk_bf16_f32 v32, v32, v33\n\t"
        "v_cvt_pk_bf16_f32 v33, v34, v35\n\t"
        "ds_write_b64 v18, v[32:33] offset:46336\n\t"
        "v_and_b32_dpp v8, v21, v4 row_newbcast:2 row_mask:0xf bank_mask:0x3\n\t"
        "v_and_b32_dpp v9, v21, v5 row_newbcast:2 row_mask:0xf bank_mask:0x3\n\t"
        "v_and_b32_dpp v10, v21, v6 row_newbcast:2 row_mask:0xf bank_mask:0x3\n\t"
        "v_and_b32_dpp v11, v21, v7 row_newbcast:2 row_mask:0xf bank_mask:0x3\n\t"
        "v_and_b32_dpp v12, v21, v4 row_newbcast:10 row_mask:0xf bank_mask:0xc\n\t"
        "v_and_b32_dpp v13, v21, v5 row_newbcast:10 row_mask:0xf bank_mask:0xc\n\t"
        "v_and_b32_dpp v14, v21, v6 row_newbcast:10 row_mask:0xf bank_mask:0xc\n\t"
        "v_and_b32_dpp v15, v21, v7 row_newbcast:10 row_mask:0xf bank_mask:0xc\n\t"
        "v_mfma_f32_16x16x32_bf16 v[48:51], v[8:11], v[56:59], 0\n\t"
        "s_nop 0\n\t"
        "v_mfma_f32_16x16x32_bf16 v[48:51], v[12:15], v[60:63], v[48:51]\n\t"
        "v_cvt_pk_bf16_f32 v40, v40, v41\n\t"
        "v_cvt_pk_bf16_f32 v41, v42, v43\n\t"
        "ds_write_b64 v19, v[40:41] offset:46336\n\t"
        "s_cmp_lt_u32 s69, 4\n\t"
        "s_cselect_b32 s78, 16, 1\n\t"
        "v_mov_b32 v0, s78\n\t"
        "s_nop 1\n\t"
        "v_cvt_pk_bf16_f32 v48, v48, v49\n\t"
        "v_cvt_pk_bf16_f32 v49, v50, v51\n\t"
        "ds_write_b64 v20, v[48:49] offset:46336\n\t"
        "s_mov_b64 exec, 1\n\t"
        "ds_add_u32 v2, v0 offset:40\n\t"
        "s_mov_b64 exec, -1\n"
        ".L7_na:\n\t"
        "ds_read_b64_tr_b16 v[124:125], v23 offset:12296\n\t"
        "ds_read_b64_tr_b16 v[126:127], v23 offset:12808\n\t"
        "ds_read_b128 v[4:7], v22\n\t"
        "s_lshl_b32 s50, s81, 6\n\t"
        "s_lshl_b32 s51, s69, 10\n\t"
        "s_add_u32 s51, s51, 134656\n\t"
        "s_mov_b32 s49, 0x10001\n\t"
        "s_mov_b32 s48, 0x07060302\n\t"
        "s_mov_b32 s55, 142848\n"
        ".L7_w17:\n\t"
        "ds_read_b32 v0, v2 offset:40\n\t"
        "s_waitcnt lgkmcnt(0)\n\t"
        "v_cmp_gt_u16 vcc, 64, v0\n\t"
        "s_cbranch_vccz .L7_d18\n\t"
        "s_setprio 0\n\t"
        "s_sleep 1\n\t"
        "s_sub_i32 s83, s83, 1\n\t"
        "s_cmp_gt_i32 s83, 0\n\t"
        "s_cbranch_scc1 .L7_w17\n"
        ".L7_d18:\n\t"
        "s_setprio 2\n\t"
        "ds_read_b128 v[8:11], v4 offset:46336\n\t"
        "ds_read_b128 v[12:15], v5 offset:46336\n\t"
        "ds_read_b128 v[16:19], v6 offset:46336\n\t"
        "ds_read_b128 v[20:23], v7 offset:46336\n\t"
        "ds_read_b128 v[48:51], v4 offset:50432\n\t"
        "ds_read_b128 v[52:55], v5 offset:50432\n\t"
        "ds_read_b128 v[56:59], v6 offset:50432\n\t"
        "ds_read_b128 v[60:63], v7 offset:50432\n\t"
        "v_and_b32 v3, 8, v1\n\t"
        "s_waitcnt lgkmcnt(6)\n\t"
        "v_mfma_f32_16x16x32_bf16 v[32:35], v[64:67], v[8:11], 0\n\t"
        "v_mfma_f32_16x16x32_bf16 v[36:39], v[68:71], v[8:11], 0\n\t"
        "v_mfma_f32_16x16x32_bf16 v[32:35], v[72:75], v[12:15], v[32:35]\n\t"
        "v_mfma_f32_16x16x32_bf16 v[36:39], v[76:79], v[12:15], v[36:39]\n\t"
        "s_waitcnt lgkmcnt(4)\n\t"
        "v_mfma_f32_16x16x32_bf16 v[32:35], v[80:83], v[16:19], v[32:35]\n\t"
        "v_mfma_f32_16x16x32_bf16 v[36:39], v[84:87], v[16:19], v[36:39]\n\t"
        "v_mfma_f32_16x16x32_bf16 v[32:35], v[88:91], v[20:23], v[32:35]\n\t"
        "v_mfma_f32_16x16x32_bf16 v[36:39], v[92:95], v[20:23], v[36:39]\n\t"
        "ds_read_b32 v0, v2 offset:40\n\t"
        "v_lshl_add_u32 v8, v3, 8, v4\n\t"
        "v_lshl_add_u32 v12, v3, 8, v5\n\t"
        "v_lshl_add_u32 v16, v3, 8, v6\n\t"
        "v_lshl_add_u32 v20, v3, 8, v7\n\t"
        "ds_read_b128 v[8:11], v8 offset:54528\n\t"
        "ds_read_b128 v[12:15], v12 offset:54528\n\t"
        "ds_read_b128 v[16:19], v16 offset:54528\n\t"
        "ds_read_b128 v[20:23], v20 offset:54528\n\t"
        "s_waitcnt lgkmcnt(7)\n\t"
        "v_mfma_f32_16x16x32_bf16 v[40:43], v[64:67], v[48:51], 0\n\t"
        "v_mfma_f32_16x16x32_bf16 v[44:47], v[68:71], v[48:51], 0\n\t"
        "v_mfma_f32_16x16x32_bf16 v[40:43], v[72:75], v[52:55], v[40:43]\n\t"
        "v_mfma_f32_16x16x32_bf16 v[44:47], v[76:79], v[52:55], v[44:47]\n\t"
        "s_waitcnt lgkmcnt(5)\n\t"
        "v_mfma_f32_16x16x32_bf16 v[40:43], v[80:83], v[56:59], v[40:43]\n\t"
        "v_mfma_f32_16x16x32_bf16 v[44:47], v[84:87], v[56:59], v[44:47]\n\t"
        "v_mfma_f32_16x16x32_bf16 v[40:43], v[88:91], v[60:63], v[40:43]\n\t"
        "v_mfma_f32_16x16x32_bf16 v[44:47], v[92:95], v[60:63], v[44:47]\n\t"
        "s_waitcnt lgkmcnt(4)\n\t"
        "v_cmp_gt_u16 vcc, 68, v0\n\t"
        "s_cbranch_vccz .L7_t0\n"
        ".L7_w19:\n\t"
        "ds_read_b32 v0, v2 offset:40\n\t"
        "s_waitcnt lgkmcnt(0)\n\t"
        "v_cmp_gt_u16 vcc, 68, v0\n\t"
        "s_cbranch_vccz .L7_d20\n\t"
        "s_setprio 0\n\t"
        "s_sleep 1\n\t"
        "s_sub_i32 s83, s83, 1\n\t"
        "s_cmp_gt_i32 s83, 0\n\t"
        "s_cbranch_scc1 .L7_w19\n"
        ".L7_d20:\n\t"
        "s_setprio 2\n\t"
        "v_lshl_add_u32 v8, v3, 8, v4\n\t"
        "v_lshl_add_u32 v12, v3, 8, v5\n\t"
        "v_lshl_add_u32 v16, v3, 8, v6\n\t"
        "v_lshl_add_u32 v20, v3, 8, v7\n\t"
        "ds_read_b128 v[8:11], v8 offset:54528\n\t"
        "ds_read_b128 v[12:15], v12 offset:54528\n\t"
        "ds_read_b128 v[16:19], v16 offset:54528\n\t"
        "ds_read_b128 v[20:23], v20 offset:54528\n"
        ".L7_t0:\n\t"
        "ds_read_b32 v0, v2 offset:52\n\t"
        "ds_read_b128 v[48:51], v4 offset:23808\n\t"
        "ds_read_b128 v[52:55], v5 offset:23808\n\t"
        "ds_read_b128 v[56:59], v6 offset:23808\n\t"
        "ds_read_b128 v[60:63], v7 offset:23808\n\t"
        "s_waitcnt lgkmcnt(7)\n\t"
        "v_mfma_f32_16x16x32_bf16 v[24:27], v[64:67], v[8:11], 0\n\t"
        "v_mfma_f32_16x16x32_bf16 v[28:31], v[68:71], v[8:11], 0\n\t"
        "v_mfma_f32_16x16x32_bf16 v[24:27], v[72:75], v[12:15], v[24:27]\n\t"
        "v_mfma_f32_16x16x32_bf16 v[28:31], v[76:79], v[12:15], v[28:31]\n\t"
        "s_waitcnt lgkmcnt(5)\n\t"
        "v_mfma_f32_16x16x32_bf16 v[24:27], v[80:83], v[16:19], v[24:27]\n\t"
        "v_mfma_f32_16x16x32_bf16 v[28:31], v[84:87], v[16:19], v[28:31]\n\t"
        "v_mfma_f32_16x16x32_bf16 v[24:27], v[88:91], v[20:23], v[24:27]\n\t"
        "v_mfma_f32_16x16x32_bf16 v[28:31], v[92:95], v[20:23], v[28:31]\n\t"
        "ds_read_b128 v[8:11], v4 offset:32000\n\t"
        "ds_read_b128 v[12:15], v5 offset:32000\n\t"
        "ds_read_b128 v[16:19], v6 offset:32000\n\t"
        "ds_read_b128 v[20:23], v7 offset:32000\n\t"
        "s_waitcnt lgkmcnt(8)\n\t"
        "v_cmp_gt_u16 vcc, 6, v0\n\t"
        "s_cbranch_vccz .L7_t1\n"
        ".L7_w21:\n\t"
        "ds_read_b32 v0, v2 offset:52\n\t"
        "s_waitcnt lgkmcnt(0)\n\t"
        "v_cmp_gt_u16 vcc, 6, v0\n\t"
        "s_cbranch_vccz .L7_d22\n\t"
        "s_setprio 0\n\t"
        "s_sleep 1\n\t"
        "s_sub_i32 s83, s83, 1\n\t"
        "s_cmp_gt_i32 s83, 0\n\t"
        "s_cbranch_scc1 .L7_w21\n"
        ".L7_d22:\n\t"
        "s_setprio 2\n\t"
        "ds_read_b128 v[48:51], v4 offset:23808\n\t"
        "ds_read_b128 v[52:55], v5 offset:23808\n\t"
        "ds_read_b128 v[56:59], v6 offset:23808\n\t"
        "ds_read_b128 v[60:63], v7 offset:23808\n\t"
        "ds_read_b128 v[8:11], v4 offset:32000\n\t"
        "ds_read_b128 v[12:15], v5 offset:32000\n\t"
        "ds_read_b128 v[16:19], v6 offset:32000\n\t"
        "ds_read_b128 v[20:23], v7 offset:32000\n"
        ".L7_t1:\n\t"
        "v_lshl_add_u32 v3, v1, 4, s55\n\t"
        "s_waitcnt lgkmcnt(6)\n\t"
        "v_mfma_f32_16x16x32_bf16 v[32:35], v[96:99], v[48:51], v[32:35]\n\t"
        "v_mfma_f32_16x16x32_bf16 v[36:39], v[100:103], v[48:51], v[36:39]\n\t"
        "v_mfma_f32_16x16x32_bf16 v[32:35], v[104:107], v[52:55], v[32:35]\n\t"
        "v_mfma_f32_16x16x32_bf16 v[36:39], v[108:111], v[52:55], v[36:39]\n\t"
        "s_waitcnt lgkmcnt(4)\n\t"
        "v_mfma_f32_16x16x32_bf16 v[32:35], v[112:115], v[56:59], v[32:35]\n\t"
        "v_mfma_f32_16x16x32_bf16 v[36:39], v[116:119], v[56:59], v[36:39]\n\t"
        "v_mfma_f32_16x16x32_bf16 v[32:35], v[120:123], v[60:63], v[32:35]\n\t"
        "v_mfma_f32_16x16x32_bf16 v[36:39], v[124:127], v[60:63], v[36:39]\n\t"
        "ds_read_b128 v[60:63], v3 offset:1024\n\t"
        "s_waitcnt lgkmcnt(3)\n\t"
        "v_mfma_f32_16x16x32_bf16 v[24:27], v[96:99], v[8:11], v[24:27]\n\t"
        "v_mfma_f32_16x16x32_bf16 v[28:31], v[100:103], v[8:11], v[28:31]\n\t"
        "v_mfma_f32_16x16x32_bf16 v[24:27], v[104:107], v[12:15], v[24:27]\n\t"
        "v_mfma_f32_16x16x32_bf16 v[28:31], v[108:111], v[12:15], v[28:31]\n\t"
        "s_waitcnt lgkmcnt(1)\n\t"
        "v_mfma_f32_16x16x32_bf16 v[24:27], v[112:115], v[16:19], v[24:27]\n\t"
        "v_mfma_f32_16x16x32_bf16 v[28:31], v[116:119], v[16:19], v[28:31]\n\t"
        "v_mfma_f32_16x16x32_bf16 v[24:27], v[120:123], v[20:23], v[24:27]\n\t"
        "v_mfma_f32_16x16x32_bf16 v[28:31], v[124:127], v[20:23], v[28:31]\n\t"
        "ds_read_b32 v0, v2 offset:56\n\t"
        "ds_read_b128 v[8:11], v4 offset:27904\n\t"
        "ds_read_b128 v[12:15], v5 offset:27904\n\t"
        "ds_read_b128 v[16:19], v6 offset:27904\n\t"
        "ds_read_b128 v[20:23], v7 offset:27904\n\t"
        "s_waitcnt lgkmcnt(5)\n\t"
        "v_add_u32 v32, 0x10000, v32\n\t"
        "v_add_u32 v33, 0x10000, v33\n\t"
        "v_add_u32 v34, 0x10000, v34\n\t"
        "v_add_u32 v35, 0x10000, v35\n\t"
        "v_perm_b32 v52, v33, v32, s48\n\t"
        "v_perm_b32 v53, v35, v34, s48\n\t"
        "v_cvt_pk_bf16_f32 v54, v36, v37\n\t"
        "v_cvt_pk_bf16_f32 v55, v38, v39\n\t"
        "v_bfi_b32 v52, s49, v60, v52\n\t"
        "v_bfi_b32 v53, s49, v61, v53\n\t"
        "buffer_store_dwordx4 v[52:55], v62, s[20:23], s25 offen sc1\n\t"
        "s_mov_b32 exec_lo, 0x00ff00ff\n\t"
        "s_mov_b32 exec_hi, 0x00ff00ff\n\t"
        "v_add_u32 v24, 0x10000, v24\n\t"
        "v_add_u32 v25, 0x10000, v25\n\t"
        "v_add_u32 v26, 0x10000, v26\n\t"
        "v_add_u32 v27, 0x10000, v27\n\t"
        "v_perm_b32 v48, v25, v24, s48\n\t"
        "v_perm_b32 v49, v27, v26, s48\n\t"
        "v_cvt_pk_bf16_f32 v50, v28, v29\n\t"
        "v_cvt_pk_bf16_f32 v51, v30, v31\n\t"
        "v_bfi_b32 v48, s49, v60, v48\n\t"
        "v_bfi_b32 v49, s49, v61, v49\n\t"
        "buffer_store_dwordx4 v[48:51], v62, s[20:23], s24 offen sc1\n\t"
        "s_mov_b64 exec, -1\n\t"
        "s_waitcnt lgkmcnt(4)\n\t"
        "v_cmp_gt_u16 vcc, 4, v0\n\t"
        "s_cbranch_vccz .L7_t2\n"
        ".L7_w23:\n\t"
        "ds_read_b32 v0, v2 offset:56\n\t"
        "s_waitcnt lgkmcnt(0)\n\t"
        "v_cmp_gt_u16 vcc, 3, v0\n\t"
        "s_cbranch_vccz .L7_d24\n\t"
        "s_setprio 0\n\t"
        "s_sleep 1\n\t"
        "s_sub_i32 s83, s83, 1\n\t"
        "s_cmp_gt_i32 s83, 0\n\t"
        "s_cbranch_scc1 .L7_w23\n"
        ".L7_d24:\n\t"
        "s_setprio 2\n"
        ".L7_w25:\n\t"
        "ds_read_b32 v0, v2 offset:56\n\t"
        "ds_read_b128 v[8:11], v4 offset:27904\n\t"
        "ds_read_b128 v[12:15], v5 offset:27904\n\t"
        "ds_read_b128 v[16:19], v6 offset:27904\n\t"
        "ds_read_b128 v[20:23], v7 offset:27904\n\t"
        "s_waitcnt lgkmcnt(4)\n\t"
        "v_cmp_gt_u16 vcc, 4, v0\n\t"
        "s_cbranch_vccz .L7_d26\n\t"
        "s_waitcnt lgkmcnt(0)\n\t"
        "s_setprio 0\n\t"
        "s_sleep 1\n\t"
        "s_sub_i32 s83, s83, 1\n\t"
        "s_cmp_gt_i32 s83, 0\n\t"
        "s_cbranch_scc1 .L7_w25\n"
        ".L7_d26:\n\t"
        "s_setprio 2\n"
        ".L7_t2:\n\t"
        "s_waitcnt lgkmcnt(2)\n\t"
        "v_mfma_f32_16x16x32_bf16 v[40:43], v[96:99], v[8:11], v[40:43]\n\t"
        "v_mfma_f32_16x16x32_bf16 v[44:47], v[100:103], v[8:11], v[44:47]\n\t"
        "v_mfma_f32_16x16x32_bf16 v[40:43], v[104:107], v[12:15], v[40:43]\n\t"
        "v_mfma_f32_16x16x32_bf16 v[44:47], v[108:111], v[12:15], v[44:47]\n\t"
        "s_waitcnt lgkmcnt(0)\n\t"
        "v_mfma_f32_16x16x32_bf16 v[40:43], v[112:115], v[16:19], v[40:43]\n\t"
        "v_mfma_f32_16x16x32_bf16 v[44:47], v[116:119], v[16:19], v[44:47]\n\t"
        "v_mfma_f32_16x16x32_bf16 v[40:43], v[120:123], v[20:23], v[40:43]\n\t"
        "v_mfma_f32_16x16x32_bf16 v[44:47], v[124:127], v[20:23], v[44:47]\n\t"
        "ds_read_b128 v[8:11], v4 offset:60672\n\t"
        "ds_read_b128 v[12:15], v5 offset:60672\n\t"
        "ds_read_b128 v[16:19], v6 offset:60672\n\t"
        "ds_read_b128 v[20:23], v7 offset:60672\n\t"
        "s_nop 2\n\t"
        "v_add_u32 v40, 0x10000, v40\n\t"
        "v_add_u32 v41, 0x10000, v41\n\t"
        "v_add_u32 v42, 0x10000, v42\n\t"
        "v_add_u32 v43, 0x10000, v43\n\t"
        "v_perm_b32 v56, v41, v40, s48\n\t"
        "v_perm_b32 v57, v43, v42, s48\n\t"
        "v_cvt_pk_bf16_f32 v58, v44, v45\n\t"
        "v_cvt_pk_bf16_f32 v59, v46, v47\n\t"
        "v_bfi_b32 v56, s49, v60, v56\n\t"
        "v_bfi_b32 v57, s49, v61, v57\n\t"
        "buffer_store_dwordx4 v[56:59], v62, s[20:23], s26 offen sc1\n\t"
        "v_mov_b32 v0, 1\n\t"
        "s_mov_b64 exec, 1\n\t"
        "ds_add_u32 v2, v0 offset:96\n\t"
        "s_mov_b64 exec, -1\n\t"
        "s_waitcnt lgkmcnt(3)\n\t"
        "v_mfma_f32_16x16x32_bf16 v[32:35], v[64:67], v[8:11], 0\n\t"
        "v_mfma_f32_16x16x32_bf16 v[36:39], v[68:71], v[8:11], 0\n\t"
        "v_mfma_f32_16x16x32_bf16 v[32:35], v[72:75], v[12:15], v[32:35]\n\t"
        "v_mfma_f32_16x16x32_bf16 v[36:39], v[76:79], v[12:15], v[36:39]\n\t"
        "s_waitcnt lgkmcnt(1)\n\t"
        "v_mfma_f32_16x16x32_bf16 v[32:35], v[80:83], v[16:19], v[32:35]\n\t"
        "v_mfma_f32_16x16x32_bf16 v[36:39], v[84:87], v[16:19], v[36:39]\n\t"
        "v_mfma_f32_16x16x32_bf16 v[32:35], v[88:91], v[20:23], v[32:35]\n\t"
        "v_mfma_f32_16x16x32_bf16 v[36:39], v[92:95], v[20:23], v[36:39]\n\t"
        "ds_read_b128 v[8:11], v4 offset:64768\n\t"
        "ds_read_b128 v[12:15], v5 offset:64768\n\t"
        "ds_read_b128 v[16:19], v6 offset:64768\n\t"
        "ds_read_b128 v[20:23], v7 offset:64768\n\t"
        "s_waitcnt lgkmcnt(2)\n\t"
        "v_mfma_f32_16x16x32_bf16 v[40:43], v[64:67], v[8:11], 0\n\t"
        "v_mfma_f32_16x16x32_bf16 v[44:47], v[68:71], v[8:11], 0\n\t"
        "v_mfma_f32_16x16x32_bf16 v[40:43], v[72:75], v[12:15], v[40:43]\n\t"
        "v_mfma_f32_16x16x32_bf16 v[44:47], v[76:79], v[12:15], v[44:47]\n\t"
        "s_waitcnt lgkmcnt(0)\n\t"
        "v_mfma_f32_16x16x32_bf16 v[40:43], v[80:83], v[16:19], v[40:43]\n\t"
        "v_mfma_f32_16x16x32_bf16 v[44:47], v[84:87], v[16:19], v[44:47]\n\t"
        "v_mfma_f32_16x16x32_bf16 v[40:43], v[88:91], v[20:23], v[40:43]\n\t"
        "v_mfma_f32_16x16x32_bf16 v[44:47], v[92:95], v[20:23], v[44:47]\n\t"
        "ds_read_b128 v[68:71], v3 offset:2048\n\t"
        "ds_read_b128 v[74:77], v3 offset:3072\n"
        ".L7_w27:\n\t"
        "ds_read_b32 v0, v2 offset:60\n\t"
        "s_waitcnt lgkmcnt(0)\n\t"
        "v_cmp_gt_u16 vcc, 2, v0\n\t"
        "s_cbranch_vccz .L7_d28\n\t"
        "s_setprio 0\n\t"
        "s_sleep 1\n\t"
        "s_sub_i32 s83, s83, 1\n\t"
        "s_cmp_gt_i32 s83, 0\n\t"
        "s_cbranch_scc1 .L7_w27\n"
        ".L7_d28:\n\t"
        "s_setprio 2\n\t"
        "ds_read_b128 v[8:11], v4 offset:34048\n\t"
        "ds_read_b128 v[12:15], v5 offset:34048\n\t"
        "ds_read_b128 v[16:19], v6 offset:34048\n\t"
        "ds_read_b128 v[20:23], v7 offset:34048\n\t"
        "v_add_u32 v71, s51, v71\n\t"
        "v_add_u32 v72, s51, v74\n\t"
        "v_mov_b64 v[74:75], 0\n\t"
        "v_mov_b64 v[78:79], 0\n\t"
        "s_waitcnt lgkmcnt(2)\n\t"
        "v_mfma_f32_16x16x32_bf16 v[24:27], v[96:99], v[8:11], v[24:27]\n\t"
        "v_mfma_f32_16x16x32_bf16 v[28:31], v[100:103], v[8:11], v[28:31]\n\t"
        "v_mfma_f32_16x16x32_bf16 v[24:27], v[104:107], v[12:15], v[24:27]\n\t"
        "v_mfma_f32_16x16x32_bf16 v[28:31], v[108:111], v[12:15], v[28:31]\n\t"
        "s_waitcnt lgkmcnt(0)\n\t"
        "v_mfma_f32_16x16x32_bf16 v[24:27], v[112:115], v[16:19], v[24:27]\n\t"
        "v_mfma_f32_16x16x32_bf16 v[28:31], v[116:119], v[16:19], v[28:31]\n\t"
        "v_mfma_f32_16x16x32_bf16 v[24:27], v[120:123], v[20:23], v[24:27]\n\t"
        "v_mfma_f32_16x16x32_bf16 v[28:31], v[124:127], v[20:23], v[28:31]\n"
        ".L7_w29:\n\t"
        "ds_read_b32 v0, v2 offset:100\n\t"
        "s_waitcnt lgkmcnt(0)\n\t"
        "v_cmp_gt_u16 vcc, 1, v0\n\t"
        "s_cbranch_vccz .L7_d30\n\t"
        "s_setprio 0\n\t"
        "s_sleep 1\n\t"
        "s_sub_i32 s83, s83, 1\n\t"
        "s_cmp_gt_i32 s83, 0\n\t"
        "s_cbranch_scc1 .L7_w29\n"
        ".L7_d30:\n\t"
        "s_setprio 2\n\t"
        "buffer_load_dwordx4 v[48:51], v63, s[20:23], s27 offen sc1\n\t"
        "buffer_load_dwordx4 v[52:55], v62, s[20:23], s28 offen sc1\n\t"
        "buffer_load_dwordx4 v[56:59], v62, s[20:23], s29 offen sc1\n"
        ".L7_w31:\n\t"
        "ds_read_b32 v0, v2 offset:64\n\t"
        "s_waitcnt lgkmcnt(0)\n\t"
        "v_cmp_gt_u16 vcc, 4, v0\n\t"
        "s_cbranch_vccz .L7_d32\n\t"
        "s_setprio 0\n\t"
        "s_sleep 1\n\t"
        "s_sub_i32 s83, s83, 1\n\t"
        "s_cmp_gt_i32 s83, 0\n\t"
        "s_cbranch_scc1 .L7_w31\n"
        ".L7_d32:\n\t"
        "s_setprio 2\n\t"
        "ds_read_b128 v[8:11], v4 offset:38144\n\t"
        "ds_read_b128 v[12:15], v5 offset:38144\n\t"
        "ds_read_b128 v[16:19], v6 offset:38144\n\t"
        "ds_read_b128 v[20:23], v7 offset:38144\n\t"
        "s_waitcnt lgkmcnt(2)\n\t"
        "v_mfma_f32_16x16x32_bf16 v[32:35], v[96:99], v[8:11], v[32:35]\n\t"
        "v_mfma_f32_16x16x32_bf16 v[36:39], v[100:103], v[8:11], v[36:39]\n\t"
        "v_mfma_f32_16x16x32_bf16 v[32:35], v[104:107], v[12:15], v[32:35]\n\t"
        "v_mfma_f32_16x16x32_bf16 v[36:39], v[108:111], v[12:15], v[36:39]\n\t"
        "s_waitcnt lgkmcnt(0)\n\t"
        "v_mfma_f32_16x16x32_bf16 v[32:35], v[112:115], v[16:19], v[32:35]\n\t"
        "v_mfma_f32_16x16x32_bf16 v[36:39], v[116:119], v[16:19], v[36:39]\n\t"
        "v_mfma_f32_16x16x32_bf16 v[32:35], v[120:123], v[20:23], v[32:35]\n\t"
        "v_mfma_f32_16x16x32_bf16 v[36:39], v[124:127], v[20:23], v[36:39]\n\t"
        "s_waitcnt vmcnt(0)\n\t"
        "s_branch .L7_s34\n"
        ".L7_r33:\n\t"
        "buffer_load_dwordx4 v[48:51], v63, s[20:23], s27 offen sc1\n\t"
        "buffer_load_dwordx4 v[52:55], v62, s[20:23], s28 offen sc1\n\t"
        "buffer_load_dwordx4 v[56:59], v62, s[20:23], s29 offen sc1\n\t"
        "s_waitcnt vmcnt(0)\n"
        ".L7_s34:\n\t"
        "v_xor_b32 v48, v48, v60\n\t"
        "v_xor_b32 v49, v49, v61\n\t"
        "v_xor_b32 v52, v52, v60\n\t"
        "v_xor_b32 v53, v53, v61\n\t"
        "v_xor_b32 v56, v56, v60\n\t"
        "v_xor_b32 v57, v57, v61\n\t"
        "v_or_b32 v0, v48, v49\n\t"
        "v_or3_b32 v0, v0, v52, v53\n\t"
        "v_or3_b32 v0, v0, v56, v57\n\t"
        "v_and_b32 v0, s49, v0\n\t"
        "v_cmp_ne_u32 vcc, 0, v0\n\t"
        "s_cbranch_vccz .L7_t35\n\t"
        "s_sleep 8\n\t"
        "s_sub_i32 s83, s83, 1\n\t"
        "s_cmp_gt_i32 s83, 0\n\t"
        "s_cbranch_scc1 .L7_r33\n"
        ".L7_t35:\n\t"
        "v_mfma_f32_16x16x32_bf16 v[24:27], v[76:79], v[48:51], v[24:27]\n\t"
        "v_mfma_f32_16x16x32_bf16 v[28:31], v[74:77], v[48:51], v[28:31]\n\t"
        "v_mfma_f32_16x16x32_bf16 v[32:35], v[76:79], v[52:55], v[32:35]\n\t"
        "v_mfma_f32_16x16x32_bf16 v[36:39], v[74:77], v[52:55], v[36:39]\n\t"
        "v_mfma_f32_16x16x32_bf16 v[40:43], v[76:79], v[56:59], v[40:43]\n\t"
        "v_mfma_f32_16x16x32_bf16 v[44:47], v[74:77], v[56:59], v[44:47]\n\t"
        "s_nop 2\n\t"
        "v_cvt_pk_bf16_f32 v48, v24, v25\n\t"
        "v_cvt_pk_bf16_f32 v49, v26, v27\n\t"
        "v_cvt_pk_bf16_f32 v50, v28, v29\n\t"
        "v_cvt_pk_bf16_f32 v51, v30, v31\n\t"
        "ds_write_b128 v71, v[48:51]\n\t"
        "ds_read_b128 v[48:51], v72\n\t"
        "v_cvt_pk_bf16_f32 v52, v32, v33\n\t"
        "v_cvt_pk_bf16_f32 v53, v34, v35\n\t"
        "v_cvt_pk_bf16_f32 v54, v36, v37\n\t"
        "v_cvt_pk_bf16_f32 v55, v38, v39\n\t"
        "ds_write_b128 v71, v[52:55]\n\t"
        "ds_read_b128 v[52:55], v72\n\t"
        "ds_read_b32 v0, v2 offset:68\n\t"
        "ds_read_b128 v[8:11], v4 offset:42240\n\t"
        "ds_read_b128 v[12:15], v5 offset:42240\n\t"
        "ds_read_b128 v[16:19], v6 offset:42240\n\t"
        "ds_read_b128 v[20:23], v7 offset:42240\n\t"
        "s_waitcnt lgkmcnt(4)\n\t"
        "buffer_store_dwordx4 v[48:51], v68, s[60:63], s50 offen sc1\n\t"
        "buffer_store_dwordx4 v[52:55], v69, s[60:63], s50 offen sc1\n\t"
        "v_cmp_gt_u16 vcc, 4, v0\n\t"
        "s_cbranch_vccz .L7_t5\n"
        ".L7_w36:\n\t"
        "ds_read_b32 v0, v2 offset:68\n\t"
        "s_waitcnt lgkmcnt(0)\n\t"
        "v_cmp_gt_u16 vcc, 3, v0\n\t"
        "s_cbranch_vccz .L7_d37\n\t"
        "s_setprio 0\n\t"
        "s_sleep 1\n\t"
        "s_sub_i32 s83, s83, 1\n\t"
        "s_cmp_gt_i32 s83, 0\n\t"
        "s_cbranch_scc1 .L7_w36\n"
        ".L7_d37:\n\t"
        "s_setprio 2\n"
        ".L7_w38:\n\t"
        "ds_read_b32 v0, v2 offset:68\n\t"
        "ds_read_b128 v[8:11], v4 offset:42240\n\t"
        "ds_read_b128 v[12:15], v5 offset:42240\n\t"
        "ds_read_b128 v[16:19], v6 offset:42240\n\t"
        "ds_read_b128 v[20:23], v7 offset:42240\n\t"
        "s_waitcnt lgkmcnt(4)\n\t"
        "v_cmp_gt_u16 vcc, 4, v0\n\t"
        "s_cbranch_vccz .L7_d39\n\t"
        "s_waitcnt lgkmcnt(0)\n\t"
        "s_setprio 0\n\t"
        "s_sleep 0\n\t"
        "s_sub_i32 s83, s83, 1\n\t"
        "s_cmp_gt_i32 s83, 0\n\t"
        "s_cbranch_scc1 .L7_w38\n"
        ".L7_d39:\n\t"
        "s_setprio 2\n"
        ".L7_t5:\n\t"
        "s_waitcnt lgkmcnt(2)\n\t"
        "v_mfma_f32_16x16x32_bf16 v[40:43], v[96:99], v[8:11], v[40:43]\n\t"
        "v_mfma_f32_16x16x32_bf16 v[44:47], v[100:103], v[8:11], v[44:47]\n\t"
        "v_mfma_f32_16x16x32_bf16 v[40:43], v[104:107], v[12:15], v[40:43]\n\t"
        "v_mfma_f32_16x16x32_bf16 v[44:47], v[108:111], v[12:15], v[44:47]\n\t"
        "s_waitcnt lgkmcnt(0)\n\t"
        "v_mfma_f32_16x16x32_bf16 v[40:43], v[112:115], v[16:19], v[40:43]\n\t"
        "v_mfma_f32_16x16x32_bf16 v[44:47], v[116:119], v[16:19], v[44:47]\n\t"
        "v_mfma_f32_16x16x32_bf16 v[40:43], v[120:123], v[20:23], v[40:43]\n\t"
        "v_mfma_f32_16x16x32_bf16 v[44:47], v[124:127], v[20:23], v[44:47]\n\t"
        "s_nop 6\n\t"
        "v_cvt_pk_bf16_f32 v56, v40, v41\n\t"
        "v_cvt_pk_bf16_f32 v57, v42, v43\n\t"
        "v_cvt_pk_bf16_f32 v58, v44, v45\n\t"
        "v_cvt_pk_bf16_f32 v59, v46, v47\n\t"
        "ds_write_b128 v71, v[56:59]\n\t"
        "ds_read_b128 v[56:59], v72\n\t"
        "s_waitcnt lgkmcnt(0)\n\t"
        "buffer_store_dwordx4 v[56:59], v70, s[60:63], s50 offen sc1\n\t"
        "s_or_b32 s78, %3, s69\n\t"
        "s_cbranch_scc1 .L7_ng\n\t"
        "v_mov_b32 v0, s68\n\t"
        "buffer_store_dword v0, off, s[20:23], 0 sc1\n"
        ".L7_ng:\n\t"
        "s_endpgm\n"
        ".L7_G2:\n\t"
        "s_setprio 1\n\t"
        "v_lshlrev_b32 v5, 4, v1\n\t"
        "s_mul_i32 s82, s76, 13\n\t"
        "s_lshr_b32 s82, s82, 6\n\t"
        "s_mul_i32 s82, s82, 5\n\t"
        "s_sub_u32 s82, s76, s82\n\t"
        "s_waitcnt lgkmcnt(0)\n\t"
        "s_lshl2_add_u32 s78, s82, s69\n\t"
        "s_lshl_b32 s78, s78, 10\n\t"
        "s_add_u32 m0, s78, 46336\n\t"
        "s_add_u32 s82, s82, 1\n\t"
        "s_cmp_eq_u32 s82, 5\n\t"
        "buffer_load_dwordx4 v5, s[56:59], s78 offen lds\n\t"
        "s_cselect_b32 s82, 0, s82\n\t"
        "v_lshrrev_b32 v18, 3, v1\n\t"
        "v_lshlrev_b32 v18, 14, v18\n\t"
        "v_and_b32 v6, 0x70, v5\n\t"
        "s_lshl_b32 s78, s69, 19\n\t"
        "s_lshl_b32 s79, s71, 7\n\t"
        "s_add_u32 s78, s78, s79\n\t"
        "v_add3_u32 v18, v18, v6, s78\n\t"
        "s_mov_b32 s24, 1048576\n\t"
        "s_mov_b32 s25, 1179648\n\t"
        "s_mov_b32 s26, 1310720\n\t"
        "s_mov_b32 s27, 1441792\n\t"
        "v_and_b32 v6, 0xf0, v5\n\t"
        "s_lshl_b32 s78, s70, 8\n\t"
        "v_add_u32 v62, s78, v6\n\t"
        "v_mov_b32 v7, 113860\n\t"
        "s_movk_i32 s38, 196\n\t"
        "s_mov_b32 s39, 12845056\n\t"
        "s_lshl_b32 s33, s69, 3\n\t"
        "s_lshl_b32 s78, 1, s33\n\t"
        "v_mov_b32 v29, s78\n\t"
        "s_sub_u32 s32, s78, 1\n\t"
        "s_and_b32 s32, s32, 0x01010101\n\t"
        "s_mov_b32 s35, 0xffffff\n\t"
        "s_mov_b32 s40, 0x13050109\n\t"
        "s_mov_b32 s41, 23\n\t"
        "s_cmp_eq_u32 s69, 1\n\t"
        "s_cselect_b32 s40, 0x12040008, s40\n\t"
        "s_cselect_b32 s41, 22, s41\n\t"
        "s_cmp_eq_u32 s69, 2\n\t"
        "s_cselect_b32 s40, 0x100e0602, s40\n\t"
        "s_cselect_b32 s41, 20, s41\n\t"
        "s_cmp_eq_u32 s69, 3\n\t"
        "s_cselect_b32 s40, 0x110f0703, s40\n\t"
        "s_cselect_b32 s41, 21, s41\n\t"
        "s_mul_i32 s41, s41, 0x01010101\n\t"
        "s_bitcmp1_b32 s40, 3\n\t"
        "s_cselect_b32 s78, 0, 4\n\t"
        "v_mov_b32 v61, s78\n\t"
        "v_lshrrev_b32 v12, 4, v1\n\t"
        "s_bfe_u32 s78, s40, 0x80000\n\t"
        "s_lshl_b32 s78, s78, 2\n\t"
        "v_add_u32 v13, s78, v12\n\t"
        "v_lshlrev_b32 v24, 2, v13\n\t"
        "v_lshlrev_b32 v15, 4, v13\n\t"
        "v_and_b32 v15, 0xf0, v15\n\t"
        "v_xor_b32 v15, v15, v6\n\t"
        "v_lshl_add_u32 v56, v13, 8, v15\n\t"
        "s_bfe_u32 s78, s40, 0x80008\n\t"
        "s_lshl_b32 s78, s78, 2\n\t"
        "v_add_u32 v13, s78, v12\n\t"
        "v_lshlrev_b32 v25, 2, v13\n\t"
        "v_lshlrev_b32 v15, 4, v13\n\t"
        "v_and_b32 v15, 0xf0, v15\n\t"
        "v_xor_b32 v15, v15, v6\n\t"
        "v_lshl_add_u32 v57, v13, 8, v15\n\t"
        "s_bfe_u32 s78, s40, 0x80010\n\t"
        "s_lshl_b32 s78, s78, 2\n\t"
        "v_add_u32 v13, s78, v12\n\t"
        "v_lshlrev_b32 v26, 2, v13\n\t"
        "v_lshlrev_b32 v15, 4, v13\n\t"
        "v_and_b32 v15, 0xf0, v15\n\t"
        "v_xor_b32 v15, v15, v6\n\t"
        "v_lshl_add_u32 v58, v13, 8, v15\n\t"
        "v_lshlrev_b32 v15, 9, v61\n\t"
        "v_sub_u32 v58, v58, v15\n\t"
        "s_bfe_u32 s78, s40, 0x80018\n\t"
        "s_lshl_b32 s78, s78, 2\n\t"
        "v_add_u32 v13, s78, v12\n\t"
        "v_lshlrev_b32 v27, 2, v13\n\t"
        "v_lshlrev_b32 v15, 4, v13\n\t"
        "v_and_b32 v15, 0xf0, v15\n\t"
        "v_xor_b32 v15, v15, v6\n\t"
        "v_lshl_add_u32 v59, v13, 8, v15\n\t"
        "s_bfe_u32 s78, s41, 0x80000\n\t"
        "s_lshl_b32 s78, s78, 2\n\t"
        "v_add_u32 v13, s78, v12\n\t"
        "v_lshlrev_b32 v28, 2, v13\n\t"
        "v_lshlrev_b32 v15, 4, v13\n\t"
        "v_and_b32 v15, 0xf0, v15\n\t"
        "v_xor_b32 v15, v15, v6\n\t"
        "v_lshl_add_u32 v60, v13, 8, v15\n\t"
        "v_and_b32 v20, 28, v1\n\t"
        "v_lshlrev_b32 v20, 1, v20\n\t"
        "v_lshrrev_b64 v[12:13], v20, s[40:41]\n\t"
        "v_and_b32 v13, 0xff, v12\n\t"
        "v_and_b32 v12, 3, v1\n\t"
        "v_lshl_add_u32 v13, v13, 2, v12\n\t"
        "v_lshlrev_b32 v31, 2, v13\n\t"
        "s_and_b32 s78, s67, 0x10000\n\t"
        "s_or_b32 s78, s78, 1\n\t"
        "v_mov_b32 v30, s78\n\t"
        "s_mov_b32 s36, 0\n\t"
        "v_and_b32 v0, 48, v5\n\t"
        "s_mov_b32 s78, 0x0f0e\n\t"
        "s_mov_b32 s79, 0\n\t"
        "v_lshlrev_b64 v[8:9], v0, s[78:79]\n\t"
        "s_mov_b32 s78, 0x0b0a\n\t"
        "v_lshlrev_b64 v[12:13], v0, s[78:79]\n\t"
        "s_mov_b32 s78, 0xf0f0f0f0\n\t"
        "s_mov_b32 s79, s78\n\t"
        "v_cndmask_b32 v8, v8, v12, s[78:79]\n\t"
        "v_cndmask_b32 v9, v9, v13, s[78:79]\n\t"
        "v_xor_b32 v8, 0x0c0c0c0c, v8\n\t"
        "v_xor_b32 v9, 0x0c0c0c0c, v9\n\t"
        "s_movk_i32 s48, 0x200\n\t"
        "s_mov_b32 s37, 0x01010101\n\t"
        "s_mov_b32 s49, 0x05040100\n\t"
        "s_getpc_b64 s[50:51]\n"
        ".L7_pc:\n\t"
        "s_add_u32 s52, s50, .L7_ce-.L7_pc\n\t"
        "s_addc_u32 s53, s51, 0\n\t"
        "s_add_u32 s42, s50, .L7_ev-.L7_pc\n\t"
        "s_addc_u32 s43, s51, 0\n\t"
        "s_add_u32 s50, s50, .L7_ie-.L7_pc\n\t"
        "s_addc_u32 s51, s51, 0\n\t"
        "s_cmp_eq_u32 s80, 10\n\t"
        "s_cselect_b32 s80, 0, s80\n\t"
        "s_mul_i32 s78, s80, 0x1080\n\t"
        "s_add_u32 m0, s78, s81\n\t"
        "s_lshr_b32 s78, s80, 1\n\t"
        "s_lshl1_add_u32 s78, s78, s80\n\t"
        "s_lshl_b32 s78, s78, 11\n\t"
        "buffer_load_dwordx4 v4, s[16:19], s78 offen lds\n\t"
        "s_add_u32 s80, s80, 1\n\t"
        "s_mul_i32 s78, s80, 0x1080\n\t"
        "s_add_u32 m0, s78, s81\n\t"
        "s_lshr_b32 s78, s80, 1\n\t"
        "s_lshl1_add_u32 s78, s78, s80\n\t"
        "s_lshl_b32 s78, s78, 11\n\t"
        "buffer_load_dwordx4 v4, s[16:19], s78 offen lds\n\t"
        "s_add_u32 s80, s80, 1\n\t"
        "s_mul_i32 s78, s80, 0x1080\n\t"
        "s_add_u32 m0, s78, s81\n\t"
        "s_lshr_b32 s78, s80, 1\n\t"
        "s_lshl1_add_u32 s78, s78, s80\n\t"
        "s_lshl_b32 s78, s78, 11\n\t"
        "buffer_load_dwordx4 v4, s[16:19], s78 offen lds\n\t"
        "s_add_u32 s80, s80, 1\n\t"
        "s_mul_i32 s78, s80, 0x1080\n\t"
        "s_add_u32 m0, s78, s81\n\t"
        "s_lshr_b32 s78, s80, 1\n\t"
        "s_lshl1_add_u32 s78, s78, s80\n\t"
        "s_lshl_b32 s78, s78, 11\n\t"
        "buffer_load_dwordx4 v4, s[16:19], s78 offen lds\n\t"
        "s_add_u32 s80, s80, 1\n\t"
        "s_mul_i32 s78, s80, 0x1080\n\t"
        "s_add_u32 m0, s78, s81\n\t"
        "s_lshr_b32 s78, s80, 1\n\t"
        "s_lshl1_add_u32 s78, s78, s80\n\t"
        "s_lshl_b32 s78, s78, 11\n\t"
        "buffer_load_dwordx4 v4, s[16:19], s78 offen lds\n\t"
        "s_add_u32 s80, s80, 1\n\t"
        "s_lshl2_add_u32 s78, s82, s69\n\t"
        "s_lshl_b32 s78, s78, 10\n\t"
        "s_add_u32 m0, s78, 46336\n\t"
        "s_add_u32 s82, s82, 1\n\t"
        "s_cmp_eq_u32 s82, 5\n\t"
        "buffer_load_dwordx4 v5, s[56:59], s78 offen lds\n\t"
        "s_cselect_b32 s82, 0, s82\n\t"
        "s_lshl2_add_u32 s78, s82, s69\n\t"
        "s_lshl_b32 s78, s78, 10\n\t"
        "s_add_u32 m0, s78, 46336\n\t"
        "s_add_u32 s82, s82, 1\n\t"
        "s_cmp_eq_u32 s82, 5\n\t"
        "buffer_load_dwordx4 v5, s[56:59], s78 offen lds\n\t"
        "s_cselect_b32 s82, 0, s82\n\t"
        "s_lshl2_add_u32 s78, s82, s69\n\t"
        "s_lshl_b32 s78, s78, 10\n\t"
        "s_add_u32 m0, s78, 46336\n\t"
        "s_add_u32 s82, s82, 1\n\t"
        "s_cmp_eq_u32 s82, 5\n\t"
        "buffer_load_dwordx4 v5, s[56:59], s78 offen lds\n\t"
        "s_cselect_b32 s82, 0, s82\n\t"
        "s_lshl2_add_u32 s78, s82, s69\n\t"
        "s_lshl_b32 s78, s78, 10\n\t"
        "s_add_u32 m0, s78, 46336\n\t"
        "s_add_u32 s82, s82, 1\n\t"
        "s_cmp_eq_u32 s82, 5\n\t"
        "buffer_load_dwordx4 v5, s[56:59], s78 offen lds\n\t"
        "s_cselect_b32 s82, 0, s82\n\t"
        "s_mov_b64 exec, 1\n\t"
        "ds_add_u32 v2, v3 offset:80\n\t"
        "s_mov_b64 exec, -1\n\t"
        "s_waitcnt vmcnt(4)\n\t"
        "s_mov_b64 exec, 1\n\t"
        "ds_add_u32 v2, v3 offset:0\n\t"
        "s_mov_b64 exec, -1\n"
        ".L7_w74:\n\t"
        "ds_read_b64 v[12:13], v2 offset:72\n\t"
        "s_waitcnt lgkmcnt(0)\n\t"
        "v_cmp_gt_u16 vcc, 2, v12\n\t"
        "s_cbranch_vccz .L7_d75\n\t"
        "s_sleep 1\n\t"
        "s_sub_i32 s83, s83, 1\n\t"
        "s_cmp_gt_i32 s83, 0\n\t"
        "s_cbranch_scc1 .L7_w74\n"
        ".L7_d75:\n\t"
        "v_readfirstlane_b32 s68, v13\n"
        ".L7_w76:\n\t"
        "ds_read_b32 v0, v2 offset:8\n\t"
        "s_waitcnt lgkmcnt(0)\n\t"
        "v_cmp_gt_u16 vcc, 2, v0\n\t"
        "s_cbranch_vccz .L7_d77\n\t"
        "s_sleep 1\n\t"
        "s_sub_i32 s83, s83, 1\n\t"
        "s_cmp_gt_i32 s83, 0\n\t"
        "s_cbranch_scc1 .L7_w76\n"
        ".L7_d77:\n\t"
        "ds_read_b32 v5, v31 offset:512\n\t"
        "ds_read_b32 v24, v24 offset:512\n\t"
        "ds_read_b32 v25, v25 offset:512\n\t"
        "ds_read_b32 v26, v26 offset:512\n\t"
        "ds_read_b32 v27, v27 offset:512\n\t"
        "ds_read_b32 v28, v28 offset:512\n\t"
        "s_waitcnt lgkmcnt(0)\n\t"
        "v_dot4_u32_u8 v31, v5, s38, v7\n\t"
        "s_waitcnt vmcnt(0)\n\t"
        "s_mov_b64 exec, 1\n\t"
        "ds_add_u32 v2, v3 offset:4\n\t"
        "s_mov_b64 exec, -1\n\t"
        "s_setprio 3\n\t"
        "s_lshl_b32 s44, s68, 24\n\t"
        "s_xor_b32 s45, s44, 0x5a000000\n\t"
        "s_mul_i32 s46, s68, 0x01010000\n\t"
        "s_mul_i32 s47, s68, 0x4000400\n\t"
        "s_and_b32 s47, s47, 0xff000000\n\t"
        "s_or_b32 s47, s47, 0xff0000\n"
        ".L7_w78:\n\t"
        "ds_read_b32 v0, v2 offset:92\n\t"
        "s_waitcnt lgkmcnt(0)\n\t"
        "v_cmp_gt_u16 vcc, 1, v0\n\t"
        "s_cbranch_vccz .L7_d79\n\t"
        "s_sleep 1\n\t"
        "s_sub_i32 s83, s83, 1\n\t"
        "s_cmp_gt_i32 s83, 0\n\t"
        "s_cbranch_scc1 .L7_w78\n"
        ".L7_d79:\n\t"
        "v_readfirstlane_b32 s78, v0\n\t"
        "s_sub_u32 s78, s78, 1\n\t"
        "s_add_u32 s77, s74, s78\n"
        ".L7_g80:\n\t"
        "s_memrealtime s[78:79]\n\t"
        "s_waitcnt lgkmcnt(0)\n\t"
        "s_sub_u32 s78, s78, s77\n\t"
        "s_cmp_ge_i32 s78, 315\n\t"
        "s_cbranch_scc1 .L7_h81\n\t"
        "s_sleep 1\n\t"
        "s_branch .L7_g80\n"
        ".L7_h81:\n\t"
        "s_branch .L7_ll\n"
        ".L7_again:\n\t"
        "s_waitcnt lgkmcnt(0)\n\t"
        "s_sleep 8\n\t"
        "s_sub_i32 s83, s83, 16\n\t"
        "s_cmp_gt_i32 s83, 0\n\t"
        "s_cbranch_scc0 .L7_got\n"
        ".L7_ll:\n\t"
        "buffer_load_dwordx4 v[64:67], v18, s[20:23], s24 offen sc1\n\t"
        "buffer_load_dwordx4 v[68:71], v18, s[20:23], s25 offen sc1\n\t"
        "buffer_load_dwordx4 v[72:75], v18, s[20:23], s26 offen sc1\n\t"
        "buffer_load_dwordx4 v[76:79], v18, s[20:23], s27 offen sc1\n\t"
        "s_mov_b64 exec, 1\n\t"
        "ds_add_u32 v2, v3 offset:108\n\t"
        "s_mov_b64 exec, -1\n"
        ".L7_w82:\n\t"
        "ds_read_b32 v0, v2 offset:4\n\t"
        "s_waitcnt lgkmcnt(0)\n\t"
        "v_cmp_gt_u16 vcc, 4, v0\n\t"
        "s_cbranch_vccz .L7_d83\n\t"
        "s_sleep 1\n\t"
        "s_sub_i32 s83, s83, 1\n\t"
        "s_cmp_gt_i32 s83, 0\n\t"
        "s_cbranch_scc1 .L7_w82\n"
        ".L7_d83:\n\t"
        "s_waitcnt vmcnt(3)\n\t"
        "v_xor_b32 v5, s44, v64\n\t"
        "v_bitop3_b32 v5, v65, v5, s45 bitop3:0xde\n\t"
        "v_bitop3_b32 v5, v66, v5, s46 bitop3:0xde\n\t"
        "v_xor_b32 v4, s47, v67\n\t"
        "v_lshlrev_b32_sdwa v112, 1, v64 dst_sel:DWORD dst_unused:UNUSED_PAD src0_sel:DWORD src1_sel:WORD_0\n\t"
        "ds_read_u16 v112, v112 offset:46336\n\t"
        "v_lshlrev_b32_sdwa v113, 1, v65 dst_sel:DWORD dst_unused:UNUSED_PAD src0_sel:DWORD src1_sel:WORD_0\n\t"
        "ds_read_u16 v113, v113 offset:46336\n\t"
        "v_lshlrev_b32_sdwa v114, 1, v66 dst_sel:DWORD dst_unused:UNUSED_PAD src0_sel:DWORD src1_sel:WORD_0\n\t"
        "ds_read_u16 v114, v114 offset:46336\n\t"
        "v_dot4_u32_u8 v80, v64, s39, v7\n\t"
        "v_dot4_u32_u8 v81, v65, s39, v7\n\t"
        "v_dot4_u32_u8 v82, v66, s39, v7\n\t"
        "s_waitcnt vmcnt(2)\n\t"
        "v_bitop3_b32 v5, v68, v5, s44 bitop3:0xde\n\t"
        "v_bitop3_b32 v5, v69, v5, s45 bitop3:0xde\n\t"
        "v_bitop3_b32 v5, v70, v5, s46 bitop3:0xde\n\t"
        "v_bitop3_b32 v4, v71, v4, s47 bitop3:0xde\n\t"
        "v_lshlrev_b32_sdwa v116, 1, v68 dst_sel:DWORD dst_unused:UNUSED_PAD src0_sel:DWORD src1_sel:WORD_0\n\t"
        "ds_read_u16 v116, v116 offset:46336\n\t"
        "v_lshlrev_b32_sdwa v117, 1, v69 dst_sel:DWORD dst_unused:UNUSED_PAD src0_sel:DWORD src1_sel:WORD_0\n\t"
        "ds_read_u16 v117, v117 offset:46336\n\t"
        "v_lshlrev_b32_sdwa v118, 1, v70 dst_sel:DWORD dst_unused:UNUSED_PAD src0_sel:DWORD src1_sel:WORD_0\n\t"
        "ds_read_u16 v118, v118 offset:46336\n\t"
        "v_dot4_u32_u8 v84, v68, s39, v7\n\t"
        "v_dot4_u32_u8 v85, v69, s39, v7\n\t"
        "v_dot4_u32_u8 v86, v70, s39, v7\n\t"
        "s_waitcnt vmcnt(1)\n\t"
        "v_bitop3_b32 v5, v72, v5, s44 bitop3:0xde\n\t"
        "v_bitop3_b32 v5, v73, v5, s45 bitop3:0xde\n\t"
        "v_bitop3_b32 v5, v74, v5, s46 bitop3:0xde\n\t"
        "v_bitop3_b32 v4, v75, v4, s47 bitop3:0xde\n\t"
        "v_lshlrev_b32_sdwa v120, 1, v72 dst_sel:DWORD dst_unused:UNUSED_PAD src0_sel:DWORD src1_sel:WORD_0\n\t"
        "ds_read_u16 v120, v120 offset:46336\n\t"
        "v_lshlrev_b32_sdwa v121, 1, v73 dst_sel:DWORD dst_unused:UNUSED_PAD src0_sel:DWORD src1_sel:WORD_0\n\t"
        "ds_read_u16 v121, v121 offset:46336\n\t"
        "v_lshlrev_b32_sdwa v122, 1, v74 dst_sel:DWORD dst_unused:UNUSED_PAD src0_sel:DWORD src1_sel:WORD_0\n\t"
        "ds_read_u16 v122, v122 offset:46336\n\t"
        "v_dot4_u32_u8 v88, v72, s39, v7\n\t"
        "v_dot4_u32_u8 v89, v73, s39, v7\n\t"
        "v_dot4_u32_u8 v90, v74, s39, v7\n\t"
        "s_waitcnt vmcnt(0)\n\t"
        "v_bitop3_b32 v5, v76, v5, s44 bitop3:0xde\n\t"
        "v_bitop3_b32 v5, v77, v5, s45 bitop3:0xde\n\t"
        "v_bitop3_b32 v5, v78, v5, s46 bitop3:0xde\n\t"
        "v_bitop3_b32 v4, v79, v4, s47 bitop3:0xde\n\t"
        "v_lshlrev_b32_sdwa v124, 1, v76 dst_sel:DWORD dst_unused:UNUSED_PAD src0_sel:DWORD src1_sel:WORD_0\n\t"
        "ds_read_u16 v124, v124 offset:46336\n\t"
        "v_lshlrev_b32_sdwa v125, 1, v77 dst_sel:DWORD dst_unused:UNUSED_PAD src0_sel:DWORD src1_sel:WORD_0\n\t"
        "ds_read_u16 v125, v125 offset:46336\n\t"
        "v_lshlrev_b32_sdwa v126, 1, v78 dst_sel:DWORD dst_unused:UNUSED_PAD src0_sel:DWORD src1_sel:WORD_0\n\t"
        "ds_read_u16 v126, v126 offset:46336\n\t"
        "v_dot4_u32_u8 v92, v76, s39, v7\n\t"
        "v_dot4_u32_u8 v93, v77, s39, v7\n\t"
        "v_dot4_u32_u8 v94, v78, s39, v7\n\t"
        "v_and_b32 v6, 0xfc000000, v4\n\t"
        "v_or_b32 v5, v5, v6\n\t"
        "v_cmp_lt_u32 vcc, s35, v5\n\t"
        "s_cbranch_vccnz .L7_again\n"
        ".L7_got:\n\t"
        "v_and_b32 v6, s35, v4\n\t"
        "s_waitcnt lgkmcnt(2)\n\t"
        "ds_add_rtn_u32 v96, v80, v29 offset:192\n\t"
        "ds_add_rtn_u32 v97, v81, v29 offset:192\n\t"
        "ds_add_rtn_u32 v98, v82, v29 offset:192\n\t"
        "ds_add_rtn_u32 v100, v84, v29 offset:192\n\t"
        "ds_add_rtn_u32 v101, v85, v29 offset:192\n\t"
        "ds_add_rtn_u32 v102, v86, v29 offset:192\n\t"
        "ds_add_rtn_u32 v104, v88, v29 offset:192\n\t"
        "ds_add_rtn_u32 v105, v89, v29 offset:192\n\t"
        "ds_add_rtn_u32 v106, v90, v29 offset:192\n\t"
        "ds_add_rtn_u32 v108, v92, v29 offset:192\n\t"
        "ds_add_rtn_u32 v109, v93, v29 offset:192\n\t"
        "ds_add_rtn_u32 v110, v94, v29 offset:192\n\t"
        "v_cmp_ne_u32 vcc, 0, v6\n\t"
        "s_cbranch_vccnz .L7_x\n"
        ".L7_y:\n\t"
        "s_mov_b64 exec, 1\n\t"
        "ds_add_u32 v2, v3 offset:32\n\t"
        "s_mov_b64 exec, -1\n\t"
        "v_perm_b32 v112, v112, v64, s49\n\t"
        "v_perm_b32 v113, v113, v65, s49\n\t"
        "v_perm_b32 v114, v114, v66, s49\n\t"
        "v_perm_b32 v116, v116, v68, s49\n\t"
        "v_perm_b32 v117, v117, v69, s49\n\t"
        "v_perm_b32 v118, v118, v70, s49\n\t"
        "v_perm_b32 v120, v120, v72, s49\n\t"
        "v_perm_b32 v121, v121, v73, s49\n\t"
        "v_perm_b32 v122, v122, v74, s49\n\t"
        "v_perm_b32 v124, v124, v76, s49\n\t"
        "s_cmp_lt_u32 s69, 1\n\t"
        "s_cbranch_scc0 .L7_nv\n\t"
        "s_mov_b32 s79, 0\n\t"
        "v_xor_b32 v32, s47, v67\n\t"
        "v_xor_b32 v33, s47, v71\n\t"
        "v_xor_b32 v34, s47, v75\n\t"
        "v_xor_b32 v35, s47, v79\n\t"
        "v_lshlrev_b32 v32, 6, v32\n\t"
        "v_lshlrev_b32 v33, 6, v33\n\t"
        "v_lshlrev_b32 v34, 6, v34\n\t"
        "v_lshlrev_b32 v35, 6, v35\n\t"
        "s_mov_b32 exec_lo, s37\n\t"
        "s_mov_b32 exec_hi, s37\n\t"
        "v_cmp_gt_i32 vcc, 0, v32\n\t"
        "s_bcnt1_i32_b64 s78, vcc\n\t"
        "s_add_u32 s79, s79, s78\n\t"
        "v_cmp_gt_i32 vcc, 0, v33\n\t"
        "s_bcnt1_i32_b64 s78, vcc\n\t"
        "s_add_u32 s79, s79, s78\n\t"
        "v_cmp_gt_i32 vcc, 0, v34\n\t"
        "s_bcnt1_i32_b64 s78, vcc\n\t"
        "s_add_u32 s79, s79, s78\n\t"
        "v_cmp_gt_i32 vcc, 0, v35\n\t"
        "s_bcnt1_i32_b64 s78, vcc\n\t"
        "s_add_u32 s79, s79, s78\n\t"
        "s_lshl_b32 s79, s79, 16\n\t"
        "s_or_b32 s79, s79, 1\n\t"
        "v_mov_b32 v32, s79\n\t"
        "s_mov_b64 exec, 1\n\t"
        "ds_add_u32 v2, v32 offset:116\n\t"
        "s_mov_b64 exec, -1\n"
        ".L7_nv:\n\t"
        "v_lshlrev_b32 v4, 7, v4\n\t"
        "v_cmp_gt_i32 vcc, 0, v4\n\t"
        "s_cmp_lg_u64 vcc, 0\n\t"
        "s_cselect_b32 s78, 0x100000, 0\n\t"
        "v_or_b32 v30, s78, v30\n\t"
        "s_waitcnt lgkmcnt(1)\n\t"
        "v_perm_b32 v125, v125, v77, s49\n\t"
        "v_perm_b32 v126, v126, v78, s49\n\t"
        "v_bfe_u32 v96, v96, s33, 8\n\t"
        "v_bfe_u32 v97, v97, s33, 8\n\t"
        "v_bfe_u32 v98, v98, s33, 8\n\t"
        "v_bfe_u32 v100, v100, s33, 8\n\t"
        "v_bfe_u32 v101, v101, s33, 8\n\t"
        "v_bfe_u32 v102, v102, s33, 8\n\t"
        "v_bfe_u32 v104, v104, s33, 8\n\t"
        "v_bfe_u32 v105, v105, s33, 8\n\t"
        "v_bfe_u32 v106, v106, s33, 8\n\t"
        "v_bfe_u32 v108, v108, s33, 8\n\t"
        "v_bfe_u32 v109, v109, s33, 8\n\t"
        "v_bfe_u32 v110, v110, s33, 8\n"
        ".L7_w84:\n\t"
        "ds_read_b32 v0, v2 offset:32\n\t"
        "s_waitcnt lgkmcnt(0)\n\t"
        "v_cmp_gt_u16 vcc, 4, v0\n\t"
        "s_cbranch_vccz .L7_d85\n\t"
        "s_sleep 1\n\t"
        "s_sub_i32 s83, s83, 1\n\t"
        "s_cmp_gt_i32 s83, 0\n\t"
        "s_cbranch_scc1 .L7_w84\n"
        ".L7_d85:\n\t"
        "ds_read_b32 v32, v80 offset:192\n\t"
        "ds_read_b32 v33, v81 offset:192\n\t"
        "ds_read_b32 v34, v82 offset:192\n\t"
        "ds_read_b32 v35, v84 offset:192\n\t"
        "ds_read_b32 v36, v85 offset:192\n\t"
        "ds_read_b32 v37, v86 offset:192\n\t"
        "ds_read_b32 v38, v88 offset:192\n\t"
        "ds_read_b32 v39, v89 offset:192\n\t"
        "ds_read_b32 v40, v90 offset:192\n\t"
        "ds_read_b32 v41, v92 offset:192\n\t"
        "ds_read_b32 v42, v93 offset:192\n\t"
        "ds_read_b32 v43, v94 offset:192\n\t"
        "ds_read_b32 v6, v31 offset:192\n\t"
        "s_waitcnt lgkmcnt(10)\n\t"
        "v_dot4_u32_u8 v96, v32, s32, v96\n\t"
        "v_dot4_u32_u8 v97, v33, s32, v97\n\t"
        "v_dot4_u32_u8 v98, v34, s32, v98\n\t"
        "s_waitcnt lgkmcnt(7)\n\t"
        "v_dot4_u32_u8 v100, v35, s32, v100\n\t"
        "v_dot4_u32_u8 v101, v36, s32, v101\n\t"
        "v_dot4_u32_u8 v102, v37, s32, v102\n\t"
        "s_waitcnt lgkmcnt(4)\n\t"
        "v_dot4_u32_u8 v104, v38, s32, v104\n\t"
        "v_dot4_u32_u8 v105, v39, s32, v105\n\t"
        "v_dot4_u32_u8 v106, v40, s32, v106\n\t"
        "s_waitcnt lgkmcnt(1)\n\t"
        "v_dot4_u32_u8 v108, v41, s32, v108\n\t"
        "v_dot4_u32_u8 v109, v42, s32, v109\n\t"
        "v_dot4_u32_u8 v110, v43, s32, v110\n\t"
        "v_min_u32 v96, 47, v96\n\t"
        "v_min_u32 v97, 47, v97\n\t"
        "v_min_u32 v98, 47, v98\n\t"
        "v_min_u32 v100, 47, v100\n\t"
        "v_min_u32 v101, 47, v101\n\t"
        "v_min_u32 v102, 47, v102\n\t"
        "v_min_u32 v104, 47, v104\n\t"
        "v_min_u32 v105, 47, v105\n\t"
        "v_min_u32 v106, 47, v106\n\t"
        "v_min_u32 v108, 47, v108\n\t"
        "v_min_u32 v109, 47, v109\n\t"
        "v_min_u32 v110, 47, v110\n\t"
        "v_lshl_add_u32 v96, v96, 2, v80\n\t"
        "ds_write_b32 v96, v112\n\t"
        "v_lshl_add_u32 v97, v97, 2, v81\n\t"
        "ds_write_b32 v97, v113\n\t"
        "v_lshl_add_u32 v98, v98, 2, v82\n\t"
        "ds_write_b32 v98, v114\n\t"
        "v_lshl_add_u32 v100, v100, 2, v84\n\t"
        "ds_write_b32 v100, v116\n\t"
        "v_lshl_add_u32 v101, v101, 2, v85\n\t"
        "ds_write_b32 v101, v117\n\t"
        "v_lshl_add_u32 v102, v102, 2, v86\n\t"
        "ds_write_b32 v102, v118\n\t"
        "v_lshl_add_u32 v104, v104, 2, v88\n\t"
        "ds_write_b32 v104, v120\n\t"
        "v_lshl_add_u32 v105, v105, 2, v89\n\t"
        "ds_write_b32 v105, v121\n\t"
        "v_lshl_add_u32 v106, v106, 2, v90\n\t"
        "ds_write_b32 v106, v122\n\t"
        "v_lshl_add_u32 v108, v108, 2, v92\n\t"
        "ds_write_b32 v108, v124\n\t"
        "v_lshl_add_u32 v109, v109, 2, v93\n\t"
        "ds_write_b32 v109, v125\n\t"
        "v_lshl_add_u32 v110, v110, 2, v94\n\t"
        "ds_write_b32 v110, v126\n\t"
        "s_cmp_lg_u32 s36, 0\n\t"
        "s_cbranch_scc1 .L7_z\n"
        ".L7_zr:\n\t"
        "s_mov_b64 exec, 1\n\t"
        "ds_add_u32 v2, v30 offset:36\n\t"
        "s_mov_b64 exec, -1\n\t"
        "s_waitcnt lgkmcnt(13)\n\t"
        "v_dot4_u32_u8 v6, v6, s37, -1\n\t"
        "v_mov_b64 v[32:33], 0\n\t"
        "v_mov_b64 v[34:35], 0\n\t"
        "v_mov_b64 v[36:37], 0\n\t"
        "v_med3_i32 v6, v6, 0, 47\n\t"
        "v_lshrrev_b32 v6, 1, v6\n\t"
        "v_mov_b64 v[38:39], 0\n\t"
        "v_dot4_u32_u8 v63, v24, s38, v7\n\t"
        "v_max_u32_dpp v6, v6, v6 quad_perm:[1,0,3,2] row_mask:0xf bank_mask:0xf\n\t"
        "s_nop 1\n\t"
        "v_max_u32_dpp v6, v6, v6 quad_perm:[2,3,0,1] row_mask:0xf bank_mask:0xf\n\t"
        "s_nop 0\n\t"
        "v_readlane_b32 s24, v6, 0\n\t"
        "v_readlane_b32 s25, v6, 4\n\t"
        "v_readlane_b32 s26, v6, 8\n\t"
        "v_readlane_b32 s27, v6, 12\n\t"
        "v_readlane_b32 s28, v6, 16\n\t"
        "s_max_u32 s28, s28, 5\n\t"
        "s_mov_b32 s56, s24\n\t"
        "s_mov_b32 s57, s24\n\t"
        "s_mov_b32 s78, 0xf0000\n"
        ".L7_w86:\n\t"
        "ds_read_b32 v4, v2 offset:36\n\t"
        "ds_read_b32 v12, v31 offset:188\n\t"
        "s_waitcnt lgkmcnt(1)\n\t"
        "v_cmp_gt_u16 vcc, 4, v4\n\t"
        "s_cbranch_vccz .L7_d87\n\t"
        "s_waitcnt lgkmcnt(0)\n\t"
        "s_sleep 1\n\t"
        "s_sub_i32 s83, s83, 1\n\t"
        "s_cmp_gt_i32 s83, 0\n\t"
        "s_cbranch_scc1 .L7_w86\n"
        ".L7_d87:\n\t"
        "ds_read2_b32 v[40:41], v63 offset0:0 offset1:1\n\t"
        "s_sub_u32 s56, s56, 1\n\t"
        "s_cbranch_scc1 .L7_i88\n"
        ".L7_j89:\n\t"
        "ds_read2_b32 v[42:43], v63 offset0:2 offset1:3\n\t"
        "s_sub_u32 s56, s56, 1\n\t"
        "s_cbranch_scc1 .L7_i90\n"
        ".L7_j91:\n\t"
        "ds_read2_b32 v[44:45], v63 offset0:4 offset1:5\n\t"
        "s_sub_u32 s56, s56, 1\n\t"
        "s_cbranch_scc1 .L7_i92\n"
        ".L7_j93:\n\t"
        "ds_read2_b32 v[46:47], v63 offset0:6 offset1:7\n\t"
        "s_sub_u32 s56, s56, 1\n\t"
        "s_cbranch_scc1 .L7_i94\n"
        ".L7_j95:\n\t"
        "ds_read2_b32 v[48:49], v63 offset0:8 offset1:9\n\t"
        "s_sub_u32 s56, s56, 1\n\t"
        "s_cbranch_scc1 .L7_i96\n"
        ".L7_j97:\n\t"
        "ds_read2_b32 v[50:51], v63 offset0:10 offset1:11\n\t"
        "s_sub_u32 s56, s56, 1\n\t"
        "s_cbranch_scc1 .L7_i98\n"
        ".L7_j99:\n\t"
        "ds_read2_b32 v[52:53], v63 offset0:12 offset1:13\n\t"
        "s_sub_u32 s56, s56, 1\n\t"
        "s_cbranch_scc1 .L7_i100\n"
        ".L7_j101:\n\t"
        "ds_read2_b32 v[54:55], v63 offset0:14 offset1:15\n\t"
        "s_sub_u32 s56, s56, 1\n\t"
        "s_cbranch_scc1 .L7_i102\n"
        ".L7_j103:\n\t"
        "s_waitcnt lgkmcnt(8)\n\t"
        "v_and_or_b32 v12, v4, s78, v12\n\t"
        "v_cmp_ne_u32 vcc, 0xffff, v12\n\t"
        "s_cbranch_vccnz .L7_exit4\n\t"
        "s_waitcnt lgkmcnt(7)\n\t"
        "v_mad_u32_u16 v10, v40, s48, v62\n\t"
        "v_mad_u32_u16 v11, v41, s48, v62\n\t"
        "buffer_load_dwordx4 v[64:67], v10, s[16:19], 0 offen\n\t"
        "buffer_load_dwordx4 v[68:71], v11, s[16:19], 0 offen\n\t"
        "s_waitcnt lgkmcnt(6)\n\t"
        "v_mad_u32_u16 v10, v42, s48, v62\n\t"
        "v_mad_u32_u16 v11, v43, s48, v62\n\t"
        "buffer_load_dwordx4 v[72:75], v10, s[16:19], 0 offen\n\t"
        "buffer_load_dwordx4 v[76:79], v11, s[16:19], 0 offen\n\t"
        "s_waitcnt lgkmcnt(5)\n\t"
        "v_mad_u32_u16 v10, v44, s48, v62\n\t"
        "v_mad_u32_u16 v11, v45, s48, v62\n\t"
        "buffer_load_dwordx4 v[80:83], v10, s[16:19], 0 offen\n\t"
        "buffer_load_dwordx4 v[84:87], v11, s[16:19], 0 offen\n\t"
        "s_waitcnt lgkmcnt(4)\n\t"
        "v_mad_u32_u16 v10, v46, s48, v62\n\t"
        "v_mad_u32_u16 v11, v47, s48, v62\n\t"
        "buffer_load_dwordx4 v[88:91], v10, s[16:19], 0 offen\n\t"
        "buffer_load_dwordx4 v[92:95], v11, s[16:19], 0 offen\n\t"
        "s_waitcnt lgkmcnt(3)\n\t"
        "v_mad_u32_u16 v10, v48, s48, v62\n\t"
        "v_mad_u32_u16 v11, v49, s48, v62\n\t"
        "buffer_load_dwordx4 v[96:99], v10, s[16:19], 0 offen\n\t"
        "buffer_load_dwordx4 v[100:103], v11, s[16:19], 0 offen\n\t"
        "s_waitcnt lgkmcnt(2)\n\t"
        "v_mad_u32_u16 v10, v50, s48, v62\n\t"
        "v_mad_u32_u16 v11, v51, s48, v62\n\t"
        "buffer_load_dwordx4 v[104:107], v10, s[16:19], 0 offen\n\t"
        "buffer_load_dwordx4 v[108:111], v11, s[16:19], 0 offen\n\t"
        "s_min_u32 s59, s56, s57\n\t"
        "s_add_u32 s58, s59, 1\n\t"
        "s_setprio 3\n"
        ".L7_loop:\n\t"
        "v_add_u32 v63, 64, v63\n\t"
        "v_perm_b32 v12, v41, v40, v8\n\t"
        "v_perm_b32 v13, v41, v40, v9\n\t"
        "s_waitcnt vmcnt(10) lgkmcnt(1)\n\t"
        "v_mad_u32_u16 v10, v52, s48, v62\n\t"
        "v_mad_u32_u16 v11, v53, s48, v62\n\t"
        "buffer_load_dwordx4 v[112:115], v10, s[16:19], 0 offen\n\t"
        "buffer_load_dwordx4 v[116:119], v11, s[16:19], 0 offen\n\t"
        "v_mfma_f32_4x4x4_16b_bf16 v[32:35], v[12:13], v[64:65], v[32:35] cbsz:1 abid:0\n\t"
        "v_mfma_f32_4x4x4_16b_bf16 v[36:39], v[12:13], v[66:67], v[36:39] cbsz:1 abid:0\n\t"
        "ds_read2_b32 v[40:41], v63 offset0:0 offset1:1\n\t"
        "v_mfma_f32_4x4x4_16b_bf16 v[32:35], v[12:13], v[68:69], v[32:35] cbsz:1 abid:1\n\t"
        "v_mfma_f32_4x4x4_16b_bf16 v[36:39], v[12:13], v[70:71], v[36:39] cbsz:1 abid:1\n\t"
        "s_sub_u32 s59, s59, 1\n\t"
        "s_cbranch_scc1 .L7_i104\n"
        ".L7_j105:\n\t"
        "v_perm_b32 v12, v43, v42, v8\n\t"
        "v_perm_b32 v13, v43, v42, v9\n\t"
        "s_waitcnt vmcnt(10) lgkmcnt(1)\n\t"
        "v_mad_u32_u16 v10, v54, s48, v62\n\t"
        "v_mad_u32_u16 v11, v55, s48, v62\n\t"
        "buffer_load_dwordx4 v[120:123], v10, s[16:19], 0 offen\n\t"
        "buffer_load_dwordx4 v[124:127], v11, s[16:19], 0 offen\n\t"
        "v_mfma_f32_4x4x4_16b_bf16 v[32:35], v[12:13], v[72:73], v[32:35] cbsz:1 abid:0\n\t"
        "v_mfma_f32_4x4x4_16b_bf16 v[36:39], v[12:13], v[74:75], v[36:39] cbsz:1 abid:0\n\t"
        "ds_read2_b32 v[42:43], v63 offset0:2 offset1:3\n\t"
        "v_mfma_f32_4x4x4_16b_bf16 v[32:35], v[12:13], v[76:77], v[32:35] cbsz:1 abid:1\n\t"
        "v_mfma_f32_4x4x4_16b_bf16 v[36:39], v[12:13], v[78:79], v[36:39] cbsz:1 abid:1\n\t"
        "s_sub_u32 s59, s59, 1\n\t"
        "s_cbranch_scc1 .L7_i106\n"
        ".L7_j107:\n\t"
        "v_perm_b32 v12, v45, v44, v8\n\t"
        "v_perm_b32 v13, v45, v44, v9\n\t"
        "s_waitcnt vmcnt(10) lgkmcnt(1)\n\t"
        "v_mad_u32_u16 v10, v40, s48, v62\n\t"
        "v_mad_u32_u16 v11, v41, s48, v62\n\t"
        "buffer_load_dwordx4 v[64:67], v10, s[16:19], 0 offen\n\t"
        "buffer_load_dwordx4 v[68:71], v11, s[16:19], 0 offen\n\t"
        "v_mfma_f32_4x4x4_16b_bf16 v[32:35], v[12:13], v[80:81], v[32:35] cbsz:1 abid:0\n\t"
        "v_mfma_f32_4x4x4_16b_bf16 v[36:39], v[12:13], v[82:83], v[36:39] cbsz:1 abid:0\n\t"
        "ds_read2_b32 v[44:45], v63 offset0:4 offset1:5\n\t"
        "v_mfma_f32_4x4x4_16b_bf16 v[32:35], v[12:13], v[84:85], v[32:35] cbsz:1 abid:1\n\t"
        "v_mfma_f32_4x4x4_16b_bf16 v[36:39], v[12:13], v[86:87], v[36:39] cbsz:1 abid:1\n\t"
        "s_sub_u32 s59, s59, 1\n\t"
        "s_cbranch_scc1 .L7_i108\n"
        ".L7_j109:\n\t"
        "v_perm_b32 v12, v47, v46, v8\n\t"
        "v_perm_b32 v13, v47, v46, v9\n\t"
        "s_waitcnt vmcnt(10) lgkmcnt(1)\n\t"
        "v_mad_u32_u16 v10, v42, s48, v62\n\t"
        "v_mad_u32_u16 v11, v43, s48, v62\n\t"
        "buffer_load_dwordx4 v[72:75], v10, s[16:19], 0 offen\n\t"
        "buffer_load_dwordx4 v[76:79], v11, s[16:19], 0 offen\n\t"
        "v_mfma_f32_4x4x4_16b_bf16 v[32:35], v[12:13], v[88:89], v[32:35] cbsz:1 abid:0\n\t"
        "v_mfma_f32_4x4x4_16b_bf16 v[36:39], v[12:13], v[90:91], v[36:39] cbsz:1 abid:0\n\t"
        "ds_read2_b32 v[46:47], v63 offset0:6 offset1:7\n\t"
        "v_mfma_f32_4x4x4_16b_bf16 v[32:35], v[12:13], v[92:93], v[32:35] cbsz:1 abid:1\n\t"
        "v_mfma_f32_4x4x4_16b_bf16 v[36:39], v[12:13], v[94:95], v[36:39] cbsz:1 abid:1\n\t"
        "s_sub_u32 s59, s59, 1\n\t"
        "s_cbranch_scc1 .L7_i110\n"
        ".L7_j111:\n\t"
        "v_perm_b32 v12, v49, v48, v8\n\t"
        "v_perm_b32 v13, v49, v48, v9\n\t"
        "s_waitcnt vmcnt(10) lgkmcnt(1)\n\t"
        "v_mad_u32_u16 v10, v44, s48, v62\n\t"
        "v_mad_u32_u16 v11, v45, s48, v62\n\t"
        "buffer_load_dwordx4 v[80:83], v10, s[16:19], 0 offen\n\t"
        "buffer_load_dwordx4 v[84:87], v11, s[16:19], 0 offen\n\t"
        "v_mfma_f32_4x4x4_16b_bf16 v[32:35], v[12:13], v[96:97], v[32:35] cbsz:1 abid:0\n\t"
        "v_mfma_f32_4x4x4_16b_bf16 v[36:39], v[12:13], v[98:99], v[36:39] cbsz:1 abid:0\n\t"
        "ds_read2_b32 v[48:49], v63 offset0:8 offset1:9\n\t"
        "v_mfma_f32_4x4x4_16b_bf16 v[32:35], v[12:13], v[100:101], v[32:35] cbsz:1 abid:1\n\t"
        "v_mfma_f32_4x4x4_16b_bf16 v[36:39], v[12:13], v[102:103], v[36:39] cbsz:1 abid:1\n\t"
        "s_sub_u32 s59, s59, 1\n\t"
        "s_cbranch_scc1 .L7_i112\n"
        ".L7_j113:\n\t"
        "v_perm_b32 v12, v51, v50, v8\n\t"
        "v_perm_b32 v13, v51, v50, v9\n\t"
        "s_waitcnt vmcnt(10) lgkmcnt(1)\n\t"
        "v_mad_u32_u16 v10, v46, s48, v62\n\t"
        "v_mad_u32_u16 v11, v47, s48, v62\n\t"
        "buffer_load_dwordx4 v[88:91], v10, s[16:19], 0 offen\n\t"
        "buffer_load_dwordx4 v[92:95], v11, s[16:19], 0 offen\n\t"
        "v_mfma_f32_4x4x4_16b_bf16 v[32:35], v[12:13], v[104:105], v[32:35] cbsz:1 abid:0\n\t"
        "v_mfma_f32_4x4x4_16b_bf16 v[36:39], v[12:13], v[106:107], v[36:39] cbsz:1 abid:0\n\t"
        "ds_read2_b32 v[50:51], v63 offset0:10 offset1:11\n\t"
        "v_mfma_f32_4x4x4_16b_bf16 v[32:35], v[12:13], v[108:109], v[32:35] cbsz:1 abid:1\n\t"
        "v_mfma_f32_4x4x4_16b_bf16 v[36:39], v[12:13], v[110:111], v[36:39] cbsz:1 abid:1\n\t"
        "s_sub_u32 s59, s59, 1\n\t"
        "s_cbranch_scc1 .L7_i114\n"
        ".L7_j115:\n\t"
        "v_perm_b32 v12, v53, v52, v8\n\t"
        "v_perm_b32 v13, v53, v52, v9\n\t"
        "s_waitcnt vmcnt(10) lgkmcnt(1)\n\t"
        "v_mad_u32_u16 v10, v48, s48, v62\n\t"
        "v_mad_u32_u16 v11, v49, s48, v62\n\t"
        "buffer_load_dwordx4 v[96:99], v10, s[16:19], 0 offen\n\t"
        "buffer_load_dwordx4 v[100:103], v11, s[16:19], 0 offen\n\t"
        "v_mfma_f32_4x4x4_16b_bf16 v[32:35], v[12:13], v[112:113], v[32:35] cbsz:1 abid:0\n\t"
        "v_mfma_f32_4x4x4_16b_bf16 v[36:39], v[12:13], v[114:115], v[36:39] cbsz:1 abid:0\n\t"
        "ds_read2_b32 v[52:53], v63 offset0:12 offset1:13\n\t"
        "v_mfma_f32_4x4x4_16b_bf16 v[32:35], v[12:13], v[116:117], v[32:35] cbsz:1 abid:1\n\t"
        "v_mfma_f32_4x4x4_16b_bf16 v[36:39], v[12:13], v[118:119], v[36:39] cbsz:1 abid:1\n\t"
        "s_sub_u32 s59, s59, 1\n\t"
        "s_cbranch_scc1 .L7_i116\n"
        ".L7_j117:\n\t"
        "v_perm_b32 v12, v55, v54, v8\n\t"
        "v_perm_b32 v13, v55, v54, v9\n\t"
        "s_waitcnt vmcnt(10) lgkmcnt(1)\n\t"
        "v_mad_u32_u16 v10, v50, s48, v62\n\t"
        "v_mad_u32_u16 v11, v51, s48, v62\n\t"
        "buffer_load_dwordx4 v[104:107], v10, s[16:19], 0 offen\n\t"
        "buffer_load_dwordx4 v[108:111], v11, s[16:19], 0 offen\n\t"
        "v_mfma_f32_4x4x4_16b_bf16 v[32:35], v[12:13], v[120:121], v[32:35] cbsz:1 abid:0\n\t"
        "v_mfma_f32_4x4x4_16b_bf16 v[36:39], v[12:13], v[122:123], v[36:39] cbsz:1 abid:0\n\t"
        "ds_read2_b32 v[54:55], v63 offset0:14 offset1:15\n\t"
        "v_mfma_f32_4x4x4_16b_bf16 v[32:35], v[12:13], v[124:125], v[32:35] cbsz:1 abid:1\n\t"
        "v_mfma_f32_4x4x4_16b_bf16 v[36:39], v[12:13], v[126:127], v[36:39] cbsz:1 abid:1\n\t"
        "s_sub_u32 s59, s59, 1\n\t"
        "s_cbranch_scc0 .L7_loop\n\t"
        "s_branch .L7_i118\n"
        ".L7_i88:\n\t"
        "s_mov_b32 s60, 8\n\t"
        "s_swappc_b64 s[54:55], s[50:51]\n\t"
        "s_branch .L7_j89\n"
        ".L7_i90:\n\t"
        "s_mov_b32 s60, 16\n\t"
        "s_swappc_b64 s[54:55], s[50:51]\n\t"
        "s_branch .L7_j91\n"
        ".L7_i92:\n\t"
        "s_mov_b32 s60, 24\n\t"
        "s_swappc_b64 s[54:55], s[50:51]\n\t"
        "s_branch .L7_j93\n"
        ".L7_i94:\n\t"
        "s_mov_b32 s60, 32\n\t"
        "s_swappc_b64 s[54:55], s[50:51]\n\t"
        "s_branch .L7_j95\n"
        ".L7_i96:\n\t"
        "s_mov_b32 s60, 40\n\t"
        "s_swappc_b64 s[54:55], s[50:51]\n\t"
        "s_branch .L7_j97\n"
        ".L7_i98:\n\t"
        "s_mov_b32 s60, 48\n\t"
        "s_swappc_b64 s[54:55], s[50:51]\n\t"
        "s_branch .L7_j99\n"
        ".L7_i100:\n\t"
        "s_mov_b32 s60, 56\n\t"
        "s_swappc_b64 s[54:55], s[50:51]\n\t"
        "s_branch .L7_j101\n"
        ".L7_i102:\n\t"
        "s_mov_b32 s60, 64\n\t"
        "s_swappc_b64 s[54:55], s[50:51]\n\t"
        "s_branch .L7_j103\n"
        ".L7_i104:\n\t"
        "s_mov_b32 s60, 8\n\t"
        "s_swappc_b64 s[62:63], s[42:43]\n\t"
        "s_cbranch_vccnz .L7_dt1\n\t"
        "s_branch .L7_j105\n"
        ".L7_i106:\n\t"
        "s_mov_b32 s60, 16\n\t"
        "s_swappc_b64 s[62:63], s[42:43]\n\t"
        "s_cbranch_vccnz .L7_dt2\n\t"
        "s_branch .L7_j107\n"
        ".L7_i108:\n\t"
        "s_mov_b32 s60, 24\n\t"
        "s_swappc_b64 s[62:63], s[42:43]\n\t"
        "s_cbranch_vccnz .L7_dt3\n\t"
        "s_branch .L7_j109\n"
        ".L7_i110:\n\t"
        "s_mov_b32 s60, 32\n\t"
        "s_swappc_b64 s[62:63], s[42:43]\n\t"
        "s_cbranch_vccnz .L7_dt4\n\t"
        "s_branch .L7_j111\n"
        ".L7_i112:\n\t"
        "s_mov_b32 s60, 40\n\t"
        "s_swappc_b64 s[62:63], s[42:43]\n\t"
        "s_cbranch_vccnz .L7_dt5\n\t"
        "s_branch .L7_j113\n"
        ".L7_i114:\n\t"
        "s_mov_b32 s60, 48\n\t"
        "s_swappc_b64 s[62:63], s[42:43]\n\t"
        "s_cbranch_vccnz .L7_dt6\n\t"
        "s_branch .L7_j115\n"
        ".L7_i116:\n\t"
        "s_mov_b32 s60, 56\n\t"
        "s_swappc_b64 s[62:63], s[42:43]\n\t"
        "s_cbranch_vccnz .L7_dt7\n\t"
        "s_branch .L7_j117\n"
        ".L7_i118:\n\t"
        "s_mov_b32 s60, 64\n\t"
        "s_swappc_b64 s[62:63], s[42:43]\n\t"
        "s_cbranch_vccnz .L7_dt0\n\t"
        "s_branch .L7_loop\n"
        ".L7_ev:\n\t"
        "s_sub_u32 s56, s56, s58\n\t"
        "s_cbranch_scc0 .L7_e1\n\t"
        "s_swappc_b64 s[54:55], s[50:51]\n"
        ".L7_e1:\n\t"
        "s_sub_u32 s57, s57, s58\n\t"
        "s_cbranch_scc0 .L7_e2\n\t"
        "s_swappc_b64 s[54:55], s[52:53]\n"
        ".L7_e2:\n\t"
        "s_min_u32 s59, s56, s57\n\t"
        "s_add_u32 s58, s59, 1\n\t"
        "s_setpc_b64 s[62:63]\n"
        ".L7_dt0:\n\t"
        "v_perm_b32 v12, v41, v40, v8\n\t"
        "v_perm_b32 v13, v41, v40, v9\n\t"
        "s_waitcnt vmcnt(6)\n\t"
        "s_cmp_lt_u32 s57, 3\n\t"
        "s_cbranch_scc0 .L7_du0\n\t"
        "s_waitcnt vmcnt(2)\n\t"
        "s_cmp_eq_u32 s57, 0\n\t"
        "s_cbranch_scc0 .L7_du0\n\t"
        "s_waitcnt vmcnt(0)\n"
        ".L7_du0:\n\t"
        "v_mfma_f32_4x4x4_16b_bf16 v[32:35], v[12:13], v[64:65], v[32:35] cbsz:1 abid:0\n\t"
        "v_mfma_f32_4x4x4_16b_bf16 v[36:39], v[12:13], v[66:67], v[36:39] cbsz:1 abid:0\n\t"
        "s_sub_u32 s57, s57, 1\n\t"
        "v_mfma_f32_4x4x4_16b_bf16 v[32:35], v[12:13], v[68:69], v[32:35] cbsz:1 abid:1\n\t"
        "v_mfma_f32_4x4x4_16b_bf16 v[36:39], v[12:13], v[70:71], v[36:39] cbsz:1 abid:1\n\t"
        "s_cbranch_scc1 .L7_last\n"
        ".L7_dt1:\n\t"
        "v_perm_b32 v12, v43, v42, v8\n\t"
        "v_perm_b32 v13, v43, v42, v9\n\t"
        "s_waitcnt vmcnt(6)\n\t"
        "s_cmp_lt_u32 s57, 3\n\t"
        "s_cbranch_scc0 .L7_du1\n\t"
        "s_waitcnt vmcnt(2)\n\t"
        "s_cmp_eq_u32 s57, 0\n\t"
        "s_cbranch_scc0 .L7_du1\n\t"
        "s_waitcnt vmcnt(0)\n"
        ".L7_du1:\n\t"
        "v_mfma_f32_4x4x4_16b_bf16 v[32:35], v[12:13], v[72:73], v[32:35] cbsz:1 abid:0\n\t"
        "v_mfma_f32_4x4x4_16b_bf16 v[36:39], v[12:13], v[74:75], v[36:39] cbsz:1 abid:0\n\t"
        "s_sub_u32 s57, s57, 1\n\t"
        "v_mfma_f32_4x4x4_16b_bf16 v[32:35], v[12:13], v[76:77], v[32:35] cbsz:1 abid:1\n\t"
        "v_mfma_f32_4x4x4_16b_bf16 v[36:39], v[12:13], v[78:79], v[36:39] cbsz:1 abid:1\n\t"
        "s_cbranch_scc1 .L7_last\n"
        ".L7_dt2:\n\t"
        "v_perm_b32 v12, v45, v44, v8\n\t"
        "v_perm_b32 v13, v45, v44, v9\n\t"
        "s_waitcnt vmcnt(6)\n\t"
        "s_cmp_lt_u32 s57, 3\n\t"
        "s_cbranch_scc0 .L7_du2\n\t"
        "s_waitcnt vmcnt(2)\n\t"
        "s_cmp_eq_u32 s57, 0\n\t"
        "s_cbranch_scc0 .L7_du2\n\t"
        "s_waitcnt vmcnt(0)\n"
        ".L7_du2:\n\t"
        "v_mfma_f32_4x4x4_16b_bf16 v[32:35], v[12:13], v[80:81], v[32:35] cbsz:1 abid:0\n\t"
        "v_mfma_f32_4x4x4_16b_bf16 v[36:39], v[12:13], v[82:83], v[36:39] cbsz:1 abid:0\n\t"
        "s_sub_u32 s57, s57, 1\n\t"
        "v_mfma_f32_4x4x4_16b_bf16 v[32:35], v[12:13], v[84:85], v[32:35] cbsz:1 abid:1\n\t"
        "v_mfma_f32_4x4x4_16b_bf16 v[36:39], v[12:13], v[86:87], v[36:39] cbsz:1 abid:1\n\t"
        "s_cbranch_scc1 .L7_last\n"
        ".L7_dt3:\n\t"
        "v_perm_b32 v12, v47, v46, v8\n\t"
        "v_perm_b32 v13, v47, v46, v9\n\t"
        "s_waitcnt vmcnt(6)\n\t"
        "s_cmp_lt_u32 s57, 3\n\t"
        "s_cbranch_scc0 .L7_du3\n\t"
        "s_waitcnt vmcnt(2)\n\t"
        "s_cmp_eq_u32 s57, 0\n\t"
        "s_cbranch_scc0 .L7_du3\n\t"
        "s_waitcnt vmcnt(0)\n"
        ".L7_du3:\n\t"
        "v_mfma_f32_4x4x4_16b_bf16 v[32:35], v[12:13], v[88:89], v[32:35] cbsz:1 abid:0\n\t"
        "v_mfma_f32_4x4x4_16b_bf16 v[36:39], v[12:13], v[90:91], v[36:39] cbsz:1 abid:0\n\t"
        "s_sub_u32 s57, s57, 1\n\t"
        "v_mfma_f32_4x4x4_16b_bf16 v[32:35], v[12:13], v[92:93], v[32:35] cbsz:1 abid:1\n\t"
        "v_mfma_f32_4x4x4_16b_bf16 v[36:39], v[12:13], v[94:95], v[36:39] cbsz:1 abid:1\n\t"
        "s_cbranch_scc1 .L7_last\n"
        ".L7_dt4:\n\t"
        "v_perm_b32 v12, v49, v48, v8\n\t"
        "v_perm_b32 v13, v49, v48, v9\n\t"
        "s_waitcnt vmcnt(6)\n\t"
        "s_cmp_lt_u32 s57, 3\n\t"
        "s_cbranch_scc0 .L7_du4\n\t"
        "s_waitcnt vmcnt(2)\n\t"
        "s_cmp_eq_u32 s57, 0\n\t"
        "s_cbranch_scc0 .L7_du4\n\t"
        "s_waitcnt vmcnt(0)\n"
        ".L7_du4:\n\t"
        "v_mfma_f32_4x4x4_16b_bf16 v[32:35], v[12:13], v[96:97], v[32:35] cbsz:1 abid:0\n\t"
        "v_mfma_f32_4x4x4_16b_bf16 v[36:39], v[12:13], v[98:99], v[36:39] cbsz:1 abid:0\n\t"
        "s_sub_u32 s57, s57, 1\n\t"
        "v_mfma_f32_4x4x4_16b_bf16 v[32:35], v[12:13], v[100:101], v[32:35] cbsz:1 abid:1\n\t"
        "v_mfma_f32_4x4x4_16b_bf16 v[36:39], v[12:13], v[102:103], v[36:39] cbsz:1 abid:1\n\t"
        "s_cbranch_scc1 .L7_last\n"
        ".L7_dt5:\n\t"
        "v_perm_b32 v12, v51, v50, v8\n\t"
        "v_perm_b32 v13, v51, v50, v9\n\t"
        "s_waitcnt vmcnt(6)\n\t"
        "s_cmp_lt_u32 s57, 3\n\t"
        "s_cbranch_scc0 .L7_du5\n\t"
        "s_waitcnt vmcnt(2)\n\t"
        "s_cmp_eq_u32 s57, 0\n\t"
        "s_cbranch_scc0 .L7_du5\n\t"
        "s_waitcnt vmcnt(0)\n"
        ".L7_du5:\n\t"
        "v_mfma_f32_4x4x4_16b_bf16 v[32:35], v[12:13], v[104:105], v[32:35] cbsz:1 abid:0\n\t"
        "v_mfma_f32_4x4x4_16b_bf16 v[36:39], v[12:13], v[106:107], v[36:39] cbsz:1 abid:0\n\t"
        "s_sub_u32 s57, s57, 1\n\t"
        "v_mfma_f32_4x4x4_16b_bf16 v[32:35], v[12:13], v[108:109], v[32:35] cbsz:1 abid:1\n\t"
        "v_mfma_f32_4x4x4_16b_bf16 v[36:39], v[12:13], v[110:111], v[36:39] cbsz:1 abid:1\n\t"
        "s_cbranch_scc1 .L7_last\n"
        ".L7_dt6:\n\t"
        "v_perm_b32 v12, v53, v52, v8\n\t"
        "v_perm_b32 v13, v53, v52, v9\n\t"
        "s_waitcnt vmcnt(6)\n\t"
        "s_cmp_lt_u32 s57, 3\n\t"
        "s_cbranch_scc0 .L7_du6\n\t"
        "s_waitcnt vmcnt(2)\n\t"
        "s_cmp_eq_u32 s57, 0\n\t"
        "s_cbranch_scc0 .L7_du6\n\t"
        "s_waitcnt vmcnt(0)\n"
        ".L7_du6:\n\t"
        "v_mfma_f32_4x4x4_16b_bf16 v[32:35], v[12:13], v[112:113], v[32:35] cbsz:1 abid:0\n\t"
        "v_mfma_f32_4x4x4_16b_bf16 v[36:39], v[12:13], v[114:115], v[36:39] cbsz:1 abid:0\n\t"
        "s_sub_u32 s57, s57, 1\n\t"
        "v_mfma_f32_4x4x4_16b_bf16 v[32:35], v[12:13], v[116:117], v[32:35] cbsz:1 abid:1\n\t"
        "v_mfma_f32_4x4x4_16b_bf16 v[36:39], v[12:13], v[118:119], v[36:39] cbsz:1 abid:1\n\t"
        "s_cbranch_scc1 .L7_last\n"
        ".L7_dt7:\n\t"
        "v_perm_b32 v12, v55, v54, v8\n\t"
        "v_perm_b32 v13, v55, v54, v9\n\t"
        "s_waitcnt vmcnt(6)\n\t"
        "s_cmp_lt_u32 s57, 3\n\t"
        "s_cbranch_scc0 .L7_du7\n\t"
        "s_waitcnt vmcnt(2)\n\t"
        "s_cmp_eq_u32 s57, 0\n\t"
        "s_cbranch_scc0 .L7_du7\n\t"
        "s_waitcnt vmcnt(0)\n"
        ".L7_du7:\n\t"
        "v_mfma_f32_4x4x4_16b_bf16 v[32:35], v[12:13], v[120:121], v[32:35] cbsz:1 abid:0\n\t"
        "v_mfma_f32_4x4x4_16b_bf16 v[36:39], v[12:13], v[122:123], v[36:39] cbsz:1 abid:0\n\t"
        "s_sub_u32 s57, s57, 1\n\t"
        "v_mfma_f32_4x4x4_16b_bf16 v[32:35], v[12:13], v[124:125], v[32:35] cbsz:1 abid:1\n\t"
        "v_mfma_f32_4x4x4_16b_bf16 v[36:39], v[12:13], v[126:127], v[36:39] cbsz:1 abid:1\n\t"
        "s_cbranch_scc1 .L7_last\n\t"
        "s_branch .L7_dt0\n"
        ".L7_ie:\n\t"
        "v_dot4_u32_u8 v63, v25, s38, v7\n\t"
        "s_nop 2\n\t"
        "v_subrev_u32 v63, s60, v63\n\t"
        "s_mov_b32 s56, s25\n\t"
        "s_swappc_b64 s[50:51], s[54:55]\n\t"
        "v_dot4_u32_u8 v63, v26, s38, v7\n\t"
        "s_nop 2\n\t"
        "v_subrev_u32 v63, s60, v63\n\t"
        "s_mov_b32 s56, s26\n\t"
        "s_swappc_b64 s[50:51], s[54:55]\n\t"
        "v_dot4_u32_u8 v63, v27, s38, v7\n\t"
        "s_nop 2\n\t"
        "v_subrev_u32 v63, s60, v63\n\t"
        "s_mov_b32 s56, s27\n\t"
        "s_swappc_b64 s[50:51], s[54:55]\n\t"
        "v_dot4_u32_u8 v63, v28, s38, v7\n\t"
        "s_nop 2\n\t"
        "v_subrev_u32 v63, s60, v63\n\t"
        "s_add_u32 s56, s28, 2\n\t"
        "s_swappc_b64 s[50:51], s[54:55]\n\t"
        "s_mov_b64 vcc, -1\n\t"
        "s_setpc_b64 s[54:55]\n"
        ".L7_ce:\n\t"
        "v_and_b32 v4, 0xffff0000, v24\n\t"
        "s_nop 2\n\t"
        "v_pk_mul_f32 v[32:33], v[32:33], v[4:5] op_sel_hi:[1,0]\n\t"
        "v_pk_mul_f32 v[34:35], v[34:35], v[4:5] op_sel_hi:[1,0]\n\t"
        "v_pk_mul_f32 v[36:37], v[36:37], v[4:5] op_sel_hi:[1,0]\n\t"
        "v_pk_mul_f32 v[38:39], v[38:39], v[4:5] op_sel_hi:[1,0]\n\t"
        "v_cvt_pk_bf16_f32 v12, v32, v33\n\t"
        "v_cvt_pk_bf16_f32 v13, v34, v35\n\t"
        "v_cvt_pk_bf16_f32 v14, v36, v37\n\t"
        "v_cvt_pk_bf16_f32 v15, v38, v39\n"
        ".L7_w119:\n\t"
        "ds_read_b32 v4, v2 offset:12\n\t"
        "s_waitcnt lgkmcnt(0)\n\t"
        "v_cmp_gt_u16 vcc, 4, v4\n\t"
        "s_cbranch_vccz .L7_d120\n\t"
        "s_sleep 1\n\t"
        "s_sub_i32 s83, s83, 1\n\t"
        "s_cmp_gt_i32 s83, 0\n\t"
        "s_cbranch_scc1 .L7_w119\n"
        ".L7_d120:\n\t"
        "s_mov_b64 vcc, 0\n\t"
        "ds_write_b128 v56, v[12:15] offset:23808\n\t"
        "s_mov_b64 exec, 1\n\t"
        "ds_add_u32 v2, v3 offset:52\n\t"
        "s_mov_b64 exec, -1\n\t"
        "v_mov_b64 v[32:33], 0\n\t"
        "v_mov_b64 v[34:35], 0\n\t"
        "v_mov_b64 v[36:37], 0\n\t"
        "v_mov_b64 v[38:39], 0\n\t"
        "s_mov_b32 s57, s25\n\t"
        "s_swappc_b64 s[52:53], s[54:55]\n\t"
        "v_and_b32 v4, 0xffff0000, v25\n\t"
        "s_nop 2\n\t"
        "v_pk_mul_f32 v[32:33], v[32:33], v[4:5] op_sel_hi:[1,0]\n\t"
        "v_pk_mul_f32 v[34:35], v[34:35], v[4:5] op_sel_hi:[1,0]\n\t"
        "v_pk_mul_f32 v[36:37], v[36:37], v[4:5] op_sel_hi:[1,0]\n\t"
        "v_pk_mul_f32 v[38:39], v[38:39], v[4:5] op_sel_hi:[1,0]\n\t"
        "v_cvt_pk_bf16_f32 v12, v32, v33\n\t"
        "v_cvt_pk_bf16_f32 v13, v34, v35\n\t"
        "v_cvt_pk_bf16_f32 v14, v36, v37\n\t"
        "v_cvt_pk_bf16_f32 v15, v38, v39\n\t"
        "ds_write_b128 v57, v[12:15] offset:23808\n\t"
        "s_mov_b64 exec, 1\n\t"
        "ds_add_u32 v61, v3 offset:52\n\t"
        "s_mov_b64 exec, -1\n\t"
        "v_mov_b64 v[32:33], 0\n\t"
        "v_mov_b64 v[34:35], 0\n\t"
        "v_mov_b64 v[36:37], 0\n\t"
        "v_mov_b64 v[38:39], 0\n\t"
        "s_mov_b32 s57, s26\n\t"
        "s_swappc_b64 s[52:53], s[54:55]\n\t"
        "v_and_b32 v4, 0xffff0000, v26\n\t"
        "s_nop 2\n\t"
        "v_pk_mul_f32 v[32:33], v[32:33], v[4:5] op_sel_hi:[1,0]\n\t"
        "v_pk_mul_f32 v[34:35], v[34:35], v[4:5] op_sel_hi:[1,0]\n\t"
        "v_pk_mul_f32 v[36:37], v[36:37], v[4:5] op_sel_hi:[1,0]\n\t"
        "v_pk_mul_f32 v[38:39], v[38:39], v[4:5] op_sel_hi:[1,0]\n\t"
        "v_cvt_pk_bf16_f32 v12, v32, v33\n\t"
        "v_cvt_pk_bf16_f32 v13, v34, v35\n\t"
        "v_cvt_pk_bf16_f32 v14, v36, v37\n\t"
        "v_cvt_pk_bf16_f32 v15, v38, v39\n\t"
        "ds_write_b128 v58, v[12:15] offset:23808\n\t"
        "s_mov_b64 exec, 1\n\t"
        "ds_add_u32 v61, v3 offset:56\n\t"
        "s_mov_b64 exec, -1\n\t"
        "v_mov_b64 v[32:33], 0\n\t"
        "v_mov_b64 v[34:35], 0\n\t"
        "v_mov_b64 v[36:37], 0\n\t"
        "v_mov_b64 v[38:39], 0\n\t"
        "s_mov_b32 s57, s27\n\t"
        "s_swappc_b64 s[52:53], s[54:55]\n\t"
        "v_and_b32 v4, 0xffff0000, v27\n\t"
        "s_nop 2\n\t"
        "v_pk_mul_f32 v[32:33], v[32:33], v[4:5] op_sel_hi:[1,0]\n\t"
        "v_pk_mul_f32 v[34:35], v[34:35], v[4:5] op_sel_hi:[1,0]\n\t"
        "v_pk_mul_f32 v[36:37], v[36:37], v[4:5] op_sel_hi:[1,0]\n\t"
        "v_pk_mul_f32 v[38:39], v[38:39], v[4:5] op_sel_hi:[1,0]\n\t"
        "v_cvt_pk_bf16_f32 v12, v32, v33\n\t"
        "v_cvt_pk_bf16_f32 v13, v34, v35\n\t"
        "v_cvt_pk_bf16_f32 v14, v36, v37\n\t"
        "v_cvt_pk_bf16_f32 v15, v38, v39\n\t"
        "ds_write_b128 v59, v[12:15] offset:21760\n\t"
        "s_mov_b64 exec, 1\n\t"
        "ds_add_u32 v2, v3 offset:64\n\t"
        "s_mov_b64 exec, -1\n\t"
        "v_mov_b64 v[32:33], 0\n\t"
        "v_mov_b64 v[34:35], 0\n\t"
        "v_mov_b64 v[36:37], 0\n\t"
        "v_mov_b64 v[38:39], 0\n\t"
        "s_mov_b32 s57, s28\n\t"
        "s_swappc_b64 s[52:53], s[54:55]\n"
        ".L7_last:\n\t"
        "v_and_b32 v4, 0xffff0000, v28\n\t"
        "s_nop 2\n\t"
        "v_pk_mul_f32 v[32:33], v[32:33], v[4:5] op_sel_hi:[1,0]\n\t"
        "v_pk_mul_f32 v[34:35], v[34:35], v[4:5] op_sel_hi:[1,0]\n\t"
        "v_pk_mul_f32 v[36:37], v[36:37], v[4:5] op_sel_hi:[1,0]\n\t"
        "v_pk_mul_f32 v[38:39], v[38:39], v[4:5] op_sel_hi:[1,0]\n\t"
        "v_cvt_pk_bf16_f32 v12, v32, v33\n\t"
        "v_cvt_pk_bf16_f32 v13, v34, v35\n\t"
        "v_cvt_pk_bf16_f32 v14, v36, v37\n\t"
        "v_cvt_pk_bf16_f32 v15, v38, v39\n\t"
        "ds_write_b128 v60, v[12:15] offset:21760\n\t"
        "s_mov_b64 exec, 1\n\t"
        "ds_add_u32 v2, v3 offset:68\n\t"
        "s_mov_b64 exec, -1\n\t"
        "s_endpgm\n"
        ".L7_x:\n\t"
        "s_mov_b32 s36, 1\n\t"
        "s_waitcnt lgkmcnt(0)\n\t"
        "v_dot4_u32_u8 v83, v67, s39, v7\n\t"
        "v_lshlrev_b32_sdwa v115, 1, v67 dst_sel:DWORD dst_unused:UNUSED_PAD src0_sel:DWORD src1_sel:WORD_0\n\t"
        "ds_read_u16 v115, v115 offset:46336\n\t"
        "v_cmp_eq_u16 vcc, -1, v67\n\t"
        "s_cmp_lg_u64 vcc, 0\n\t"
        "s_cselect_b32 s78, 0x10000, 0\n\t"
        "v_or_b32 v30, s78, v30\n\t"
        "ds_add_rtn_u32 v99, v83, v29 offset:192\n\t"
        "v_dot4_u32_u8 v87, v71, s39, v7\n\t"
        "v_lshlrev_b32_sdwa v119, 1, v71 dst_sel:DWORD dst_unused:UNUSED_PAD src0_sel:DWORD src1_sel:WORD_0\n\t"
        "ds_read_u16 v119, v119 offset:46336\n\t"
        "v_cmp_eq_u16 vcc, -1, v71\n\t"
        "s_cmp_lg_u64 vcc, 0\n\t"
        "s_cselect_b32 s78, 0x10000, 0\n\t"
        "v_or_b32 v30, s78, v30\n\t"
        "ds_add_rtn_u32 v103, v87, v29 offset:192\n\t"
        "v_dot4_u32_u8 v91, v75, s39, v7\n\t"
        "v_lshlrev_b32_sdwa v123, 1, v75 dst_sel:DWORD dst_unused:UNUSED_PAD src0_sel:DWORD src1_sel:WORD_0\n\t"
        "ds_read_u16 v123, v123 offset:46336\n\t"
        "v_cmp_eq_u16 vcc, -1, v75\n\t"
        "s_cmp_lg_u64 vcc, 0\n\t"
        "s_cselect_b32 s78, 0x10000, 0\n\t"
        "v_or_b32 v30, s78, v30\n\t"
        "ds_add_rtn_u32 v107, v91, v29 offset:192\n\t"
        "v_dot4_u32_u8 v95, v79, s39, v7\n\t"
        "v_lshlrev_b32_sdwa v127, 1, v79 dst_sel:DWORD dst_unused:UNUSED_PAD src0_sel:DWORD src1_sel:WORD_0\n\t"
        "ds_read_u16 v127, v127 offset:46336\n\t"
        "v_cmp_eq_u16 vcc, -1, v79\n\t"
        "s_cmp_lg_u64 vcc, 0\n\t"
        "s_cselect_b32 s78, 0x10000, 0\n\t"
        "v_or_b32 v30, s78, v30\n\t"
        "ds_add_rtn_u32 v111, v95, v29 offset:192\n\t"
        "s_branch .L7_y\n"
        ".L7_z:\n\t"
        "s_waitcnt lgkmcnt(0)\n\t"
        "ds_read_b32 v32, v83 offset:192\n\t"
        "ds_read_b32 v33, v87 offset:192\n\t"
        "ds_read_b32 v34, v91 offset:192\n\t"
        "ds_read_b32 v35, v95 offset:192\n\t"
        "v_bfe_u32 v99, v99, s33, 8\n\t"
        "v_perm_b32 v115, v115, v67, s49\n\t"
        "v_bfe_u32 v103, v103, s33, 8\n\t"
        "v_perm_b32 v119, v119, v71, s49\n\t"
        "v_bfe_u32 v107, v107, s33, 8\n\t"
        "v_perm_b32 v123, v123, v75, s49\n\t"
        "v_bfe_u32 v111, v111, s33, 8\n\t"
        "v_perm_b32 v127, v127, v79, s49\n\t"
        "s_waitcnt lgkmcnt(0)\n\t"
        "v_dot4_u32_u8 v99, v32, s32, v99\n\t"
        "v_dot4_u32_u8 v103, v33, s32, v103\n\t"
        "v_dot4_u32_u8 v107, v34, s32, v107\n\t"
        "v_dot4_u32_u8 v111, v35, s32, v111\n\t"
        "v_min_u32 v99, 47, v99\n\t"
        "v_min_u32 v103, 47, v103\n\t"
        "v_min_u32 v107, 47, v107\n\t"
        "v_min_u32 v111, 47, v111\n\t"
        "v_lshl_add_u32 v99, v99, 2, v83\n\t"
        "ds_write_b32 v99, v115\n\t"
        "v_lshl_add_u32 v103, v103, 2, v87\n\t"
        "ds_write_b32 v103, v119\n\t"
        "v_lshl_add_u32 v107, v107, 2, v91\n\t"
        "ds_write_b32 v107, v123\n\t"
        "v_lshl_add_u32 v111, v111, 2, v95\n\t"
        "ds_write_b32 v111, v127\n\t"
        "s_branch .L7_zr\n"
        ".L7_exit4:\n\t"
        "s_add_u32 s69, s69, 4\n"
        ".L7_exit:\n\t"
        "s_waitcnt vmcnt(0) lgkmcnt(0)\n\t"
        "s_setprio 0\n\t"
        : "+{v0}"(tid), "=&{s69}"(wv)
        : "s"(__builtin_amdgcn_kernarg_segment_ptr()), "s"(blockIdx.x)
        : "scc", "vcc", "memory", "s16", "s17", "s18", "s19", "s20", "s21", "s22", "s23", "s24", "s25", "s26", "s27", "s28", "s29", "s30", "s31", "s32", "s33", "s34", "s35",
          "s36", "s37", "s38", "s39", "s40", "s41", "s42", "s43", "s44", "s45", "s46", "s47", "s48", "s49", "s50", "s51", "s52", "s53", "s54", "s55",
          "s56", "s57", "s58", "s59", "s60", "s61", "s62", "s63", "s64", "s65", "s66", "s67", "s68", "s70", "s71", "s72", "s73", "s74", "s75", "s76",
          "s77", "s78", "s79", "s80", "s81", "s82", "s83", "s84", "s85", "s86", "s87", "s88", "s89", "s90", "s91", "s92", "s93", "s94", "s95", "s96",
          "s97", "s98", "s99", "s100", "s101", "v1", "v2", "v3", "v4", "v5", "v6", "v7", "v8", "v9", "v10", "v11", "v12", "v13", "v14", "v15",
          "v16", "v17", "v18", "v19", "v20", "v21", "v22", "v23", "v24", "v25", "v26", "v27", "v28", "v29", "v30", "v31", "v32", "v33", "v34", "v35",
          "v36", "v37", "v38", "v39", "v40", "v41", "v42", "v43", "v44", "v45", "v46", "v47", "v48", "v49", "v50", "v51", "v52", "v53", "v54", "v55",
          "v56", "v57", "v58", "v59", "v60", "v61", "v62", "v63", "v64", "v65", "v66", "v67", "v68", "v69", "v70", "v71", "v72", "v73", "v74", "v75",
          "v76", "v77", "v78", "v79", "v80", "v81", "v82", "v83", "v84", "v85", "v86", "v87", "v88", "v89", "v90", "v91", "v92", "v93", "v94", "v95",
          "v96", "v97", "v98", "v99", "v100", "v101", "v102", "v103", "v104", "v105", "v106", "v107", "v108", "v109", "v110", "v111", "v112", "v113", "v114", "v115",
          "v116", "v117", "v118", "v119", "v120", "v121", "v122", "v123", "v124", "v125", "v126", "v127");
    const __attribute__((address_space(4))) Args* q = (const __attribute__((address_space(4))) Args*)__builtin_amdgcn_kernarg_segment_ptr();
    asm volatile("" : "+s"(q));
    const u32 wave = __builtin_amdgcn_readfirstlane(wv);
    u32 lane;
    asm volatile("v_mbcnt_lo_u32_b32 %0, -1, 0\n\tv_mbcnt_hi_u32_b32 %0, -1, %0" : "=v"(lane));
    const u32 half = blockIdx.x & 1u, TN = q->TN, r0 = (blockIdx.x >> 1) * TN;
    const u16* x = q->xall;
    _Pragma("nounroll") for (u32 n = 0; n < (1u << 26) && *(volatile __attribute__((address_space(3))) u32*)S_QK < 4u; ++n) __builtin_amdgcn_s_sleep(2);
    if (wave >= 4u) {
        const int E = q->E, *dst = (const int*)q->dst.p, *src = (const int*)q->src.p;
        const u16* dis = q->disall;
        const u32 w = wave - 4u;
        _Pragma("nounroll") for (u32 k = 0; k < 5u; ++k) {
            const u32 grp = (u32)(((w == 0u ? 0x1713050109ull : w == 1u ? 0x1612040008ull : w == 2u ? 0x14100e0602ull : 0x15110f0703ull) >> (8u * k)) & 255u);
            const u32 row = grp * 4u + (lane >> 4), iv = *(const u32*)(smem + L_INV + row * 4u);
            const int me = (iv & 0x8000u) ? -1 : q->nbase + (int)(r0 + (iv & 255u) + TN - 255u);
            const int m0 = __builtin_amdgcn_readlane(me, 0), m1 = __builtin_amdgcn_readlane(me, 16), m2 = __builtin_amdgcn_readlane(me, 32), m3 = __builtin_amdgcn_readlane(me, 48);
            float s[8] = {0.f, 0.f, 0.f, 0.f, 0.f, 0.f, 0.f, 0.f};
            for (int e0 = 0; e0 < E; e0 += 64) {
                const int e = min(e0 + (int)lane, E - 1), d = dst[e], si = src[e];
                unsigned long long m = __ballot(e0 + (int)lane < E && (d == m0 || d == m1 || d == m2 || d == m3));
                while (m) {
                    const int l = __builtin_ctzll(m);
                    m &= m - 1;
                    const int sl = __builtin_amdgcn_readlane(si, l);
                    if (__builtin_amdgcn_readlane(d, l) == me) {
                        const u32x4 r = *(const u32x4*)(x + (size_t)sl * 256 + half * 128u + (lane & 15u) * 8u);
                        const float wt = bf2f(dis[sl]);
#pragma unroll
                        for (int j = 0; j < 4; ++j) { s[2 * j] += wt * lo(r[j]); s[2 * j + 1] += wt * hi(r[j]); }
                    }
                }
            }
            const float sc = bf2f(dis[max(me, 0)]);
            *(u32x4*)(smem + L_AL + (row < 48u ? row : row - 8u) * 256u + (((lane ^ row) & 15u) << 4)) =
                (u32x4){pack2(s[0] * sc, s[1] * sc), pack2(s[2] * sc, s[3] * sc), pack2(s[4] * sc, s[5] * sc), pack2(s[6] * sc, s[7] * sc)};
            lds_signal(S_T0 + 4u * (grp < 12u ? 1u + ((grp >> 2) & 1u) : grp >> 2), 1u);
        }
        return;
    }
    const int Ee = q->Ee, *edst = (const int*)q->edst.p, *esrc = (const int*)q->esrc.p;
    const u16 *wq = (const u16*)q->wq.p, *wk = (const u16*)q->wk.p;
    float* qs = (float*)(smem + L_QK + wave * 1024u);
    _Pragma("nounroll") for (u32 n = wave; n < TN && r0 + n < q->N; n += 4u) {
        const int me = q->nbase + (int)(r0 + n);
        float c0 = 0.f, c1 = 0.f, c2 = 0.f, c3 = 0.f;
        _Pragma("nounroll") for (int i = 0; i < 256; ++i) {
            const float xv = bf2f(x[(size_t)me * 256 + i]);
            const u32x2 r = *(const u32x2*)(wq + i * 256 + lane * 4u);
            c0 += xv * lo(r[0]); c1 += xv * hi(r[0]); c2 += xv * lo(r[1]); c3 += xv * hi(r[1]);
        }
        qs[lane * 4u] = c0; qs[lane * 4u + 1u] = c1; qs[lane * 4u + 2u] = c2; qs[lane * 4u + 3u] = c3;
        float k[4] = {0.f, 0.f, 0.f, 0.f};
        _Pragma("nounroll") for (int c = 0; c < 256; c += 2) {
            const float qa = qs[c], qb = qs[c + 1];
#pragma unroll
            for (int j = 0; j < 4; ++j) { const u32 r = *(const u32*)(wk + (lane * 4u + j) * 256u + c); k[j] += qa * lo(r) + qb * hi(r); }
        }
        float s0 = 0.f, s1 = 0.f, s2 = 0.f, s3 = 0.f, dn = 0.f;
        for (int e0 = 0; e0 < Ee; e0 += 64) {
            const int e = min(e0 + (int)lane, Ee - 1), si = esrc[e];
            unsigned long long m = __ballot(e0 + (int)lane < Ee && edst[e] == me);
            while (m) {
                const int l = __builtin_ctzll(m);
                m &= m - 1;
                const u32x2 r = *(const u32x2*)(x + (size_t)__builtin_amdgcn_readlane(si, l) * 256 + lane * 4u);
                float d = k[0] * lo(r[0]) + k[1] * hi(r[0]) + k[2] * lo(r[1]) + k[3] * hi(r[1]);
#pragma unroll
                for (int o = 1; o < 64; o <<= 1)
                    d += __uint_as_float((u32)__builtin_amdgcn_ds_bpermute((int)((lane ^ (u32)o) * 4u), (int)__float_as_uint(d)));
                const float wt = __builtin_amdgcn_exp2f(d * 0.09016844f);
                s0 += wt * lo(r[0]); s1 += wt * hi(r[0]); s2 += wt * lo(r[1]); s3 += wt * hi(r[1]);
                dn += wt;
            }
        }
        const float sc = __builtin_amdgcn_rcpf(dn + 1e-9f);
        if ((lane >> 5) == half)
            *(u32x2*)(smem + L_AA + ((u32)*(const u16*)(smem + L_MAP + n * 2u) ^ ((lane & 31u) * 8u))) =
                (u32x2){pack2(s0 * sc, s1 * sc), pack2(s2 * sc, s3 * sc)};
    }
    lds_signal(S_Z, 17u);
}

extern "C" void kernel_launch(void* const* d_in, const int* in_sizes, int n_in,
                              void* d_out, int out_size, void* d_ws, size_t ws_size,
                              hipStream_t stream) {
    const u32 N = in_sizes[5], E = in_sizes[1], Ee = in_sizes[3];
    const u16* x = (const u16*)d_in[0];
    const u16* dis = (const u16*)d_in[5];
    const u32 FL = 0x00020000u;
    const bool generic = N > NMAX || E > EMAX || (E & 3u) || (N & 1u);
    const bool irr = N > NMAX || Ee != 4u * N;
    (void)hipFuncSetAttribute((const void*)agg_kernel, hipFuncAttributeMaxDynamicSharedMemorySize, LDS_BYTES);
    for (u32 nbase = 0; nbase < N; nbase += NMAX) {
        const u32 n = N - nbase < NMAX ? N - nbase : NMAX;
        Args a;
        a.x = Desc{x + (size_t)nbase * 256, n * 512u, FL};
        a.ws = Desc{d_ws, WS_END, FL};
        a.wl = Desc{d_in[6], 131072u, FL}; a.wq = Desc{d_in[7], 131072u, FL}; a.wk = Desc{d_in[8], 131072u, FL}; a.wv = Desc{d_in[9], 131072u, FL};
        a.src = Desc{d_in[1], generic ? 0u : E * 4u, FL}; a.dst = Desc{d_in[2], generic ? 0u : E * 4u, FL};
        a.esrc = Desc{d_in[3], irr ? 0u : Ee * 4u, FL}; a.edst = Desc{d_in[4], irr ? 0u : Ee * 4u, FL};
        a.dis = Desc{dis + nbase, n * 2u, FL};
        a.out = Desc{(u16*)d_out + (size_t)nbase * 256, n * 512u, FL};
        a.N = n; a.TN = n > 256u ? (n + 127u) / 128u : 2u; a.M = ((1u << 24) + a.TN - 1u) / a.TN;
        a.irr = (irr ? 1u : 0u) | (generic ? 0x10000u : 0u);
        a.xall = x; a.disall = dis;
        a.E = (int)E; a.Ee = (int)Ee; a.nbase = (int)nbase; a.pad = 0;
        agg_kernel<<<256, 1024, LDS_BYTES, stream>>>(a);
    }
}
```

```cpp
#include <hip/hip_runtime.h>
#include <hip/hip_bf16.h>
#include <stdint.h>

typedef unsigned short u16;
typedef unsigned int u32;
typedef __attribute__((ext_vector_type(4))) u32 u32x4;
typedef __attribute__((ext_vector_type(2))) u32 u32x2;
struct Desc { const void* p; u32 bytes, flags; };
struct Args {
    Desc x, ws, wl, wv, wq, wk, src, dst, esrc, edst, dis, out;
    u32 N, TN, M, irr;
    const u16* xall;
    const u16* disall;
    int E, Ee, nbase, pad;
};
constexpr u32 NMAX = 10240, EMAX = 163840;
constexpr u32 WS_END = 9437184;
constexpr int LDS_BYTES = 163840;
constexpr u32 L_INV = 512, L_MAP = 1792, L_QK = 2048, L_AL = 23808, L_AA = 46336, S_QK = 16, S_Z = 40, S_T0 = 48;

__device__ __forceinline__ float bf2f(u32 h) { return __uint_as_float(h << 16); }
__device__ __forceinline__ float lo(u32 v) { return __uint_as_float(v << 16); }
__device__ __forceinline__ float hi(u32 v) { return __uint_as_float(v & 0xFFFF0000u); }
__device__ __forceinline__ u32 pack2(float a, float b) {
    return (u32)__builtin_bit_cast(u16, (__bf16)a) | ((u32)__builtin_bit_cast(u16, (__bf16)b) << 16);
}
__device__ __forceinline__ void lds_signal(const u32 addr, const u32 v) {
    asm volatile("s_mov_b64 exec, 1\n\tds_add_u32 %0, %1\n\ts_mov_b64 exec, -1" :: "v"(addr), "v"(v) : "memory");
}

extern "C" __global__ void __launch_bounds__(1024, 4)
agg_kernel(const Args a) {
    extern __shared__ __attribute__((aligned(16))) char smem[];
    u32 tid = threadIdx.x, wv;
    asm volatile(
        "s_memrealtime s[74:75]\n\t"
        "s_load_dwordx8 s[16:23], %2, 0\n\t"
        "s_load_dwordx4 s[64:67], %2, 192\n\t"
        "v_readfirstlane_b32 s69, v0\n\t"
        "v_and_b32 v1, 63, v0\n\t"
        "v_mov_b32 v2, 0\n\t"
        "v_mov_b32 v3, 1\n\t"
        "s_lshr_b32 s76, %3, 3\n\t"
        "s_and_b32 s70, %3, 1\n\t"
        "s_lshr_b32 s71, %3, 1\n\t"
        "s_mov_b32 s83, 0x2000000\n\t"
        "s_lshr_b32 s69, s69, 6\n\t"
        "s_cbranch_scc0 .L7_U\n\t"
        "s_cmp_lt_u32 s69, 8\n\t"
        "s_cbranch_scc1 .L7_G0\n\t"
        "s_load_dwordx4 s[48:51], %2, 128\n\t"
        "s_sub_u32 s69, s69, 8\n\t"
        "v_lshlrev_b32 v4, 2, v1\n\t"
        "s_lshl_b32 s77, s70, 2\n\t"
        "s_xor_b32 s77, s77, s69\n\t"
        "s_xor_b32 s77, s77, 4\n\t"
        "s_mul_i32 s80, s77, 10\n\t"
        "s_waitcnt lgkmcnt(0)\n\t"
        "s_mul_i32 s72, s71, s65\n\t"
        "s_add_u32 s78, s72, s80\n\t"
        "s_lshl_b32 s78, s78, 4\n\t"
        "s_mov_b32 exec_hi, 0xff\n\t"
        "buffer_load_dword v5, v4, s[48:51], s78 offen\n\t"
        "s_mov_b64 exec, -1\n\t"
        "s_barrier\n\t"
        "s_branch .L7_O2\n"
        ".L7_U:\n\t"
        "s_load_dwordx16 s[32:47], %2, 64\n\t"
        "s_load_dwordx16 s[48:63], %2, 128\n\t"
        "s_setprio 3\n\t"
        "s_getpc_b64 s[24:25]\n\t"
        "s_add_u32 s24, s24, agg_kernel@rel32@lo+4\n\t"
        "s_addc_u32 s25, s25, agg_kernel@rel32@hi+12\n\t"
        "s_and_b32 s25, s25, 0xffff\n\t"
        "s_mov_b32 s26, .L7_x-agg_kernel\n\t"
        "s_mov_b32 s27, 0x20000\n\t"
        "v_lshlrev_b32 v6, 12, v1\n\t"
        "s_lshl_b32 s78, s76, 7\n\t"
        "s_mov_b32 m0, 134400\n\t"
        "s_mov_b64 exec, 63\n\t"
        "buffer_load_dword v6, s[24:27], s78 offen lds\n\t"
        "s_mov_b64 exec, -1\n\t"
        "s_cmp_eq_u32 s70, 0\n\t"
        "s_cbranch_scc0 .L7_V\n\t"
        "v_lshrrev_b32 v4, 3, v1\n\t"
        "v_and_b32 v5, 7, v1\n\t"
        "v_lshlrev_b32 v4, 14, v4\n\t"
        "v_lshl_add_u32 v4, v5, 4, v4\n\t"
        "s_lshl_b32 s78, s71, 7\n\t"
        "v_add_u32 v4, s78, v4\n\t"
        "v_add_u32 v5, 0x20000, v4\n\t"
        "v_add_u32 v6, 0x40000, v4\n\t"
        "v_add_u32 v7, 0x60000, v4\n\t"
        "v_add_u32 v8, 0x80000, v4\n\t"
        "s_waitcnt lgkmcnt(0)\n\t"
        "s_mul_i32 s72, s71, s65\n\t"
        "buffer_load_dwordx4 v[16:19], v4, s[44:47], 0 offen\n\t"
        "buffer_load_dwordx4 v[20:23], v5, s[44:47], 0 offen\n\t"
        "buffer_load_dwordx4 v[24:27], v6, s[44:47], 0 offen\n\t"
        "buffer_load_dwordx4 v[28:31], v7, s[44:47], 0 offen\n\t"
        "buffer_load_dwordx4 v[32:35], v8, s[44:47], 0 offen\n\t"
        "s_mul_i32 s80, s71, 0x50\n\t"
        "s_lshl_b32 s78, s80, 4\n\t"
        "v_lshlrev_b32 v12, 4, v1\n\t"
        "buffer_load_dwordx4 v[116:119], v12, s[52:55], s78 offen\n\t"
        "s_mov_b64 exec, 0xffff\n\t"
        "buffer_load_dwordx4 v[120:123], v12, s[52:55], s78 offen offset:1024\n\t"
        "s_mov_b64 exec, -1\n\t"
        "buffer_load_dwordx4 v[36:39], v4, s[40:43], 0 offen\n\t"
        "buffer_load_dwordx4 v[40:43], v5, s[40:43], 0 offen\n\t"
        "buffer_load_dwordx4 v[44:47], v6, s[40:43], 0 offen\n\t"
        "buffer_load_dwordx4 v[48:51], v7, s[40:43], 0 offen\n\t"
        "buffer_load_dwordx4 v[52:55], v8, s[40:43], 0 offen\n\t"
        "s_barrier\n\t"
        "s_branch .L7_U2\n"
        ".L7_G0:\n\t"
        "s_cmp_lt_u32 s69, 4\n\t"
        "s_cbranch_scc1 .L7_L\n\t"
        "s_sub_u32 s69, s69, 4\n\t"
        "v_lshlrev_b32 v4, 4, v1\n\t"
        "v_and_b32 v5, 0x200, v4\n\t"
        "v_mad_u32_u24 v4, v5, 7, v4\n\t"
        "s_mul_i32 s80, s70, 5\n\t"
        "s_mul_i32 s81, s69, 0x420\n\t"
        "s_add_u32 s81, s81, 2048\n\t"
        "s_mul_i32 s78, s80, 0x1080\n\t"
        "s_add_u32 m0, s78, s81\n\t"
        "s_lshr_b32 s78, s80, 1\n\t"
        "s_lshl1_add_u32 s78, s78, s80\n\t"
        "s_lshl_b32 s78, s78, 11\n\t"
        "s_waitcnt lgkmcnt(0)\n\t"
        "s_mul_i32 s72, s71, s65\n\t"
        "s_add_u32 s79, s72, s69\n\t"
        "s_lshl_b32 s79, s79, 9\n\t"
        "v_add_u32 v4, s79, v4\n\t"
        "s_sleep 3\n\t"
        "s_cmp_eq_u32 s70, 0\n\t"
        "s_cbranch_scc1 .L7_gs\n\t"
        "s_sleep 4\n"
        ".L7_gs:\n\t"
        "buffer_load_dwordx4 v4, s[16:19], s78 offen lds\n\t"
        "s_add_u32 s80, s80, 1\n\t"
        "s_load_dwordx4 s[56:59], %2, 160\n\t"
        "s_mul_i32 s78, s80, 0x1080\n\t"
        "s_add_u32 m0, s78, s81\n\t"
        "s_lshr_b32 s78, s80, 1\n\t"
        "s_lshl1_add_u32 s78, s78, s80\n\t"
        "s_lshl_b32 s78, s78, 11\n\t"
        "buffer_load_dwordx4 v4, s[16:19], s78 offen lds\n\t"
        "s_add_u32 s80, s80, 1\n\t"
        "s_mul_i32 s78, s80, 0x1080\n\t"
        "s_add_u32 m0, s78, s81\n\t"
        "s_lshr_b32 s78, s80, 1\n\t"
        "s_lshl1_add_u32 s78, s78, s80\n\t"
        "s_lshl_b32 s78, s78, 11\n\t"
        "buffer_load_dwordx4 v4, s[16:19], s78 offen lds\n\t"
        "s_add_u32 s80, s80, 1\n\t"
        "s_mul_i32 s78, s80, 0x1080\n\t"
        "s_add_u32 m0, s78, s81\n\t"
        "s_lshr_b32 s78, s80, 1\n\t"
        "s_lshl1_add_u32 s78, s78, s80\n\t"
        "s_lshl_b32 s78, s78, 11\n\t"
        "buffer_load_dwordx4 v4, s[16:19], s78 offen lds\n\t"
        "s_add_u32 s80, s80, 1\n\t"
        "s_mul_i32 s78, s80, 0x1080\n\t"
        "s_add_u32 m0, s78, s81\n\t"
        "s_lshr_b32 s78, s80, 1\n\t"
        "s_lshl1_add_u32 s78, s78, s80\n\t"
        "s_lshl_b32 s78, s78, 11\n\t"
        "buffer_load_dwordx4 v4, s[16:19], s78 offen lds\n\t"
        "s_add_u32 s80, s80, 1\n\t"
        "s_barrier\n\t"
        "s_branch .L7_G2\n"
        ".L7_L:\n\t"
        "s_cmp_eq_u32 s69, 2\n\t"
        "s_cbranch_scc0 .L7_L1\n\t"
        "s_waitcnt lgkmcnt(0)\n\t"
        "s_mul_i32 s72, s71, s65\n\t"
        "s_buffer_load_dword s68, s[20:23], 0\n\t"
        "s_barrier\n\t"
        "s_branch .L7_P2\n"
        ".L7_L1:\n\t"
        "s_cmp_eq_u32 s69, 1\n\t"
        "s_cbranch_scc0 .L7_T\n\t"
        "s_load_dwordx4 s[56:59], %2, 160\n\t"
        "v_lshlrev_b32 v4, 3, v1\n\t"
        "v_mov_b64 v[8:9], 0\n\t"
        "ds_write_b64 v4, v[8:9]\n\t"
        "s_waitcnt lgkmcnt(0)\n\t"
        "s_mul_i32 s72, s71, s65\n\t"
        "v_add_lshl_u32 v4, v1, s72, 1\n\t"
        "buffer_load_ushort v16, v4, s[56:59], 0 offen\n\t"
        "buffer_load_ushort v17, v4, s[56:59], 0 offen offset:128\n\t"
        "s_barrier\n\t"
        "s_branch .L7_R2\n"
        ".L7_T:\n\t"
        "s_load_dwordx8 s[24:31], %2, 32\n\t"
        "v_lshlrev_b32 v6, 7, v1\n\t"
        "s_mov_b32 m0, 134400\n\t"
        "s_lshl_b32 s78, s70, 16\n\t"
        "s_lshl_b32 s79, s76, 11\n\t"
        "s_add_u32 s80, s78, s79\n\t"
        "s_mul_i32 s81, s76, 0x300\n\t"
        "s_lshl_b32 s82, %3, 7\n\t"
        "s_add_u32 s82, s82, 4096\n\t"
        "v_lshlrev_b32 v8, 2, v1\n\t"
        "s_and_b32 s78, s71, 63\n\t"
        "s_lshl_b32 s78, s78, 7\n\t"
        "s_and_b32 s79, s71, 64\n\t"
        "s_lshl_b32 s79, s79, 8\n\t"
        "s_add_u32 s42, s78, s79\n\t"
        "s_and_b32 s44, s76, 1\n\t"
        "s_lshl_b32 s78, s44, 13\n\t"
        "s_add_u32 s42, s42, s78\n\t"
        "s_lshl_b32 s78, s70, 15\n\t"
        "s_add_u32 s42, s42, s78\n\t"
        "s_add_u32 s42, s42, 65536\n\t"
        "s_xor_b32 s43, s42, 0x2000\n\t"
        "s_waitcnt lgkmcnt(0)\n\t"
        "s_mul_i32 s72, s71, s65\n\t"
        "s_buffer_load_dword s40, s[20:23], s42 glc\n\t"
        "s_buffer_load_dword s41, s[20:23], s43 glc\n\t"
        "v_mov_b32 v7, s74\n\t"
        "s_mov_b32 exec_hi, 0\n\t"
        "buffer_store_dword v7, v8, s[20:23], s82 offen sc1\n\t"
        "s_lshr_b32 s78, %3, 2\n\t"
        "s_xor_b32 s44, s44, s78\n\t"
        "s_waitcnt lgkmcnt(1)\n\t"
        "s_nop 8\n\t"
        "s_getreg_b32 s45, hwreg(HW_REG_IB_STS, 8, 4)\n\t"
        "s_xnor_b32 s44, s44, s45\n\t"
        "s_and_b32 s44, s44, 1\n\t"
        "s_lshl_b32 s44, s44, 25\n\t"
        "v_mov_b32 v7, s44\n\t"
        "ds_write_b32 v2, v7 offset:1984\n\t"
        "s_waitcnt lgkmcnt(0)\n\t"
        "s_barrier\n\t"
        "s_mov_b64 exec, -1\n\t"
        "s_mov_b64 exec, 0xffff\n\t"
        "buffer_load_dword v6, s[28:31], s80 offen lds\n\t"
        "buffer_load_dword v6, s[24:27], s80 offen lds\n\t"
        "s_mov_b64 exec, -1\n\t"
        "s_branch .L7_T2\n"
        ".L7_V:\n\t"
        "s_and_b32 s78, s71, 1\n\t"
        "s_and_b32 s79, s76, 3\n\t"
        "s_lshl2_add_u32 s80, s78, s79\n\t"
        "s_and_b32 s78, s71, 2\n\t"
        "s_lshr_b32 s79, s76, 2\n\t"
        "s_lshl2_add_u32 s81, s78, s79\n\t"
        "v_lshlrev_b32 v12, 4, v1\n\t"
        "v_and_b32 v5, 0x200, v12\n\t"
        "v_mad_u32_u24 v4, v5, 15, v12\n\t"
        "s_lshl_b32 s82, s80, 14\n\t"
        "s_lshr_b32 s78, s81, 1\n\t"
        "s_lshl_b32 s78, s78, 14\n\t"
        "s_and_b32 s79, s81, 1\n\t"
        "s_lshl_b32 s79, s79, 11\n\t"
        "s_add_u32 s77, s78, s79\n\t"
        "s_mov_b32 m0, 68864\n\t"
        "s_waitcnt lgkmcnt(0)\n\t"
        "s_mul_i32 s72, s71, s65\n\t"
        "buffer_load_dwordx4 v4, s[36:39], s77 offen offset:0 lds\n\t"
        "s_add_u32 m0, m0, 544\n\t"
        "s_nop 0\n\t"
        "buffer_load_dwordx4 v4, s[36:39], s77 offen offset:512 lds\n\t"
        "s_add_u32 m0, m0, 544\n\t"
        "s_nop 0\n\t"
        "buffer_load_dwordx4 v4, s[36:39], s77 offen offset:1024 lds\n\t"
        "s_add_u32 m0, m0, 544\n\t"
        "s_nop 0\n\t"
        "buffer_load_dwordx4 v4, s[36:39], s77 offen offset:1536 lds\n\t"
        "s_add_u32 m0, m0, 2592\n\t"
        "s_add_u32 s77, s77, 0x1000\n\t"
        "buffer_load_dwordx4 v4, s[36:39], s77 offen offset:0 lds\n\t"
        "s_add_u32 m0, m0, 544\n\t"
        "s_nop 0\n\t"
        "buffer_load_dwordx4 v4, s[36:39], s77 offen offset:512 lds\n\t"
        "s_add_u32 m0, m0, 544\n\t"
        "s_nop 0\n\t"
        "buffer_load_dwordx4 v4, s[36:39], s77 offen offset:1024 lds\n\t"
        "s_add_u32 m0, m0, 544\n\t"
        "s_nop 0\n\t"
        "buffer_load_dwordx4 v4, s[36:39], s77 offen offset:1536 lds\n\t"
        "s_add_u32 m0, m0, 2592\n\t"
        "s_nop 0\n\t"
        "buffer_load_dwordx4 v4, s[32:35], s82 offen offset:0 lds\n\t"
        "s_add_u32 m0, m0, 544\n\t"
        "s_nop 0\n\t"
        "buffer_load_dwordx4 v4, s[32:35], s82 offen offset:512 lds\n\t"
        "s_add_u32 m0, m0, 544\n\t"
        "s_nop 0\n\t"
        "buffer_load_dwordx4 v4, s[32:35], s82 offen offset:1024 lds\n\t"
        "s_add_u32 m0, m0, 544\n\t"
        "s_nop 0\n\t"
        "buffer_load_dwordx4 v4, s[32:35], s82 offen offset:1536 lds\n\t"
        "s_add_u32 m0, m0, 2592\n\t"
        "s_add_u32 s82, s82, 0x1000\n\t"
        "buffer_load_dwordx4 v4, s[32:35], s82 offen offset:0 lds\n\t"
        "s_add_u32 m0, m0, 544\n\t"
        "s_nop 0\n\t"
        "buffer_load_dwordx4 v4, s[32:35], s82 offen offset:512 lds\n\t"
        "s_add_u32 m0, m0, 544\n\t"
        "s_nop 0\n\t"
        "buffer_load_dwordx4 v4, s[32:35], s82 offen offset:1024 lds\n\t"
        "s_add_u32 m0, m0, 544\n\t"
        "s_nop 0\n\t"
        "buffer_load_dwordx4 v4, s[32:35], s82 offen offset:1536 lds\n\t"
        "s_add_u32 m0, m0, 2592\n\t"
        "s_sub_u32 s82, s82, 0x800\n\t"
        "buffer_load_dwordx4 v4, s[32:35], s82 offen offset:0 lds\n\t"
        "s_add_u32 m0, m0, 544\n\t"
        "s_nop 0\n\t"
        "buffer_load_dwordx4 v4, s[32:35], s82 offen offset:512 lds\n\t"
        "s_add_u32 m0, m0, 544\n\t"
        "s_nop 0\n\t"
        "buffer_load_dwordx4 v4, s[32:35], s82 offen offset:1024 lds\n\t"
        "s_add_u32 m0, m0, 544\n\t"
        "s_nop 0\n\t"
        "buffer_load_dwordx4 v4, s[32:35], s82 offen offset:1536 lds\n\t"
        "s_add_u32 m0, m0, 2592\n\t"
        "s_add_u32 s82, s82, 0x1000\n\t"
        "buffer_load_dwordx4 v4, s[32:35], s82 offen offset:0 lds\n\t"
        "s_add_u32 m0, m0, 544\n\t"
        "s_nop 0\n\t"
        "buffer_load_dwordx4 v4, s[32:35], s82 offen offset:512 lds\n\t"
        "s_add_u32 m0, m0, 544\n\t"
        "s_nop 0\n\t"
        "buffer_load_dwordx4 v4, s[32:35], s82 offen offset:1024 lds\n\t"
        "s_add_u32 m0, m0, 544\n\t"
        "s_nop 0\n\t"
        "buffer_load_dwordx4 v4, s[32:35], s82 offen offset:1536 lds\n\t"
        "s_barrier\n\t"
        "s_branch .L7_V2\n"
        ".L7_U2:\n\t"
        "v_cmp_gt_u32 s[24:25], s46, v4\n\t"
        "v_cmp_gt_u32 s[26:27], s46, v5\n\t"
        "v_cmp_gt_u32 s[28:29], s46, v6\n\t"
        "v_cmp_gt_u32 s[30:31], s46, v7\n\t"
        "v_cmp_gt_u32 s[32:33], s46, v8\n\t"
        "v_mul_u32_u24 v112, 0xa0, v1\n\t"
        "v_add_u32 v112, 68864, v112\n\t"
        "v_add_u32 v113, 10240, v112\n\t"
        "v_lshlrev_b32 v7, 2, v1\n\t"
        "ds_write_b32 v7, v112 offset:1028\n\t"
        "ds_write_b32 v7, v113 offset:1284\n\t"
        "v_lshrrev_b32 v15, 3, v1\n\t"
        "v_mul_u32_u24 v15, 0xa0, v15\n\t"
        "v_add_u32 v15, 68864, v15\n\t"
        "v_and_b32 v4, 7, v1\n\t"
        "v_lshl_add_u32 v15, v4, 2, v15\n\t"
        "v_mov_b32 v13, 0x1000000\n\t"
        "v_mov_b32 v14, 4\n\t"
        "s_lshl_b32 s54, s65, 14\n\t"
        "s_sub_u32 s54, 0, s54\n\t"
        "s_lshl_b32 s48, s71, 14\n\t"
        "s_add_u32 s48, s48, 1048576\n\t"
        "s_add_u32 s49, s48, 4096\n\t"
        "v_add_u32 v97, 1280, v15\n\t"
        "v_add_u32 v98, 2560, v15\n\t"
        "v_add_u32 v99, 3840, v15\n\t"
        "v_add_u32 v100, 5120, v15\n\t"
        "v_add_u32 v101, 6400, v15\n\t"
        "v_add_u32 v102, 7680, v15\n\t"
        "v_add_u32 v103, 8960, v15\n\t"
        "v_add_u32 v112, 0x80, v112\n\t"
        "v_add_u32 v113, 0x80, v113\n\t"
        "v_add_u32 v114, s80, v1\n\t"
        "v_add_u32 v115, 64, v114\n\t"
        "v_cmp_gt_u32 s[36:37], s64, v114\n\t"
        "v_cmp_gt_u32 s[38:39], s64, v115\n\t"
        "s_waitcnt vmcnt(11) lgkmcnt(8)\n\t"
        "s_mov_b64 exec, s[24:25]\n\t"
        "v_mad_u32_u24 v56, v16, s66, v13\n\t"
        "v_mad_u32_u24 v57, v17, s66, v13\n\t"
        "v_mad_u32_u24 v58, v18, s66, v13\n\t"
        "v_mad_u32_u24 v59, v19, s66, v13\n\t"
        "v_lshlrev_b32_sdwa v56, 2, v56 dst_sel:DWORD dst_unused:UNUSED_PAD src0_sel:DWORD src1_sel:BYTE_3\n\t"
        "ds_add_rtn_u32 v76, v56, v14 offset:1024\n\t"
        "v_lshlrev_b32_sdwa v57, 2, v57 dst_sel:DWORD dst_unused:UNUSED_PAD src0_sel:DWORD src1_sel:BYTE_3\n\t"
        "ds_add_rtn_u32 v77, v57, v14 offset:1024\n\t"
        "v_lshlrev_b32_sdwa v58, 2, v58 dst_sel:DWORD dst_unused:UNUSED_PAD src0_sel:DWORD src1_sel:BYTE_3\n\t"
        "ds_add_rtn_u32 v78, v58, v14 offset:1024\n\t"
        "v_lshlrev_b32_sdwa v59, 2, v59 dst_sel:DWORD dst_unused:UNUSED_PAD src0_sel:DWORD src1_sel:BYTE_3\n\t"
        "ds_add_rtn_u32 v79, v59, v14 offset:1024\n\t"
        "s_waitcnt vmcnt(10) lgkmcnt(8)\n\t"
        "s_mov_b64 exec, s[26:27]\n\t"
        "v_mad_u32_u24 v60, v20, s66, v13\n\t"
        "v_mad_u32_u24 v61, v21, s66, v13\n\t"
        "v_mad_u32_u24 v62, v22, s66, v13\n\t"
        "v_mad_u32_u24 v63, v23, s66, v13\n\t"
        "v_lshlrev_b32_sdwa v60, 2, v60 dst_sel:DWORD dst_unused:UNUSED_PAD src0_sel:DWORD src1_sel:BYTE_3\n\t"
        "ds_add_rtn_u32 v80, v60, v14 offset:1024\n\t"
        "v_lshlrev_b32_sdwa v61, 2, v61 dst_sel:DWORD dst_unused:UNUSED_PAD src0_sel:DWORD src1_sel:BYTE_3\n\t"
        "ds_add_rtn_u32 v81, v61, v14 offset:1024\n\t"
        "v_lshlrev_b32_sdwa v62, 2, v62 dst_sel:DWORD dst_unused:UNUSED_PAD src0_sel:DWORD src1_sel:BYTE_3\n\t"
        "ds_add_rtn_u32 v82, v62, v14 offset:1024\n\t"
        "v_lshlrev_b32_sdwa v63, 2, v63 dst_sel:DWORD dst_unused:UNUSED_PAD src0_sel:DWORD src1_sel:BYTE_3\n\t"
        "ds_add_rtn_u32 v83, v63, v14 offset:1024\n\t"
        "s_waitcnt vmcnt(9) lgkmcnt(8)\n\t"
        "s_mov_b64 exec, s[28:29]\n\t"
        "v_mad_u32_u24 v64, v24, s66, v13\n\t"
        "v_mad_u32_u24 v65, v25, s66, v13\n\t"
        "v_mad_u32_u24 v66, v26, s66, v13\n\t"
        "v_mad_u32_u24 v67, v27, s66, v13\n\t"
        "v_lshlrev_b32_sdwa v64, 2, v64 dst_sel:DWORD dst_unused:UNUSED_PAD src0_sel:DWORD src1_sel:BYTE_3\n\t"
        "ds_add_rtn_u32 v84, v64, v14 offset:1024\n\t"
        "v_lshlrev_b32_sdwa v65, 2, v65 dst_sel:DWORD dst_unused:UNUSED_PAD src0_sel:DWORD src1_sel:BYTE_3\n\t"
        "ds_add_rtn_u32 v85, v65, v14 offset:1024\n\t"
        "v_lshlrev_b32_sdwa v66, 2, v66 dst_sel:DWORD dst_unused:UNUSED_PAD src0_sel:DWORD src1_sel:BYTE_3\n\t"
        "ds_add_rtn_u32 v86, v66, v14 offset:1024\n\t"
        "v_lshlrev_b32_sdwa v67, 2, v67 dst_sel:DWORD dst_unused:UNUSED_PAD src0_sel:DWORD src1_sel:BYTE_3\n\t"
        "ds_add_rtn_u32 v87, v67, v14 offset:1024\n\t"
        "s_waitcnt vmcnt(8) lgkmcnt(8)\n\t"
        "s_mov_b64 exec, s[30:31]\n\t"
        "v_mad_u32_u24 v68, v28, s66, v13\n\t"
        "v_mad_u32_u24 v69, v29, s66, v13\n\t"
        "v_mad_u32_u24 v70, v30, s66, v13\n\t"
        "v_mad_u32_u24 v71, v31, s66, v13\n\t"
        "v_lshlrev_b32_sdwa v68, 2, v68 dst_sel:DWORD dst_unused:UNUSED_PAD src0_sel:DWORD src1_sel:BYTE_3\n\t"
        "ds_add_rtn_u32 v88, v68, v14 offset:1024\n\t"
        "v_lshlrev_b32_sdwa v69, 2, v69 dst_sel:DWORD dst_unused:UNUSED_PAD src0_sel:DWORD src1_sel:BYTE_3\n\t"
        "ds_add_rtn_u32 v89, v69, v14 offset:1024\n\t"
        "v_lshlrev_b32_sdwa v70, 2, v70 dst_sel:DWORD dst_unused:UNUSED_PAD src0_sel:DWORD src1_sel:BYTE_3\n\t"
        "ds_add_rtn_u32 v90, v70, v14 offset:1024\n\t"
        "v_lshlrev_b32_sdwa v71, 2, v71 dst_sel:DWORD dst_unused:UNUSED_PAD src0_sel:DWORD src1_sel:BYTE_3\n\t"
        "ds_add_rtn_u32 v91, v71, v14 offset:1024\n\t"
        "s_waitcnt vmcnt(7) lgkmcnt(8)\n\t"
        "s_mov_b64 exec, s[32:33]\n\t"
        "v_mad_u32_u24 v72, v32, s66, v13\n\t"
        "v_mad_u32_u24 v73, v33, s66, v13\n\t"
        "v_mad_u32_u24 v74, v34, s66, v13\n\t"
        "v_mad_u32_u24 v75, v35, s66, v13\n\t"
        "v_lshlrev_b32_sdwa v72, 2, v72 dst_sel:DWORD dst_unused:UNUSED_PAD src0_sel:DWORD src1_sel:BYTE_3\n\t"
        "ds_add_rtn_u32 v92, v72, v14 offset:1024\n\t"
        "v_lshlrev_b32_sdwa v73, 2, v73 dst_sel:DWORD dst_unused:UNUSED_PAD src0_sel:DWORD src1_sel:BYTE_3\n\t"
        "ds_add_rtn_u32 v93, v73, v14 offset:1024\n\t"
        "v_lshlrev_b32_sdwa v74, 2, v74 dst_sel:DWORD dst_unused:UNUSED_PAD src0_sel:DWORD src1_sel:BYTE_3\n\t"
        "ds_add_rtn_u32 v94, v74, v14 offset:1024\n\t"
        "v_lshlrev_b32_sdwa v75, 2, v75 dst_sel:DWORD dst_unused:UNUSED_PAD src0_sel:DWORD src1_sel:BYTE_3\n\t"
        "ds_add_rtn_u32 v95, v75, v14 offset:1024\n\t"
        "s_mov_b64 exec, -1\n\t"
        "ds_read_b32 v4, v7 offset:1028\n\t"
        "ds_read_b32 v5, v7 offset:1284\n\t"
        "s_waitcnt vmcnt(5)\n\t"
        "v_xor_b32 v116, v116, v114\n\t"
        "v_bitop3_b32 v116, v117, v116, v114 bitop3:0xde\n\t"
        "v_bitop3_b32 v116, v118, v116, v114 bitop3:0xde\n\t"
        "v_bitop3_b32 v116, v119, v116, v114 bitop3:0xde\n\t"
        "v_xor_b32 v120, v120, v115\n\t"
        "v_bitop3_b32 v120, v121, v120, v115 bitop3:0xde\n\t"
        "v_bitop3_b32 v120, v122, v120, v115 bitop3:0xde\n\t"
        "v_bitop3_b32 v120, v123, v120, v115 bitop3:0xde\n\t"
        "v_cmp_ne_u32 s[40:41], 0, v116\n\t"
        "v_cmp_ne_u32 s[42:43], 0, v120\n\t"
        "s_and_b64 s[38:39], s[38:39], 0xffff\n\t"
        "s_and_b64 s[36:37], s[36:37], s[40:41]\n\t"
        "s_and_b64 s[38:39], s[38:39], s[42:43]\n\t"
        "s_or_b64 s[36:37], s[36:37], s[38:39]\n\t"
        "s_cselect_b32 s67, 1, s67\n"
        ".L7_w38:\n\t"
        "ds_read_b32 v0, v2 offset:104\n\t"
        "ds_read_b32 v6, v2 offset:76\n\t"
        "s_waitcnt lgkmcnt(1)\n\t"
        "v_cmp_gt_u16 vcc, 1, v0\n\t"
        "s_cbranch_vccz .L7_d39\n\t"
        "s_waitcnt lgkmcnt(0)\n\t"
        "s_sleep 0\n\t"
        "s_sub_i32 s83, s83, 1\n\t"
        "s_cmp_gt_i32 s83, 0\n\t"
        "s_cbranch_scc1 .L7_w38\n"
        ".L7_d39:\n\t"
        "v_cmp_gt_u32 s[34:35], v4, v112\n\t"
        "v_cmp_gt_u32 s[36:37], v5, v113\n\t"
        "s_or_b64 s[38:39], s[34:35], s[36:37]\n\t"
        "s_cbranch_scc1 .L7_ov\n"
        ".L7_ov1:\n\t"
        "s_waitcnt lgkmcnt(0)\n\t"
        "v_readfirstlane_b32 s68, v6\n\t"
        "s_waitcnt vmcnt(4) lgkmcnt(8)\n\t"
        "s_mov_b64 exec, s[24:25]\n\t"
        "v_lshl_add_u32 v16, v16, 16, v36\n\t"
        "v_lshl_add_u32 v17, v17, 16, v37\n\t"
        "v_lshl_add_u32 v18, v18, 16, v38\n\t"
        "v_lshl_add_u32 v19, v19, 16, v39\n\t"
        "v_mad_i32_i24 v16, v56, s54, v16\n\t"
        "ds_add_u32 v76, v16\n\t"
        "v_mad_i32_i24 v17, v57, s54, v17\n\t"
        "ds_add_u32 v77, v17\n\t"
        "v_mad_i32_i24 v18, v58, s54, v18\n\t"
        "ds_add_u32 v78, v18\n\t"
        "v_mad_i32_i24 v19, v59, s54, v19\n\t"
        "ds_add_u32 v79, v19\n\t"
        "s_waitcnt vmcnt(3) lgkmcnt(8)\n\t"
        "s_mov_b64 exec, s[26:27]\n\t"
        "v_lshl_add_u32 v20, v20, 16, v40\n\t"
        "v_lshl_add_u32 v21, v21, 16, v41\n\t"
        "v_lshl_add_u32 v22, v22, 16, v42\n\t"
        "v_lshl_add_u32 v23, v23, 16, v43\n\t"
        "v_mad_i32_i24 v20, v60, s54, v20\n\t"
        "ds_add_u32 v80, v20\n\t"
        "v_mad_i32_i24 v21, v61, s54, v21\n\t"
        "ds_add_u32 v81, v21\n\t"
        "v_mad_i32_i24 v22, v62, s54, v22\n\t"
        "ds_add_u32 v82, v22\n\t"
        "v_mad_i32_i24 v23, v63, s54, v23\n\t"
        "ds_add_u32 v83, v23\n\t"
        "s_waitcnt vmcnt(2) lgkmcnt(8)\n\t"
        "s_mov_b64 exec, s[28:29]\n\t"
        "v_lshl_add_u32 v24, v24, 16, v44\n\t"
        "v_lshl_add_u32 v25, v25, 16, v45\n\t"
        "v_lshl_add_u32 v26, v26, 16, v46\n\t"
        "v_lshl_add_u32 v27, v27, 16, v47\n\t"
        "v_mad_i32_i24 v24, v64, s54, v24\n\t"
        "ds_add_u32 v84, v24\n\t"
        "v_mad_i32_i24 v25, v65, s54, v25\n\t"
        "ds_add_u32 v85, v25\n\t"
        "v_mad_i32_i24 v26, v66, s54, v26\n\t"
        "ds_add_u32 v86, v26\n\t"
        "v_mad_i32_i24 v27, v67, s54, v27\n\t"
        "ds_add_u32 v87, v27\n\t"
        "s_waitcnt vmcnt(1) lgkmcnt(8)\n\t"
        "s_mov_b64 exec, s[30:31]\n\t"
        "v_lshl_add_u32 v28, v28, 16, v48\n\t"
        "v_lshl_add_u32 v29, v29, 16, v49\n\t"
        "v_lshl_add_u32 v30, v30, 16, v50\n\t"
        "v_lshl_add_u32 v31, v31, 16, v51\n\t"
        "v_mad_i32_i24 v28, v68, s54, v28\n\t"
        "ds_add_u32 v88, v28\n\t"
        "v_mad_i32_i24 v29, v69, s54, v29\n\t"
        "ds_add_u32 v89, v29\n\t"
        "v_mad_i32_i24 v30, v70, s54, v30\n\t"
        "ds_add_u32 v90, v30\n\t"
        "v_mad_i32_i24 v31, v71, s54, v31\n\t"
        "ds_add_u32 v91, v31\n\t"
        "s_waitcnt vmcnt(0) lgkmcnt(8)\n\t"
        "s_mov_b64 exec, s[32:33]\n\t"
        "v_lshl_add_u32 v32, v32, 16, v52\n\t"
        "v_lshl_add_u32 v33, v33, 16, v53\n\t"
        "v_lshl_add_u32 v34, v34, 16, v54\n\t"
        "v_lshl_add_u32 v35, v35, 16, v55\n\t"
        "v_mad_i32_i24 v32, v72, s54, v32\n\t"
        "ds_add_u32 v92, v32\n\t"
        "v_mad_i32_i24 v33, v73, s54, v33\n\t"
        "ds_add_u32 v93, v33\n\t"
        "v_mad_i32_i24 v34, v74, s54, v34\n\t"
        "ds_add_u32 v94, v34\n\t"
        "v_mad_i32_i24 v35, v75, s54, v35\n\t"
        "ds_add_u32 v95, v35\n\t"
        "s_mov_b64 exec, -1\n\t"
        "s_and_b32 s78, s67, 1\n\t"
        "s_or_b32 s78, s78, s38\n\t"
        "s_or_b32 s78, s78, s39\n\t"
        "s_cbranch_scc1 .L7_mk\n"
        ".L7_mk1:\n\t"
        "s_mov_b64 exec, 1\n\t"
        "ds_add_u32 v2, v3 offset:112\n\t"
        "s_mov_b64 exec, -1\n\t"
        "s_waitcnt lgkmcnt(3)\n\t"
        "ds_read2_b32 v[16:17], v15 offset0:0 offset1:8\n\t"
        "ds_read2_b32 v[18:19], v15 offset0:16 offset1:24\n\t"
        "ds_read2_b32 v[20:21], v97 offset0:0 offset1:8\n\t"
        "ds_read2_b32 v[22:23], v97 offset0:16 offset1:24\n\t"
        "ds_read2_b32 v[24:25], v98 offset0:0 offset1:8\n\t"
        "ds_read2_b32 v[26:27], v98 offset0:16 offset1:24\n\t"
        "ds_read2_b32 v[28:29], v99 offset0:0 offset1:8\n\t"
        "ds_read2_b32 v[30:31], v99 offset0:16 offset1:24\n\t"
        "ds_read2_b32 v[32:33], v100 offset0:0 offset1:8\n\t"
        "ds_read2_b32 v[34:35], v100 offset0:16 offset1:24\n\t"
        "ds_read2_b32 v[36:37], v101 offset0:0 offset1:8\n\t"
        "ds_read2_b32 v[38:39], v101 offset0:16 offset1:24\n\t"
        "s_waitcnt lgkmcnt(10)\n\t"
        "buffer_store_dwordx4 v[16:19], v12, s[20:23], s48 offen offset:0 sc1\n\t"
        "ds_read2_b32 v[40:41], v102 offset0:0 offset1:8\n\t"
        "ds_read2_b32 v[42:43], v102 offset0:16 offset1:24\n\t"
        "s_waitcnt lgkmcnt(10)\n\t"
        "buffer_store_dwordx4 v[20:23], v12, s[20:23], s48 offen offset:1024 sc1\n\t"
        "ds_read2_b32 v[44:45], v103 offset0:0 offset1:8\n\t"
        "ds_read2_b32 v[46:47], v103 offset0:16 offset1:24\n\t"
        "s_mov_b64 exec, 1\n\t"
        "ds_add_u32 v2, v3 offset:72\n\t"
        "s_mov_b64 exec, -1\n\t"
        "s_waitcnt lgkmcnt(11)\n\t"
        "buffer_store_dwordx4 v[24:27], v12, s[20:23], s48 offen offset:2048 sc1\n\t"
        "s_waitcnt lgkmcnt(9)\n\t"
        "buffer_store_dwordx4 v[28:31], v12, s[20:23], s48 offen offset:3072 sc1\n\t"
        "s_waitcnt lgkmcnt(7)\n\t"
        "buffer_store_dwordx4 v[32:35], v12, s[20:23], s49 offen offset:0 sc1\n\t"
        "s_waitcnt lgkmcnt(5)\n\t"
        "buffer_store_dwordx4 v[36:39], v12, s[20:23], s49 offen offset:1024 sc1\n\t"
        "s_waitcnt lgkmcnt(3)\n\t"
        "buffer_store_dwordx4 v[40:43], v12, s[20:23], s49 offen offset:2048 sc1\n\t"
        "s_waitcnt lgkmcnt(1)\n\t"
        "buffer_store_dwordx4 v[44:47], v12, s[20:23], s49 offen offset:3072 sc1\n\t"
        "s_mov_b64 exec, 1\n\t"
        "ds_add_u32 v2, v3 offset:44\n\t"
        "s_mov_b64 exec, -1\n\t"
        "s_branch .L7_Q\n"
        ".L7_ov:\n\t"
        "s_mov_b32 s78, 68828\n\t"
        "v_mov_b32 v9, 40\n\t"
        "v_mad_u32_u24 v8, v56, v9, s78\n\t"
        "v_min_u32 v76, v76, v8\n\t"
        "v_mad_u32_u24 v8, v57, v9, s78\n\t"
        "v_min_u32 v77, v77, v8\n\t"
        "v_mad_u32_u24 v8, v58, v9, s78\n\t"
        "v_min_u32 v78, v78, v8\n\t"
        "v_mad_u32_u24 v8, v59, v9, s78\n\t"
        "v_min_u32 v79, v79, v8\n\t"
        "v_mad_u32_u24 v8, v60, v9, s78\n\t"
        "v_min_u32 v80, v80, v8\n\t"
        "v_mad_u32_u24 v8, v61, v9, s78\n\t"
        "v_min_u32 v81, v81, v8\n\t"
        "v_mad_u32_u24 v8, v62, v9, s78\n\t"
        "v_min_u32 v82, v82, v8\n\t"
        "v_mad_u32_u24 v8, v63, v9, s78\n\t"
        "v_min_u32 v83, v83, v8\n\t"
        "v_mad_u32_u24 v8, v64, v9, s78\n\t"
        "v_min_u32 v84, v84, v8\n\t"
        "v_mad_u32_u24 v8, v65, v9, s78\n\t"
        "v_min_u32 v85, v85, v8\n\t"
        "v_mad_u32_u24 v8, v66, v9, s78\n\t"
        "v_min_u32 v86, v86, v8\n\t"
        "v_mad_u32_u24 v8, v67, v9, s78\n\t"
        "v_min_u32 v87, v87, v8\n\t"
        "v_mad_u32_u24 v8, v68, v9, s78\n\t"
        "v_min_u32 v88, v88, v8\n\t"
        "v_mad_u32_u24 v8, v69, v9, s78\n\t"
        "v_min_u32 v89, v89, v8\n\t"
        "v_mad_u32_u24 v8, v70, v9, s78\n\t"
        "v_min_u32 v90, v90, v8\n\t"
        "v_mad_u32_u24 v8, v71, v9, s78\n\t"
        "v_min_u32 v91, v91, v8\n\t"
        "v_mad_u32_u24 v8, v72, v9, s78\n\t"
        "v_min_u32 v92, v92, v8\n\t"
        "v_mad_u32_u24 v8, v73, v9, s78\n\t"
        "v_min_u32 v93, v93, v8\n\t"
        "v_mad_u32_u24 v8, v74, v9, s78\n\t"
        "v_min_u32 v94, v94, v8\n\t"
        "v_mad_u32_u24 v8, v75, v9, s78\n\t"
        "v_min_u32 v95, v95, v8\n\t"
        "s_branch .L7_ov1\n"
        ".L7_mk:\n\t"
        "s_mul_i32 s78, s68, 0x4000400\n\t"
        "s_or_b32 s78, s78, 0xffffff\n\t"
        "v_mov_b32 v6, s78\n\t"
        "v_add_u32 v112, -4, v112\n\t"
        "v_add_u32 v113, -4, v113\n\t"
        "s_mov_b64 exec, s[34:35]\n\t"
        "ds_write_b32 v112, v6\n\t"
        "s_mov_b64 exec, s[36:37]\n\t"
        "ds_write_b32 v113, v6\n\t"
        "s_mov_b64 exec, -1\n\t"
        "s_and_b32 s78, s67, 1\n\t"
        "s_lshl_b32 s78, s78, 24\n\t"
        "v_mov_b32 v6, s78\n\t"
        "ds_xor_b32 v112, v6\n\t"
        "ds_xor_b32 v113, v6\n\t"
        "s_branch .L7_mk1\n"
        ".L7_P2:\n\t"
        "s_setprio 3\n\t"
        "ds_read_b32 v6, v2 offset:1984\n\t"
        "v_lshrrev_b32 v15, 3, v1\n\t"
        "v_mul_u32_u24 v15, 0xa0, v15\n\t"
        "v_add_u32 v15, 68864, v15\n\t"
        "v_and_b32 v4, 7, v1\n\t"
        "v_lshl_add_u32 v5, v4, 4, v15\n\t"
        "v_lshl_add_u32 v15, v4, 2, v15\n\t"
        "s_mov_b32 s34, 0x0c0c0c0c\n\t"
        "s_mov_b32 s35, s34\n\t"
        "s_lshl_b64 s[36:37], s[34:35], 2\n\t"
        "s_lshl_b64 s[38:39], s[34:35], 4\n\t"
        "s_waitcnt lgkmcnt(0)\n\t"
        "s_add_u32 s68, s68, 1\n\t"
        "v_mov_b32 v4, s68\n\t"
        "ds_write_b32 v2, v4 offset:76\n\t"
        "s_cmp_eq_u32 s70, 0\n\t"
        "s_cbranch_scc0 .L7_P3\n\t"
        "s_lshl_b32 s78, s68, 24\n\t"
        "v_mov_b32 v8, s78\n\t"
        "s_xor_b32 s78, s78, 0x5a000000\n\t"
        "v_mov_b32 v9, s78\n\t"
        "v_cndmask_b32 v8, v8, v9, s[34:35]\n\t"
        "s_mul_i32 s78, s68, 0x01010000\n\t"
        "v_mov_b32 v9, s78\n\t"
        "v_cndmask_b32 v8, v8, v9, s[36:37]\n\t"
        "s_mul_i32 s55, s68, 0x4000400\n\t"
        "v_xor_b32 v9, s55, v6\n\t"
        "v_cndmask_b32 v8, v8, v9, s[38:39]\n\t"
        "v_and_b32 v8, 0xff000000, v8\n\t"
        "v_or_b32 v8, 0xff0000, v8\n\t"
        "v_mov_b32 v9, v8\n\t"
        "v_mov_b64 v[10:11], v[8:9]\n\t"
        "ds_write_b128 v5, v[8:11] offset:0\n\t"
        "ds_write_b128 v5, v[8:11] offset:1280\n\t"
        "ds_write_b128 v5, v[8:11] offset:2560\n\t"
        "ds_write_b128 v5, v[8:11] offset:3840\n\t"
        "ds_write_b128 v5, v[8:11] offset:5120\n\t"
        "ds_write_b128 v5, v[8:11] offset:6400\n\t"
        "ds_write_b128 v5, v[8:11] offset:7680\n\t"
        "ds_write_b128 v5, v[8:11] offset:8960\n\t"
        "ds_write_b128 v5, v[8:11] offset:10240\n\t"
        "ds_write_b128 v5, v[8:11] offset:11520\n\t"
        "ds_write_b128 v5, v[8:11] offset:12800\n\t"
        "ds_write_b128 v5, v[8:11] offset:14080\n\t"
        "s_waitcnt lgkmcnt(6)\n\t"
        "ds_write_b128 v5, v[8:11] offset:15360\n\t"
        "ds_write_b128 v5, v[8:11] offset:16640\n\t"
        "ds_write_b128 v5, v[8:11] offset:17920\n\t"
        "ds_write_b128 v5, v[8:11] offset:19200\n"
        ".L7_P3:\n\t"
        "s_mov_b64 exec, 1\n\t"
        "ds_add_u32 v2, v3 offset:104\n\t"
        "s_mov_b64 exec, -1\n\t"
        "v_lshlrev_b32 v12, 4, v1\n\t"
        "s_cmp_eq_u32 s70, 0\n\t"
        "s_cbranch_scc0 .L7_Q\n\t"
        "v_add_u32 v15, 10240, v15\n\t"
        "v_add_u32 v97, 1280, v15\n\t"
        "v_add_u32 v98, 2560, v15\n\t"
        "v_add_u32 v99, 3840, v15\n\t"
        "v_add_u32 v100, 5120, v15\n\t"
        "v_add_u32 v101, 6400, v15\n\t"
        "v_add_u32 v102, 7680, v15\n\t"
        "v_add_u32 v103, 8960, v15\n\t"
        "s_lshl_b32 s48, s71, 14\n\t"
        "s_add_u32 s48, s48, 1056768\n\t"
        "s_add_u32 s49, s48, 4096\n"
        ".L7_w44:\n\t"
        "ds_read_b32 v0, v2 offset:112\n\t"
        "s_waitcnt lgkmcnt(0)\n\t"
        "v_cmp_gt_u16 vcc, 1, v0\n\t"
        "s_cbranch_vccz .L7_d45\n\t"
        "s_sleep 0\n\t"
        "s_sub_i32 s83, s83, 1\n\t"
        "s_cmp_gt_i32 s83, 0\n\t"
        "s_cbranch_scc1 .L7_w44\n"
        ".L7_d45:\n\t"
        "ds_read2_b32 v[16:17], v15 offset0:0 offset1:8\n\t"
        "ds_read2_b32 v[18:19], v15 offset0:16 offset1:24\n\t"
        "ds_read2_b32 v[20:21], v97 offset0:0 offset1:8\n\t"
        "ds_read2_b32 v[22:23], v97 offset0:16 offset1:24\n\t"
        "ds_read2_b32 v[24:25], v98 offset0:0 offset1:8\n\t"
        "ds_read2_b32 v[26:27], v98 offset0:16 offset1:24\n\t"
        "ds_read2_b32 v[28:29], v99 offset0:0 offset1:8\n\t"
        "ds_read2_b32 v[30:31], v99 offset0:16 offset1:24\n\t"
        "ds_read2_b32 v[32:33], v100 offset0:0 offset1:8\n\t"
        "ds_read2_b32 v[34:35], v100 offset0:16 offset1:24\n\t"
        "ds_read2_b32 v[36:37], v101 offset0:0 offset1:8\n\t"
        "ds_read2_b32 v[38:39], v101 offset0:16 offset1:24\n\t"
        "s_waitcnt lgkmcnt(10)\n\t"
        "buffer_store_dwordx4 v[16:19], v12, s[20:23], s48 offen offset:0 sc1\n\t"
        "ds_read2_b32 v[40:41], v102 offset0:0 offset1:8\n\t"
        "ds_read2_b32 v[42:43], v102 offset0:16 offset1:24\n\t"
        "s_waitcnt lgkmcnt(10)\n\t"
        "buffer_store_dwordx4 v[20:23], v12, s[20:23], s48 offen offset:1024 sc1\n\t"
        "ds_read2_b32 v[44:45], v103 offset0:0 offset1:8\n\t"
        "ds_read2_b32 v[46:47], v103 offset0:16 offset1:24\n\t"
        "s_mov_b64 exec, 1\n\t"
        "ds_add_u32 v2, v3 offset:72\n\t"
        "s_mov_b64 exec, -1\n\t"
        "s_waitcnt lgkmcnt(11)\n\t"
        "buffer_store_dwordx4 v[24:27], v12, s[20:23], s48 offen offset:2048 sc1\n\t"
        "s_waitcnt lgkmcnt(9)\n\t"
        "buffer_store_dwordx4 v[28:31], v12, s[20:23], s48 offen offset:3072 sc1\n\t"
        "s_waitcnt lgkmcnt(7)\n\t"
        "buffer_store_dwordx4 v[32:35], v12, s[20:23], s49 offen offset:0 sc1\n\t"
        "s_waitcnt lgkmcnt(5)\n\t"
        "buffer_store_dwordx4 v[36:39], v12, s[20:23], s49 offen offset:1024 sc1\n\t"
        "s_waitcnt lgkmcnt(3)\n\t"
        "buffer_store_dwordx4 v[40:43], v12, s[20:23], s49 offen offset:2048 sc1\n\t"
        "s_waitcnt lgkmcnt(1)\n\t"
        "buffer_store_dwordx4 v[44:47], v12, s[20:23], s49 offen offset:3072 sc1\n\t"
        "s_mov_b64 exec, 1\n\t"
        "ds_add_u32 v2, v3 offset:44\n\t"
        "s_mov_b64 exec, -1\n\t"
        "s_branch .L7_Q\n"
        ".L7_V2:\n\t"
        "v_and_b32 v5, 7, v1\n\t"
        "v_mul_u32_u24 v5, 0x420, v5\n\t"
        "v_and_b32 v6, 8, v1\n\t"
        "v_lshl_add_u32 v5, v6, 6, v5\n\t"
        "v_and_b32 v6, 48, v1\n\t"
        "v_add_u32 v5, v5, v6\n\t"
        "v_add_u32 v5, 68864, v5\n\t"
        "s_lshr_b32 s78, s81, 1\n\t"
        "s_lshl3_add_u32 s78, s78, s80\n\t"
        "s_and_b32 s79, s81, 1\n\t"
        "s_lshl1_add_u32 s78, s78, s79\n\t"
        "s_lshl_b32 s82, s78, 10\n\t"
        "s_add_u32 s82, s82, 131072\n"
        ".L7_w40:\n\t"
        "ds_read_b32 v0, v2 offset:104\n\t"
        "ds_read_b32 v6, v2 offset:76\n\t"
        "s_waitcnt lgkmcnt(1)\n\t"
        "v_cmp_gt_u16 vcc, 1, v0\n\t"
        "s_cbranch_vccz .L7_d41\n\t"
        "s_waitcnt lgkmcnt(0)\n\t"
        "s_sleep 1\n\t"
        "s_sub_i32 s83, s83, 1\n\t"
        "s_cmp_gt_i32 s83, 0\n\t"
        "s_cbranch_scc1 .L7_w40\n"
        ".L7_d41:\n\t"
        "s_waitcnt lgkmcnt(0)\n\t"
        "v_readfirstlane_b32 s68, v6\n\t"
        "v_mov_b32 v7, 2\n\t"
        "s_and_b32 s73, s68, 1\n\t"
        "s_cselect_b32 s73, 1, 0x10000\n\t"
        "v_mov_b32 v16, s73\n\t"
        "v_and_b32 v17, 7, v1\n\t"
        "v_add_u32 v17, 1, v17\n\t"
        "v_lshrrev_b32 v17, v17, s68\n\t"
        "s_mov_b32 s49, 0x10001\n\t"
        "v_and_b32 v17, s49, v17\n\t"
        "s_mov_b32 s48, 0x3db8aa3b\n\t"
        "s_waitcnt vmcnt(0)\n\t"
        "ds_read_b128 v[32:35], v5 offset:0\n\t"
        "ds_read_b128 v[36:39], v5 offset:8448\n\t"
        "ds_read_b128 v[40:43], v5 offset:16896\n\t"
        "ds_read_b128 v[44:47], v5 offset:64\n\t"
        "ds_read_b128 v[48:51], v5 offset:8512\n\t"
        "ds_read_b128 v[52:55], v5 offset:16960\n\t"
        "ds_read_b128 v[56:59], v5 offset:128\n\t"
        "ds_read_b128 v[60:63], v5 offset:8576\n\t"
        "ds_read_b128 v[64:67], v5 offset:17024\n\t"
        "ds_read_b128 v[68:71], v5 offset:192\n\t"
        "ds_read_b128 v[72:75], v5 offset:8640\n\t"
        "ds_read_b128 v[76:79], v5 offset:17088\n\t"
        "s_waitcnt lgkmcnt(9)\n\t"
        "v_mfma_f32_16x16x32_bf16 v[20:23], v[36:39], v[32:35], 0\n\t"
        "v_mfma_f32_16x16x32_bf16 v[24:27], v[40:43], v[32:35], 0\n\t"
        "ds_read_b128 v[80:83], v5 offset:256\n\t"
        "ds_read_b128 v[84:87], v5 offset:8704\n\t"
        "ds_read_b128 v[88:91], v5 offset:17152\n\t"
        "s_waitcnt lgkmcnt(9)\n\t"
        "v_mfma_f32_16x16x32_bf16 v[20:23], v[48:51], v[44:47], v[20:23]\n\t"
        "v_mfma_f32_16x16x32_bf16 v[24:27], v[52:55], v[44:47], v[24:27]\n\t"
        "ds_read_b128 v[92:95], v5 offset:320\n\t"
        "ds_read_b128 v[96:99], v5 offset:8768\n\t"
        "ds_read_b128 v[100:103], v5 offset:17216\n\t"
        "s_waitcnt lgkmcnt(9)\n\t"
        "v_mfma_f32_16x16x32_bf16 v[20:23], v[60:63], v[56:59], v[20:23]\n\t"
        "v_mfma_f32_16x16x32_bf16 v[24:27], v[64:67], v[56:59], v[24:27]\n\t"
        "ds_read_b128 v[104:107], v5 offset:384\n\t"
        "ds_read_b128 v[108:111], v5 offset:8832\n\t"
        "ds_read_b128 v[112:115], v5 offset:17280\n\t"
        "s_waitcnt lgkmcnt(9)\n\t"
        "v_mfma_f32_16x16x32_bf16 v[20:23], v[72:75], v[68:71], v[20:23]\n\t"
        "v_mfma_f32_16x16x32_bf16 v[24:27], v[76:79], v[68:71], v[24:27]\n\t"
        "ds_read_b128 v[116:119], v5 offset:448\n\t"
        "ds_read_b128 v[120:123], v5 offset:8896\n\t"
        "ds_read_b128 v[124:127], v5 offset:17344\n\t"
        "s_waitcnt lgkmcnt(9)\n\t"
        "v_mfma_f32_16x16x32_bf16 v[20:23], v[84:87], v[80:83], v[20:23]\n\t"
        "v_mfma_f32_16x16x32_bf16 v[24:27], v[88:91], v[80:83], v[24:27]\n\t"
        "s_mov_b64 exec, 1\n\t"
        "ds_add_u32 v2, v7 offset:72\n\t"
        "s_mov_b64 exec, -1\n\t"
        "s_waitcnt lgkmcnt(7)\n\t"
        "v_mfma_f32_16x16x32_bf16 v[20:23], v[96:99], v[92:95], v[20:23]\n\t"
        "v_mfma_f32_16x16x32_bf16 v[24:27], v[100:103], v[92:95], v[24:27]\n\t"
        "s_waitcnt lgkmcnt(4)\n\t"
        "v_mfma_f32_16x16x32_bf16 v[20:23], v[108:111], v[104:107], v[20:23]\n\t"
        "v_mfma_f32_16x16x32_bf16 v[24:27], v[112:115], v[104:107], v[24:27]\n\t"
        "s_waitcnt lgkmcnt(1)\n\t"
        "v_mfma_f32_16x16x32_bf16 v[20:23], v[120:123], v[116:119], v[20:23]\n\t"
        "v_mfma_f32_16x16x32_bf16 v[24:27], v[124:127], v[116:119], v[24:27]\n\t"
        "s_nop 6\n\t"
        "v_mul_f32 v20, s48, v20\n\t"
        "v_mul_f32 v21, s48, v21\n\t"
        "v_mul_f32 v22, s48, v22\n\t"
        "v_mul_f32 v23, s48, v23\n\t"
        "v_mul_f32 v24, s48, v24\n\t"
        "v_mul_f32 v25, s48, v25\n\t"
        "v_mul_f32 v26, s48, v26\n\t"
        "v_mul_f32 v27, s48, v27\n\t"
        "s_mov_b32 s48, 0x07060302\n\t"
        "v_add_u32 v20, 0x10000, v20\n\t"
        "v_add_u32 v21, 0x10000, v21\n\t"
        "v_add_u32 v22, 0x10000, v22\n\t"
        "v_add_u32 v23, 0x10000, v23\n\t"
        "v_perm_b32 v20, v21, v20, s48\n\t"
        "v_perm_b32 v21, v23, v22, s48\n\t"
        "v_cvt_pk_bf16_f32 v22, v24, v25\n\t"
        "v_cvt_pk_bf16_f32 v23, v26, v27\n\t"
        "v_bfi_b32 v20, s49, v16, v20\n\t"
        "v_bfi_b32 v21, s49, v17, v21\n\t"
        "buffer_store_dwordx4 v[20:23], v12, s[20:23], s82 offen sc1\n\t"
        "s_mov_b64 exec, 1\n\t"
        "ds_add_u32 v2, v7 offset:44\n\t"
        "s_mov_b64 exec, -1\n\t"
        "s_branch .L7_Q\n"
        ".L7_T2:\n\t"
        "v_lshlrev_b32 v4, 4, v1\n\t"
        "v_add_u32 v4, 148160, v4\n\t"
        "v_mul_u32_u24 v5, 196, v1\n\t"
        "v_add_u32 v5, 148160, v5\n\t"
        "v_mov_b32 v12, 0xffff\n\t"
        "v_mov_b32 v13, v12\n\t"
        "v_mov_b64 v[14:15], v[12:13]\n\t"
        "ds_write_b128 v4, v[12:15] offset:0\n\t"
        "ds_write_b128 v4, v[12:15] offset:1024\n\t"
        "ds_write_b128 v4, v[12:15] offset:2048\n\t"
        "ds_write_b128 v4, v[12:15] offset:3072\n\t"
        "ds_write_b128 v4, v[12:15] offset:4096\n\t"
        "ds_write_b128 v4, v[12:15] offset:5120\n\t"
        "ds_write_b128 v4, v[12:15] offset:6144\n\t"
        "ds_write_b128 v4, v[12:15] offset:7168\n\t"
        "ds_write_b128 v4, v[12:15] offset:8192\n\t"
        "ds_write_b128 v4, v[12:15] offset:9216\n\t"
        "ds_write_b128 v4, v[12:15] offset:10240\n\t"
        "ds_write_b128 v4, v[12:15] offset:11264\n\t"
        "s_waitcnt lgkmcnt(6)\n\t"
        "ds_write_b128 v4, v[12:15] offset:12288\n\t"
        "ds_write_b128 v4, v[12:15] offset:13312\n\t"
        "ds_write_b128 v4, v[12:15] offset:14336\n\t"
        "ds_write_b128 v4, v[12:15] offset:15360\n\t"
        "ds_write_b32 v5, v2 offset:192\n\t"
        "s_mov_b64 exec, 0xffff\n\t"
        "ds_write_b32 v5, v2 offset:12736\n\t"
        "s_mov_b64 exec, 1\n\t"
        "ds_add_u32 v2, v3 offset:8\n\t"
        "s_mov_b64 exec, -1\n\t"
        "s_and_b32 s82, s82, -1024\n\t"
        "v_mov_b32 v9, 4\n"
        ".L7_g42:\n\t"
        "s_memrealtime s[78:79]\n\t"
        "s_waitcnt lgkmcnt(0)\n\t"
        "s_sub_u32 s78, s78, s74\n\t"
        "s_cmp_ge_i32 s78, 150\n\t"
        "s_cbranch_scc1 .L7_h43\n\t"
        "s_sleep 1\n\t"
        "s_branch .L7_g42\n"
        ".L7_h43:\n"
        ".L7_b1:\n\t"
        "s_buffer_load_dword s40, s[20:23], s82 offset:0 glc\n\t"
        "s_buffer_load_dword s41, s[20:23], s82 offset:128 glc\n\t"
        "s_buffer_load_dword s42, s[20:23], s82 offset:256 glc\n\t"
        "s_buffer_load_dword s43, s[20:23], s82 offset:384 glc\n\t"
        "s_buffer_load_dword s44, s[20:23], s82 offset:512 glc\n\t"
        "s_buffer_load_dword s45, s[20:23], s82 offset:640 glc\n\t"
        "s_buffer_load_dword s46, s[20:23], s82 offset:768 glc\n\t"
        "s_buffer_load_dword s47, s[20:23], s82 offset:896 glc\n\t"
        "s_waitcnt lgkmcnt(0)\n\t"
        "s_mov_b32 s80, 0\n\t"
        "s_mov_b32 s81, 0\n\t"
        "s_sub_u32 s40, s40, s74\n\t"
        "s_max_i32 s80, s80, s40\n\t"
        "s_min_i32 s81, s81, s40\n\t"
        "s_sub_u32 s41, s41, s74\n\t"
        "s_max_i32 s80, s80, s41\n\t"
        "s_min_i32 s81, s81, s41\n\t"
        "s_sub_u32 s42, s42, s74\n\t"
        "s_max_i32 s80, s80, s42\n\t"
        "s_min_i32 s81, s81, s42\n\t"
        "s_sub_u32 s43, s43, s74\n\t"
        "s_max_i32 s80, s80, s43\n\t"
        "s_min_i32 s81, s81, s43\n\t"
        "s_sub_u32 s44, s44, s74\n\t"
        "s_max_i32 s80, s80, s44\n\t"
        "s_min_i32 s81, s81, s44\n\t"
        "s_sub_u32 s45, s45, s74\n\t"
        "s_max_i32 s80, s80, s45\n\t"
        "s_min_i32 s81, s81, s45\n\t"
        "s_sub_u32 s46, s46, s74\n\t"
        "s_max_i32 s80, s80, s46\n\t"
        "s_min_i32 s81, s81, s46\n\t"
        "s_sub_u32 s47, s47, s74\n\t"
        "s_max_i32 s80, s80, s47\n\t"
        "s_min_i32 s81, s81, s47\n\t"
        "s_sub_u32 s81, s80, s81\n\t"
        "s_cmp_lt_u32 s81, 0x3e8\n\t"
        "s_cbranch_scc1 .L7_b2\n\t"
        "s_mov_b64 exec, 1\n\t"
        "ds_add_u32 v2, v9 offset:84\n\t"
        "s_mov_b64 exec, -1\n\t"
        "s_mov_b64 exec, 1\n\t"
        "ds_add_u32 v2, v9 offset:108\n\t"
        "s_mov_b64 exec, -1\n\t"
        "v_mov_b32 v9, 0\n\t"
        "s_mov_b32 s80, 0\n\t"
        "s_sleep 4\n\t"
        "s_memrealtime s[78:79]\n\t"
        "s_waitcnt lgkmcnt(0)\n\t"
        "s_sub_u32 s78, s78, s74\n\t"
        "s_cmp_lt_u32 s78, 0x4b0\n\t"
        "s_cbranch_scc1 .L7_b1\n"
        ".L7_b2:\n\t"
        "s_add_u32 s80, s80, 1\n\t"
        "v_mov_b32 v7, s80\n\t"
        "s_mov_b64 exec, 1\n\t"
        "ds_add_u32 v2, v7 offset:92\n\t"
        "s_mov_b64 exec, -1\n\t"
        "s_branch .L7_Q0\n"
        ".L7_R2:\n\t"
        "s_sub_u32 s79, s64, s72\n\t"
        "s_min_i32 s79, s79, s65\n\t"
        "s_max_i32 s79, s79, 0\n\t"
        "v_mov_b32 v32, v1\n\t"
        "v_add_u32 v33, 64, v1\n\t"
        "v_cmp_gt_u32 s[32:33], s79, v32\n\t"
        "v_cmp_gt_u32 s[34:35], s79, v33\n\t"
        "v_cmp_gt_u32 s[36:37], 40, v1\n\t"
        "s_sub_u32 s78, 0xff, s65\n\t"
        "v_add_u32 v34, s78, v32\n\t"
        "v_add_u32 v6, 0xffffffb0, v34\n\t"
        "v_cmp_gt_u32 vcc, 0xff, v34\n\t"
        "s_nop 1\n\t"
        "v_cndmask_b32 v34, v6, v34, vcc\n\t"
        "v_add_u32 v35, s78, v33\n\t"
        "v_add_u32 v6, 0xffffffb0, v35\n\t"
        "v_cmp_gt_u32 vcc, 0xff, v35\n\t"
        "s_nop 1\n\t"
        "v_cndmask_b32 v35, v6, v35, vcc\n\t"
        "v_mov_b32 v12, 0x8000\n\t"
        "v_cndmask_b32 v44, v12, 0, s[32:33]\n\t"
        "v_cndmask_b32 v45, v12, 0, s[34:35]\n\t"
        "v_cndmask_b32 v46, 63, 31, s[36:37]\n\t"
        "v_mov_b32 v47, 63\n\t"
        "s_waitcnt vmcnt(0)\n\t"
        "v_lshlrev_b32 v16, 16, v16\n\t"
        "v_lshlrev_b32 v17, 16, v17\n\t"
        "v_mul_f32 v36, v16, v16\n\t"
        "v_mul_f32 v37, v17, v17\n\t"
        "v_rcp_f32 v36, v36\n\t"
        "v_rcp_f32 v37, v37\n\t"
        "v_or3_b32 v16, v16, v34, v44\n\t"
        "v_or3_b32 v17, v17, v35, v45\n\t"
        "v_add_f32 v36, 0xc0600000, v36\n\t"
        "v_add_f32 v37, 0xc0600000, v37\n\t"
        "v_cvt_u32_f32 v36, v36\n\t"
        "v_cvt_u32_f32 v37, v37\n\t"
        "v_min_u32 v36, 31, v36\n\t"
        "v_min_u32 v37, 31, v37\n\t"
        "v_cndmask_b32 v36, 0, v36, s[32:33]\n\t"
        "v_cndmask_b32 v37, 0, v37, s[34:35]\n\t"
        "v_sub_u32 v36, v46, v36\n\t"
        "v_sub_u32 v37, v47, v37\n\t"
        "v_lshlrev_b32 v36, 2, v36\n\t"
        "v_lshlrev_b32 v37, 2, v37\n\t"
        "ds_add_rtn_u32 v40, v36, v3 offset:256\n\t"
        "s_mov_b64 exec, 0xffff\n\t"
        "ds_add_rtn_u32 v41, v37, v3 offset:256\n\t"
        "s_mov_b64 exec, -1\n\t"
        "v_lshlrev_b32 v9, 2, v1\n\t"
        "ds_read_b32 v10, v9 offset:256\n\t"
        "s_cmp_eq_u32 s70, 0\n\t"
        "s_cselect_b32 s78, 56, 0\n\t"
        "s_cselect_b32 s79, 0, 56\n\t"
        "v_mov_b32 v49, -40\n\t"
        "v_cndmask_b32 v48, v49, 0, s[36:37]\n\t"
        "s_mov_b32 s40, 0x23108954\n\t"
        "s_mov_b32 s41, 0x67\n\t"
        "s_mov_b32 s42, 0x1986732\n\t"
        "s_mov_b32 s43, 0x45\n\t"
        "v_mov_b32 v13, s78\n\t"
        "v_mov_b32 v14, s79\n\t"
        "v_cndmask_b32 v13, v14, v13, s[36:37]\n\t"
        "s_waitcnt lgkmcnt(0)\n\t"
        "v_mov_b32 v11, v10\n\t"
        "s_nop 1\n\t"
        "v_add_u32_dpp v11, v11, v11 row_shr:1 row_mask:0xf bank_mask:0xf bound_ctrl:1\n\t"
        "s_nop 1\n\t"
        "v_add_u32_dpp v11, v11, v11 row_shr:2 row_mask:0xf bank_mask:0xf bound_ctrl:1\n\t"
        "s_nop 1\n\t"
        "v_add_u32_dpp v11, v11, v11 row_shr:4 row_mask:0xf bank_mask:0xf bound_ctrl:1\n\t"
        "s_nop 1\n\t"
        "v_add_u32_dpp v11, v11, v11 row_shr:8 row_mask:0xf bank_mask:0xf bound_ctrl:1\n\t"
        "s_nop 1\n\t"
        "v_add_u32_dpp v11, v11, v11 row_bcast:15 row_mask:0xa bank_mask:0xf\n\t"
        "s_nop 1\n\t"
        "v_add_u32_dpp v11, v11, v11 row_bcast:31 row_mask:0xc bank_mask:0xf\n\t"
        "v_sub_u32 v11, v11, v10\n\t"
        "ds_bpermute_b32 v36, v36, v11\n\t"
        "ds_bpermute_b32 v37, v37, v11\n\t"
        "v_lshlrev_b32 v9, 1, v1\n\t"
        "s_waitcnt lgkmcnt(0)\n\t"
        "v_add3_u32 v36, v36, v40, v48\n\t"
        "v_and_b32 v6, -4, v36\n\t"
        "v_lshrrev_b64 v[10:11], v6, s[40:41]\n\t"
        "v_lshrrev_b64 v[50:51], v6, s[42:43]\n\t"
        "v_cmp_eq_u32 vcc, 0, v13\n\t"
        "s_nop 1\n\t"
        "v_cndmask_b32 v10, v10, v50, vcc\n\t"
        "v_and_b32 v10, 15, v10\n\t"
        "v_and_b32 v6, 3, v36\n\t"
        "v_lshl_add_u32 v6, v10, 2, v6\n\t"
        "v_add_u32 v36, v6, v13\n\t"
        "v_and_b32 v6, 15, v36\n\t"
        "v_lshlrev_b32 v6, 4, v6\n\t"
        "v_lshl_add_u32 v6, v36, 8, v6\n\t"
        "v_and_b32 v10, 8, v13\n\t"
        "v_lshlrev_b32 v10, 8, v10\n\t"
        "v_sub_u32 v6, v6, v10\n\t"
        "v_lshlrev_b32 v36, 2, v36\n\t"
        "ds_write_b32 v36, v16 offset:512\n\t"
        "ds_write_b16 v9, v6 offset:1792\n\t"
        "v_add3_u32 v37, v37, v41, v49\n\t"
        "v_and_b32 v6, -4, v37\n\t"
        "v_lshrrev_b64 v[10:11], v6, s[40:41]\n\t"
        "v_lshrrev_b64 v[50:51], v6, s[42:43]\n\t"
        "v_cmp_eq_u32 vcc, 0, v14\n\t"
        "s_nop 1\n\t"
        "v_cndmask_b32 v10, v10, v50, vcc\n\t"
        "v_and_b32 v10, 15, v10\n\t"
        "v_and_b32 v6, 3, v37\n\t"
        "v_lshl_add_u32 v6, v10, 2, v6\n\t"
        "v_add_u32 v37, v6, v14\n\t"
        "v_and_b32 v6, 15, v37\n\t"
        "v_lshlrev_b32 v6, 4, v6\n\t"
        "v_lshl_add_u32 v6, v37, 8, v6\n\t"
        "v_and_b32 v10, 8, v14\n\t"
        "v_lshlrev_b32 v10, 8, v10\n\t"
        "v_sub_u32 v6, v6, v10\n\t"
        "v_lshlrev_b32 v37, 2, v37\n\t"
        "s_mov_b64 exec, 0xffff\n\t"
        "ds_write_b32 v37, v17 offset:512\n\t"
        "ds_write_b16 v9, v6 offset:1920\n\t"
        "s_mov_b64 exec, 1\n\t"
        "ds_add_u32 v2, v3 offset:8\n\t"
        "s_mov_b64 exec, -1\n"
        ".L7_w46:\n\t"
        "ds_read_b64 v[4:5], v2 offset:72\n\t"
        "s_waitcnt lgkmcnt(0)\n\t"
        "v_cmp_gt_u16 vcc, 2, v4\n\t"
        "s_cbranch_vccz .L7_d47\n\t"
        "s_sleep 2\n\t"
        "s_sub_i32 s83, s83, 1\n\t"
        "s_cmp_gt_i32 s83, 0\n\t"
        "s_cbranch_scc1 .L7_w46\n"
        ".L7_d47:\n\t"
        "v_readfirstlane_b32 s68, v5\n\t"
        "v_lshlrev_b32 v22, 4, v1\n\t"
        "v_add_u32 v9, 142848, v22\n\t"
        "v_and_b32 v10, 60, v1\n\t"
        "ds_read_b32 v24, v10 offset:704\n\t"
        "ds_read_b32 v25, v10 offset:768\n\t"
        "ds_read_b32 v26, v10 offset:832\n\t"
        "v_and_b32 v11, 15, v1\n\t"
        "v_lshrrev_b32 v12, 4, v1\n\t"
        "v_xor_b32 v16, v11, v12\n\t"
        "v_lshlrev_b32 v16, 4, v16\n\t"
        "v_lshl_add_u32 v16, v11, 8, v16\n\t"
        "v_xor_b32 v17, 64, v16\n\t"
        "v_xor_b32 v18, 128, v16\n\t"
        "v_xor_b32 v19, 192, v16\n\t"
        "ds_write_b128 v9, v[16:19]\n\t"
        "s_and_b32 s73, s68, 1\n\t"
        "s_cselect_b32 s73, 1, 0x10000\n\t"
        "v_mov_b32 v20, s73\n\t"
        "v_and_b32 v21, 7, v1\n\t"
        "v_add_u32 v21, 1, v21\n\t"
        "v_lshrrev_b32 v21, v21, s68\n\t"
        "s_mov_b32 s49, 0x10001\n\t"
        "v_and_b32 v21, s49, v21\n\t"
        "v_and_b32 v23, 0x37f, v22\n\t"
        "ds_write_b128 v9, v[20:23] offset:1024\n\t"
        "s_add_u32 s78, s72, s65\n\t"
        "s_sub_u32 s78, s78, 0xff\n\t"
        "s_lshl_b32 s78, s78, 9\n\t"
        "v_and_b32 v13, 48, v22\n\t"
        "v_add_u32 v14, s78, v13\n\t"
        "s_movk_i32 s78, 0x200\n\t"
        "v_bfe_u32 v27, v1, 1, 2\n\t"
        "v_xor_b32 v27, v27, v12\n\t"
        "v_lshlrev_b32 v27, 4, v27\n\t"
        "v_lshl_add_u32 v27, v11, 6, v27\n\t"
        "v_lshlrev_b32 v28, 1, v1\n\t"
        "v_and_b32 v28, 48, v28\n\t"
        "v_xor_b32 v28, v28, v22\n\t"
        "s_waitcnt lgkmcnt(2)\n\t"
        "v_mad_u32_u16 v24, v24, s78, v14\n\t"
        "v_mad_u32_u16 v25, v25, s78, v14\n\t"
        "v_mad_u32_u16 v26, v26, s78, v14\n\t"
        "s_mov_b32 exec_hi, 0\n\t"
        "v_mov_b32 v24, -2.0\n\t"
        "s_mov_b64 exec, -1\n\t"
        "ds_write_b128 v9, v[24:27] offset:2048\n\t"
        "s_mov_b32 s78, 0x3f80\n\t"
        "s_mov_b32 s79, 0\n\t"
        "v_lshlrev_b64 v[30:31], v13, s[78:79]\n\t"
        "s_mov_b32 s78, 0x00f0000f\n\t"
        "s_mov_b32 s79, 0xf0000f00\n\t"
        "v_cndmask_b32 v30, 0, v30, s[78:79]\n\t"
        "v_cndmask_b32 v31, 0, v31, s[78:79]\n\t"
        "ds_write_b128 v9, v[28:31] offset:3072\n\t"
        "s_branch .L7_Q\n"
        ".L7_Q0:\n"
        ".L7_w48:\n\t"
        "ds_read_b64 v[4:5], v2 offset:72\n\t"
        "s_waitcnt lgkmcnt(0)\n\t"
        "v_cmp_gt_u16 vcc, 2, v4\n\t"
        "s_cbranch_vccz .L7_d49\n\t"
        "s_sleep 2\n\t"
        "s_sub_i32 s83, s83, 1\n\t"
        "s_cmp_gt_i32 s83, 0\n\t"
        "s_cbranch_scc1 .L7_w48\n"
        ".L7_d49:\n\t"
        "v_readfirstlane_b32 s68, v5\n"
        ".L7_Q:\n\t"
        "s_setprio 2\n\t"
        "v_and_b32 v5, 7, v1\n\t"
        "v_mul_u32_u24 v5, 0x420, v5\n\t"
        "v_and_b32 v6, 8, v1\n\t"
        "v_lshl_add_u32 v5, v6, 6, v5\n\t"
        "v_and_b32 v6, 48, v1\n\t"
        "v_add_u32 v5, v5, v6\n\t"
        "s_and_b32 s73, s68, 1\n\t"
        "s_cselect_b32 s73, 1, 0x10000\n\t"
        "v_and_b32 v6, 7, v1\n\t"
        "v_add_u32 v6, 1, v6\n\t"
        "v_lshrrev_b32 v6, v6, s68\n\t"
        "s_mov_b32 s49, 0x10001\n\t"
        "v_and_b32 v6, s49, v6\n\t"
        "s_add_u32 s80, s69, s76\n\t"
        "s_and_b32 s80, s80, 3\n\t"
        "s_lshl2_add_u32 s78, s70, s80\n\t"
        "s_lshl_b32 s78, s78, 14\n\t"
        "s_add_u32 s78, s78, 131072\n\t"
        "v_lshl_add_u32 v4, v1, 4, s78\n\t"
        "s_mov_b32 s24, 4096\n\t"
        "s_mov_b32 s25, 8192\n\t"
        "s_mov_b32 s26, 12288\n"
        ".L7_w50:\n\t"
        "ds_read_b32 v0, v2 offset:92\n\t"
        "s_waitcnt lgkmcnt(0)\n\t"
        "v_cmp_gt_u16 vcc, 1, v0\n\t"
        "s_cbranch_vccz .L7_d51\n\t"
        "s_sleep 1\n\t"
        "s_sub_i32 s83, s83, 1\n\t"
        "s_cmp_gt_i32 s83, 0\n\t"
        "s_cbranch_scc1 .L7_w50\n"
        ".L7_d51:\n\t"
        "v_readfirstlane_b32 s78, v0\n\t"
        "s_sub_u32 s78, s78, 1\n\t"
        "s_add_u32 s77, s74, s78\n"
        ".L7_g52:\n\t"
        "s_memrealtime s[78:79]\n\t"
        "s_waitcnt lgkmcnt(0)\n\t"
        "s_sub_u32 s78, s78, s77\n\t"
        "s_cmp_ge_i32 s78, 280\n\t"
        "s_cbranch_scc1 .L7_h53\n\t"
        "s_sleep 1\n\t"
        "s_branch .L7_g52\n"
        ".L7_h53:\n"
        ".L7_w54:\n\t"
        "ds_read_b32 v0, v2 offset:88\n\t"
        "s_waitcnt lgkmcnt(0)\n\t"
        "v_cmp_gt_u16 vcc, 8, v0\n\t"
        "s_cbranch_vccz .L7_d55\n\t"
        "s_sleep 1\n\t"
        "s_sub_i32 s83, s83, 1\n\t"
        "s_cmp_gt_i32 s83, 0\n\t"
        "s_cbranch_scc1 .L7_w54\n"
        ".L7_d55:\n\t"
        "buffer_load_dwordx4 v[64:67], v4, s[20:23], 0 offen offset:0 sc1\n\t"
        "buffer_load_dwordx4 v[68:71], v4, s[20:23], 0 offen offset:1024 sc1\n\t"
        "buffer_load_dwordx4 v[72:75], v4, s[20:23], 0 offen offset:2048 sc1\n\t"
        "buffer_load_dwordx4 v[76:79], v4, s[20:23], 0 offen offset:3072 sc1\n\t"
        "buffer_load_dwordx4 v[80:83], v4, s[20:23], s24 offen offset:0 sc1\n\t"
        "buffer_load_dwordx4 v[84:87], v4, s[20:23], s24 offen offset:1024 sc1\n\t"
        "buffer_load_dwordx4 v[88:91], v4, s[20:23], s24 offen offset:2048 sc1\n\t"
        "buffer_load_dwordx4 v[92:95], v4, s[20:23], s24 offen offset:3072 sc1\n\t"
        "buffer_load_dwordx4 v[96:99], v4, s[20:23], s25 offen offset:0 sc1\n\t"
        "buffer_load_dwordx4 v[100:103], v4, s[20:23], s25 offen offset:1024 sc1\n\t"
        "buffer_load_dwordx4 v[104:107], v4, s[20:23], s25 offen offset:2048 sc1\n\t"
        "buffer_load_dwordx4 v[108:111], v4, s[20:23], s25 offen offset:3072 sc1\n\t"
        "buffer_load_dwordx4 v[112:115], v4, s[20:23], s26 offen offset:0 sc1\n\t"
        "buffer_load_dwordx4 v[116:119], v4, s[20:23], s26 offen offset:1024 sc1\n\t"
        "buffer_load_dwordx4 v[120:123], v4, s[20:23], s26 offen offset:2048 sc1\n\t"
        "buffer_load_dwordx4 v[124:127], v4, s[20:23], s26 offen offset:3072 sc1\n\t"
        "s_setprio 3\n\t"
        "s_mov_b64 exec, 1\n\t"
        "ds_add_u32 v2, v3 offset:84\n\t"
        "s_mov_b64 exec, -1\n"
        ".L7_w56:\n\t"
        "ds_read_b32 v0, v2 offset:0\n\t"
        "s_waitcnt lgkmcnt(0)\n\t"
        "v_cmp_gt_u16 vcc, 4, v0\n\t"
        "s_cbranch_vccz .L7_d57\n\t"
        "s_sleep 1\n\t"
        "s_sub_i32 s83, s83, 1\n\t"
        "s_cmp_gt_i32 s83, 0\n\t"
        "s_cbranch_scc1 .L7_w56\n"
        ".L7_d57:\n\t"
        "s_branch .L7_qg\n"
        ".L7_qr:\n\t"
        "s_waitcnt lgkmcnt(0)\n\t"
        "s_sleep 8\n\t"
        "s_sub_i32 s83, s83, 64\n\t"
        "s_cmp_gt_i32 s83, 0\n\t"
        "s_cbranch_scc0 .L7_qd\n\t"
        "buffer_load_dwordx4 v[64:67], v4, s[20:23], 0 offen offset:0 sc1\n\t"
        "buffer_load_dwordx4 v[68:71], v4, s[20:23], 0 offen offset:1024 sc1\n\t"
        "buffer_load_dwordx4 v[72:75], v4, s[20:23], 0 offen offset:2048 sc1\n\t"
        "buffer_load_dwordx4 v[76:79], v4, s[20:23], 0 offen offset:3072 sc1\n\t"
        "buffer_load_dwordx4 v[80:83], v4, s[20:23], s24 offen offset:0 sc1\n\t"
        "buffer_load_dwordx4 v[84:87], v4, s[20:23], s24 offen offset:1024 sc1\n\t"
        "buffer_load_dwordx4 v[88:91], v4, s[20:23], s24 offen offset:2048 sc1\n\t"
        "buffer_load_dwordx4 v[92:95], v4, s[20:23], s24 offen offset:3072 sc1\n\t"
        "buffer_load_dwordx4 v[96:99], v4, s[20:23], s25 offen offset:0 sc1\n\t"
        "buffer_load_dwordx4 v[100:103], v4, s[20:23], s25 offen offset:1024 sc1\n\t"
        "buffer_load_dwordx4 v[104:107], v4, s[20:23], s25 offen offset:2048 sc1\n\t"
        "buffer_load_dwordx4 v[108:111], v4, s[20:23], s25 offen offset:3072 sc1\n\t"
        "buffer_load_dwordx4 v[112:115], v4, s[20:23], s26 offen offset:0 sc1\n\t"
        "buffer_load_dwordx4 v[116:119], v4, s[20:23], s26 offen offset:1024 sc1\n\t"
        "buffer_load_dwordx4 v[120:123], v4, s[20:23], s26 offen offset:2048 sc1\n\t"
        "buffer_load_dwordx4 v[124:127], v4, s[20:23], s26 offen offset:3072 sc1\n"
        ".L7_qg:\n\t"
        "v_mov_b32 v7, 0\n\t"
        "ds_read_b128 v[8:11], v5 offset:2048\n\t"
        "ds_read_b128 v[12:15], v5 offset:10496\n\t"
        "ds_read_b128 v[16:19], v5 offset:18944\n\t"
        "ds_read_b128 v[20:23], v5 offset:27392\n\t"
        "s_waitcnt vmcnt(14) lgkmcnt(3)\n\t"
        "v_xor_b32 v64, s73, v64\n\t"
        "v_xor_b32 v65, v6, v65\n\t"
        "v_xor_b32 v68, s73, v68\n\t"
        "v_xor_b32 v69, v6, v69\n\t"
        "v_mfma_f32_16x16x32_bf16 v[24:27], v[64:67], v[8:11], 0\n\t"
        "s_nop 0\n\t"
        "v_mfma_f32_16x16x32_bf16 v[28:31], v[68:71], v[8:11], 0\n\t"
        "v_or3_b32 v7, v7, v64, v65\n\t"
        "ds_read_b128 v[8:11], v5 offset:35840\n\t"
        "s_waitcnt lgkmcnt(3)\n\t"
        "v_mfma_f32_16x16x32_bf16 v[32:35], v[64:67], v[12:15], 0\n\t"
        "v_mfma_f32_16x16x32_bf16 v[36:39], v[68:71], v[12:15], 0\n\t"
        "v_or3_b32 v7, v7, v68, v69\n\t"
        "ds_read_b128 v[12:15], v5 offset:2112\n\t"
        "s_waitcnt lgkmcnt(3)\n\t"
        "v_mfma_f32_16x16x32_bf16 v[40:43], v[64:67], v[16:19], 0\n\t"
        "v_mfma_f32_16x16x32_bf16 v[44:47], v[68:71], v[16:19], 0\n\t"
        "ds_read_b128 v[16:19], v5 offset:10560\n\t"
        "s_waitcnt lgkmcnt(3)\n\t"
        "v_mfma_f32_16x16x32_bf16 v[48:51], v[64:67], v[20:23], 0\n\t"
        "v_mfma_f32_16x16x32_bf16 v[52:55], v[68:71], v[20:23], 0\n\t"
        "ds_read_b128 v[20:23], v5 offset:19008\n\t"
        "s_waitcnt vmcnt(12) lgkmcnt(3)\n\t"
        "v_xor_b32 v72, s73, v72\n\t"
        "v_xor_b32 v73, v6, v73\n\t"
        "v_xor_b32 v76, s73, v76\n\t"
        "v_xor_b32 v77, v6, v77\n\t"
        "v_mfma_f32_16x16x32_bf16 v[56:59], v[64:67], v[8:11], 0\n\t"
        "v_mfma_f32_16x16x32_bf16 v[60:63], v[68:71], v[8:11], 0\n\t"
        "ds_read_b128 v[8:11], v5 offset:27456\n\t"
        "ds_read_b128 v[64:67], v5 offset:35904\n\t"
        "ds_read_b128 v[68:71], v5 offset:2176\n\t"
        "s_waitcnt lgkmcnt(4)\n\t"
        "v_mfma_f32_16x16x32_bf16 v[24:27], v[72:75], v[12:15], v[24:27]\n\t"
        "v_mfma_f32_16x16x32_bf16 v[28:31], v[76:79], v[12:15], v[28:31]\n\t"
        "v_or3_b32 v7, v7, v72, v73\n\t"
        "ds_read_b128 v[12:15], v5 offset:10624\n\t"
        "v_mfma_f32_16x16x32_bf16 v[32:35], v[72:75], v[16:19], v[32:35]\n\t"
        "v_mfma_f32_16x16x32_bf16 v[36:39], v[76:79], v[16:19], v[36:39]\n\t"
        "v_or3_b32 v7, v7, v76, v77\n\t"
        "ds_read_b128 v[16:19], v5 offset:19072\n\t"
        "s_waitcnt lgkmcnt(4)\n\t"
        "v_mfma_f32_16x16x32_bf16 v[40:43], v[72:75], v[20:23], v[40:43]\n\t"
        "v_mfma_f32_16x16x32_bf16 v[44:47], v[76:79], v[20:23], v[44:47]\n\t"
        "ds_read_b128 v[20:23], v5 offset:27520\n\t"
        "v_mfma_f32_16x16x32_bf16 v[48:51], v[72:75], v[8:11], v[48:51]\n\t"
        "v_mfma_f32_16x16x32_bf16 v[52:55], v[76:79], v[8:11], v[52:55]\n\t"
        "ds_read_b128 v[8:11], v5 offset:35968\n\t"
        "s_waitcnt vmcnt(10) lgkmcnt(4)\n\t"
        "v_xor_b32 v80, s73, v80\n\t"
        "v_xor_b32 v81, v6, v81\n\t"
        "v_xor_b32 v84, s73, v84\n\t"
        "v_xor_b32 v85, v6, v85\n\t"
        "v_mfma_f32_16x16x32_bf16 v[56:59], v[72:75], v[64:67], v[56:59]\n\t"
        "v_mfma_f32_16x16x32_bf16 v[60:63], v[76:79], v[64:67], v[60:63]\n\t"
        "ds_read_b128 v[64:67], v5 offset:2240\n\t"
        "ds_read_b128 v[72:75], v5 offset:10688\n\t"
        "ds_read_b128 v[76:79], v5 offset:19136\n\t"
        "v_mfma_f32_16x16x32_bf16 v[24:27], v[80:83], v[68:71], v[24:27]\n\t"
        "v_mfma_f32_16x16x32_bf16 v[28:31], v[84:87], v[68:71], v[28:31]\n\t"
        "v_or3_b32 v7, v7, v80, v81\n\t"
        "ds_read_b128 v[68:71], v5 offset:27584\n\t"
        "s_waitcnt lgkmcnt(6)\n\t"
        "v_mfma_f32_16x16x32_bf16 v[32:35], v[80:83], v[12:15], v[32:35]\n\t"
        "v_mfma_f32_16x16x32_bf16 v[36:39], v[84:87], v[12:15], v[36:39]\n\t"
        "v_or3_b32 v7, v7, v84, v85\n\t"
        "ds_read_b128 v[12:15], v5 offset:36032\n\t"
        "v_mfma_f32_16x16x32_bf16 v[40:43], v[80:83], v[16:19], v[40:43]\n\t"
        "v_mfma_f32_16x16x32_bf16 v[44:47], v[84:87], v[16:19], v[44:47]\n\t"
        "ds_read_b128 v[16:19], v5 offset:2304\n\t"
        "s_waitcnt lgkmcnt(6)\n\t"
        "v_mfma_f32_16x16x32_bf16 v[48:51], v[80:83], v[20:23], v[48:51]\n\t"
        "v_mfma_f32_16x16x32_bf16 v[52:55], v[84:87], v[20:23], v[52:55]\n\t"
        "ds_read_b128 v[20:23], v5 offset:10752\n\t"
        "s_waitcnt vmcnt(8) lgkmcnt(6)\n\t"
        "v_xor_b32 v88, s73, v88\n\t"
        "v_xor_b32 v89, v6, v89\n\t"
        "v_xor_b32 v92, s73, v92\n\t"
        "v_xor_b32 v93, v6, v93\n\t"
        "v_mfma_f32_16x16x32_bf16 v[56:59], v[80:83], v[8:11], v[56:59]\n\t"
        "v_mfma_f32_16x16x32_bf16 v[60:63], v[84:87], v[8:11], v[60:63]\n\t"
        "ds_read_b128 v[8:11], v5 offset:19200\n\t"
        "ds_read_b128 v[80:83], v5 offset:27648\n\t"
        "v_mfma_f32_16x16x32_bf16 v[24:27], v[88:91], v[64:67], v[24:27]\n\t"
        "v_mfma_f32_16x16x32_bf16 v[28:31], v[92:95], v[64:67], v[28:31]\n\t"
        "v_or3_b32 v7, v7, v88, v89\n\t"
        "ds_read_b128 v[84:87], v5 offset:36096\n\t"
        "s_waitcnt lgkmcnt(7)\n\t"
        "v_mfma_f32_16x16x32_bf16 v[32:35], v[88:91], v[72:75], v[32:35]\n\t"
        "v_mfma_f32_16x16x32_bf16 v[36:39], v[92:95], v[72:75], v[36:39]\n\t"
        "v_or3_b32 v7, v7, v92, v93\n\t"
        "ds_read_b128 v[64:67], v5 offset:2368\n\t"
        "v_mfma_f32_16x16x32_bf16 v[40:43], v[88:91], v[76:79], v[40:43]\n\t"
        "v_mfma_f32_16x16x32_bf16 v[44:47], v[92:95], v[76:79], v[44:47]\n\t"
        "ds_read_b128 v[72:75], v5 offset:10816\n\t"
        "s_waitcnt lgkmcnt(7)\n\t"
        "v_mfma_f32_16x16x32_bf16 v[48:51], v[88:91], v[68:71], v[48:51]\n\t"
        "v_mfma_f32_16x16x32_bf16 v[52:55], v[92:95], v[68:71], v[52:55]\n\t"
        "ds_read_b128 v[76:79], v5 offset:19264\n\t"
        "s_waitcnt vmcnt(6) lgkmcnt(7)\n\t"
        "v_xor_b32 v96, s73, v96\n\t"
        "v_xor_b32 v97, v6, v97\n\t"
        "v_xor_b32 v100, s73, v100\n\t"
        "v_xor_b32 v101, v6, v101\n\t"
        "v_mfma_f32_16x16x32_bf16 v[56:59], v[88:91], v[12:15], v[56:59]\n\t"
        "v_mfma_f32_16x16x32_bf16 v[60:63], v[92:95], v[12:15], v[60:63]\n\t"
        "ds_read_b128 v[68:71], v5 offset:27712\n\t"
        "v_mfma_f32_16x16x32_bf16 v[24:27], v[96:99], v[16:19], v[24:27]\n\t"
        "v_mfma_f32_16x16x32_bf16 v[28:31], v[100:103], v[16:19], v[28:31]\n\t"
        "v_or3_b32 v7, v7, v96, v97\n\t"
        "ds_read_b128 v[12:15], v5 offset:36160\n\t"
        "s_waitcnt lgkmcnt(7)\n\t"
        "v_mfma_f32_16x16x32_bf16 v[32:35], v[96:99], v[20:23], v[32:35]\n\t"
        "v_mfma_f32_16x16x32_bf16 v[36:39], v[100:103], v[20:23], v[36:39]\n\t"
        "v_or3_b32 v7, v7, v100, v101\n\t"
        "ds_read_b128 v[88:91], v5 offset:2432\n\t"
        "v_mfma_f32_16x16x32_bf16 v[40:43], v[96:99], v[8:11], v[40:43]\n\t"
        "v_mfma_f32_16x16x32_bf16 v[44:47], v[100:103], v[8:11], v[44:47]\n\t"
        "ds_read_b128 v[92:95], v5 offset:10880\n\t"
        "s_waitcnt lgkmcnt(7)\n\t"
        "v_mfma_f32_16x16x32_bf16 v[48:51], v[96:99], v[80:83], v[48:51]\n\t"
        "v_mfma_f32_16x16x32_bf16 v[52:55], v[100:103], v[80:83], v[52:55]\n\t"
        "ds_read_b128 v[16:19], v5 offset:19328\n\t"
        "s_waitcnt vmcnt(4) lgkmcnt(7)\n\t"
        "v_xor_b32 v104, s73, v104\n\t"
        "v_xor_b32 v105, v6, v105\n\t"
        "v_xor_b32 v108, s73, v108\n\t"
        "v_xor_b32 v109, v6, v109\n\t"
        "v_mfma_f32_16x16x32_bf16 v[56:59], v[96:99], v[84:87], v[56:59]\n\t"
        "v_mfma_f32_16x16x32_bf16 v[60:63], v[100:103], v[84:87], v[60:63]\n\t"
        "ds_read_b128 v[20:23], v5 offset:27776\n\t"
        "v_mfma_f32_16x16x32_bf16 v[24:27], v[104:107], v[64:67], v[24:27]\n\t"
        "v_mfma_f32_16x16x32_bf16 v[28:31], v[108:111], v[64:67], v[28:31]\n\t"
        "v_or3_b32 v7, v7, v104, v105\n\t"
        "ds_read_b128 v[8:11], v5 offset:36224\n\t"
        "s_waitcnt lgkmcnt(7)\n\t"
        "v_mfma_f32_16x16x32_bf16 v[32:35], v[104:107], v[72:75], v[32:35]\n\t"
        "v_mfma_f32_16x16x32_bf16 v[36:39], v[108:111], v[72:75], v[36:39]\n\t"
        "v_or3_b32 v7, v7, v108, v109\n\t"
        "ds_read_b128 v[80:83], v5 offset:2496\n\t"
        "v_mfma_f32_16x16x32_bf16 v[40:43], v[104:107], v[76:79], v[40:43]\n\t"
        "v_mfma_f32_16x16x32_bf16 v[44:47], v[108:111], v[76:79], v[44:47]\n\t"
        "ds_read_b128 v[84:87], v5 offset:10944\n\t"
        "s_waitcnt lgkmcnt(7)\n\t"
        "v_mfma_f32_16x16x32_bf16 v[48:51], v[104:107], v[68:71], v[48:51]\n\t"
        "v_mfma_f32_16x16x32_bf16 v[52:55], v[108:111], v[68:71], v[52:55]\n\t"
        "ds_read_b128 v[96:99], v5 offset:19392\n\t"
        "s_waitcnt vmcnt(2) lgkmcnt(7)\n\t"
        "v_xor_b32 v112, s73, v112\n\t"
        "v_xor_b32 v113, v6, v113\n\t"
        "v_xor_b32 v116, s73, v116\n\t"
        "v_xor_b32 v117, v6, v117\n\t"
        "v_mfma_f32_16x16x32_bf16 v[56:59], v[104:107], v[12:15], v[56:59]\n\t"
        "v_mfma_f32_16x16x32_bf16 v[60:63], v[108:111], v[12:15], v[60:63]\n\t"
        "ds_read_b128 v[100:103], v5 offset:27840\n\t"
        "v_mfma_f32_16x16x32_bf16 v[24:27], v[112:115], v[88:91], v[24:27]\n\t"
        "v_mfma_f32_16x16x32_bf16 v[28:31], v[116:119], v[88:91], v[28:31]\n\t"
        "v_or3_b32 v7, v7, v112, v113\n\t"
        "ds_read_b128 v[64:67], v5 offset:36288\n\t"
        "s_waitcnt lgkmcnt(7)\n\t"
        "v_mfma_f32_16x16x32_bf16 v[32:35], v[112:115], v[92:95], v[32:35]\n\t"
        "v_mfma_f32_16x16x32_bf16 v[36:39], v[116:119], v[92:95], v[36:39]\n\t"
        "v_or3_b32 v7, v7, v116, v117\n\t"
        "v_mfma_f32_16x16x32_bf16 v[40:43], v[112:115], v[16:19], v[40:43]\n\t"
        "v_mfma_f32_16x16x32_bf16 v[44:47], v[116:119], v[16:19], v[44:47]\n\t"
        "s_waitcnt lgkmcnt(5)\n\t"
        "v_mfma_f32_16x16x32_bf16 v[48:51], v[112:115], v[20:23], v[48:51]\n\t"
        "v_mfma_f32_16x16x32_bf16 v[52:55], v[116:119], v[20:23], v[52:55]\n\t"
        "s_waitcnt vmcnt(0) lgkmcnt(4)\n\t"
        "v_xor_b32 v120, s73, v120\n\t"
        "v_xor_b32 v121, v6, v121\n\t"
        "v_xor_b32 v124, s73, v124\n\t"
        "v_xor_b32 v125, v6, v125\n\t"
        "v_mfma_f32_16x16x32_bf16 v[56:59], v[112:115], v[8:11], v[56:59]\n\t"
        "v_mfma_f32_16x16x32_bf16 v[60:63], v[116:119], v[8:11], v[60:63]\n\t"
        "v_mfma_f32_16x16x32_bf16 v[24:27], v[120:123], v[80:83], v[24:27]\n\t"
        "v_mfma_f32_16x16x32_bf16 v[28:31], v[124:127], v[80:83], v[28:31]\n\t"
        "v_or3_b32 v7, v7, v120, v121\n\t"
        "s_waitcnt lgkmcnt(3)\n\t"
        "v_mfma_f32_16x16x32_bf16 v[32:35], v[120:123], v[84:87], v[32:35]\n\t"
        "v_mfma_f32_16x16x32_bf16 v[36:39], v[124:127], v[84:87], v[36:39]\n\t"
        "v_or3_b32 v7, v7, v124, v125\n\t"
        "v_and_b32 v7, s49, v7\n\t"
        "v_cmp_ne_u32 vcc, 0, v7\n\t"
        "s_cbranch_vccnz .L7_qr\n"
        ".L7_qd:\n\t"
        "s_mov_b64 exec, 1\n\t"
        "ds_add_u32 v2, v3 offset:12\n\t"
        "s_mov_b64 exec, -1\n\t"
        "v_and_b32 v6, 15, v1\n\t"
        "v_mul_u32_u24 v6, 272, v6\n\t"
        "v_and_b32 v7, 48, v1\n\t"
        "s_lshl_b32 s78, s80, 6\n\t"
        "v_add3_u32 v6, v6, v7, s78\n\t"
        "s_waitcnt lgkmcnt(2)\n\t"
        "v_mfma_f32_16x16x32_bf16 v[40:43], v[120:123], v[96:99], v[40:43]\n\t"
        "v_mfma_f32_16x16x32_bf16 v[44:47], v[124:127], v[96:99], v[44:47]\n\t"
        "v_cvt_pk_bf16_f32 v24, v24, v25\n\t"
        "v_cvt_pk_bf16_f32 v25, v26, v27\n\t"
        "v_cvt_pk_bf16_f32 v26, v28, v29\n\t"
        "v_cvt_pk_bf16_f32 v27, v30, v31\n\t"
        "s_waitcnt lgkmcnt(0)\n\t"
        "v_mfma_f32_16x16x32_bf16 v[48:51], v[120:123], v[100:103], v[48:51]\n\t"
        "v_mfma_f32_16x16x32_bf16 v[52:55], v[124:127], v[100:103], v[52:55]\n\t"
        "v_cvt_pk_bf16_f32 v32, v32, v33\n\t"
        "v_cvt_pk_bf16_f32 v33, v34, v35\n\t"
        "v_cvt_pk_bf16_f32 v34, v36, v37\n\t"
        "v_cvt_pk_bf16_f32 v35, v38, v39\n\t"
        "v_mfma_f32_16x16x32_bf16 v[56:59], v[120:123], v[64:67], v[56:59]\n\t"
        "v_mfma_f32_16x16x32_bf16 v[60:63], v[124:127], v[64:67], v[60:63]\n\t"
        "v_cvt_pk_bf16_f32 v40, v40, v41\n\t"
        "v_cvt_pk_bf16_f32 v41, v42, v43\n\t"
        "v_cvt_pk_bf16_f32 v42, v44, v45\n\t"
        "v_cvt_pk_bf16_f32 v43, v46, v47\n\t"
        "ds_read_b32 v0, v2 offset:12\n\t"
        "v_cvt_pk_bf16_f32 v48, v48, v49\n\t"
        "v_cvt_pk_bf16_f32 v49, v50, v51\n\t"
        "v_cvt_pk_bf16_f32 v50, v52, v53\n\t"
        "v_cvt_pk_bf16_f32 v51, v54, v55\n\t"
        "v_cvt_pk_bf16_f32 v56, v56, v57\n\t"
        "v_cvt_pk_bf16_f32 v57, v58, v59\n\t"
        "v_cvt_pk_bf16_f32 v58, v60, v61\n\t"
        "v_cvt_pk_bf16_f32 v59, v62, v63\n\t"
        "s_waitcnt lgkmcnt(0)\n\t"
        "v_cmp_gt_u16 vcc, 4, v0\n\t"
        "s_cbranch_vccz .L7_qx\n"
        ".L7_w58:\n\t"
        "ds_read_b32 v0, v2 offset:12\n\t"
        "s_waitcnt lgkmcnt(0)\n\t"
        "v_cmp_gt_u16 vcc, 4, v0\n\t"
        "s_cbranch_vccz .L7_d59\n\t"
        "s_sleep 0\n\t"
        "s_sub_i32 s83, s83, 1\n\t"
        "s_cmp_gt_i32 s83, 0\n\t"
        "s_cbranch_scc1 .L7_w58\n"
        ".L7_d59:\n"
        ".L7_qx:\n\t"
        "ds_write_b128 v6, v[24:27] offset:2048\n\t"
        "ds_write_b128 v6, v[32:35] offset:6400\n\t"
        "ds_write_b128 v6, v[40:43] offset:10752\n\t"
        "ds_write_b128 v6, v[48:51] offset:15104\n\t"
        "ds_write_b128 v6, v[56:59] offset:19456\n\t"
        "s_lshl_b32 s78, s69, 9\n\t"
        "v_lshl_add_u32 v7, v1, 3, s78\n\t"
        "v_mov_b64 v[8:9], 0\n\t"
        "ds_write_b64 v7, v[8:9] offset:34048\n\t"
        "s_mov_b64 exec, 1\n\t"
        "ds_add_u32 v2, v3 offset:16\n\t"
        "s_mov_b64 exec, -1\n\t"
        "v_lshrrev_b32 v10, 3, v1\n\t"
        "v_lshlrev_b32 v10, 9, v10\n\t"
        "v_and_b32 v11, 7, v1\n\t"
        "v_lshl_add_u32 v10, v11, 4, v10\n\t"
        "v_and_b32 v11, 16, v1\n\t"
        "v_lshlrev_b32 v11, 2, v11\n\t"
        "v_xor_b32 v10, v10, v11\n\t"
        "s_load_dwordx4 s[24:27], %2, 32\n\t"
        "s_lshl_b32 s78, s80, 14\n\t"
        "s_add_u32 m0, s78, 68864\n\t"
        "s_lshl_b32 s78, s70, 9\n\t"
        "s_add_u32 s78, s78, s80\n\t"
        "s_lshl_b32 s78, s78, 7\n"
        ".L7_w60:\n\t"
        "ds_read_b32 v0, v2 offset:24\n\t"
        "s_waitcnt lgkmcnt(0)\n\t"
        "v_cmp_gt_u16 vcc, 8, v0\n\t"
        "s_cbranch_vccz .L7_d61\n\t"
        "s_sleep 1\n\t"
        "s_sub_i32 s83, s83, 1\n\t"
        "s_cmp_gt_i32 s83, 0\n\t"
        "s_cbranch_scc1 .L7_w60\n"
        ".L7_d61:\n"
        ".L7_w62:\n\t"
        "ds_read_b32 v0, v2 offset:88\n\t"
        "s_waitcnt lgkmcnt(0)\n\t"
        "v_cmp_gt_u16 vcc, 16, v0\n\t"
        "s_cbranch_vccz .L7_d63\n\t"
        "s_sleep 1\n\t"
        "s_sub_i32 s83, s83, 1\n\t"
        "s_cmp_gt_i32 s83, 0\n\t"
        "s_cbranch_scc1 .L7_w62\n"
        ".L7_d63:\n\t"
        "s_setprio 3\n\t"
        "buffer_load_dwordx4 v10, s[24:27], s78 offen lds\n\t"
        "s_add_u32 m0, m0, 0x400\n\t"
        "s_add_u32 s78, s78, 0x1000\n\t"
        "buffer_load_dwordx4 v10, s[24:27], s78 offen lds\n\t"
        "s_add_u32 m0, m0, 0x400\n\t"
        "s_add_u32 s78, s78, 0x1000\n\t"
        "buffer_load_dwordx4 v10, s[24:27], s78 offen lds\n\t"
        "s_add_u32 m0, m0, 0x400\n\t"
        "s_add_u32 s78, s78, 0x1000\n\t"
        "buffer_load_dwordx4 v10, s[24:27], s78 offen lds\n\t"
        "s_add_u32 m0, m0, 0x400\n\t"
        "s_add_u32 s78, s78, 0x1000\n\t"
        "buffer_load_dwordx4 v10, s[24:27], s78 offen lds\n\t"
        "s_add_u32 m0, m0, 0x400\n\t"
        "s_add_u32 s78, s78, 0x1000\n\t"
        "buffer_load_dwordx4 v10, s[24:27], s78 offen lds\n\t"
        "s_add_u32 m0, m0, 0x400\n\t"
        "s_add_u32 s78, s78, 0x1000\n\t"
        "buffer_load_dwordx4 v10, s[24:27], s78 offen lds\n\t"
        "s_add_u32 m0, m0, 0x400\n\t"
        "s_add_u32 s78, s78, 0x1000\n\t"
        "buffer_load_dwordx4 v10, s[24:27], s78 offen lds\n\t"
        "s_add_u32 m0, m0, 0x400\n\t"
        "s_add_u32 s78, s78, 0x1000\n\t"
        "buffer_load_dwordx4 v10, s[24:27], s78 offen lds\n\t"
        "s_add_u32 m0, m0, 0x400\n\t"
        "s_add_u32 s78, s78, 0x1000\n\t"
        "buffer_load_dwordx4 v10, s[24:27], s78 offen lds\n\t"
        "s_add_u32 m0, m0, 0x400\n\t"
        "s_add_u32 s78, s78, 0x1000\n\t"
        "buffer_load_dwordx4 v10, s[24:27], s78 offen lds\n\t"
        "s_add_u32 m0, m0, 0x400\n\t"
        "s_add_u32 s78, s78, 0x1000\n\t"
        "buffer_load_dwordx4 v10, s[24:27], s78 offen lds\n\t"
        "s_add_u32 m0, m0, 0x400\n\t"
        "s_add_u32 s78, s78, 0x1000\n\t"
        "buffer_load_dwordx4 v10, s[24:27], s78 offen lds\n\t"
        "s_add_u32 m0, m0, 0x400\n\t"
        "s_add_u32 s78, s78, 0x1000\n\t"
        "buffer_load_dwordx4 v10, s[24:27], s78 offen lds\n\t"
        "s_add_u32 m0, m0, 0x400\n\t"
        "s_add_u32 s78, s78, 0x1000\n\t"
        "buffer_load_dwordx4 v10, s[24:27], s78 offen lds\n\t"
        "s_add_u32 m0, m0, 0x400\n\t"
        "s_add_u32 s78, s78, 0x1000\n\t"
        "buffer_load_dwordx4 v10, s[24:27], s78 offen lds\n\t"
        "s_waitcnt vmcnt(0)\n"
        ".L7_vw:\n\t"
        "s_mov_b64 exec, 1\n\t"
        "ds_add_u32 v2, v3 offset:28\n\t"
        "s_mov_b64 exec, -1\n\t"
        "s_setprio 0\n"
        ".L7_w64:\n\t"
        "ds_read_b32 v0, v2 offset:36\n\t"
        "s_waitcnt lgkmcnt(0)\n\t"
        "v_cmp_gt_u16 vcc, 4, v0\n\t"
        "s_cbranch_vccz .L7_d65\n\t"
        "s_sleep 2\n\t"
        "s_sub_i32 s83, s83, 1\n\t"
        "s_cmp_gt_i32 s83, 0\n\t"
        "s_cbranch_scc1 .L7_w64\n"
        ".L7_d65:\n\t"
        "v_cmp_lt_u32 vcc, 0xfffff, v0\n\t"
        "s_cbranch_vccnz .L7_exh\n\t"
        "s_cmp_eq_u32 s69, 0\n\t"
        "s_cbranch_scc0 .L7_hz\n\t"
        "s_lshr_b32 s47, s68, 20\n\t"
        "s_add_u32 s47, s47, s68\n\t"
        "s_and_b32 s47, s47, 0xfff\n\t"
        "s_mul_i32 s46, s68, 0x9e377000\n\t"
        "s_or_b32 s46, s46, s47\n\t"
        "v_mov_b32 v4, s46\n\t"
        "s_xor_b32 s44, s70, 1\n\t"
        "s_lshl1_add_u32 s44, %3, s44\n\t"
        "s_lshl_b32 s44, s44, 10\n\t"
        "s_add_u32 s44, s44, 262144\n"
        ".L7_w66:\n\t"
        "ds_read_b32 v0, v2 offset:116\n\t"
        "s_waitcnt lgkmcnt(0)\n\t"
        "v_cmp_gt_u16 vcc, 1, v0\n\t"
        "s_cbranch_vccz .L7_d67\n\t"
        "s_sleep 1\n\t"
        "s_sub_i32 s83, s83, 1\n\t"
        "s_cmp_gt_i32 s83, 0\n\t"
        "s_cbranch_scc1 .L7_w66\n"
        ".L7_d67:\n\t"
        "v_readfirstlane_b32 s78, v0\n\t"
        "s_lshr_b32 s78, s78, 16\n\t"
        "s_cmp_gt_u32 s78, 16\n\t"
        "s_cselect_b32 s45, 0x2000, 0\n\t"
        "s_xor_b32 s44, s44, s45\n\t"
        "s_xor_b32 s45, s44, 0xc00\n"
        ".L7_w68:\n\t"
        "ds_read_b32 v0, v2 offset:56\n\t"
        "s_waitcnt lgkmcnt(0)\n\t"
        "v_cmp_gt_u16 vcc, 3, v0\n\t"
        "s_cbranch_vccz .L7_d69\n\t"
        "s_sleep 8\n\t"
        "s_sub_i32 s83, s83, 1\n\t"
        "s_cmp_gt_i32 s83, 0\n\t"
        "s_cbranch_scc1 .L7_w68\n"
        ".L7_d69:\n\t"
        "s_setprio 3\n"
        ".L7_w70:\n\t"
        "ds_read_b32 v0, v2 offset:96\n\t"
        "s_waitcnt lgkmcnt(0)\n\t"
        "v_cmp_gt_u16 vcc, 7, v0\n\t"
        "s_cbranch_vccz .L7_d71\n\t"
        "s_sleep 1\n\t"
        "s_sub_i32 s83, s83, 1\n\t"
        "s_cmp_gt_i32 s83, 0\n\t"
        "s_cbranch_scc1 .L7_w70\n"
        ".L7_d71:\n\t"
        "s_mov_b64 exec, 1\n\t"
        "buffer_store_dword v4, off, s[20:23], s44 offset:12 sc1\n\t"
        "s_mov_b32 s83, 64\n\t"
        "s_sleep 4\n"
        ".L7_h1:\n\t"
        "s_buffer_load_dword s47, s[20:23], s45 offset:12 glc\n\t"
        "s_waitcnt lgkmcnt(0)\n\t"
        "s_cmp_eq_u32 s47, s46\n\t"
        "s_cbranch_scc1 .L7_h2\n\t"
        "s_sleep 3\n\t"
        "s_sub_i32 s83, s83, 1\n\t"
        "s_cmp_gt_i32 s83, 0\n\t"
        "s_cbranch_scc1 .L7_h1\n"
        ".L7_h2:\n\t"
        "s_mov_b64 exec, 1\n\t"
        "ds_add_u32 v2, v3 offset:100\n\t"
        "s_mov_b64 exec, -1\n"
        ".L7_hz:\n\t"
        "s_endpgm\n"
        ".L7_exh:\n\t"
        "s_mov_b64 exec, 1\n\t"
        "ds_add_u32 v2, v3 offset:100\n\t"
        "s_mov_b64 exec, -1\n\t"
        "s_branch .L7_exit\n"
        ".L7_O2:\n\t"
        "s_load_dwordx8 s[24:31], %2, 32\n\t"
        "s_load_dwordx4 s[60:63], %2, 176\n\t"
        "v_lshrrev_b32 v6, 4, v1\n\t"
        "v_and_b32 v7, 48, v1\n\t"
        "v_and_b32 v4, 2, v6\n\t"
        "v_add_lshl_u32 v4, v4, v6, 2\n\t"
        "v_and_b32 v8, 15, v1\n\t"
        "s_lshl_b32 s78, s70, 8\n\t"
        "v_lshl_add_u32 v9, v8, 4, s78\n\t"
        "v_lshrrev_b32 v10, 3, v1\n\t"
        "v_lshlrev_b32 v10, 9, v10\n\t"
        "v_and_b32 v11, 7, v1\n\t"
        "v_lshl_add_u32 v10, v11, 4, v10\n\t"
        "v_and_b32 v11, 16, v1\n\t"
        "v_lshlrev_b32 v11, 2, v11\n\t"
        "v_xor_b32 v10, v10, v11\n\t"
        "s_add_u32 s81, s69, s76\n\t"
        "s_and_b32 s81, s81, 7\n\t"
        "s_lshr_b32 s78, s81, 1\n\t"
        "s_lshl_b32 s78, s78, 7\n\t"
        "s_and_b32 s79, s81, 1\n\t"
        "s_lshl_b32 s79, s79, 15\n\t"
        "s_add_u32 s78, s78, s79\n\t"
        "s_lshl_b32 s79, s70, 16\n\t"
        "s_add_u32 s78, s78, s79\n\t"
        "v_add_u32 v10, s78, v10\n\t"
        "s_lshl_b32 s82, s81, 13\n\t"
        "s_add_u32 s82, s82, 68864\n\t"
        "v_bfe_u32 v12, v1, 2, 2\n\t"
        "v_lshlrev_b32 v11, 10, v6\n\t"
        "v_lshl_add_u32 v11, v12, 7, v11\n\t"
        "v_lshrrev_b32 v12, 1, v12\n\t"
        "s_and_b32 s78, s81, 1\n\t"
        "v_xor_b32 v12, s78, v12\n\t"
        "v_lshl_add_u32 v11, v12, 6, v11\n\t"
        "v_and_b32 v12, 3, v1\n\t"
        "v_lshl_add_u32 v11, v12, 4, v11\n\t"
        "s_lshr_b32 s78, s81, 1\n\t"
        "s_lshl_b32 s78, s78, 14\n\t"
        "s_add_u32 s78, s78, 68864\n\t"
        "v_add_u32 v23, s78, v11\n\t"
        "s_waitcnt vmcnt(0)\n\t"
        "ds_bpermute_b32 v12, v7, v5 offset:0\n\t"
        "ds_bpermute_b32 v13, v7, v5 offset:4\n\t"
        "ds_bpermute_b32 v14, v7, v5 offset:8\n\t"
        "ds_bpermute_b32 v15, v7, v5 offset:12\n\t"
        "ds_bpermute_b32 v16, v7, v5 offset:64\n\t"
        "ds_bpermute_b32 v17, v7, v5 offset:68\n\t"
        "ds_bpermute_b32 v18, v7, v5 offset:72\n\t"
        "ds_bpermute_b32 v19, v7, v5 offset:76\n\t"
        "ds_bpermute_b32 v20, v4, v5 offset:128\n\t"
        "ds_bpermute_b32 v21, v4, v5 offset:136\n\t"
        "s_waitcnt lgkmcnt(0)\n\t"
        "v_lshl_add_u32 v12, v12, 9, v9\n\t"
        "v_lshl_add_u32 v13, v13, 9, v9\n\t"
        "v_lshl_add_u32 v14, v14, 9, v9\n\t"
        "v_lshl_add_u32 v15, v15, 9, v9\n\t"
        "v_lshl_add_u32 v16, v16, 9, v9\n\t"
        "v_lshl_add_u32 v17, v17, 9, v9\n\t"
        "v_lshl_add_u32 v18, v18, 9, v9\n\t"
        "v_lshl_add_u32 v19, v19, 9, v9\n\t"
        "v_lshl_add_u32 v20, v20, 9, v9\n\t"
        "v_lshl_add_u32 v21, v21, 9, v9\n\t"
        "s_setprio 0\n"
        ".L7_w1:\n\t"
        "ds_read_b32 v0, v2 offset:44\n\t"
        "s_waitcnt lgkmcnt(0)\n\t"
        "v_cmp_gt_u16 vcc, 2, v0\n\t"
        "s_cbranch_vccz .L7_d2\n\t"
        "s_sleep 1\n\t"
        "s_sub_i32 s83, s83, 1\n\t"
        "s_cmp_gt_i32 s83, 0\n\t"
        "s_cbranch_scc1 .L7_w1\n"
        ".L7_d2:\n"
        ".L7_w3:\n\t"
        "ds_read_b64 v[4:5], v2 offset:72\n\t"
        "s_waitcnt lgkmcnt(0)\n\t"
        "v_cmp_gt_u16 vcc, 2, v4\n\t"
        "s_cbranch_vccz .L7_d4\n\t"
        "s_sleep 1\n\t"
        "s_sub_i32 s83, s83, 1\n\t"
        "s_cmp_gt_i32 s83, 0\n\t"
        "s_cbranch_scc1 .L7_w3\n"
        ".L7_d4:\n\t"
        "v_readfirstlane_b32 s68, v5\n\t"
        "s_add_u32 m0, s82, 0\n\t"
        "s_mov_b32 s78, 0\n\t"
        "buffer_load_dwordx4 v10, s[28:31], s78 offen lds\n\t"
        "s_add_u32 m0, m0, 0x400\n\t"
        "s_add_u32 s78, s78, 0x1000\n\t"
        "buffer_load_dwordx4 v10, s[28:31], s78 offen lds\n\t"
        "s_add_u32 m0, m0, 0x400\n\t"
        "s_add_u32 s78, s78, 0x1000\n\t"
        "buffer_load_dwordx4 v10, s[28:31], s78 offen lds\n\t"
        "s_add_u32 m0, m0, 0x400\n\t"
        "s_add_u32 s78, s78, 0x1000\n\t"
        "buffer_load_dwordx4 v10, s[28:31], s78 offen lds\n\t"
        "s_add_u32 m0, m0, 0x400\n\t"
        "s_add_u32 s78, s78, 0x1000\n\t"
        "buffer_load_dwordx4 v10, s[28:31], s78 offen lds\n\t"
        "s_add_u32 m0, m0, 0x400\n\t"
        "s_add_u32 s78, s78, 0x1000\n\t"
        "buffer_load_dwordx4 v10, s[28:31], s78 offen lds\n\t"
        "s_add_u32 m0, m0, 0x400\n\t"
        "s_add_u32 s78, s78, 0x1000\n\t"
        "buffer_load_dwordx4 v10, s[28:31], s78 offen lds\n\t"
        "s_add_u32 m0, m0, 0x400\n\t"
        "s_add_u32 s78, s78, 0x1000\n\t"
        "buffer_load_dwordx4 v10, s[28:31], s78 offen lds\n\t"
        "s_mov_b64 exec, 1\n\t"
        "ds_add_u32 v2, v3 offset:88\n\t"
        "s_mov_b64 exec, -1\n\t"
        "s_mov_b32 s36, 0x22222222\n\t"
        "s_mov_b32 s37, s36\n\t"
        "s_lshl_b64 s[38:39], s[36:37], 1\n\t"
        "s_lshl_b64 s[40:41], s[36:37], 2\n\t"
        "s_and_b32 s44, s68, 0xfff\n\t"
        "s_xor_b32 s45, s44, 0x5a5\n\t"
        "s_lshr_b32 s46, s68, 12\n\t"
        "s_lshr_b32 s47, s68, 20\n\t"
        "s_add_u32 s46, s46, s68\n\t"
        "s_and_b32 s46, s46, 0xfff\n\t"
        "s_add_u32 s47, s47, s68\n\t"
        "s_and_b32 s47, s47, 0xfff\n\t"
        "v_mov_b32 v22, s44\n\t"
        "v_mov_b32 v0, s45\n\t"
        "v_cndmask_b32 v22, v22, v0, s[36:37]\n\t"
        "v_mov_b32 v0, s46\n\t"
        "v_cndmask_b32 v22, v22, v0, s[38:39]\n\t"
        "v_mov_b32 v0, s47\n\t"
        "v_cndmask_b32 v22, v22, v0, s[40:41]\n\t"
        "s_lshl3_add_u32 s42, %3, s77\n\t"
        "s_lshl_b32 s42, s42, 8\n\t"
        "s_add_u32 s42, s42, 262144\n\t"
        "s_xor_b32 s43, s42, 0x800\n\t"
        "s_movk_i32 s48, 0xfff\n\t"
        "s_waitcnt vmcnt(0)\n\t"
        "s_mov_b64 exec, 1\n\t"
        "ds_add_u32 v2, v3 offset:20\n\t"
        "s_mov_b64 exec, -1\n"
        ".L7_w5:\n\t"
        "ds_read_b32 v0, v2 offset:20\n\t"
        "s_waitcnt lgkmcnt(0)\n\t"
        "v_cmp_gt_u16 vcc, 8, v0\n\t"
        "s_cbranch_vccz .L7_d6\n\t"
        "s_sleep 1\n\t"
        "s_sub_i32 s83, s83, 1\n\t"
        "s_cmp_gt_i32 s83, 0\n\t"
        "s_cbranch_scc1 .L7_w5\n"
        ".L7_d6:\n\t"
        "ds_read_b64_tr_b16 v[64:65], v23 offset:0\n\t"
        "ds_read_b64_tr_b16 v[66:67], v23 offset:512\n\t"
        "ds_read_b64_tr_b16 v[68:69], v23 offset:8\n\t"
        "ds_read_b64_tr_b16 v[70:71], v23 offset:520\n\t"
        "ds_read_b64_tr_b16 v[72:73], v23 offset:4096\n\t"
        "ds_read_b64_tr_b16 v[74:75], v23 offset:4608\n\t"
        "ds_read_b64_tr_b16 v[76:77], v23 offset:4104\n\t"
        "ds_read_b64_tr_b16 v[78:79], v23 offset:4616\n\t"
        "ds_read_b64_tr_b16 v[80:81], v23 offset:8192\n\t"
        "ds_read_b64_tr_b16 v[82:83], v23 offset:8704\n\t"
        "ds_read_b64_tr_b16 v[84:85], v23 offset:8200\n\t"
        "ds_read_b64_tr_b16 v[86:87], v23 offset:8712\n\t"
        "ds_read_b64_tr_b16 v[88:89], v23 offset:12288\n\t"
        "ds_read_b64_tr_b16 v[90:91], v23 offset:12800\n\t"
        "s_waitcnt lgkmcnt(10)\n\t"
        "ds_read_b64_tr_b16 v[92:93], v23 offset:12296\n\t"
        "ds_read_b64_tr_b16 v[94:95], v23 offset:12808\n\t"
        "s_mov_b64 exec, 1\n\t"
        "ds_add_u32 v2, v3 offset:24\n\t"
        "s_mov_b64 exec, -1\n\t"
        "buffer_load_dwordx4 v[24:27], v12, s[16:19], 0 offen\n\t"
        "buffer_load_dwordx4 v[28:31], v13, s[16:19], 0 offen\n\t"
        "buffer_load_dwordx4 v[32:35], v14, s[16:19], 0 offen\n\t"
        "buffer_load_dwordx4 v[36:39], v15, s[16:19], 0 offen\n\t"
        "buffer_load_dwordx4 v[40:43], v16, s[16:19], 0 offen\n\t"
        "buffer_load_dwordx4 v[44:47], v17, s[16:19], 0 offen\n\t"
        "buffer_load_dwordx4 v[48:51], v18, s[16:19], 0 offen\n\t"
        "buffer_load_dwordx4 v[52:55], v19, s[16:19], 0 offen\n\t"
        "buffer_load_dwordx4 v[56:59], v20, s[16:19], 0 offen\n\t"
        "buffer_load_dwordx4 v[60:63], v21, s[16:19], 0 offen\n\t"
        "s_mov_b64 exec, 1\n\t"
        "ds_add_u32 v2, v3 offset:88\n\t"
        "s_mov_b64 exec, -1\n"
        ".L7_o2:\n\t"
        "s_setprio 2\n\t"
        "v_add_u32 v20, s80, v6\n\t"
        "v_lshrrev_b32 v21, 5, v1\n\t"
        "v_add_u32 v21, s80, v21\n\t"
        "v_lshlrev_b32 v8, 4, v8\n\t"
        "s_movk_i32 s78, 272\n\t"
        "v_mad_u32_u24 v10, v20, s78, v8\n\t"
        "v_mad_u32_u24 v11, v21, s78, v8\n\t"
        "v_lshlrev_b32 v108, 1, v20\n\t"
        "v_lshlrev_b32 v109, 1, v21\n\t"
        "s_mov_b32 s50, 0x0f0ff0f0\n\t"
        "s_mov_b32 s51, s50\n\t"
        "s_and_b32 s57, s81, 1\n\t"
        "s_lshl_b32 s57, s57, 13\n"
        ".L7_w7:\n\t"
        "ds_read_b32 v0, v2 offset:16\n\t"
        "s_waitcnt lgkmcnt(0)\n\t"
        "v_cmp_gt_u16 vcc, 4, v0\n\t"
        "s_cbranch_vccz .L7_d8\n\t"
        "s_sleep 1\n\t"
        "s_sub_i32 s83, s83, 1\n\t"
        "s_cmp_gt_i32 s83, 0\n\t"
        "s_cbranch_scc1 .L7_w7\n"
        ".L7_d8:\n\t"
        "ds_read_b128 v[96:99], v10 offset:2048\n\t"
        "ds_read_b128 v[100:103], v10 offset:3136\n\t"
        "ds_read_b128 v[104:107], v11 offset:4224\n\t"
        "ds_read_b32 v9, v2 offset:116\n\t"
        "ds_read_u16 v110, v108 offset:1792\n\t"
        "ds_read_u16 v111, v108 offset:1800\n\t"
        "ds_read_u16 v112, v109 offset:1808\n\t"
        "s_waitcnt vmcnt(0)\n\t"
        "s_waitcnt lgkmcnt(6)\n\t"
        "v_dot2_f32_bf16 v12, v24, v96, 0\n\t"
        "v_dot2_f32_bf16 v13, v28, v96, 0\n\t"
        "v_dot2_f32_bf16 v14, v32, v96, 0\n\t"
        "v_dot2_f32_bf16 v15, v36, v96, 0\n\t"
        "v_dot2_f32_bf16 v12, v25, v97, v12\n\t"
        "v_dot2_f32_bf16 v13, v29, v97, v13\n\t"
        "v_dot2_f32_bf16 v14, v33, v97, v14\n\t"
        "v_dot2_f32_bf16 v15, v37, v97, v15\n\t"
        "v_dot2_f32_bf16 v12, v26, v98, v12\n\t"
        "v_dot2_f32_bf16 v13, v30, v98, v13\n\t"
        "v_dot2_f32_bf16 v14, v34, v98, v14\n\t"
        "v_dot2_f32_bf16 v15, v38, v98, v15\n\t"
        "v_dot2_f32_bf16 v12, v27, v99, v12\n\t"
        "v_dot2_f32_bf16 v13, v31, v99, v13\n\t"
        "v_dot2_f32_bf16 v14, v35, v99, v14\n\t"
        "v_dot2_f32_bf16 v15, v39, v99, v15\n\t"
        "s_waitcnt lgkmcnt(5)\n\t"
        "v_dot2_f32_bf16 v16, v40, v100, 0\n\t"
        "v_dot2_f32_bf16 v17, v44, v100, 0\n\t"
        "v_dot2_f32_bf16 v18, v48, v100, 0\n\t"
        "v_dot2_f32_bf16 v19, v52, v100, 0\n\t"
        "v_dot2_f32_bf16 v16, v41, v101, v16\n\t"
        "v_dot2_f32_bf16 v17, v45, v101, v17\n\t"
        "v_dot2_f32_bf16 v18, v49, v101, v18\n\t"
        "v_dot2_f32_bf16 v19, v53, v101, v19\n\t"
        "v_dot2_f32_bf16 v16, v42, v102, v16\n\t"
        "v_dot2_f32_bf16 v17, v46, v102, v17\n\t"
        "v_dot2_f32_bf16 v18, v50, v102, v18\n\t"
        "v_dot2_f32_bf16 v19, v54, v102, v19\n\t"
        "v_dot2_f32_bf16 v16, v43, v103, v16\n\t"
        "v_dot2_f32_bf16 v17, v47, v103, v17\n\t"
        "v_dot2_f32_bf16 v18, v51, v103, v18\n\t"
        "v_dot2_f32_bf16 v19, v55, v103, v19\n\t"
        "s_waitcnt lgkmcnt(4)\n\t"
        "v_dot2_f32_bf16 v20, v56, v104, 0\n\t"
        "v_dot2_f32_bf16 v21, v60, v104, 0\n\t"
        "v_dot2_f32_bf16 v20, v57, v105, v20\n\t"
        "v_dot2_f32_bf16 v21, v61, v105, v21\n\t"
        "v_dot2_f32_bf16 v20, v58, v106, v20\n\t"
        "v_dot2_f32_bf16 v21, v62, v106, v21\n\t"
        "v_dot2_f32_bf16 v20, v59, v107, v20\n\t"
        "v_dot2_f32_bf16 v21, v63, v107, v21\n\t"
        "s_nop 1\n\t"
        "v_add_f32_dpp v20, v20, v20 row_ror:8 row_mask:0xf bank_mask:0x3\n\t"
        "v_add_f32_dpp v12, v12, v12 row_ror:8 row_mask:0xf bank_mask:0x3\n\t"
        "v_add_f32_dpp v14, v14, v14 row_ror:8 row_mask:0xf bank_mask:0x3\n\t"
        "v_add_f32_dpp v16, v16, v16 row_ror:8 row_mask:0xf bank_mask:0x3\n\t"
        "v_add_f32_dpp v18, v18, v18 row_ror:8 row_mask:0xf bank_mask:0x3\n\t"
        "v_add_f32_dpp v20, v21, v21 row_ror:8 row_mask:0xf bank_mask:0xc\n\t"
        "v_add_f32_dpp v12, v13, v13 row_ror:8 row_mask:0xf bank_mask:0xc\n\t"
        "v_add_f32_dpp v14, v15, v15 row_ror:8 row_mask:0xf bank_mask:0xc\n\t"
        "v_add_f32_dpp v16, v17, v17 row_ror:8 row_mask:0xf bank_mask:0xc\n\t"
        "v_add_f32_dpp v18, v19, v19 row_ror:8 row_mask:0xf bank_mask:0xc\n\t"
        "v_add_f32_dpp v20, v20, v20 row_half_mirror row_mask:0xf bank_mask:0xf\n\t"
        "v_add_f32_dpp v12, v12, v12 row_half_mirror row_mask:0xf bank_mask:0x5\n\t"
        "v_add_f32_dpp v16, v16, v16 row_half_mirror row_mask:0xf bank_mask:0x5\n\t"
        "v_mov_b32 v21, v20\n\t"
        "v_add_f32_dpp v12, v14, v14 row_half_mirror row_mask:0xf bank_mask:0xa\n\t"
        "v_add_f32_dpp v16, v18, v18 row_half_mirror row_mask:0xf bank_mask:0xa\n\t"
        "v_permlane16_swap_b32 v20, v21\n\t"
        "v_cndmask_b32 v20, v20, v21, s[50:51]\n\t"
        "v_add_f32_dpp v12, v12, v12 quad_perm:[1,0,3,2] row_mask:0xf bank_mask:0xf\n\t"
        "v_add_f32_dpp v16, v16, v16 quad_perm:[1,0,3,2] row_mask:0xf bank_mask:0xf\n\t"
        "v_add_f32_dpp v20, v20, v20 quad_perm:[1,0,3,2] row_mask:0xf bank_mask:0xf\n\t"
        "v_add_f32_dpp v12, v12, v12 quad_perm:[2,3,0,1] row_mask:0xf bank_mask:0xf\n\t"
        "v_add_f32_dpp v16, v16, v16 quad_perm:[2,3,0,1] row_mask:0xf bank_mask:0xf\n\t"
        "v_add_f32_dpp v20, v20, v20 quad_perm:[2,3,0,1] row_mask:0xf bank_mask:0xf\n\t"
        "s_waitcnt lgkmcnt(0)\n\t"
        "v_cmp_gt_u16 vcc, 1, v9\n\t"
        "s_cbranch_vccz .L7_sv\n"
        ".L7_w9:\n\t"
        "ds_read_b32 v9, v2 offset:116\n\t"
        "s_waitcnt lgkmcnt(0)\n\t"
        "v_cmp_gt_u16 vcc, 1, v9\n\t"
        "s_cbranch_vccz .L7_d10\n\t"
        "s_sleep 1\n\t"
        "s_sub_i32 s83, s83, 1\n\t"
        "s_cmp_gt_i32 s83, 0\n\t"
        "s_cbranch_scc1 .L7_w9\n"
        ".L7_d10:\n"
        ".L7_sv:\n\t"
        "v_readfirstlane_b32 s78, v9\n\t"
        "s_lshr_b32 s78, s78, 16\n\t"
        "s_cmp_gt_u32 s78, 16\n\t"
        "s_cselect_b32 s56, 0x2000, 0\n\t"
        "s_sub_u32 s78, s78, 13\n\t"
        "s_cmp_le_u32 s78, 6\n\t"
        "s_cselect_b32 s57, s57, s56\n\t"
        "v_cndmask_b32 v12, v12, v16, s[36:37]\n\t"
        "v_cndmask_b32 v12, v12, v20, s[38:39]\n\t"
        "v_add_u32 v12, 0x800, v12\n\t"
        "v_bfi_b32 v21, s48, v22, v12\n\t"
        "v_lshlrev_b32 v13, 2, v1\n\t"
        "s_xor_b32 s42, s42, s56\n\t"
        "s_setprio 3\n\t"
        "buffer_store_dword v21, v13, s[20:23], s42 offen sc1\n\t"
        "s_setprio 2\n\t"
        "s_xor_b32 s43, s43, s56\n\t"
        "s_lshr_b32 s78, %3, 3\n\t"
        "s_and_b32 s79, %3, 3\n\t"
        "s_lshl2_add_u32 s78, s78, s79\n\t"
        "s_and_b32 s79, %3, 4\n\t"
        "s_lshl_b32 s79, s79, 11\n\t"
        "s_lshl_b32 s30, s81, 10\n\t"
        "s_add_u32 s79, s79, s30\n\t"
        "s_add_u32 s79, s79, 3145728\n\t"
        "s_xor_b32 s79, s79, s57\n\t"
        "s_mul_i32 s24, s78, 0xc000\n\t"
        "s_xor_b32 s78, s78, 1\n\t"
        "s_mul_i32 s27, s78, 0xc000\n\t"
        "s_add_u32 s24, s24, s79\n\t"
        "s_add_u32 s25, s24, 0x4000\n\t"
        "s_add_u32 s26, s24, 0x8000\n\t"
        "s_add_u32 s27, s27, s79\n\t"
        "s_add_u32 s28, s27, 0x4000\n\t"
        "s_add_u32 s29, s27, 0x8000\n\t"
        "s_lshr_b32 s78, s24, 8\n\t"
        "s_and_b32 s78, s78, 0x2000\n\t"
        "s_xor_b32 s24, s24, s78\n\t"
        "s_lshr_b32 s78, s25, 8\n\t"
        "s_and_b32 s78, s78, 0x2000\n\t"
        "s_xor_b32 s25, s25, s78\n\t"
        "s_lshr_b32 s78, s26, 8\n\t"
        "s_and_b32 s78, s78, 0x2000\n\t"
        "s_xor_b32 s26, s26, s78\n\t"
        "s_lshr_b32 s78, s27, 8\n\t"
        "s_and_b32 s78, s78, 0x2000\n\t"
        "s_xor_b32 s27, s27, s78\n\t"
        "s_lshr_b32 s78, s28, 8\n\t"
        "s_and_b32 s78, s78, 0x2000\n\t"
        "s_xor_b32 s28, s28, s78\n\t"
        "s_lshr_b32 s78, s29, 8\n\t"
        "s_and_b32 s78, s78, 0x2000\n\t"
        "s_xor_b32 s29, s29, s78\n\t"
        "v_and_b32 v14, 48, v8\n\t"
        "s_mov_b32 s78, 0xffff\n\t"
        "s_mov_b32 s79, 0\n\t"
        "v_lshlrev_b64 v[4:5], v14, s[78:79]\n\t"
        "v_xor_b32 v18, v110, v8\n\t"
        "v_xor_b32 v19, v111, v8\n\t"
        "v_and_b32 v14, 1, v6\n\t"
        "v_lshl_add_u32 v14, v14, 3, v8\n\t"
        "v_xor_b32 v20, v112, v14\n\t"
        "v_xor_b32 v21, v21, v22\n"
        ".L7_w11:\n\t"
        "ds_read_b32 v0, v2 offset:36\n\t"
        "s_waitcnt lgkmcnt(0)\n\t"
        "v_cmp_gt_u16 vcc, 4, v0\n\t"
        "s_cbranch_vccz .L7_d12\n\t"
        "s_sleep 1\n\t"
        "s_sub_i32 s83, s83, 1\n\t"
        "s_cmp_gt_i32 s83, 0\n\t"
        "s_cbranch_scc1 .L7_w11\n"
        ".L7_d12:\n\t"
        "v_lshrrev_b32 v0, 20, v0\n\t"
        "s_nop 0\n\t"
        "v_readfirstlane_b32 s58, v0\n"
        ".L7_w13:\n\t"
        "ds_read_b32 v0, v2 offset:28\n\t"
        "s_waitcnt lgkmcnt(0)\n\t"
        "v_cmp_gt_u16 vcc, 4, v0\n\t"
        "s_cbranch_vccz .L7_d14\n\t"
        "s_sleep 1\n\t"
        "s_sub_i32 s83, s83, 1\n\t"
        "s_cmp_gt_i32 s83, 0\n\t"
        "s_cbranch_scc1 .L7_w13\n"
        ".L7_d14:\n\t"
        "ds_read_b64_tr_b16 v[96:97], v23 offset:0\n\t"
        "ds_read_b64_tr_b16 v[98:99], v23 offset:512\n\t"
        "ds_read_b64_tr_b16 v[100:101], v23 offset:8\n\t"
        "ds_read_b64_tr_b16 v[102:103], v23 offset:520\n\t"
        "ds_read_b64_tr_b16 v[104:105], v23 offset:4096\n\t"
        "ds_read_b64_tr_b16 v[106:107], v23 offset:4608\n\t"
        "ds_read_b64_tr_b16 v[108:109], v23 offset:4104\n\t"
        "ds_read_b64_tr_b16 v[110:111], v23 offset:4616\n\t"
        "ds_read_b64_tr_b16 v[112:113], v23 offset:8192\n\t"
        "ds_read_b64_tr_b16 v[114:115], v23 offset:8704\n\t"
        "ds_read_b64_tr_b16 v[116:117], v23 offset:8200\n\t"
        "ds_read_b64_tr_b16 v[118:119], v23 offset:8712\n"
        ".L7_sp:\n\t"
        "buffer_load_dword v12, v13, s[20:23], s43 offen sc1\n\t"
        "s_waitcnt vmcnt(0)\n\t"
        "v_xor_b32 v12, v12, v22\n\t"
        "v_and_b32 v0, s48, v12\n\t"
        "v_cmp_ne_u32 vcc, 0, v0\n\t"
        "s_cbranch_vccz .L7_sq\n\t"
        "s_sleep 8\n\t"
        "s_sub_i32 s83, s83, 1\n\t"
        "s_cmp_gt_i32 s83, 0\n\t"
        "s_cbranch_scc1 .L7_sp\n"
        ".L7_sq:\n\t"
        "v_add_f32 v21, v21, v12\n\t"
        "v_exp_f32 v21, v21\n\t"
        "v_lshlrev_b32 v22, 4, v1\n\t"
        "v_add_u32 v22, 142848, v22\n\t"
        "s_cmp_lg_u32 s58, 0\n\t"
        "s_cbranch_scc1 .L7_na\n\t"
        "v_add_f32_dpp v0, v21, v21 row_ror:4 row_mask:0xf bank_mask:0xf\n\t"
        "s_cmp_lt_u32 s69, 4\n\t"
        "s_cselect_b32 s78, 16, 1\n\t"
        "v_add_f32_dpp v0, v0, v0 row_ror:8 row_mask:0xf bank_mask:0xf\n\t"
        "v_add_f32 v0, 0x3089705f, v0\n\t"
        "v_rcp_f32 v0, v0\n\t"
        "s_nop 0\n\t"
        "v_mul_f32 v21, v21, v0\n\t"
        "v_cvt_pk_bf16_f32 v21, v21, v21\n\t"
        "v_mov_b32 v0, s78\n\t"
        "s_nop 0\n\t"
        "v_and_b32_dpp v6, v21, v4 quad_perm:[0,0,0,0] row_mask:0xf bank_mask:0xf\n\t"
        "v_and_b32_dpp v7, v21, v5 quad_perm:[0,0,0,0] row_mask:0xf bank_mask:0xf\n\t"
        "v_and_b32_dpp v16, v21, v4 quad_perm:[1,1,1,1] row_mask:0xf bank_mask:0xf\n\t"
        "v_and_b32_dpp v17, v21, v5 quad_perm:[1,1,1,1] row_mask:0xf bank_mask:0xf\n\t"
        "v_mfma_f32_4x4x4_16b_bf16 v[8:11], v[6:7], v[24:25], 0 cbsz:2 abid:0\n\t"
        "v_mfma_f32_4x4x4_16b_bf16 v[12:15], v[6:7], v[26:27], 0 cbsz:2 abid:0\n\t"
        "v_mfma_f32_4x4x4_16b_bf16 v[120:123], v[16:17], v[40:41], 0 cbsz:2 abid:0\n\t"
        "v_mfma_f32_4x4x4_16b_bf16 v[124:127], v[16:17], v[42:43], 0 cbsz:2 abid:0\n\t"
        "v_mfma_f32_4x4x4_16b_bf16 v[8:11], v[6:7], v[28:29], v[8:11] cbsz:2 abid:2\n\t"
        "v_mfma_f32_4x4x4_16b_bf16 v[12:15], v[6:7], v[30:31], v[12:15] cbsz:2 abid:2\n\t"
        "v_mfma_f32_4x4x4_16b_bf16 v[120:123], v[16:17], v[44:45], v[120:123] cbsz:2 abid:2\n\t"
        "v_mfma_f32_4x4x4_16b_bf16 v[124:127], v[16:17], v[46:47], v[124:127] cbsz:2 abid:2\n\t"
        "v_mfma_f32_4x4x4_16b_bf16 v[8:11], v[6:7], v[32:33], v[8:11] cbsz:2 abid:1\n\t"
        "v_mfma_f32_4x4x4_16b_bf16 v[12:15], v[6:7], v[34:35], v[12:15] cbsz:2 abid:1\n\t"
        "v_mfma_f32_4x4x4_16b_bf16 v[120:123], v[16:17], v[48:49], v[120:123] cbsz:2 abid:1\n\t"
        "v_mfma_f32_4x4x4_16b_bf16 v[124:127], v[16:17], v[50:51], v[124:127] cbsz:2 abid:1\n\t"
        "v_mfma_f32_4x4x4_16b_bf16 v[8:11], v[6:7], v[36:37], v[8:11] cbsz:2 abid:3\n\t"
        "v_mfma_f32_4x4x4_16b_bf16 v[12:15], v[6:7], v[38:39], v[12:15] cbsz:2 abid:3\n\t"
        "v_mfma_f32_4x4x4_16b_bf16 v[120:123], v[16:17], v[52:53], v[120:123] cbsz:2 abid:3\n\t"
        "v_mfma_f32_4x4x4_16b_bf16 v[124:127], v[16:17], v[54:55], v[124:127] cbsz:2 abid:3\n\t"
        "v_and_b32_dpp v6, v21, v4 quad_perm:[2,2,2,2] row_mask:0xf bank_mask:0xf\n\t"
        "v_and_b32_dpp v7, v21, v5 quad_perm:[2,2,2,2] row_mask:0xf bank_mask:0xf\n\t"
        "s_nop 0\n\t"
        "s_nop 0\n\t"
        "v_mfma_f32_4x4x4_16b_bf16 v[24:27], v[6:7], v[56:57], 0 cbsz:2 abid:0\n\t"
        "v_mfma_f32_4x4x4_16b_bf16 v[28:31], v[6:7], v[58:59], 0 cbsz:2 abid:0\n\t"
        "v_cvt_pk_bf16_f32 v8, v8, v9\n\t"
        "v_cvt_pk_bf16_f32 v9, v10, v11\n\t"
        "v_cvt_pk_bf16_f32 v10, v12, v13\n\t"
        "v_cvt_pk_bf16_f32 v11, v14, v15\n\t"
        "ds_write_b128 v18, v[8:11] offset:46336\n\t"
        "v_mfma_f32_4x4x4_16b_bf16 v[24:27], v[6:7], v[60:61], v[24:27] cbsz:2 abid:2\n\t"
        "v_mfma_f32_4x4x4_16b_bf16 v[28:31], v[6:7], v[62:63], v[28:31] cbsz:2 abid:2\n\t"
        "v_cvt_pk_bf16_f32 v120, v120, v121\n\t"
        "v_cvt_pk_bf16_f32 v121, v122, v123\n\t"
        "v_cvt_pk_bf16_f32 v122, v124, v125\n\t"
        "v_cvt_pk_bf16_f32 v123, v126, v127\n\t"
        "ds_write_b128 v19, v[120:123] offset:46336\n\t"
        "v_permlane16_swap_b32 v24, v28\n\t"
        "v_permlane16_swap_b32 v25, v29\n\t"
        "v_permlane16_swap_b32 v26, v30\n\t"
        "v_permlane16_swap_b32 v27, v31\n\t"
        "v_pk_add_f32 v[24:25], v[24:25], v[28:29]\n\t"
        "v_pk_add_f32 v[26:27], v[26:27], v[30:31]\n\t"
        "v_cvt_pk_bf16_f32 v24, v24, v25\n\t"
        "v_cvt_pk_bf16_f32 v25, v26, v27\n\t"
        "ds_write_b64 v20, v[24:25] offset:46336\n\t"
        "s_mov_b64 exec, 1\n\t"
        "ds_add_u32 v2, v0 offset:40\n\t"
        "s_mov_b64 exec, -1\n"
        ".L7_na:\n\t"
        "ds_read_b64_tr_b16 v[120:121], v23 offset:12288\n\t"
        "ds_read_b64_tr_b16 v[122:123], v23 offset:12800\n\t"
        "ds_read_b64_tr_b16 v[124:125], v23 offset:12296\n\t"
        "ds_read_b64_tr_b16 v[126:127], v23 offset:12808\n\t"
        "ds_read_b128 v[4:7], v22\n\t"
        "s_lshl_b32 s50, s81, 6\n\t"
        "s_lshl_b32 s51, s69, 10\n\t"
        "s_add_u32 s51, s51, 134656\n\t"
        "s_mov_b32 s49, 0x10001\n\t"
        "s_mov_b32 s48, 0x07060302\n\t"
        "s_mov_b32 s55, 142848\n"
        ".L7_w15:\n\t"
        "ds_read_b32 v0, v2 offset:40\n\t"
        "s_waitcnt lgkmcnt(0)\n\t"
        "v_cmp_gt_u16 vcc, 64, v0\n\t"
        "s_cbranch_vccz .L7_d16\n\t"
        "s_setprio 0\n\t"
        "s_sleep 1\n\t"
        "s_sub_i32 s83, s83, 1\n\t"
        "s_cmp_gt_i32 s83, 0\n\t"
        "s_cbranch_scc1 .L7_w15\n"
        ".L7_d16:\n\t"
        "s_setprio 2\n\t"
        "ds_read_b128 v[8:11], v4 offset:46336\n\t"
        "ds_read_b128 v[12:15], v5 offset:46336\n\t"
        "ds_read_b128 v[16:19], v6 offset:46336\n\t"
        "ds_read_b128 v[20:23], v7 offset:46336\n\t"
        "ds_read_b128 v[48:51], v4 offset:50432\n\t"
        "ds_read_b128 v[52:55], v5 offset:50432\n\t"
        "ds_read_b128 v[56:59], v6 offset:50432\n\t"
        "ds_read_b128 v[60:63], v7 offset:50432\n\t"
        "v_and_b32 v3, 8, v1\n\t"
        "s_waitcnt lgkmcnt(6)\n\t"
        "v_mfma_f32_16x16x32_bf16 v[32:35], v[64:67], v[8:11], 0\n\t"
        "v_mfma_f32_16x16x32_bf16 v[36:39], v[68:71], v[8:11], 0\n\t"
        "v_mfma_f32_16x16x32_bf16 v[32:35], v[72:75], v[12:15], v[32:35]\n\t"
        "v_mfma_f32_16x16x32_bf16 v[36:39], v[76:79], v[12:15], v[36:39]\n\t"
        "s_waitcnt lgkmcnt(4)\n\t"
        "v_mfma_f32_16x16x32_bf16 v[32:35], v[80:83], v[16:19], v[32:35]\n\t"
        "v_mfma_f32_16x16x32_bf16 v[36:39], v[84:87], v[16:19], v[36:39]\n\t"
        "v_mfma_f32_16x16x32_bf16 v[32:35], v[88:91], v[20:23], v[32:35]\n\t"
        "v_mfma_f32_16x16x32_bf16 v[36:39], v[92:95], v[20:23], v[36:39]\n\t"
        "ds_read_b32 v0, v2 offset:40\n\t"
        "v_lshl_add_u32 v8, v3, 8, v4\n\t"
        "v_lshl_add_u32 v12, v3, 8, v5\n\t"
        "v_lshl_add_u32 v16, v3, 8, v6\n\t"
        "v_lshl_add_u32 v20, v3, 8, v7\n\t"
        "ds_read_b128 v[8:11], v8 offset:54528\n\t"
        "ds_read_b128 v[12:15], v12 offset:54528\n\t"
        "ds_read_b128 v[16:19], v16 offset:54528\n\t"
        "ds_read_b128 v[20:23], v20 offset:54528\n\t"
        "s_waitcnt lgkmcnt(7)\n\t"
        "v_mfma_f32_16x16x32_bf16 v[40:43], v[64:67], v[48:51], 0\n\t"
        "v_mfma_f32_16x16x32_bf16 v[44:47], v[68:71], v[48:51], 0\n\t"
        "v_mfma_f32_16x16x32_bf16 v[40:43], v[72:75], v[52:55], v[40:43]\n\t"
        "v_mfma_f32_16x16x32_bf16 v[44:47], v[76:79], v[52:55], v[44:47]\n\t"
        "s_waitcnt lgkmcnt(5)\n\t"
        "v_mfma_f32_16x16x32_bf16 v[40:43], v[80:83], v[56:59], v[40:43]\n\t"
        "v_mfma_f32_16x16x32_bf16 v[44:47], v[84:87], v[56:59], v[44:47]\n\t"
        "v_mfma_f32_16x16x32_bf16 v[40:43], v[88:91], v[60:63], v[40:43]\n\t"
        "v_mfma_f32_16x16x32_bf16 v[44:47], v[92:95], v[60:63], v[44:47]\n\t"
        "s_waitcnt lgkmcnt(4)\n\t"
        "v_cmp_gt_u16 vcc, 68, v0\n\t"
        "s_cbranch_vccz .L7_t0\n"
        ".L7_w17:\n\t"
        "ds_read_b32 v0, v2 offset:40\n\t"
        "s_waitcnt lgkmcnt(0)\n\t"
        "v_cmp_gt_u16 vcc, 68, v0\n\t"
        "s_cbranch_vccz .L7_d18\n\t"
        "s_setprio 0\n\t"
        "s_sleep 1\n\t"
        "s_sub_i32 s83, s83, 1\n\t"
        "s_cmp_gt_i32 s83, 0\n\t"
        "s_cbranch_scc1 .L7_w17\n"
        ".L7_d18:\n\t"
        "s_setprio 2\n\t"
        "v_lshl_add_u32 v8, v3, 8, v4\n\t"
        "v_lshl_add_u32 v12, v3, 8, v5\n\t"
        "v_lshl_add_u32 v16, v3, 8, v6\n\t"
        "v_lshl_add_u32 v20, v3, 8, v7\n\t"
        "ds_read_b128 v[8:11], v8 offset:54528\n\t"
        "ds_read_b128 v[12:15], v12 offset:54528\n\t"
        "ds_read_b128 v[16:19], v16 offset:54528\n\t"
        "ds_read_b128 v[20:23], v20 offset:54528\n"
        ".L7_t0:\n\t"
        "ds_read_b32 v0, v2 offset:52\n\t"
        "ds_read_b128 v[48:51], v4 offset:23808\n\t"
        "ds_read_b128 v[52:55], v5 offset:23808\n\t"
        "ds_read_b128 v[56:59], v6 offset:23808\n\t"
        "ds_read_b128 v[60:63], v7 offset:23808\n\t"
        "s_waitcnt lgkmcnt(7)\n\t"
        "v_mfma_f32_16x16x32_bf16 v[24:27], v[64:67], v[8:11], 0\n\t"
        "v_mfma_f32_16x16x32_bf16 v[28:31], v[68:71], v[8:11], 0\n\t"
        "v_mfma_f32_16x16x32_bf16 v[24:27], v[72:75], v[12:15], v[24:27]\n\t"
        "v_mfma_f32_16x16x32_bf16 v[28:31], v[76:79], v[12:15], v[28:31]\n\t"
        "s_waitcnt lgkmcnt(5)\n\t"
        "v_mfma_f32_16x16x32_bf16 v[24:27], v[80:83], v[16:19], v[24:27]\n\t"
        "v_mfma_f32_16x16x32_bf16 v[28:31], v[84:87], v[16:19], v[28:31]\n\t"
        "v_mfma_f32_16x16x32_bf16 v[24:27], v[88:91], v[20:23], v[24:27]\n\t"
        "v_mfma_f32_16x16x32_bf16 v[28:31], v[92:95], v[20:23], v[28:31]\n\t"
        "ds_read_b128 v[8:11], v4 offset:32000\n\t"
        "ds_read_b128 v[12:15], v5 offset:32000\n\t"
        "ds_read_b128 v[16:19], v6 offset:32000\n\t"
        "ds_read_b128 v[20:23], v7 offset:32000\n\t"
        "s_waitcnt lgkmcnt(8)\n\t"
        "v_cmp_gt_u16 vcc, 6, v0\n\t"
        "s_cbranch_vccz .L7_t1\n"
        ".L7_w19:\n\t"
        "ds_read_b32 v0, v2 offset:52\n\t"
        "s_waitcnt lgkmcnt(0)\n\t"
        "v_cmp_gt_u16 vcc, 6, v0\n\t"
        "s_cbranch_vccz .L7_d20\n\t"
        "s_setprio 0\n\t"
        "s_sleep 1\n\t"
        "s_sub_i32 s83, s83, 1\n\t"
        "s_cmp_gt_i32 s83, 0\n\t"
        "s_cbranch_scc1 .L7_w19\n"
        ".L7_d20:\n\t"
        "s_setprio 2\n\t"
        "ds_read_b128 v[48:51], v4 offset:23808\n\t"
        "ds_read_b128 v[52:55], v5 offset:23808\n\t"
        "ds_read_b128 v[56:59], v6 offset:23808\n\t"
        "ds_read_b128 v[60:63], v7 offset:23808\n\t"
        "ds_read_b128 v[8:11], v4 offset:32000\n\t"
        "ds_read_b128 v[12:15], v5 offset:32000\n\t"
        "ds_read_b128 v[16:19], v6 offset:32000\n\t"
        "ds_read_b128 v[20:23], v7 offset:32000\n"
        ".L7_t1:\n\t"
        "v_lshl_add_u32 v3, v1, 4, s55\n\t"
        "s_waitcnt lgkmcnt(6)\n\t"
        "v_mfma_f32_16x16x32_bf16 v[32:35], v[96:99], v[48:51], v[32:35]\n\t"
        "v_mfma_f32_16x16x32_bf16 v[36:39], v[100:103], v[48:51], v[36:39]\n\t"
        "v_mfma_f32_16x16x32_bf16 v[32:35], v[104:107], v[52:55], v[32:35]\n\t"
        "v_mfma_f32_16x16x32_bf16 v[36:39], v[108:111], v[52:55], v[36:39]\n\t"
        "s_waitcnt lgkmcnt(4)\n\t"
        "v_mfma_f32_16x16x32_bf16 v[32:35], v[112:115], v[56:59], v[32:35]\n\t"
        "v_mfma_f32_16x16x32_bf16 v[36:39], v[116:119], v[56:59], v[36:39]\n\t"
        "v_mfma_f32_16x16x32_bf16 v[32:35], v[120:123], v[60:63], v[32:35]\n\t"
        "v_mfma_f32_16x16x32_bf16 v[36:39], v[124:127], v[60:63], v[36:39]\n\t"
        "ds_read_b128 v[60:63], v3 offset:1024\n\t"
        "s_waitcnt lgkmcnt(3)\n\t"
        "v_mfma_f32_16x16x32_bf16 v[24:27], v[96:99], v[8:11], v[24:27]\n\t"
        "v_mfma_f32_16x16x32_bf16 v[28:31], v[100:103], v[8:11], v[28:31]\n\t"
        "v_mfma_f32_16x16x32_bf16 v[24:27], v[104:107], v[12:15], v[24:27]\n\t"
        "v_mfma_f32_16x16x32_bf16 v[28:31], v[108:111], v[12:15], v[28:31]\n\t"
        "s_waitcnt lgkmcnt(1)\n\t"
        "v_mfma_f32_16x16x32_bf16 v[24:27], v[112:115], v[16:19], v[24:27]\n\t"
        "v_mfma_f32_16x16x32_bf16 v[28:31], v[116:119], v[16:19], v[28:31]\n\t"
        "v_mfma_f32_16x16x32_bf16 v[24:27], v[120:123], v[20:23], v[24:27]\n\t"
        "v_mfma_f32_16x16x32_bf16 v[28:31], v[124:127], v[20:23], v[28:31]\n\t"
        "ds_read_b32 v0, v2 offset:56\n\t"
        "ds_read_b128 v[8:11], v4 offset:27904\n\t"
        "ds_read_b128 v[12:15], v5 offset:27904\n\t"
        "ds_read_b128 v[16:19], v6 offset:27904\n\t"
        "ds_read_b128 v[20:23], v7 offset:27904\n\t"
        "s_waitcnt lgkmcnt(5)\n\t"
        "v_add_u32 v32, 0x10000, v32\n\t"
        "v_add_u32 v33, 0x10000, v33\n\t"
        "v_add_u32 v34, 0x10000, v34\n\t"
        "v_add_u32 v35, 0x10000, v35\n\t"
        "v_perm_b32 v52, v33, v32, s48\n\t"
        "v_perm_b32 v53, v35, v34, s48\n\t"
        "v_cvt_pk_bf16_f32 v54, v36, v37\n\t"
        "v_cvt_pk_bf16_f32 v55, v38, v39\n\t"
        "v_bfi_b32 v52, s49, v60, v52\n\t"
        "v_bfi_b32 v53, s49, v61, v53\n\t"
        "buffer_store_dwordx4 v[52:55], v62, s[20:23], s25 offen sc1\n\t"
        "s_mov_b32 exec_lo, 0x00ff00ff\n\t"
        "s_mov_b32 exec_hi, 0x00ff00ff\n\t"
        "v_add_u32 v24, 0x10000, v24\n\t"
        "v_add_u32 v25, 0x10000, v25\n\t"
        "v_add_u32 v26, 0x10000, v26\n\t"
        "v_add_u32 v27, 0x10000, v27\n\t"
        "v_perm_b32 v48, v25, v24, s48\n\t"
        "v_perm_b32 v49, v27, v26, s48\n\t"
        "v_cvt_pk_bf16_f32 v50, v28, v29\n\t"
        "v_cvt_pk_bf16_f32 v51, v30, v31\n\t"
        "v_bfi_b32 v48, s49, v60, v48\n\t"
        "v_bfi_b32 v49, s49, v61, v49\n\t"
        "buffer_store_dwordx4 v[48:51], v62, s[20:23], s24 offen sc1\n\t"
        "s_mov_b64 exec, -1\n\t"
        "s_waitcnt lgkmcnt(4)\n\t"
        "v_cmp_gt_u16 vcc, 4, v0\n\t"
        "s_cbranch_vccz .L7_t2\n"
        ".L7_w21:\n\t"
        "ds_read_b32 v0, v2 offset:56\n\t"
        "s_waitcnt lgkmcnt(0)\n\t"
        "v_cmp_gt_u16 vcc, 3, v0\n\t"
        "s_cbranch_vccz .L7_d22\n\t"
        "s_setprio 0\n\t"
        "s_sleep 1\n\t"
        "s_sub_i32 s83, s83, 1\n\t"
        "s_cmp_gt_i32 s83, 0\n\t"
        "s_cbranch_scc1 .L7_w21\n"
        ".L7_d22:\n\t"
        "s_setprio 2\n"
        ".L7_w23:\n\t"
        "ds_read_b32 v0, v2 offset:56\n\t"
        "ds_read_b128 v[8:11], v4 offset:27904\n\t"
        "ds_read_b128 v[12:15], v5 offset:27904\n\t"
        "ds_read_b128 v[16:19], v6 offset:27904\n\t"
        "ds_read_b128 v[20:23], v7 offset:27904\n\t"
        "s_waitcnt lgkmcnt(4)\n\t"
        "v_cmp_gt_u16 vcc, 4, v0\n\t"
        "s_cbranch_vccz .L7_d24\n\t"
        "s_waitcnt lgkmcnt(0)\n\t"
        "s_setprio 0\n\t"
        "s_sleep 1\n\t"
        "s_sub_i32 s83, s83, 1\n\t"
        "s_cmp_gt_i32 s83, 0\n\t"
        "s_cbranch_scc1 .L7_w23\n"
        ".L7_d24:\n\t"
        "s_setprio 2\n"
        ".L7_t2:\n\t"
        "s_waitcnt lgkmcnt(2)\n\t"
        "v_mfma_f32_16x16x32_bf16 v[40:43], v[96:99], v[8:11], v[40:43]\n\t"
        "v_mfma_f32_16x16x32_bf16 v[44:47], v[100:103], v[8:11], v[44:47]\n\t"
        "v_mfma_f32_16x16x32_bf16 v[40:43], v[104:107], v[12:15], v[40:43]\n\t"
        "v_mfma_f32_16x16x32_bf16 v[44:47], v[108:111], v[12:15], v[44:47]\n\t"
        "s_waitcnt lgkmcnt(0)\n\t"
        "v_mfma_f32_16x16x32_bf16 v[40:43], v[112:115], v[16:19], v[40:43]\n\t"
        "v_mfma_f32_16x16x32_bf16 v[44:47], v[116:119], v[16:19], v[44:47]\n\t"
        "v_mfma_f32_16x16x32_bf16 v[40:43], v[120:123], v[20:23], v[40:43]\n\t"
        "v_mfma_f32_16x16x32_bf16 v[44:47], v[124:127], v[20:23], v[44:47]\n\t"
        "ds_read_b128 v[8:11], v4 offset:60672\n\t"
        "ds_read_b128 v[12:15], v5 offset:60672\n\t"
        "ds_read_b128 v[16:19], v6 offset:60672\n\t"
        "ds_read_b128 v[20:23], v7 offset:60672\n\t"
        "s_nop 2\n\t"
        "v_add_u32 v40, 0x10000, v40\n\t"
        "v_add_u32 v41, 0x10000, v41\n\t"
        "v_add_u32 v42, 0x10000, v42\n\t"
        "v_add_u32 v43, 0x10000, v43\n\t"
        "v_perm_b32 v56, v41, v40, s48\n\t"
        "v_perm_b32 v57, v43, v42, s48\n\t"
        "v_cvt_pk_bf16_f32 v58, v44, v45\n\t"
        "v_cvt_pk_bf16_f32 v59, v46, v47\n\t"
        "v_bfi_b32 v56, s49, v60, v56\n\t"
        "v_bfi_b32 v57, s49, v61, v57\n\t"
        "buffer_store_dwordx4 v[56:59], v62, s[20:23], s26 offen sc1\n\t"
        "v_mov_b32 v0, 1\n\t"
        "s_mov_b64 exec, 1\n\t"
        "ds_add_u32 v2, v0 offset:96\n\t"
        "s_mov_b64 exec, -1\n\t"
        "s_waitcnt lgkmcnt(3)\n\t"
        "v_mfma_f32_16x16x32_bf16 v[32:35], v[64:67], v[8:11], 0\n\t"
        "v_mfma_f32_16x16x32_bf16 v[36:39], v[68:71], v[8:11], 0\n\t"
        "v_mfma_f32_16x16x32_bf16 v[32:35], v[72:75], v[12:15], v[32:35]\n\t"
        "v_mfma_f32_16x16x32_bf16 v[36:39], v[76:79], v[12:15], v[36:39]\n\t"
        "s_waitcnt lgkmcnt(1)\n\t"
        "v_mfma_f32_16x16x32_bf16 v[32:35], v[80:83], v[16:19], v[32:35]\n\t"
        "v_mfma_f32_16x16x32_bf16 v[36:39], v[84:87], v[16:19], v[36:39]\n\t"
        "v_mfma_f32_16x16x32_bf16 v[32:35], v[88:91], v[20:23], v[32:35]\n\t"
        "v_mfma_f32_16x16x32_bf16 v[36:39], v[92:95], v[20:23], v[36:39]\n\t"
        "ds_read_b128 v[8:11], v4 offset:64768\n\t"
        "ds_read_b128 v[12:15], v5 offset:64768\n\t"
        "ds_read_b128 v[16:19], v6 offset:64768\n\t"
        "ds_read_b128 v[20:23], v7 offset:64768\n\t"
        "s_waitcnt lgkmcnt(2)\n\t"
        "v_mfma_f32_16x16x32_bf16 v[40:43], v[64:67], v[8:11], 0\n\t"
        "v_mfma_f32_16x16x32_bf16 v[44:47], v[68:71], v[8:11], 0\n\t"
        "v_mfma_f32_16x16x32_bf16 v[40:43], v[72:75], v[12:15], v[40:43]\n\t"
        "v_mfma_f32_16x16x32_bf16 v[44:47], v[76:79], v[12:15], v[44:47]\n\t"
        "s_waitcnt lgkmcnt(0)\n\t"
        "v_mfma_f32_16x16x32_bf16 v[40:43], v[80:83], v[16:19], v[40:43]\n\t"
        "v_mfma_f32_16x16x32_bf16 v[44:47], v[84:87], v[16:19], v[44:47]\n\t"
        "v_mfma_f32_16x16x32_bf16 v[40:43], v[88:91], v[20:23], v[40:43]\n\t"
        "v_mfma_f32_16x16x32_bf16 v[44:47], v[92:95], v[20:23], v[44:47]\n\t"
        "ds_read_b128 v[68:71], v3 offset:2048\n\t"
        "ds_read_b128 v[74:77], v3 offset:3072\n"
        ".L7_w25:\n\t"
        "ds_read_b32 v0, v2 offset:60\n\t"
        "s_waitcnt lgkmcnt(0)\n\t"
        "v_cmp_gt_u16 vcc, 2, v0\n\t"
        "s_cbranch_vccz .L7_d26\n\t"
        "s_setprio 0\n\t"
        "s_sleep 1\n\t"
        "s_sub_i32 s83, s83, 1\n\t"
        "s_cmp_gt_i32 s83, 0\n\t"
        "s_cbranch_scc1 .L7_w25\n"
        ".L7_d26:\n\t"
        "s_setprio 2\n\t"
        "ds_read_b128 v[8:11], v4 offset:34048\n\t"
        "ds_read_b128 v[12:15], v5 offset:34048\n\t"
        "ds_read_b128 v[16:19], v6 offset:34048\n\t"
        "ds_read_b128 v[20:23], v7 offset:34048\n\t"
        "v_add_u32 v71, s51, v71\n\t"
        "v_add_u32 v72, s51, v74\n\t"
        "v_mov_b64 v[74:75], 0\n\t"
        "v_mov_b64 v[78:79], 0\n\t"
        "s_waitcnt lgkmcnt(2)\n\t"
        "v_mfma_f32_16x16x32_bf16 v[24:27], v[96:99], v[8:11], v[24:27]\n\t"
        "v_mfma_f32_16x16x32_bf16 v[28:31], v[100:103], v[8:11], v[28:31]\n\t"
        "v_mfma_f32_16x16x32_bf16 v[24:27], v[104:107], v[12:15], v[24:27]\n\t"
        "v_mfma_f32_16x16x32_bf16 v[28:31], v[108:111], v[12:15], v[28:31]\n\t"
        "s_waitcnt lgkmcnt(0)\n\t"
        "v_mfma_f32_16x16x32_bf16 v[24:27], v[112:115], v[16:19], v[24:27]\n\t"
        "v_mfma_f32_16x16x32_bf16 v[28:31], v[116:119], v[16:19], v[28:31]\n\t"
        "v_mfma_f32_16x16x32_bf16 v[24:27], v[120:123], v[20:23], v[24:27]\n\t"
        "v_mfma_f32_16x16x32_bf16 v[28:31], v[124:127], v[20:23], v[28:31]\n"
        ".L7_w27:\n\t"
        "ds_read_b32 v0, v2 offset:100\n\t"
        "s_waitcnt lgkmcnt(0)\n\t"
        "v_cmp_gt_u16 vcc, 1, v0\n\t"
        "s_cbranch_vccz .L7_d28\n\t"
        "s_setprio 0\n\t"
        "s_sleep 1\n\t"
        "s_sub_i32 s83, s83, 1\n\t"
        "s_cmp_gt_i32 s83, 0\n\t"
        "s_cbranch_scc1 .L7_w27\n"
        ".L7_d28:\n\t"
        "s_setprio 2\n\t"
        "buffer_load_dwordx4 v[48:51], v63, s[20:23], s27 offen sc1\n\t"
        "buffer_load_dwordx4 v[52:55], v62, s[20:23], s28 offen sc1\n\t"
        "buffer_load_dwordx4 v[56:59], v62, s[20:23], s29 offen sc1\n"
        ".L7_w29:\n\t"
        "ds_read_b32 v0, v2 offset:64\n\t"
        "s_waitcnt lgkmcnt(0)\n\t"
        "v_cmp_gt_u16 vcc, 4, v0\n\t"
        "s_cbranch_vccz .L7_d30\n\t"
        "s_setprio 0\n\t"
        "s_sleep 1\n\t"
        "s_sub_i32 s83, s83, 1\n\t"
        "s_cmp_gt_i32 s83, 0\n\t"
        "s_cbranch_scc1 .L7_w29\n"
        ".L7_d30:\n\t"
        "s_setprio 2\n\t"
        "ds_read_b128 v[8:11], v4 offset:38144\n\t"
        "ds_read_b128 v[12:15], v5 offset:38144\n\t"
        "ds_read_b128 v[16:19], v6 offset:38144\n\t"
        "ds_read_b128 v[20:23], v7 offset:38144\n\t"
        "s_waitcnt lgkmcnt(2)\n\t"
        "v_mfma_f32_16x16x32_bf16 v[32:35], v[96:99], v[8:11], v[32:35]\n\t"
        "v_mfma_f32_16x16x32_bf16 v[36:39], v[100:103], v[8:11], v[36:39]\n\t"
        "v_mfma_f32_16x16x32_bf16 v[32:35], v[104:107], v[12:15], v[32:35]\n\t"
        "v_mfma_f32_16x16x32_bf16 v[36:39], v[108:111], v[12:15], v[36:39]\n\t"
        "s_waitcnt lgkmcnt(0)\n\t"
        "v_mfma_f32_16x16x32_bf16 v[32:35], v[112:115], v[16:19], v[32:35]\n\t"
        "v_mfma_f32_16x16x32_bf16 v[36:39], v[116:119], v[16:19], v[36:39]\n\t"
        "v_mfma_f32_16x16x32_bf16 v[32:35], v[120:123], v[20:23], v[32:35]\n\t"
        "v_mfma_f32_16x16x32_bf16 v[36:39], v[124:127], v[20:23], v[36:39]\n\t"
        "s_waitcnt vmcnt(0)\n\t"
        "s_branch .L7_s32\n"
        ".L7_r31:\n\t"
        "buffer_load_dwordx4 v[48:51], v63, s[20:23], s27 offen sc1\n\t"
        "buffer_load_dwordx4 v[52:55], v62, s[20:23], s28 offen sc1\n\t"
        "buffer_load_dwordx4 v[56:59], v62, s[20:23], s29 offen sc1\n\t"
        "s_waitcnt vmcnt(0)\n"
        ".L7_s32:\n\t"
        "v_xor_b32 v48, v48, v60\n\t"
        "v_xor_b32 v49, v49, v61\n\t"
        "v_xor_b32 v52, v52, v60\n\t"
        "v_xor_b32 v53, v53, v61\n\t"
        "v_xor_b32 v56, v56, v60\n\t"
        "v_xor_b32 v57, v57, v61\n\t"
        "v_or_b32 v0, v48, v49\n\t"
        "v_or3_b32 v0, v0, v52, v53\n\t"
        "v_or3_b32 v0, v0, v56, v57\n\t"
        "v_and_b32 v0, s49, v0\n\t"
        "v_cmp_ne_u32 vcc, 0, v0\n\t"
        "s_cbranch_vccz .L7_t33\n\t"
        "s_sleep 8\n\t"
        "s_sub_i32 s83, s83, 1\n\t"
        "s_cmp_gt_i32 s83, 0\n\t"
        "s_cbranch_scc1 .L7_r31\n"
        ".L7_t33:\n\t"
        "v_mfma_f32_16x16x32_bf16 v[24:27], v[76:79], v[48:51], v[24:27]\n\t"
        "v_mfma_f32_16x16x32_bf16 v[28:31], v[74:77], v[48:51], v[28:31]\n\t"
        "v_mfma_f32_16x16x32_bf16 v[32:35], v[76:79], v[52:55], v[32:35]\n\t"
        "v_mfma_f32_16x16x32_bf16 v[36:39], v[74:77], v[52:55], v[36:39]\n\t"
        "v_mfma_f32_16x16x32_bf16 v[40:43], v[76:79], v[56:59], v[40:43]\n\t"
        "v_mfma_f32_16x16x32_bf16 v[44:47], v[74:77], v[56:59], v[44:47]\n\t"
        "s_nop 2\n\t"
        "v_cvt_pk_bf16_f32 v48, v24, v25\n\t"
        "v_cvt_pk_bf16_f32 v49, v26, v27\n\t"
        "v_cvt_pk_bf16_f32 v50, v28, v29\n\t"
        "v_cvt_pk_bf16_f32 v51, v30, v31\n\t"
        "ds_write_b128 v71, v[48:51]\n\t"
        "ds_read_b128 v[48:51], v72\n\t"
        "v_cvt_pk_bf16_f32 v52, v32, v33\n\t"
        "v_cvt_pk_bf16_f32 v53, v34, v35\n\t"
        "v_cvt_pk_bf16_f32 v54, v36, v37\n\t"
        "v_cvt_pk_bf16_f32 v55, v38, v39\n\t"
        "ds_write_b128 v71, v[52:55]\n\t"
        "ds_read_b128 v[52:55], v72\n\t"
        "ds_read_b32 v0, v2 offset:68\n\t"
        "ds_read_b128 v[8:11], v4 offset:42240\n\t"
        "ds_read_b128 v[12:15], v5 offset:42240\n\t"
        "ds_read_b128 v[16:19], v6 offset:42240\n\t"
        "ds_read_b128 v[20:23], v7 offset:42240\n\t"
        "s_waitcnt lgkmcnt(4)\n\t"
        "buffer_store_dwordx4 v[48:51], v68, s[60:63], s50 offen sc1\n\t"
        "buffer_store_dwordx4 v[52:55], v69, s[60:63], s50 offen sc1\n\t"
        "v_cmp_gt_u16 vcc, 4, v0\n\t"
        "s_cbranch_vccz .L7_t5\n"
        ".L7_w34:\n\t"
        "ds_read_b32 v0, v2 offset:68\n\t"
        "s_waitcnt lgkmcnt(0)\n\t"
        "v_cmp_gt_u16 vcc, 3, v0\n\t"
        "s_cbranch_vccz .L7_d35\n\t"
        "s_setprio 0\n\t"
        "s_sleep 1\n\t"
        "s_sub_i32 s83, s83, 1\n\t"
        "s_cmp_gt_i32 s83, 0\n\t"
        "s_cbranch_scc1 .L7_w34\n"
        ".L7_d35:\n\t"
        "s_setprio 2\n"
        ".L7_w36:\n\t"
        "ds_read_b32 v0, v2 offset:68\n\t"
        "ds_read_b128 v[8:11], v4 offset:42240\n\t"
        "ds_read_b128 v[12:15], v5 offset:42240\n\t"
        "ds_read_b128 v[16:19], v6 offset:42240\n\t"
        "ds_read_b128 v[20:23], v7 offset:42240\n\t"
        "s_waitcnt lgkmcnt(4)\n\t"
        "v_cmp_gt_u16 vcc, 4, v0\n\t"
        "s_cbranch_vccz .L7_d37\n\t"
        "s_waitcnt lgkmcnt(0)\n\t"
        "s_setprio 0\n\t"
        "s_sleep 0\n\t"
        "s_sub_i32 s83, s83, 1\n\t"
        "s_cmp_gt_i32 s83, 0\n\t"
        "s_cbranch_scc1 .L7_w36\n"
        ".L7_d37:\n\t"
        "s_setprio 2\n"
        ".L7_t5:\n\t"
        "s_waitcnt lgkmcnt(2)\n\t"
        "v_mfma_f32_16x16x32_bf16 v[40:43], v[96:99], v[8:11], v[40:43]\n\t"
        "v_mfma_f32_16x16x32_bf16 v[44:47], v[100:103], v[8:11], v[44:47]\n\t"
        "v_mfma_f32_16x16x32_bf16 v[40:43], v[104:107], v[12:15], v[40:43]\n\t"
        "v_mfma_f32_16x16x32_bf16 v[44:47], v[108:111], v[12:15], v[44:47]\n\t"
        "s_waitcnt lgkmcnt(0)\n\t"
        "v_mfma_f32_16x16x32_bf16 v[40:43], v[112:115], v[16:19], v[40:43]\n\t"
        "v_mfma_f32_16x16x32_bf16 v[44:47], v[116:119], v[16:19], v[44:47]\n\t"
        "v_mfma_f32_16x16x32_bf16 v[40:43], v[120:123], v[20:23], v[40:43]\n\t"
        "v_mfma_f32_16x16x32_bf16 v[44:47], v[124:127], v[20:23], v[44:47]\n\t"
        "s_nop 6\n\t"
        "v_cvt_pk_bf16_f32 v56, v40, v41\n\t"
        "v_cvt_pk_bf16_f32 v57, v42, v43\n\t"
        "v_cvt_pk_bf16_f32 v58, v44, v45\n\t"
        "v_cvt_pk_bf16_f32 v59, v46, v47\n\t"
        "ds_write_b128 v71, v[56:59]\n\t"
        "ds_read_b128 v[56:59], v72\n\t"
        "s_waitcnt lgkmcnt(0)\n\t"
        "buffer_store_dwordx4 v[56:59], v70, s[60:63], s50 offen sc1\n\t"
        "s_or_b32 s78, %3, s69\n\t"
        "s_cbranch_scc1 .L7_ng\n\t"
        "v_mov_b32 v0, s68\n\t"
        "buffer_store_dword v0, off, s[20:23], 0 sc1\n"
        ".L7_ng:\n\t"
        "s_endpgm\n"
        ".L7_G2:\n\t"
        "s_setprio 3\n\t"
        "v_lshlrev_b32 v5, 4, v1\n\t"
        "s_mul_i32 s82, s76, 13\n\t"
        "s_lshr_b32 s82, s82, 6\n\t"
        "s_mul_i32 s82, s82, 5\n\t"
        "s_sub_u32 s82, s76, s82\n\t"
        "s_waitcnt lgkmcnt(0)\n\t"
        "s_lshl2_add_u32 s78, s82, s69\n\t"
        "s_lshl_b32 s78, s78, 10\n\t"
        "s_add_u32 m0, s78, 46336\n\t"
        "s_add_u32 s82, s82, 1\n\t"
        "s_cmp_eq_u32 s82, 5\n\t"
        "buffer_load_dwordx4 v5, s[56:59], s78 offen lds\n\t"
        "s_cselect_b32 s82, 0, s82\n\t"
        "v_lshrrev_b32 v18, 3, v1\n\t"
        "v_lshlrev_b32 v18, 14, v18\n\t"
        "v_and_b32 v6, 0x70, v5\n\t"
        "s_lshl_b32 s78, s69, 19\n\t"
        "s_lshl_b32 s79, s71, 7\n\t"
        "s_add_u32 s78, s78, s79\n\t"
        "v_add3_u32 v18, v18, v6, s78\n\t"
        "s_mov_b32 s24, 1048576\n\t"
        "s_mov_b32 s25, 1179648\n\t"
        "s_mov_b32 s26, 1310720\n\t"
        "s_mov_b32 s27, 1441792\n\t"
        "v_and_b32 v6, 0xf0, v5\n\t"
        "s_lshl_b32 s78, s70, 8\n\t"
        "v_add_u32 v62, s78, v6\n\t"
        "v_mov_b32 v7, 113860\n\t"
        "s_movk_i32 s38, 196\n\t"
        "s_mov_b32 s39, 12845056\n\t"
        "s_lshl_b32 s33, s69, 3\n\t"
        "s_lshl_b32 s78, 1, s33\n\t"
        "v_mov_b32 v29, s78\n\t"
        "s_sub_u32 s32, s78, 1\n\t"
        "s_and_b32 s32, s32, 0x01010101\n\t"
        "s_mov_b32 s35, 0xffffff\n\t"
        "s_mov_b32 s40, 0x13050109\n\t"
        "s_mov_b32 s41, 23\n\t"
        "s_cmp_eq_u32 s69, 1\n\t"
        "s_cselect_b32 s40, 0x12040008, s40\n\t"
        "s_cselect_b32 s41, 22, s41\n\t"
        "s_cmp_eq_u32 s69, 2\n\t"
        "s_cselect_b32 s40, 0x100e0602, s40\n\t"
        "s_cselect_b32 s41, 20, s41\n\t"
        "s_cmp_eq_u32 s69, 3\n\t"
        "s_cselect_b32 s40, 0x110f0703, s40\n\t"
        "s_cselect_b32 s41, 21, s41\n\t"
        "s_mul_i32 s41, s41, 0x01010101\n\t"
        "s_bitcmp1_b32 s40, 3\n\t"
        "s_cselect_b32 s78, 0, 4\n\t"
        "v_mov_b32 v61, s78\n\t"
        "v_lshrrev_b32 v12, 4, v1\n\t"
        "s_bfe_u32 s78, s40, 0x80000\n\t"
        "s_lshl_b32 s78, s78, 2\n\t"
        "v_add_u32 v13, s78, v12\n\t"
        "v_lshlrev_b32 v24, 2, v13\n\t"
        "v_lshlrev_b32 v15, 4, v13\n\t"
        "v_and_b32 v15, 0xf0, v15\n\t"
        "v_xor_b32 v15, v15, v6\n\t"
        "v_lshl_add_u32 v56, v13, 8, v15\n\t"
        "s_bfe_u32 s78, s40, 0x80008\n\t"
        "s_lshl_b32 s78, s78, 2\n\t"
        "v_add_u32 v13, s78, v12\n\t"
        "v_lshlrev_b32 v25, 2, v13\n\t"
        "v_lshlrev_b32 v15, 4, v13\n\t"
        "v_and_b32 v15, 0xf0, v15\n\t"
        "v_xor_b32 v15, v15, v6\n\t"
        "v_lshl_add_u32 v57, v13, 8, v15\n\t"
        "s_bfe_u32 s78, s40, 0x80010\n\t"
        "s_lshl_b32 s78, s78, 2\n\t"
        "v_add_u32 v13, s78, v12\n\t"
        "v_lshlrev_b32 v26, 2, v13\n\t"
        "v_lshlrev_b32 v15, 4, v13\n\t"
        "v_and_b32 v15, 0xf0, v15\n\t"
        "v_xor_b32 v15, v15, v6\n\t"
        "v_lshl_add_u32 v58, v13, 8, v15\n\t"
        "v_lshlrev_b32 v15, 9, v61\n\t"
        "v_sub_u32 v58, v58, v15\n\t"
        "s_bfe_u32 s78, s40, 0x80018\n\t"
        "s_lshl_b32 s78, s78, 2\n\t"
        "v_add_u32 v13, s78, v12\n\t"
        "v_lshlrev_b32 v27, 2, v13\n\t"
        "v_lshlrev_b32 v15, 4, v13\n\t"
        "v_and_b32 v15, 0xf0, v15\n\t"
        "v_xor_b32 v15, v15, v6\n\t"
        "v_lshl_add_u32 v59, v13, 8, v15\n\t"
        "s_bfe_u32 s78, s41, 0x80000\n\t"
        "s_lshl_b32 s78, s78, 2\n\t"
        "v_add_u32 v13, s78, v12\n\t"
        "v_lshlrev_b32 v28, 2, v13\n\t"
        "v_lshlrev_b32 v15, 4, v13\n\t"
        "v_and_b32 v15, 0xf0, v15\n\t"
        "v_xor_b32 v15, v15, v6\n\t"
        "v_lshl_add_u32 v60, v13, 8, v15\n\t"
        "v_and_b32 v20, 28, v1\n\t"
        "v_lshlrev_b32 v20, 1, v20\n\t"
        "v_lshrrev_b64 v[12:13], v20, s[40:41]\n\t"
        "v_and_b32 v13, 0xff, v12\n\t"
        "v_and_b32 v12, 3, v1\n\t"
        "v_lshl_add_u32 v13, v13, 2, v12\n\t"
        "v_lshlrev_b32 v31, 2, v13\n\t"
        "s_and_b32 s78, s67, 0x10000\n\t"
        "s_or_b32 s78, s78, 1\n\t"
        "v_mov_b32 v30, s78\n\t"
        "s_mov_b32 s36, 0\n\t"
        "v_and_b32 v0, 48, v5\n\t"
        "s_mov_b32 s78, 0x0f0e\n\t"
        "s_mov_b32 s79, 0\n\t"
        "v_lshlrev_b64 v[8:9], v0, s[78:79]\n\t"
        "s_mov_b32 s78, 0x0b0a\n\t"
        "v_lshlrev_b64 v[12:13], v0, s[78:79]\n\t"
        "s_mov_b32 s78, 0xf0f0f0f0\n\t"
        "s_mov_b32 s79, s78\n\t"
        "v_cndmask_b32 v8, v8, v12, s[78:79]\n\t"
        "v_cndmask_b32 v9, v9, v13, s[78:79]\n\t"
        "v_xor_b32 v8, 0x0c0c0c0c, v8\n\t"
        "v_xor_b32 v9, 0x0c0c0c0c, v9\n\t"
        "s_movk_i32 s48, 0x200\n\t"
        "s_mov_b32 s37, 0x01010101\n\t"
        "s_mov_b32 s49, 0x05040100\n\t"
        "s_getpc_b64 s[50:51]\n"
        ".L7_pc:\n\t"
        "s_add_u32 s52, s50, .L7_ce-.L7_pc\n\t"
        "s_addc_u32 s53, s51, 0\n\t"
        "s_add_u32 s42, s50, .L7_ev-.L7_pc\n\t"
        "s_addc_u32 s43, s51, 0\n\t"
        "s_add_u32 s50, s50, .L7_ie-.L7_pc\n\t"
        "s_addc_u32 s51, s51, 0\n\t"
        "s_cmp_eq_u32 s80, 10\n\t"
        "s_cselect_b32 s80, 0, s80\n\t"
        "s_mul_i32 s78, s80, 0x1080\n\t"
        "s_add_u32 m0, s78, s81\n\t"
        "s_lshr_b32 s78, s80, 1\n\t"
        "s_lshl1_add_u32 s78, s78, s80\n\t"
        "s_lshl_b32 s78, s78, 11\n\t"
        "buffer_load_dwordx4 v4, s[16:19], s78 offen lds\n\t"
        "s_add_u32 s80, s80, 1\n\t"
        "s_mul_i32 s78, s80, 0x1080\n\t"
        "s_add_u32 m0, s78, s81\n\t"
        "s_lshr_b32 s78, s80, 1\n\t"
        "s_lshl1_add_u32 s78, s78, s80\n\t"
        "s_lshl_b32 s78, s78, 11\n\t"
        "buffer_load_dwordx4 v4, s[16:19], s78 offen lds\n\t"
        "s_add_u32 s80, s80, 1\n\t"
        "s_mul_i32 s78, s80, 0x1080\n\t"
        "s_add_u32 m0, s78, s81\n\t"
        "s_lshr_b32 s78, s80, 1\n\t"
        "s_lshl1_add_u32 s78, s78, s80\n\t"
        "s_lshl_b32 s78, s78, 11\n\t"
        "buffer_load_dwordx4 v4, s[16:19], s78 offen lds\n\t"
        "s_add_u32 s80, s80, 1\n\t"
        "s_mul_i32 s78, s80, 0x1080\n\t"
        "s_add_u32 m0, s78, s81\n\t"
        "s_lshr_b32 s78, s80, 1\n\t"
        "s_lshl1_add_u32 s78, s78, s80\n\t"
        "s_lshl_b32 s78, s78, 11\n\t"
        "buffer_load_dwordx4 v4, s[16:19], s78 offen lds\n\t"
        "s_add_u32 s80, s80, 1\n\t"
        "s_mul_i32 s78, s80, 0x1080\n\t"
        "s_add_u32 m0, s78, s81\n\t"
        "s_lshr_b32 s78, s80, 1\n\t"
        "s_lshl1_add_u32 s78, s78, s80\n\t"
        "s_lshl_b32 s78, s78, 11\n\t"
        "buffer_load_dwordx4 v4, s[16:19], s78 offen lds\n\t"
        "s_add_u32 s80, s80, 1\n\t"
        "s_lshl2_add_u32 s78, s82, s69\n\t"
        "s_lshl_b32 s78, s78, 10\n\t"
        "s_add_u32 m0, s78, 46336\n\t"
        "s_add_u32 s82, s82, 1\n\t"
        "s_cmp_eq_u32 s82, 5\n\t"
        "buffer_load_dwordx4 v5, s[56:59], s78 offen lds\n\t"
        "s_cselect_b32 s82, 0, s82\n\t"
        "s_lshl2_add_u32 s78, s82, s69\n\t"
        "s_lshl_b32 s78, s78, 10\n\t"
        "s_add_u32 m0, s78, 46336\n\t"
        "s_add_u32 s82, s82, 1\n\t"
        "s_cmp_eq_u32 s82, 5\n\t"
        "buffer_load_dwordx4 v5, s[56:59], s78 offen lds\n\t"
        "s_cselect_b32 s82, 0, s82\n\t"
        "s_lshl2_add_u32 s78, s82, s69\n\t"
        "s_lshl_b32 s78, s78, 10\n\t"
        "s_add_u32 m0, s78, 46336\n\t"
        "s_add_u32 s82, s82, 1\n\t"
        "s_cmp_eq_u32 s82, 5\n\t"
        "buffer_load_dwordx4 v5, s[56:59], s78 offen lds\n\t"
        "s_cselect_b32 s82, 0, s82\n\t"
        "s_lshl2_add_u32 s78, s82, s69\n\t"
        "s_lshl_b32 s78, s78, 10\n\t"
        "s_add_u32 m0, s78, 46336\n\t"
        "s_add_u32 s82, s82, 1\n\t"
        "s_cmp_eq_u32 s82, 5\n\t"
        "buffer_load_dwordx4 v5, s[56:59], s78 offen lds\n\t"
        "s_cselect_b32 s82, 0, s82\n\t"
        "s_mov_b64 exec, 1\n\t"
        "ds_add_u32 v2, v3 offset:80\n\t"
        "s_mov_b64 exec, -1\n\t"
        "s_waitcnt vmcnt(4)\n\t"
        "s_mov_b64 exec, 1\n\t"
        "ds_add_u32 v2, v3 offset:0\n\t"
        "s_mov_b64 exec, -1\n"
        ".L7_w72:\n\t"
        "ds_read_b64 v[12:13], v2 offset:72\n\t"
        "s_waitcnt lgkmcnt(0)\n\t"
        "v_cmp_gt_u16 vcc, 2, v12\n\t"
        "s_cbranch_vccz .L7_d73\n\t"
        "s_sleep 1\n\t"
        "s_sub_i32 s83, s83, 1\n\t"
        "s_cmp_gt_i32 s83, 0\n\t"
        "s_cbranch_scc1 .L7_w72\n"
        ".L7_d73:\n\t"
        "v_readfirstlane_b32 s68, v13\n"
        ".L7_w74:\n\t"
        "ds_read_b32 v0, v2 offset:8\n\t"
        "s_waitcnt lgkmcnt(0)\n\t"
        "v_cmp_gt_u16 vcc, 2, v0\n\t"
        "s_cbranch_vccz .L7_d75\n\t"
        "s_sleep 1\n\t"
        "s_sub_i32 s83, s83, 1\n\t"
        "s_cmp_gt_i32 s83, 0\n\t"
        "s_cbranch_scc1 .L7_w74\n"
        ".L7_d75:\n\t"
        "ds_read_b32 v5, v31 offset:512\n\t"
        "ds_read_b32 v24, v24 offset:512\n\t"
        "ds_read_b32 v25, v25 offset:512\n\t"
        "ds_read_b32 v26, v26 offset:512\n\t"
        "ds_read_b32 v27, v27 offset:512\n\t"
        "ds_read_b32 v28, v28 offset:512\n\t"
        "s_waitcnt lgkmcnt(0)\n\t"
        "v_dot4_u32_u8 v31, v5, s38, v7\n\t"
        "s_waitcnt vmcnt(0)\n\t"
        "s_mov_b64 exec, 1\n\t"
        "ds_add_u32 v2, v3 offset:4\n\t"
        "s_mov_b64 exec, -1\n\t"
        "s_setprio 3\n\t"
        "s_lshl_b32 s44, s68, 24\n\t"
        "s_xor_b32 s45, s44, 0x5a000000\n\t"
        "s_mul_i32 s46, s68, 0x01010000\n\t"
        "s_mul_i32 s47, s68, 0x4000400\n\t"
        "s_and_b32 s47, s47, 0xff000000\n\t"
        "s_or_b32 s47, s47, 0xff0000\n"
        ".L7_w76:\n\t"
        "ds_read_b32 v0, v2 offset:92\n\t"
        "s_waitcnt lgkmcnt(0)\n\t"
        "v_cmp_gt_u16 vcc, 1, v0\n\t"
        "s_cbranch_vccz .L7_d77\n\t"
        "s_sleep 1\n\t"
        "s_sub_i32 s83, s83, 1\n\t"
        "s_cmp_gt_i32 s83, 0\n\t"
        "s_cbranch_scc1 .L7_w76\n"
        ".L7_d77:\n\t"
        "v_readfirstlane_b32 s78, v0\n\t"
        "s_sub_u32 s78, s78, 1\n\t"
        "s_add_u32 s77, s74, s78\n"
        ".L7_g78:\n\t"
        "s_memrealtime s[78:79]\n\t"
        "s_waitcnt lgkmcnt(0)\n\t"
        "s_sub_u32 s78, s78, s77\n\t"
        "s_cmp_ge_i32 s78, 295\n\t"
        "s_cbranch_scc1 .L7_h79\n\t"
        "s_sleep 1\n\t"
        "s_branch .L7_g78\n"
        ".L7_h79:\n\t"
        "s_branch .L7_ll\n"
        ".L7_again:\n\t"
        "s_waitcnt lgkmcnt(0)\n\t"
        "s_sleep 8\n\t"
        "s_sub_i32 s83, s83, 16\n\t"
        "s_cmp_gt_i32 s83, 0\n\t"
        "s_cbranch_scc0 .L7_got\n"
        ".L7_ll:\n\t"
        "buffer_load_dwordx4 v[64:67], v18, s[20:23], s24 offen sc1\n\t"
        "buffer_load_dwordx4 v[68:71], v18, s[20:23], s25 offen sc1\n\t"
        "buffer_load_dwordx4 v[72:75], v18, s[20:23], s26 offen sc1\n\t"
        "buffer_load_dwordx4 v[76:79], v18, s[20:23], s27 offen sc1\n\t"
        "s_mov_b64 exec, 1\n\t"
        "ds_add_u32 v2, v3 offset:108\n\t"
        "s_mov_b64 exec, -1\n"
        ".L7_w80:\n\t"
        "ds_read_b32 v0, v2 offset:4\n\t"
        "s_waitcnt lgkmcnt(0)\n\t"
        "v_cmp_gt_u16 vcc, 4, v0\n\t"
        "s_cbranch_vccz .L7_d81\n\t"
        "s_sleep 1\n\t"
        "s_sub_i32 s83, s83, 1\n\t"
        "s_cmp_gt_i32 s83, 0\n\t"
        "s_cbranch_scc1 .L7_w80\n"
        ".L7_d81:\n\t"
        "s_waitcnt vmcnt(3)\n\t"
        "v_xor_b32 v5, s44, v64\n\t"
        "v_bitop3_b32 v5, v65, v5, s45 bitop3:0xde\n\t"
        "v_bitop3_b32 v5, v66, v5, s46 bitop3:0xde\n\t"
        "v_xor_b32 v4, s47, v67\n\t"
        "v_lshlrev_b32_sdwa v112, 1, v64 dst_sel:DWORD dst_unused:UNUSED_PAD src0_sel:DWORD src1_sel:WORD_0\n\t"
        "ds_read_u16 v112, v112 offset:46336\n\t"
        "v_lshlrev_b32_sdwa v113, 1, v65 dst_sel:DWORD dst_unused:UNUSED_PAD src0_sel:DWORD src1_sel:WORD_0\n\t"
        "ds_read_u16 v113, v113 offset:46336\n\t"
        "v_lshlrev_b32_sdwa v114, 1, v66 dst_sel:DWORD dst_unused:UNUSED_PAD src0_sel:DWORD src1_sel:WORD_0\n\t"
        "ds_read_u16 v114, v114 offset:46336\n\t"
        "v_dot4_u32_u8 v80, v64, s39, v7\n\t"
        "v_dot4_u32_u8 v81, v65, s39, v7\n\t"
        "v_dot4_u32_u8 v82, v66, s39, v7\n\t"
        "s_waitcnt vmcnt(2)\n\t"
        "v_bitop3_b32 v5, v68, v5, s44 bitop3:0xde\n\t"
        "v_bitop3_b32 v5, v69, v5, s45 bitop3:0xde\n\t"
        "v_bitop3_b32 v5, v70, v5, s46 bitop3:0xde\n\t"
        "v_bitop3_b32 v4, v71, v4, s47 bitop3:0xde\n\t"
        "v_lshlrev_b32_sdwa v116, 1, v68 dst_sel:DWORD dst_unused:UNUSED_PAD src0_sel:DWORD src1_sel:WORD_0\n\t"
        "ds_read_u16 v116, v116 offset:46336\n\t"
        "v_lshlrev_b32_sdwa v117, 1, v69 dst_sel:DWORD dst_unused:UNUSED_PAD src0_sel:DWORD src1_sel:WORD_0\n\t"
        "ds_read_u16 v117, v117 offset:46336\n\t"
        "v_lshlrev_b32_sdwa v118, 1, v70 dst_sel:DWORD dst_unused:UNUSED_PAD src0_sel:DWORD src1_sel:WORD_0\n\t"
        "ds_read_u16 v118, v118 offset:46336\n\t"
        "v_dot4_u32_u8 v84, v68, s39, v7\n\t"
        "v_dot4_u32_u8 v85, v69, s39, v7\n\t"
        "v_dot4_u32_u8 v86, v70, s39, v7\n\t"
        "s_waitcnt vmcnt(1)\n\t"
        "v_bitop3_b32 v5, v72, v5, s44 bitop3:0xde\n\t"
        "v_bitop3_b32 v5, v73, v5, s45 bitop3:0xde\n\t"
        "v_bitop3_b32 v5, v74, v5, s46 bitop3:0xde\n\t"
        "v_bitop3_b32 v4, v75, v4, s47 bitop3:0xde\n\t"
        "v_lshlrev_b32_sdwa v120, 1, v72 dst_sel:DWORD dst_unused:UNUSED_PAD src0_sel:DWORD src1_sel:WORD_0\n\t"
        "ds_read_u16 v120, v120 offset:46336\n\t"
        "v_lshlrev_b32_sdwa v121, 1, v73 dst_sel:DWORD dst_unused:UNUSED_PAD src0_sel:DWORD src1_sel:WORD_0\n\t"
        "ds_read_u16 v121, v121 offset:46336\n\t"
        "v_lshlrev_b32_sdwa v122, 1, v74 dst_sel:DWORD dst_unused:UNUSED_PAD src0_sel:DWORD src1_sel:WORD_0\n\t"
        "ds_read_u16 v122, v122 offset:46336\n\t"
        "v_dot4_u32_u8 v88, v72, s39, v7\n\t"
        "v_dot4_u32_u8 v89, v73, s39, v7\n\t"
        "v_dot4_u32_u8 v90, v74, s39, v7\n\t"
        "s_waitcnt vmcnt(0)\n\t"
        "v_bitop3_b32 v5, v76, v5, s44 bitop3:0xde\n\t"
        "v_bitop3_b32 v5, v77, v5, s45 bitop3:0xde\n\t"
        "v_bitop3_b32 v5, v78, v5, s46 bitop3:0xde\n\t"
        "v_bitop3_b32 v4, v79, v4, s47 bitop3:0xde\n\t"
        "v_lshlrev_b32_sdwa v124, 1, v76 dst_sel:DWORD dst_unused:UNUSED_PAD src0_sel:DWORD src1_sel:WORD_0\n\t"
        "ds_read_u16 v124, v124 offset:46336\n\t"
        "v_lshlrev_b32_sdwa v125, 1, v77 dst_sel:DWORD dst_unused:UNUSED_PAD src0_sel:DWORD src1_sel:WORD_0\n\t"
        "ds_read_u16 v125, v125 offset:46336\n\t"
        "v_lshlrev_b32_sdwa v126, 1, v78 dst_sel:DWORD dst_unused:UNUSED_PAD src0_sel:DWORD src1_sel:WORD_0\n\t"
        "ds_read_u16 v126, v126 offset:46336\n\t"
        "v_dot4_u32_u8 v92, v76, s39, v7\n\t"
        "v_dot4_u32_u8 v93, v77, s39, v7\n\t"
        "v_dot4_u32_u8 v94, v78, s39, v7\n\t"
        "v_and_b32 v6, 0xfc000000, v4\n\t"
        "v_or_b32 v5, v5, v6\n\t"
        "v_cmp_lt_u32 vcc, s35, v5\n\t"
        "s_cbranch_vccnz .L7_again\n"
        ".L7_got:\n\t"
        "v_and_b32 v6, s35, v4\n\t"
        "s_waitcnt lgkmcnt(2)\n\t"
        "ds_add_rtn_u32 v96, v80, v29 offset:192\n\t"
        "ds_add_rtn_u32 v97, v81, v29 offset:192\n\t"
        "ds_add_rtn_u32 v98, v82, v29 offset:192\n\t"
        "ds_add_rtn_u32 v100, v84, v29 offset:192\n\t"
        "ds_add_rtn_u32 v101, v85, v29 offset:192\n\t"
        "ds_add_rtn_u32 v102, v86, v29 offset:192\n\t"
        "ds_add_rtn_u32 v104, v88, v29 offset:192\n\t"
        "ds_add_rtn_u32 v105, v89, v29 offset:192\n\t"
        "ds_add_rtn_u32 v106, v90, v29 offset:192\n\t"
        "ds_add_rtn_u32 v108, v92, v29 offset:192\n\t"
        "ds_add_rtn_u32 v109, v93, v29 offset:192\n\t"
        "ds_add_rtn_u32 v110, v94, v29 offset:192\n\t"
        "v_cmp_ne_u32 vcc, 0, v6\n\t"
        "s_cbranch_vccnz .L7_x\n"
        ".L7_y:\n\t"
        "s_mov_b64 exec, 1\n\t"
        "ds_add_u32 v2, v3 offset:32\n\t"
        "s_mov_b64 exec, -1\n\t"
        "v_perm_b32 v112, v112, v64, s49\n\t"
        "v_perm_b32 v113, v113, v65, s49\n\t"
        "v_perm_b32 v114, v114, v66, s49\n\t"
        "v_perm_b32 v116, v116, v68, s49\n\t"
        "v_perm_b32 v117, v117, v69, s49\n\t"
        "v_perm_b32 v118, v118, v70, s49\n\t"
        "v_perm_b32 v120, v120, v72, s49\n\t"
        "v_perm_b32 v121, v121, v73, s49\n\t"
        "v_perm_b32 v122, v122, v74, s49\n\t"
        "v_perm_b32 v124, v124, v76, s49\n\t"
        "s_cmp_lt_u32 s69, 1\n\t"
        "s_cbranch_scc0 .L7_nv\n\t"
        "s_mov_b32 s79, 0\n\t"
        "v_xor_b32 v32, s47, v67\n\t"
        "v_xor_b32 v33, s47, v71\n\t"
        "v_xor_b32 v34, s47, v75\n\t"
        "v_xor_b32 v35, s47, v79\n\t"
        "v_lshlrev_b32 v32, 6, v32\n\t"
        "v_lshlrev_b32 v33, 6, v33\n\t"
        "v_lshlrev_b32 v34, 6, v34\n\t"
        "v_lshlrev_b32 v35, 6, v35\n\t"
        "s_mov_b32 exec_lo, s37\n\t"
        "s_mov_b32 exec_hi, s37\n\t"
        "v_cmp_gt_i32 vcc, 0, v32\n\t"
        "s_bcnt1_i32_b64 s78, vcc\n\t"
        "s_add_u32 s79, s79, s78\n\t"
        "v_cmp_gt_i32 vcc, 0, v33\n\t"
        "s_bcnt1_i32_b64 s78, vcc\n\t"
        "s_add_u32 s79, s79, s78\n\t"
        "v_cmp_gt_i32 vcc, 0, v34\n\t"
        "s_bcnt1_i32_b64 s78, vcc\n\t"
        "s_add_u32 s79, s79, s78\n\t"
        "v_cmp_gt_i32 vcc, 0, v35\n\t"
        "s_bcnt1_i32_b64 s78, vcc\n\t"
        "s_add_u32 s79, s79, s78\n\t"
        "s_lshl_b32 s79, s79, 16\n\t"
        "s_or_b32 s79, s79, 1\n\t"
        "v_mov_b32 v32, s79\n\t"
        "s_mov_b64 exec, 1\n\t"
        "ds_add_u32 v2, v32 offset:116\n\t"
        "s_mov_b64 exec, -1\n"
        ".L7_nv:\n\t"
        "v_lshlrev_b32 v4, 7, v4\n\t"
        "v_cmp_gt_i32 vcc, 0, v4\n\t"
        "s_cmp_lg_u64 vcc, 0\n\t"
        "s_cselect_b32 s78, 0x100000, 0\n\t"
        "v_or_b32 v30, s78, v30\n\t"
        "s_waitcnt lgkmcnt(1)\n\t"
        "v_perm_b32 v125, v125, v77, s49\n\t"
        "v_perm_b32 v126, v126, v78, s49\n\t"
        "v_bfe_u32 v96, v96, s33, 8\n\t"
        "v_bfe_u32 v97, v97, s33, 8\n\t"
        "v_bfe_u32 v98, v98, s33, 8\n\t"
        "v_bfe_u32 v100, v100, s33, 8\n\t"
        "v_bfe_u32 v101, v101, s33, 8\n\t"
        "v_bfe_u32 v102, v102, s33, 8\n\t"
        "v_bfe_u32 v104, v104, s33, 8\n\t"
        "v_bfe_u32 v105, v105, s33, 8\n\t"
        "v_bfe_u32 v106, v106, s33, 8\n\t"
        "v_bfe_u32 v108, v108, s33, 8\n\t"
        "v_bfe_u32 v109, v109, s33, 8\n\t"
        "v_bfe_u32 v110, v110, s33, 8\n"
        ".L7_w82:\n\t"
        "ds_read_b32 v0, v2 offset:32\n\t"
        "s_waitcnt lgkmcnt(0)\n\t"
        "v_cmp_gt_u16 vcc, 4, v0\n\t"
        "s_cbranch_vccz .L7_d83\n\t"
        "s_sleep 1\n\t"
        "s_sub_i32 s83, s83, 1\n\t"
        "s_cmp_gt_i32 s83, 0\n\t"
        "s_cbranch_scc1 .L7_w82\n"
        ".L7_d83:\n\t"
        "ds_read_b32 v32, v80 offset:192\n\t"
        "ds_read_b32 v33, v81 offset:192\n\t"
        "ds_read_b32 v34, v82 offset:192\n\t"
        "ds_read_b32 v35, v84 offset:192\n\t"
        "ds_read_b32 v36, v85 offset:192\n\t"
        "ds_read_b32 v37, v86 offset:192\n\t"
        "ds_read_b32 v38, v88 offset:192\n\t"
        "ds_read_b32 v39, v89 offset:192\n\t"
        "ds_read_b32 v40, v90 offset:192\n\t"
        "ds_read_b32 v41, v92 offset:192\n\t"
        "ds_read_b32 v42, v93 offset:192\n\t"
        "ds_read_b32 v43, v94 offset:192\n\t"
        "ds_read_b32 v6, v31 offset:192\n\t"
        "s_waitcnt lgkmcnt(10)\n\t"
        "v_dot4_u32_u8 v96, v32, s32, v96\n\t"
        "v_dot4_u32_u8 v97, v33, s32, v97\n\t"
        "v_dot4_u32_u8 v98, v34, s32, v98\n\t"
        "s_waitcnt lgkmcnt(7)\n\t"
        "v_dot4_u32_u8 v100, v35, s32, v100\n\t"
        "v_dot4_u32_u8 v101, v36, s32, v101\n\t"
        "v_dot4_u32_u8 v102, v37, s32, v102\n\t"
        "s_waitcnt lgkmcnt(4)\n\t"
        "v_dot4_u32_u8 v104, v38, s32, v104\n\t"
        "v_dot4_u32_u8 v105, v39, s32, v105\n\t"
        "v_dot4_u32_u8 v106, v40, s32, v106\n\t"
        "s_waitcnt lgkmcnt(1)\n\t"
        "v_dot4_u32_u8 v108, v41, s32, v108\n\t"
        "v_dot4_u32_u8 v109, v42, s32, v109\n\t"
        "v_dot4_u32_u8 v110, v43, s32, v110\n\t"
        "v_min_u32 v96, 47, v96\n\t"
        "v_min_u32 v97, 47, v97\n\t"
        "v_min_u32 v98, 47, v98\n\t"
        "v_min_u32 v100, 47, v100\n\t"
        "v_min_u32 v101, 47, v101\n\t"
        "v_min_u32 v102, 47, v102\n\t"
        "v_min_u32 v104, 47, v104\n\t"
        "v_min_u32 v105, 47, v105\n\t"
        "v_min_u32 v106, 47, v106\n\t"
        "v_min_u32 v108, 47, v108\n\t"
        "v_min_u32 v109, 47, v109\n\t"
        "v_min_u32 v110, 47, v110\n\t"
        "v_lshl_add_u32 v96, v96, 2, v80\n\t"
        "ds_write_b32 v96, v112\n\t"
        "v_lshl_add_u32 v97, v97, 2, v81\n\t"
        "ds_write_b32 v97, v113\n\t"
        "v_lshl_add_u32 v98, v98, 2, v82\n\t"
        "ds_write_b32 v98, v114\n\t"
        "v_lshl_add_u32 v100, v100, 2, v84\n\t"
        "ds_write_b32 v100, v116\n\t"
        "v_lshl_add_u32 v101, v101, 2, v85\n\t"
        "ds_write_b32 v101, v117\n\t"
        "v_lshl_add_u32 v102, v102, 2, v86\n\t"
        "ds_write_b32 v102, v118\n\t"
        "v_lshl_add_u32 v104, v104, 2, v88\n\t"
        "ds_write_b32 v104, v120\n\t"
        "v_lshl_add_u32 v105, v105, 2, v89\n\t"
        "ds_write_b32 v105, v121\n\t"
        "v_lshl_add_u32 v106, v106, 2, v90\n\t"
        "ds_write_b32 v106, v122\n\t"
        "v_lshl_add_u32 v108, v108, 2, v92\n\t"
        "ds_write_b32 v108, v124\n\t"
        "v_lshl_add_u32 v109, v109, 2, v93\n\t"
        "ds_write_b32 v109, v125\n\t"
        "v_lshl_add_u32 v110, v110, 2, v94\n\t"
        "ds_write_b32 v110, v126\n\t"
        "s_cmp_lg_u32 s36, 0\n\t"
        "s_cbranch_scc1 .L7_z\n"
        ".L7_zr:\n\t"
        "s_mov_b64 exec, 1\n\t"
        "ds_add_u32 v2, v30 offset:36\n\t"
        "s_mov_b64 exec, -1\n\t"
        "s_waitcnt lgkmcnt(13)\n\t"
        "v_dot4_u32_u8 v6, v6, s37, -1\n\t"
        "v_mov_b64 v[32:33], 0\n\t"
        "v_mov_b64 v[34:35], 0\n\t"
        "v_mov_b64 v[36:37], 0\n\t"
        "v_med3_i32 v6, v6, 0, 47\n\t"
        "v_lshrrev_b32 v6, 1, v6\n\t"
        "v_mov_b64 v[38:39], 0\n\t"
        "v_dot4_u32_u8 v63, v24, s38, v7\n\t"
        "v_max_u32_dpp v6, v6, v6 quad_perm:[1,0,3,2] row_mask:0xf bank_mask:0xf\n\t"
        "s_nop 1\n\t"
        "v_max_u32_dpp v6, v6, v6 quad_perm:[2,3,0,1] row_mask:0xf bank_mask:0xf\n\t"
        "s_nop 0\n\t"
        "v_readlane_b32 s24, v6, 0\n\t"
        "v_readlane_b32 s25, v6, 4\n\t"
        "v_readlane_b32 s26, v6, 8\n\t"
        "v_readlane_b32 s27, v6, 12\n\t"
        "v_readlane_b32 s28, v6, 16\n\t"
        "s_max_u32 s28, s28, 5\n\t"
        "s_mov_b32 s56, s24\n\t"
        "s_mov_b32 s57, s24\n\t"
        "s_mov_b32 s78, 0xf0000\n"
        ".L7_w84:\n\t"
        "ds_read_b32 v4, v2 offset:36\n\t"
        "ds_read_b32 v12, v31 offset:188\n\t"
        "s_waitcnt lgkmcnt(1)\n\t"
        "v_cmp_gt_u16 vcc, 4, v4\n\t"
        "s_cbranch_vccz .L7_d85\n\t"
        "s_waitcnt lgkmcnt(0)\n\t"
        "s_sleep 1\n\t"
        "s_sub_i32 s83, s83, 1\n\t"
        "s_cmp_gt_i32 s83, 0\n\t"
        "s_cbranch_scc1 .L7_w84\n"
        ".L7_d85:\n\t"
        "ds_read2_b32 v[40:41], v63 offset0:0 offset1:1\n\t"
        "s_sub_u32 s56, s56, 1\n\t"
        "s_cbranch_scc1 .L7_i86\n"
        ".L7_j87:\n\t"
        "ds_read2_b32 v[42:43], v63 offset0:2 offset1:3\n\t"
        "s_sub_u32 s56, s56, 1\n\t"
        "s_cbranch_scc1 .L7_i88\n"
        ".L7_j89:\n\t"
        "ds_read2_b32 v[44:45], v63 offset0:4 offset1:5\n\t"
        "s_sub_u32 s56, s56, 1\n\t"
        "s_cbranch_scc1 .L7_i90\n"
        ".L7_j91:\n\t"
        "ds_read2_b32 v[46:47], v63 offset0:6 offset1:7\n\t"
        "s_sub_u32 s56, s56, 1\n\t"
        "s_cbranch_scc1 .L7_i92\n"
        ".L7_j93:\n\t"
        "ds_read2_b32 v[48:49], v63 offset0:8 offset1:9\n\t"
        "s_sub_u32 s56, s56, 1\n\t"
        "s_cbranch_scc1 .L7_i94\n"
        ".L7_j95:\n\t"
        "ds_read2_b32 v[50:51], v63 offset0:10 offset1:11\n\t"
        "s_sub_u32 s56, s56, 1\n\t"
        "s_cbranch_scc1 .L7_i96\n"
        ".L7_j97:\n\t"
        "ds_read2_b32 v[52:53], v63 offset0:12 offset1:13\n\t"
        "s_sub_u32 s56, s56, 1\n\t"
        "s_cbranch_scc1 .L7_i98\n"
        ".L7_j99:\n\t"
        "ds_read2_b32 v[54:55], v63 offset0:14 offset1:15\n\t"
        "s_sub_u32 s56, s56, 1\n\t"
        "s_cbranch_scc1 .L7_i100\n"
        ".L7_j101:\n\t"
        "s_waitcnt lgkmcnt(8)\n\t"
        "v_and_or_b32 v12, v4, s78, v12\n\t"
        "v_cmp_ne_u32 vcc, 0xffff, v12\n\t"
        "s_cbranch_vccnz .L7_exit4\n\t"
        "s_waitcnt lgkmcnt(7)\n\t"
        "v_mad_u32_u16 v10, v40, s48, v62\n\t"
        "v_mad_u32_u16 v11, v41, s48, v62\n\t"
        "buffer_load_dwordx4 v[64:67], v10, s[16:19], 0 offen\n\t"
        "buffer_load_dwordx4 v[68:71], v11, s[16:19], 0 offen\n\t"
        "s_waitcnt lgkmcnt(6)\n\t"
        "v_mad_u32_u16 v10, v42, s48, v62\n\t"
        "v_mad_u32_u16 v11, v43, s48, v62\n\t"
        "buffer_load_dwordx4 v[72:75], v10, s[16:19], 0 offen\n\t"
        "buffer_load_dwordx4 v[76:79], v11, s[16:19], 0 offen\n\t"
        "s_waitcnt lgkmcnt(5)\n\t"
        "v_mad_u32_u16 v10, v44, s48, v62\n\t"
        "v_mad_u32_u16 v11, v45, s48, v62\n\t"
        "buffer_load_dwordx4 v[80:83], v10, s[16:19], 0 offen\n\t"
        "buffer_load_dwordx4 v[84:87], v11, s[16:19], 0 offen\n\t"
        "s_waitcnt lgkmcnt(4)\n\t"
        "v_mad_u32_u16 v10, v46, s48, v62\n\t"
        "v_mad_u32_u16 v11, v47, s48, v62\n\t"
        "buffer_load_dwordx4 v[88:91], v10, s[16:19], 0 offen\n\t"
        "buffer_load_dwordx4 v[92:95], v11, s[16:19], 0 offen\n\t"
        "s_waitcnt lgkmcnt(3)\n\t"
        "v_mad_u32_u16 v10, v48, s48, v62\n\t"
        "v_mad_u32_u16 v11, v49, s48, v62\n\t"
        "buffer_load_dwordx4 v[96:99], v10, s[16:19], 0 offen\n\t"
        "buffer_load_dwordx4 v[100:103], v11, s[16:19], 0 offen\n\t"
        "s_waitcnt lgkmcnt(2)\n\t"
        "v_mad_u32_u16 v10, v50, s48, v62\n\t"
        "v_mad_u32_u16 v11, v51, s48, v62\n\t"
        "buffer_load_dwordx4 v[104:107], v10, s[16:19], 0 offen\n\t"
        "buffer_load_dwordx4 v[108:111], v11, s[16:19], 0 offen\n\t"
        "s_min_u32 s59, s56, s57\n\t"
        "s_add_u32 s58, s59, 1\n\t"
        "s_setprio 3\n"
        ".L7_loop:\n\t"
        "v_add_u32 v63, 64, v63\n\t"
        "v_perm_b32 v12, v41, v40, v8\n\t"
        "v_perm_b32 v13, v41, v40, v9\n\t"
        "s_waitcnt vmcnt(10) lgkmcnt(1)\n\t"
        "v_mad_u32_u16 v10, v52, s48, v62\n\t"
        "v_mad_u32_u16 v11, v53, s48, v62\n\t"
        "buffer_load_dwordx4 v[112:115], v10, s[16:19], 0 offen\n\t"
        "buffer_load_dwordx4 v[116:119], v11, s[16:19], 0 offen\n\t"
        "v_mfma_f32_4x4x4_16b_bf16 v[32:35], v[12:13], v[64:65], v[32:35] cbsz:1 abid:0\n\t"
        "v_mfma_f32_4x4x4_16b_bf16 v[36:39], v[12:13], v[66:67], v[36:39] cbsz:1 abid:0\n\t"
        "ds_read2_b32 v[40:41], v63 offset0:0 offset1:1\n\t"
        "v_mfma_f32_4x4x4_16b_bf16 v[32:35], v[12:13], v[68:69], v[32:35] cbsz:1 abid:1\n\t"
        "v_mfma_f32_4x4x4_16b_bf16 v[36:39], v[12:13], v[70:71], v[36:39] cbsz:1 abid:1\n\t"
        "s_sub_u32 s59, s59, 1\n\t"
        "s_cbranch_scc1 .L7_i102\n"
        ".L7_j103:\n\t"
        "v_perm_b32 v12, v43, v42, v8\n\t"
        "v_perm_b32 v13, v43, v42, v9\n\t"
        "s_waitcnt vmcnt(10) lgkmcnt(1)\n\t"
        "v_mad_u32_u16 v10, v54, s48, v62\n\t"
        "v_mad_u32_u16 v11, v55, s48, v62\n\t"
        "buffer_load_dwordx4 v[120:123], v10, s[16:19], 0 offen\n\t"
        "buffer_load_dwordx4 v[124:127], v11, s[16:19], 0 offen\n\t"
        "v_mfma_f32_4x4x4_16b_bf16 v[32:35], v[12:13], v[72:73], v[32:35] cbsz:1 abid:0\n\t"
        "v_mfma_f32_4x4x4_16b_bf16 v[36:39], v[12:13], v[74:75], v[36:39] cbsz:1 abid:0\n\t"
        "ds_read2_b32 v[42:43], v63 offset0:2 offset1:3\n\t"
        "v_mfma_f32_4x4x4_16b_bf16 v[32:35], v[12:13], v[76:77], v[32:35] cbsz:1 abid:1\n\t"
        "v_mfma_f32_4x4x4_16b_bf16 v[36:39], v[12:13], v[78:79], v[36:39] cbsz:1 abid:1\n\t"
        "s_sub_u32 s59, s59, 1\n\t"
        "s_cbranch_scc1 .L7_i104\n"
        ".L7_j105:\n\t"
        "v_perm_b32 v12, v45, v44, v8\n\t"
        "v_perm_b32 v13, v45, v44, v9\n\t"
        "s_waitcnt vmcnt(10) lgkmcnt(1)\n\t"
        "v_mad_u32_u16 v10, v40, s48, v62\n\t"
        "v_mad_u32_u16 v11, v41, s48, v62\n\t"
        "buffer_load_dwordx4 v[64:67], v10, s[16:19], 0 offen\n\t"
        "buffer_load_dwordx4 v[68:71], v11, s[16:19], 0 offen\n\t"
        "v_mfma_f32_4x4x4_16b_bf16 v[32:35], v[12:13], v[80:81], v[32:35] cbsz:1 abid:0\n\t"
        "v_mfma_f32_4x4x4_16b_bf16 v[36:39], v[12:13], v[82:83], v[36:39] cbsz:1 abid:0\n\t"
        "ds_read2_b32 v[44:45], v63 offset0:4 offset1:5\n\t"
        "v_mfma_f32_4x4x4_16b_bf16 v[32:35], v[12:13], v[84:85], v[32:35] cbsz:1 abid:1\n\t"
        "v_mfma_f32_4x4x4_16b_bf16 v[36:39], v[12:13], v[86:87], v[36:39] cbsz:1 abid:1\n\t"
        "s_sub_u32 s59, s59, 1\n\t"
        "s_cbranch_scc1 .L7_i106\n"
        ".L7_j107:\n\t"
        "v_perm_b32 v12, v47, v46, v8\n\t"
        "v_perm_b32 v13, v47, v46, v9\n\t"
        "s_waitcnt vmcnt(10) lgkmcnt(1)\n\t"
        "v_mad_u32_u16 v10, v42, s48, v62\n\t"
        "v_mad_u32_u16 v11, v43, s48, v62\n\t"
        "buffer_load_dwordx4 v[72:75], v10, s[16:19], 0 offen\n\t"
        "buffer_load_dwordx4 v[76:79], v11, s[16:19], 0 offen\n\t"
        "v_mfma_f32_4x4x4_16b_bf16 v[32:35], v[12:13], v[88:89], v[32:35] cbsz:1 abid:0\n\t"
        "v_mfma_f32_4x4x4_16b_bf16 v[36:39], v[12:13], v[90:91], v[36:39] cbsz:1 abid:0\n\t"
        "ds_read2_b32 v[46:47], v63 offset0:6 offset1:7\n\t"
        "v_mfma_f32_4x4x4_16b_bf16 v[32:35], v[12:13], v[92:93], v[32:35] cbsz:1 abid:1\n\t"
        "v_mfma_f32_4x4x4_16b_bf16 v[36:39], v[12:13], v[94:95], v[36:39] cbsz:1 abid:1\n\t"
        "s_sub_u32 s59, s59, 1\n\t"
        "s_cbranch_scc1 .L7_i108\n"
        ".L7_j109:\n\t"
        "v_perm_b32 v12, v49, v48, v8\n\t"
        "v_perm_b32 v13, v49, v48, v9\n\t"
        "s_waitcnt vmcnt(10) lgkmcnt(1)\n\t"
        "v_mad_u32_u16 v10, v44, s48, v62\n\t"
        "v_mad_u32_u16 v11, v45, s48, v62\n\t"
        "buffer_load_dwordx4 v[80:83], v10, s[16:19], 0 offen\n\t"
        "buffer_load_dwordx4 v[84:87], v11, s[16:19], 0 offen\n\t"
        "v_mfma_f32_4x4x4_16b_bf16 v[32:35], v[12:13], v[96:97], v[32:35] cbsz:1 abid:0\n\t"
        "v_mfma_f32_4x4x4_16b_bf16 v[36:39], v[12:13], v[98:99], v[36:39] cbsz:1 abid:0\n\t"
        "ds_read2_b32 v[48:49], v63 offset0:8 offset1:9\n\t"
        "v_mfma_f32_4x4x4_16b_bf16 v[32:35], v[12:13], v[100:101], v[32:35] cbsz:1 abid:1\n\t"
        "v_mfma_f32_4x4x4_16b_bf16 v[36:39], v[12:13], v[102:103], v[36:39] cbsz:1 abid:1\n\t"
        "s_sub_u32 s59, s59, 1\n\t"
        "s_cbranch_scc1 .L7_i110\n"
        ".L7_j111:\n\t"
        "v_perm_b32 v12, v51, v50, v8\n\t"
        "v_perm_b32 v13, v51, v50, v9\n\t"
        "s_waitcnt vmcnt(10) lgkmcnt(1)\n\t"
        "v_mad_u32_u16 v10, v46, s48, v62\n\t"
        "v_mad_u32_u16 v11, v47, s48, v62\n\t"
        "buffer_load_dwordx4 v[88:91], v10, s[16:19], 0 offen\n\t"
        "buffer_load_dwordx4 v[92:95], v11, s[16:19], 0 offen\n\t"
        "v_mfma_f32_4x4x4_16b_bf16 v[32:35], v[12:13], v[104:105], v[32:35] cbsz:1 abid:0\n\t"
        "v_mfma_f32_4x4x4_16b_bf16 v[36:39], v[12:13], v[106:107], v[36:39] cbsz:1 abid:0\n\t"
        "ds_read2_b32 v[50:51], v63 offset0:10 offset1:11\n\t"
        "v_mfma_f32_4x4x4_16b_bf16 v[32:35], v[12:13], v[108:109], v[32:35] cbsz:1 abid:1\n\t"
        "v_mfma_f32_4x4x4_16b_bf16 v[36:39], v[12:13], v[110:111], v[36:39] cbsz:1 abid:1\n\t"
        "s_sub_u32 s59, s59, 1\n\t"
        "s_cbranch_scc1 .L7_i112\n"
        ".L7_j113:\n\t"
        "v_perm_b32 v12, v53, v52, v8\n\t"
        "v_perm_b32 v13, v53, v52, v9\n\t"
        "s_waitcnt vmcnt(10) lgkmcnt(1)\n\t"
        "v_mad_u32_u16 v10, v48, s48, v62\n\t"
        "v_mad_u32_u16 v11, v49, s48, v62\n\t"
        "buffer_load_dwordx4 v[96:99], v10, s[16:19], 0 offen\n\t"
        "buffer_load_dwordx4 v[100:103], v11, s[16:19], 0 offen\n\t"
        "v_mfma_f32_4x4x4_16b_bf16 v[32:35], v[12:13], v[112:113], v[32:35] cbsz:1 abid:0\n\t"
        "v_mfma_f32_4x4x4_16b_bf16 v[36:39], v[12:13], v[114:115], v[36:39] cbsz:1 abid:0\n\t"
        "ds_read2_b32 v[52:53], v63 offset0:12 offset1:13\n\t"
        "v_mfma_f32_4x4x4_16b_bf16 v[32:35], v[12:13], v[116:117], v[32:35] cbsz:1 abid:1\n\t"
        "v_mfma_f32_4x4x4_16b_bf16 v[36:39], v[12:13], v[118:119], v[36:39] cbsz:1 abid:1\n\t"
        "s_sub_u32 s59, s59, 1\n\t"
        "s_cbranch_scc1 .L7_i114\n"
        ".L7_j115:\n\t"
        "v_perm_b32 v12, v55, v54, v8\n\t"
        "v_perm_b32 v13, v55, v54, v9\n\t"
        "s_waitcnt vmcnt(10) lgkmcnt(1)\n\t"
        "v_mad_u32_u16 v10, v50, s48, v62\n\t"
        "v_mad_u32_u16 v11, v51, s48, v62\n\t"
        "buffer_load_dwordx4 v[104:107], v10, s[16:19], 0 offen\n\t"
        "buffer_load_dwordx4 v[108:111], v11, s[16:19], 0 offen\n\t"
        "v_mfma_f32_4x4x4_16b_bf16 v[32:35], v[12:13], v[120:121], v[32:35] cbsz:1 abid:0\n\t"
        "v_mfma_f32_4x4x4_16b_bf16 v[36:39], v[12:13], v[122:123], v[36:39] cbsz:1 abid:0\n\t"
        "ds_read2_b32 v[54:55], v63 offset0:14 offset1:15\n\t"
        "v_mfma_f32_4x4x4_16b_bf16 v[32:35], v[12:13], v[124:125], v[32:35] cbsz:1 abid:1\n\t"
        "v_mfma_f32_4x4x4_16b_bf16 v[36:39], v[12:13], v[126:127], v[36:39] cbsz:1 abid:1\n\t"
        "s_sub_u32 s59, s59, 1\n\t"
        "s_cbranch_scc0 .L7_loop\n\t"
        "s_branch .L7_i116\n"
        ".L7_i86:\n\t"
        "s_mov_b32 s60, 8\n\t"
        "s_swappc_b64 s[54:55], s[50:51]\n\t"
        "s_branch .L7_j87\n"
        ".L7_i88:\n\t"
        "s_mov_b32 s60, 16\n\t"
        "s_swappc_b64 s[54:55], s[50:51]\n\t"
        "s_branch .L7_j89\n"
        ".L7_i90:\n\t"
        "s_mov_b32 s60, 24\n\t"
        "s_swappc_b64 s[54:55], s[50:51]\n\t"
        "s_branch .L7_j91\n"
        ".L7_i92:\n\t"
        "s_mov_b32 s60, 32\n\t"
        "s_swappc_b64 s[54:55], s[50:51]\n\t"
        "s_branch .L7_j93\n"
        ".L7_i94:\n\t"
        "s_mov_b32 s60, 40\n\t"
        "s_swappc_b64 s[54:55], s[50:51]\n\t"
        "s_branch .L7_j95\n"
        ".L7_i96:\n\t"
        "s_mov_b32 s60, 48\n\t"
        "s_swappc_b64 s[54:55], s[50:51]\n\t"
        "s_branch .L7_j97\n"
        ".L7_i98:\n\t"
        "s_mov_b32 s60, 56\n\t"
        "s_swappc_b64 s[54:55], s[50:51]\n\t"
        "s_branch .L7_j99\n"
        ".L7_i100:\n\t"
        "s_mov_b32 s60, 64\n\t"
        "s_swappc_b64 s[54:55], s[50:51]\n\t"
        "s_branch .L7_j101\n"
        ".L7_i102:\n\t"
        "s_mov_b32 s60, 8\n\t"
        "s_swappc_b64 s[62:63], s[42:43]\n\t"
        "s_cbranch_vccnz .L7_dt1\n\t"
        "s_branch .L7_j103\n"
        ".L7_i104:\n\t"
        "s_mov_b32 s60, 16\n\t"
        "s_swappc_b64 s[62:63], s[42:43]\n\t"
        "s_cbranch_vccnz .L7_dt2\n\t"
        "s_branch .L7_j105\n"
        ".L7_i106:\n\t"
        "s_mov_b32 s60, 24\n\t"
        "s_swappc_b64 s[62:63], s[42:43]\n\t"
        "s_cbranch_vccnz .L7_dt3\n\t"
        "s_branch .L7_j107\n"
        ".L7_i108:\n\t"
        "s_mov_b32 s60, 32\n\t"
        "s_swappc_b64 s[62:63], s[42:43]\n\t"
        "s_cbranch_vccnz .L7_dt4\n\t"
        "s_branch .L7_j109\n"
        ".L7_i110:\n\t"
        "s_mov_b32 s60, 40\n\t"
        "s_swappc_b64 s[62:63], s[42:43]\n\t"
        "s_cbranch_vccnz .L7_dt5\n\t"
        "s_branch .L7_j111\n"
        ".L7_i112:\n\t"
        "s_mov_b32 s60, 48\n\t"
        "s_swappc_b64 s[62:63], s[42:43]\n\t"
        "s_cbranch_vccnz .L7_dt6\n\t"
        "s_branch .L7_j113\n"
        ".L7_i114:\n\t"
        "s_mov_b32 s60, 56\n\t"
        "s_swappc_b64 s[62:63], s[42:43]\n\t"
        "s_cbranch_vccnz .L7_dt7\n\t"
        "s_branch .L7_j115\n"
        ".L7_i116:\n\t"
        "s_mov_b32 s60, 64\n\t"
        "s_swappc_b64 s[62:63], s[42:43]\n\t"
        "s_cbranch_vccnz .L7_dt0\n\t"
        "s_branch .L7_loop\n"
        ".L7_ev:\n\t"
        "s_sub_u32 s56, s56, s58\n\t"
        "s_cbranch_scc0 .L7_e1\n\t"
        "s_swappc_b64 s[54:55], s[50:51]\n"
        ".L7_e1:\n\t"
        "s_sub_u32 s57, s57, s58\n\t"
        "s_cbranch_scc0 .L7_e2\n\t"
        "s_swappc_b64 s[54:55], s[52:53]\n"
        ".L7_e2:\n\t"
        "s_min_u32 s59, s56, s57\n\t"
        "s_add_u32 s58, s59, 1\n\t"
        "s_setpc_b64 s[62:63]\n"
        ".L7_dt0:\n\t"
        "v_perm_b32 v12, v41, v40, v8\n\t"
        "v_perm_b32 v13, v41, v40, v9\n\t"
        "s_waitcnt vmcnt(6)\n\t"
        "s_cmp_lt_u32 s57, 3\n\t"
        "s_cbranch_scc0 .L7_du0\n\t"
        "s_waitcnt vmcnt(2)\n\t"
        "s_cmp_eq_u32 s57, 0\n\t"
        "s_cbranch_scc0 .L7_du0\n\t"
        "s_waitcnt vmcnt(0)\n"
        ".L7_du0:\n\t"
        "v_mfma_f32_4x4x4_16b_bf16 v[32:35], v[12:13], v[64:65], v[32:35] cbsz:1 abid:0\n\t"
        "v_mfma_f32_4x4x4_16b_bf16 v[36:39], v[12:13], v[66:67], v[36:39] cbsz:1 abid:0\n\t"
        "s_sub_u32 s57, s57, 1\n\t"
        "v_mfma_f32_4x4x4_16b_bf16 v[32:35], v[12:13], v[68:69], v[32:35] cbsz:1 abid:1\n\t"
        "v_mfma_f32_4x4x4_16b_bf16 v[36:39], v[12:13], v[70:71], v[36:39] cbsz:1 abid:1\n\t"
        "s_cbranch_scc1 .L7_last\n"
        ".L7_dt1:\n\t"
        "v_perm_b32 v12, v43, v42, v8\n\t"
        "v_perm_b32 v13, v43, v42, v9\n\t"
        "s_waitcnt vmcnt(6)\n\t"
        "s_cmp_lt_u32 s57, 3\n\t"
        "s_cbranch_scc0 .L7_du1\n\t"
        "s_waitcnt vmcnt(2)\n\t"
        "s_cmp_eq_u32 s57, 0\n\t"
        "s_cbranch_scc0 .L7_du1\n\t"
        "s_waitcnt vmcnt(0)\n"
        ".L7_du1:\n\t"
        "v_mfma_f32_4x4x4_16b_bf16 v[32:35], v[12:13], v[72:73], v[32:35] cbsz:1 abid:0\n\t"
        "v_mfma_f32_4x4x4_16b_bf16 v[36:39], v[12:13], v[74:75], v[36:39] cbsz:1 abid:0\n\t"
        "s_sub_u32 s57, s57, 1\n\t"
        "v_mfma_f32_4x4x4_16b_bf16 v[32:35], v[12:13], v[76:77], v[32:35] cbsz:1 abid:1\n\t"
        "v_mfma_f32_4x4x4_16b_bf16 v[36:39], v[12:13], v[78:79], v[36:39] cbsz:1 abid:1\n\t"
        "s_cbranch_scc1 .L7_last\n"
        ".L7_dt2:\n\t"
        "v_perm_b32 v12, v45, v44, v8\n\t"
        "v_perm_b32 v13, v45, v44, v9\n\t"
        "s_waitcnt vmcnt(6)\n\t"
        "s_cmp_lt_u32 s57, 3\n\t"
        "s_cbranch_scc0 .L7_du2\n\t"
        "s_waitcnt vmcnt(2)\n\t"
        "s_cmp_eq_u32 s57, 0\n\t"
        "s_cbranch_scc0 .L7_du2\n\t"
        "s_waitcnt vmcnt(0)\n"
        ".L7_du2:\n\t"
        "v_mfma_f32_4x4x4_16b_bf16 v[32:35], v[12:13], v[80:81], v[32:35] cbsz:1 abid:0\n\t"
        "v_mfma_f32_4x4x4_16b_bf16 v[36:39], v[12:13], v[82:83], v[36:39] cbsz:1 abid:0\n\t"
        "s_sub_u32 s57, s57, 1\n\t"
        "v_mfma_f32_4x4x4_16b_bf16 v[32:35], v[12:13], v[84:85], v[32:35] cbsz:1 abid:1\n\t"
        "v_mfma_f32_4x4x4_16b_bf16 v[36:39], v[12:13], v[86:87], v[36:39] cbsz:1 abid:1\n\t"
        "s_cbranch_scc1 .L7_last\n"
        ".L7_dt3:\n\t"
        "v_perm_b32 v12, v47, v46, v8\n\t"
        "v_perm_b32 v13, v47, v46, v9\n\t"
        "s_waitcnt vmcnt(6)\n\t"
        "s_cmp_lt_u32 s57, 3\n\t"
        "s_cbranch_scc0 .L7_du3\n\t"
        "s_waitcnt vmcnt(2)\n\t"
        "s_cmp_eq_u32 s57, 0\n\t"
        "s_cbranch_scc0 .L7_du3\n\t"
        "s_waitcnt vmcnt(0)\n"
        ".L7_du3:\n\t"
        "v_mfma_f32_4x4x4_16b_bf16 v[32:35], v[12:13], v[88:89], v[32:35] cbsz:1 abid:0\n\t"
        "v_mfma_f32_4x4x4_16b_bf16 v[36:39], v[12:13], v[90:91], v[36:39] cbsz:1 abid:0\n\t"
        "s_sub_u32 s57, s57, 1\n\t"
        "v_mfma_f32_4x4x4_16b_bf16 v[32:35], v[12:13], v[92:93], v[32:35] cbsz:1 abid:1\n\t"
        "v_mfma_f32_4x4x4_16b_bf16 v[36:39], v[12:13], v[94:95], v[36:39] cbsz:1 abid:1\n\t"
        "s_cbranch_scc1 .L7_last\n"
        ".L7_dt4:\n\t"
        "v_perm_b32 v12, v49, v48, v8\n\t"
        "v_perm_b32 v13, v49, v48, v9\n\t"
        "s_waitcnt vmcnt(6)\n\t"
        "s_cmp_lt_u32 s57, 3\n\t"
        "s_cbranch_scc0 .L7_du4\n\t"
        "s_waitcnt vmcnt(2)\n\t"
        "s_cmp_eq_u32 s57, 0\n\t"
        "s_cbranch_scc0 .L7_du4\n\t"
        "s_waitcnt vmcnt(0)\n"
        ".L7_du4:\n\t"
        "v_mfma_f32_4x4x4_16b_bf16 v[32:35], v[12:13], v[96:97], v[32:35] cbsz:1 abid:0\n\t"
        "v_mfma_f32_4x4x4_16b_bf16 v[36:39], v[12:13], v[98:99], v[36:39] cbsz:1 abid:0\n\t"
        "s_sub_u32 s57, s57, 1\n\t"
        "v_mfma_f32_4x4x4_16b_bf16 v[32:35], v[12:13], v[100:101], v[32:35] cbsz:1 abid:1\n\t"
        "v_mfma_f32_4x4x4_16b_bf16 v[36:39], v[12:13], v[102:103], v[36:39] cbsz:1 abid:1\n\t"
        "s_cbranch_scc1 .L7_last\n"
        ".L7_dt5:\n\t"
        "v_perm_b32 v12, v51, v50, v8\n\t"
        "v_perm_b32 v13, v51, v50, v9\n\t"
        "s_waitcnt vmcnt(6)\n\t"
        "s_cmp_lt_u32 s57, 3\n\t"
        "s_cbranch_scc0 .L7_du5\n\t"
        "s_waitcnt vmcnt(2)\n\t"
        "s_cmp_eq_u32 s57, 0\n\t"
        "s_cbranch_scc0 .L7_du5\n\t"
        "s_waitcnt vmcnt(0)\n"
        ".L7_du5:\n\t"
        "v_mfma_f32_4x4x4_16b_bf16 v[32:35], v[12:13], v[104:105], v[32:35] cbsz:1 abid:0\n\t"
        "v_mfma_f32_4x4x4_16b_bf16 v[36:39], v[12:13], v[106:107], v[36:39] cbsz:1 abid:0\n\t"
        "s_sub_u32 s57, s57, 1\n\t"
        "v_mfma_f32_4x4x4_16b_bf16 v[32:35], v[12:13], v[108:109], v[32:35] cbsz:1 abid:1\n\t"
        "v_mfma_f32_4x4x4_16b_bf16 v[36:39], v[12:13], v[110:111], v[36:39] cbsz:1 abid:1\n\t"
        "s_cbranch_scc1 .L7_last\n"
        ".L7_dt6:\n\t"
        "v_perm_b32 v12, v53, v52, v8\n\t"
        "v_perm_b32 v13, v53, v52, v9\n\t"
        "s_waitcnt vmcnt(6)\n\t"
        "s_cmp_lt_u32 s57, 3\n\t"
        "s_cbranch_scc0 .L7_du6\n\t"
        "s_waitcnt vmcnt(2)\n\t"
        "s_cmp_eq_u32 s57, 0\n\t"
        "s_cbranch_scc0 .L7_du6\n\t"
        "s_waitcnt vmcnt(0)\n"
        ".L7_du6:\n\t"
        "v_mfma_f32_4x4x4_16b_bf16 v[32:35], v[12:13], v[112:113], v[32:35] cbsz:1 abid:0\n\t"
        "v_mfma_f32_4x4x4_16b_bf16 v[36:39], v[12:13], v[114:115], v[36:39] cbsz:1 abid:0\n\t"
        "s_sub_u32 s57, s57, 1\n\t"
        "v_mfma_f32_4x4x4_16b_bf16 v[32:35], v[12:13], v[116:117], v[32:35] cbsz:1 abid:1\n\t"
        "v_mfma_f32_4x4x4_16b_bf16 v[36:39], v[12:13], v[118:119], v[36:39] cbsz:1 abid:1\n\t"
        "s_cbranch_scc1 .L7_last\n"
        ".L7_dt7:\n\t"
        "v_perm_b32 v12, v55, v54, v8\n\t"
        "v_perm_b32 v13, v55, v54, v9\n\t"
        "s_waitcnt vmcnt(6)\n\t"
        "s_cmp_lt_u32 s57, 3\n\t"
        "s_cbranch_scc0 .L7_du7\n\t"
        "s_waitcnt vmcnt(2)\n\t"
        "s_cmp_eq_u32 s57, 0\n\t"
        "s_cbranch_scc0 .L7_du7\n\t"
        "s_waitcnt vmcnt(0)\n"
        ".L7_du7:\n\t"
        "v_mfma_f32_4x4x4_16b_bf16 v[32:35], v[12:13], v[120:121], v[32:35] cbsz:1 abid:0\n\t"
        "v_mfma_f32_4x4x4_16b_bf16 v[36:39], v[12:13], v[122:123], v[36:39] cbsz:1 abid:0\n\t"
        "s_sub_u32 s57, s57, 1\n\t"
        "v_mfma_f32_4x4x4_16b_bf16 v[32:35], v[12:13], v[124:125], v[32:35] cbsz:1 abid:1\n\t"
        "v_mfma_f32_4x4x4_16b_bf16 v[36:39], v[12:13], v[126:127], v[36:39] cbsz:1 abid:1\n\t"
        "s_cbranch_scc1 .L7_last\n\t"
        "s_branch .L7_dt0\n"
        ".L7_ie:\n\t"
        "v_dot4_u32_u8 v63, v25, s38, v7\n\t"
        "s_nop 2\n\t"
        "v_subrev_u32 v63, s60, v63\n\t"
        "s_mov_b32 s56, s25\n\t"
        "s_swappc_b64 s[50:51], s[54:55]\n\t"
        "v_dot4_u32_u8 v63, v26, s38, v7\n\t"
        "s_nop 2\n\t"
        "v_subrev_u32 v63, s60, v63\n\t"
        "s_mov_b32 s56, s26\n\t"
        "s_swappc_b64 s[50:51], s[54:55]\n\t"
        "v_dot4_u32_u8 v63, v27, s38, v7\n\t"
        "s_nop 2\n\t"
        "v_subrev_u32 v63, s60, v63\n\t"
        "s_mov_b32 s56, s27\n\t"
        "s_swappc_b64 s[50:51], s[54:55]\n\t"
        "v_dot4_u32_u8 v63, v28, s38, v7\n\t"
        "s_nop 2\n\t"
        "v_subrev_u32 v63, s60, v63\n\t"
        "s_add_u32 s56, s28, 2\n\t"
        "s_swappc_b64 s[50:51], s[54:55]\n\t"
        "s_mov_b64 vcc, -1\n\t"
        "s_setpc_b64 s[54:55]\n"
        ".L7_ce:\n\t"
        "v_and_b32 v4, 0xffff0000, v24\n\t"
        "s_nop 2\n\t"
        "v_pk_mul_f32 v[32:33], v[32:33], v[4:5] op_sel_hi:[1,0]\n\t"
        "v_pk_mul_f32 v[34:35], v[34:35], v[4:5] op_sel_hi:[1,0]\n\t"
        "v_pk_mul_f32 v[36:37], v[36:37], v[4:5] op_sel_hi:[1,0]\n\t"
        "v_pk_mul_f32 v[38:39], v[38:39], v[4:5] op_sel_hi:[1,0]\n\t"
        "v_cvt_pk_bf16_f32 v12, v32, v33\n\t"
        "v_cvt_pk_bf16_f32 v13, v34, v35\n\t"
        "v_cvt_pk_bf16_f32 v14, v36, v37\n\t"
        "v_cvt_pk_bf16_f32 v15, v38, v39\n"
        ".L7_w117:\n\t"
        "ds_read_b32 v4, v2 offset:12\n\t"
        "s_waitcnt lgkmcnt(0)\n\t"
        "v_cmp_gt_u16 vcc, 4, v4\n\t"
        "s_cbranch_vccz .L7_d118\n\t"
        "s_sleep 1\n\t"
        "s_sub_i32 s83, s83, 1\n\t"
        "s_cmp_gt_i32 s83, 0\n\t"
        "s_cbranch_scc1 .L7_w117\n"
        ".L7_d118:\n\t"
        "s_mov_b64 vcc, 0\n\t"
        "ds_write_b128 v56, v[12:15] offset:23808\n\t"
        "s_mov_b64 exec, 1\n\t"
        "ds_add_u32 v2, v3 offset:52\n\t"
        "s_mov_b64 exec, -1\n\t"
        "v_mov_b64 v[32:33], 0\n\t"
        "v_mov_b64 v[34:35], 0\n\t"
        "v_mov_b64 v[36:37], 0\n\t"
        "v_mov_b64 v[38:39], 0\n\t"
        "s_mov_b32 s57, s25\n\t"
        "s_swappc_b64 s[52:53], s[54:55]\n\t"
        "v_and_b32 v4, 0xffff0000, v25\n\t"
        "s_nop 2\n\t"
        "v_pk_mul_f32 v[32:33], v[32:33], v[4:5] op_sel_hi:[1,0]\n\t"
        "v_pk_mul_f32 v[34:35], v[34:35], v[4:5] op_sel_hi:[1,0]\n\t"
        "v_pk_mul_f32 v[36:37], v[36:37], v[4:5] op_sel_hi:[1,0]\n\t"
        "v_pk_mul_f32 v[38:39], v[38:39], v[4:5] op_sel_hi:[1,0]\n\t"
        "v_cvt_pk_bf16_f32 v12, v32, v33\n\t"
        "v_cvt_pk_bf16_f32 v13, v34, v35\n\t"
        "v_cvt_pk_bf16_f32 v14, v36, v37\n\t"
        "v_cvt_pk_bf16_f32 v15, v38, v39\n\t"
        "ds_write_b128 v57, v[12:15] offset:23808\n\t"
        "s_mov_b64 exec, 1\n\t"
        "ds_add_u32 v61, v3 offset:52\n\t"
        "s_mov_b64 exec, -1\n\t"
        "v_mov_b64 v[32:33], 0\n\t"
        "v_mov_b64 v[34:35], 0\n\t"
        "v_mov_b64 v[36:37], 0\n\t"
        "v_mov_b64 v[38:39], 0\n\t"
        "s_mov_b32 s57, s26\n\t"
        "s_swappc_b64 s[52:53], s[54:55]\n\t"
        "v_and_b32 v4, 0xffff0000, v26\n\t"
        "s_nop 2\n\t"
        "v_pk_mul_f32 v[32:33], v[32:33], v[4:5] op_sel_hi:[1,0]\n\t"
        "v_pk_mul_f32 v[34:35], v[34:35], v[4:5] op_sel_hi:[1,0]\n\t"
        "v_pk_mul_f32 v[36:37], v[36:37], v[4:5] op_sel_hi:[1,0]\n\t"
        "v_pk_mul_f32 v[38:39], v[38:39], v[4:5] op_sel_hi:[1,0]\n\t"
        "v_cvt_pk_bf16_f32 v12, v32, v33\n\t"
        "v_cvt_pk_bf16_f32 v13, v34, v35\n\t"
        "v_cvt_pk_bf16_f32 v14, v36, v37\n\t"
        "v_cvt_pk_bf16_f32 v15, v38, v39\n\t"
        "ds_write_b128 v58, v[12:15] offset:23808\n\t"
        "s_mov_b64 exec, 1\n\t"
        "ds_add_u32 v61, v3 offset:56\n\t"
        "s_mov_b64 exec, -1\n\t"
        "v_mov_b64 v[32:33], 0\n\t"
        "v_mov_b64 v[34:35], 0\n\t"
        "v_mov_b64 v[36:37], 0\n\t"
        "v_mov_b64 v[38:39], 0\n\t"
        "s_mov_b32 s57, s27\n\t"
        "s_swappc_b64 s[52:53], s[54:55]\n\t"
        "v_and_b32 v4, 0xffff0000, v27\n\t"
        "s_nop 2\n\t"
        "v_pk_mul_f32 v[32:33], v[32:33], v[4:5] op_sel_hi:[1,0]\n\t"
        "v_pk_mul_f32 v[34:35], v[34:35], v[4:5] op_sel_hi:[1,0]\n\t"
        "v_pk_mul_f32 v[36:37], v[36:37], v[4:5] op_sel_hi:[1,0]\n\t"
        "v_pk_mul_f32 v[38:39], v[38:39], v[4:5] op_sel_hi:[1,0]\n\t"
        "v_cvt_pk_bf16_f32 v12, v32, v33\n\t"
        "v_cvt_pk_bf16_f32 v13, v34, v35\n\t"
        "v_cvt_pk_bf16_f32 v14, v36, v37\n\t"
        "v_cvt_pk_bf16_f32 v15, v38, v39\n\t"
        "ds_write_b128 v59, v[12:15] offset:21760\n\t"
        "s_mov_b64 exec, 1\n\t"
        "ds_add_u32 v2, v3 offset:64\n\t"
        "s_mov_b64 exec, -1\n\t"
        "v_mov_b64 v[32:33], 0\n\t"
        "v_mov_b64 v[34:35], 0\n\t"
        "v_mov_b64 v[36:37], 0\n\t"
        "v_mov_b64 v[38:39], 0\n\t"
        "s_mov_b32 s57, s28\n\t"
        "s_swappc_b64 s[52:53], s[54:55]\n"
        ".L7_last:\n\t"
        "v_and_b32 v4, 0xffff0000, v28\n\t"
        "s_nop 2\n\t"
        "v_pk_mul_f32 v[32:33], v[32:33], v[4:5] op_sel_hi:[1,0]\n\t"
        "v_pk_mul_f32 v[34:35], v[34:35], v[4:5] op_sel_hi:[1,0]\n\t"
        "v_pk_mul_f32 v[36:37], v[36:37], v[4:5] op_sel_hi:[1,0]\n\t"
        "v_pk_mul_f32 v[38:39], v[38:39], v[4:5] op_sel_hi:[1,0]\n\t"
        "v_cvt_pk_bf16_f32 v12, v32, v33\n\t"
        "v_cvt_pk_bf16_f32 v13, v34, v35\n\t"
        "v_cvt_pk_bf16_f32 v14, v36, v37\n\t"
        "v_cvt_pk_bf16_f32 v15, v38, v39\n\t"
        "ds_write_b128 v60, v[12:15] offset:21760\n\t"
        "s_mov_b64 exec, 1\n\t"
        "ds_add_u32 v2, v3 offset:68\n\t"
        "s_mov_b64 exec, -1\n\t"
        "s_endpgm\n"
        ".L7_x:\n\t"
        "s_mov_b32 s36, 1\n\t"
        "s_waitcnt lgkmcnt(0)\n\t"
        "v_dot4_u32_u8 v83, v67, s39, v7\n\t"
        "v_lshlrev_b32_sdwa v115, 1, v67 dst_sel:DWORD dst_unused:UNUSED_PAD src0_sel:DWORD src1_sel:WORD_0\n\t"
        "ds_read_u16 v115, v115 offset:46336\n\t"
        "v_cmp_eq_u16 vcc, -1, v67\n\t"
        "s_cmp_lg_u64 vcc, 0\n\t"
        "s_cselect_b32 s78, 0x10000, 0\n\t"
        "v_or_b32 v30, s78, v30\n\t"
        "ds_add_rtn_u32 v99, v83, v29 offset:192\n\t"
        "v_dot4_u32_u8 v87, v71, s39, v7\n\t"
        "v_lshlrev_b32_sdwa v119, 1, v71 dst_sel:DWORD dst_unused:UNUSED_PAD src0_sel:DWORD src1_sel:WORD_0\n\t"
        "ds_read_u16 v119, v119 offset:46336\n\t"
        "v_cmp_eq_u16 vcc, -1, v71\n\t"
        "s_cmp_lg_u64 vcc, 0\n\t"
        "s_cselect_b32 s78, 0x10000, 0\n\t"
        "v_or_b32 v30, s78, v30\n\t"
        "ds_add_rtn_u32 v103, v87, v29 offset:192\n\t"
        "v_dot4_u32_u8 v91, v75, s39, v7\n\t"
        "v_lshlrev_b32_sdwa v123, 1, v75 dst_sel:DWORD dst_unused:UNUSED_PAD src0_sel:DWORD src1_sel:WORD_0\n\t"
        "ds_read_u16 v123, v123 offset:46336\n\t"
        "v_cmp_eq_u16 vcc, -1, v75\n\t"
        "s_cmp_lg_u64 vcc, 0\n\t"
        "s_cselect_b32 s78, 0x10000, 0\n\t"
        "v_or_b32 v30, s78, v30\n\t"
        "ds_add_rtn_u32 v107, v91, v29 offset:192\n\t"
        "v_dot4_u32_u8 v95, v79, s39, v7\n\t"
        "v_lshlrev_b32_sdwa v127, 1, v79 dst_sel:DWORD dst_unused:UNUSED_PAD src0_sel:DWORD src1_sel:WORD_0\n\t"
        "ds_read_u16 v127, v127 offset:46336\n\t"
        "v_cmp_eq_u16 vcc, -1, v79\n\t"
        "s_cmp_lg_u64 vcc, 0\n\t"
        "s_cselect_b32 s78, 0x10000, 0\n\t"
        "v_or_b32 v30, s78, v30\n\t"
        "ds_add_rtn_u32 v111, v95, v29 offset:192\n\t"
        "s_branch .L7_y\n"
        ".L7_z:\n\t"
        "s_waitcnt lgkmcnt(0)\n\t"
        "ds_read_b32 v32, v83 offset:192\n\t"
        "ds_read_b32 v33, v87 offset:192\n\t"
        "ds_read_b32 v34, v91 offset:192\n\t"
        "ds_read_b32 v35, v95 offset:192\n\t"
        "v_bfe_u32 v99, v99, s33, 8\n\t"
        "v_perm_b32 v115, v115, v67, s49\n\t"
        "v_bfe_u32 v103, v103, s33, 8\n\t"
        "v_perm_b32 v119, v119, v71, s49\n\t"
        "v_bfe_u32 v107, v107, s33, 8\n\t"
        "v_perm_b32 v123, v123, v75, s49\n\t"
        "v_bfe_u32 v111, v111, s33, 8\n\t"
        "v_perm_b32 v127, v127, v79, s49\n\t"
        "s_waitcnt lgkmcnt(0)\n\t"
        "v_dot4_u32_u8 v99, v32, s32, v99\n\t"
        "v_dot4_u32_u8 v103, v33, s32, v103\n\t"
        "v_dot4_u32_u8 v107, v34, s32, v107\n\t"
        "v_dot4_u32_u8 v111, v35, s32, v111\n\t"
        "v_min_u32 v99, 47, v99\n\t"
        "v_min_u32 v103, 47, v103\n\t"
        "v_min_u32 v107, 47, v107\n\t"
        "v_min_u32 v111, 47, v111\n\t"
        "v_lshl_add_u32 v99, v99, 2, v83\n\t"
        "ds_write_b32 v99, v115\n\t"
        "v_lshl_add_u32 v103, v103, 2, v87\n\t"
        "ds_write_b32 v103, v119\n\t"
        "v_lshl_add_u32 v107, v107, 2, v91\n\t"
        "ds_write_b32 v107, v123\n\t"
        "v_lshl_add_u32 v111, v111, 2, v95\n\t"
        "ds_write_b32 v111, v127\n\t"
        "s_branch .L7_zr\n"
        ".L7_exit4:\n\t"
        "s_add_u32 s69, s69, 4\n"
        ".L7_exit:\n\t"
        "s_waitcnt vmcnt(0) lgkmcnt(0)\n\t"
        "s_setprio 0\n\t"
        : "+{v0}"(tid), "=&{s69}"(wv)
        : "s"(__builtin_amdgcn_kernarg_segment_ptr()), "s"(blockIdx.x)
        : "scc", "vcc", "memory", "s16", "s17", "s18", "s19", "s20", "s21", "s22", "s23", "s24", "s25", "s26", "s27", "s28", "s29", "s30", "s31", "s32", "s33", "s34", "s35",
          "s36", "s37", "s38", "s39", "s40", "s41", "s42", "s43", "s44", "s45", "s46", "s47", "s48", "s49", "s50", "s51", "s52", "s53", "s54", "s55",
          "s56", "s57", "s58", "s59", "s60", "s61", "s62", "s63", "s64", "s65", "s66", "s67", "s68", "s70", "s71", "s72", "s73", "s74", "s75", "s76",
          "s77", "s78", "s79", "s80", "s81", "s82", "s83", "s84", "s85", "s86", "s87", "s88", "s89", "s90", "s91", "s92", "s93", "s94", "s95", "s96",
          "s97", "s98", "s99", "s100", "s101", "v1", "v2", "v3", "v4", "v5", "v6", "v7", "v8", "v9", "v10", "v11", "v12", "v13", "v14", "v15",
          "v16", "v17", "v18", "v19", "v20", "v21", "v22", "v23", "v24", "v25", "v26", "v27", "v28", "v29", "v30", "v31", "v32", "v33", "v34", "v35",
          "v36", "v37", "v38", "v39", "v40", "v41", "v42", "v43", "v44", "v45", "v46", "v47", "v48", "v49", "v50", "v51", "v52", "v53", "v54", "v55",
          "v56", "v57", "v58", "v59", "v60", "v61", "v62", "v63", "v64", "v65", "v66", "v67", "v68", "v69", "v70", "v71", "v72", "v73", "v74", "v75",
          "v76", "v77", "v78", "v79", "v80", "v81", "v82", "v83", "v84", "v85", "v86", "v87", "v88", "v89", "v90", "v91", "v92", "v93", "v94", "v95",
          "v96", "v97", "v98", "v99", "v100", "v101", "v102", "v103", "v104", "v105", "v106", "v107", "v108", "v109", "v110", "v111", "v112", "v113", "v114", "v115",
          "v116", "v117", "v118", "v119", "v120", "v121", "v122", "v123", "v124", "v125", "v126", "v127");
    const __attribute__((address_space(4))) Args* q = (const __attribute__((address_space(4))) Args*)__builtin_amdgcn_kernarg_segment_ptr();
    asm volatile("" : "+s"(q));
    const u32 wave = __builtin_amdgcn_readfirstlane(wv);
    u32 lane;
    asm volatile("v_mbcnt_lo_u32_b32 %0, -1, 0\n\tv_mbcnt_hi_u32_b32 %0, -1, %0" : "=v"(lane));
    const u32 half = blockIdx.x & 1u, TN = q->TN, r0 = (blockIdx.x >> 1) * TN;
    const u16* x = q->xall;
    _Pragma("nounroll") for (u32 n = 0; n < (1u << 26) && *(volatile __attribute__((address_space(3))) u32*)S_QK < 4u; ++n) __builtin_amdgcn_s_sleep(2);
    if (wave >= 4u) {
        const int E = q->E, *dst = (const int*)q->dst.p, *src = (const int*)q->src.p;
        const u16* dis = q->disall;
        const u32 w = wave - 4u;
        _Pragma("nounroll") for (u32 k = 0; k < 5u; ++k) {
            const u32 grp = (u32)(((w == 0u ? 0x1713050109ull : w == 1u ? 0x1612040008ull : w == 2u ? 0x14100e0602ull : 0x15110f0703ull) >> (8u * k)) & 255u);
            const u32 row = grp * 4u + (lane >> 4), iv = *(const u32*)(smem + L_INV + row * 4u);
            const int me = (iv & 0x8000u) ? -1 : q->nbase + (int)(r0 + (iv & 255u) + TN - 255u);
            const int m0 = __builtin_amdgcn_readlane(me, 0), m1 = __builtin_amdgcn_readlane(me, 16), m2 = __builtin_amdgcn_readlane(me, 32), m3 = __builtin_amdgcn_readlane(me, 48);
            float s[8] = {0.f, 0.f, 0.f, 0.f, 0.f, 0.f, 0.f, 0.f};
            for (int e0 = 0; e0 < E; e0 += 64) {
                const int e = min(e0 + (int)lane, E - 1), d = dst[e], si = src[e];
                unsigned long long m = __ballot(e0 + (int)lane < E && (d == m0 || d == m1 || d == m2 || d == m3));
                while (m) {
                    const int l = __builtin_ctzll(m);
                    m &= m - 1;
                    const int sl = __builtin_amdgcn_readlane(si, l);
                    if (__builtin_amdgcn_readlane(d, l) == me) {
                        const u32x4 r = *(const u32x4*)(x + (size_t)sl * 256 + half * 128u + (lane & 15u) * 8u);
                        const float wt = bf2f(dis[sl]);
#pragma unroll
                        for (int j = 0; j < 4; ++j) { s[2 * j] += wt * lo(r[j]); s[2 * j + 1] += wt * hi(r[j]); }
                    }
                }
            }
            const float sc = bf2f(dis[max(me, 0)]);
            *(u32x4*)(smem + L_AL + (row < 48u ? row : row - 8u) * 256u + (((lane ^ row) & 15u) << 4)) =
                (u32x4){pack2(s[0] * sc, s[1] * sc), pack2(s[2] * sc, s[3] * sc), pack2(s[4] * sc, s[5] * sc), pack2(s[6] * sc, s[7] * sc)};
            lds_signal(S_T0 + 4u * (grp < 12u ? 1u + ((grp >> 2) & 1u) : grp >> 2), 1u);
        }
        return;
    }
    const int Ee = q->Ee, *edst = (const int*)q->edst.p, *esrc = (const int*)q->esrc.p;
    const u16 *wq = (const u16*)q->wq.p, *wk = (const u16*)q->wk.p;
    float* qs = (float*)(smem + L_QK + wave * 1024u);
    _Pragma("nounroll") for (u32 n = wave; n < TN && r0 + n < q->N; n += 4u) {
        const int me = q->nbase + (int)(r0 + n);
        float c0 = 0.f, c1 = 0.f, c2 = 0.f, c3 = 0.f;
        _Pragma("nounroll") for (int i = 0; i < 256; ++i) {
            const float xv = bf2f(x[(size_t)me * 256 + i]);
            const u32x2 r = *(const u32x2*)(wq + i * 256 + lane * 4u);
            c0 += xv * lo(r[0]); c1 += xv * hi(r[0]); c2 += xv * lo(r[1]); c3 += xv * hi(r[1]);
        }
        qs[lane * 4u] = c0; qs[lane * 4u + 1u] = c1; qs[lane * 4u + 2u] = c2; qs[lane * 4u + 3u] = c3;
        float k[4] = {0.f, 0.f, 0.f, 0.f};
        _Pragma("nounroll") for (int c = 0; c < 256; c += 2) {
            const float qa = qs[c], qb = qs[c + 1];
#pragma unroll
            for (int j = 0; j < 4; ++j) { const u32 r = *(const u32*)(wk + (lane * 4u + j) * 256u + c); k[j] += qa * lo(r) + qb * hi(r); }
        }
        float s0 = 0.f, s1 = 0.f, s2 = 0.f, s3 = 0.f, dn = 0.f;
        for (int e0 = 0; e0 < Ee; e0 += 64) {
            const int e = min(e0 + (int)lane, Ee - 1), si = esrc[e];
            unsigned long long m = __ballot(e0 + (int)lane < Ee && edst[e] == me);
            while (m) {
                const int l = __builtin_ctzll(m);
                m &= m - 1;
                const u32x2 r = *(const u32x2*)(x + (size_t)__builtin_amdgcn_readlane(si, l) * 256 + lane * 4u);
                float d = k[0] * lo(r[0]) + k[1] * hi(r[0]) + k[2] * lo(r[1]) + k[3] * hi(r[1]);
#pragma unroll
                for (int o = 1; o < 64; o <<= 1)
                    d += __uint_as_float((u32)__builtin_amdgcn_ds_bpermute((int)((lane ^ (u32)o) * 4u), (int)__float_as_uint(d)));
                const float wt = __builtin_amdgcn_exp2f(d * 0.09016844f);
                s0 += wt * lo(r[0]); s1 += wt * hi(r[0]); s2 += wt * lo(r[1]); s3 += wt * hi(r[1]);
                dn += wt;
            }
        }
        const float sc = __builtin_amdgcn_rcpf(dn + 1e-9f);
        if ((lane >> 5) == half)
            *(u32x2*)(smem + L_AA + ((u32)*(const u16*)(smem + L_MAP + n * 2u) ^ ((lane & 31u) * 8u))) =
                (u32x2){pack2(s0 * sc, s1 * sc), pack2(s2 * sc, s3 * sc)};
    }
    lds_signal(S_Z, 17u);
}

extern "C" void kernel_launch(void* const* d_in, const int* in_sizes, int n_in,
                              void* d_out, int out_size, void* d_ws, size_t ws_size,
                              hipStream_t stream) {
    const u32 N = in_sizes[5], E = in_sizes[1], Ee = in_sizes[3];
    const u16* x = (const u16*)d_in[0];
    const u16* dis = (const u16*)d_in[5];
    const u32 FL = 0x00020000u;
    const bool generic = N > NMAX || E > EMAX || (E & 3u) || (N & 1u);
    const bool irr = N > NMAX || Ee != 4u * N;
    (void)hipFuncSetAttribute((const void*)agg_kernel, hipFuncAttributeMaxDynamicSharedMemorySize, LDS_BYTES);
    for (u32 nbase = 0; nbase < N; nbase += NMAX) {
        const u32 n = N - nbase < NMAX ? N - nbase : NMAX;
        Args a;
        a.x = Desc{x + (size_t)nbase * 256, n * 512u, FL};
        a.ws = Desc{d_ws, WS_END, FL};
        a.wl = Desc{d_in[6], 131072u, FL}; a.wq = Desc{d_in[7], 131072u, FL}; a.wk = Desc{d_in[8], 131072u, FL}; a.wv = Desc{d_in[9], 131072u, FL};
        a.src = Desc{d_in[1], generic ? 0u : E * 4u, FL}; a.dst = Desc{d_in[2], generic ? 0u : E * 4u, FL};
        a.esrc = Desc{d_in[3], irr ? 0u : Ee * 4u, FL}; a.edst = Desc{d_in[4], irr ? 0u : Ee * 4u, FL};
        a.dis = Desc{dis + nbase, n * 2u, FL};
        a.out = Desc{(u16*)d_out + (size_t)nbase * 256, n * 512u, FL};
        a.N = n; a.TN = n > 256u ? (n + 127u) / 128u : 2u; a.M = ((1u << 24) + a.TN - 1u) / a.TN;
        a.irr = (irr ? 1u : 0u) | (generic ? 0x10000u : 0u);
        a.xall = x; a.disall = dis;
        a.E = (int)E; a.Ee = (int)Ee; a.nbase = (int)nbase; a.pad = 0;
        agg_kernel<<<256, 1024, LDS_BYTES, stream>>>(a);
    }
}
```

```cpp
#include <hip/hip_runtime.h>
#include <hip/hip_bf16.h>
#include <stdint.h>

typedef unsigned short u16;
typedef unsigned int u32;
typedef __attribute__((ext_vector_type(4))) u32 u32x4;
typedef __attribute__((ext_vector_type(2))) u32 u32x2;
struct Desc { const void* p; u32 bytes, flags; };
struct Args {
    Desc x, ws, wl, wv, wq, wk, src, dst, esrc, edst, dis, out;
    u32 N, TN, M, irr;
    const u16* xall;
    const u16* disall;
    int E, Ee, nbase, pad;
};
constexpr u32 NMAX = 10240, EMAX = 163840;
constexpr u32 WS_END = 9437184;
constexpr int LDS_BYTES = 163840;
constexpr u32 L_INV = 512, L_MAP = 1792, L_QK = 2048, L_AL = 23808, L_AA = 46336, S_QK = 16, S_Z = 40, S_T0 = 48;

__device__ __forceinline__ float bf2f(u32 h) { return __uint_as_float(h << 16); }
__device__ __forceinline__ float lo(u32 v) { return __uint_as_float(v << 16); }
__device__ __forceinline__ float hi(u32 v) { return __uint_as_float(v & 0xFFFF0000u); }
__device__ __forceinline__ u32 pack2(float a, float b) {
    return (u32)__builtin_bit_cast(u16, (__bf16)a) | ((u32)__builtin_bit_cast(u16, (__bf16)b) << 16);
}
__device__ __forceinline__ void lds_signal(const u32 addr, const u32 v) {
    asm volatile("s_mov_b64 exec, 1\n\tds_add_u32 %0, %1\n\ts_mov_b64 exec, -1" :: "v"(addr), "v"(v) : "memory");
}

extern "C" __global__ void __launch_bounds__(1024, 4)
agg_kernel(const Args a) {
    extern __shared__ __attribute__((aligned(16))) char smem[];
    u32 tid = threadIdx.x, wv;
    asm volatile(
        "s_memrealtime s[74:75]\n\t"
        "s_load_dwordx8 s[16:23], %2, 0\n\t"
        "s_load_dwordx4 s[64:67], %2, 192\n\t"
        "v_readfirstlane_b32 s69, v0\n\t"
        "v_and_b32 v1, 63, v0\n\t"
        "v_mov_b32 v2, 0\n\t"
        "v_mov_b32 v3, 1\n\t"
        "s_lshr_b32 s76, %3, 3\n\t"
        "s_and_b32 s70, %3, 1\n\t"
        "s_lshr_b32 s71, %3, 1\n\t"
        "s_mov_b32 s83, 0x2000000\n\t"
        "s_lshr_b32 s69, s69, 6\n\t"
        "s_cbranch_scc0 .L7_U\n\t"
        "s_cmp_lt_u32 s69, 8\n\t"
        "s_cbranch_scc1 .L7_G0\n\t"
        "s_load_dwordx4 s[48:51], %2, 128\n\t"
        "s_sub_u32 s69, s69, 8\n\t"
        "v_lshlrev_b32 v4, 2, v1\n\t"
        "s_lshl_b32 s77, s70, 2\n\t"
        "s_xor_b32 s77, s77, s69\n\t"
        "s_xor_b32 s77, s77, 4\n\t"
        "s_mul_i32 s80, s77, 10\n\t"
        "s_waitcnt lgkmcnt(0)\n\t"
        "s_mul_i32 s72, s71, s65\n\t"
        "s_add_u32 s78, s72, s80\n\t"
        "s_lshl_b32 s78, s78, 4\n\t"
        "s_mov_b32 exec_hi, 0xff\n\t"
        "buffer_load_dword v5, v4, s[48:51], s78 offen\n\t"
        "s_mov_b64 exec, -1\n\t"
        "s_barrier\n\t"
        "s_branch .L7_O2\n"
        ".L7_U:\n\t"
        "s_load_dwordx16 s[32:47], %2, 64\n\t"
        "s_load_dwordx16 s[48:63], %2, 128\n\t"
        "s_setprio 3\n\t"
        "s_getpc_b64 s[24:25]\n\t"
        "s_add_u32 s24, s24, agg_kernel@rel32@lo+4\n\t"
        "s_addc_u32 s25, s25, agg_kernel@rel32@hi+12\n\t"
        "s_and_b32 s25, s25, 0xffff\n\t"
        "s_mov_b32 s26, .L7_x-agg_kernel\n\t"
        "s_mov_b32 s27, 0x20000\n\t"
        "v_lshlrev_b32 v6, 12, v1\n\t"
        "s_lshl_b32 s78, s76, 7\n\t"
        "s_mov_b32 m0, 134400\n\t"
        "s_mov_b64 exec, 63\n\t"
        "buffer_load_dword v6, s[24:27], s78 offen lds\n\t"
        "s_mov_b64 exec, -1\n\t"
        "s_cmp_eq_u32 s70, 0\n\t"
        "s_cbranch_scc0 .L7_V\n\t"
        "v_lshrrev_b32 v4, 3, v1\n\t"
        "v_and_b32 v5, 7, v1\n\t"
        "v_lshlrev_b32 v4, 14, v4\n\t"
        "v_lshl_add_u32 v4, v5, 4, v4\n\t"
        "s_lshl_b32 s78, s71, 7\n\t"
        "v_add_u32 v4, s78, v4\n\t"
        "v_add_u32 v5, 0x20000, v4\n\t"
        "v_add_u32 v6, 0x40000, v4\n\t"
        "v_add_u32 v7, 0x60000, v4\n\t"
        "v_add_u32 v8, 0x80000, v4\n\t"
        "s_waitcnt lgkmcnt(0)\n\t"
        "s_mul_i32 s72, s71, s65\n\t"
        "buffer_load_dwordx4 v[16:19], v4, s[44:47], 0 offen\n\t"
        "buffer_load_dwordx4 v[20:23], v5, s[44:47], 0 offen\n\t"
        "buffer_load_dwordx4 v[24:27], v6, s[44:47], 0 offen\n\t"
        "buffer_load_dwordx4 v[28:31], v7, s[44:47], 0 offen\n\t"
        "buffer_load_dwordx4 v[32:35], v8, s[44:47], 0 offen\n\t"
        "s_mul_i32 s80, s71, 0x50\n\t"
        "s_lshl_b32 s78, s80, 4\n\t"
        "v_lshlrev_b32 v12, 4, v1\n\t"
        "buffer_load_dwordx4 v[116:119], v12, s[52:55], s78 offen\n\t"
        "s_mov_b64 exec, 0xffff\n\t"
        "buffer_load_dwordx4 v[120:123], v12, s[52:55], s78 offen offset:1024\n\t"
        "s_mov_b64 exec, -1\n\t"
        "buffer_load_dwordx4 v[36:39], v4, s[40:43], 0 offen\n\t"
        "buffer_load_dwordx4 v[40:43], v5, s[40:43], 0 offen\n\t"
        "buffer_load_dwordx4 v[44:47], v6, s[40:43], 0 offen\n\t"
        "buffer_load_dwordx4 v[48:51], v7, s[40:43], 0 offen\n\t"
        "buffer_load_dwordx4 v[52:55], v8, s[40:43], 0 offen\n\t"
        "s_barrier\n\t"
        "s_branch .L7_U2\n"
        ".L7_G0:\n\t"
        "s_cmp_lt_u32 s69, 4\n\t"
        "s_cbranch_scc1 .L7_L\n\t"
        "s_sub_u32 s69, s69, 4\n\t"
        "v_lshlrev_b32 v4, 4, v1\n\t"
        "v_and_b32 v5, 0x200, v4\n\t"
        "v_mad_u32_u24 v4, v5, 7, v4\n\t"
        "s_mul_i32 s80, s70, 5\n\t"
        "s_mul_i32 s81, s69, 0x420\n\t"
        "s_add_u32 s81, s81, 2048\n\t"
        "s_mul_i32 s78, s80, 0x1080\n\t"
        "s_add_u32 m0, s78, s81\n\t"
        "s_lshr_b32 s78, s80, 1\n\t"
        "s_lshl1_add_u32 s78, s78, s80\n\t"
        "s_lshl_b32 s78, s78, 11\n\t"
        "s_waitcnt lgkmcnt(0)\n\t"
        "s_mul_i32 s72, s71, s65\n\t"
        "s_add_u32 s79, s72, s69\n\t"
        "s_lshl_b32 s79, s79, 9\n\t"
        "v_add_u32 v4, s79, v4\n\t"
        "s_sleep 3\n\t"
        "s_cmp_eq_u32 s70, 0\n\t"
        "s_cbranch_scc1 .L7_gs\n\t"
        "s_sleep 4\n"
        ".L7_gs:\n\t"
        "buffer_load_dwordx4 v4, s[16:19], s78 offen lds\n\t"
        "s_add_u32 s80, s80, 1\n\t"
        "s_load_dwordx4 s[56:59], %2, 160\n\t"
        "s_mul_i32 s78, s80, 0x1080\n\t"
        "s_add_u32 m0, s78, s81\n\t"
        "s_lshr_b32 s78, s80, 1\n\t"
        "s_lshl1_add_u32 s78, s78, s80\n\t"
        "s_lshl_b32 s78, s78, 11\n\t"
        "buffer_load_dwordx4 v4, s[16:19], s78 offen lds\n\t"
        "s_add_u32 s80, s80, 1\n\t"
        "s_mul_i32 s78, s80, 0x1080\n\t"
        "s_add_u32 m0, s78, s81\n\t"
        "s_lshr_b32 s78, s80, 1\n\t"
        "s_lshl1_add_u32 s78, s78, s80\n\t"
        "s_lshl_b32 s78, s78, 11\n\t"
        "buffer_load_dwordx4 v4, s[16:19], s78 offen lds\n\t"
        "s_add_u32 s80, s80, 1\n\t"
        "s_mul_i32 s78, s80, 0x1080\n\t"
        "s_add_u32 m0, s78, s81\n\t"
        "s_lshr_b32 s78, s80, 1\n\t"
        "s_lshl1_add_u32 s78, s78, s80\n\t"
        "s_lshl_b32 s78, s78, 11\n\t"
        "buffer_load_dwordx4 v4, s[16:19], s78 offen lds\n\t"
        "s_add_u32 s80, s80, 1\n\t"
        "s_mul_i32 s78, s80, 0x1080\n\t"
        "s_add_u32 m0, s78, s81\n\t"
        "s_lshr_b32 s78, s80, 1\n\t"
        "s_lshl1_add_u32 s78, s78, s80\n\t"
        "s_lshl_b32 s78, s78, 11\n\t"
        "buffer_load_dwordx4 v4, s[16:19], s78 offen lds\n\t"
        "s_add_u32 s80, s80, 1\n\t"
        "s_barrier\n\t"
        "s_branch .L7_G2\n"
        ".L7_L:\n\t"
        "s_cmp_eq_u32 s69, 2\n\t"
        "s_cbranch_scc0 .L7_L1\n\t"
        "s_waitcnt lgkmcnt(0)\n\t"
        "s_mul_i32 s72, s71, s65\n\t"
        "s_buffer_load_dword s68, s[20:23], 0\n\t"
        "s_barrier\n\t"
        "s_branch .L7_P2\n"
        ".L7_L1:\n\t"
        "s_cmp_eq_u32 s69, 1\n\t"
        "s_cbranch_scc0 .L7_T\n\t"
        "s_load_dwordx4 s[56:59], %2, 160\n\t"
        "v_lshlrev_b32 v4, 3, v1\n\t"
        "v_mov_b64 v[8:9], 0\n\t"
        "ds_write_b64 v4, v[8:9]\n\t"
        "s_waitcnt lgkmcnt(0)\n\t"
        "s_mul_i32 s72, s71, s65\n\t"
        "v_add_lshl_u32 v4, v1, s72, 1\n\t"
        "buffer_load_ushort v16, v4, s[56:59], 0 offen\n\t"
        "buffer_load_ushort v17, v4, s[56:59], 0 offen offset:128\n\t"
        "s_barrier\n\t"
        "s_branch .L7_R2\n"
        ".L7_T:\n\t"
        "s_load_dwordx8 s[24:31], %2, 32\n\t"
        "v_lshlrev_b32 v6, 7, v1\n\t"
        "s_mov_b32 m0, 134400\n\t"
        "s_lshl_b32 s78, s70, 16\n\t"
        "s_lshl_b32 s79, s76, 11\n\t"
        "s_add_u32 s80, s78, s79\n\t"
        "s_mul_i32 s81, s76, 0x300\n\t"
        "s_lshl_b32 s82, %3, 7\n\t"
        "s_add_u32 s82, s82, 4096\n\t"
        "v_lshlrev_b32 v8, 2, v1\n\t"
        "s_and_b32 s78, s71, 63\n\t"
        "s_lshl_b32 s78, s78, 7\n\t"
        "s_and_b32 s79, s71, 64\n\t"
        "s_lshl_b32 s79, s79, 8\n\t"
        "s_add_u32 s42, s78, s79\n\t"
        "s_and_b32 s44, s76, 1\n\t"
        "s_lshl_b32 s78, s44, 13\n\t"
        "s_add_u32 s42, s42, s78\n\t"
        "s_lshl_b32 s78, s70, 15\n\t"
        "s_add_u32 s42, s42, s78\n\t"
        "s_add_u32 s42, s42, 65536\n\t"
        "s_xor_b32 s43, s42, 0x2000\n\t"
        "s_waitcnt lgkmcnt(0)\n\t"
        "s_mul_i32 s72, s71, s65\n\t"
        "s_buffer_load_dword s40, s[20:23], s42 glc\n\t"
        "s_buffer_load_dword s41, s[20:23], s43 glc\n\t"
        "v_mov_b32 v7, s74\n\t"
        "s_mov_b32 exec_hi, 0\n\t"
        "buffer_store_dword v7, v8, s[20:23], s82 offen sc1\n\t"
        "s_lshr_b32 s78, %3, 2\n\t"
        "s_xor_b32 s44, s44, s78\n\t"
        "s_waitcnt lgkmcnt(1)\n\t"
        "s_nop 8\n\t"
        "s_getreg_b32 s45, hwreg(HW_REG_IB_STS, 8, 4)\n\t"
        "s_xnor_b32 s44, s44, s45\n\t"
        "s_and_b32 s44, s44, 1\n\t"
        "s_lshl_b32 s44, s44, 25\n\t"
        "v_mov_b32 v7, s44\n\t"
        "ds_write_b32 v2, v7 offset:1984\n\t"
        "s_waitcnt lgkmcnt(0)\n\t"
        "s_barrier\n\t"
        "s_mov_b64 exec, -1\n\t"
        "s_mov_b64 exec, 0xffff\n\t"
        "buffer_load_dword v6, s[28:31], s80 offen lds\n\t"
        "buffer_load_dword v6, s[24:27], s80 offen lds\n\t"
        "s_mov_b64 exec, -1\n\t"
        "s_branch .L7_T2\n"
        ".L7_V:\n\t"
        "s_and_b32 s78, s71, 1\n\t"
        "s_and_b32 s79, s76, 3\n\t"
        "s_lshl2_add_u32 s80, s78, s79\n\t"
        "s_and_b32 s78, s71, 2\n\t"
        "s_lshr_b32 s79, s76, 2\n\t"
        "s_lshl2_add_u32 s81, s78, s79\n\t"
        "v_lshlrev_b32 v12, 4, v1\n\t"
        "v_and_b32 v5, 0x200, v12\n\t"
        "v_mad_u32_u24 v4, v5, 15, v12\n\t"
        "s_lshl_b32 s82, s80, 14\n\t"
        "s_lshr_b32 s78, s81, 1\n\t"
        "s_lshl_b32 s78, s78, 14\n\t"
        "s_and_b32 s79, s81, 1\n\t"
        "s_lshl_b32 s79, s79, 11\n\t"
        "s_add_u32 s77, s78, s79\n\t"
        "s_mov_b32 m0, 68864\n\t"
        "s_waitcnt lgkmcnt(0)\n\t"
        "s_mul_i32 s72, s71, s65\n\t"
        "buffer_load_dwordx4 v4, s[36:39], s77 offen offset:0 lds\n\t"
        "s_add_u32 m0, m0, 544\n\t"
        "s_nop 0\n\t"
        "buffer_load_dwordx4 v4, s[36:39], s77 offen offset:512 lds\n\t"
        "s_add_u32 m0, m0, 544\n\t"
        "s_nop 0\n\t"
        "buffer_load_dwordx4 v4, s[36:39], s77 offen offset:1024 lds\n\t"
        "s_add_u32 m0, m0, 544\n\t"
        "s_nop 0\n\t"
        "buffer_load_dwordx4 v4, s[36:39], s77 offen offset:1536 lds\n\t"
        "s_add_u32 m0, m0, 2592\n\t"
        "s_add_u32 s77, s77, 0x1000\n\t"
        "buffer_load_dwordx4 v4, s[36:39], s77 offen offset:0 lds\n\t"
        "s_add_u32 m0, m0, 544\n\t"
        "s_nop 0\n\t"
        "buffer_load_dwordx4 v4, s[36:39], s77 offen offset:512 lds\n\t"
        "s_add_u32 m0, m0, 544\n\t"
        "s_nop 0\n\t"
        "buffer_load_dwordx4 v4, s[36:39], s77 offen offset:1024 lds\n\t"
        "s_add_u32 m0, m0, 544\n\t"
        "s_nop 0\n\t"
        "buffer_load_dwordx4 v4, s[36:39], s77 offen offset:1536 lds\n\t"
        "s_add_u32 m0, m0, 2592\n\t"
        "s_nop 0\n\t"
        "buffer_load_dwordx4 v4, s[32:35], s82 offen offset:0 lds\n\t"
        "s_add_u32 m0, m0, 544\n\t"
        "s_nop 0\n\t"
        "buffer_load_dwordx4 v4, s[32:35], s82 offen offset:512 lds\n\t"
        "s_add_u32 m0, m0, 544\n\t"
        "s_nop 0\n\t"
        "buffer_load_dwordx4 v4, s[32:35], s82 offen offset:1024 lds\n\t"
        "s_add_u32 m0, m0, 544\n\t"
        "s_nop 0\n\t"
        "buffer_load_dwordx4 v4, s[32:35], s82 offen offset:1536 lds\n\t"
        "s_add_u32 m0, m0, 2592\n\t"
        "s_add_u32 s82, s82, 0x1000\n\t"
        "buffer_load_dwordx4 v4, s[32:35], s82 offen offset:0 lds\n\t"
        "s_add_u32 m0, m0, 544\n\t"
        "s_nop 0\n\t"
        "buffer_load_dwordx4 v4, s[32:35], s82 offen offset:512 lds\n\t"
        "s_add_u32 m0, m0, 544\n\t"
        "s_nop 0\n\t"
        "buffer_load_dwordx4 v4, s[32:35], s82 offen offset:1024 lds\n\t"
        "s_add_u32 m0, m0, 544\n\t"
        "s_nop 0\n\t"
        "buffer_load_dwordx4 v4, s[32:35], s82 offen offset:1536 lds\n\t"
        "s_add_u32 m0, m0, 2592\n\t"
        "s_sub_u32 s82, s82, 0x800\n\t"
        "buffer_load_dwordx4 v4, s[32:35], s82 offen offset:0 lds\n\t"
        "s_add_u32 m0, m0, 544\n\t"
        "s_nop 0\n\t"
        "buffer_load_dwordx4 v4, s[32:35], s82 offen offset:512 lds\n\t"
        "s_add_u32 m0, m0, 544\n\t"
        "s_nop 0\n\t"
        "buffer_load_dwordx4 v4, s[32:35], s82 offen offset:1024 lds\n\t"
        "s_add_u32 m0, m0, 544\n\t"
        "s_nop 0\n\t"
        "buffer_load_dwordx4 v4, s[32:35], s82 offen offset:1536 lds\n\t"
        "s_add_u32 m0, m0, 2592\n\t"
        "s_add_u32 s82, s82, 0x1000\n\t"
        "buffer_load_dwordx4 v4, s[32:35], s82 offen offset:0 lds\n\t"
        "s_add_u32 m0, m0, 544\n\t"
        "s_nop 0\n\t"
        "buffer_load_dwordx4 v4, s[32:35], s82 offen offset:512 lds\n\t"
        "s_add_u32 m0, m0, 544\n\t"
        "s_nop 0\n\t"
        "buffer_load_dwordx4 v4, s[32:35], s82 offen offset:1024 lds\n\t"
        "s_add_u32 m0, m0, 544\n\t"
        "s_nop 0\n\t"
        "buffer_load_dwordx4 v4, s[32:35], s82 offen offset:1536 lds\n\t"
        "s_barrier\n\t"
        "s_branch .L7_V2\n"
        ".L7_U2:\n\t"
        "v_cmp_gt_u32 s[24:25], s46, v4\n\t"
        "v_cmp_gt_u32 s[26:27], s46, v5\n\t"
        "v_cmp_gt_u32 s[28:29], s46, v6\n\t"
        "v_cmp_gt_u32 s[30:31], s46, v7\n\t"
        "v_cmp_gt_u32 s[32:33], s46, v8\n\t"
        "v_mul_u32_u24 v112, 0xa0, v1\n\t"
        "v_add_u32 v112, 68864, v112\n\t"
        "v_add_u32 v113, 10240, v112\n\t"
        "v_lshlrev_b32 v7, 2, v1\n\t"
        "ds_write_b32 v7, v112 offset:1028\n\t"
        "ds_write_b32 v7, v113 offset:1284\n\t"
        "v_lshrrev_b32 v15, 3, v1\n\t"
        "v_mul_u32_u24 v15, 0xa0, v15\n\t"
        "v_add_u32 v15, 68864, v15\n\t"
        "v_and_b32 v4, 7, v1\n\t"
        "v_lshl_add_u32 v15, v4, 2, v15\n\t"
        "v_mov_b32 v13, 0x1000000\n\t"
        "v_mov_b32 v14, 4\n\t"
        "s_lshl_b32 s54, s65, 14\n\t"
        "s_sub_u32 s54, 0, s54\n\t"
        "s_lshl_b32 s48, s71, 14\n\t"
        "s_add_u32 s48, s48, 1048576\n\t"
        "s_add_u32 s49, s48, 4096\n\t"
        "v_add_u32 v97, 1280, v15\n\t"
        "v_add_u32 v98, 2560, v15\n\t"
        "v_add_u32 v99, 3840, v15\n\t"
        "v_add_u32 v100, 5120, v15\n\t"
        "v_add_u32 v101, 6400, v15\n\t"
        "v_add_u32 v102, 7680, v15\n\t"
        "v_add_u32 v103, 8960, v15\n\t"
        "v_add_u32 v112, 0x80, v112\n\t"
        "v_add_u32 v113, 0x80, v113\n\t"
        "v_add_u32 v114, s80, v1\n\t"
        "v_add_u32 v115, 64, v114\n\t"
        "v_cmp_gt_u32 s[36:37], s64, v114\n\t"
        "v_cmp_gt_u32 s[38:39], s64, v115\n\t"
        "s_waitcnt vmcnt(11) lgkmcnt(8)\n\t"
        "s_mov_b64 exec, s[24:25]\n\t"
        "v_mad_u32_u24 v56, v16, s66, v13\n\t"
        "v_mad_u32_u24 v57, v17, s66, v13\n\t"
        "v_mad_u32_u24 v58, v18, s66, v13\n\t"
        "v_mad_u32_u24 v59, v19, s66, v13\n\t"
        "v_lshlrev_b32_sdwa v56, 2, v56 dst_sel:DWORD dst_unused:UNUSED_PAD src0_sel:DWORD src1_sel:BYTE_3\n\t"
        "ds_add_rtn_u32 v76, v56, v14 offset:1024\n\t"
        "v_lshlrev_b32_sdwa v57, 2, v57 dst_sel:DWORD dst_unused:UNUSED_PAD src0_sel:DWORD src1_sel:BYTE_3\n\t"
        "ds_add_rtn_u32 v77, v57, v14 offset:1024\n\t"
        "v_lshlrev_b32_sdwa v58, 2, v58 dst_sel:DWORD dst_unused:UNUSED_PAD src0_sel:DWORD src1_sel:BYTE_3\n\t"
        "ds_add_rtn_u32 v78, v58, v14 offset:1024\n\t"
        "v_lshlrev_b32_sdwa v59, 2, v59 dst_sel:DWORD dst_unused:UNUSED_PAD src0_sel:DWORD src1_sel:BYTE_3\n\t"
        "ds_add_rtn_u32 v79, v59, v14 offset:1024\n\t"
        "s_waitcnt vmcnt(10) lgkmcnt(8)\n\t"
        "s_mov_b64 exec, s[26:27]\n\t"
        "v_mad_u32_u24 v60, v20, s66, v13\n\t"
        "v_mad_u32_u24 v61, v21, s66, v13\n\t"
        "v_mad_u32_u24 v62, v22, s66, v13\n\t"
        "v_mad_u32_u24 v63, v23, s66, v13\n\t"
        "v_lshlrev_b32_sdwa v60, 2, v60 dst_sel:DWORD dst_unused:UNUSED_PAD src0_sel:DWORD src1_sel:BYTE_3\n\t"
        "ds_add_rtn_u32 v80, v60, v14 offset:1024\n\t"
        "v_lshlrev_b32_sdwa v61, 2, v61 dst_sel:DWORD dst_unused:UNUSED_PAD src0_sel:DWORD src1_sel:BYTE_3\n\t"
        "ds_add_rtn_u32 v81, v61, v14 offset:1024\n\t"
        "v_lshlrev_b32_sdwa v62, 2, v62 dst_sel:DWORD dst_unused:UNUSED_PAD src0_sel:DWORD src1_sel:BYTE_3\n\t"
        "ds_add_rtn_u32 v82, v62, v14 offset:1024\n\t"
        "v_lshlrev_b32_sdwa v63, 2, v63 dst_sel:DWORD dst_unused:UNUSED_PAD src0_sel:DWORD src1_sel:BYTE_3\n\t"
        "ds_add_rtn_u32 v83, v63, v14 offset:1024\n\t"
        "s_waitcnt vmcnt(9) lgkmcnt(8)\n\t"
        "s_mov_b64 exec, s[28:29]\n\t"
        "v_mad_u32_u24 v64, v24, s66, v13\n\t"
        "v_mad_u32_u24 v65, v25, s66, v13\n\t"
        "v_mad_u32_u24 v66, v26, s66, v13\n\t"
        "v_mad_u32_u24 v67, v27, s66, v13\n\t"
        "v_lshlrev_b32_sdwa v64, 2, v64 dst_sel:DWORD dst_unused:UNUSED_PAD src0_sel:DWORD src1_sel:BYTE_3\n\t"
        "ds_add_rtn_u32 v84, v64, v14 offset:1024\n\t"
        "v_lshlrev_b32_sdwa v65, 2, v65 dst_sel:DWORD dst_unused:UNUSED_PAD src0_sel:DWORD src1_sel:BYTE_3\n\t"
        "ds_add_rtn_u32 v85, v65, v14 offset:1024\n\t"
        "v_lshlrev_b32_sdwa v66, 2, v66 dst_sel:DWORD dst_unused:UNUSED_PAD src0_sel:DWORD src1_sel:BYTE_3\n\t"
        "ds_add_rtn_u32 v86, v66, v14 offset:1024\n\t"
        "v_lshlrev_b32_sdwa v67, 2, v67 dst_sel:DWORD dst_unused:UNUSED_PAD src0_sel:DWORD src1_sel:BYTE_3\n\t"
        "ds_add_rtn_u32 v87, v67, v14 offset:1024\n\t"
        "s_waitcnt vmcnt(8) lgkmcnt(8)\n\t"
        "s_mov_b64 exec, s[30:31]\n\t"
        "v_mad_u32_u24 v68, v28, s66, v13\n\t"
        "v_mad_u32_u24 v69, v29, s66, v13\n\t"
        "v_mad_u32_u24 v70, v30, s66, v13\n\t"
        "v_mad_u32_u24 v71, v31, s66, v13\n\t"
        "v_lshlrev_b32_sdwa v68, 2, v68 dst_sel:DWORD dst_unused:UNUSED_PAD src0_sel:DWORD src1_sel:BYTE_3\n\t"
        "ds_add_rtn_u32 v88, v68, v14 offset:1024\n\t"
        "v_lshlrev_b32_sdwa v69, 2, v69 dst_sel:DWORD dst_unused:UNUSED_PAD src0_sel:DWORD src1_sel:BYTE_3\n\t"
        "ds_add_rtn_u32 v89, v69, v14 offset:1024\n\t"
        "v_lshlrev_b32_sdwa v70, 2, v70 dst_sel:DWORD dst_unused:UNUSED_PAD src0_sel:DWORD src1_sel:BYTE_3\n\t"
        "ds_add_rtn_u32 v90, v70, v14 offset:1024\n\t"
        "v_lshlrev_b32_sdwa v71, 2, v71 dst_sel:DWORD dst_unused:UNUSED_PAD src0_sel:DWORD src1_sel:BYTE_3\n\t"
        "ds_add_rtn_u32 v91, v71, v14 offset:1024\n\t"
        "s_waitcnt vmcnt(7) lgkmcnt(8)\n\t"
        "s_mov_b64 exec, s[32:33]\n\t"
        "v_mad_u32_u24 v72, v32, s66, v13\n\t"
        "v_mad_u32_u24 v73, v33, s66, v13\n\t"
        "v_mad_u32_u24 v74, v34, s66, v13\n\t"
        "v_mad_u32_u24 v75, v35, s66, v13\n\t"
        "v_lshlrev_b32_sdwa v72, 2, v72 dst_sel:DWORD dst_unused:UNUSED_PAD src0_sel:DWORD src1_sel:BYTE_3\n\t"
        "ds_add_rtn_u32 v92, v72, v14 offset:1024\n\t"
        "v_lshlrev_b32_sdwa v73, 2, v73 dst_sel:DWORD dst_unused:UNUSED_PAD src0_sel:DWORD src1_sel:BYTE_3\n\t"
        "ds_add_rtn_u32 v93, v73, v14 offset:1024\n\t"
        "v_lshlrev_b32_sdwa v74, 2, v74 dst_sel:DWORD dst_unused:UNUSED_PAD src0_sel:DWORD src1_sel:BYTE_3\n\t"
        "ds_add_rtn_u32 v94, v74, v14 offset:1024\n\t"
        "v_lshlrev_b32_sdwa v75, 2, v75 dst_sel:DWORD dst_unused:UNUSED_PAD src0_sel:DWORD src1_sel:BYTE_3\n\t"
        "ds_add_rtn_u32 v95, v75, v14 offset:1024\n\t"
        "s_mov_b64 exec, -1\n\t"
        "ds_read_b32 v4, v7 offset:1028\n\t"
        "ds_read_b32 v5, v7 offset:1284\n\t"
        "s_waitcnt vmcnt(5)\n\t"
        "v_xor_b32 v116, v116, v114\n\t"
        "v_bitop3_b32 v116, v117, v116, v114 bitop3:0xde\n\t"
        "v_bitop3_b32 v116, v118, v116, v114 bitop3:0xde\n\t"
        "v_bitop3_b32 v116, v119, v116, v114 bitop3:0xde\n\t"
        "v_xor_b32 v120, v120, v115\n\t"
        "v_bitop3_b32 v120, v121, v120, v115 bitop3:0xde\n\t"
        "v_bitop3_b32 v120, v122, v120, v115 bitop3:0xde\n\t"
        "v_bitop3_b32 v120, v123, v120, v115 bitop3:0xde\n\t"
        "v_cmp_ne_u32 s[40:41], 0, v116\n\t"
        "v_cmp_ne_u32 s[42:43], 0, v120\n\t"
        "s_and_b64 s[38:39], s[38:39], 0xffff\n\t"
        "s_and_b64 s[36:37], s[36:37], s[40:41]\n\t"
        "s_and_b64 s[38:39], s[38:39], s[42:43]\n\t"
        "s_or_b64 s[36:37], s[36:37], s[38:39]\n\t"
        "s_cselect_b32 s67, 1, s67\n"
        ".L7_w38:\n\t"
        "ds_read_b32 v0, v2 offset:104\n\t"
        "ds_read_b32 v6, v2 offset:76\n\t"
        "s_waitcnt lgkmcnt(1)\n\t"
        "v_cmp_gt_u16 vcc, 1, v0\n\t"
        "s_cbranch_vccz .L7_d39\n\t"
        "s_waitcnt lgkmcnt(0)\n\t"
        "s_sleep 0\n\t"
        "s_sub_i32 s83, s83, 1\n\t"
        "s_cmp_gt_i32 s83, 0\n\t"
        "s_cbranch_scc1 .L7_w38\n"
        ".L7_d39:\n\t"
        "v_cmp_gt_u32 s[34:35], v4, v112\n\t"
        "v_cmp_gt_u32 s[36:37], v5, v113\n\t"
        "s_or_b64 s[38:39], s[34:35], s[36:37]\n\t"
        "s_cbranch_scc1 .L7_ov\n"
        ".L7_ov1:\n\t"
        "s_waitcnt lgkmcnt(0)\n\t"
        "v_readfirstlane_b32 s68, v6\n\t"
        "s_waitcnt vmcnt(4) lgkmcnt(8)\n\t"
        "s_mov_b64 exec, s[24:25]\n\t"
        "v_lshl_add_u32 v16, v16, 16, v36\n\t"
        "v_lshl_add_u32 v17, v17, 16, v37\n\t"
        "v_lshl_add_u32 v18, v18, 16, v38\n\t"
        "v_lshl_add_u32 v19, v19, 16, v39\n\t"
        "v_mad_i32_i24 v16, v56, s54, v16\n\t"
        "ds_add_u32 v76, v16\n\t"
        "v_mad_i32_i24 v17, v57, s54, v17\n\t"
        "ds_add_u32 v77, v17\n\t"
        "v_mad_i32_i24 v18, v58, s54, v18\n\t"
        "ds_add_u32 v78, v18\n\t"
        "v_mad_i32_i24 v19, v59, s54, v19\n\t"
        "ds_add_u32 v79, v19\n\t"
        "s_waitcnt vmcnt(3) lgkmcnt(8)\n\t"
        "s_mov_b64 exec, s[26:27]\n\t"
        "v_lshl_add_u32 v20, v20, 16, v40\n\t"
        "v_lshl_add_u32 v21, v21, 16, v41\n\t"
        "v_lshl_add_u32 v22, v22, 16, v42\n\t"
        "v_lshl_add_u32 v23, v23, 16, v43\n\t"
        "v_mad_i32_i24 v20, v60, s54, v20\n\t"
        "ds_add_u32 v80, v20\n\t"
        "v_mad_i32_i24 v21, v61, s54, v21\n\t"
        "ds_add_u32 v81, v21\n\t"
        "v_mad_i32_i24 v22, v62, s54, v22\n\t"
        "ds_add_u32 v82, v22\n\t"
        "v_mad_i32_i24 v23, v63, s54, v23\n\t"
        "ds_add_u32 v83, v23\n\t"
        "s_waitcnt vmcnt(2) lgkmcnt(8)\n\t"
        "s_mov_b64 exec, s[28:29]\n\t"
        "v_lshl_add_u32 v24, v24, 16, v44\n\t"
        "v_lshl_add_u32 v25, v25, 16, v45\n\t"
        "v_lshl_add_u32 v26, v26, 16, v46\n\t"
        "v_lshl_add_u32 v27, v27, 16, v47\n\t"
        "v_mad_i32_i24 v24, v64, s54, v24\n\t"
        "ds_add_u32 v84, v24\n\t"
        "v_mad_i32_i24 v25, v65, s54, v25\n\t"
        "ds_add_u32 v85, v25\n\t"
        "v_mad_i32_i24 v26, v66, s54, v26\n\t"
        "ds_add_u32 v86, v26\n\t"
        "v_mad_i32_i24 v27, v67, s54, v27\n\t"
        "ds_add_u32 v87, v27\n\t"
        "s_waitcnt vmcnt(1) lgkmcnt(8)\n\t"
        "s_mov_b64 exec, s[30:31]\n\t"
        "v_lshl_add_u32 v28, v28, 16, v48\n\t"
        "v_lshl_add_u32 v29, v29, 16, v49\n\t"
        "v_lshl_add_u32 v30, v30, 16, v50\n\t"
        "v_lshl_add_u32 v31, v31, 16, v51\n\t"
        "v_mad_i32_i24 v28, v68, s54, v28\n\t"
        "ds_add_u32 v88, v28\n\t"
        "v_mad_i32_i24 v29, v69, s54, v29\n\t"
        "ds_add_u32 v89, v29\n\t"
        "v_mad_i32_i24 v30, v70, s54, v30\n\t"
        "ds_add_u32 v90, v30\n\t"
        "v_mad_i32_i24 v31, v71, s54, v31\n\t"
        "ds_add_u32 v91, v31\n\t"
        "s_waitcnt vmcnt(0) lgkmcnt(8)\n\t"
        "s_mov_b64 exec, s[32:33]\n\t"
        "v_lshl_add_u32 v32, v32, 16, v52\n\t"
        "v_lshl_add_u32 v33, v33, 16, v53\n\t"
        "v_lshl_add_u32 v34, v34, 16, v54\n\t"
        "v_lshl_add_u32 v35, v35, 16, v55\n\t"
        "v_mad_i32_i24 v32, v72, s54, v32\n\t"
        "ds_add_u32 v92, v32\n\t"
        "v_mad_i32_i24 v33, v73, s54, v33\n\t"
        "ds_add_u32 v93, v33\n\t"
        "v_mad_i32_i24 v34, v74, s54, v34\n\t"
        "ds_add_u32 v94, v34\n\t"
        "v_mad_i32_i24 v35, v75, s54, v35\n\t"
        "ds_add_u32 v95, v35\n\t"
        "s_mov_b64 exec, -1\n\t"
        "s_and_b32 s78, s67, 1\n\t"
        "s_or_b32 s78, s78, s38\n\t"
        "s_or_b32 s78, s78, s39\n\t"
        "s_cbranch_scc1 .L7_mk\n"
        ".L7_mk1:\n\t"
        "s_mov_b64 exec, 1\n\t"
        "ds_add_u32 v2, v3 offset:112\n\t"
        "s_mov_b64 exec, -1\n\t"
        "s_waitcnt lgkmcnt(3)\n\t"
        "ds_read2_b32 v[16:17], v15 offset0:0 offset1:8\n\t"
        "ds_read2_b32 v[18:19], v15 offset0:16 offset1:24\n\t"
        "ds_read2_b32 v[20:21], v97 offset0:0 offset1:8\n\t"
        "ds_read2_b32 v[22:23], v97 offset0:16 offset1:24\n\t"
        "ds_read2_b32 v[24:25], v98 offset0:0 offset1:8\n\t"
        "ds_read2_b32 v[26:27], v98 offset0:16 offset1:24\n\t"
        "ds_read2_b32 v[28:29], v99 offset0:0 offset1:8\n\t"
        "ds_read2_b32 v[30:31], v99 offset0:16 offset1:24\n\t"
        "ds_read2_b32 v[32:33], v100 offset0:0 offset1:8\n\t"
        "ds_read2_b32 v[34:35], v100 offset0:16 offset1:24\n\t"
        "ds_read2_b32 v[36:37], v101 offset0:0 offset1:8\n\t"
        "ds_read2_b32 v[38:39], v101 offset0:16 offset1:24\n\t"
        "s_waitcnt lgkmcnt(10)\n\t"
        "buffer_store_dwordx4 v[16:19], v12, s[20:23], s48 offen offset:0 sc1\n\t"
        "ds_read2_b32 v[40:41], v102 offset0:0 offset1:8\n\t"
        "ds_read2_b32 v[42:43], v102 offset0:16 offset1:24\n\t"
        "s_waitcnt lgkmcnt(10)\n\t"
        "buffer_store_dwordx4 v[20:23], v12, s[20:23], s48 offen offset:1024 sc1\n\t"
        "ds_read2_b32 v[44:45], v103 offset0:0 offset1:8\n\t"
        "ds_read2_b32 v[46:47], v103 offset0:16 offset1:24\n\t"
        "s_mov_b64 exec, 1\n\t"
        "ds_add_u32 v2, v3 offset:72\n\t"
        "s_mov_b64 exec, -1\n\t"
        "s_waitcnt lgkmcnt(11)\n\t"
        "buffer_store_dwordx4 v[24:27], v12, s[20:23], s48 offen offset:2048 sc1\n\t"
        "s_waitcnt lgkmcnt(9)\n\t"
        "buffer_store_dwordx4 v[28:31], v12, s[20:23], s48 offen offset:3072 sc1\n\t"
        "s_waitcnt lgkmcnt(7)\n\t"
        "buffer_store_dwordx4 v[32:35], v12, s[20:23], s49 offen offset:0 sc1\n\t"
        "s_waitcnt lgkmcnt(5)\n\t"
        "buffer_store_dwordx4 v[36:39], v12, s[20:23], s49 offen offset:1024 sc1\n\t"
        "s_waitcnt lgkmcnt(3)\n\t"
        "buffer_store_dwordx4 v[40:43], v12, s[20:23], s49 offen offset:2048 sc1\n\t"
        "s_waitcnt lgkmcnt(1)\n\t"
        "buffer_store_dwordx4 v[44:47], v12, s[20:23], s49 offen offset:3072 sc1\n\t"
        "s_mov_b64 exec, 1\n\t"
        "ds_add_u32 v2, v3 offset:44\n\t"
        "s_mov_b64 exec, -1\n\t"
        "s_branch .L7_Q\n"
        ".L7_ov:\n\t"
        "s_mov_b32 s78, 68828\n\t"
        "v_mov_b32 v9, 40\n\t"
        "v_mad_u32_u24 v8, v56, v9, s78\n\t"
        "v_min_u32 v76, v76, v8\n\t"
        "v_mad_u32_u24 v8, v57, v9, s78\n\t"
        "v_min_u32 v77, v77, v8\n\t"
        "v_mad_u32_u24 v8, v58, v9, s78\n\t"
        "v_min_u32 v78, v78, v8\n\t"
        "v_mad_u32_u24 v8, v59, v9, s78\n\t"
        "v_min_u32 v79, v79, v8\n\t"
        "v_mad_u32_u24 v8, v60, v9, s78\n\t"
        "v_min_u32 v80, v80, v8\n\t"
        "v_mad_u32_u24 v8, v61, v9, s78\n\t"
        "v_min_u32 v81, v81, v8\n\t"
        "v_mad_u32_u24 v8, v62, v9, s78\n\t"
        "v_min_u32 v82, v82, v8\n\t"
        "v_mad_u32_u24 v8, v63, v9, s78\n\t"
        "v_min_u32 v83, v83, v8\n\t"
        "v_mad_u32_u24 v8, v64, v9, s78\n\t"
        "v_min_u32 v84, v84, v8\n\t"
        "v_mad_u32_u24 v8, v65, v9, s78\n\t"
        "v_min_u32 v85, v85, v8\n\t"
        "v_mad_u32_u24 v8, v66, v9, s78\n\t"
        "v_min_u32 v86, v86, v8\n\t"
        "v_mad_u32_u24 v8, v67, v9, s78\n\t"
        "v_min_u32 v87, v87, v8\n\t"
        "v_mad_u32_u24 v8, v68, v9, s78\n\t"
        "v_min_u32 v88, v88, v8\n\t"
        "v_mad_u32_u24 v8, v69, v9, s78\n\t"
        "v_min_u32 v89, v89, v8\n\t"
        "v_mad_u32_u24 v8, v70, v9, s78\n\t"
        "v_min_u32 v90, v90, v8\n\t"
        "v_mad_u32_u24 v8, v71, v9, s78\n\t"
        "v_min_u32 v91, v91, v8\n\t"
        "v_mad_u32_u24 v8, v72, v9, s78\n\t"
        "v_min_u32 v92, v92, v8\n\t"
        "v_mad_u32_u24 v8, v73, v9, s78\n\t"
        "v_min_u32 v93, v93, v8\n\t"
        "v_mad_u32_u24 v8, v74, v9, s78\n\t"
        "v_min_u32 v94, v94, v8\n\t"
        "v_mad_u32_u24 v8, v75, v9, s78\n\t"
        "v_min_u32 v95, v95, v8\n\t"
        "s_branch .L7_ov1\n"
        ".L7_mk:\n\t"
        "s_mul_i32 s78, s68, 0x4000400\n\t"
        "s_or_b32 s78, s78, 0xffffff\n\t"
        "v_mov_b32 v6, s78\n\t"
        "v_add_u32 v112, -4, v112\n\t"
        "v_add_u32 v113, -4, v113\n\t"
        "s_mov_b64 exec, s[34:35]\n\t"
        "ds_write_b32 v112, v6\n\t"
        "s_mov_b64 exec, s[36:37]\n\t"
        "ds_write_b32 v113, v6\n\t"
        "s_mov_b64 exec, -1\n\t"
        "s_and_b32 s78, s67, 1\n\t"
        "s_lshl_b32 s78, s78, 24\n\t"
        "v_mov_b32 v6, s78\n\t"
        "ds_xor_b32 v112, v6\n\t"
        "ds_xor_b32 v113, v6\n\t"
        "s_branch .L7_mk1\n"
        ".L7_P2:\n\t"
        "s_setprio 3\n\t"
        "ds_read_b32 v6, v2 offset:1984\n\t"
        "v_lshrrev_b32 v15, 3, v1\n\t"
        "v_mul_u32_u24 v15, 0xa0, v15\n\t"
        "v_add_u32 v15, 68864, v15\n\t"
        "v_and_b32 v4, 7, v1\n\t"
        "v_lshl_add_u32 v5, v4, 4, v15\n\t"
        "v_lshl_add_u32 v15, v4, 2, v15\n\t"
        "s_mov_b32 s34, 0x0c0c0c0c\n\t"
        "s_mov_b32 s35, s34\n\t"
        "s_lshl_b64 s[36:37], s[34:35], 2\n\t"
        "s_lshl_b64 s[38:39], s[34:35], 4\n\t"
        "s_waitcnt lgkmcnt(0)\n\t"
        "s_add_u32 s68, s68, 1\n\t"
        "v_mov_b32 v4, s68\n\t"
        "ds_write_b32 v2, v4 offset:76\n\t"
        "s_cmp_eq_u32 s70, 0\n\t"
        "s_cbranch_scc0 .L7_P3\n\t"
        "s_lshl_b32 s78, s68, 24\n\t"
        "v_mov_b32 v8, s78\n\t"
        "s_xor_b32 s78, s78, 0x5a000000\n\t"
        "v_mov_b32 v9, s78\n\t"
        "v_cndmask_b32 v8, v8, v9, s[34:35]\n\t"
        "s_mul_i32 s78, s68, 0x01010000\n\t"
        "v_mov_b32 v9, s78\n\t"
        "v_cndmask_b32 v8, v8, v9, s[36:37]\n\t"
        "s_mul_i32 s55, s68, 0x4000400\n\t"
        "v_xor_b32 v9, s55, v6\n\t"
        "v_cndmask_b32 v8, v8, v9, s[38:39]\n\t"
        "v_and_b32 v8, 0xff000000, v8\n\t"
        "v_or_b32 v8, 0xff0000, v8\n\t"
        "v_mov_b32 v9, v8\n\t"
        "v_mov_b64 v[10:11], v[8:9]\n\t"
        "ds_write_b128 v5, v[8:11] offset:0\n\t"
        "ds_write_b128 v5, v[8:11] offset:1280\n\t"
        "ds_write_b128 v5, v[8:11] offset:2560\n\t"
        "ds_write_b128 v5, v[8:11] offset:3840\n\t"
        "ds_write_b128 v5, v[8:11] offset:5120\n\t"
        "ds_write_b128 v5, v[8:11] offset:6400\n\t"
        "ds_write_b128 v5, v[8:11] offset:7680\n\t"
        "ds_write_b128 v5, v[8:11] offset:8960\n\t"
        "ds_write_b128 v5, v[8:11] offset:10240\n\t"
        "ds_write_b128 v5, v[8:11] offset:11520\n\t"
        "ds_write_b128 v5, v[8:11] offset:12800\n\t"
        "ds_write_b128 v5, v[8:11] offset:14080\n\t"
        "s_waitcnt lgkmcnt(6)\n\t"
        "ds_write_b128 v5, v[8:11] offset:15360\n\t"
        "ds_write_b128 v5, v[8:11] offset:16640\n\t"
        "ds_write_b128 v5, v[8:11] offset:17920\n\t"
        "ds_write_b128 v5, v[8:11] offset:19200\n"
        ".L7_P3:\n\t"
        "s_mov_b64 exec, 1\n\t"
        "ds_add_u32 v2, v3 offset:104\n\t"
        "s_mov_b64 exec, -1\n\t"
        "v_lshlrev_b32 v12, 4, v1\n\t"
        "s_cmp_eq_u32 s70, 0\n\t"
        "s_cbranch_scc0 .L7_Q\n\t"
        "v_add_u32 v15, 10240, v15\n\t"
        "v_add_u32 v97, 1280, v15\n\t"
        "v_add_u32 v98, 2560, v15\n\t"
        "v_add_u32 v99, 3840, v15\n\t"
        "v_add_u32 v100, 5120, v15\n\t"
        "v_add_u32 v101, 6400, v15\n\t"
        "v_add_u32 v102, 7680, v15\n\t"
        "v_add_u32 v103, 8960, v15\n\t"
        "s_lshl_b32 s48, s71, 14\n\t"
        "s_add_u32 s48, s48, 1056768\n\t"
        "s_add_u32 s49, s48, 4096\n"
        ".L7_w44:\n\t"
        "ds_read_b32 v0, v2 offset:112\n\t"
        "s_waitcnt lgkmcnt(0)\n\t"
        "v_cmp_gt_u16 vcc, 1, v0\n\t"
        "s_cbranch_vccz .L7_d45\n\t"
        "s_sleep 0\n\t"
        "s_sub_i32 s83, s83, 1\n\t"
        "s_cmp_gt_i32 s83, 0\n\t"
        "s_cbranch_scc1 .L7_w44\n"
        ".L7_d45:\n\t"
        "ds_read2_b32 v[16:17], v15 offset0:0 offset1:8\n\t"
        "ds_read2_b32 v[18:19], v15 offset0:16 offset1:24\n\t"
        "ds_read2_b32 v[20:21], v97 offset0:0 offset1:8\n\t"
        "ds_read2_b32 v[22:23], v97 offset0:16 offset1:24\n\t"
        "ds_read2_b32 v[24:25], v98 offset0:0 offset1:8\n\t"
        "ds_read2_b32 v[26:27], v98 offset0:16 offset1:24\n\t"
        "ds_read2_b32 v[28:29], v99 offset0:0 offset1:8\n\t"
        "ds_read2_b32 v[30:31], v99 offset0:16 offset1:24\n\t"
        "ds_read2_b32 v[32:33], v100 offset0:0 offset1:8\n\t"
        "ds_read2_b32 v[34:35], v100 offset0:16 offset1:24\n\t"
        "ds_read2_b32 v[36:37], v101 offset0:0 offset1:8\n\t"
        "ds_read2_b32 v[38:39], v101 offset0:16 offset1:24\n\t"
        "s_waitcnt lgkmcnt(10)\n\t"
        "buffer_store_dwordx4 v[16:19], v12, s[20:23], s48 offen offset:0 sc1\n\t"
        "ds_read2_b32 v[40:41], v102 offset0:0 offset1:8\n\t"
        "ds_read2_b32 v[42:43], v102 offset0:16 offset1:24\n\t"
        "s_waitcnt lgkmcnt(10)\n\t"
        "buffer_store_dwordx4 v[20:23], v12, s[20:23], s48 offen offset:1024 sc1\n\t"
        "ds_read2_b32 v[44:45], v103 offset0:0 offset1:8\n\t"
        "ds_read2_b32 v[46:47], v103 offset0:16 offset1:24\n\t"
        "s_mov_b64 exec, 1\n\t"
        "ds_add_u32 v2, v3 offset:72\n\t"
        "s_mov_b64 exec, -1\n\t"
        "s_waitcnt lgkmcnt(11)\n\t"
        "buffer_store_dwordx4 v[24:27], v12, s[20:23], s48 offen offset:2048 sc1\n\t"
        "s_waitcnt lgkmcnt(9)\n\t"
        "buffer_store_dwordx4 v[28:31], v12, s[20:23], s48 offen offset:3072 sc1\n\t"
        "s_waitcnt lgkmcnt(7)\n\t"
        "buffer_store_dwordx4 v[32:35], v12, s[20:23], s49 offen offset:0 sc1\n\t"
        "s_waitcnt lgkmcnt(5)\n\t"
        "buffer_store_dwordx4 v[36:39], v12, s[20:23], s49 offen offset:1024 sc1\n\t"
        "s_waitcnt lgkmcnt(3)\n\t"
        "buffer_store_dwordx4 v[40:43], v12, s[20:23], s49 offen offset:2048 sc1\n\t"
        "s_waitcnt lgkmcnt(1)\n\t"
        "buffer_store_dwordx4 v[44:47], v12, s[20:23], s49 offen offset:3072 sc1\n\t"
        "s_mov_b64 exec, 1\n\t"
        "ds_add_u32 v2, v3 offset:44\n\t"
        "s_mov_b64 exec, -1\n\t"
        "s_branch .L7_Q\n"
        ".L7_V2:\n\t"
        "v_and_b32 v5, 7, v1\n\t"
        "v_mul_u32_u24 v5, 0x420, v5\n\t"
        "v_and_b32 v6, 8, v1\n\t"
        "v_lshl_add_u32 v5, v6, 6, v5\n\t"
        "v_and_b32 v6, 48, v1\n\t"
        "v_add_u32 v5, v5, v6\n\t"
        "v_add_u32 v5, 68864, v5\n\t"
        "s_lshr_b32 s78, s81, 1\n\t"
        "s_lshl3_add_u32 s78, s78, s80\n\t"
        "s_and_b32 s79, s81, 1\n\t"
        "s_lshl1_add_u32 s78, s78, s79\n\t"
        "s_lshl_b32 s82, s78, 10\n\t"
        "s_add_u32 s82, s82, 131072\n"
        ".L7_w40:\n\t"
        "ds_read_b32 v0, v2 offset:104\n\t"
        "ds_read_b32 v6, v2 offset:76\n\t"
        "s_waitcnt lgkmcnt(1)\n\t"
        "v_cmp_gt_u16 vcc, 1, v0\n\t"
        "s_cbranch_vccz .L7_d41\n\t"
        "s_waitcnt lgkmcnt(0)\n\t"
        "s_sleep 1\n\t"
        "s_sub_i32 s83, s83, 1\n\t"
        "s_cmp_gt_i32 s83, 0\n\t"
        "s_cbranch_scc1 .L7_w40\n"
        ".L7_d41:\n\t"
        "s_waitcnt lgkmcnt(0)\n\t"
        "v_readfirstlane_b32 s68, v6\n\t"
        "v_mov_b32 v7, 2\n\t"
        "s_and_b32 s73, s68, 1\n\t"
        "s_cselect_b32 s73, 1, 0x10000\n\t"
        "v_mov_b32 v16, s73\n\t"
        "v_and_b32 v17, 7, v1\n\t"
        "v_add_u32 v17, 1, v17\n\t"
        "v_lshrrev_b32 v17, v17, s68\n\t"
        "s_mov_b32 s49, 0x10001\n\t"
        "v_and_b32 v17, s49, v17\n\t"
        "s_mov_b32 s48, 0x3db8aa3b\n\t"
        "s_waitcnt vmcnt(0)\n\t"
        "ds_read_b128 v[32:35], v5 offset:0\n\t"
        "ds_read_b128 v[36:39], v5 offset:8448\n\t"
        "ds_read_b128 v[40:43], v5 offset:16896\n\t"
        "ds_read_b128 v[44:47], v5 offset:64\n\t"
        "ds_read_b128 v[48:51], v5 offset:8512\n\t"
        "ds_read_b128 v[52:55], v5 offset:16960\n\t"
        "ds_read_b128 v[56:59], v5 offset:128\n\t"
        "ds_read_b128 v[60:63], v5 offset:8576\n\t"
        "ds_read_b128 v[64:67], v5 offset:17024\n\t"
        "ds_read_b128 v[68:71], v5 offset:192\n\t"
        "ds_read_b128 v[72:75], v5 offset:8640\n\t"
        "ds_read_b128 v[76:79], v5 offset:17088\n\t"
        "s_waitcnt lgkmcnt(9)\n\t"
        "v_mfma_f32_16x16x32_bf16 v[20:23], v[36:39], v[32:35], 0\n\t"
        "v_mfma_f32_16x16x32_bf16 v[24:27], v[40:43], v[32:35], 0\n\t"
        "ds_read_b128 v[80:83], v5 offset:256\n\t"
        "ds_read_b128 v[84:87], v5 offset:8704\n\t"
        "ds_read_b128 v[88:91], v5 offset:17152\n\t"
        "s_waitcnt lgkmcnt(9)\n\t"
        "v_mfma_f32_16x16x32_bf16 v[20:23], v[48:51], v[44:47], v[20:23]\n\t"
        "v_mfma_f32_16x16x32_bf16 v[24:27], v[52:55], v[44:47], v[24:27]\n\t"
        "ds_read_b128 v[92:95], v5 offset:320\n\t"
        "ds_read_b128 v[96:99], v5 offset:8768\n\t"
        "ds_read_b128 v[100:103], v5 offset:17216\n\t"
        "s_waitcnt lgkmcnt(9)\n\t"
        "v_mfma_f32_16x16x32_bf16 v[20:23], v[60:63], v[56:59], v[20:23]\n\t"
        "v_mfma_f32_16x16x32_bf16 v[24:27], v[64:67], v[56:59], v[24:27]\n\t"
        "ds_read_b128 v[104:107], v5 offset:384\n\t"
        "ds_read_b128 v[108:111], v5 offset:8832\n\t"
        "ds_read_b128 v[112:115], v5 offset:17280\n\t"
        "s_waitcnt lgkmcnt(9)\n\t"
        "v_mfma_f32_16x16x32_bf16 v[20:23], v[72:75], v[68:71], v[20:23]\n\t"
        "v_mfma_f32_16x16x32_bf16 v[24:27], v[76:79], v[68:71], v[24:27]\n\t"
        "ds_read_b128 v[116:119], v5 offset:448\n\t"
        "ds_read_b128 v[120:123], v5 offset:8896\n\t"
        "ds_read_b128 v[124:127], v5 offset:17344\n\t"
        "s_waitcnt lgkmcnt(9)\n\t"
        "v_mfma_f32_16x16x32_bf16 v[20:23], v[84:87], v[80:83], v[20:23]\n\t"
        "v_mfma_f32_16x16x32_bf16 v[24:27], v[88:91], v[80:83], v[24:27]\n\t"
        "s_mov_b64 exec, 1\n\t"
        "ds_add_u32 v2, v7 offset:72\n\t"
        "s_mov_b64 exec, -1\n\t"
        "s_waitcnt lgkmcnt(7)\n\t"
        "v_mfma_f32_16x16x32_bf16 v[20:23], v[96:99], v[92:95], v[20:23]\n\t"
        "v_mfma_f32_16x16x32_bf16 v[24:27], v[100:103], v[92:95], v[24:27]\n\t"
        "s_waitcnt lgkmcnt(4)\n\t"
        "v_mfma_f32_16x16x32_bf16 v[20:23], v[108:111], v[104:107], v[20:23]\n\t"
        "v_mfma_f32_16x16x32_bf16 v[24:27], v[112:115], v[104:107], v[24:27]\n\t"
        "s_waitcnt lgkmcnt(1)\n\t"
        "v_mfma_f32_16x16x32_bf16 v[20:23], v[120:123], v[116:119], v[20:23]\n\t"
        "v_mfma_f32_16x16x32_bf16 v[24:27], v[124:127], v[116:119], v[24:27]\n\t"
        "s_nop 6\n\t"
        "v_mul_f32 v20, s48, v20\n\t"
        "v_mul_f32 v21, s48, v21\n\t"
        "v_mul_f32 v22, s48, v22\n\t"
        "v_mul_f32 v23, s48, v23\n\t"
        "v_mul_f32 v24, s48, v24\n\t"
        "v_mul_f32 v25, s48, v25\n\t"
        "v_mul_f32 v26, s48, v26\n\t"
        "v_mul_f32 v27, s48, v27\n\t"
        "s_mov_b32 s48, 0x07060302\n\t"
        "v_add_u32 v20, 0x10000, v20\n\t"
        "v_add_u32 v21, 0x10000, v21\n\t"
        "v_add_u32 v22, 0x10000, v22\n\t"
        "v_add_u32 v23, 0x10000, v23\n\t"
        "v_perm_b32 v20, v21, v20, s48\n\t"
        "v_perm_b32 v21, v23, v22, s48\n\t"
        "v_cvt_pk_bf16_f32 v22, v24, v25\n\t"
        "v_cvt_pk_bf16_f32 v23, v26, v27\n\t"
        "v_bfi_b32 v20, s49, v16, v20\n\t"
        "v_bfi_b32 v21, s49, v17, v21\n\t"
        "buffer_store_dwordx4 v[20:23], v12, s[20:23], s82 offen sc1\n\t"
        "s_mov_b64 exec, 1\n\t"
        "ds_add_u32 v2, v7 offset:44\n\t"
        "s_mov_b64 exec, -1\n\t"
        "s_branch .L7_Q\n"
        ".L7_T2:\n\t"
        "v_lshlrev_b32 v4, 4, v1\n\t"
        "v_add_u32 v4, 148160, v4\n\t"
        "v_mul_u32_u24 v5, 196, v1\n\t"
        "v_add_u32 v5, 148160, v5\n\t"
        "v_mov_b32 v12, 0xffff\n\t"
        "v_mov_b32 v13, v12\n\t"
        "v_mov_b64 v[14:15], v[12:13]\n\t"
        "ds_write_b128 v4, v[12:15] offset:0\n\t"
        "ds_write_b128 v4, v[12:15] offset:1024\n\t"
        "ds_write_b128 v4, v[12:15] offset:2048\n\t"
        "ds_write_b128 v4, v[12:15] offset:3072\n\t"
        "ds_write_b128 v4, v[12:15] offset:4096\n\t"
        "ds_write_b128 v4, v[12:15] offset:5120\n\t"
        "ds_write_b128 v4, v[12:15] offset:6144\n\t"
        "ds_write_b128 v4, v[12:15] offset:7168\n\t"
        "ds_write_b128 v4, v[12:15] offset:8192\n\t"
        "ds_write_b128 v4, v[12:15] offset:9216\n\t"
        "ds_write_b128 v4, v[12:15] offset:10240\n\t"
        "ds_write_b128 v4, v[12:15] offset:11264\n\t"
        "s_waitcnt lgkmcnt(6)\n\t"
        "ds_write_b128 v4, v[12:15] offset:12288\n\t"
        "ds_write_b128 v4, v[12:15] offset:13312\n\t"
        "ds_write_b128 v4, v[12:15] offset:14336\n\t"
        "ds_write_b128 v4, v[12:15] offset:15360\n\t"
        "ds_write_b32 v5, v2 offset:192\n\t"
        "s_mov_b64 exec, 0xffff\n\t"
        "ds_write_b32 v5, v2 offset:12736\n\t"
        "s_mov_b64 exec, 1\n\t"
        "ds_add_u32 v2, v3 offset:8\n\t"
        "s_mov_b64 exec, -1\n\t"
        "s_and_b32 s82, s82, -1024\n\t"
        "v_mov_b32 v9, 4\n"
        ".L7_g42:\n\t"
        "s_memrealtime s[78:79]\n\t"
        "s_waitcnt lgkmcnt(0)\n\t"
        "s_sub_u32 s78, s78, s74\n\t"
        "s_cmp_ge_i32 s78, 150\n\t"
        "s_cbranch_scc1 .L7_h43\n\t"
        "s_sleep 1\n\t"
        "s_branch .L7_g42\n"
        ".L7_h43:\n"
        ".L7_b1:\n\t"
        "s_buffer_load_dword s40, s[20:23], s82 offset:0 glc\n\t"
        "s_buffer_load_dword s41, s[20:23], s82 offset:128 glc\n\t"
        "s_buffer_load_dword s42, s[20:23], s82 offset:256 glc\n\t"
        "s_buffer_load_dword s43, s[20:23], s82 offset:384 glc\n\t"
        "s_buffer_load_dword s44, s[20:23], s82 offset:512 glc\n\t"
        "s_buffer_load_dword s45, s[20:23], s82 offset:640 glc\n\t"
        "s_buffer_load_dword s46, s[20:23], s82 offset:768 glc\n\t"
        "s_buffer_load_dword s47, s[20:23], s82 offset:896 glc\n\t"
        "s_waitcnt lgkmcnt(0)\n\t"
        "s_mov_b32 s80, 0\n\t"
        "s_mov_b32 s81, 0\n\t"
        "s_sub_u32 s40, s40, s74\n\t"
        "s_max_i32 s80, s80, s40\n\t"
        "s_min_i32 s81, s81, s40\n\t"
        "s_sub_u32 s41, s41, s74\n\t"
        "s_max_i32 s80, s80, s41\n\t"
        "s_min_i32 s81, s81, s41\n\t"
        "s_sub_u32 s42, s42, s74\n\t"
        "s_max_i32 s80, s80, s42\n\t"
        "s_min_i32 s81, s81, s42\n\t"
        "s_sub_u32 s43, s43, s74\n\t"
        "s_max_i32 s80, s80, s43\n\t"
        "s_min_i32 s81, s81, s43\n\t"
        "s_sub_u32 s44, s44, s74\n\t"
        "s_max_i32 s80, s80, s44\n\t"
        "s_min_i32 s81, s81, s44\n\t"
        "s_sub_u32 s45, s45, s74\n\t"
        "s_max_i32 s80, s80, s45\n\t"
        "s_min_i32 s81, s81, s45\n\t"
        "s_sub_u32 s46, s46, s74\n\t"
        "s_max_i32 s80, s80, s46\n\t"
        "s_min_i32 s81, s81, s46\n\t"
        "s_sub_u32 s47, s47, s74\n\t"
        "s_max_i32 s80, s80, s47\n\t"
        "s_min_i32 s81, s81, s47\n\t"
        "s_sub_u32 s81, s80, s81\n\t"
        "s_cmp_lt_u32 s81, 0x3e8\n\t"
        "s_cbranch_scc1 .L7_b2\n\t"
        "s_mov_b64 exec, 1\n\t"
        "ds_add_u32 v2, v9 offset:84\n\t"
        "s_mov_b64 exec, -1\n\t"
        "s_mov_b64 exec, 1\n\t"
        "ds_add_u32 v2, v9 offset:108\n\t"
        "s_mov_b64 exec, -1\n\t"
        "v_mov_b32 v9, 0\n\t"
        "s_mov_b32 s80, 0\n\t"
        "s_sleep 4\n\t"
        "s_memrealtime s[78:79]\n\t"
        "s_waitcnt lgkmcnt(0)\n\t"
        "s_sub_u32 s78, s78, s74\n\t"
        "s_cmp_lt_u32 s78, 0x4b0\n\t"
        "s_cbranch_scc1 .L7_b1\n"
        ".L7_b2:\n\t"
        "s_add_u32 s80, s80, 1\n\t"
        "v_mov_b32 v7, s80\n\t"
        "s_mov_b64 exec, 1\n\t"
        "ds_add_u32 v2, v7 offset:92\n\t"
        "s_mov_b64 exec, -1\n\t"
        "s_branch .L7_Q0\n"
        ".L7_R2:\n\t"
        "s_sub_u32 s79, s64, s72\n\t"
        "s_min_i32 s79, s79, s65\n\t"
        "s_max_i32 s79, s79, 0\n\t"
        "v_mov_b32 v32, v1\n\t"
        "v_add_u32 v33, 64, v1\n\t"
        "v_cmp_gt_u32 s[32:33], s79, v32\n\t"
        "v_cmp_gt_u32 s[34:35], s79, v33\n\t"
        "v_cmp_gt_u32 s[36:37], 40, v1\n\t"
        "s_sub_u32 s78, 0xff, s65\n\t"
        "v_add_u32 v34, s78, v32\n\t"
        "v_add_u32 v6, 0xffffffb0, v34\n\t"
        "v_cmp_gt_u32 vcc, 0xff, v34\n\t"
        "s_nop 1\n\t"
        "v_cndmask_b32 v34, v6, v34, vcc\n\t"
        "v_add_u32 v35, s78, v33\n\t"
        "v_add_u32 v6, 0xffffffb0, v35\n\t"
        "v_cmp_gt_u32 vcc, 0xff, v35\n\t"
        "s_nop 1\n\t"
        "v_cndmask_b32 v35, v6, v35, vcc\n\t"
        "v_mov_b32 v12, 0x8000\n\t"
        "v_cndmask_b32 v44, v12, 0, s[32:33]\n\t"
        "v_cndmask_b32 v45, v12, 0, s[34:35]\n\t"
        "v_cndmask_b32 v46, 63, 31, s[36:37]\n\t"
        "v_mov_b32 v47, 63\n\t"
        "s_waitcnt vmcnt(0)\n\t"
        "v_lshlrev_b32 v16, 16, v16\n\t"
        "v_lshlrev_b32 v17, 16, v17\n\t"
        "v_mul_f32 v36, v16, v16\n\t"
        "v_mul_f32 v37, v17, v17\n\t"
        "v_rcp_f32 v36, v36\n\t"
        "v_rcp_f32 v37, v37\n\t"
        "v_or3_b32 v16, v16, v34, v44\n\t"
        "v_or3_b32 v17, v17, v35, v45\n\t"
        "v_add_f32 v36, 0xc0600000, v36\n\t"
        "v_add_f32 v37, 0xc0600000, v37\n\t"
        "v_cvt_u32_f32 v36, v36\n\t"
        "v_cvt_u32_f32 v37, v37\n\t"
        "v_min_u32 v36, 31, v36\n\t"
        "v_min_u32 v37, 31, v37\n\t"
        "v_cndmask_b32 v36, 0, v36, s[32:33]\n\t"
        "v_cndmask_b32 v37, 0, v37, s[34:35]\n\t"
        "v_sub_u32 v36, v46, v36\n\t"
        "v_sub_u32 v37, v47, v37\n\t"
        "v_lshlrev_b32 v36, 2, v36\n\t"
        "v_lshlrev_b32 v37, 2, v37\n\t"
        "ds_add_rtn_u32 v40, v36, v3 offset:256\n\t"
        "s_mov_b64 exec, 0xffff\n\t"
        "ds_add_rtn_u32 v41, v37, v3 offset:256\n\t"
        "s_mov_b64 exec, -1\n\t"
        "v_lshlrev_b32 v9, 2, v1\n\t"
        "ds_read_b32 v10, v9 offset:256\n\t"
        "s_cmp_eq_u32 s70, 0\n\t"
        "s_cselect_b32 s78, 56, 0\n\t"
        "s_cselect_b32 s79, 0, 56\n\t"
        "v_mov_b32 v49, -40\n\t"
        "v_cndmask_b32 v48, v49, 0, s[36:37]\n\t"
        "s_mov_b32 s40, 0x23108954\n\t"
        "s_mov_b32 s41, 0x67\n\t"
        "s_mov_b32 s42, 0x1986732\n\t"
        "s_mov_b32 s43, 0x45\n\t"
        "v_mov_b32 v13, s78\n\t"
        "v_mov_b32 v14, s79\n\t"
        "v_cndmask_b32 v13, v14, v13, s[36:37]\n\t"
        "s_waitcnt lgkmcnt(0)\n\t"
        "v_mov_b32 v11, v10\n\t"
        "s_nop 1\n\t"
        "v_add_u32_dpp v11, v11, v11 row_shr:1 row_mask:0xf bank_mask:0xf bound_ctrl:1\n\t"
        "s_nop 1\n\t"
        "v_add_u32_dpp v11, v11, v11 row_shr:2 row_mask:0xf bank_mask:0xf bound_ctrl:1\n\t"
        "s_nop 1\n\t"
        "v_add_u32_dpp v11, v11, v11 row_shr:4 row_mask:0xf bank_mask:0xf bound_ctrl:1\n\t"
        "s_nop 1\n\t"
        "v_add_u32_dpp v11, v11, v11 row_shr:8 row_mask:0xf bank_mask:0xf bound_ctrl:1\n\t"
        "s_nop 1\n\t"
        "v_add_u32_dpp v11, v11, v11 row_bcast:15 row_mask:0xa bank_mask:0xf\n\t"
        "s_nop 1\n\t"
        "v_add_u32_dpp v11, v11, v11 row_bcast:31 row_mask:0xc bank_mask:0xf\n\t"
        "v_sub_u32 v11, v11, v10\n\t"
        "ds_bpermute_b32 v36, v36, v11\n\t"
        "ds_bpermute_b32 v37, v37, v11\n\t"
        "v_lshlrev_b32 v9, 1, v1\n\t"
        "s_waitcnt lgkmcnt(0)\n\t"
        "v_add3_u32 v36, v36, v40, v48\n\t"
        "v_and_b32 v6, -4, v36\n\t"
        "v_lshrrev_b64 v[10:11], v6, s[40:41]\n\t"
        "v_lshrrev_b64 v[50:51], v6, s[42:43]\n\t"
        "v_cmp_eq_u32 vcc, 0, v13\n\t"
        "s_nop 1\n\t"
        "v_cndmask_b32 v10, v10, v50, vcc\n\t"
        "v_and_b32 v10, 15, v10\n\t"
        "v_and_b32 v6, 3, v36\n\t"
        "v_lshl_add_u32 v6, v10, 2, v6\n\t"
        "v_add_u32 v36, v6, v13\n\t"
        "v_and_b32 v6, 15, v36\n\t"
        "v_lshlrev_b32 v6, 4, v6\n\t"
        "v_lshl_add_u32 v6, v36, 8, v6\n\t"
        "v_and_b32 v10, 8, v13\n\t"
        "v_lshlrev_b32 v10, 8, v10\n\t"
        "v_sub_u32 v6, v6, v10\n\t"
        "v_lshlrev_b32 v36, 2, v36\n\t"
        "ds_write_b32 v36, v16 offset:512\n\t"
        "ds_write_b16 v9, v6 offset:1792\n\t"
        "v_add3_u32 v37, v37, v41, v49\n\t"
        "v_and_b32 v6, -4, v37\n\t"
        "v_lshrrev_b64 v[10:11], v6, s[40:41]\n\t"
        "v_lshrrev_b64 v[50:51], v6, s[42:43]\n\t"
        "v_cmp_eq_u32 vcc, 0, v14\n\t"
        "s_nop 1\n\t"
        "v_cndmask_b32 v10, v10, v50, vcc\n\t"
        "v_and_b32 v10, 15, v10\n\t"
        "v_and_b32 v6, 3, v37\n\t"
        "v_lshl_add_u32 v6, v10, 2, v6\n\t"
        "v_add_u32 v37, v6, v14\n\t"
        "v_and_b32 v6, 15, v37\n\t"
        "v_lshlrev_b32 v6, 4, v6\n\t"
        "v_lshl_add_u32 v6, v37, 8, v6\n\t"
        "v_and_b32 v10, 8, v14\n\t"
        "v_lshlrev_b32 v10, 8, v10\n\t"
        "v_sub_u32 v6, v6, v10\n\t"
        "v_lshlrev_b32 v37, 2, v37\n\t"
        "s_mov_b64 exec, 0xffff\n\t"
        "ds_write_b32 v37, v17 offset:512\n\t"
        "ds_write_b16 v9, v6 offset:1920\n\t"
        "s_mov_b64 exec, 1\n\t"
        "ds_add_u32 v2, v3 offset:8\n\t"
        "s_mov_b64 exec, -1\n"
        ".L7_w46:\n\t"
        "ds_read_b64 v[4:5], v2 offset:72\n\t"
        "s_waitcnt lgkmcnt(0)\n\t"
        "v_cmp_gt_u16 vcc, 2, v4\n\t"
        "s_cbranch_vccz .L7_d47\n\t"
        "s_sleep 2\n\t"
        "s_sub_i32 s83, s83, 1\n\t"
        "s_cmp_gt_i32 s83, 0\n\t"
        "s_cbranch_scc1 .L7_w46\n"
        ".L7_d47:\n\t"
        "v_readfirstlane_b32 s68, v5\n\t"
        "v_lshlrev_b32 v22, 4, v1\n\t"
        "v_add_u32 v9, 142848, v22\n\t"
        "v_and_b32 v10, 60, v1\n\t"
        "ds_read_b32 v24, v10 offset:704\n\t"
        "ds_read_b32 v25, v10 offset:768\n\t"
        "ds_read_b32 v26, v10 offset:832\n\t"
        "v_and_b32 v11, 15, v1\n\t"
        "v_lshrrev_b32 v12, 4, v1\n\t"
        "v_xor_b32 v16, v11, v12\n\t"
        "v_lshlrev_b32 v16, 4, v16\n\t"
        "v_lshl_add_u32 v16, v11, 8, v16\n\t"
        "v_xor_b32 v17, 64, v16\n\t"
        "v_xor_b32 v18, 128, v16\n\t"
        "v_xor_b32 v19, 192, v16\n\t"
        "ds_write_b128 v9, v[16:19]\n\t"
        "s_and_b32 s73, s68, 1\n\t"
        "s_cselect_b32 s73, 1, 0x10000\n\t"
        "v_mov_b32 v20, s73\n\t"
        "v_and_b32 v21, 7, v1\n\t"
        "v_add_u32 v21, 1, v21\n\t"
        "v_lshrrev_b32 v21, v21, s68\n\t"
        "s_mov_b32 s49, 0x10001\n\t"
        "v_and_b32 v21, s49, v21\n\t"
        "v_and_b32 v23, 0x37f, v22\n\t"
        "ds_write_b128 v9, v[20:23] offset:1024\n\t"
        "s_add_u32 s78, s72, s65\n\t"
        "s_sub_u32 s78, s78, 0xff\n\t"
        "s_lshl_b32 s78, s78, 9\n\t"
        "v_and_b32 v13, 48, v22\n\t"
        "v_add_u32 v14, s78, v13\n\t"
        "s_movk_i32 s78, 0x200\n\t"
        "v_bfe_u32 v27, v1, 1, 2\n\t"
        "v_xor_b32 v27, v27, v12\n\t"
        "v_lshlrev_b32 v27, 4, v27\n\t"
        "v_lshl_add_u32 v27, v11, 6, v27\n\t"
        "v_lshlrev_b32 v28, 1, v1\n\t"
        "v_and_b32 v28, 48, v28\n\t"
        "v_xor_b32 v28, v28, v22\n\t"
        "s_waitcnt lgkmcnt(2)\n\t"
        "v_mad_u32_u16 v24, v24, s78, v14\n\t"
        "v_mad_u32_u16 v25, v25, s78, v14\n\t"
        "v_mad_u32_u16 v26, v26, s78, v14\n\t"
        "s_mov_b32 exec_hi, 0\n\t"
        "v_mov_b32 v24, -2.0\n\t"
        "s_mov_b64 exec, -1\n\t"
        "ds_write_b128 v9, v[24:27] offset:2048\n\t"
        "s_mov_b32 s78, 0x3f80\n\t"
        "s_mov_b32 s79, 0\n\t"
        "v_lshlrev_b64 v[30:31], v13, s[78:79]\n\t"
        "s_mov_b32 s78, 0x00f0000f\n\t"
        "s_mov_b32 s79, 0xf0000f00\n\t"
        "v_cndmask_b32 v30, 0, v30, s[78:79]\n\t"
        "v_cndmask_b32 v31, 0, v31, s[78:79]\n\t"
        "ds_write_b128 v9, v[28:31] offset:3072\n\t"
        "s_branch .L7_Q\n"
        ".L7_Q0:\n"
        ".L7_w48:\n\t"
        "ds_read_b64 v[4:5], v2 offset:72\n\t"
        "s_waitcnt lgkmcnt(0)\n\t"
        "v_cmp_gt_u16 vcc, 2, v4\n\t"
        "s_cbranch_vccz .L7_d49\n\t"
        "s_sleep 2\n\t"
        "s_sub_i32 s83, s83, 1\n\t"
        "s_cmp_gt_i32 s83, 0\n\t"
        "s_cbranch_scc1 .L7_w48\n"
        ".L7_d49:\n\t"
        "v_readfirstlane_b32 s68, v5\n"
        ".L7_Q:\n\t"
        "s_setprio 2\n\t"
        "v_and_b32 v5, 7, v1\n\t"
        "v_mul_u32_u24 v5, 0x420, v5\n\t"
        "v_and_b32 v6, 8, v1\n\t"
        "v_lshl_add_u32 v5, v6, 6, v5\n\t"
        "v_and_b32 v6, 48, v1\n\t"
        "v_add_u32 v5, v5, v6\n\t"
        "s_and_b32 s73, s68, 1\n\t"
        "s_cselect_b32 s73, 1, 0x10000\n\t"
        "v_and_b32 v6, 7, v1\n\t"
        "v_add_u32 v6, 1, v6\n\t"
        "v_lshrrev_b32 v6, v6, s68\n\t"
        "s_mov_b32 s49, 0x10001\n\t"
        "v_and_b32 v6, s49, v6\n\t"
        "s_add_u32 s80, s69, s76\n\t"
        "s_and_b32 s80, s80, 3\n\t"
        "s_lshl2_add_u32 s78, s70, s80\n\t"
        "s_lshl_b32 s78, s78, 14\n\t"
        "s_add_u32 s78, s78, 131072\n\t"
        "v_lshl_add_u32 v4, v1, 4, s78\n\t"
        "s_mov_b32 s24, 4096\n\t"
        "s_mov_b32 s25, 8192\n\t"
        "s_mov_b32 s26, 12288\n"
        ".L7_w50:\n\t"
        "ds_read_b32 v0, v2 offset:92\n\t"
        "s_waitcnt lgkmcnt(0)\n\t"
        "v_cmp_gt_u16 vcc, 1, v0\n\t"
        "s_cbranch_vccz .L7_d51\n\t"
        "s_sleep 1\n\t"
        "s_sub_i32 s83, s83, 1\n\t"
        "s_cmp_gt_i32 s83, 0\n\t"
        "s_cbranch_scc1 .L7_w50\n"
        ".L7_d51:\n\t"
        "v_readfirstlane_b32 s78, v0\n\t"
        "s_sub_u32 s78, s78, 1\n\t"
        "s_add_u32 s77, s74, s78\n"
        ".L7_g52:\n\t"
        "s_memrealtime s[78:79]\n\t"
        "s_waitcnt lgkmcnt(0)\n\t"
        "s_sub_u32 s78, s78, s77\n\t"
        "s_cmp_ge_i32 s78, 280\n\t"
        "s_cbranch_scc1 .L7_h53\n\t"
        "s_sleep 1\n\t"
        "s_branch .L7_g52\n"
        ".L7_h53:\n"
        ".L7_w54:\n\t"
        "ds_read_b32 v0, v2 offset:88\n\t"
        "s_waitcnt lgkmcnt(0)\n\t"
        "v_cmp_gt_u16 vcc, 8, v0\n\t"
        "s_cbranch_vccz .L7_d55\n\t"
        "s_sleep 1\n\t"
        "s_sub_i32 s83, s83, 1\n\t"
        "s_cmp_gt_i32 s83, 0\n\t"
        "s_cbranch_scc1 .L7_w54\n"
        ".L7_d55:\n\t"
        "buffer_load_dwordx4 v[64:67], v4, s[20:23], 0 offen offset:0 sc1\n\t"
        "buffer_load_dwordx4 v[68:71], v4, s[20:23], 0 offen offset:1024 sc1\n\t"
        "buffer_load_dwordx4 v[72:75], v4, s[20:23], 0 offen offset:2048 sc1\n\t"
        "buffer_load_dwordx4 v[76:79], v4, s[20:23], 0 offen offset:3072 sc1\n\t"
        "buffer_load_dwordx4 v[80:83], v4, s[20:23], s24 offen offset:0 sc1\n\t"
        "buffer_load_dwordx4 v[84:87], v4, s[20:23], s24 offen offset:1024 sc1\n\t"
        "buffer_load_dwordx4 v[88:91], v4, s[20:23], s24 offen offset:2048 sc1\n\t"
        "buffer_load_dwordx4 v[92:95], v4, s[20:23], s24 offen offset:3072 sc1\n\t"
        "buffer_load_dwordx4 v[96:99], v4, s[20:23], s25 offen offset:0 sc1\n\t"
        "buffer_load_dwordx4 v[100:103], v4, s[20:23], s25 offen offset:1024 sc1\n\t"
        "buffer_load_dwordx4 v[104:107], v4, s[20:23], s25 offen offset:2048 sc1\n\t"
        "buffer_load_dwordx4 v[108:111], v4, s[20:23], s25 offen offset:3072 sc1\n\t"
        "buffer_load_dwordx4 v[112:115], v4, s[20:23], s26 offen offset:0 sc1\n\t"
        "buffer_load_dwordx4 v[116:119], v4, s[20:23], s26 offen offset:1024 sc1\n\t"
        "buffer_load_dwordx4 v[120:123], v4, s[20:23], s26 offen offset:2048 sc1\n\t"
        "buffer_load_dwordx4 v[124:127], v4, s[20:23], s26 offen offset:3072 sc1\n\t"
        "s_setprio 3\n\t"
        "s_mov_b64 exec, 1\n\t"
        "ds_add_u32 v2, v3 offset:84\n\t"
        "s_mov_b64 exec, -1\n"
        ".L7_w56:\n\t"
        "ds_read_b32 v0, v2 offset:0\n\t"
        "s_waitcnt lgkmcnt(0)\n\t"
        "v_cmp_gt_u16 vcc, 4, v0\n\t"
        "s_cbranch_vccz .L7_d57\n\t"
        "s_sleep 1\n\t"
        "s_sub_i32 s83, s83, 1\n\t"
        "s_cmp_gt_i32 s83, 0\n\t"
        "s_cbranch_scc1 .L7_w56\n"
        ".L7_d57:\n\t"
        "s_branch .L7_qg\n"
        ".L7_qr:\n\t"
        "s_waitcnt lgkmcnt(0)\n\t"
        "s_sleep 8\n\t"
        "s_sub_i32 s83, s83, 64\n\t"
        "s_cmp_gt_i32 s83, 0\n\t"
        "s_cbranch_scc0 .L7_qd\n\t"
        "buffer_load_dwordx4 v[64:67], v4, s[20:23], 0 offen offset:0 sc1\n\t"
        "buffer_load_dwordx4 v[68:71], v4, s[20:23], 0 offen offset:1024 sc1\n\t"
        "buffer_load_dwordx4 v[72:75], v4, s[20:23], 0 offen offset:2048 sc1\n\t"
        "buffer_load_dwordx4 v[76:79], v4, s[20:23], 0 offen offset:3072 sc1\n\t"
        "buffer_load_dwordx4 v[80:83], v4, s[20:23], s24 offen offset:0 sc1\n\t"
        "buffer_load_dwordx4 v[84:87], v4, s[20:23], s24 offen offset:1024 sc1\n\t"
        "buffer_load_dwordx4 v[88:91], v4, s[20:23], s24 offen offset:2048 sc1\n\t"
        "buffer_load_dwordx4 v[92:95], v4, s[20:23], s24 offen offset:3072 sc1\n\t"
        "buffer_load_dwordx4 v[96:99], v4, s[20:23], s25 offen offset:0 sc1\n\t"
        "buffer_load_dwordx4 v[100:103], v4, s[20:23], s25 offen offset:1024 sc1\n\t"
        "buffer_load_dwordx4 v[104:107], v4, s[20:23], s25 offen offset:2048 sc1\n\t"
        "buffer_load_dwordx4 v[108:111], v4, s[20:23], s25 offen offset:3072 sc1\n\t"
        "buffer_load_dwordx4 v[112:115], v4, s[20:23], s26 offen offset:0 sc1\n\t"
        "buffer_load_dwordx4 v[116:119], v4, s[20:23], s26 offen offset:1024 sc1\n\t"
        "buffer_load_dwordx4 v[120:123], v4, s[20:23], s26 offen offset:2048 sc1\n\t"
        "buffer_load_dwordx4 v[124:127], v4, s[20:23], s26 offen offset:3072 sc1\n"
        ".L7_qg:\n\t"
        "v_mov_b32 v7, 0\n\t"
        "ds_read_b128 v[8:11], v5 offset:2048\n\t"
        "ds_read_b128 v[12:15], v5 offset:10496\n\t"
        "ds_read_b128 v[16:19], v5 offset:18944\n\t"
        "ds_read_b128 v[20:23], v5 offset:27392\n\t"
        "s_waitcnt vmcnt(14) lgkmcnt(3)\n\t"
        "v_xor_b32 v64, s73, v64\n\t"
        "v_xor_b32 v65, v6, v65\n\t"
        "v_xor_b32 v68, s73, v68\n\t"
        "v_xor_b32 v69, v6, v69\n\t"
        "v_mfma_f32_16x16x32_bf16 v[24:27], v[64:67], v[8:11], 0\n\t"
        "s_nop 0\n\t"
        "v_mfma_f32_16x16x32_bf16 v[28:31], v[68:71], v[8:11], 0\n\t"
        "v_or3_b32 v7, v7, v64, v65\n\t"
        "ds_read_b128 v[8:11], v5 offset:35840\n\t"
        "s_waitcnt lgkmcnt(3)\n\t"
        "v_mfma_f32_16x16x32_bf16 v[32:35], v[64:67], v[12:15], 0\n\t"
        "v_mfma_f32_16x16x32_bf16 v[36:39], v[68:71], v[12:15], 0\n\t"
        "v_or3_b32 v7, v7, v68, v69\n\t"
        "ds_read_b128 v[12:15], v5 offset:2112\n\t"
        "s_waitcnt lgkmcnt(3)\n\t"
        "v_mfma_f32_16x16x32_bf16 v[40:43], v[64:67], v[16:19], 0\n\t"
        "v_mfma_f32_16x16x32_bf16 v[44:47], v[68:71], v[16:19], 0\n\t"
        "ds_read_b128 v[16:19], v5 offset:10560\n\t"
        "s_waitcnt lgkmcnt(3)\n\t"
        "v_mfma_f32_16x16x32_bf16 v[48:51], v[64:67], v[20:23], 0\n\t"
        "v_mfma_f32_16x16x32_bf16 v[52:55], v[68:71], v[20:23], 0\n\t"
        "ds_read_b128 v[20:23], v5 offset:19008\n\t"
        "s_waitcnt vmcnt(12) lgkmcnt(3)\n\t"
        "v_xor_b32 v72, s73, v72\n\t"
        "v_xor_b32 v73, v6, v73\n\t"
        "v_xor_b32 v76, s73, v76\n\t"
        "v_xor_b32 v77, v6, v77\n\t"
        "v_mfma_f32_16x16x32_bf16 v[56:59], v[64:67], v[8:11], 0\n\t"
        "v_mfma_f32_16x16x32_bf16 v[60:63], v[68:71], v[8:11], 0\n\t"
        "ds_read_b128 v[8:11], v5 offset:27456\n\t"
        "ds_read_b128 v[64:67], v5 offset:35904\n\t"
        "ds_read_b128 v[68:71], v5 offset:2176\n\t"
        "s_waitcnt lgkmcnt(4)\n\t"
        "v_mfma_f32_16x16x32_bf16 v[24:27], v[72:75], v[12:15], v[24:27]\n\t"
        "v_mfma_f32_16x16x32_bf16 v[28:31], v[76:79], v[12:15], v[28:31]\n\t"
        "v_or3_b32 v7, v7, v72, v73\n\t"
        "ds_read_b128 v[12:15], v5 offset:10624\n\t"
        "v_mfma_f32_16x16x32_bf16 v[32:35], v[72:75], v[16:19], v[32:35]\n\t"
        "v_mfma_f32_16x16x32_bf16 v[36:39], v[76:79], v[16:19], v[36:39]\n\t"
        "v_or3_b32 v7, v7, v76, v77\n\t"
        "ds_read_b128 v[16:19], v5 offset:19072\n\t"
        "s_waitcnt lgkmcnt(4)\n\t"
        "v_mfma_f32_16x16x32_bf16 v[40:43], v[72:75], v[20:23], v[40:43]\n\t"
        "v_mfma_f32_16x16x32_bf16 v[44:47], v[76:79], v[20:23], v[44:47]\n\t"
        "ds_read_b128 v[20:23], v5 offset:27520\n\t"
        "v_mfma_f32_16x16x32_bf16 v[48:51], v[72:75], v[8:11], v[48:51]\n\t"
        "v_mfma_f32_16x16x32_bf16 v[52:55], v[76:79], v[8:11], v[52:55]\n\t"
        "ds_read_b128 v[8:11], v5 offset:35968\n\t"
        "s_waitcnt vmcnt(10) lgkmcnt(4)\n\t"
        "v_xor_b32 v80, s73, v80\n\t"
        "v_xor_b32 v81, v6, v81\n\t"
        "v_xor_b32 v84, s73, v84\n\t"
        "v_xor_b32 v85, v6, v85\n\t"
        "v_mfma_f32_16x16x32_bf16 v[56:59], v[72:75], v[64:67], v[56:59]\n\t"
        "v_mfma_f32_16x16x32_bf16 v[60:63], v[76:79], v[64:67], v[60:63]\n\t"
        "ds_read_b128 v[64:67], v5 offset:2240\n\t"
        "ds_read_b128 v[72:75], v5 offset:10688\n\t"
        "ds_read_b128 v[76:79], v5 offset:19136\n\t"
        "v_mfma_f32_16x16x32_bf16 v[24:27], v[80:83], v[68:71], v[24:27]\n\t"
        "v_mfma_f32_16x16x32_bf16 v[28:31], v[84:87], v[68:71], v[28:31]\n\t"
        "v_or3_b32 v7, v7, v80, v81\n\t"
        "ds_read_b128 v[68:71], v5 offset:27584\n\t"
        "s_waitcnt lgkmcnt(6)\n\t"
        "v_mfma_f32_16x16x32_bf16 v[32:35], v[80:83], v[12:15], v[32:35]\n\t"
        "v_mfma_f32_16x16x32_bf16 v[36:39], v[84:87], v[12:15], v[36:39]\n\t"
        "v_or3_b32 v7, v7, v84, v85\n\t"
        "ds_read_b128 v[12:15], v5 offset:36032\n\t"
        "v_mfma_f32_16x16x32_bf16 v[40:43], v[80:83], v[16:19], v[40:43]\n\t"
        "v_mfma_f32_16x16x32_bf16 v[44:47], v[84:87], v[16:19], v[44:47]\n\t"
        "ds_read_b128 v[16:19], v5 offset:2304\n\t"
        "s_waitcnt lgkmcnt(6)\n\t"
        "v_mfma_f32_16x16x32_bf16 v[48:51], v[80:83], v[20:23], v[48:51]\n\t"
        "v_mfma_f32_16x16x32_bf16 v[52:55], v[84:87], v[20:23], v[52:55]\n\t"
        "ds_read_b128 v[20:23], v5 offset:10752\n\t"
        "s_waitcnt vmcnt(8) lgkmcnt(6)\n\t"
        "v_xor_b32 v88, s73, v88\n\t"
        "v_xor_b32 v89, v6, v89\n\t"
        "v_xor_b32 v92, s73, v92\n\t"
        "v_xor_b32 v93, v6, v93\n\t"
        "v_mfma_f32_16x16x32_bf16 v[56:59], v[80:83], v[8:11], v[56:59]\n\t"
        "v_mfma_f32_16x16x32_bf16 v[60:63], v[84:87], v[8:11], v[60:63]\n\t"
        "ds_read_b128 v[8:11], v5 offset:19200\n\t"
        "ds_read_b128 v[80:83], v5 offset:27648\n\t"
        "v_mfma_f32_16x16x32_bf16 v[24:27], v[88:91], v[64:67], v[24:27]\n\t"
        "v_mfma_f32_16x16x32_bf16 v[28:31], v[92:95], v[64:67], v[28:31]\n\t"
        "v_or3_b32 v7, v7, v88, v89\n\t"
        "ds_read_b128 v[84:87], v5 offset:36096\n\t"
        "s_waitcnt lgkmcnt(7)\n\t"
        "v_mfma_f32_16x16x32_bf16 v[32:35], v[88:91], v[72:75], v[32:35]\n\t"
        "v_mfma_f32_16x16x32_bf16 v[36:39], v[92:95], v[72:75], v[36:39]\n\t"
        "v_or3_b32 v7, v7, v92, v93\n\t"
        "ds_read_b128 v[64:67], v5 offset:2368\n\t"
        "v_mfma_f32_16x16x32_bf16 v[40:43], v[88:91], v[76:79], v[40:43]\n\t"
        "v_mfma_f32_16x16x32_bf16 v[44:47], v[92:95], v[76:79], v[44:47]\n\t"
        "ds_read_b128 v[72:75], v5 offset:10816\n\t"
        "s_waitcnt lgkmcnt(7)\n\t"
        "v_mfma_f32_16x16x32_bf16 v[48:51], v[88:91], v[68:71], v[48:51]\n\t"
        "v_mfma_f32_16x16x32_bf16 v[52:55], v[92:95], v[68:71], v[52:55]\n\t"
        "ds_read_b128 v[76:79], v5 offset:19264\n\t"
        "s_waitcnt vmcnt(6) lgkmcnt(7)\n\t"
        "v_xor_b32 v96, s73, v96\n\t"
        "v_xor_b32 v97, v6, v97\n\t"
        "v_xor_b32 v100, s73, v100\n\t"
        "v_xor_b32 v101, v6, v101\n\t"
        "v_mfma_f32_16x16x32_bf16 v[56:59], v[88:91], v[12:15], v[56:59]\n\t"
        "v_mfma_f32_16x16x32_bf16 v[60:63], v[92:95], v[12:15], v[60:63]\n\t"
        "ds_read_b128 v[68:71], v5 offset:27712\n\t"
        "v_mfma_f32_16x16x32_bf16 v[24:27], v[96:99], v[16:19], v[24:27]\n\t"
        "v_mfma_f32_16x16x32_bf16 v[28:31], v[100:103], v[16:19], v[28:31]\n\t"
        "v_or3_b32 v7, v7, v96, v97\n\t"
        "ds_read_b128 v[12:15], v5 offset:36160\n\t"
        "s_waitcnt lgkmcnt(7)\n\t"
        "v_mfma_f32_16x16x32_bf16 v[32:35], v[96:99], v[20:23], v[32:35]\n\t"
        "v_mfma_f32_16x16x32_bf16 v[36:39], v[100:103], v[20:23], v[36:39]\n\t"
        "v_or3_b32 v7, v7, v100, v101\n\t"
        "ds_read_b128 v[88:91], v5 offset:2432\n\t"
        "v_mfma_f32_16x16x32_bf16 v[40:43], v[96:99], v[8:11], v[40:43]\n\t"
        "v_mfma_f32_16x16x32_bf16 v[44:47], v[100:103], v[8:11], v[44:47]\n\t"
        "ds_read_b128 v[92:95], v5 offset:10880\n\t"
        "s_waitcnt lgkmcnt(7)\n\t"
        "v_mfma_f32_16x16x32_bf16 v[48:51], v[96:99], v[80:83], v[48:51]\n\t"
        "v_mfma_f32_16x16x32_bf16 v[52:55], v[100:103], v[80:83], v[52:55]\n\t"
        "ds_read_b128 v[16:19], v5 offset:19328\n\t"
        "s_waitcnt vmcnt(4) lgkmcnt(7)\n\t"
        "v_xor_b32 v104, s73, v104\n\t"
        "v_xor_b32 v105, v6, v105\n\t"
        "v_xor_b32 v108, s73, v108\n\t"
        "v_xor_b32 v109, v6, v109\n\t"
        "v_mfma_f32_16x16x32_bf16 v[56:59], v[96:99], v[84:87], v[56:59]\n\t"
        "v_mfma_f32_16x16x32_bf16 v[60:63], v[100:103], v[84:87], v[60:63]\n\t"
        "ds_read_b128 v[20:23], v5 offset:27776\n\t"
        "v_mfma_f32_16x16x32_bf16 v[24:27], v[104:107], v[64:67], v[24:27]\n\t"
        "v_mfma_f32_16x16x32_bf16 v[28:31], v[108:111], v[64:67], v[28:31]\n\t"
        "v_or3_b32 v7, v7, v104, v105\n\t"
        "ds_read_b128 v[8:11], v5 offset:36224\n\t"
        "s_waitcnt lgkmcnt(7)\n\t"
        "v_mfma_f32_16x16x32_bf16 v[32:35], v[104:107], v[72:75], v[32:35]\n\t"
        "v_mfma_f32_16x16x32_bf16 v[36:39], v[108:111], v[72:75], v[36:39]\n\t"
        "v_or3_b32 v7, v7, v108, v109\n\t"
        "ds_read_b128 v[80:83], v5 offset:2496\n\t"
        "v_mfma_f32_16x16x32_bf16 v[40:43], v[104:107], v[76:79], v[40:43]\n\t"
        "v_mfma_f32_16x16x32_bf16 v[44:47], v[108:111], v[76:79], v[44:47]\n\t"
        "ds_read_b128 v[84:87], v5 offset:10944\n\t"
        "s_waitcnt lgkmcnt(7)\n\t"
        "v_mfma_f32_16x16x32_bf16 v[48:51], v[104:107], v[68:71], v[48:51]\n\t"
        "v_mfma_f32_16x16x32_bf16 v[52:55], v[108:111], v[68:71], v[52:55]\n\t"
        "ds_read_b128 v[96:99], v5 offset:19392\n\t"
        "s_waitcnt vmcnt(2) lgkmcnt(7)\n\t"
        "v_xor_b32 v112, s73, v112\n\t"
        "v_xor_b32 v113, v6, v113\n\t"
        "v_xor_b32 v116, s73, v116\n\t"
        "v_xor_b32 v117, v6, v117\n\t"
        "v_mfma_f32_16x16x32_bf16 v[56:59], v[104:107], v[12:15], v[56:59]\n\t"
        "v_mfma_f32_16x16x32_bf16 v[60:63], v[108:111], v[12:15], v[60:63]\n\t"
        "ds_read_b128 v[100:103], v5 offset:27840\n\t"
        "v_mfma_f32_16x16x32_bf16 v[24:27], v[112:115], v[88:91], v[24:27]\n\t"
        "v_mfma_f32_16x16x32_bf16 v[28:31], v[116:119], v[88:91], v[28:31]\n\t"
        "v_or3_b32 v7, v7, v112, v113\n\t"
        "ds_read_b128 v[64:67], v5 offset:36288\n\t"
        "s_waitcnt lgkmcnt(7)\n\t"
        "v_mfma_f32_16x16x32_bf16 v[32:35], v[112:115], v[92:95], v[32:35]\n\t"
        "v_mfma_f32_16x16x32_bf16 v[36:39], v[116:119], v[92:95], v[36:39]\n\t"
        "v_or3_b32 v7, v7, v116, v117\n\t"
        "v_mfma_f32_16x16x32_bf16 v[40:43], v[112:115], v[16:19], v[40:43]\n\t"
        "v_mfma_f32_16x16x32_bf16 v[44:47], v[116:119], v[16:19], v[44:47]\n\t"
        "s_waitcnt lgkmcnt(5)\n\t"
        "v_mfma_f32_16x16x32_bf16 v[48:51], v[112:115], v[20:23], v[48:51]\n\t"
        "v_mfma_f32_16x16x32_bf16 v[52:55], v[116:119], v[20:23], v[52:55]\n\t"
        "s_waitcnt vmcnt(0) lgkmcnt(4)\n\t"
        "v_xor_b32 v120, s73, v120\n\t"
        "v_xor_b32 v121, v6, v121\n\t"
        "v_xor_b32 v124, s73, v124\n\t"
        "v_xor_b32 v125, v6, v125\n\t"
        "v_mfma_f32_16x16x32_bf16 v[56:59], v[112:115], v[8:11], v[56:59]\n\t"
        "v_mfma_f32_16x16x32_bf16 v[60:63], v[116:119], v[8:11], v[60:63]\n\t"
        "v_mfma_f32_16x16x32_bf16 v[24:27], v[120:123], v[80:83], v[24:27]\n\t"
        "v_mfma_f32_16x16x32_bf16 v[28:31], v[124:127], v[80:83], v[28:31]\n\t"
        "v_or3_b32 v7, v7, v120, v121\n\t"
        "s_waitcnt lgkmcnt(3)\n\t"
        "v_mfma_f32_16x16x32_bf16 v[32:35], v[120:123], v[84:87], v[32:35]\n\t"
        "v_mfma_f32_16x16x32_bf16 v[36:39], v[124:127], v[84:87], v[36:39]\n\t"
        "v_or3_b32 v7, v7, v124, v125\n\t"
        "v_and_b32 v7, s49, v7\n\t"
        "v_cmp_ne_u32 vcc, 0, v7\n\t"
        "s_cbranch_vccnz .L7_qr\n"
        ".L7_qd:\n\t"
        "s_mov_b64 exec, 1\n\t"
        "ds_add_u32 v2, v3 offset:12\n\t"
        "s_mov_b64 exec, -1\n\t"
        "v_and_b32 v6, 15, v1\n\t"
        "v_mul_u32_u24 v6, 272, v6\n\t"
        "v_and_b32 v7, 48, v1\n\t"
        "s_lshl_b32 s78, s80, 6\n\t"
        "v_add3_u32 v6, v6, v7, s78\n\t"
        "s_waitcnt lgkmcnt(2)\n\t"
        "v_mfma_f32_16x16x32_bf16 v[40:43], v[120:123], v[96:99], v[40:43]\n\t"
        "v_mfma_f32_16x16x32_bf16 v[44:47], v[124:127], v[96:99], v[44:47]\n\t"
        "v_cvt_pk_bf16_f32 v24, v24, v25\n\t"
        "v_cvt_pk_bf16_f32 v25, v26, v27\n\t"
        "v_cvt_pk_bf16_f32 v26, v28, v29\n\t"
        "v_cvt_pk_bf16_f32 v27, v30, v31\n\t"
        "s_waitcnt lgkmcnt(0)\n\t"
        "v_mfma_f32_16x16x32_bf16 v[48:51], v[120:123], v[100:103], v[48:51]\n\t"
        "v_mfma_f32_16x16x32_bf16 v[52:55], v[124:127], v[100:103], v[52:55]\n\t"
        "v_cvt_pk_bf16_f32 v32, v32, v33\n\t"
        "v_cvt_pk_bf16_f32 v33, v34, v35\n\t"
        "v_cvt_pk_bf16_f32 v34, v36, v37\n\t"
        "v_cvt_pk_bf16_f32 v35, v38, v39\n\t"
        "v_mfma_f32_16x16x32_bf16 v[56:59], v[120:123], v[64:67], v[56:59]\n\t"
        "v_mfma_f32_16x16x32_bf16 v[60:63], v[124:127], v[64:67], v[60:63]\n\t"
        "v_cvt_pk_bf16_f32 v40, v40, v41\n\t"
        "v_cvt_pk_bf16_f32 v41, v42, v43\n\t"
        "v_cvt_pk_bf16_f32 v42, v44, v45\n\t"
        "v_cvt_pk_bf16_f32 v43, v46, v47\n\t"
        "ds_read_b32 v0, v2 offset:12\n\t"
        "v_cvt_pk_bf16_f32 v48, v48, v49\n\t"
        "v_cvt_pk_bf16_f32 v49, v50, v51\n\t"
        "v_cvt_pk_bf16_f32 v50, v52, v53\n\t"
        "v_cvt_pk_bf16_f32 v51, v54, v55\n\t"
        "v_cvt_pk_bf16_f32 v56, v56, v57\n\t"
        "v_cvt_pk_bf16_f32 v57, v58, v59\n\t"
        "v_cvt_pk_bf16_f32 v58, v60, v61\n\t"
        "v_cvt_pk_bf16_f32 v59, v62, v63\n\t"
        "s_waitcnt lgkmcnt(0)\n\t"
        "v_cmp_gt_u16 vcc, 4, v0\n\t"
        "s_cbranch_vccz .L7_qx\n"
        ".L7_w58:\n\t"
        "ds_read_b32 v0, v2 offset:12\n\t"
        "s_waitcnt lgkmcnt(0)\n\t"
        "v_cmp_gt_u16 vcc, 4, v0\n\t"
        "s_cbranch_vccz .L7_d59\n\t"
        "s_sleep 0\n\t"
        "s_sub_i32 s83, s83, 1\n\t"
        "s_cmp_gt_i32 s83, 0\n\t"
        "s_cbranch_scc1 .L7_w58\n"
        ".L7_d59:\n"
        ".L7_qx:\n\t"
        "ds_write_b128 v6, v[24:27] offset:2048\n\t"
        "ds_write_b128 v6, v[32:35] offset:6400\n\t"
        "ds_write_b128 v6, v[40:43] offset:10752\n\t"
        "ds_write_b128 v6, v[48:51] offset:15104\n\t"
        "ds_write_b128 v6, v[56:59] offset:19456\n\t"
        "s_lshl_b32 s78, s69, 9\n\t"
        "v_lshl_add_u32 v7, v1, 3, s78\n\t"
        "v_mov_b64 v[8:9], 0\n\t"
        "ds_write_b64 v7, v[8:9] offset:34048\n\t"
        "s_mov_b64 exec, 1\n\t"
        "ds_add_u32 v2, v3 offset:16\n\t"
        "s_mov_b64 exec, -1\n\t"
        "v_lshrrev_b32 v10, 3, v1\n\t"
        "v_lshlrev_b32 v10, 9, v10\n\t"
        "v_and_b32 v11, 7, v1\n\t"
        "v_lshl_add_u32 v10, v11, 4, v10\n\t"
        "v_and_b32 v11, 16, v1\n\t"
        "v_lshlrev_b32 v11, 2, v11\n\t"
        "v_xor_b32 v10, v10, v11\n\t"
        "s_load_dwordx4 s[24:27], %2, 32\n\t"
        "s_lshl_b32 s78, s80, 14\n\t"
        "s_add_u32 m0, s78, 68864\n\t"
        "s_lshl_b32 s78, s70, 9\n\t"
        "s_add_u32 s78, s78, s80\n\t"
        "s_lshl_b32 s78, s78, 7\n"
        ".L7_w60:\n\t"
        "ds_read_b32 v0, v2 offset:24\n\t"
        "s_waitcnt lgkmcnt(0)\n\t"
        "v_cmp_gt_u16 vcc, 8, v0\n\t"
        "s_cbranch_vccz .L7_d61\n\t"
        "s_sleep 1\n\t"
        "s_sub_i32 s83, s83, 1\n\t"
        "s_cmp_gt_i32 s83, 0\n\t"
        "s_cbranch_scc1 .L7_w60\n"
        ".L7_d61:\n"
        ".L7_w62:\n\t"
        "ds_read_b32 v0, v2 offset:88\n\t"
        "s_waitcnt lgkmcnt(0)\n\t"
        "v_cmp_gt_u16 vcc, 16, v0\n\t"
        "s_cbranch_vccz .L7_d63\n\t"
        "s_sleep 1\n\t"
        "s_sub_i32 s83, s83, 1\n\t"
        "s_cmp_gt_i32 s83, 0\n\t"
        "s_cbranch_scc1 .L7_w62\n"
        ".L7_d63:\n\t"
        "s_setprio 2\n\t"
        "buffer_load_dwordx4 v10, s[24:27], s78 offen lds\n\t"
        "s_add_u32 m0, m0, 0x400\n\t"
        "s_add_u32 s78, s78, 0x1000\n\t"
        "buffer_load_dwordx4 v10, s[24:27], s78 offen lds\n\t"
        "s_add_u32 m0, m0, 0x400\n\t"
        "s_add_u32 s78, s78, 0x1000\n\t"
        "buffer_load_dwordx4 v10, s[24:27], s78 offen lds\n\t"
        "s_add_u32 m0, m0, 0x400\n\t"
        "s_add_u32 s78, s78, 0x1000\n\t"
        "buffer_load_dwordx4 v10, s[24:27], s78 offen lds\n\t"
        "s_add_u32 m0, m0, 0x400\n\t"
        "s_add_u32 s78, s78, 0x1000\n\t"
        "buffer_load_dwordx4 v10, s[24:27], s78 offen lds\n\t"
        "s_add_u32 m0, m0, 0x400\n\t"
        "s_add_u32 s78, s78, 0x1000\n\t"
        "buffer_load_dwordx4 v10, s[24:27], s78 offen lds\n\t"
        "s_add_u32 m0, m0, 0x400\n\t"
        "s_add_u32 s78, s78, 0x1000\n\t"
        "buffer_load_dwordx4 v10, s[24:27], s78 offen lds\n\t"
        "s_add_u32 m0, m0, 0x400\n\t"
        "s_add_u32 s78, s78, 0x1000\n\t"
        "buffer_load_dwordx4 v10, s[24:27], s78 offen lds\n\t"
        "s_add_u32 m0, m0, 0x400\n\t"
        "s_add_u32 s78, s78, 0x1000\n\t"
        "buffer_load_dwordx4 v10, s[24:27], s78 offen lds\n\t"
        "s_add_u32 m0, m0, 0x400\n\t"
        "s_add_u32 s78, s78, 0x1000\n\t"
        "buffer_load_dwordx4 v10, s[24:27], s78 offen lds\n\t"
        "s_add_u32 m0, m0, 0x400\n\t"
        "s_add_u32 s78, s78, 0x1000\n\t"
        "buffer_load_dwordx4 v10, s[24:27], s78 offen lds\n\t"
        "s_add_u32 m0, m0, 0x400\n\t"
        "s_add_u32 s78, s78, 0x1000\n\t"
        "buffer_load_dwordx4 v10, s[24:27], s78 offen lds\n\t"
        "s_add_u32 m0, m0, 0x400\n\t"
        "s_add_u32 s78, s78, 0x1000\n\t"
        "buffer_load_dwordx4 v10, s[24:27], s78 offen lds\n\t"
        "s_add_u32 m0, m0, 0x400\n\t"
        "s_add_u32 s78, s78, 0x1000\n\t"
        "buffer_load_dwordx4 v10, s[24:27], s78 offen lds\n\t"
        "s_add_u32 m0, m0, 0x400\n\t"
        "s_add_u32 s78, s78, 0x1000\n\t"
        "buffer_load_dwordx4 v10, s[24:27], s78 offen lds\n\t"
        "s_add_u32 m0, m0, 0x400\n\t"
        "s_add_u32 s78, s78, 0x1000\n\t"
        "buffer_load_dwordx4 v10, s[24:27], s78 offen lds\n\t"
        "s_waitcnt vmcnt(0)\n"
        ".L7_vw:\n\t"
        "s_mov_b64 exec, 1\n\t"
        "ds_add_u32 v2, v3 offset:28\n\t"
        "s_mov_b64 exec, -1\n\t"
        "s_setprio 0\n"
        ".L7_w64:\n\t"
        "ds_read_b32 v0, v2 offset:36\n\t"
        "s_waitcnt lgkmcnt(0)\n\t"
        "v_cmp_gt_u16 vcc, 4, v0\n\t"
        "s_cbranch_vccz .L7_d65\n\t"
        "s_sleep 2\n\t"
        "s_sub_i32 s83, s83, 1\n\t"
        "s_cmp_gt_i32 s83, 0\n\t"
        "s_cbranch_scc1 .L7_w64\n"
        ".L7_d65:\n\t"
        "v_cmp_lt_u32 vcc, 0xfffff, v0\n\t"
        "s_cbranch_vccnz .L7_exh\n\t"
        "s_cmp_eq_u32 s69, 0\n\t"
        "s_cbranch_scc0 .L7_hz\n\t"
        "s_lshr_b32 s47, s68, 20\n\t"
        "s_add_u32 s47, s47, s68\n\t"
        "s_and_b32 s47, s47, 0xfff\n\t"
        "s_mul_i32 s46, s68, 0x9e377000\n\t"
        "s_or_b32 s46, s46, s47\n\t"
        "v_mov_b32 v4, s46\n\t"
        "s_xor_b32 s44, s70, 1\n\t"
        "s_lshl1_add_u32 s44, %3, s44\n\t"
        "s_lshl_b32 s44, s44, 10\n\t"
        "s_add_u32 s44, s44, 262144\n"
        ".L7_w66:\n\t"
        "ds_read_b32 v0, v2 offset:116\n\t"
        "s_waitcnt lgkmcnt(0)\n\t"
        "v_cmp_gt_u16 vcc, 1, v0\n\t"
        "s_cbranch_vccz .L7_d67\n\t"
        "s_sleep 1\n\t"
        "s_sub_i32 s83, s83, 1\n\t"
        "s_cmp_gt_i32 s83, 0\n\t"
        "s_cbranch_scc1 .L7_w66\n"
        ".L7_d67:\n\t"
        "v_readfirstlane_b32 s78, v0\n\t"
        "s_lshr_b32 s78, s78, 16\n\t"
        "s_cmp_gt_u32 s78, 16\n\t"
        "s_cselect_b32 s45, 0x2000, 0\n\t"
        "s_xor_b32 s44, s44, s45\n\t"
        "s_xor_b32 s45, s44, 0xc00\n"
        ".L7_w68:\n\t"
        "ds_read_b32 v0, v2 offset:56\n\t"
        "s_waitcnt lgkmcnt(0)\n\t"
        "v_cmp_gt_u16 vcc, 3, v0\n\t"
        "s_cbranch_vccz .L7_d69\n\t"
        "s_sleep 8\n\t"
        "s_sub_i32 s83, s83, 1\n\t"
        "s_cmp_gt_i32 s83, 0\n\t"
        "s_cbranch_scc1 .L7_w68\n"
        ".L7_d69:\n\t"
        "s_setprio 3\n"
        ".L7_w70:\n\t"
        "ds_read_b32 v0, v2 offset:96\n\t"
        "s_waitcnt lgkmcnt(0)\n\t"
        "v_cmp_gt_u16 vcc, 7, v0\n\t"
        "s_cbranch_vccz .L7_d71\n\t"
        "s_sleep 1\n\t"
        "s_sub_i32 s83, s83, 1\n\t"
        "s_cmp_gt_i32 s83, 0\n\t"
        "s_cbranch_scc1 .L7_w70\n"
        ".L7_d71:\n\t"
        "s_mov_b64 exec, 1\n\t"
        "buffer_store_dword v4, off, s[20:23], s44 offset:12 sc1\n\t"
        "s_mov_b32 s83, 64\n\t"
        "s_sleep 4\n"
        ".L7_h1:\n\t"
        "s_buffer_load_dword s47, s[20:23], s45 offset:12 glc\n\t"
        "s_waitcnt lgkmcnt(0)\n\t"
        "s_cmp_eq_u32 s47, s46\n\t"
        "s_cbranch_scc1 .L7_h2\n\t"
        "s_sleep 3\n\t"
        "s_sub_i32 s83, s83, 1\n\t"
        "s_cmp_gt_i32 s83, 0\n\t"
        "s_cbranch_scc1 .L7_h1\n"
        ".L7_h2:\n\t"
        "s_mov_b64 exec, 1\n\t"
        "ds_add_u32 v2, v3 offset:100\n\t"
        "s_mov_b64 exec, -1\n"
        ".L7_hz:\n\t"
        "s_endpgm\n"
        ".L7_exh:\n\t"
        "s_mov_b64 exec, 1\n\t"
        "ds_add_u32 v2, v3 offset:100\n\t"
        "s_mov_b64 exec, -1\n\t"
        "s_branch .L7_exit\n"
        ".L7_O2:\n\t"
        "s_load_dwordx8 s[24:31], %2, 32\n\t"
        "s_load_dwordx4 s[60:63], %2, 176\n\t"
        "v_lshrrev_b32 v6, 4, v1\n\t"
        "v_and_b32 v7, 48, v1\n\t"
        "v_and_b32 v4, 2, v6\n\t"
        "v_add_lshl_u32 v4, v4, v6, 2\n\t"
        "v_and_b32 v8, 15, v1\n\t"
        "s_lshl_b32 s78, s70, 8\n\t"
        "v_lshl_add_u32 v9, v8, 4, s78\n\t"
        "v_lshrrev_b32 v10, 3, v1\n\t"
        "v_lshlrev_b32 v10, 9, v10\n\t"
        "v_and_b32 v11, 7, v1\n\t"
        "v_lshl_add_u32 v10, v11, 4, v10\n\t"
        "v_and_b32 v11, 16, v1\n\t"
        "v_lshlrev_b32 v11, 2, v11\n\t"
        "v_xor_b32 v10, v10, v11\n\t"
        "s_add_u32 s81, s69, s76\n\t"
        "s_and_b32 s81, s81, 7\n\t"
        "s_lshr_b32 s78, s81, 1\n\t"
        "s_lshl_b32 s78, s78, 7\n\t"
        "s_and_b32 s79, s81, 1\n\t"
        "s_lshl_b32 s79, s79, 15\n\t"
        "s_add_u32 s78, s78, s79\n\t"
        "s_lshl_b32 s79, s70, 16\n\t"
        "s_add_u32 s78, s78, s79\n\t"
        "v_add_u32 v10, s78, v10\n\t"
        "s_lshl_b32 s82, s81, 13\n\t"
        "s_add_u32 s82, s82, 68864\n\t"
        "v_bfe_u32 v12, v1, 2, 2\n\t"
        "v_lshlrev_b32 v11, 10, v6\n\t"
        "v_lshl_add_u32 v11, v12, 7, v11\n\t"
        "v_lshrrev_b32 v12, 1, v12\n\t"
        "s_and_b32 s78, s81, 1\n\t"
        "v_xor_b32 v12, s78, v12\n\t"
        "v_lshl_add_u32 v11, v12, 6, v11\n\t"
        "v_and_b32 v12, 3, v1\n\t"
        "v_lshl_add_u32 v11, v12, 4, v11\n\t"
        "s_lshr_b32 s78, s81, 1\n\t"
        "s_lshl_b32 s78, s78, 14\n\t"
        "s_add_u32 s78, s78, 68864\n\t"
        "v_add_u32 v23, s78, v11\n\t"
        "s_waitcnt vmcnt(0)\n\t"
        "ds_bpermute_b32 v12, v7, v5 offset:0\n\t"
        "ds_bpermute_b32 v13, v7, v5 offset:4\n\t"
        "ds_bpermute_b32 v14, v7, v5 offset:8\n\t"
        "ds_bpermute_b32 v15, v7, v5 offset:12\n\t"
        "ds_bpermute_b32 v16, v7, v5 offset:64\n\t"
        "ds_bpermute_b32 v17, v7, v5 offset:68\n\t"
        "ds_bpermute_b32 v18, v7, v5 offset:72\n\t"
        "ds_bpermute_b32 v19, v7, v5 offset:76\n\t"
        "ds_bpermute_b32 v20, v4, v5 offset:128\n\t"
        "ds_bpermute_b32 v21, v4, v5 offset:136\n\t"
        "s_waitcnt lgkmcnt(0)\n\t"
        "v_lshl_add_u32 v12, v12, 9, v9\n\t"
        "v_lshl_add_u32 v13, v13, 9, v9\n\t"
        "v_lshl_add_u32 v14, v14, 9, v9\n\t"
        "v_lshl_add_u32 v15, v15, 9, v9\n\t"
        "v_lshl_add_u32 v16, v16, 9, v9\n\t"
        "v_lshl_add_u32 v17, v17, 9, v9\n\t"
        "v_lshl_add_u32 v18, v18, 9, v9\n\t"
        "v_lshl_add_u32 v19, v19, 9, v9\n\t"
        "v_lshl_add_u32 v20, v20, 9, v9\n\t"
        "v_lshl_add_u32 v21, v21, 9, v9\n\t"
        "s_setprio 0\n"
        ".L7_w1:\n\t"
        "ds_read_b32 v0, v2 offset:44\n\t"
        "s_waitcnt lgkmcnt(0)\n\t"
        "v_cmp_gt_u16 vcc, 2, v0\n\t"
        "s_cbranch_vccz .L7_d2\n\t"
        "s_sleep 1\n\t"
        "s_sub_i32 s83, s83, 1\n\t"
        "s_cmp_gt_i32 s83, 0\n\t"
        "s_cbranch_scc1 .L7_w1\n"
        ".L7_d2:\n"
        ".L7_w3:\n\t"
        "ds_read_b64 v[4:5], v2 offset:72\n\t"
        "s_waitcnt lgkmcnt(0)\n\t"
        "v_cmp_gt_u16 vcc, 2, v4\n\t"
        "s_cbranch_vccz .L7_d4\n\t"
        "s_sleep 1\n\t"
        "s_sub_i32 s83, s83, 1\n\t"
        "s_cmp_gt_i32 s83, 0\n\t"
        "s_cbranch_scc1 .L7_w3\n"
        ".L7_d4:\n\t"
        "v_readfirstlane_b32 s68, v5\n\t"
        "s_add_u32 m0, s82, 0\n\t"
        "s_mov_b32 s78, 0\n\t"
        "buffer_load_dwordx4 v10, s[28:31], s78 offen lds\n\t"
        "s_add_u32 m0, m0, 0x400\n\t"
        "s_add_u32 s78, s78, 0x1000\n\t"
        "buffer_load_dwordx4 v10, s[28:31], s78 offen lds\n\t"
        "s_add_u32 m0, m0, 0x400\n\t"
        "s_add_u32 s78, s78, 0x1000\n\t"
        "buffer_load_dwordx4 v10, s[28:31], s78 offen lds\n\t"
        "s_add_u32 m0, m0, 0x400\n\t"
        "s_add_u32 s78, s78, 0x1000\n\t"
        "buffer_load_dwordx4 v10, s[28:31], s78 offen lds\n\t"
        "s_add_u32 m0, m0, 0x400\n\t"
        "s_add_u32 s78, s78, 0x1000\n\t"
        "buffer_load_dwordx4 v10, s[28:31], s78 offen lds\n\t"
        "s_add_u32 m0, m0, 0x400\n\t"
        "s_add_u32 s78, s78, 0x1000\n\t"
        "buffer_load_dwordx4 v10, s[28:31], s78 offen lds\n\t"
        "s_add_u32 m0, m0, 0x400\n\t"
        "s_add_u32 s78, s78, 0x1000\n\t"
        "buffer_load_dwordx4 v10, s[28:31], s78 offen lds\n\t"
        "s_add_u32 m0, m0, 0x400\n\t"
        "s_add_u32 s78, s78, 0x1000\n\t"
        "buffer_load_dwordx4 v10, s[28:31], s78 offen lds\n\t"
        "s_mov_b64 exec, 1\n\t"
        "ds_add_u32 v2, v3 offset:88\n\t"
        "s_mov_b64 exec, -1\n\t"
        "s_mov_b32 s36, 0x22222222\n\t"
        "s_mov_b32 s37, s36\n\t"
        "s_lshl_b64 s[38:39], s[36:37], 1\n\t"
        "s_lshl_b64 s[40:41], s[36:37], 2\n\t"
        "s_and_b32 s44, s68, 0xfff\n\t"
        "s_xor_b32 s45, s44, 0x5a5\n\t"
        "s_lshr_b32 s46, s68, 12\n\t"
        "s_lshr_b32 s47, s68, 20\n\t"
        "s_add_u32 s46, s46, s68\n\t"
        "s_and_b32 s46, s46, 0xfff\n\t"
        "s_add_u32 s47, s47, s68\n\t"
        "s_and_b32 s47, s47, 0xfff\n\t"
        "v_mov_b32 v22, s44\n\t"
        "v_mov_b32 v0, s45\n\t"
        "v_cndmask_b32 v22, v22, v0, s[36:37]\n\t"
        "v_mov_b32 v0, s46\n\t"
        "v_cndmask_b32 v22, v22, v0, s[38:39]\n\t"
        "v_mov_b32 v0, s47\n\t"
        "v_cndmask_b32 v22, v22, v0, s[40:41]\n\t"
        "s_lshl3_add_u32 s42, %3, s77\n\t"
        "s_lshl_b32 s42, s42, 8\n\t"
        "s_add_u32 s42, s42, 262144\n\t"
        "s_xor_b32 s43, s42, 0x800\n\t"
        "s_movk_i32 s48, 0xfff\n\t"
        "s_waitcnt vmcnt(0)\n\t"
        "s_mov_b64 exec, 1\n\t"
        "ds_add_u32 v2, v3 offset:20\n\t"
        "s_mov_b64 exec, -1\n"
        ".L7_w5:\n\t"
        "ds_read_b32 v0, v2 offset:20\n\t"
        "s_waitcnt lgkmcnt(0)\n\t"
        "v_cmp_gt_u16 vcc, 8, v0\n\t"
        "s_cbranch_vccz .L7_d6\n\t"
        "s_sleep 1\n\t"
        "s_sub_i32 s83, s83, 1\n\t"
        "s_cmp_gt_i32 s83, 0\n\t"
        "s_cbranch_scc1 .L7_w5\n"
        ".L7_d6:\n\t"
        "ds_read_b64_tr_b16 v[64:65], v23 offset:0\n\t"
        "ds_read_b64_tr_b16 v[66:67], v23 offset:512\n\t"
        "ds_read_b64_tr_b16 v[68:69], v23 offset:8\n\t"
        "ds_read_b64_tr_b16 v[70:71], v23 offset:520\n\t"
        "ds_read_b64_tr_b16 v[72:73], v23 offset:4096\n\t"
        "ds_read_b64_tr_b16 v[74:75], v23 offset:4608\n\t"
        "ds_read_b64_tr_b16 v[76:77], v23 offset:4104\n\t"
        "ds_read_b64_tr_b16 v[78:79], v23 offset:4616\n\t"
        "ds_read_b64_tr_b16 v[80:81], v23 offset:8192\n\t"
        "ds_read_b64_tr_b16 v[82:83], v23 offset:8704\n\t"
        "ds_read_b64_tr_b16 v[84:85], v23 offset:8200\n\t"
        "ds_read_b64_tr_b16 v[86:87], v23 offset:8712\n\t"
        "ds_read_b64_tr_b16 v[88:89], v23 offset:12288\n\t"
        "ds_read_b64_tr_b16 v[90:91], v23 offset:12800\n\t"
        "s_waitcnt lgkmcnt(10)\n\t"
        "ds_read_b64_tr_b16 v[92:93], v23 offset:12296\n\t"
        "ds_read_b64_tr_b16 v[94:95], v23 offset:12808\n\t"
        "s_mov_b64 exec, 1\n\t"
        "ds_add_u32 v2, v3 offset:24\n\t"
        "s_mov_b64 exec, -1\n\t"
        "buffer_load_dwordx4 v[24:27], v12, s[16:19], 0 offen\n\t"
        "buffer_load_dwordx4 v[28:31], v13, s[16:19], 0 offen\n\t"
        "buffer_load_dwordx4 v[32:35], v14, s[16:19], 0 offen\n\t"
        "buffer_load_dwordx4 v[36:39], v15, s[16:19], 0 offen\n\t"
        "buffer_load_dwordx4 v[40:43], v16, s[16:19], 0 offen\n\t"
        "buffer_load_dwordx4 v[44:47], v17, s[16:19], 0 offen\n\t"
        "buffer_load_dwordx4 v[48:51], v18, s[16:19], 0 offen\n\t"
        "buffer_load_dwordx4 v[52:55], v19, s[16:19], 0 offen\n\t"
        "buffer_load_dwordx4 v[56:59], v20, s[16:19], 0 offen\n\t"
        "buffer_load_dwordx4 v[60:63], v21, s[16:19], 0 offen\n\t"
        "s_mov_b64 exec, 1\n\t"
        "ds_add_u32 v2, v3 offset:88\n\t"
        "s_mov_b64 exec, -1\n"
        ".L7_o2:\n\t"
        "s_setprio 2\n\t"
        "v_add_u32 v20, s80, v6\n\t"
        "v_lshrrev_b32 v21, 5, v1\n\t"
        "v_add_u32 v21, s80, v21\n\t"
        "v_lshlrev_b32 v8, 4, v8\n\t"
        "s_movk_i32 s78, 272\n\t"
        "v_mad_u32_u24 v10, v20, s78, v8\n\t"
        "v_mad_u32_u24 v11, v21, s78, v8\n\t"
        "v_lshlrev_b32 v108, 1, v20\n\t"
        "v_lshlrev_b32 v109, 1, v21\n\t"
        "s_mov_b32 s50, 0x0f0ff0f0\n\t"
        "s_mov_b32 s51, s50\n\t"
        "s_and_b32 s57, s81, 1\n\t"
        "s_lshl_b32 s57, s57, 13\n"
        ".L7_w7:\n\t"
        "ds_read_b32 v0, v2 offset:16\n\t"
        "s_waitcnt lgkmcnt(0)\n\t"
        "v_cmp_gt_u16 vcc, 4, v0\n\t"
        "s_cbranch_vccz .L7_d8\n\t"
        "s_sleep 1\n\t"
        "s_sub_i32 s83, s83, 1\n\t"
        "s_cmp_gt_i32 s83, 0\n\t"
        "s_cbranch_scc1 .L7_w7\n"
        ".L7_d8:\n\t"
        "ds_read_b128 v[96:99], v10 offset:2048\n\t"
        "ds_read_b128 v[100:103], v10 offset:3136\n\t"
        "ds_read_b128 v[104:107], v11 offset:4224\n\t"
        "ds_read_b32 v9, v2 offset:116\n\t"
        "ds_read_u16 v110, v108 offset:1792\n\t"
        "ds_read_u16 v111, v108 offset:1800\n\t"
        "ds_read_u16 v112, v109 offset:1808\n\t"
        "s_waitcnt vmcnt(0)\n\t"
        "s_waitcnt lgkmcnt(6)\n\t"
        "v_dot2_f32_bf16 v12, v24, v96, 0\n\t"
        "v_dot2_f32_bf16 v13, v28, v96, 0\n\t"
        "v_dot2_f32_bf16 v14, v32, v96, 0\n\t"
        "v_dot2_f32_bf16 v15, v36, v96, 0\n\t"
        "v_dot2_f32_bf16 v12, v25, v97, v12\n\t"
        "v_dot2_f32_bf16 v13, v29, v97, v13\n\t"
        "v_dot2_f32_bf16 v14, v33, v97, v14\n\t"
        "v_dot2_f32_bf16 v15, v37, v97, v15\n\t"
        "v_dot2_f32_bf16 v12, v26, v98, v12\n\t"
        "v_dot2_f32_bf16 v13, v30, v98, v13\n\t"
        "v_dot2_f32_bf16 v14, v34, v98, v14\n\t"
        "v_dot2_f32_bf16 v15, v38, v98, v15\n\t"
        "v_dot2_f32_bf16 v12, v27, v99, v12\n\t"
        "v_dot2_f32_bf16 v13, v31, v99, v13\n\t"
        "v_dot2_f32_bf16 v14, v35, v99, v14\n\t"
        "v_dot2_f32_bf16 v15, v39, v99, v15\n\t"
        "s_waitcnt lgkmcnt(5)\n\t"
        "v_dot2_f32_bf16 v16, v40, v100, 0\n\t"
        "v_dot2_f32_bf16 v17, v44, v100, 0\n\t"
        "v_dot2_f32_bf16 v18, v48, v100, 0\n\t"
        "v_dot2_f32_bf16 v19, v52, v100, 0\n\t"
        "v_dot2_f32_bf16 v16, v41, v101, v16\n\t"
        "v_dot2_f32_bf16 v17, v45, v101, v17\n\t"
        "v_dot2_f32_bf16 v18, v49, v101, v18\n\t"
        "v_dot2_f32_bf16 v19, v53, v101, v19\n\t"
        "v_dot2_f32_bf16 v16, v42, v102, v16\n\t"
        "v_dot2_f32_bf16 v17, v46, v102, v17\n\t"
        "v_dot2_f32_bf16 v18, v50, v102, v18\n\t"
        "v_dot2_f32_bf16 v19, v54, v102, v19\n\t"
        "v_dot2_f32_bf16 v16, v43, v103, v16\n\t"
        "v_dot2_f32_bf16 v17, v47, v103, v17\n\t"
        "v_dot2_f32_bf16 v18, v51, v103, v18\n\t"
        "v_dot2_f32_bf16 v19, v55, v103, v19\n\t"
        "s_waitcnt lgkmcnt(4)\n\t"
        "v_dot2_f32_bf16 v20, v56, v104, 0\n\t"
        "v_dot2_f32_bf16 v21, v60, v104, 0\n\t"
        "v_dot2_f32_bf16 v20, v57, v105, v20\n\t"
        "v_dot2_f32_bf16 v21, v61, v105, v21\n\t"
        "v_dot2_f32_bf16 v20, v58, v106, v20\n\t"
        "v_dot2_f32_bf16 v21, v62, v106, v21\n\t"
        "v_dot2_f32_bf16 v20, v59, v107, v20\n\t"
        "v_dot2_f32_bf16 v21, v63, v107, v21\n\t"
        "s_nop 1\n\t"
        "v_add_f32_dpp v20, v20, v20 row_ror:8 row_mask:0xf bank_mask:0x3\n\t"
        "v_add_f32_dpp v12, v12, v12 row_ror:8 row_mask:0xf bank_mask:0x3\n\t"
        "v_add_f32_dpp v14, v14, v14 row_ror:8 row_mask:0xf bank_mask:0x3\n\t"
        "v_add_f32_dpp v16, v16, v16 row_ror:8 row_mask:0xf bank_mask:0x3\n\t"
        "v_add_f32_dpp v18, v18, v18 row_ror:8 row_mask:0xf bank_mask:0x3\n\t"
        "v_add_f32_dpp v20, v21, v21 row_ror:8 row_mask:0xf bank_mask:0xc\n\t"
        "v_add_f32_dpp v12, v13, v13 row_ror:8 row_mask:0xf bank_mask:0xc\n\t"
        "v_add_f32_dpp v14, v15, v15 row_ror:8 row_mask:0xf bank_mask:0xc\n\t"
        "v_add_f32_dpp v16, v17, v17 row_ror:8 row_mask:0xf bank_mask:0xc\n\t"
        "v_add_f32_dpp v18, v19, v19 row_ror:8 row_mask:0xf bank_mask:0xc\n\t"
        "v_add_f32_dpp v20, v20, v20 row_half_mirror row_mask:0xf bank_mask:0xf\n\t"
        "v_add_f32_dpp v12, v12, v12 row_half_mirror row_mask:0xf bank_mask:0x5\n\t"
        "v_add_f32_dpp v16, v16, v16 row_half_mirror row_mask:0xf bank_mask:0x5\n\t"
        "v_mov_b32 v21, v20\n\t"
        "v_add_f32_dpp v12, v14, v14 row_half_mirror row_mask:0xf bank_mask:0xa\n\t"
        "v_add_f32_dpp v16, v18, v18 row_half_mirror row_mask:0xf bank_mask:0xa\n\t"
        "v_permlane16_swap_b32 v20, v21\n\t"
        "v_cndmask_b32 v20, v20, v21, s[50:51]\n\t"
        "v_add_f32_dpp v12, v12, v12 quad_perm:[1,0,3,2] row_mask:0xf bank_mask:0xf\n\t"
        "v_add_f32_dpp v16, v16, v16 quad_perm:[1,0,3,2] row_mask:0xf bank_mask:0xf\n\t"
        "v_add_f32_dpp v20, v20, v20 quad_perm:[1,0,3,2] row_mask:0xf bank_mask:0xf\n\t"
        "v_add_f32_dpp v12, v12, v12 quad_perm:[2,3,0,1] row_mask:0xf bank_mask:0xf\n\t"
        "v_add_f32_dpp v16, v16, v16 quad_perm:[2,3,0,1] row_mask:0xf bank_mask:0xf\n\t"
        "v_add_f32_dpp v20, v20, v20 quad_perm:[2,3,0,1] row_mask:0xf bank_mask:0xf\n\t"
        "s_waitcnt lgkmcnt(0)\n\t"
        "v_cmp_gt_u16 vcc, 1, v9\n\t"
        "s_cbranch_vccz .L7_sv\n"
        ".L7_w9:\n\t"
        "ds_read_b32 v9, v2 offset:116\n\t"
        "s_waitcnt lgkmcnt(0)\n\t"
        "v_cmp_gt_u16 vcc, 1, v9\n\t"
        "s_cbranch_vccz .L7_d10\n\t"
        "s_sleep 1\n\t"
        "s_sub_i32 s83, s83, 1\n\t"
        "s_cmp_gt_i32 s83, 0\n\t"
        "s_cbranch_scc1 .L7_w9\n"
        ".L7_d10:\n"
        ".L7_sv:\n\t"
        "v_readfirstlane_b32 s78, v9\n\t"
        "s_lshr_b32 s78, s78, 16\n\t"
        "s_cmp_gt_u32 s78, 16\n\t"
        "s_cselect_b32 s56, 0x2000, 0\n\t"
        "s_sub_u32 s78, s78, 13\n\t"
        "s_cmp_le_u32 s78, 6\n\t"
        "s_cselect_b32 s57, s57, s56\n\t"
        "v_cndmask_b32 v12, v12, v16, s[36:37]\n\t"
        "v_cndmask_b32 v12, v12, v20, s[38:39]\n\t"
        "v_add_u32 v12, 0x800, v12\n\t"
        "v_bfi_b32 v21, s48, v22, v12\n\t"
        "v_lshlrev_b32 v13, 2, v1\n\t"
        "s_xor_b32 s42, s42, s56\n\t"
        "s_setprio 3\n\t"
        "buffer_store_dword v21, v13, s[20:23], s42 offen sc1\n\t"
        "s_setprio 2\n\t"
        "s_xor_b32 s43, s43, s56\n\t"
        "s_lshr_b32 s78, %3, 3\n\t"
        "s_and_b32 s79, %3, 3\n\t"
        "s_lshl2_add_u32 s78, s78, s79\n\t"
        "s_and_b32 s79, %3, 4\n\t"
        "s_lshl_b32 s79, s79, 11\n\t"
        "s_lshl_b32 s30, s81, 10\n\t"
        "s_add_u32 s79, s79, s30\n\t"
        "s_add_u32 s79, s79, 3145728\n\t"
        "s_xor_b32 s79, s79, s57\n\t"
        "s_mul_i32 s24, s78, 0xc000\n\t"
        "s_xor_b32 s78, s78, 1\n\t"
        "s_mul_i32 s27, s78, 0xc000\n\t"
        "s_add_u32 s24, s24, s79\n\t"
        "s_add_u32 s25, s24, 0x4000\n\t"
        "s_add_u32 s26, s24, 0x8000\n\t"
        "s_add_u32 s27, s27, s79\n\t"
        "s_add_u32 s28, s27, 0x4000\n\t"
        "s_add_u32 s29, s27, 0x8000\n\t"
        "s_lshr_b32 s78, s24, 8\n\t"
        "s_and_b32 s78, s78, 0x2000\n\t"
        "s_xor_b32 s24, s24, s78\n\t"
        "s_lshr_b32 s78, s25, 8\n\t"
        "s_and_b32 s78, s78, 0x2000\n\t"
        "s_xor_b32 s25, s25, s78\n\t"
        "s_lshr_b32 s78, s26, 8\n\t"
        "s_and_b32 s78, s78, 0x2000\n\t"
        "s_xor_b32 s26, s26, s78\n\t"
        "s_lshr_b32 s78, s27, 8\n\t"
        "s_and_b32 s78, s78, 0x2000\n\t"
        "s_xor_b32 s27, s27, s78\n\t"
        "s_lshr_b32 s78, s28, 8\n\t"
        "s_and_b32 s78, s78, 0x2000\n\t"
        "s_xor_b32 s28, s28, s78\n\t"
        "s_lshr_b32 s78, s29, 8\n\t"
        "s_and_b32 s78, s78, 0x2000\n\t"
        "s_xor_b32 s29, s29, s78\n\t"
        "v_and_b32 v14, 48, v8\n\t"
        "s_mov_b32 s78, 0xffff\n\t"
        "s_mov_b32 s79, 0\n\t"
        "v_lshlrev_b64 v[4:5], v14, s[78:79]\n\t"
        "v_xor_b32 v18, v110, v8\n\t"
        "v_xor_b32 v19, v111, v8\n\t"
        "v_and_b32 v14, 1, v6\n\t"
        "v_lshl_add_u32 v14, v14, 3, v8\n\t"
        "v_xor_b32 v20, v112, v14\n\t"
        "v_xor_b32 v21, v21, v22\n"
        ".L7_w11:\n\t"
        "ds_read_b32 v0, v2 offset:36\n\t"
        "s_waitcnt lgkmcnt(0)\n\t"
        "v_cmp_gt_u16 vcc, 4, v0\n\t"
        "s_cbranch_vccz .L7_d12\n\t"
        "s_sleep 1\n\t"
        "s_sub_i32 s83, s83, 1\n\t"
        "s_cmp_gt_i32 s83, 0\n\t"
        "s_cbranch_scc1 .L7_w11\n"
        ".L7_d12:\n\t"
        "v_lshrrev_b32 v0, 20, v0\n\t"
        "s_nop 0\n\t"
        "v_readfirstlane_b32 s58, v0\n\t"
        "s_sleep 8\n"
        ".L7_sp:\n\t"
        "s_setprio 3\n\t"
        "buffer_load_dword v12, v13, s[20:23], s43 offen sc1\n\t"
        "s_setprio 2\n\t"
        "s_waitcnt vmcnt(0)\n\t"
        "v_xor_b32 v12, v12, v22\n\t"
        "v_and_b32 v0, s48, v12\n\t"
        "v_cmp_ne_u32 vcc, 0, v0\n\t"
        "s_cbranch_vccz .L7_sq\n\t"
        "s_sleep 8\n\t"
        "s_sub_i32 s83, s83, 1\n\t"
        "s_cmp_gt_i32 s83, 0\n\t"
        "s_cbranch_scc1 .L7_sp\n"
        ".L7_sq:\n\t"
        "v_add_f32 v21, v21, v12\n\t"
        "v_exp_f32 v21, v21\n\t"
        "v_lshlrev_b32 v22, 4, v1\n\t"
        "v_add_u32 v22, 142848, v22\n\t"
        "s_cmp_lg_u32 s58, 0\n\t"
        "s_cbranch_scc1 .L7_na\n\t"
        "v_add_f32_dpp v0, v21, v21 row_ror:4 row_mask:0xf bank_mask:0xf\n\t"
        "s_cmp_lt_u32 s69, 4\n\t"
        "s_cselect_b32 s78, 16, 1\n\t"
        "v_add_f32_dpp v0, v0, v0 row_ror:8 row_mask:0xf bank_mask:0xf\n\t"
        "v_add_f32 v0, 0x3089705f, v0\n\t"
        "v_rcp_f32 v0, v0\n\t"
        "s_nop 0\n\t"
        "v_mul_f32 v21, v21, v0\n\t"
        "v_cvt_pk_bf16_f32 v21, v21, v21\n\t"
        "v_mov_b32 v0, s78\n\t"
        "s_nop 0\n\t"
        "v_and_b32_dpp v6, v21, v4 quad_perm:[0,0,0,0] row_mask:0xf bank_mask:0xf\n\t"
        "v_and_b32_dpp v7, v21, v5 quad_perm:[0,0,0,0] row_mask:0xf bank_mask:0xf\n\t"
        "v_and_b32_dpp v16, v21, v4 quad_perm:[1,1,1,1] row_mask:0xf bank_mask:0xf\n\t"
        "v_and_b32_dpp v17, v21, v5 quad_perm:[1,1,1,1] row_mask:0xf bank_mask:0xf\n\t"
        "v_mfma_f32_4x4x4_16b_bf16 v[8:11], v[6:7], v[24:25], 0 cbsz:2 abid:0\n\t"
        "v_mfma_f32_4x4x4_16b_bf16 v[12:15], v[6:7], v[26:27], 0 cbsz:2 abid:0\n\t"
        "v_mfma_f32_4x4x4_16b_bf16 v[120:123], v[16:17], v[40:41], 0 cbsz:2 abid:0\n\t"
        "v_mfma_f32_4x4x4_16b_bf16 v[124:127], v[16:17], v[42:43], 0 cbsz:2 abid:0\n\t"
        "v_mfma_f32_4x4x4_16b_bf16 v[8:11], v[6:7], v[28:29], v[8:11] cbsz:2 abid:2\n\t"
        "v_mfma_f32_4x4x4_16b_bf16 v[12:15], v[6:7], v[30:31], v[12:15] cbsz:2 abid:2\n\t"
        "v_mfma_f32_4x4x4_16b_bf16 v[120:123], v[16:17], v[44:45], v[120:123] cbsz:2 abid:2\n\t"
        "v_mfma_f32_4x4x4_16b_bf16 v[124:127], v[16:17], v[46:47], v[124:127] cbsz:2 abid:2\n\t"
        "v_mfma_f32_4x4x4_16b_bf16 v[8:11], v[6:7], v[32:33], v[8:11] cbsz:2 abid:1\n\t"
        "v_mfma_f32_4x4x4_16b_bf16 v[12:15], v[6:7], v[34:35], v[12:15] cbsz:2 abid:1\n\t"
        "v_mfma_f32_4x4x4_16b_bf16 v[120:123], v[16:17], v[48:49], v[120:123] cbsz:2 abid:1\n\t"
        "v_mfma_f32_4x4x4_16b_bf16 v[124:127], v[16:17], v[50:51], v[124:127] cbsz:2 abid:1\n\t"
        "v_mfma_f32_4x4x4_16b_bf16 v[8:11], v[6:7], v[36:37], v[8:11] cbsz:2 abid:3\n\t"
        "v_mfma_f32_4x4x4_16b_bf16 v[12:15], v[6:7], v[38:39], v[12:15] cbsz:2 abid:3\n\t"
        "v_mfma_f32_4x4x4_16b_bf16 v[120:123], v[16:17], v[52:53], v[120:123] cbsz:2 abid:3\n\t"
        "v_mfma_f32_4x4x4_16b_bf16 v[124:127], v[16:17], v[54:55], v[124:127] cbsz:2 abid:3\n\t"
        "v_and_b32_dpp v6, v21, v4 quad_perm:[2,2,2,2] row_mask:0xf bank_mask:0xf\n\t"
        "v_and_b32_dpp v7, v21, v5 quad_perm:[2,2,2,2] row_mask:0xf bank_mask:0xf\n\t"
        "s_nop 0\n\t"
        "s_nop 0\n\t"
        "v_mfma_f32_4x4x4_16b_bf16 v[24:27], v[6:7], v[56:57], 0 cbsz:2 abid:0\n\t"
        "v_mfma_f32_4x4x4_16b_bf16 v[28:31], v[6:7], v[58:59], 0 cbsz:2 abid:0\n\t"
        "v_cvt_pk_bf16_f32 v8, v8, v9\n\t"
        "v_cvt_pk_bf16_f32 v9, v10, v11\n\t"
        "v_cvt_pk_bf16_f32 v10, v12, v13\n\t"
        "v_cvt_pk_bf16_f32 v11, v14, v15\n\t"
        "ds_write_b128 v18, v[8:11] offset:46336\n\t"
        "v_mfma_f32_4x4x4_16b_bf16 v[24:27], v[6:7], v[60:61], v[24:27] cbsz:2 abid:2\n\t"
        "v_mfma_f32_4x4x4_16b_bf16 v[28:31], v[6:7], v[62:63], v[28:31] cbsz:2 abid:2\n\t"
        "v_cvt_pk_bf16_f32 v120, v120, v121\n\t"
        "v_cvt_pk_bf16_f32 v121, v122, v123\n\t"
        "v_cvt_pk_bf16_f32 v122, v124, v125\n\t"
        "v_cvt_pk_bf16_f32 v123, v126, v127\n\t"
        "ds_write_b128 v19, v[120:123] offset:46336\n\t"
        "v_permlane16_swap_b32 v24, v28\n\t"
        "v_permlane16_swap_b32 v25, v29\n\t"
        "v_permlane16_swap_b32 v26, v30\n\t"
        "v_permlane16_swap_b32 v27, v31\n\t"
        "v_pk_add_f32 v[24:25], v[24:25], v[28:29]\n\t"
        "v_pk_add_f32 v[26:27], v[26:27], v[30:31]\n\t"
        "v_cvt_pk_bf16_f32 v24, v24, v25\n\t"
        "v_cvt_pk_bf16_f32 v25, v26, v27\n\t"
        "ds_write_b64 v20, v[24:25] offset:46336\n\t"
        "s_mov_b64 exec, 1\n\t"
        "ds_add_u32 v2, v0 offset:40\n\t"
        "s_mov_b64 exec, -1\n"
        ".L7_na:\n"
        ".L7_w13:\n\t"
        "ds_read_b32 v0, v2 offset:28\n\t"
        "s_waitcnt lgkmcnt(0)\n\t"
        "v_cmp_gt_u16 vcc, 4, v0\n\t"
        "s_cbranch_vccz .L7_d14\n\t"
        "s_sleep 1\n\t"
        "s_sub_i32 s83, s83, 1\n\t"
        "s_cmp_gt_i32 s83, 0\n\t"
        "s_cbranch_scc1 .L7_w13\n"
        ".L7_d14:\n\t"
        "ds_read_b64_tr_b16 v[96:97], v23 offset:0\n\t"
        "ds_read_b64_tr_b16 v[98:99], v23 offset:512\n\t"
        "ds_read_b64_tr_b16 v[100:101], v23 offset:8\n\t"
        "ds_read_b64_tr_b16 v[102:103], v23 offset:520\n\t"
        "ds_read_b64_tr_b16 v[104:105], v23 offset:4096\n\t"
        "ds_read_b64_tr_b16 v[106:107], v23 offset:4608\n\t"
        "ds_read_b64_tr_b16 v[108:109], v23 offset:4104\n\t"
        "ds_read_b64_tr_b16 v[110:111], v23 offset:4616\n\t"
        "ds_read_b64_tr_b16 v[112:113], v23 offset:8192\n\t"
        "ds_read_b64_tr_b16 v[114:115], v23 offset:8704\n\t"
        "ds_read_b64_tr_b16 v[116:117], v23 offset:8200\n\t"
        "ds_read_b64_tr_b16 v[118:119], v23 offset:8712\n\t"
        "s_waitcnt lgkmcnt(6)\n\t"
        "ds_read_b64_tr_b16 v[120:121], v23 offset:12288\n\t"
        "ds_read_b64_tr_b16 v[122:123], v23 offset:12800\n\t"
        "ds_read_b64_tr_b16 v[124:125], v23 offset:12296\n\t"
        "ds_read_b64_tr_b16 v[126:127], v23 offset:12808\n\t"
        "ds_read_b128 v[4:7], v22\n\t"
        "s_lshl_b32 s50, s81, 6\n\t"
        "s_lshl_b32 s51, s69, 10\n\t"
        "s_add_u32 s51, s51, 134656\n\t"
        "s_mov_b32 s49, 0x10001\n\t"
        "s_mov_b32 s48, 0x07060302\n\t"
        "s_mov_b32 s55, 142848\n"
        ".L7_w15:\n\t"
        "ds_read_b32 v0, v2 offset:40\n\t"
        "s_waitcnt lgkmcnt(0)\n\t"
        "v_cmp_gt_u16 vcc, 64, v0\n\t"
        "s_cbranch_vccz .L7_d16\n\t"
        "s_setprio 0\n\t"
        "s_sleep 1\n\t"
        "s_sub_i32 s83, s83, 1\n\t"
        "s_cmp_gt_i32 s83, 0\n\t"
        "s_cbranch_scc1 .L7_w15\n"
        ".L7_d16:\n\t"
        "s_setprio 2\n\t"
        "ds_read_b128 v[8:11], v4 offset:46336\n\t"
        "ds_read_b128 v[12:15], v5 offset:46336\n\t"
        "ds_read_b128 v[16:19], v6 offset:46336\n\t"
        "ds_read_b128 v[20:23], v7 offset:46336\n\t"
        "ds_read_b128 v[48:51], v4 offset:50432\n\t"
        "ds_read_b128 v[52:55], v5 offset:50432\n\t"
        "ds_read_b128 v[56:59], v6 offset:50432\n\t"
        "ds_read_b128 v[60:63], v7 offset:50432\n\t"
        "v_and_b32 v3, 8, v1\n\t"
        "s_waitcnt lgkmcnt(6)\n\t"
        "v_mfma_f32_16x16x32_bf16 v[32:35], v[64:67], v[8:11], 0\n\t"
        "v_mfma_f32_16x16x32_bf16 v[36:39], v[68:71], v[8:11], 0\n\t"
        "v_mfma_f32_16x16x32_bf16 v[32:35], v[72:75], v[12:15], v[32:35]\n\t"
        "v_mfma_f32_16x16x32_bf16 v[36:39], v[76:79], v[12:15], v[36:39]\n\t"
        "s_waitcnt lgkmcnt(4)\n\t"
        "v_mfma_f32_16x16x32_bf16 v[32:35], v[80:83], v[16:19], v[32:35]\n\t"
        "v_mfma_f32_16x16x32_bf16 v[36:39], v[84:87], v[16:19], v[36:39]\n\t"
        "v_mfma_f32_16x16x32_bf16 v[32:35], v[88:91], v[20:23], v[32:35]\n\t"
        "v_mfma_f32_16x16x32_bf16 v[36:39], v[92:95], v[20:23], v[36:39]\n\t"
        "ds_read_b32 v0, v2 offset:40\n\t"
        "v_lshl_add_u32 v8, v3, 8, v4\n\t"
        "v_lshl_add_u32 v12, v3, 8, v5\n\t"
        "v_lshl_add_u32 v16, v3, 8, v6\n\t"
        "v_lshl_add_u32 v20, v3, 8, v7\n\t"
        "ds_read_b128 v[8:11], v8 offset:54528\n\t"
        "ds_read_b128 v[12:15], v12 offset:54528\n\t"
        "ds_read_b128 v[16:19], v16 offset:54528\n\t"
        "ds_read_b128 v[20:23], v20 offset:54528\n\t"
        "s_waitcnt lgkmcnt(7)\n\t"
        "v_mfma_f32_16x16x32_bf16 v[40:43], v[64:67], v[48:51], 0\n\t"
        "v_mfma_f32_16x16x32_bf16 v[44:47], v[68:71], v[48:51], 0\n\t"
        "v_mfma_f32_16x16x32_bf16 v[40:43], v[72:75], v[52:55], v[40:43]\n\t"
        "v_mfma_f32_16x16x32_bf16 v[44:47], v[76:79], v[52:55], v[44:47]\n\t"
        "s_waitcnt lgkmcnt(5)\n\t"
        "v_mfma_f32_16x16x32_bf16 v[40:43], v[80:83], v[56:59], v[40:43]\n\t"
        "v_mfma_f32_16x16x32_bf16 v[44:47], v[84:87], v[56:59], v[44:47]\n\t"
        "v_mfma_f32_16x16x32_bf16 v[40:43], v[88:91], v[60:63], v[40:43]\n\t"
        "v_mfma_f32_16x16x32_bf16 v[44:47], v[92:95], v[60:63], v[44:47]\n\t"
        "s_waitcnt lgkmcnt(4)\n\t"
        "v_cmp_gt_u16 vcc, 68, v0\n\t"
        "s_cbranch_vccz .L7_t0\n"
        ".L7_w17:\n\t"
        "ds_read_b32 v0, v2 offset:40\n\t"
        "s_waitcnt lgkmcnt(0)\n\t"
        "v_cmp_gt_u16 vcc, 68, v0\n\t"
        "s_cbranch_vccz .L7_d18\n\t"
        "s_setprio 0\n\t"
        "s_sleep 1\n\t"
        "s_sub_i32 s83, s83, 1\n\t"
        "s_cmp_gt_i32 s83, 0\n\t"
        "s_cbranch_scc1 .L7_w17\n"
        ".L7_d18:\n\t"
        "s_setprio 2\n\t"
        "v_lshl_add_u32 v8, v3, 8, v4\n\t"
        "v_lshl_add_u32 v12, v3, 8, v5\n\t"
        "v_lshl_add_u32 v16, v3, 8, v6\n\t"
        "v_lshl_add_u32 v20, v3, 8, v7\n\t"
        "ds_read_b128 v[8:11], v8 offset:54528\n\t"
        "ds_read_b128 v[12:15], v12 offset:54528\n\t"
        "ds_read_b128 v[16:19], v16 offset:54528\n\t"
        "ds_read_b128 v[20:23], v20 offset:54528\n"
        ".L7_t0:\n\t"
        "ds_read_b32 v0, v2 offset:52\n\t"
        "ds_read_b128 v[48:51], v4 offset:23808\n\t"
        "ds_read_b128 v[52:55], v5 offset:23808\n\t"
        "ds_read_b128 v[56:59], v6 offset:23808\n\t"
        "ds_read_b128 v[60:63], v7 offset:23808\n\t"
        "s_waitcnt lgkmcnt(7)\n\t"
        "v_mfma_f32_16x16x32_bf16 v[24:27], v[64:67], v[8:11], 0\n\t"
        "v_mfma_f32_16x16x32_bf16 v[28:31], v[68:71], v[8:11], 0\n\t"
        "v_mfma_f32_16x16x32_bf16 v[24:27], v[72:75], v[12:15], v[24:27]\n\t"
        "v_mfma_f32_16x16x32_bf16 v[28:31], v[76:79], v[12:15], v[28:31]\n\t"
        "s_waitcnt lgkmcnt(5)\n\t"
        "v_mfma_f32_16x16x32_bf16 v[24:27], v[80:83], v[16:19], v[24:27]\n\t"
        "v_mfma_f32_16x16x32_bf16 v[28:31], v[84:87], v[16:19], v[28:31]\n\t"
        "v_mfma_f32_16x16x32_bf16 v[24:27], v[88:91], v[20:23], v[24:27]\n\t"
        "v_mfma_f32_16x16x32_bf16 v[28:31], v[92:95], v[20:23], v[28:31]\n\t"
        "ds_read_b128 v[8:11], v4 offset:32000\n\t"
        "ds_read_b128 v[12:15], v5 offset:32000\n\t"
        "ds_read_b128 v[16:19], v6 offset:32000\n\t"
        "ds_read_b128 v[20:23], v7 offset:32000\n\t"
        "s_waitcnt lgkmcnt(8)\n\t"
        "v_cmp_gt_u16 vcc, 6, v0\n\t"
        "s_cbranch_vccz .L7_t1\n"
        ".L7_w19:\n\t"
        "ds_read_b32 v0, v2 offset:52\n\t"
        "s_waitcnt lgkmcnt(0)\n\t"
        "v_cmp_gt_u16 vcc, 6, v0\n\t"
        "s_cbranch_vccz .L7_d20\n\t"
        "s_setprio 0\n\t"
        "s_sleep 1\n\t"
        "s_sub_i32 s83, s83, 1\n\t"
        "s_cmp_gt_i32 s83, 0\n\t"
        "s_cbranch_scc1 .L7_w19\n"
        ".L7_d20:\n\t"
        "s_setprio 2\n\t"
        "ds_read_b128 v[48:51], v4 offset:23808\n\t"
        "ds_read_b128 v[52:55], v5 offset:23808\n\t"
        "ds_read_b128 v[56:59], v6 offset:23808\n\t"
        "ds_read_b128 v[60:63], v7 offset:23808\n\t"
        "ds_read_b128 v[8:11], v4 offset:32000\n\t"
        "ds_read_b128 v[12:15], v5 offset:32000\n\t"
        "ds_read_b128 v[16:19], v6 offset:32000\n\t"
        "ds_read_b128 v[20:23], v7 offset:32000\n"
        ".L7_t1:\n\t"
        "v_lshl_add_u32 v3, v1, 4, s55\n\t"
        "s_waitcnt lgkmcnt(6)\n\t"
        "v_mfma_f32_16x16x32_bf16 v[32:35], v[96:99], v[48:51], v[32:35]\n\t"
        "v_mfma_f32_16x16x32_bf16 v[36:39], v[100:103], v[48:51], v[36:39]\n\t"
        "v_mfma_f32_16x16x32_bf16 v[32:35], v[104:107], v[52:55], v[32:35]\n\t"
        "v_mfma_f32_16x16x32_bf16 v[36:39], v[108:111], v[52:55], v[36:39]\n\t"
        "s_waitcnt lgkmcnt(4)\n\t"
        "v_mfma_f32_16x16x32_bf16 v[32:35], v[112:115], v[56:59], v[32:35]\n\t"
        "v_mfma_f32_16x16x32_bf16 v[36:39], v[116:119], v[56:59], v[36:39]\n\t"
        "v_mfma_f32_16x16x32_bf16 v[32:35], v[120:123], v[60:63], v[32:35]\n\t"
        "v_mfma_f32_16x16x32_bf16 v[36:39], v[124:127], v[60:63], v[36:39]\n\t"
        "ds_read_b128 v[60:63], v3 offset:1024\n\t"
        "s_waitcnt lgkmcnt(3)\n\t"
        "v_mfma_f32_16x16x32_bf16 v[24:27], v[96:99], v[8:11], v[24:27]\n\t"
        "v_mfma_f32_16x16x32_bf16 v[28:31], v[100:103], v[8:11], v[28:31]\n\t"
        "v_mfma_f32_16x16x32_bf16 v[24:27], v[104:107], v[12:15], v[24:27]\n\t"
        "v_mfma_f32_16x16x32_bf16 v[28:31], v[108:111], v[12:15], v[28:31]\n\t"
        "s_waitcnt lgkmcnt(1)\n\t"
        "v_mfma_f32_16x16x32_bf16 v[24:27], v[112:115], v[16:19], v[24:27]\n\t"
        "v_mfma_f32_16x16x32_bf16 v[28:31], v[116:119], v[16:19], v[28:31]\n\t"
        "v_mfma_f32_16x16x32_bf16 v[24:27], v[120:123], v[20:23], v[24:27]\n\t"
        "v_mfma_f32_16x16x32_bf16 v[28:31], v[124:127], v[20:23], v[28:31]\n\t"
        "ds_read_b32 v0, v2 offset:56\n\t"
        "ds_read_b128 v[8:11], v4 offset:27904\n\t"
        "ds_read_b128 v[12:15], v5 offset:27904\n\t"
        "ds_read_b128 v[16:19], v6 offset:27904\n\t"
        "ds_read_b128 v[20:23], v7 offset:27904\n\t"
        "s_waitcnt lgkmcnt(5)\n\t"
        "v_add_u32 v32, 0x10000, v32\n\t"
        "v_add_u32 v33, 0x10000, v33\n\t"
        "v_add_u32 v34, 0x10000, v34\n\t"
        "v_add_u32 v35, 0x10000, v35\n\t"
        "v_perm_b32 v52, v33, v32, s48\n\t"
        "v_perm_b32 v53, v35, v34, s48\n\t"
        "v_cvt_pk_bf16_f32 v54, v36, v37\n\t"
        "v_cvt_pk_bf16_f32 v55, v38, v39\n\t"
        "v_bfi_b32 v52, s49, v60, v52\n\t"
        "v_bfi_b32 v53, s49, v61, v53\n\t"
        "buffer_store_dwordx4 v[52:55], v62, s[20:23], s25 offen sc1\n\t"
        "s_mov_b32 exec_lo, 0x00ff00ff\n\t"
        "s_mov_b32 exec_hi, 0x00ff00ff\n\t"
        "v_add_u32 v24, 0x10000, v24\n\t"
        "v_add_u32 v25, 0x10000, v25\n\t"
        "v_add_u32 v26, 0x10000, v26\n\t"
        "v_add_u32 v27, 0x10000, v27\n\t"
        "v_perm_b32 v48, v25, v24, s48\n\t"
        "v_perm_b32 v49, v27, v26, s48\n\t"
        "v_cvt_pk_bf16_f32 v50, v28, v29\n\t"
        "v_cvt_pk_bf16_f32 v51, v30, v31\n\t"
        "v_bfi_b32 v48, s49, v60, v48\n\t"
        "v_bfi_b32 v49, s49, v61, v49\n\t"
        "buffer_store_dwordx4 v[48:51], v62, s[20:23], s24 offen sc1\n\t"
        "s_mov_b64 exec, -1\n\t"
        "s_waitcnt lgkmcnt(4)\n\t"
        "v_cmp_gt_u16 vcc, 4, v0\n\t"
        "s_cbranch_vccz .L7_t2\n"
        ".L7_w21:\n\t"
        "ds_read_b32 v0, v2 offset:56\n\t"
        "s_waitcnt lgkmcnt(0)\n\t"
        "v_cmp_gt_u16 vcc, 3, v0\n\t"
        "s_cbranch_vccz .L7_d22\n\t"
        "s_setprio 0\n\t"
        "s_sleep 1\n\t"
        "s_sub_i32 s83, s83, 1\n\t"
        "s_cmp_gt_i32 s83, 0\n\t"
        "s_cbranch_scc1 .L7_w21\n"
        ".L7_d22:\n\t"
        "s_setprio 2\n"
        ".L7_w23:\n\t"
        "ds_read_b32 v0, v2 offset:56\n\t"
        "ds_read_b128 v[8:11], v4 offset:27904\n\t"
        "ds_read_b128 v[12:15], v5 offset:27904\n\t"
        "ds_read_b128 v[16:19], v6 offset:27904\n\t"
        "ds_read_b128 v[20:23], v7 offset:27904\n\t"
        "s_waitcnt lgkmcnt(4)\n\t"
        "v_cmp_gt_u16 vcc, 4, v0\n\t"
        "s_cbranch_vccz .L7_d24\n\t"
        "s_waitcnt lgkmcnt(0)\n\t"
        "s_setprio 0\n\t"
        "s_sleep 1\n\t"
        "s_sub_i32 s83, s83, 1\n\t"
        "s_cmp_gt_i32 s83, 0\n\t"
        "s_cbranch_scc1 .L7_w23\n"
        ".L7_d24:\n\t"
        "s_setprio 2\n"
        ".L7_t2:\n\t"
        "s_waitcnt lgkmcnt(2)\n\t"
        "v_mfma_f32_16x16x32_bf16 v[40:43], v[96:99], v[8:11], v[40:43]\n\t"
        "v_mfma_f32_16x16x32_bf16 v[44:47], v[100:103], v[8:11], v[44:47]\n\t"
        "v_mfma_f32_16x16x32_bf16 v[40:43], v[104:107], v[12:15], v[40:43]\n\t"
        "v_mfma_f32_16x16x32_bf16 v[44:47], v[108:111], v[12:15], v[44:47]\n\t"
        "s_waitcnt lgkmcnt(0)\n\t"
        "v_mfma_f32_16x16x32_bf16 v[40:43], v[112:115], v[16:19], v[40:43]\n\t"
        "v_mfma_f32_16x16x32_bf16 v[44:47], v[116:119], v[16:19], v[44:47]\n\t"
        "v_mfma_f32_16x16x32_bf16 v[40:43], v[120:123], v[20:23], v[40:43]\n\t"
        "v_mfma_f32_16x16x32_bf16 v[44:47], v[124:127], v[20:23], v[44:47]\n\t"
        "ds_read_b128 v[8:11], v4 offset:60672\n\t"
        "ds_read_b128 v[12:15], v5 offset:60672\n\t"
        "ds_read_b128 v[16:19], v6 offset:60672\n\t"
        "ds_read_b128 v[20:23], v7 offset:60672\n\t"
        "s_nop 2\n\t"
        "v_add_u32 v40, 0x10000, v40\n\t"
        "v_add_u32 v41, 0x10000, v41\n\t"
        "v_add_u32 v42, 0x10000, v42\n\t"
        "v_add_u32 v43, 0x10000, v43\n\t"
        "v_perm_b32 v56, v41, v40, s48\n\t"
        "v_perm_b32 v57, v43, v42, s48\n\t"
        "v_cvt_pk_bf16_f32 v58, v44, v45\n\t"
        "v_cvt_pk_bf16_f32 v59, v46, v47\n\t"
        "v_bfi_b32 v56, s49, v60, v56\n\t"
        "v_bfi_b32 v57, s49, v61, v57\n\t"
        "buffer_store_dwordx4 v[56:59], v62, s[20:23], s26 offen sc1\n\t"
        "v_mov_b32 v0, 1\n\t"
        "s_mov_b64 exec, 1\n\t"
        "ds_add_u32 v2, v0 offset:96\n\t"
        "s_mov_b64 exec, -1\n\t"
        "s_waitcnt lgkmcnt(3)\n\t"
        "v_mfma_f32_16x16x32_bf16 v[32:35], v[64:67], v[8:11], 0\n\t"
        "v_mfma_f32_16x16x32_bf16 v[36:39], v[68:71], v[8:11], 0\n\t"
        "v_mfma_f32_16x16x32_bf16 v[32:35], v[72:75], v[12:15], v[32:35]\n\t"
        "v_mfma_f32_16x16x32_bf16 v[36:39], v[76:79], v[12:15], v[36:39]\n\t"
        "s_waitcnt lgkmcnt(1)\n\t"
        "v_mfma_f32_16x16x32_bf16 v[32:35], v[80:83], v[16:19], v[32:35]\n\t"
        "v_mfma_f32_16x16x32_bf16 v[36:39], v[84:87], v[16:19], v[36:39]\n\t"
        "v_mfma_f32_16x16x32_bf16 v[32:35], v[88:91], v[20:23], v[32:35]\n\t"
        "v_mfma_f32_16x16x32_bf16 v[36:39], v[92:95], v[20:23], v[36:39]\n\t"
        "ds_read_b128 v[8:11], v4 offset:64768\n\t"
        "ds_read_b128 v[12:15], v5 offset:64768\n\t"
        "ds_read_b128 v[16:19], v6 offset:64768\n\t"
        "ds_read_b128 v[20:23], v7 offset:64768\n\t"
        "s_waitcnt lgkmcnt(2)\n\t"
        "v_mfma_f32_16x16x32_bf16 v[40:43], v[64:67], v[8:11], 0\n\t"
        "v_mfma_f32_16x16x32_bf16 v[44:47], v[68:71], v[8:11], 0\n\t"
        "v_mfma_f32_16x16x32_bf16 v[40:43], v[72:75], v[12:15], v[40:43]\n\t"
        "v_mfma_f32_16x16x32_bf16 v[44:47], v[76:79], v[12:15], v[44:47]\n\t"
        "s_waitcnt lgkmcnt(0)\n\t"
        "v_mfma_f32_16x16x32_bf16 v[40:43], v[80:83], v[16:19], v[40:43]\n\t"
        "v_mfma_f32_16x16x32_bf16 v[44:47], v[84:87], v[16:19], v[44:47]\n\t"
        "v_mfma_f32_16x16x32_bf16 v[40:43], v[88:91], v[20:23], v[40:43]\n\t"
        "v_mfma_f32_16x16x32_bf16 v[44:47], v[92:95], v[20:23], v[44:47]\n\t"
        "ds_read_b128 v[68:71], v3 offset:2048\n\t"
        "ds_read_b128 v[74:77], v3 offset:3072\n"
        ".L7_w25:\n\t"
        "ds_read_b32 v0, v2 offset:60\n\t"
        "s_waitcnt lgkmcnt(0)\n\t"
        "v_cmp_gt_u16 vcc, 2, v0\n\t"
        "s_cbranch_vccz .L7_d26\n\t"
        "s_setprio 0\n\t"
        "s_sleep 1\n\t"
        "s_sub_i32 s83, s83, 1\n\t"
        "s_cmp_gt_i32 s83, 0\n\t"
        "s_cbranch_scc1 .L7_w25\n"
        ".L7_d26:\n\t"
        "s_setprio 2\n\t"
        "ds_read_b128 v[8:11], v4 offset:34048\n\t"
        "ds_read_b128 v[12:15], v5 offset:34048\n\t"
        "ds_read_b128 v[16:19], v6 offset:34048\n\t"
        "ds_read_b128 v[20:23], v7 offset:34048\n\t"
        "v_add_u32 v71, s51, v71\n\t"
        "v_add_u32 v72, s51, v74\n\t"
        "v_mov_b64 v[74:75], 0\n\t"
        "v_mov_b64 v[78:79], 0\n\t"
        "s_waitcnt lgkmcnt(2)\n\t"
        "v_mfma_f32_16x16x32_bf16 v[24:27], v[96:99], v[8:11], v[24:27]\n\t"
        "v_mfma_f32_16x16x32_bf16 v[28:31], v[100:103], v[8:11], v[28:31]\n\t"
        "v_mfma_f32_16x16x32_bf16 v[24:27], v[104:107], v[12:15], v[24:27]\n\t"
        "v_mfma_f32_16x16x32_bf16 v[28:31], v[108:111], v[12:15], v[28:31]\n\t"
        "s_waitcnt lgkmcnt(0)\n\t"
        "v_mfma_f32_16x16x32_bf16 v[24:27], v[112:115], v[16:19], v[24:27]\n\t"
        "v_mfma_f32_16x16x32_bf16 v[28:31], v[116:119], v[16:19], v[28:31]\n\t"
        "v_mfma_f32_16x16x32_bf16 v[24:27], v[120:123], v[20:23], v[24:27]\n\t"
        "v_mfma_f32_16x16x32_bf16 v[28:31], v[124:127], v[20:23], v[28:31]\n"
        ".L7_w27:\n\t"
        "ds_read_b32 v0, v2 offset:100\n\t"
        "s_waitcnt lgkmcnt(0)\n\t"
        "v_cmp_gt_u16 vcc, 1, v0\n\t"
        "s_cbranch_vccz .L7_d28\n\t"
        "s_setprio 0\n\t"
        "s_sleep 1\n\t"
        "s_sub_i32 s83, s83, 1\n\t"
        "s_cmp_gt_i32 s83, 0\n\t"
        "s_cbranch_scc1 .L7_w27\n"
        ".L7_d28:\n\t"
        "s_setprio 2\n\t"
        "buffer_load_dwordx4 v[48:51], v63, s[20:23], s27 offen sc1\n\t"
        "buffer_load_dwordx4 v[52:55], v62, s[20:23], s28 offen sc1\n\t"
        "buffer_load_dwordx4 v[56:59], v62, s[20:23], s29 offen sc1\n"
        ".L7_w29:\n\t"
        "ds_read_b32 v0, v2 offset:64\n\t"
        "s_waitcnt lgkmcnt(0)\n\t"
        "v_cmp_gt_u16 vcc, 4, v0\n\t"
        "s_cbranch_vccz .L7_d30\n\t"
        "s_setprio 0\n\t"
        "s_sleep 1\n\t"
        "s_sub_i32 s83, s83, 1\n\t"
        "s_cmp_gt_i32 s83, 0\n\t"
        "s_cbranch_scc1 .L7_w29\n"
        ".L7_d30:\n\t"
        "s_setprio 2\n\t"
        "ds_read_b128 v[8:11], v4 offset:38144\n\t"
        "ds_read_b128 v[12:15], v5 offset:38144\n\t"
        "ds_read_b128 v[16:19], v6 offset:38144\n\t"
        "ds_read_b128 v[20:23], v7 offset:38144\n\t"
        "s_waitcnt lgkmcnt(2)\n\t"
        "v_mfma_f32_16x16x32_bf16 v[32:35], v[96:99], v[8:11], v[32:35]\n\t"
        "v_mfma_f32_16x16x32_bf16 v[36:39], v[100:103], v[8:11], v[36:39]\n\t"
        "v_mfma_f32_16x16x32_bf16 v[32:35], v[104:107], v[12:15], v[32:35]\n\t"
        "v_mfma_f32_16x16x32_bf16 v[36:39], v[108:111], v[12:15], v[36:39]\n\t"
        "s_waitcnt lgkmcnt(0)\n\t"
        "v_mfma_f32_16x16x32_bf16 v[32:35], v[112:115], v[16:19], v[32:35]\n\t"
        "v_mfma_f32_16x16x32_bf16 v[36:39], v[116:119], v[16:19], v[36:39]\n\t"
        "v_mfma_f32_16x16x32_bf16 v[32:35], v[120:123], v[20:23], v[32:35]\n\t"
        "v_mfma_f32_16x16x32_bf16 v[36:39], v[124:127], v[20:23], v[36:39]\n\t"
        "s_waitcnt vmcnt(0)\n\t"
        "s_branch .L7_s32\n"
        ".L7_r31:\n\t"
        "buffer_load_dwordx4 v[48:51], v63, s[20:23], s27 offen sc1\n\t"
        "buffer_load_dwordx4 v[52:55], v62, s[20:23], s28 offen sc1\n\t"
        "buffer_load_dwordx4 v[56:59], v62, s[20:23], s29 offen sc1\n\t"
        "s_waitcnt vmcnt(0)\n"
        ".L7_s32:\n\t"
        "v_xor_b32 v48, v48, v60\n\t"
        "v_xor_b32 v49, v49, v61\n\t"
        "v_xor_b32 v52, v52, v60\n\t"
        "v_xor_b32 v53, v53, v61\n\t"
        "v_xor_b32 v56, v56, v60\n\t"
        "v_xor_b32 v57, v57, v61\n\t"
        "v_or_b32 v0, v48, v49\n\t"
        "v_or3_b32 v0, v0, v52, v53\n\t"
        "v_or3_b32 v0, v0, v56, v57\n\t"
        "v_and_b32 v0, s49, v0\n\t"
        "v_cmp_ne_u32 vcc, 0, v0\n\t"
        "s_cbranch_vccz .L7_t33\n\t"
        "s_sleep 8\n\t"
        "s_sub_i32 s83, s83, 1\n\t"
        "s_cmp_gt_i32 s83, 0\n\t"
        "s_cbranch_scc1 .L7_r31\n"
        ".L7_t33:\n\t"
        "v_mfma_f32_16x16x32_bf16 v[24:27], v[76:79], v[48:51], v[24:27]\n\t"
        "v_mfma_f32_16x16x32_bf16 v[28:31], v[74:77], v[48:51], v[28:31]\n\t"
        "v_mfma_f32_16x16x32_bf16 v[32:35], v[76:79], v[52:55], v[32:35]\n\t"
        "v_mfma_f32_16x16x32_bf16 v[36:39], v[74:77], v[52:55], v[36:39]\n\t"
        "v_mfma_f32_16x16x32_bf16 v[40:43], v[76:79], v[56:59], v[40:43]\n\t"
        "v_mfma_f32_16x16x32_bf16 v[44:47], v[74:77], v[56:59], v[44:47]\n\t"
        "s_nop 2\n\t"
        "v_cvt_pk_bf16_f32 v48, v24, v25\n\t"
        "v_cvt_pk_bf16_f32 v49, v26, v27\n\t"
        "v_cvt_pk_bf16_f32 v50, v28, v29\n\t"
        "v_cvt_pk_bf16_f32 v51, v30, v31\n\t"
        "ds_write_b128 v71, v[48:51]\n\t"
        "ds_read_b128 v[48:51], v72\n\t"
        "v_cvt_pk_bf16_f32 v52, v32, v33\n\t"
        "v_cvt_pk_bf16_f32 v53, v34, v35\n\t"
        "v_cvt_pk_bf16_f32 v54, v36, v37\n\t"
        "v_cvt_pk_bf16_f32 v55, v38, v39\n\t"
        "ds_write_b128 v71, v[52:55]\n\t"
        "ds_read_b128 v[52:55], v72\n\t"
        "ds_read_b32 v0, v2 offset:68\n\t"
        "ds_read_b128 v[8:11], v4 offset:42240\n\t"
        "ds_read_b128 v[12:15], v5 offset:42240\n\t"
        "ds_read_b128 v[16:19], v6 offset:42240\n\t"
        "ds_read_b128 v[20:23], v7 offset:42240\n\t"
        "s_waitcnt lgkmcnt(4)\n\t"
        "buffer_store_dwordx4 v[48:51], v68, s[60:63], s50 offen sc1\n\t"
        "buffer_store_dwordx4 v[52:55], v69, s[60:63], s50 offen sc1\n\t"
        "v_cmp_gt_u16 vcc, 4, v0\n\t"
        "s_cbranch_vccz .L7_t5\n"
        ".L7_w34:\n\t"
        "ds_read_b32 v0, v2 offset:68\n\t"
        "s_waitcnt lgkmcnt(0)\n\t"
        "v_cmp_gt_u16 vcc, 3, v0\n\t"
        "s_cbranch_vccz .L7_d35\n\t"
        "s_setprio 0\n\t"
        "s_sleep 1\n\t"
        "s_sub_i32 s83, s83, 1\n\t"
        "s_cmp_gt_i32 s83, 0\n\t"
        "s_cbranch_scc1 .L7_w34\n"
        ".L7_d35:\n\t"
        "s_setprio 2\n"
        ".L7_w36:\n\t"
        "ds_read_b32 v0, v2 offset:68\n\t"
        "ds_read_b128 v[8:11], v4 offset:42240\n\t"
        "ds_read_b128 v[12:15], v5 offset:42240\n\t"
        "ds_read_b128 v[16:19], v6 offset:42240\n\t"
        "ds_read_b128 v[20:23], v7 offset:42240\n\t"
        "s_waitcnt lgkmcnt(4)\n\t"
        "v_cmp_gt_u16 vcc, 4, v0\n\t"
        "s_cbranch_vccz .L7_d37\n\t"
        "s_waitcnt lgkmcnt(0)\n\t"
        "s_setprio 0\n\t"
        "s_sleep 0\n\t"
        "s_sub_i32 s83, s83, 1\n\t"
        "s_cmp_gt_i32 s83, 0\n\t"
        "s_cbranch_scc1 .L7_w36\n"
        ".L7_d37:\n\t"
        "s_setprio 2\n"
        ".L7_t5:\n\t"
        "s_waitcnt lgkmcnt(2)\n\t"
        "v_mfma_f32_16x16x32_bf16 v[40:43], v[96:99], v[8:11], v[40:43]\n\t"
        "v_mfma_f32_16x16x32_bf16 v[44:47], v[100:103], v[8:11], v[44:47]\n\t"
        "v_mfma_f32_16x16x32_bf16 v[40:43], v[104:107], v[12:15], v[40:43]\n\t"
        "v_mfma_f32_16x16x32_bf16 v[44:47], v[108:111], v[12:15], v[44:47]\n\t"
        "s_waitcnt lgkmcnt(0)\n\t"
        "v_mfma_f32_16x16x32_bf16 v[40:43], v[112:115], v[16:19], v[40:43]\n\t"
        "v_mfma_f32_16x16x32_bf16 v[44:47], v[116:119], v[16:19], v[44:47]\n\t"
        "v_mfma_f32_16x16x32_bf16 v[40:43], v[120:123], v[20:23], v[40:43]\n\t"
        "v_mfma_f32_16x16x32_bf16 v[44:47], v[124:127], v[20:23], v[44:47]\n\t"
        "s_nop 6\n\t"
        "v_cvt_pk_bf16_f32 v56, v40, v41\n\t"
        "v_cvt_pk_bf16_f32 v57, v42, v43\n\t"
        "v_cvt_pk_bf16_f32 v58, v44, v45\n\t"
        "v_cvt_pk_bf16_f32 v59, v46, v47\n\t"
        "ds_write_b128 v71, v[56:59]\n\t"
        "ds_read_b128 v[56:59], v72\n\t"
        "s_waitcnt lgkmcnt(0)\n\t"
        "buffer_store_dwordx4 v[56:59], v70, s[60:63], s50 offen sc1\n\t"
        "s_or_b32 s78, %3, s69\n\t"
        "s_cbranch_scc1 .L7_ng\n\t"
        "v_mov_b32 v0, s68\n\t"
        "buffer_store_dword v0, off, s[20:23], 0 sc1\n"
        ".L7_ng:\n\t"
        "s_endpgm\n"
        ".L7_G2:\n\t"
        "s_setprio 3\n\t"
        "v_lshlrev_b32 v5, 4, v1\n\t"
        "s_mul_i32 s82, s76, 13\n\t"
        "s_lshr_b32 s82, s82, 6\n\t"
        "s_mul_i32 s82, s82, 5\n\t"
        "s_sub_u32 s82, s76, s82\n\t"
        "s_waitcnt lgkmcnt(0)\n\t"
        "s_lshl2_add_u32 s78, s82, s69\n\t"
        "s_lshl_b32 s78, s78, 10\n\t"
        "s_add_u32 m0, s78, 46336\n\t"
        "s_add_u32 s82, s82, 1\n\t"
        "s_cmp_eq_u32 s82, 5\n\t"
        "buffer_load_dwordx4 v5, s[56:59], s78 offen lds\n\t"
        "s_cselect_b32 s82, 0, s82\n\t"
        "v_lshrrev_b32 v18, 3, v1\n\t"
        "v_lshlrev_b32 v18, 14, v18\n\t"
        "v_and_b32 v6, 0x70, v5\n\t"
        "s_lshl_b32 s78, s69, 19\n\t"
        "s_lshl_b32 s79, s71, 7\n\t"
        "s_add_u32 s78, s78, s79\n\t"
        "v_add3_u32 v18, v18, v6, s78\n\t"
        "s_mov_b32 s24, 1048576\n\t"
        "s_mov_b32 s25, 1179648\n\t"
        "s_mov_b32 s26, 1310720\n\t"
        "s_mov_b32 s27, 1441792\n\t"
        "v_and_b32 v6, 0xf0, v5\n\t"
        "s_lshl_b32 s78, s70, 8\n\t"
        "v_add_u32 v62, s78, v6\n\t"
        "v_mov_b32 v7, 113860\n\t"
        "s_movk_i32 s38, 196\n\t"
        "s_mov_b32 s39, 12845056\n\t"
        "s_lshl_b32 s33, s69, 3\n\t"
        "s_lshl_b32 s78, 1, s33\n\t"
        "v_mov_b32 v29, s78\n\t"
        "s_sub_u32 s32, s78, 1\n\t"
        "s_and_b32 s32, s32, 0x01010101\n\t"
        "s_mov_b32 s35, 0xffffff\n\t"
        "s_mov_b32 s40, 0x13050109\n\t"
        "s_mov_b32 s41, 23\n\t"
        "s_cmp_eq_u32 s69, 1\n\t"
        "s_cselect_b32 s40, 0x12040008, s40\n\t"
        "s_cselect_b32 s41, 22, s41\n\t"
        "s_cmp_eq_u32 s69, 2\n\t"
        "s_cselect_b32 s40, 0x100e0602, s40\n\t"
        "s_cselect_b32 s41, 20, s41\n\t"
        "s_cmp_eq_u32 s69, 3\n\t"
        "s_cselect_b32 s40, 0x110f0703, s40\n\t"
        "s_cselect_b32 s41, 21, s41\n\t"
        "s_mul_i32 s41, s41, 0x01010101\n\t"
        "s_bitcmp1_b32 s40, 3\n\t"
        "s_cselect_b32 s78, 0, 4\n\t"
        "v_mov_b32 v61, s78\n\t"
        "v_lshrrev_b32 v12, 4, v1\n\t"
        "s_bfe_u32 s78, s40, 0x80000\n\t"
        "s_lshl_b32 s78, s78, 2\n\t"
        "v_add_u32 v13, s78, v12\n\t"
        "v_lshlrev_b32 v24, 2, v13\n\t"
        "v_lshlrev_b32 v15, 4, v13\n\t"
        "v_and_b32 v15, 0xf0, v15\n\t"
        "v_xor_b32 v15, v15, v6\n\t"
        "v_lshl_add_u32 v56, v13, 8, v15\n\t"
        "s_bfe_u32 s78, s40, 0x80008\n\t"
        "s_lshl_b32 s78, s78, 2\n\t"
        "v_add_u32 v13, s78, v12\n\t"
        "v_lshlrev_b32 v25, 2, v13\n\t"
        "v_lshlrev_b32 v15, 4, v13\n\t"
        "v_and_b32 v15, 0xf0, v15\n\t"
        "v_xor_b32 v15, v15, v6\n\t"
        "v_lshl_add_u32 v57, v13, 8, v15\n\t"
        "s_bfe_u32 s78, s40, 0x80010\n\t"
        "s_lshl_b32 s78, s78, 2\n\t"
        "v_add_u32 v13, s78, v12\n\t"
        "v_lshlrev_b32 v26, 2, v13\n\t"
        "v_lshlrev_b32 v15, 4, v13\n\t"
        "v_and_b32 v15, 0xf0, v15\n\t"
        "v_xor_b32 v15, v15, v6\n\t"
        "v_lshl_add_u32 v58, v13, 8, v15\n\t"
        "v_lshlrev_b32 v15, 9, v61\n\t"
        "v_sub_u32 v58, v58, v15\n\t"
        "s_bfe_u32 s78, s40, 0x80018\n\t"
        "s_lshl_b32 s78, s78, 2\n\t"
        "v_add_u32 v13, s78, v12\n\t"
        "v_lshlrev_b32 v27, 2, v13\n\t"
        "v_lshlrev_b32 v15, 4, v13\n\t"
        "v_and_b32 v15, 0xf0, v15\n\t"
        "v_xor_b32 v15, v15, v6\n\t"
        "v_lshl_add_u32 v59, v13, 8, v15\n\t"
        "s_bfe_u32 s78, s41, 0x80000\n\t"
        "s_lshl_b32 s78, s78, 2\n\t"
        "v_add_u32 v13, s78, v12\n\t"
        "v_lshlrev_b32 v28, 2, v13\n\t"
        "v_lshlrev_b32 v15, 4, v13\n\t"
        "v_and_b32 v15, 0xf0, v15\n\t"
        "v_xor_b32 v15, v15, v6\n\t"
        "v_lshl_add_u32 v60, v13, 8, v15\n\t"
        "v_and_b32 v20, 28, v1\n\t"
        "v_lshlrev_b32 v20, 1, v20\n\t"
        "v_lshrrev_b64 v[12:13], v20, s[40:41]\n\t"
        "v_and_b32 v13, 0xff, v12\n\t"
        "v_and_b32 v12, 3, v1\n\t"
        "v_lshl_add_u32 v13, v13, 2, v12\n\t"
        "v_lshlrev_b32 v31, 2, v13\n\t"
        "s_and_b32 s78, s67, 0x10000\n\t"
        "s_or_b32 s78, s78, 1\n\t"
        "v_mov_b32 v30, s78\n\t"
        "s_mov_b32 s36, 0\n\t"
        "v_and_b32 v0, 48, v5\n\t"
        "s_mov_b32 s78, 0x0f0e\n\t"
        "s_mov_b32 s79, 0\n\t"
        "v_lshlrev_b64 v[8:9], v0, s[78:79]\n\t"
        "s_mov_b32 s78, 0x0b0a\n\t"
        "v_lshlrev_b64 v[12:13], v0, s[78:79]\n\t"
        "s_mov_b32 s78, 0xf0f0f0f0\n\t"
        "s_mov_b32 s79, s78\n\t"
        "v_cndmask_b32 v8, v8, v12, s[78:79]\n\t"
        "v_cndmask_b32 v9, v9, v13, s[78:79]\n\t"
        "v_xor_b32 v8, 0x0c0c0c0c, v8\n\t"
        "v_xor_b32 v9, 0x0c0c0c0c, v9\n\t"
        "s_movk_i32 s48, 0x200\n\t"
        "s_mov_b32 s37, 0x01010101\n\t"
        "s_mov_b32 s49, 0x05040100\n\t"
        "s_getpc_b64 s[50:51]\n"
        ".L7_pc:\n\t"
        "s_add_u32 s52, s50, .L7_ce-.L7_pc\n\t"
        "s_addc_u32 s53, s51, 0\n\t"
        "s_add_u32 s42, s50, .L7_ev-.L7_pc\n\t"
        "s_addc_u32 s43, s51, 0\n\t"
        "s_add_u32 s50, s50, .L7_ie-.L7_pc\n\t"
        "s_addc_u32 s51, s51, 0\n\t"
        "s_cmp_eq_u32 s80, 10\n\t"
        "s_cselect_b32 s80, 0, s80\n\t"
        "s_mul_i32 s78, s80, 0x1080\n\t"
        "s_add_u32 m0, s78, s81\n\t"
        "s_lshr_b32 s78, s80, 1\n\t"
        "s_lshl1_add_u32 s78, s78, s80\n\t"
        "s_lshl_b32 s78, s78, 11\n\t"
        "buffer_load_dwordx4 v4, s[16:19], s78 offen lds\n\t"
        "s_add_u32 s80, s80, 1\n\t"
        "s_mul_i32 s78, s80, 0x1080\n\t"
        "s_add_u32 m0, s78, s81\n\t"
        "s_lshr_b32 s78, s80, 1\n\t"
        "s_lshl1_add_u32 s78, s78, s80\n\t"
        "s_lshl_b32 s78, s78, 11\n\t"
        "buffer_load_dwordx4 v4, s[16:19], s78 offen lds\n\t"
        "s_add_u32 s80, s80, 1\n\t"
        "s_mul_i32 s78, s80, 0x1080\n\t"
        "s_add_u32 m0, s78, s81\n\t"
        "s_lshr_b32 s78, s80, 1\n\t"
        "s_lshl1_add_u32 s78, s78, s80\n\t"
        "s_lshl_b32 s78, s78, 11\n\t"
        "buffer_load_dwordx4 v4, s[16:19], s78 offen lds\n\t"
        "s_add_u32 s80, s80, 1\n\t"
        "s_mul_i32 s78, s80, 0x1080\n\t"
        "s_add_u32 m0, s78, s81\n\t"
        "s_lshr_b32 s78, s80, 1\n\t"
        "s_lshl1_add_u32 s78, s78, s80\n\t"
        "s_lshl_b32 s78, s78, 11\n\t"
        "buffer_load_dwordx4 v4, s[16:19], s78 offen lds\n\t"
        "s_add_u32 s80, s80, 1\n\t"
        "s_mul_i32 s78, s80, 0x1080\n\t"
        "s_add_u32 m0, s78, s81\n\t"
        "s_lshr_b32 s78, s80, 1\n\t"
        "s_lshl1_add_u32 s78, s78, s80\n\t"
        "s_lshl_b32 s78, s78, 11\n\t"
        "buffer_load_dwordx4 v4, s[16:19], s78 offen lds\n\t"
        "s_add_u32 s80, s80, 1\n\t"
        "s_lshl2_add_u32 s78, s82, s69\n\t"
        "s_lshl_b32 s78, s78, 10\n\t"
        "s_add_u32 m0, s78, 46336\n\t"
        "s_add_u32 s82, s82, 1\n\t"
        "s_cmp_eq_u32 s82, 5\n\t"
        "buffer_load_dwordx4 v5, s[56:59], s78 offen lds\n\t"
        "s_cselect_b32 s82, 0, s82\n\t"
        "s_lshl2_add_u32 s78, s82, s69\n\t"
        "s_lshl_b32 s78, s78, 10\n\t"
        "s_add_u32 m0, s78, 46336\n\t"
        "s_add_u32 s82, s82, 1\n\t"
        "s_cmp_eq_u32 s82, 5\n\t"
        "buffer_load_dwordx4 v5, s[56:59], s78 offen lds\n\t"
        "s_cselect_b32 s82, 0, s82\n\t"
        "s_lshl2_add_u32 s78, s82, s69\n\t"
        "s_lshl_b32 s78, s78, 10\n\t"
        "s_add_u32 m0, s78, 46336\n\t"
        "s_add_u32 s82, s82, 1\n\t"
        "s_cmp_eq_u32 s82, 5\n\t"
        "buffer_load_dwordx4 v5, s[56:59], s78 offen lds\n\t"
        "s_cselect_b32 s82, 0, s82\n\t"
        "s_lshl2_add_u32 s78, s82, s69\n\t"
        "s_lshl_b32 s78, s78, 10\n\t"
        "s_add_u32 m0, s78, 46336\n\t"
        "s_add_u32 s82, s82, 1\n\t"
        "s_cmp_eq_u32 s82, 5\n\t"
        "buffer_load_dwordx4 v5, s[56:59], s78 offen lds\n\t"
        "s_cselect_b32 s82, 0, s82\n\t"
        "s_mov_b64 exec, 1\n\t"
        "ds_add_u32 v2, v3 offset:80\n\t"
        "s_mov_b64 exec, -1\n\t"
        "s_waitcnt vmcnt(4)\n\t"
        "s_mov_b64 exec, 1\n\t"
        "ds_add_u32 v2, v3 offset:0\n\t"
        "s_mov_b64 exec, -1\n"
        ".L7_w72:\n\t"
        "ds_read_b64 v[12:13], v2 offset:72\n\t"
        "s_waitcnt lgkmcnt(0)\n\t"
        "v_cmp_gt_u16 vcc, 2, v12\n\t"
        "s_cbranch_vccz .L7_d73\n\t"
        "s_sleep 1\n\t"
        "s_sub_i32 s83, s83, 1\n\t"
        "s_cmp_gt_i32 s83, 0\n\t"
        "s_cbranch_scc1 .L7_w72\n"
        ".L7_d73:\n\t"
        "v_readfirstlane_b32 s68, v13\n"
        ".L7_w74:\n\t"
        "ds_read_b32 v0, v2 offset:8\n\t"
        "s_waitcnt lgkmcnt(0)\n\t"
        "v_cmp_gt_u16 vcc, 2, v0\n\t"
        "s_cbranch_vccz .L7_d75\n\t"
        "s_sleep 1\n\t"
        "s_sub_i32 s83, s83, 1\n\t"
        "s_cmp_gt_i32 s83, 0\n\t"
        "s_cbranch_scc1 .L7_w74\n"
        ".L7_d75:\n\t"
        "ds_read_b32 v5, v31 offset:512\n\t"
        "ds_read_b32 v24, v24 offset:512\n\t"
        "ds_read_b32 v25, v25 offset:512\n\t"
        "ds_read_b32 v26, v26 offset:512\n\t"
        "ds_read_b32 v27, v27 offset:512\n\t"
        "ds_read_b32 v28, v28 offset:512\n\t"
        "s_waitcnt lgkmcnt(0)\n\t"
        "v_dot4_u32_u8 v31, v5, s38, v7\n\t"
        "s_waitcnt vmcnt(0)\n\t"
        "s_mov_b64 exec, 1\n\t"
        "ds_add_u32 v2, v3 offset:4\n\t"
        "s_mov_b64 exec, -1\n\t"
        "s_setprio 3\n\t"
        "s_lshl_b32 s44, s68, 24\n\t"
        "s_xor_b32 s45, s44, 0x5a000000\n\t"
        "s_mul_i32 s46, s68, 0x01010000\n\t"
        "s_mul_i32 s47, s68, 0x4000400\n\t"
        "s_and_b32 s47, s47, 0xff000000\n\t"
        "s_or_b32 s47, s47, 0xff0000\n\t"
        "s_and_b32 s46, s46, 0xff000000\n\t"
        "s_mul_i32 s34, s32, 0xff\n"
        ".L7_w76:\n\t"
        "ds_read_b32 v0, v2 offset:92\n\t"
        "s_waitcnt lgkmcnt(0)\n\t"
        "v_cmp_gt_u16 vcc, 1, v0\n\t"
        "s_cbranch_vccz .L7_d77\n\t"
        "s_sleep 1\n\t"
        "s_sub_i32 s83, s83, 1\n\t"
        "s_cmp_gt_i32 s83, 0\n\t"
        "s_cbranch_scc1 .L7_w76\n"
        ".L7_d77:\n\t"
        "v_readfirstlane_b32 s78, v0\n\t"
        "s_sub_u32 s78, s78, 1\n\t"
        "s_add_u32 s77, s74, s78\n"
        ".L7_g78:\n\t"
        "s_memrealtime s[78:79]\n\t"
        "s_waitcnt lgkmcnt(0)\n\t"
        "s_sub_u32 s78, s78, s77\n\t"
        "s_cmp_ge_i32 s78, 305\n\t"
        "s_cbranch_scc1 .L7_h79\n\t"
        "s_sleep 1\n\t"
        "s_branch .L7_g78\n"
        ".L7_h79:\n\t"
        "buffer_load_dwordx4 v[64:67], v18, s[20:23], s24 offen sc1\n\t"
        "buffer_load_dwordx4 v[68:71], v18, s[20:23], s25 offen sc1\n\t"
        "buffer_load_dwordx4 v[72:75], v18, s[20:23], s26 offen sc1\n\t"
        "buffer_load_dwordx4 v[76:79], v18, s[20:23], s27 offen sc1\n\t"
        "s_mov_b64 exec, 1\n\t"
        "ds_add_u32 v2, v3 offset:108\n\t"
        "s_mov_b64 exec, -1\n"
        ".L7_w80:\n\t"
        "ds_read_b32 v0, v2 offset:4\n\t"
        "s_waitcnt lgkmcnt(0)\n\t"
        "v_cmp_gt_u16 vcc, 4, v0\n\t"
        "s_cbranch_vccz .L7_d81\n\t"
        "s_sleep 1\n\t"
        "s_sub_i32 s83, s83, 1\n\t"
        "s_cmp_gt_i32 s83, 0\n\t"
        "s_cbranch_scc1 .L7_w80\n"
        ".L7_d81:\n\t"
        "s_branch .L7_ll\n"
        ".L7_again:\n\t"
        "s_waitcnt lgkmcnt(0)\n\t"
        "s_sleep 8\n\t"
        "s_sub_i32 s83, s83, 16\n\t"
        "s_cmp_gt_i32 s83, 0\n\t"
        "s_cbranch_scc0 .L7_got\n\t"
        "buffer_load_dwordx4 v[64:67], v18, s[20:23], s24 offen sc1\n\t"
        "buffer_load_dwordx4 v[68:71], v18, s[20:23], s25 offen sc1\n\t"
        "buffer_load_dwordx4 v[72:75], v18, s[20:23], s26 offen sc1\n\t"
        "buffer_load_dwordx4 v[76:79], v18, s[20:23], s27 offen sc1\n"
        ".L7_ll:\n\t"
        "s_waitcnt vmcnt(3)\n\t"
        "v_xor_b32 v64, s44, v64\n\t"
        "v_xor_b32 v65, s45, v65\n\t"
        "v_xor_b32 v66, s46, v66\n\t"
        "v_xor_b32 v4, s47, v67\n\t"
        "v_or_b32 v5, v64, v65\n\t"
        "v_lshlrev_b32_sdwa v112, 1, v64 dst_sel:DWORD dst_unused:UNUSED_PAD src0_sel:DWORD src1_sel:WORD_0\n\t"
        "ds_read_u16 v112, v112 offset:46336\n\t"
        "v_lshlrev_b32_sdwa v113, 1, v65 dst_sel:DWORD dst_unused:UNUSED_PAD src0_sel:DWORD src1_sel:WORD_0\n\t"
        "ds_read_u16 v113, v113 offset:46336\n\t"
        "v_lshlrev_b32_sdwa v114, 1, v66 dst_sel:DWORD dst_unused:UNUSED_PAD src0_sel:DWORD src1_sel:WORD_0\n\t"
        "ds_read_u16 v114, v114 offset:46336\n\t"
        "v_mad_u32_u16 v80, v64, s38, v7 op_sel:[1,0,0,0]\n\t"
        "v_mad_u32_u16 v81, v65, s38, v7 op_sel:[1,0,0,0]\n\t"
        "v_mad_u32_u16 v82, v66, s38, v7 op_sel:[1,0,0,0]\n\t"
        "s_waitcnt vmcnt(2)\n\t"
        "v_xor_b32 v68, s44, v68\n\t"
        "v_xor_b32 v69, s45, v69\n\t"
        "v_xor_b32 v70, s46, v70\n\t"
        "v_bitop3_b32 v4, v71, v4, s47 bitop3:0xde\n\t"
        "v_or3_b32 v5, v5, v68, v69\n\t"
        "v_or3_b32 v5, v5, v66, v70\n\t"
        "v_lshlrev_b32_sdwa v116, 1, v68 dst_sel:DWORD dst_unused:UNUSED_PAD src0_sel:DWORD src1_sel:WORD_0\n\t"
        "ds_read_u16 v116, v116 offset:46336\n\t"
        "v_lshlrev_b32_sdwa v117, 1, v69 dst_sel:DWORD dst_unused:UNUSED_PAD src0_sel:DWORD src1_sel:WORD_0\n\t"
        "ds_read_u16 v117, v117 offset:46336\n\t"
        "v_lshlrev_b32_sdwa v118, 1, v70 dst_sel:DWORD dst_unused:UNUSED_PAD src0_sel:DWORD src1_sel:WORD_0\n\t"
        "ds_read_u16 v118, v118 offset:46336\n\t"
        "v_mad_u32_u16 v84, v68, s38, v7 op_sel:[1,0,0,0]\n\t"
        "v_mad_u32_u16 v85, v69, s38, v7 op_sel:[1,0,0,0]\n\t"
        "v_mad_u32_u16 v86, v70, s38, v7 op_sel:[1,0,0,0]\n\t"
        "s_waitcnt vmcnt(1)\n\t"
        "v_xor_b32 v72, s44, v72\n\t"
        "v_xor_b32 v73, s45, v73\n\t"
        "v_xor_b32 v74, s46, v74\n\t"
        "v_bitop3_b32 v4, v75, v4, s47 bitop3:0xde\n\t"
        "v_or3_b32 v5, v5, v72, v73\n\t"
        "v_lshlrev_b32_sdwa v120, 1, v72 dst_sel:DWORD dst_unused:UNUSED_PAD src0_sel:DWORD src1_sel:WORD_0\n\t"
        "ds_read_u16 v120, v120 offset:46336\n\t"
        "v_lshlrev_b32_sdwa v121, 1, v73 dst_sel:DWORD dst_unused:UNUSED_PAD src0_sel:DWORD src1_sel:WORD_0\n\t"
        "ds_read_u16 v121, v121 offset:46336\n\t"
        "v_lshlrev_b32_sdwa v122, 1, v74 dst_sel:DWORD dst_unused:UNUSED_PAD src0_sel:DWORD src1_sel:WORD_0\n\t"
        "ds_read_u16 v122, v122 offset:46336\n\t"
        "v_mad_u32_u16 v88, v72, s38, v7 op_sel:[1,0,0,0]\n\t"
        "v_mad_u32_u16 v89, v73, s38, v7 op_sel:[1,0,0,0]\n\t"
        "v_mad_u32_u16 v90, v74, s38, v7 op_sel:[1,0,0,0]\n\t"
        "s_waitcnt vmcnt(0)\n\t"
        "v_xor_b32 v76, s44, v76\n\t"
        "v_xor_b32 v77, s45, v77\n\t"
        "v_xor_b32 v78, s46, v78\n\t"
        "v_bitop3_b32 v4, v79, v4, s47 bitop3:0xde\n\t"
        "v_or3_b32 v5, v5, v76, v77\n\t"
        "v_or3_b32 v5, v5, v74, v78\n\t"
        "v_lshlrev_b32_sdwa v124, 1, v76 dst_sel:DWORD dst_unused:UNUSED_PAD src0_sel:DWORD src1_sel:WORD_0\n\t"
        "ds_read_u16 v124, v124 offset:46336\n\t"
        "v_lshlrev_b32_sdwa v125, 1, v77 dst_sel:DWORD dst_unused:UNUSED_PAD src0_sel:DWORD src1_sel:WORD_0\n\t"
        "ds_read_u16 v125, v125 offset:46336\n\t"
        "v_lshlrev_b32_sdwa v126, 1, v78 dst_sel:DWORD dst_unused:UNUSED_PAD src0_sel:DWORD src1_sel:WORD_0\n\t"
        "ds_read_u16 v126, v126 offset:46336\n\t"
        "v_mad_u32_u16 v92, v76, s38, v7 op_sel:[1,0,0,0]\n\t"
        "v_mad_u32_u16 v93, v77, s38, v7 op_sel:[1,0,0,0]\n\t"
        "v_mad_u32_u16 v94, v78, s38, v7 op_sel:[1,0,0,0]\n\t"
        "v_and_b32 v6, 0xfc000000, v4\n\t"
        "v_or_b32 v5, v5, v6\n\t"
        "v_cmp_lt_u32 vcc, s35, v5\n\t"
        "s_cbranch_vccnz .L7_again\n"
        ".L7_got:\n\t"
        "v_and_b32 v6, s35, v4\n\t"
        "s_waitcnt lgkmcnt(2)\n\t"
        "ds_add_rtn_u32 v96, v80, v29 offset:192\n\t"
        "ds_add_rtn_u32 v97, v81, v29 offset:192\n\t"
        "ds_add_rtn_u32 v98, v82, v29 offset:192\n\t"
        "ds_add_rtn_u32 v100, v84, v29 offset:192\n\t"
        "ds_add_rtn_u32 v101, v85, v29 offset:192\n\t"
        "ds_add_rtn_u32 v102, v86, v29 offset:192\n\t"
        "ds_add_rtn_u32 v104, v88, v29 offset:192\n\t"
        "ds_add_rtn_u32 v105, v89, v29 offset:192\n\t"
        "ds_add_rtn_u32 v106, v90, v29 offset:192\n\t"
        "ds_add_rtn_u32 v108, v92, v29 offset:192\n\t"
        "ds_add_rtn_u32 v109, v93, v29 offset:192\n\t"
        "ds_add_rtn_u32 v110, v94, v29 offset:192\n\t"
        "v_cmp_ne_u32 vcc, 0, v6\n\t"
        "s_cbranch_vccnz .L7_x\n"
        ".L7_y:\n\t"
        "s_mov_b64 exec, 1\n\t"
        "ds_add_u32 v2, v3 offset:32\n\t"
        "s_mov_b64 exec, -1\n\t"
        "v_perm_b32 v112, v112, v64, s49\n\t"
        "v_perm_b32 v113, v113, v65, s49\n\t"
        "v_perm_b32 v114, v114, v66, s49\n\t"
        "v_perm_b32 v116, v116, v68, s49\n\t"
        "v_perm_b32 v117, v117, v69, s49\n\t"
        "v_perm_b32 v118, v118, v70, s49\n\t"
        "v_perm_b32 v120, v120, v72, s49\n\t"
        "v_perm_b32 v121, v121, v73, s49\n\t"
        "v_perm_b32 v122, v122, v74, s49\n\t"
        "v_perm_b32 v124, v124, v76, s49\n\t"
        "s_cmp_lt_u32 s69, 1\n\t"
        "s_cbranch_scc0 .L7_nv\n\t"
        "s_mov_b32 s79, 0\n\t"
        "v_xor_b32 v32, s47, v67\n\t"
        "v_xor_b32 v33, s47, v71\n\t"
        "v_xor_b32 v34, s47, v75\n\t"
        "v_xor_b32 v35, s47, v79\n\t"
        "v_lshlrev_b32 v32, 6, v32\n\t"
        "v_lshlrev_b32 v33, 6, v33\n\t"
        "v_lshlrev_b32 v34, 6, v34\n\t"
        "v_lshlrev_b32 v35, 6, v35\n\t"
        "s_mov_b32 exec_lo, s37\n\t"
        "s_mov_b32 exec_hi, s37\n\t"
        "v_cmp_gt_i32 vcc, 0, v32\n\t"
        "s_bcnt1_i32_b64 s78, vcc\n\t"
        "s_add_u32 s79, s79, s78\n\t"
        "v_cmp_gt_i32 vcc, 0, v33\n\t"
        "s_bcnt1_i32_b64 s78, vcc\n\t"
        "s_add_u32 s79, s79, s78\n\t"
        "v_cmp_gt_i32 vcc, 0, v34\n\t"
        "s_bcnt1_i32_b64 s78, vcc\n\t"
        "s_add_u32 s79, s79, s78\n\t"
        "v_cmp_gt_i32 vcc, 0, v35\n\t"
        "s_bcnt1_i32_b64 s78, vcc\n\t"
        "s_add_u32 s79, s79, s78\n\t"
        "s_lshl_b32 s79, s79, 16\n\t"
        "s_or_b32 s79, s79, 1\n\t"
        "v_mov_b32 v32, s79\n\t"
        "s_mov_b64 exec, 1\n\t"
        "ds_add_u32 v2, v32 offset:116\n\t"
        "s_mov_b64 exec, -1\n"
        ".L7_nv:\n\t"
        "v_lshlrev_b32 v4, 7, v4\n\t"
        "v_cmp_gt_i32 vcc, 0, v4\n\t"
        "s_cmp_lg_u64 vcc, 0\n\t"
        "s_cselect_b32 s78, 0x100000, 0\n\t"
        "v_or_b32 v30, s78, v30\n\t"
        "s_waitcnt lgkmcnt(1)\n\t"
        "v_perm_b32 v125, v125, v77, s49\n\t"
        "v_perm_b32 v126, v126, v78, s49\n\t"
        "v_bfe_u32 v96, v96, s33, 8\n\t"
        "v_bfe_u32 v97, v97, s33, 8\n\t"
        "v_bfe_u32 v98, v98, s33, 8\n\t"
        "v_bfe_u32 v100, v100, s33, 8\n\t"
        "v_bfe_u32 v101, v101, s33, 8\n\t"
        "v_bfe_u32 v102, v102, s33, 8\n\t"
        "v_bfe_u32 v104, v104, s33, 8\n\t"
        "v_bfe_u32 v105, v105, s33, 8\n\t"
        "v_bfe_u32 v106, v106, s33, 8\n\t"
        "v_bfe_u32 v108, v108, s33, 8\n\t"
        "v_bfe_u32 v109, v109, s33, 8\n\t"
        "v_bfe_u32 v110, v110, s33, 8\n"
        ".L7_w82:\n\t"
        "ds_read_b32 v0, v2 offset:32\n\t"
        "s_waitcnt lgkmcnt(0)\n\t"
        "v_cmp_gt_u16 vcc, 4, v0\n\t"
        "s_cbranch_vccz .L7_d83\n\t"
        "s_sleep 1\n\t"
        "s_sub_i32 s83, s83, 1\n\t"
        "s_cmp_gt_i32 s83, 0\n\t"
        "s_cbranch_scc1 .L7_w82\n"
        ".L7_d83:\n\t"
        "ds_read_b32 v32, v80 offset:192\n\t"
        "ds_read_b32 v33, v81 offset:192\n\t"
        "ds_read_b32 v34, v82 offset:192\n\t"
        "ds_read_b32 v35, v84 offset:192\n\t"
        "ds_read_b32 v36, v85 offset:192\n\t"
        "ds_read_b32 v37, v86 offset:192\n\t"
        "ds_read_b32 v38, v88 offset:192\n\t"
        "ds_read_b32 v39, v89 offset:192\n\t"
        "ds_read_b32 v40, v90 offset:192\n\t"
        "ds_read_b32 v41, v92 offset:192\n\t"
        "ds_read_b32 v42, v93 offset:192\n\t"
        "ds_read_b32 v43, v94 offset:192\n\t"
        "ds_read_b32 v6, v31 offset:192\n\t"
        "s_waitcnt lgkmcnt(10)\n\t"
        "v_and_b32 v32, s34, v32\n\t"
        "v_sad_u8 v96, v32, 0, v96\n\t"
        "v_and_b32 v33, s34, v33\n\t"
        "v_sad_u8 v97, v33, 0, v97\n\t"
        "v_and_b32 v34, s34, v34\n\t"
        "v_sad_u8 v98, v34, 0, v98\n\t"
        "s_waitcnt lgkmcnt(7)\n\t"
        "v_and_b32 v35, s34, v35\n\t"
        "v_sad_u8 v100, v35, 0, v100\n\t"
        "v_and_b32 v36, s34, v36\n\t"
        "v_sad_u8 v101, v36, 0, v101\n\t"
        "v_and_b32 v37, s34, v37\n\t"
        "v_sad_u8 v102, v37, 0, v102\n\t"
        "s_waitcnt lgkmcnt(4)\n\t"
        "v_and_b32 v38, s34, v38\n\t"
        "v_sad_u8 v104, v38, 0, v104\n\t"
        "v_and_b32 v39, s34, v39\n\t"
        "v_sad_u8 v105, v39, 0, v105\n\t"
        "v_and_b32 v40, s34, v40\n\t"
        "v_sad_u8 v106, v40, 0, v106\n\t"
        "s_waitcnt lgkmcnt(1)\n\t"
        "v_and_b32 v41, s34, v41\n\t"
        "v_sad_u8 v108, v41, 0, v108\n\t"
        "v_and_b32 v42, s34, v42\n\t"
        "v_sad_u8 v109, v42, 0, v109\n\t"
        "v_and_b32 v43, s34, v43\n\t"
        "v_sad_u8 v110, v43, 0, v110\n\t"
        "v_min_u32 v96, 47, v96\n\t"
        "v_min_u32 v97, 47, v97\n\t"
        "v_min_u32 v98, 47, v98\n\t"
        "v_min_u32 v100, 47, v100\n\t"
        "v_min_u32 v101, 47, v101\n\t"
        "v_min_u32 v102, 47, v102\n\t"
        "v_min_u32 v104, 47, v104\n\t"
        "v_min_u32 v105, 47, v105\n\t"
        "v_min_u32 v106, 47, v106\n\t"
        "v_min_u32 v108, 47, v108\n\t"
        "v_min_u32 v109, 47, v109\n\t"
        "v_min_u32 v110, 47, v110\n\t"
        "v_lshl_add_u32 v96, v96, 2, v80\n\t"
        "ds_write_b32 v96, v112\n\t"
        "v_lshl_add_u32 v97, v97, 2, v81\n\t"
        "ds_write_b32 v97, v113\n\t"
        "v_lshl_add_u32 v98, v98, 2, v82\n\t"
        "ds_write_b32 v98, v114\n\t"
        "v_lshl_add_u32 v100, v100, 2, v84\n\t"
        "ds_write_b32 v100, v116\n\t"
        "v_lshl_add_u32 v101, v101, 2, v85\n\t"
        "ds_write_b32 v101, v117\n\t"
        "v_lshl_add_u32 v102, v102, 2, v86\n\t"
        "ds_write_b32 v102, v118\n\t"
        "v_lshl_add_u32 v104, v104, 2, v88\n\t"
        "ds_write_b32 v104, v120\n\t"
        "v_lshl_add_u32 v105, v105, 2, v89\n\t"
        "ds_write_b32 v105, v121\n\t"
        "v_lshl_add_u32 v106, v106, 2, v90\n\t"
        "ds_write_b32 v106, v122\n\t"
        "v_lshl_add_u32 v108, v108, 2, v92\n\t"
        "ds_write_b32 v108, v124\n\t"
        "v_lshl_add_u32 v109, v109, 2, v93\n\t"
        "ds_write_b32 v109, v125\n\t"
        "v_lshl_add_u32 v110, v110, 2, v94\n\t"
        "ds_write_b32 v110, v126\n\t"
        "s_cmp_lg_u32 s36, 0\n\t"
        "s_cbranch_scc1 .L7_z\n"
        ".L7_zr:\n\t"
        "s_mov_b64 exec, 1\n\t"
        "ds_add_u32 v2, v30 offset:36\n\t"
        "s_mov_b64 exec, -1\n\t"
        "s_waitcnt lgkmcnt(13)\n\t"
        "v_sad_u8 v6, v6, 0, -1\n\t"
        "v_mov_b64 v[32:33], 0\n\t"
        "v_mov_b64 v[34:35], 0\n\t"
        "v_mov_b64 v[36:37], 0\n\t"
        "v_med3_i32 v6, v6, 0, 47\n\t"
        "v_lshrrev_b32 v6, 1, v6\n\t"
        "v_mov_b64 v[38:39], 0\n\t"
        "v_and_b32 v63, 0xff, v24\n\t"
        "v_mad_u32_u24 v63, v63, s38, v7\n\t"
        "v_max_u32_dpp v6, v6, v6 quad_perm:[1,0,3,2] row_mask:0xf bank_mask:0xf\n\t"
        "s_nop 1\n\t"
        "v_max_u32_dpp v6, v6, v6 quad_perm:[2,3,0,1] row_mask:0xf bank_mask:0xf\n\t"
        "s_nop 0\n\t"
        "v_readlane_b32 s24, v6, 0\n\t"
        "v_readlane_b32 s25, v6, 4\n\t"
        "v_readlane_b32 s26, v6, 8\n\t"
        "v_readlane_b32 s27, v6, 12\n\t"
        "v_readlane_b32 s28, v6, 16\n\t"
        "s_max_u32 s28, s28, 5\n\t"
        "s_mov_b32 s56, s24\n\t"
        "s_mov_b32 s57, s24\n\t"
        "s_mov_b32 s78, 0xf0000\n"
        ".L7_w84:\n\t"
        "ds_read_b32 v4, v2 offset:36\n\t"
        "ds_read_b32 v12, v31 offset:188\n\t"
        "s_waitcnt lgkmcnt(1)\n\t"
        "v_cmp_gt_u16 vcc, 4, v4\n\t"
        "s_cbranch_vccz .L7_d85\n\t"
        "s_waitcnt lgkmcnt(0)\n\t"
        "s_sleep 1\n\t"
        "s_sub_i32 s83, s83, 1\n\t"
        "s_cmp_gt_i32 s83, 0\n\t"
        "s_cbranch_scc1 .L7_w84\n"
        ".L7_d85:\n\t"
        "ds_read2_b32 v[40:41], v63 offset0:0 offset1:1\n\t"
        "s_sub_u32 s56, s56, 1\n\t"
        "s_cbranch_scc1 .L7_i86\n"
        ".L7_j87:\n\t"
        "ds_read2_b32 v[42:43], v63 offset0:2 offset1:3\n\t"
        "s_sub_u32 s56, s56, 1\n\t"
        "s_cbranch_scc1 .L7_i88\n"
        ".L7_j89:\n\t"
        "ds_read2_b32 v[44:45], v63 offset0:4 offset1:5\n\t"
        "s_sub_u32 s56, s56, 1\n\t"
        "s_cbranch_scc1 .L7_i90\n"
        ".L7_j91:\n\t"
        "ds_read2_b32 v[46:47], v63 offset0:6 offset1:7\n\t"
        "s_sub_u32 s56, s56, 1\n\t"
        "s_cbranch_scc1 .L7_i92\n"
        ".L7_j93:\n\t"
        "ds_read2_b32 v[48:49], v63 offset0:8 offset1:9\n\t"
        "s_sub_u32 s56, s56, 1\n\t"
        "s_cbranch_scc1 .L7_i94\n"
        ".L7_j95:\n\t"
        "ds_read2_b32 v[50:51], v63 offset0:10 offset1:11\n\t"
        "s_sub_u32 s56, s56, 1\n\t"
        "s_cbranch_scc1 .L7_i96\n"
        ".L7_j97:\n\t"
        "ds_read2_b32 v[52:53], v63 offset0:12 offset1:13\n\t"
        "s_sub_u32 s56, s56, 1\n\t"
        "s_cbranch_scc1 .L7_i98\n"
        ".L7_j99:\n\t"
        "ds_read2_b32 v[54:55], v63 offset0:14 offset1:15\n\t"
        "s_sub_u32 s56, s56, 1\n\t"
        "s_cbranch_scc1 .L7_i100\n"
        ".L7_j101:\n\t"
        "s_waitcnt lgkmcnt(8)\n\t"
        "v_and_or_b32 v12, v4, s78, v12\n\t"
        "v_cmp_ne_u32 vcc, 0xffff, v12\n\t"
        "s_cbranch_vccnz .L7_exit4\n\t"
        "s_waitcnt lgkmcnt(7)\n\t"
        "v_mad_u32_u16 v10, v40, s48, v62\n\t"
        "v_mad_u32_u16 v11, v41, s48, v62\n\t"
        "buffer_load_dwordx4 v[64:67], v10, s[16:19], 0 offen\n\t"
        "buffer_load_dwordx4 v[68:71], v11, s[16:19], 0 offen\n\t"
        "s_waitcnt lgkmcnt(6)\n\t"
        "v_mad_u32_u16 v10, v42, s48, v62\n\t"
        "v_mad_u32_u16 v11, v43, s48, v62\n\t"
        "buffer_load_dwordx4 v[72:75], v10, s[16:19], 0 offen\n\t"
        "buffer_load_dwordx4 v[76:79], v11, s[16:19], 0 offen\n\t"
        "s_waitcnt lgkmcnt(5)\n\t"
        "v_mad_u32_u16 v10, v44, s48, v62\n\t"
        "v_mad_u32_u16 v11, v45, s48, v62\n\t"
        "buffer_load_dwordx4 v[80:83], v10, s[16:19], 0 offen\n\t"
        "buffer_load_dwordx4 v[84:87], v11, s[16:19], 0 offen\n\t"
        "s_waitcnt lgkmcnt(4)\n\t"
        "v_mad_u32_u16 v10, v46, s48, v62\n\t"
        "v_mad_u32_u16 v11, v47, s48, v62\n\t"
        "buffer_load_dwordx4 v[88:91], v10, s[16:19], 0 offen\n\t"
        "buffer_load_dwordx4 v[92:95], v11, s[16:19], 0 offen\n\t"
        "s_waitcnt lgkmcnt(3)\n\t"
        "v_mad_u32_u16 v10, v48, s48, v62\n\t"
        "v_mad_u32_u16 v11, v49, s48, v62\n\t"
        "buffer_load_dwordx4 v[96:99], v10, s[16:19], 0 offen\n\t"
        "buffer_load_dwordx4 v[100:103], v11, s[16:19], 0 offen\n\t"
        "s_waitcnt lgkmcnt(2)\n\t"
        "v_mad_u32_u16 v10, v50, s48, v62\n\t"
        "v_mad_u32_u16 v11, v51, s48, v62\n\t"
        "buffer_load_dwordx4 v[104:107], v10, s[16:19], 0 offen\n\t"
        "buffer_load_dwordx4 v[108:111], v11, s[16:19], 0 offen\n\t"
        "s_min_u32 s59, s56, s57\n\t"
        "s_add_u32 s58, s59, 1\n\t"
        "s_setprio 2\n"
        ".L7_loop:\n\t"
        "v_add_u32 v63, 64, v63\n\t"
        "v_perm_b32 v12, v41, v40, v8\n\t"
        "v_perm_b32 v13, v41, v40, v9\n\t"
        "s_waitcnt vmcnt(10) lgkmcnt(1)\n\t"
        "v_mad_u32_u16 v10, v52, s48, v62\n\t"
        "v_mad_u32_u16 v11, v53, s48, v62\n\t"
        "buffer_load_dwordx4 v[112:115], v10, s[16:19], 0 offen\n\t"
        "buffer_load_dwordx4 v[116:119], v11, s[16:19], 0 offen\n\t"
        "v_mfma_f32_4x4x4_16b_bf16 v[32:35], v[12:13], v[64:65], v[32:35] cbsz:1 abid:0\n\t"
        "v_mfma_f32_4x4x4_16b_bf16 v[36:39], v[12:13], v[66:67], v[36:39] cbsz:1 abid:0\n\t"
        "ds_read2_b32 v[40:41], v63 offset0:0 offset1:1\n\t"
        "v_mfma_f32_4x4x4_16b_bf16 v[32:35], v[12:13], v[68:69], v[32:35] cbsz:1 abid:1\n\t"
        "v_mfma_f32_4x4x4_16b_bf16 v[36:39], v[12:13], v[70:71], v[36:39] cbsz:1 abid:1\n\t"
        "s_sub_u32 s59, s59, 1\n\t"
        "s_cbranch_scc1 .L7_i102\n"
        ".L7_j103:\n\t"
        "v_perm_b32 v12, v43, v42, v8\n\t"
        "v_perm_b32 v13, v43, v42, v9\n\t"
        "s_waitcnt vmcnt(10) lgkmcnt(1)\n\t"
        "v_mad_u32_u16 v10, v54, s48, v62\n\t"
        "v_mad_u32_u16 v11, v55, s48, v62\n\t"
        "buffer_load_dwordx4 v[120:123], v10, s[16:19], 0 offen\n\t"
        "buffer_load_dwordx4 v[124:127], v11, s[16:19], 0 offen\n\t"
        "v_mfma_f32_4x4x4_16b_bf16 v[32:35], v[12:13], v[72:73], v[32:35] cbsz:1 abid:0\n\t"
        "v_mfma_f32_4x4x4_16b_bf16 v[36:39], v[12:13], v[74:75], v[36:39] cbsz:1 abid:0\n\t"
        "ds_read2_b32 v[42:43], v63 offset0:2 offset1:3\n\t"
        "v_mfma_f32_4x4x4_16b_bf16 v[32:35], v[12:13], v[76:77], v[32:35] cbsz:1 abid:1\n\t"
        "v_mfma_f32_4x4x4_16b_bf16 v[36:39], v[12:13], v[78:79], v[36:39] cbsz:1 abid:1\n\t"
        "s_sub_u32 s59, s59, 1\n\t"
        "s_cbranch_scc1 .L7_i104\n"
        ".L7_j105:\n\t"
        "v_perm_b32 v12, v45, v44, v8\n\t"
        "v_perm_b32 v13, v45, v44, v9\n\t"
        "s_waitcnt vmcnt(10) lgkmcnt(1)\n\t"
        "v_mad_u32_u16 v10, v40, s48, v62\n\t"
        "v_mad_u32_u16 v11, v41, s48, v62\n\t"
        "buffer_load_dwordx4 v[64:67], v10, s[16:19], 0 offen\n\t"
        "buffer_load_dwordx4 v[68:71], v11, s[16:19], 0 offen\n\t"
        "v_mfma_f32_4x4x4_16b_bf16 v[32:35], v[12:13], v[80:81], v[32:35] cbsz:1 abid:0\n\t"
        "v_mfma_f32_4x4x4_16b_bf16 v[36:39], v[12:13], v[82:83], v[36:39] cbsz:1 abid:0\n\t"
        "ds_read2_b32 v[44:45], v63 offset0:4 offset1:5\n\t"
        "v_mfma_f32_4x4x4_16b_bf16 v[32:35], v[12:13], v[84:85], v[32:35] cbsz:1 abid:1\n\t"
        "v_mfma_f32_4x4x4_16b_bf16 v[36:39], v[12:13], v[86:87], v[36:39] cbsz:1 abid:1\n\t"
        "s_sub_u32 s59, s59, 1\n\t"
        "s_cbranch_scc1 .L7_i106\n"
        ".L7_j107:\n\t"
        "v_perm_b32 v12, v47, v46, v8\n\t"
        "v_perm_b32 v13, v47, v46, v9\n\t"
        "s_waitcnt vmcnt(10) lgkmcnt(1)\n\t"
        "v_mad_u32_u16 v10, v42, s48, v62\n\t"
        "v_mad_u32_u16 v11, v43, s48, v62\n\t"
        "buffer_load_dwordx4 v[72:75], v10, s[16:19], 0 offen\n\t"
        "buffer_load_dwordx4 v[76:79], v11, s[16:19], 0 offen\n\t"
        "v_mfma_f32_4x4x4_16b_bf16 v[32:35], v[12:13], v[88:89], v[32:35] cbsz:1 abid:0\n\t"
        "v_mfma_f32_4x4x4_16b_bf16 v[36:39], v[12:13], v[90:91], v[36:39] cbsz:1 abid:0\n\t"
        "ds_read2_b32 v[46:47], v63 offset0:6 offset1:7\n\t"
        "v_mfma_f32_4x4x4_16b_bf16 v[32:35], v[12:13], v[92:93], v[32:35] cbsz:1 abid:1\n\t"
        "v_mfma_f32_4x4x4_16b_bf16 v[36:39], v[12:13], v[94:95], v[36:39] cbsz:1 abid:1\n\t"
        "s_sub_u32 s59, s59, 1\n\t"
        "s_cbranch_scc1 .L7_i108\n"
        ".L7_j109:\n\t"
        "v_perm_b32 v12, v49, v48, v8\n\t"
        "v_perm_b32 v13, v49, v48, v9\n\t"
        "s_waitcnt vmcnt(10) lgkmcnt(1)\n\t"
        "v_mad_u32_u16 v10, v44, s48, v62\n\t"
        "v_mad_u32_u16 v11, v45, s48, v62\n\t"
        "buffer_load_dwordx4 v[80:83], v10, s[16:19], 0 offen\n\t"
        "buffer_load_dwordx4 v[84:87], v11, s[16:19], 0 offen\n\t"
        "v_mfma_f32_4x4x4_16b_bf16 v[32:35], v[12:13], v[96:97], v[32:35] cbsz:1 abid:0\n\t"
        "v_mfma_f32_4x4x4_16b_bf16 v[36:39], v[12:13], v[98:99], v[36:39] cbsz:1 abid:0\n\t"
        "ds_read2_b32 v[48:49], v63 offset0:8 offset1:9\n\t"
        "v_mfma_f32_4x4x4_16b_bf16 v[32:35], v[12:13], v[100:101], v[32:35] cbsz:1 abid:1\n\t"
        "v_mfma_f32_4x4x4_16b_bf16 v[36:39], v[12:13], v[102:103], v[36:39] cbsz:1 abid:1\n\t"
        "s_sub_u32 s59, s59, 1\n\t"
        "s_cbranch_scc1 .L7_i110\n"
        ".L7_j111:\n\t"
        "v_perm_b32 v12, v51, v50, v8\n\t"
        "v_perm_b32 v13, v51, v50, v9\n\t"
        "s_waitcnt vmcnt(10) lgkmcnt(1)\n\t"
        "v_mad_u32_u16 v10, v46, s48, v62\n\t"
        "v_mad_u32_u16 v11, v47, s48, v62\n\t"
        "buffer_load_dwordx4 v[88:91], v10, s[16:19], 0 offen\n\t"
        "buffer_load_dwordx4 v[92:95], v11, s[16:19], 0 offen\n\t"
        "v_mfma_f32_4x4x4_16b_bf16 v[32:35], v[12:13], v[104:105], v[32:35] cbsz:1 abid:0\n\t"
        "v_mfma_f32_4x4x4_16b_bf16 v[36:39], v[12:13], v[106:107], v[36:39] cbsz:1 abid:0\n\t"
        "ds_read2_b32 v[50:51], v63 offset0:10 offset1:11\n\t"
        "v_mfma_f32_4x4x4_16b_bf16 v[32:35], v[12:13], v[108:109], v[32:35] cbsz:1 abid:1\n\t"
        "v_mfma_f32_4x4x4_16b_bf16 v[36:39], v[12:13], v[110:111], v[36:39] cbsz:1 abid:1\n\t"
        "s_sub_u32 s59, s59, 1\n\t"
        "s_cbranch_scc1 .L7_i112\n"
        ".L7_j113:\n\t"
        "v_perm_b32 v12, v53, v52, v8\n\t"
        "v_perm_b32 v13, v53, v52, v9\n\t"
        "s_waitcnt vmcnt(10) lgkmcnt(1)\n\t"
        "v_mad_u32_u16 v10, v48, s48, v62\n\t"
        "v_mad_u32_u16 v11, v49, s48, v62\n\t"
        "buffer_load_dwordx4 v[96:99], v10, s[16:19], 0 offen\n\t"
        "buffer_load_dwordx4 v[100:103], v11, s[16:19], 0 offen\n\t"
        "v_mfma_f32_4x4x4_16b_bf16 v[32:35], v[12:13], v[112:113], v[32:35] cbsz:1 abid:0\n\t"
        "v_mfma_f32_4x4x4_16b_bf16 v[36:39], v[12:13], v[114:115], v[36:39] cbsz:1 abid:0\n\t"
        "ds_read2_b32 v[52:53], v63 offset0:12 offset1:13\n\t"
        "v_mfma_f32_4x4x4_16b_bf16 v[32:35], v[12:13], v[116:117], v[32:35] cbsz:1 abid:1\n\t"
        "v_mfma_f32_4x4x4_16b_bf16 v[36:39], v[12:13], v[118:119], v[36:39] cbsz:1 abid:1\n\t"
        "s_sub_u32 s59, s59, 1\n\t"
        "s_cbranch_scc1 .L7_i114\n"
        ".L7_j115:\n\t"
        "v_perm_b32 v12, v55, v54, v8\n\t"
        "v_perm_b32 v13, v55, v54, v9\n\t"
        "s_waitcnt vmcnt(10) lgkmcnt(1)\n\t"
        "v_mad_u32_u16 v10, v50, s48, v62\n\t"
        "v_mad_u32_u16 v11, v51, s48, v62\n\t"
        "buffer_load_dwordx4 v[104:107], v10, s[16:19], 0 offen\n\t"
        "buffer_load_dwordx4 v[108:111], v11, s[16:19], 0 offen\n\t"
        "v_mfma_f32_4x4x4_16b_bf16 v[32:35], v[12:13], v[120:121], v[32:35] cbsz:1 abid:0\n\t"
        "v_mfma_f32_4x4x4_16b_bf16 v[36:39], v[12:13], v[122:123], v[36:39] cbsz:1 abid:0\n\t"
        "ds_read2_b32 v[54:55], v63 offset0:14 offset1:15\n\t"
        "v_mfma_f32_4x4x4_16b_bf16 v[32:35], v[12:13], v[124:125], v[32:35] cbsz:1 abid:1\n\t"
        "v_mfma_f32_4x4x4_16b_bf16 v[36:39], v[12:13], v[126:127], v[36:39] cbsz:1 abid:1\n\t"
        "s_sub_u32 s59, s59, 1\n\t"
        "s_cbranch_scc0 .L7_loop\n\t"
        "s_branch .L7_i116\n"
        ".L7_i86:\n\t"
        "s_mov_b32 s60, 8\n\t"
        "s_swappc_b64 s[54:55], s[50:51]\n\t"
        "s_branch .L7_j87\n"
        ".L7_i88:\n\t"
        "s_mov_b32 s60, 16\n\t"
        "s_swappc_b64 s[54:55], s[50:51]\n\t"
        "s_branch .L7_j89\n"
        ".L7_i90:\n\t"
        "s_mov_b32 s60, 24\n\t"
        "s_swappc_b64 s[54:55], s[50:51]\n\t"
        "s_branch .L7_j91\n"
        ".L7_i92:\n\t"
        "s_mov_b32 s60, 32\n\t"
        "s_swappc_b64 s[54:55], s[50:51]\n\t"
        "s_branch .L7_j93\n"
        ".L7_i94:\n\t"
        "s_mov_b32 s60, 40\n\t"
        "s_swappc_b64 s[54:55], s[50:51]\n\t"
        "s_branch .L7_j95\n"
        ".L7_i96:\n\t"
        "s_mov_b32 s60, 48\n\t"
        "s_swappc_b64 s[54:55], s[50:51]\n\t"
        "s_branch .L7_j97\n"
        ".L7_i98:\n\t"
        "s_mov_b32 s60, 56\n\t"
        "s_swappc_b64 s[54:55], s[50:51]\n\t"
        "s_branch .L7_j99\n"
        ".L7_i100:\n\t"
        "s_mov_b32 s60, 64\n\t"
        "s_swappc_b64 s[54:55], s[50:51]\n\t"
        "s_branch .L7_j101\n"
        ".L7_i102:\n\t"
        "s_mov_b32 s60, 8\n\t"
        "s_swappc_b64 s[62:63], s[42:43]\n\t"
        "s_cbranch_vccnz .L7_dt1\n\t"
        "s_branch .L7_j103\n"
        ".L7_i104:\n\t"
        "s_mov_b32 s60, 16\n\t"
        "s_swappc_b64 s[62:63], s[42:43]\n\t"
        "s_cbranch_vccnz .L7_dt2\n\t"
        "s_branch .L7_j105\n"
        ".L7_i106:\n\t"
        "s_mov_b32 s60, 24\n\t"
        "s_swappc_b64 s[62:63], s[42:43]\n\t"
        "s_cbranch_vccnz .L7_dt3\n\t"
        "s_branch .L7_j107\n"
        ".L7_i108:\n\t"
        "s_mov_b32 s60, 32\n\t"
        "s_swappc_b64 s[62:63], s[42:43]\n\t"
        "s_cbranch_vccnz .L7_dt4\n\t"
        "s_branch .L7_j109\n"
        ".L7_i110:\n\t"
        "s_mov_b32 s60, 40\n\t"
        "s_swappc_b64 s[62:63], s[42:43]\n\t"
        "s_cbranch_vccnz .L7_dt5\n\t"
        "s_branch .L7_j111\n"
        ".L7_i112:\n\t"
        "s_mov_b32 s60, 48\n\t"
        "s_swappc_b64 s[62:63], s[42:43]\n\t"
        "s_cbranch_vccnz .L7_dt6\n\t"
        "s_branch .L7_j113\n"
        ".L7_i114:\n\t"
        "s_mov_b32 s60, 56\n\t"
        "s_swappc_b64 s[62:63], s[42:43]\n\t"
        "s_cbranch_vccnz .L7_dt7\n\t"
        "s_branch .L7_j115\n"
        ".L7_i116:\n\t"
        "s_mov_b32 s60, 64\n\t"
        "s_swappc_b64 s[62:63], s[42:43]\n\t"
        "s_cbranch_vccnz .L7_dt0\n\t"
        "s_branch .L7_loop\n"
        ".L7_ev:\n\t"
        "s_sub_u32 s56, s56, s58\n\t"
        "s_cbranch_scc0 .L7_e1\n\t"
        "s_swappc_b64 s[54:55], s[50:51]\n"
        ".L7_e1:\n\t"
        "s_sub_u32 s57, s57, s58\n\t"
        "s_cbranch_scc0 .L7_e2\n\t"
        "s_swappc_b64 s[54:55], s[52:53]\n"
        ".L7_e2:\n\t"
        "s_min_u32 s59, s56, s57\n\t"
        "s_add_u32 s58, s59, 1\n\t"
        "s_setpc_b64 s[62:63]\n"
        ".L7_dt0:\n\t"
        "v_perm_b32 v12, v41, v40, v8\n\t"
        "v_perm_b32 v13, v41, v40, v9\n\t"
        "s_waitcnt vmcnt(6)\n\t"
        "s_cmp_lt_u32 s57, 3\n\t"
        "s_cbranch_scc0 .L7_du0\n\t"
        "s_waitcnt vmcnt(2)\n\t"
        "s_cmp_eq_u32 s57, 0\n\t"
        "s_cbranch_scc0 .L7_du0\n\t"
        "s_waitcnt vmcnt(0)\n"
        ".L7_du0:\n\t"
        "v_mfma_f32_4x4x4_16b_bf16 v[32:35], v[12:13], v[64:65], v[32:35] cbsz:1 abid:0\n\t"
        "v_mfma_f32_4x4x4_16b_bf16 v[36:39], v[12:13], v[66:67], v[36:39] cbsz:1 abid:0\n\t"
        "s_sub_u32 s57, s57, 1\n\t"
        "v_mfma_f32_4x4x4_16b_bf16 v[32:35], v[12:13], v[68:69], v[32:35] cbsz:1 abid:1\n\t"
        "v_mfma_f32_4x4x4_16b_bf16 v[36:39], v[12:13], v[70:71], v[36:39] cbsz:1 abid:1\n\t"
        "s_cbranch_scc1 .L7_last\n"
        ".L7_dt1:\n\t"
        "v_perm_b32 v12, v43, v42, v8\n\t"
        "v_perm_b32 v13, v43, v42, v9\n\t"
        "s_waitcnt vmcnt(6)\n\t"
        "s_cmp_lt_u32 s57, 3\n\t"
        "s_cbranch_scc0 .L7_du1\n\t"
        "s_waitcnt vmcnt(2)\n\t"
        "s_cmp_eq_u32 s57, 0\n\t"
        "s_cbranch_scc0 .L7_du1\n\t"
        "s_waitcnt vmcnt(0)\n"
        ".L7_du1:\n\t"
        "v_mfma_f32_4x4x4_16b_bf16 v[32:35], v[12:13], v[72:73], v[32:35] cbsz:1 abid:0\n\t"
        "v_mfma_f32_4x4x4_16b_bf16 v[36:39], v[12:13], v[74:75], v[36:39] cbsz:1 abid:0\n\t"
        "s_sub_u32 s57, s57, 1\n\t"
        "v_mfma_f32_4x4x4_16b_bf16 v[32:35], v[12:13], v[76:77], v[32:35] cbsz:1 abid:1\n\t"
        "v_mfma_f32_4x4x4_16b_bf16 v[36:39], v[12:13], v[78:79], v[36:39] cbsz:1 abid:1\n\t"
        "s_cbranch_scc1 .L7_last\n"
        ".L7_dt2:\n\t"
        "v_perm_b32 v12, v45, v44, v8\n\t"
        "v_perm_b32 v13, v45, v44, v9\n\t"
        "s_waitcnt vmcnt(6)\n\t"
        "s_cmp_lt_u32 s57, 3\n\t"
        "s_cbranch_scc0 .L7_du2\n\t"
        "s_waitcnt vmcnt(2)\n\t"
        "s_cmp_eq_u32 s57, 0\n\t"
        "s_cbranch_scc0 .L7_du2\n\t"
        "s_waitcnt vmcnt(0)\n"
        ".L7_du2:\n\t"
        "v_mfma_f32_4x4x4_16b_bf16 v[32:35], v[12:13], v[80:81], v[32:35] cbsz:1 abid:0\n\t"
        "v_mfma_f32_4x4x4_16b_bf16 v[36:39], v[12:13], v[82:83], v[36:39] cbsz:1 abid:0\n\t"
        "s_sub_u32 s57, s57, 1\n\t"
        "v_mfma_f32_4x4x4_16b_bf16 v[32:35], v[12:13], v[84:85], v[32:35] cbsz:1 abid:1\n\t"
        "v_mfma_f32_4x4x4_16b_bf16 v[36:39], v[12:13], v[86:87], v[36:39] cbsz:1 abid:1\n\t"
        "s_cbranch_scc1 .L7_last\n"
        ".L7_dt3:\n\t"
        "v_perm_b32 v12, v47, v46, v8\n\t"
        "v_perm_b32 v13, v47, v46, v9\n\t"
        "s_waitcnt vmcnt(6)\n\t"
        "s_cmp_lt_u32 s57, 3\n\t"
        "s_cbranch_scc0 .L7_du3\n\t"
        "s_waitcnt vmcnt(2)\n\t"
        "s_cmp_eq_u32 s57, 0\n\t"
        "s_cbranch_scc0 .L7_du3\n\t"
        "s_waitcnt vmcnt(0)\n"
        ".L7_du3:\n\t"
        "v_mfma_f32_4x4x4_16b_bf16 v[32:35], v[12:13], v[88:89], v[32:35] cbsz:1 abid:0\n\t"
        "v_mfma_f32_4x4x4_16b_bf16 v[36:39], v[12:13], v[90:91], v[36:39] cbsz:1 abid:0\n\t"
        "s_sub_u32 s57, s57, 1\n\t"
        "v_mfma_f32_4x4x4_16b_bf16 v[32:35], v[12:13], v[92:93], v[32:35] cbsz:1 abid:1\n\t"
        "v_mfma_f32_4x4x4_16b_bf16 v[36:39], v[12:13], v[94:95], v[36:39] cbsz:1 abid:1\n\t"
        "s_cbranch_scc1 .L7_last\n"
        ".L7_dt4:\n\t"
        "v_perm_b32 v12, v49, v48, v8\n\t"
        "v_perm_b32 v13, v49, v48, v9\n\t"
        "s_waitcnt vmcnt(6)\n\t"
        "s_cmp_lt_u32 s57, 3\n\t"
        "s_cbranch_scc0 .L7_du4\n\t"
        "s_waitcnt vmcnt(2)\n\t"
        "s_cmp_eq_u32 s57, 0\n\t"
        "s_cbranch_scc0 .L7_du4\n\t"
        "s_waitcnt vmcnt(0)\n"
        ".L7_du4:\n\t"
        "v_mfma_f32_4x4x4_16b_bf16 v[32:35], v[12:13], v[96:97], v[32:35] cbsz:1 abid:0\n\t"
        "v_mfma_f32_4x4x4_16b_bf16 v[36:39], v[12:13], v[98:99], v[36:39] cbsz:1 abid:0\n\t"
        "s_sub_u32 s57, s57, 1\n\t"
        "v_mfma_f32_4x4x4_16b_bf16 v[32:35], v[12:13], v[100:101], v[32:35] cbsz:1 abid:1\n\t"
        "v_mfma_f32_4x4x4_16b_bf16 v[36:39], v[12:13], v[102:103], v[36:39] cbsz:1 abid:1\n\t"
        "s_cbranch_scc1 .L7_last\n"
        ".L7_dt5:\n\t"
        "v_perm_b32 v12, v51, v50, v8\n\t"
        "v_perm_b32 v13, v51, v50, v9\n\t"
        "s_waitcnt vmcnt(6)\n\t"
        "s_cmp_lt_u32 s57, 3\n\t"
        "s_cbranch_scc0 .L7_du5\n\t"
        "s_waitcnt vmcnt(2)\n\t"
        "s_cmp_eq_u32 s57, 0\n\t"
        "s_cbranch_scc0 .L7_du5\n\t"
        "s_waitcnt vmcnt(0)\n"
        ".L7_du5:\n\t"
        "v_mfma_f32_4x4x4_16b_bf16 v[32:35], v[12:13], v[104:105], v[32:35] cbsz:1 abid:0\n\t"
        "v_mfma_f32_4x4x4_16b_bf16 v[36:39], v[12:13], v[106:107], v[36:39] cbsz:1 abid:0\n\t"
        "s_sub_u32 s57, s57, 1\n\t"
        "v_mfma_f32_4x4x4_16b_bf16 v[32:35], v[12:13], v[108:109], v[32:35] cbsz:1 abid:1\n\t"
        "v_mfma_f32_4x4x4_16b_bf16 v[36:39], v[12:13], v[110:111], v[36:39] cbsz:1 abid:1\n\t"
        "s_cbranch_scc1 .L7_last\n"
        ".L7_dt6:\n\t"
        "v_perm_b32 v12, v53, v52, v8\n\t"
        "v_perm_b32 v13, v53, v52, v9\n\t"
        "s_waitcnt vmcnt(6)\n\t"
        "s_cmp_lt_u32 s57, 3\n\t"
        "s_cbranch_scc0 .L7_du6\n\t"
        "s_waitcnt vmcnt(2)\n\t"
        "s_cmp_eq_u32 s57, 0\n\t"
        "s_cbranch_scc0 .L7_du6\n\t"
        "s_waitcnt vmcnt(0)\n"
        ".L7_du6:\n\t"
        "v_mfma_f32_4x4x4_16b_bf16 v[32:35], v[12:13], v[112:113], v[32:35] cbsz:1 abid:0\n\t"
        "v_mfma_f32_4x4x4_16b_bf16 v[36:39], v[12:13], v[114:115], v[36:39] cbsz:1 abid:0\n\t"
        "s_sub_u32 s57, s57, 1\n\t"
        "v_mfma_f32_4x4x4_16b_bf16 v[32:35], v[12:13], v[116:117], v[32:35] cbsz:1 abid:1\n\t"
        "v_mfma_f32_4x4x4_16b_bf16 v[36:39], v[12:13], v[118:119], v[36:39] cbsz:1 abid:1\n\t"
        "s_cbranch_scc1 .L7_last\n"
        ".L7_dt7:\n\t"
        "v_perm_b32 v12, v55, v54, v8\n\t"
        "v_perm_b32 v13, v55, v54, v9\n\t"
        "s_waitcnt vmcnt(6)\n\t"
        "s_cmp_lt_u32 s57, 3\n\t"
        "s_cbranch_scc0 .L7_du7\n\t"
        "s_waitcnt vmcnt(2)\n\t"
        "s_cmp_eq_u32 s57, 0\n\t"
        "s_cbranch_scc0 .L7_du7\n\t"
        "s_waitcnt vmcnt(0)\n"
        ".L7_du7:\n\t"
        "v_mfma_f32_4x4x4_16b_bf16 v[32:35], v[12:13], v[120:121], v[32:35] cbsz:1 abid:0\n\t"
        "v_mfma_f32_4x4x4_16b_bf16 v[36:39], v[12:13], v[122:123], v[36:39] cbsz:1 abid:0\n\t"
        "s_sub_u32 s57, s57, 1\n\t"
        "v_mfma_f32_4x4x4_16b_bf16 v[32:35], v[12:13], v[124:125], v[32:35] cbsz:1 abid:1\n\t"
        "v_mfma_f32_4x4x4_16b_bf16 v[36:39], v[12:13], v[126:127], v[36:39] cbsz:1 abid:1\n\t"
        "s_cbranch_scc1 .L7_last\n\t"
        "s_branch .L7_dt0\n"
        ".L7_ie:\n\t"
        "v_and_b32 v63, 0xff, v25\n\t"
        "v_mad_u32_u24 v63, v63, s38, v7\n\t"
        "v_subrev_u32 v63, s60, v63\n\t"
        "s_mov_b32 s56, s25\n\t"
        "s_swappc_b64 s[50:51], s[54:55]\n\t"
        "v_and_b32 v63, 0xff, v26\n\t"
        "v_mad_u32_u24 v63, v63, s38, v7\n\t"
        "v_subrev_u32 v63, s60, v63\n\t"
        "s_mov_b32 s56, s26\n\t"
        "s_swappc_b64 s[50:51], s[54:55]\n\t"
        "v_and_b32 v63, 0xff, v27\n\t"
        "v_mad_u32_u24 v63, v63, s38, v7\n\t"
        "v_subrev_u32 v63, s60, v63\n\t"
        "s_mov_b32 s56, s27\n\t"
        "s_swappc_b64 s[50:51], s[54:55]\n\t"
        "v_and_b32 v63, 0xff, v28\n\t"
        "v_mad_u32_u24 v63, v63, s38, v7\n\t"
        "v_subrev_u32 v63, s60, v63\n\t"
        "s_add_u32 s56, s28, 2\n\t"
        "s_swappc_b64 s[50:51], s[54:55]\n\t"
        "s_mov_b64 vcc, -1\n\t"
        "s_setpc_b64 s[54:55]\n"
        ".L7_ce:\n\t"
        "v_and_b32 v4, 0xffff0000, v24\n\t"
        "s_nop 2\n\t"
        "v_pk_mul_f32 v[32:33], v[32:33], v[4:5] op_sel_hi:[1,0]\n\t"
        "v_pk_mul_f32 v[34:35], v[34:35], v[4:5] op_sel_hi:[1,0]\n\t"
        "v_pk_mul_f32 v[36:37], v[36:37], v[4:5] op_sel_hi:[1,0]\n\t"
        "v_pk_mul_f32 v[38:39], v[38:39], v[4:5] op_sel_hi:[1,0]\n\t"
        "v_cvt_pk_bf16_f32 v12, v32, v33\n\t"
        "v_cvt_pk_bf16_f32 v13, v34, v35\n\t"
        "v_cvt_pk_bf16_f32 v14, v36, v37\n\t"
        "v_cvt_pk_bf16_f32 v15, v38, v39\n"
        ".L7_w117:\n\t"
        "ds_read_b32 v4, v2 offset:12\n\t"
        "s_waitcnt lgkmcnt(0)\n\t"
        "v_cmp_gt_u16 vcc, 4, v4\n\t"
        "s_cbranch_vccz .L7_d118\n\t"
        "s_sleep 1\n\t"
        "s_sub_i32 s83, s83, 1\n\t"
        "s_cmp_gt_i32 s83, 0\n\t"
        "s_cbranch_scc1 .L7_w117\n"
        ".L7_d118:\n\t"
        "s_mov_b64 vcc, 0\n\t"
        "s_setprio 3\n\t"
        "ds_write_b128 v56, v[12:15] offset:23808\n\t"
        "s_mov_b64 exec, 1\n\t"
        "ds_add_u32 v2, v3 offset:52\n\t"
        "s_mov_b64 exec, -1\n\t"
        "v_mov_b64 v[32:33], 0\n\t"
        "v_mov_b64 v[34:35], 0\n\t"
        "v_mov_b64 v[36:37], 0\n\t"
        "v_mov_b64 v[38:39], 0\n\t"
        "s_mov_b32 s57, s25\n\t"
        "s_swappc_b64 s[52:53], s[54:55]\n\t"
        "v_and_b32 v4, 0xffff0000, v25\n\t"
        "s_nop 2\n\t"
        "v_pk_mul_f32 v[32:33], v[32:33], v[4:5] op_sel_hi:[1,0]\n\t"
        "v_pk_mul_f32 v[34:35], v[34:35], v[4:5] op_sel_hi:[1,0]\n\t"
        "v_pk_mul_f32 v[36:37], v[36:37], v[4:5] op_sel_hi:[1,0]\n\t"
        "v_pk_mul_f32 v[38:39], v[38:39], v[4:5] op_sel_hi:[1,0]\n\t"
        "v_cvt_pk_bf16_f32 v12, v32, v33\n\t"
        "v_cvt_pk_bf16_f32 v13, v34, v35\n\t"
        "v_cvt_pk_bf16_f32 v14, v36, v37\n\t"
        "v_cvt_pk_bf16_f32 v15, v38, v39\n\t"
        "ds_write_b128 v57, v[12:15] offset:23808\n\t"
        "s_mov_b64 exec, 1\n\t"
        "ds_add_u32 v61, v3 offset:52\n\t"
        "s_mov_b64 exec, -1\n\t"
        "v_mov_b64 v[32:33], 0\n\t"
        "v_mov_b64 v[34:35], 0\n\t"
        "v_mov_b64 v[36:37], 0\n\t"
        "v_mov_b64 v[38:39], 0\n\t"
        "s_mov_b32 s57, s26\n\t"
        "s_swappc_b64 s[52:53], s[54:55]\n\t"
        "v_and_b32 v4, 0xffff0000, v26\n\t"
        "s_nop 2\n\t"
        "v_pk_mul_f32 v[32:33], v[32:33], v[4:5] op_sel_hi:[1,0]\n\t"
        "v_pk_mul_f32 v[34:35], v[34:35], v[4:5] op_sel_hi:[1,0]\n\t"
        "v_pk_mul_f32 v[36:37], v[36:37], v[4:5] op_sel_hi:[1,0]\n\t"
        "v_pk_mul_f32 v[38:39], v[38:39], v[4:5] op_sel_hi:[1,0]\n\t"
        "v_cvt_pk_bf16_f32 v12, v32, v33\n\t"
        "v_cvt_pk_bf16_f32 v13, v34, v35\n\t"
        "v_cvt_pk_bf16_f32 v14, v36, v37\n\t"
        "v_cvt_pk_bf16_f32 v15, v38, v39\n\t"
        "ds_write_b128 v58, v[12:15] offset:23808\n\t"
        "s_mov_b64 exec, 1\n\t"
        "ds_add_u32 v61, v3 offset:56\n\t"
        "s_mov_b64 exec, -1\n\t"
        "v_mov_b64 v[32:33], 0\n\t"
        "v_mov_b64 v[34:35], 0\n\t"
        "v_mov_b64 v[36:37], 0\n\t"
        "v_mov_b64 v[38:39], 0\n\t"
        "s_mov_b32 s57, s27\n\t"
        "s_swappc_b64 s[52:53], s[54:55]\n\t"
        "v_and_b32 v4, 0xffff0000, v27\n\t"
        "s_nop 2\n\t"
        "v_pk_mul_f32 v[32:33], v[32:33], v[4:5] op_sel_hi:[1,0]\n\t"
        "v_pk_mul_f32 v[34:35], v[34:35], v[4:5] op_sel_hi:[1,0]\n\t"
        "v_pk_mul_f32 v[36:37], v[36:37], v[4:5] op_sel_hi:[1,0]\n\t"
        "v_pk_mul_f32 v[38:39], v[38:39], v[4:5] op_sel_hi:[1,0]\n\t"
        "v_cvt_pk_bf16_f32 v12, v32, v33\n\t"
        "v_cvt_pk_bf16_f32 v13, v34, v35\n\t"
        "v_cvt_pk_bf16_f32 v14, v36, v37\n\t"
        "v_cvt_pk_bf16_f32 v15, v38, v39\n\t"
        "ds_write_b128 v59, v[12:15] offset:21760\n\t"
        "s_mov_b64 exec, 1\n\t"
        "ds_add_u32 v2, v3 offset:64\n\t"
        "s_mov_b64 exec, -1\n\t"
        "v_mov_b64 v[32:33], 0\n\t"
        "v_mov_b64 v[34:35], 0\n\t"
        "v_mov_b64 v[36:37], 0\n\t"
        "v_mov_b64 v[38:39], 0\n\t"
        "s_mov_b32 s57, s28\n\t"
        "s_swappc_b64 s[52:53], s[54:55]\n"
        ".L7_last:\n\t"
        "v_and_b32 v4, 0xffff0000, v28\n\t"
        "s_nop 2\n\t"
        "v_pk_mul_f32 v[32:33], v[32:33], v[4:5] op_sel_hi:[1,0]\n\t"
        "v_pk_mul_f32 v[34:35], v[34:35], v[4:5] op_sel_hi:[1,0]\n\t"
        "v_pk_mul_f32 v[36:37], v[36:37], v[4:5] op_sel_hi:[1,0]\n\t"
        "v_pk_mul_f32 v[38:39], v[38:39], v[4:5] op_sel_hi:[1,0]\n\t"
        "v_cvt_pk_bf16_f32 v12, v32, v33\n\t"
        "v_cvt_pk_bf16_f32 v13, v34, v35\n\t"
        "v_cvt_pk_bf16_f32 v14, v36, v37\n\t"
        "v_cvt_pk_bf16_f32 v15, v38, v39\n\t"
        "ds_write_b128 v60, v[12:15] offset:21760\n\t"
        "s_mov_b64 exec, 1\n\t"
        "ds_add_u32 v2, v3 offset:68\n\t"
        "s_mov_b64 exec, -1\n\t"
        "s_endpgm\n"
        ".L7_x:\n\t"
        "s_mov_b32 s36, 1\n\t"
        "s_waitcnt lgkmcnt(0)\n\t"
        "v_dot4_u32_u8 v83, v67, s39, v7\n\t"
        "v_lshlrev_b32_sdwa v115, 1, v67 dst_sel:DWORD dst_unused:UNUSED_PAD src0_sel:DWORD src1_sel:WORD_0\n\t"
        "ds_read_u16 v115, v115 offset:46336\n\t"
        "v_cmp_eq_u16 vcc, -1, v67\n\t"
        "s_cmp_lg_u64 vcc, 0\n\t"
        "s_cselect_b32 s78, 0x10000, 0\n\t"
        "v_or_b32 v30, s78, v30\n\t"
        "ds_add_rtn_u32 v99, v83, v29 offset:192\n\t"
        "v_dot4_u32_u8 v87, v71, s39, v7\n\t"
        "v_lshlrev_b32_sdwa v119, 1, v71 dst_sel:DWORD dst_unused:UNUSED_PAD src0_sel:DWORD src1_sel:WORD_0\n\t"
        "ds_read_u16 v119, v119 offset:46336\n\t"
        "v_cmp_eq_u16 vcc, -1, v71\n\t"
        "s_cmp_lg_u64 vcc, 0\n\t"
        "s_cselect_b32 s78, 0x10000, 0\n\t"
        "v_or_b32 v30, s78, v30\n\t"
        "ds_add_rtn_u32 v103, v87, v29 offset:192\n\t"
        "v_dot4_u32_u8 v91, v75, s39, v7\n\t"
        "v_lshlrev_b32_sdwa v123, 1, v75 dst_sel:DWORD dst_unused:UNUSED_PAD src0_sel:DWORD src1_sel:WORD_0\n\t"
        "ds_read_u16 v123, v123 offset:46336\n\t"
        "v_cmp_eq_u16 vcc, -1, v75\n\t"
        "s_cmp_lg_u64 vcc, 0\n\t"
        "s_cselect_b32 s78, 0x10000, 0\n\t"
        "v_or_b32 v30, s78, v30\n\t"
        "ds_add_rtn_u32 v107, v91, v29 offset:192\n\t"
        "v_dot4_u32_u8 v95, v79, s39, v7\n\t"
        "v_lshlrev_b32_sdwa v127, 1, v79 dst_sel:DWORD dst_unused:UNUSED_PAD src0_sel:DWORD src1_sel:WORD_0\n\t"
        "ds_read_u16 v127, v127 offset:46336\n\t"
        "v_cmp_eq_u16 vcc, -1, v79\n\t"
        "s_cmp_lg_u64 vcc, 0\n\t"
        "s_cselect_b32 s78, 0x10000, 0\n\t"
        "v_or_b32 v30, s78, v30\n\t"
        "ds_add_rtn_u32 v111, v95, v29 offset:192\n\t"
        "s_branch .L7_y\n"
        ".L7_z:\n\t"
        "s_waitcnt lgkmcnt(0)\n\t"
        "ds_read_b32 v32, v83 offset:192\n\t"
        "ds_read_b32 v33, v87 offset:192\n\t"
        "ds_read_b32 v34, v91 offset:192\n\t"
        "ds_read_b32 v35, v95 offset:192\n\t"
        "v_bfe_u32 v99, v99, s33, 8\n\t"
        "v_perm_b32 v115, v115, v67, s49\n\t"
        "v_bfe_u32 v103, v103, s33, 8\n\t"
        "v_perm_b32 v119, v119, v71, s49\n\t"
        "v_bfe_u32 v107, v107, s33, 8\n\t"
        "v_perm_b32 v123, v123, v75, s49\n\t"
        "v_bfe_u32 v111, v111, s33, 8\n\t"
        "v_perm_b32 v127, v127, v79, s49\n\t"
        "s_waitcnt lgkmcnt(0)\n\t"
        "v_and_b32 v32, s34, v32\n\t"
        "v_sad_u8 v99, v32, 0, v99\n\t"
        "v_and_b32 v33, s34, v33\n\t"
        "v_sad_u8 v103, v33, 0, v103\n\t"
        "v_and_b32 v34, s34, v34\n\t"
        "v_sad_u8 v107, v34, 0, v107\n\t"
        "v_and_b32 v35, s34, v35\n\t"
        "v_sad_u8 v111, v35, 0, v111\n\t"
        "v_min_u32 v99, 47, v99\n\t"
        "v_min_u32 v103, 47, v103\n\t"
        "v_min_u32 v107, 47, v107\n\t"
        "v_min_u32 v111, 47, v111\n\t"
        "v_lshl_add_u32 v99, v99, 2, v83\n\t"
        "ds_write_b32 v99, v115\n\t"
        "v_lshl_add_u32 v103, v103, 2, v87\n\t"
        "ds_write_b32 v103, v119\n\t"
        "v_lshl_add_u32 v107, v107, 2, v91\n\t"
        "ds_write_b32 v107, v123\n\t"
        "v_lshl_add_u32 v111, v111, 2, v95\n\t"
        "ds_write_b32 v111, v127\n\t"
        "s_branch .L7_zr\n"
        ".L7_exit4:\n\t"
        "s_add_u32 s69, s69, 4\n"
        ".L7_exit:\n\t"
        "s_waitcnt vmcnt(0) lgkmcnt(0)\n\t"
        "s_setprio 0\n\t"
        : "+{v0}"(tid), "=&{s69}"(wv)
        : "s"(__builtin_amdgcn_kernarg_segment_ptr()), "s"(blockIdx.x)
        : "scc", "vcc", "memory", "s16", "s17", "s18", "s19", "s20", "s21", "s22", "s23", "s24", "s25", "s26", "s27", "s28", "s29", "s30", "s31", "s32", "s33", "s34", "s35",
          "s36", "s37", "s38", "s39", "s40", "s41", "s42", "s43", "s44", "s45", "s46", "s47", "s48", "s49", "s50", "s51", "s52", "s53", "s54", "s55",
          "s56", "s57", "s58", "s59", "s60", "s61", "s62", "s63", "s64", "s65", "s66", "s67", "s68", "s70", "s71", "s72", "s73", "s74", "s75", "s76",
          "s77", "s78", "s79", "s80", "s81", "s82", "s83", "s84", "s85", "s86", "s87", "s88", "s89", "s90", "s91", "s92", "s93", "s94", "s95", "s96",
          "s97", "s98", "s99", "s100", "s101", "v1", "v2", "v3", "v4", "v5", "v6", "v7", "v8", "v9", "v10", "v11", "v12", "v13", "v14", "v15",
          "v16", "v17", "v18", "v19", "v20", "v21", "v22", "v23", "v24", "v25", "v26", "v27", "v28", "v29", "v30", "v31", "v32", "v33", "v34", "v35",
          "v36", "v37", "v38", "v39", "v40", "v41", "v42", "v43", "v44", "v45", "v46", "v47", "v48", "v49", "v50", "v51", "v52", "v53", "v54", "v55",
          "v56", "v57", "v58", "v59", "v60", "v61", "v62", "v63", "v64", "v65", "v66", "v67", "v68", "v69", "v70", "v71", "v72", "v73", "v74", "v75",
          "v76", "v77", "v78", "v79", "v80", "v81", "v82", "v83", "v84", "v85", "v86", "v87", "v88", "v89", "v90", "v91", "v92", "v93", "v94", "v95",
          "v96", "v97", "v98", "v99", "v100", "v101", "v102", "v103", "v104", "v105", "v106", "v107", "v108", "v109", "v110", "v111", "v112", "v113", "v114", "v115",
          "v116", "v117", "v118", "v119", "v120", "v121", "v122", "v123", "v124", "v125", "v126", "v127");
    const __attribute__((address_space(4))) Args* q = (const __attribute__((address_space(4))) Args*)__builtin_amdgcn_kernarg_segment_ptr();
    asm volatile("" : "+s"(q));
    const u32 wave = __builtin_amdgcn_readfirstlane(wv);
    u32 lane;
    asm volatile("v_mbcnt_lo_u32_b32 %0, -1, 0\n\tv_mbcnt_hi_u32_b32 %0, -1, %0" : "=v"(lane));
    const u32 half = blockIdx.x & 1u, TN = q->TN, r0 = (blockIdx.x >> 1) * TN;
    const u16* x = q->xall;
    _Pragma("nounroll") for (u32 n = 0; n < (1u << 26) && *(volatile __attribute__((address_space(3))) u32*)S_QK < 4u; ++n) __builtin_amdgcn_s_sleep(2);
    if (wave >= 4u) {
        const int E = q->E, *dst = (const int*)q->dst.p, *src = (const int*)q->src.p;
        const u16* dis = q->disall;
        const u32 w = wave - 4u;
        _Pragma("nounroll") for (u32 k = 0; k < 5u; ++k) {
            const u32 grp = (u32)(((w == 0u ? 0x1713050109ull : w == 1u ? 0x1612040008ull : w == 2u ? 0x14100e0602ull : 0x15110f0703ull) >> (8u * k)) & 255u);
            const u32 row = grp * 4u + (lane >> 4), iv = *(const u32*)(smem + L_INV + row * 4u);
            const int me = (iv & 0x8000u) ? -1 : q->nbase + (int)(r0 + (iv & 255u) + TN - 255u);
            const int m0 = __builtin_amdgcn_readlane(me, 0), m1 = __builtin_amdgcn_readlane(me, 16), m2 = __builtin_amdgcn_readlane(me, 32), m3 = __builtin_amdgcn_readlane(me, 48);
            float s[8] = {0.f, 0.f, 0.f, 0.f, 0.f, 0.f, 0.f, 0.f};
            for (int e0 = 0; e0 < E; e0 += 64) {
                const int e = min(e0 + (int)lane, E - 1), d = dst[e], si = src[e];
                unsigned long long m = __ballot(e0 + (int)lane < E && (d == m0 || d == m1 || d == m2 || d == m3));
                while (m) {
                    const int l = __builtin_ctzll(m);
                    m &= m - 1;
                    const int sl = __builtin_amdgcn_readlane(si, l);
                    if (__builtin_amdgcn_readlane(d, l) == me) {
                        const u32x4 r = *(const u32x4*)(x + (size_t)sl * 256 + half * 128u + (lane & 15u) * 8u);
                        const float wt = bf2f(dis[sl]);
#pragma unroll
                        for (int j = 0; j < 4; ++j) { s[2 * j] += wt * lo(r[j]); s[2 * j + 1] += wt * hi(r[j]); }
                    }
                }
            }
            const float sc = bf2f(dis[max(me, 0)]);
            *(u32x4*)(smem + L_AL + (row < 48u ? row : row - 8u) * 256u + (((lane ^ row) & 15u) << 4)) =
                (u32x4){pack2(s[0] * sc, s[1] * sc), pack2(s[2] * sc, s[3] * sc), pack2(s[4] * sc, s[5] * sc), pack2(s[6] * sc, s[7] * sc)};
            lds_signal(S_T0 + 4u * (grp < 12u ? 1u + ((grp >> 2) & 1u) : grp >> 2), 1u);
        }
        return;
    }
    const int Ee = q->Ee, *edst = (const int*)q->edst.p, *esrc = (const int*)q->esrc.p;
    const u16 *wq = (const u16*)q->wq.p, *wk = (const u16*)q->wk.p;
    float* qs = (float*)(smem + L_QK + wave * 1024u);
    _Pragma("nounroll") for (u32 n = wave; n < TN && r0 + n < q->N; n += 4u) {
        const int me = q->nbase + (int)(r0 + n);
        float c0 = 0.f, c1 = 0.f, c2 = 0.f, c3 = 0.f;
        _Pragma("nounroll") for (int i = 0; i < 256; ++i) {
            const float xv = bf2f(x[(size_t)me * 256 + i]);
            const u32x2 r = *(const u32x2*)(wq + i * 256 + lane * 4u);
            c0 += xv * lo(r[0]); c1 += xv * hi(r[0]); c2 += xv * lo(r[1]); c3 += xv * hi(r[1]);
        }
        qs[lane * 4u] = c0; qs[lane * 4u + 1u] = c1; qs[lane * 4u + 2u] = c2; qs[lane * 4u + 3u] = c3;
        float k[4] = {0.f, 0.f, 0.f, 0.f};
        _Pragma("nounroll") for (int c = 0; c < 256; c += 2) {
            const float qa = qs[c], qb = qs[c + 1];
#pragma unroll
            for (int j = 0; j < 4; ++j) { const u32 r = *(const u32*)(wk + (lane * 4u + j) * 256u + c); k[j] += qa * lo(r) + qb * hi(r); }
        }
        float s0 = 0.f, s1 = 0.f, s2 = 0.f, s3 = 0.f, dn = 0.f;
        for (int e0 = 0; e0 < Ee; e0 += 64) {
            const int e = min(e0 + (int)lane, Ee - 1), si = esrc[e];
            unsigned long long m = __ballot(e0 + (int)lane < Ee && edst[e] == me);
            while (m) {
                const int l = __builtin_ctzll(m);
                m &= m - 1;
                const u32x2 r = *(const u32x2*)(x + (size_t)__builtin_amdgcn_readlane(si, l) * 256 + lane * 4u);
                float d = k[0] * lo(r[0]) + k[1] * hi(r[0]) + k[2] * lo(r[1]) + k[3] * hi(r[1]);
#pragma unroll
                for (int o = 1; o < 64; o <<= 1)
                    d += __uint_as_float((u32)__builtin_amdgcn_ds_bpermute((int)((lane ^ (u32)o) * 4u), (int)__float_as_uint(d)));
                const float wt = __builtin_amdgcn_exp2f(d * 0.09016844f);
                s0 += wt * lo(r[0]); s1 += wt * hi(r[0]); s2 += wt * lo(r[1]); s3 += wt * hi(r[1]);
                dn += wt;
            }
        }
        const float sc = __builtin_amdgcn_rcpf(dn + 1e-9f);
        if ((lane >> 5) == half)
            *(u32x2*)(smem + L_AA + ((u32)*(const u16*)(smem + L_MAP + n * 2u) ^ ((lane & 31u) * 8u))) =
                (u32x2){pack2(s0 * sc, s1 * sc), pack2(s2 * sc, s3 * sc)};
    }
    lds_signal(S_Z, 17u);
}

extern "C" void kernel_launch(void* const* d_in, const int* in_sizes, int n_in,
                              void* d_out, int out_size, void* d_ws, size_t ws_size,
                              hipStream_t stream) {
    const u32 N = in_sizes[5], E = in_sizes[1], Ee = in_sizes[3];
    const u16* x = (const u16*)d_in[0];
    const u16* dis = (const u16*)d_in[5];
    const u32 FL = 0x00020000u;
    const bool generic = N > NMAX || E > EMAX || (E & 3u) || (N & 1u);
    const bool irr = N > NMAX || Ee != 4u * N;
    (void)hipFuncSetAttribute((const void*)agg_kernel, hipFuncAttributeMaxDynamicSharedMemorySize, LDS_BYTES);
    for (u32 nbase = 0; nbase < N; nbase += NMAX) {
        const u32 n = N - nbase < NMAX ? N - nbase : NMAX;
        Args a;
        a.x = Desc{x + (size_t)nbase * 256, n * 512u, FL};
        a.ws = Desc{d_ws, WS_END, FL};
        a.wl = Desc{d_in[6], 131072u, FL}; a.wq = Desc{d_in[7], 131072u, FL}; a.wk = Desc{d_in[8], 131072u, FL}; a.wv = Desc{d_in[9], 131072u, FL};
        a.src = Desc{d_in[1], generic ? 0u : E * 4u, FL}; a.dst = Desc{d_in[2], generic ? 0u : E * 4u, FL};
        a.esrc = Desc{d_in[3], irr ? 0u : Ee * 4u, FL}; a.edst = Desc{d_in[4], irr ? 0u : Ee * 4u, FL};
        a.dis = Desc{dis + nbase, n * 2u, FL};
        a.out = Desc{(u16*)d_out + (size_t)nbase * 256, n * 512u, FL};
        a.N = n; a.TN = n > 256u ? (n + 127u) / 128u : 2u; a.M = ((1u << 24) + a.TN - 1u) / a.TN;
        a.irr = (irr ? 1u : 0u) | (generic ? 0x10000u : 0u);
        a.xall = x; a.disall = dis;
        a.E = (int)E; a.Ee = (int)Ee; a.nbase = (int)nbase; a.pad = 0;
        agg_kernel<<<256, 1024, LDS_BYTES, stream>>>(a);
    }
}
```

```cpp
#include <hip/hip_runtime.h>
#include <hip/hip_bf16.h>
#include <stdint.h>

typedef unsigned short u16;
typedef unsigned int u32;
typedef __attribute__((ext_vector_type(4))) u32 u32x4;
typedef __attribute__((ext_vector_type(2))) u32 u32x2;
struct Desc { const void* p; u32 bytes, flags; };
struct Args {
    Desc x, ws, wl, wv, wq, wk, src, dst, esrc, edst, dis, out;
    u32 N, TN, M, irr;
    const u16* xall;
    const u16* disall;
    int E, Ee, nbase, pad;
};
constexpr u32 NMAX = 10240, EMAX = 163840;
constexpr u32 WS_END = 9437184;
constexpr int LDS_BYTES = 163840;
constexpr u32 L_INV = 512, L_MAP = 1792, L_QK = 2048, L_AL = 23808, L_AA = 46336, S_QK = 16, S_Z = 40, S_T0 = 48;

__device__ __forceinline__ float bf2f(u32 h) { return __uint_as_float(h << 16); }
__device__ __forceinline__ float lo(u32 v) { return __uint_as_float(v << 16); }
__device__ __forceinline__ float hi(u32 v) { return __uint_as_float(v & 0xFFFF0000u); }
__device__ __forceinline__ u32 pack2(float a, float b) {
    return (u32)__builtin_bit_cast(u16, (__bf16)a) | ((u32)__builtin_bit_cast(u16, (__bf16)b) << 16);
}
__device__ __forceinline__ void lds_signal(const u32 addr, const u32 v) {
    asm volatile("s_mov_b64 exec, 1\n\tds_add_u32 %0, %1\n\ts_mov_b64 exec, -1" :: "v"(addr), "v"(v) : "memory");
}

extern "C" __global__ void __launch_bounds__(1024, 4)
agg_kernel(const Args a) {
    extern __shared__ __attribute__((aligned(16))) char smem[];
    u32 tid = threadIdx.x, wv;
    asm volatile(
        "s_memrealtime s[74:75]\n\t"
        "s_load_dwordx8 s[16:23], %2, 0\n\t"
        "s_load_dwordx4 s[64:67], %2, 192\n\t"
        "v_readfirstlane_b32 s69, v0\n\t"
        "v_and_b32 v1, 63, v0\n\t"
        "v_mov_b32 v2, 0\n\t"
        "v_mov_b32 v3, 1\n\t"
        "s_lshr_b32 s76, %3, 3\n\t"
        "s_and_b32 s70, %3, 1\n\t"
        "s_lshr_b32 s71, %3, 1\n\t"
        "s_mov_b32 s83, 0x2000000\n\t"
        "s_lshr_b32 s69, s69, 6\n\t"
        "s_cbranch_scc0 .L7_U\n\t"
        "s_cmp_lt_u32 s69, 8\n\t"
        "s_cbranch_scc1 .L7_G0\n\t"
        "s_load_dwordx4 s[48:51], %2, 128\n\t"
        "s_sub_u32 s69, s69, 8\n\t"
        "v_lshlrev_b32 v4, 2, v1\n\t"
        "s_lshl_b32 s77, s70, 2\n\t"
        "s_xor_b32 s77, s77, s69\n\t"
        "s_xor_b32 s77, s77, 4\n\t"
        "s_mul_i32 s80, s77, 10\n\t"
        "s_waitcnt lgkmcnt(0)\n\t"
        "s_mul_i32 s72, s71, s65\n\t"
        "s_add_u32 s78, s72, s80\n\t"
        "s_lshl_b32 s78, s78, 4\n\t"
        "s_mov_b32 exec_hi, 0xff\n\t"
        "buffer_load_dword v5, v4, s[48:51], s78 offen\n\t"
        "s_mov_b64 exec, -1\n\t"
        "s_barrier\n\t"
        "s_branch .L7_O2\n"
        ".L7_U:\n\t"
        "s_load_dwordx16 s[32:47], %2, 64\n\t"
        "s_load_dwordx16 s[48:63], %2, 128\n\t"
        "s_setprio 3\n\t"
        "s_getpc_b64 s[24:25]\n\t"
        "s_add_u32 s24, s24, agg_kernel@rel32@lo+4\n\t"
        "s_addc_u32 s25, s25, agg_kernel@rel32@hi+12\n\t"
        "s_and_b32 s25, s25, 0xffff\n\t"
        "s_mov_b32 s26, .L7_x-agg_kernel\n\t"
        "s_mov_b32 s27, 0x20000\n\t"
        "v_lshlrev_b32 v6, 12, v1\n\t"
        "s_lshl_b32 s78, s76, 7\n\t"
        "s_mov_b32 m0, 134400\n\t"
        "s_mov_b64 exec, 63\n\t"
        "buffer_load_dword v6, s[24:27], s78 offen lds\n\t"
        "s_mov_b64 exec, -1\n\t"
        "s_cmp_eq_u32 s70, 0\n\t"
        "s_cbranch_scc0 .L7_V\n\t"
        "v_lshrrev_b32 v4, 3, v1\n\t"
        "v_and_b32 v5, 7, v1\n\t"
        "v_lshlrev_b32 v4, 14, v4\n\t"
        "v_lshl_add_u32 v4, v5, 4, v4\n\t"
        "s_lshl_b32 s78, s71, 7\n\t"
        "v_add_u32 v4, s78, v4\n\t"
        "v_add_u32 v5, 0x20000, v4\n\t"
        "v_add_u32 v6, 0x40000, v4\n\t"
        "v_add_u32 v7, 0x60000, v4\n\t"
        "v_add_u32 v8, 0x80000, v4\n\t"
        "s_waitcnt lgkmcnt(0)\n\t"
        "s_mul_i32 s72, s71, s65\n\t"
        "buffer_load_dwordx4 v[16:19], v4, s[44:47], 0 offen\n\t"
        "buffer_load_dwordx4 v[20:23], v5, s[44:47], 0 offen\n\t"
        "buffer_load_dwordx4 v[24:27], v6, s[44:47], 0 offen\n\t"
        "buffer_load_dwordx4 v[28:31], v7, s[44:47], 0 offen\n\t"
        "buffer_load_dwordx4 v[32:35], v8, s[44:47], 0 offen\n\t"
        "s_mul_i32 s80, s71, 0x50\n\t"
        "s_lshl_b32 s78, s80, 4\n\t"
        "v_lshlrev_b32 v12, 4, v1\n\t"
        "buffer_load_dwordx4 v[116:119], v12, s[52:55], s78 offen\n\t"
        "s_mov_b64 exec, 0xffff\n\t"
        "buffer_load_dwordx4 v[120:123], v12, s[52:55], s78 offen offset:1024\n\t"
        "s_mov_b64 exec, -1\n\t"
        "buffer_load_dwordx4 v[36:39], v4, s[40:43], 0 offen\n\t"
        "buffer_load_dwordx4 v[40:43], v5, s[40:43], 0 offen\n\t"
        "buffer_load_dwordx4 v[44:47], v6, s[40:43], 0 offen\n\t"
        "buffer_load_dwordx4 v[48:51], v7, s[40:43], 0 offen\n\t"
        "buffer_load_dwordx4 v[52:55], v8, s[40:43], 0 offen\n\t"
        "s_barrier\n\t"
        "s_branch .L7_U2\n"
        ".L7_G0:\n\t"
        "s_cmp_lt_u32 s69, 4\n\t"
        "s_cbranch_scc1 .L7_L\n\t"
        "s_sub_u32 s69, s69, 4\n\t"
        "v_lshlrev_b32 v4, 4, v1\n\t"
        "v_and_b32 v5, 0x200, v4\n\t"
        "v_mad_u32_u24 v4, v5, 7, v4\n\t"
        "s_mul_i32 s80, s70, 5\n\t"
        "s_mul_i32 s81, s69, 0x420\n\t"
        "s_add_u32 s81, s81, 2048\n\t"
        "s_mul_i32 s78, s80, 0x1080\n\t"
        "s_add_u32 m0, s78, s81\n\t"
        "s_lshr_b32 s78, s80, 1\n\t"
        "s_lshl1_add_u32 s78, s78, s80\n\t"
        "s_lshl_b32 s78, s78, 11\n\t"
        "s_waitcnt lgkmcnt(0)\n\t"
        "s_mul_i32 s72, s71, s65\n\t"
        "s_add_u32 s79, s72, s69\n\t"
        "s_lshl_b32 s79, s79, 9\n\t"
        "v_add_u32 v4, s79, v4\n\t"
        "s_sleep 3\n\t"
        "s_cmp_eq_u32 s70, 0\n\t"
        "s_cbranch_scc1 .L7_gs\n\t"
        "s_sleep 4\n"
        ".L7_gs:\n\t"
        "buffer_load_dwordx4 v4, s[16:19], s78 offen lds\n\t"
        "s_add_u32 s80, s80, 1\n\t"
        "s_load_dwordx4 s[56:59], %2, 160\n\t"
        "s_mul_i32 s78, s80, 0x1080\n\t"
        "s_add_u32 m0, s78, s81\n\t"
        "s_lshr_b32 s78, s80, 1\n\t"
        "s_lshl1_add_u32 s78, s78, s80\n\t"
        "s_lshl_b32 s78, s78, 11\n\t"
        "buffer_load_dwordx4 v4, s[16:19], s78 offen lds\n\t"
        "s_add_u32 s80, s80, 1\n\t"
        "s_mul_i32 s78, s80, 0x1080\n\t"
        "s_add_u32 m0, s78, s81\n\t"
        "s_lshr_b32 s78, s80, 1\n\t"
        "s_lshl1_add_u32 s78, s78, s80\n\t"
        "s_lshl_b32 s78, s78, 11\n\t"
        "buffer_load_dwordx4 v4, s[16:19], s78 offen lds\n\t"
        "s_add_u32 s80, s80, 1\n\t"
        "s_mul_i32 s78, s80, 0x1080\n\t"
        "s_add_u32 m0, s78, s81\n\t"
        "s_lshr_b32 s78, s80, 1\n\t"
        "s_lshl1_add_u32 s78, s78, s80\n\t"
        "s_lshl_b32 s78, s78, 11\n\t"
        "buffer_load_dwordx4 v4, s[16:19], s78 offen lds\n\t"
        "s_add_u32 s80, s80, 1\n\t"
        "s_mul_i32 s78, s80, 0x1080\n\t"
        "s_add_u32 m0, s78, s81\n\t"
        "s_lshr_b32 s78, s80, 1\n\t"
        "s_lshl1_add_u32 s78, s78, s80\n\t"
        "s_lshl_b32 s78, s78, 11\n\t"
        "buffer_load_dwordx4 v4, s[16:19], s78 offen lds\n\t"
        "s_add_u32 s80, s80, 1\n\t"
        "s_barrier\n\t"
        "s_branch .L7_G2\n"
        ".L7_L:\n\t"
        "s_cmp_eq_u32 s69, 2\n\t"
        "s_cbranch_scc0 .L7_L1\n\t"
        "s_waitcnt lgkmcnt(0)\n\t"
        "s_mul_i32 s72, s71, s65\n\t"
        "s_buffer_load_dword s68, s[20:23], 0\n\t"
        "s_barrier\n\t"
        "s_branch .L7_P2\n"
        ".L7_L1:\n\t"
        "s_cmp_eq_u32 s69, 1\n\t"
        "s_cbranch_scc0 .L7_T\n\t"
        "s_load_dwordx4 s[56:59], %2, 160\n\t"
        "v_lshlrev_b32 v4, 3, v1\n\t"
        "v_mov_b64 v[8:9], 0\n\t"
        "ds_write_b64 v4, v[8:9]\n\t"
        "s_waitcnt lgkmcnt(0)\n\t"
        "s_mul_i32 s72, s71, s65\n\t"
        "v_add_lshl_u32 v4, v1, s72, 1\n\t"
        "buffer_load_ushort v16, v4, s[56:59], 0 offen\n\t"
        "buffer_load_ushort v17, v4, s[56:59], 0 offen offset:128\n\t"
        "s_barrier\n\t"
        "s_branch .L7_R2\n"
        ".L7_T:\n\t"
        "s_load_dwordx8 s[24:31], %2, 32\n\t"
        "v_lshlrev_b32 v6, 7, v1\n\t"
        "s_mov_b32 m0, 134400\n\t"
        "s_lshl_b32 s78, s70, 16\n\t"
        "s_lshl_b32 s79, s76, 11\n\t"
        "s_add_u32 s80, s78, s79\n\t"
        "s_mul_i32 s81, s76, 0x300\n\t"
        "s_lshl_b32 s82, %3, 7\n\t"
        "s_add_u32 s82, s82, 4096\n\t"
        "v_lshlrev_b32 v8, 2, v1\n\t"
        "s_and_b32 s78, s71, 63\n\t"
        "s_lshl_b32 s78, s78, 7\n\t"
        "s_and_b32 s79, s71, 64\n\t"
        "s_lshl_b32 s79, s79, 8\n\t"
        "s_add_u32 s42, s78, s79\n\t"
        "s_and_b32 s44, s76, 1\n\t"
        "s_lshl_b32 s78, s44, 13\n\t"
        "s_add_u32 s42, s42, s78\n\t"
        "s_lshl_b32 s78, s70, 15\n\t"
        "s_add_u32 s42, s42, s78\n\t"
        "s_add_u32 s42, s42, 65536\n\t"
        "s_xor_b32 s43, s42, 0x2000\n\t"
        "s_waitcnt lgkmcnt(0)\n\t"
        "s_mul_i32 s72, s71, s65\n\t"
        "s_buffer_load_dword s40, s[20:23], s42 glc\n\t"
        "s_buffer_load_dword s41, s[20:23], s43 glc\n\t"
        "v_mov_b32 v7, s74\n\t"
        "s_mov_b32 exec_hi, 0\n\t"
        "buffer_store_dword v7, v8, s[20:23], s82 offen sc1\n\t"
        "s_lshr_b32 s78, %3, 2\n\t"
        "s_xor_b32 s44, s44, s78\n\t"
        "s_waitcnt lgkmcnt(1)\n\t"
        "s_nop 8\n\t"
        "s_getreg_b32 s45, hwreg(HW_REG_IB_STS, 8, 4)\n\t"
        "s_xnor_b32 s44, s44, s45\n\t"
        "s_and_b32 s44, s44, 1\n\t"
        "s_lshl_b32 s44, s44, 25\n\t"
        "v_mov_b32 v7, s44\n\t"
        "ds_write_b32 v2, v7 offset:1984\n\t"
        "s_waitcnt lgkmcnt(0)\n\t"
        "s_barrier\n\t"
        "s_mov_b64 exec, -1\n\t"
        "s_mov_b64 exec, 0xffff\n\t"
        "buffer_load_dword v6, s[28:31], s80 offen lds\n\t"
        "buffer_load_dword v6, s[24:27], s80 offen lds\n\t"
        "s_mov_b64 exec, -1\n\t"
        "s_branch .L7_T2\n"
        ".L7_V:\n\t"
        "s_and_b32 s78, s71, 1\n\t"
        "s_and_b32 s79, s76, 3\n\t"
        "s_lshl2_add_u32 s80, s78, s79\n\t"
        "s_and_b32 s78, s71, 2\n\t"
        "s_lshr_b32 s79, s76, 2\n\t"
        "s_lshl2_add_u32 s81, s78, s79\n\t"
        "v_lshlrev_b32 v12, 4, v1\n\t"
        "v_and_b32 v5, 0x200, v12\n\t"
        "v_mad_u32_u24 v4, v5, 15, v12\n\t"
        "s_lshl_b32 s82, s80, 14\n\t"
        "s_lshr_b32 s78, s81, 1\n\t"
        "s_lshl_b32 s78, s78, 14\n\t"
        "s_and_b32 s79, s81, 1\n\t"
        "s_lshl_b32 s79, s79, 11\n\t"
        "s_add_u32 s77, s78, s79\n\t"
        "s_mov_b32 m0, 68864\n\t"
        "s_waitcnt lgkmcnt(0)\n\t"
        "s_mul_i32 s72, s71, s65\n\t"
        "buffer_load_dwordx4 v4, s[36:39], s77 offen offset:0 lds\n\t"
        "s_add_u32 m0, m0, 544\n\t"
        "s_nop 0\n\t"
        "buffer_load_dwordx4 v4, s[36:39], s77 offen offset:512 lds\n\t"
        "s_add_u32 m0, m0, 544\n\t"
        "s_nop 0\n\t"
        "buffer_load_dwordx4 v4, s[36:39], s77 offen offset:1024 lds\n\t"
        "s_add_u32 m0, m0, 544\n\t"
        "s_nop 0\n\t"
        "buffer_load_dwordx4 v4, s[36:39], s77 offen offset:1536 lds\n\t"
        "s_add_u32 m0, m0, 2592\n\t"
        "s_add_u32 s77, s77, 0x1000\n\t"
        "buffer_load_dwordx4 v4, s[36:39], s77 offen offset:0 lds\n\t"
        "s_add_u32 m0, m0, 544\n\t"
        "s_nop 0\n\t"
        "buffer_load_dwordx4 v4, s[36:39], s77 offen offset:512 lds\n\t"
        "s_add_u32 m0, m0, 544\n\t"
        "s_nop 0\n\t"
        "buffer_load_dwordx4 v4, s[36:39], s77 offen offset:1024 lds\n\t"
        "s_add_u32 m0, m0, 544\n\t"
        "s_nop 0\n\t"
        "buffer_load_dwordx4 v4, s[36:39], s77 offen offset:1536 lds\n\t"
        "s_add_u32 m0, m0, 2592\n\t"
        "s_nop 0\n\t"
        "buffer_load_dwordx4 v4, s[32:35], s82 offen offset:0 lds\n\t"
        "s_add_u32 m0, m0, 544\n\t"
        "s_nop 0\n\t"
        "buffer_load_dwordx4 v4, s[32:35], s82 offen offset:512 lds\n\t"
        "s_add_u32 m0, m0, 544\n\t"
        "s_nop 0\n\t"
        "buffer_load_dwordx4 v4, s[32:35], s82 offen offset:1024 lds\n\t"
        "s_add_u32 m0, m0, 544\n\t"
        "s_nop 0\n\t"
        "buffer_load_dwordx4 v4, s[32:35], s82 offen offset:1536 lds\n\t"
        "s_add_u32 m0, m0, 2592\n\t"
        "s_add_u32 s82, s82, 0x1000\n\t"
        "buffer_load_dwordx4 v4, s[32:35], s82 offen offset:0 lds\n\t"
        "s_add_u32 m0, m0, 544\n\t"
        "s_nop 0\n\t"
        "buffer_load_dwordx4 v4, s[32:35], s82 offen offset:512 lds\n\t"
        "s_add_u32 m0, m0, 544\n\t"
        "s_nop 0\n\t"
        "buffer_load_dwordx4 v4, s[32:35], s82 offen offset:1024 lds\n\t"
        "s_add_u32 m0, m0, 544\n\t"
        "s_nop 0\n\t"
        "buffer_load_dwordx4 v4, s[32:35], s82 offen offset:1536 lds\n\t"
        "s_add_u32 m0, m0, 2592\n\t"
        "s_sub_u32 s82, s82, 0x800\n\t"
        "buffer_load_dwordx4 v4, s[32:35], s82 offen offset:0 lds\n\t"
        "s_add_u32 m0, m0, 544\n\t"
        "s_nop 0\n\t"
        "buffer_load_dwordx4 v4, s[32:35], s82 offen offset:512 lds\n\t"
        "s_add_u32 m0, m0, 544\n\t"
        "s_nop 0\n\t"
        "buffer_load_dwordx4 v4, s[32:35], s82 offen offset:1024 lds\n\t"
        "s_add_u32 m0, m0, 544\n\t"
        "s_nop 0\n\t"
        "buffer_load_dwordx4 v4, s[32:35], s82 offen offset:1536 lds\n\t"
        "s_add_u32 m0, m0, 2592\n\t"
        "s_add_u32 s82, s82, 0x1000\n\t"
        "buffer_load_dwordx4 v4, s[32:35], s82 offen offset:0 lds\n\t"
        "s_add_u32 m0, m0, 544\n\t"
        "s_nop 0\n\t"
        "buffer_load_dwordx4 v4, s[32:35], s82 offen offset:512 lds\n\t"
        "s_add_u32 m0, m0, 544\n\t"
        "s_nop 0\n\t"
        "buffer_load_dwordx4 v4, s[32:35], s82 offen offset:1024 lds\n\t"
        "s_add_u32 m0, m0, 544\n\t"
        "s_nop 0\n\t"
        "buffer_load_dwordx4 v4, s[32:35], s82 offen offset:1536 lds\n\t"
        "s_barrier\n\t"
        "s_branch .L7_V2\n"
        ".L7_U2:\n\t"
        "v_cmp_gt_u32 s[24:25], s46, v4\n\t"
        "v_cmp_gt_u32 s[26:27], s46, v5\n\t"
        "v_cmp_gt_u32 s[28:29], s46, v6\n\t"
        "v_cmp_gt_u32 s[30:31], s46, v7\n\t"
        "v_cmp_gt_u32 s[32:33], s46, v8\n\t"
        "v_mul_u32_u24 v112, 0xa0, v1\n\t"
        "v_add_u32 v112, 68864, v112\n\t"
        "v_add_u32 v113, 10240, v112\n\t"
        "v_lshlrev_b32 v7, 2, v1\n\t"
        "ds_write_b32 v7, v112 offset:1028\n\t"
        "ds_write_b32 v7, v113 offset:1284\n\t"
        "v_lshrrev_b32 v15, 3, v1\n\t"
        "v_mul_u32_u24 v15, 0xa0, v15\n\t"
        "v_add_u32 v15, 68864, v15\n\t"
        "v_and_b32 v4, 7, v1\n\t"
        "v_lshl_add_u32 v15, v4, 2, v15\n\t"
        "v_mov_b32 v13, 0x1000000\n\t"
        "v_mov_b32 v14, 4\n\t"
        "s_lshl_b32 s54, s65, 14\n\t"
        "s_sub_u32 s54, 0, s54\n\t"
        "s_lshl_b32 s48, s71, 14\n\t"
        "s_add_u32 s48, s48, 1048576\n\t"
        "s_add_u32 s49, s48, 4096\n\t"
        "v_add_u32 v97, 1280, v15\n\t"
        "v_add_u32 v98, 2560, v15\n\t"
        "v_add_u32 v99, 3840, v15\n\t"
        "v_add_u32 v100, 5120, v15\n\t"
        "v_add_u32 v101, 6400, v15\n\t"
        "v_add_u32 v102, 7680, v15\n\t"
        "v_add_u32 v103, 8960, v15\n\t"
        "v_add_u32 v112, 0x80, v112\n\t"
        "v_add_u32 v113, 0x80, v113\n\t"
        "v_add_u32 v114, s80, v1\n\t"
        "v_add_u32 v115, 64, v114\n\t"
        "v_cmp_gt_u32 s[36:37], s64, v114\n\t"
        "v_cmp_gt_u32 s[38:39], s64, v115\n\t"
        "s_waitcnt vmcnt(11) lgkmcnt(8)\n\t"
        "s_mov_b64 exec, s[24:25]\n\t"
        "v_mad_u32_u24 v56, v16, s66, v13\n\t"
        "v_mad_u32_u24 v57, v17, s66, v13\n\t"
        "v_mad_u32_u24 v58, v18, s66, v13\n\t"
        "v_mad_u32_u24 v59, v19, s66, v13\n\t"
        "v_lshlrev_b32_sdwa v56, 2, v56 dst_sel:DWORD dst_unused:UNUSED_PAD src0_sel:DWORD src1_sel:BYTE_3\n\t"
        "ds_add_rtn_u32 v76, v56, v14 offset:1024\n\t"
        "v_lshlrev_b32_sdwa v57, 2, v57 dst_sel:DWORD dst_unused:UNUSED_PAD src0_sel:DWORD src1_sel:BYTE_3\n\t"
        "ds_add_rtn_u32 v77, v57, v14 offset:1024\n\t"
        "v_lshlrev_b32_sdwa v58, 2, v58 dst_sel:DWORD dst_unused:UNUSED_PAD src0_sel:DWORD src1_sel:BYTE_3\n\t"
        "ds_add_rtn_u32 v78, v58, v14 offset:1024\n\t"
        "v_lshlrev_b32_sdwa v59, 2, v59 dst_sel:DWORD dst_unused:UNUSED_PAD src0_sel:DWORD src1_sel:BYTE_3\n\t"
        "ds_add_rtn_u32 v79, v59, v14 offset:1024\n\t"
        "s_waitcnt vmcnt(10) lgkmcnt(8)\n\t"
        "s_mov_b64 exec, s[26:27]\n\t"
        "v_mad_u32_u24 v60, v20, s66, v13\n\t"
        "v_mad_u32_u24 v61, v21, s66, v13\n\t"
        "v_mad_u32_u24 v62, v22, s66, v13\n\t"
        "v_mad_u32_u24 v63, v23, s66, v13\n\t"
        "v_lshlrev_b32_sdwa v60, 2, v60 dst_sel:DWORD dst_unused:UNUSED_PAD src0_sel:DWORD src1_sel:BYTE_3\n\t"
        "ds_add_rtn_u32 v80, v60, v14 offset:1024\n\t"
        "v_lshlrev_b32_sdwa v61, 2, v61 dst_sel:DWORD dst_unused:UNUSED_PAD src0_sel:DWORD src1_sel:BYTE_3\n\t"
        "ds_add_rtn_u32 v81, v61, v14 offset:1024\n\t"
        "v_lshlrev_b32_sdwa v62, 2, v62 dst_sel:DWORD dst_unused:UNUSED_PAD src0_sel:DWORD src1_sel:BYTE_3\n\t"
        "ds_add_rtn_u32 v82, v62, v14 offset:1024\n\t"
        "v_lshlrev_b32_sdwa v63, 2, v63 dst_sel:DWORD dst_unused:UNUSED_PAD src0_sel:DWORD src1_sel:BYTE_3\n\t"
        "ds_add_rtn_u32 v83, v63, v14 offset:1024\n\t"
        "s_waitcnt vmcnt(9) lgkmcnt(8)\n\t"
        "s_mov_b64 exec, s[28:29]\n\t"
        "v_mad_u32_u24 v64, v24, s66, v13\n\t"
        "v_mad_u32_u24 v65, v25, s66, v13\n\t"
        "v_mad_u32_u24 v66, v26, s66, v13\n\t"
        "v_mad_u32_u24 v67, v27, s66, v13\n\t"
        "v_lshlrev_b32_sdwa v64, 2, v64 dst_sel:DWORD dst_unused:UNUSED_PAD src0_sel:DWORD src1_sel:BYTE_3\n\t"
        "ds_add_rtn_u32 v84, v64, v14 offset:1024\n\t"
        "v_lshlrev_b32_sdwa v65, 2, v65 dst_sel:DWORD dst_unused:UNUSED_PAD src0_sel:DWORD src1_sel:BYTE_3\n\t"
        "ds_add_rtn_u32 v85, v65, v14 offset:1024\n\t"
        "v_lshlrev_b32_sdwa v66, 2, v66 dst_sel:DWORD dst_unused:UNUSED_PAD src0_sel:DWORD src1_sel:BYTE_3\n\t"
        "ds_add_rtn_u32 v86, v66, v14 offset:1024\n\t"
        "v_lshlrev_b32_sdwa v67, 2, v67 dst_sel:DWORD dst_unused:UNUSED_PAD src0_sel:DWORD src1_sel:BYTE_3\n\t"
        "ds_add_rtn_u32 v87, v67, v14 offset:1024\n\t"
        "s_waitcnt vmcnt(8) lgkmcnt(8)\n\t"
        "s_mov_b64 exec, s[30:31]\n\t"
        "v_mad_u32_u24 v68, v28, s66, v13\n\t"
        "v_mad_u32_u24 v69, v29, s66, v13\n\t"
        "v_mad_u32_u24 v70, v30, s66, v13\n\t"
        "v_mad_u32_u24 v71, v31, s66, v13\n\t"
        "v_lshlrev_b32_sdwa v68, 2, v68 dst_sel:DWORD dst_unused:UNUSED_PAD src0_sel:DWORD src1_sel:BYTE_3\n\t"
        "ds_add_rtn_u32 v88, v68, v14 offset:1024\n\t"
        "v_lshlrev_b32_sdwa v69, 2, v69 dst_sel:DWORD dst_unused:UNUSED_PAD src0_sel:DWORD src1_sel:BYTE_3\n\t"
        "ds_add_rtn_u32 v89, v69, v14 offset:1024\n\t"
        "v_lshlrev_b32_sdwa v70, 2, v70 dst_sel:DWORD dst_unused:UNUSED_PAD src0_sel:DWORD src1_sel:BYTE_3\n\t"
        "ds_add_rtn_u32 v90, v70, v14 offset:1024\n\t"
        "v_lshlrev_b32_sdwa v71, 2, v71 dst_sel:DWORD dst_unused:UNUSED_PAD src0_sel:DWORD src1_sel:BYTE_3\n\t"
        "ds_add_rtn_u32 v91, v71, v14 offset:1024\n\t"
        "s_waitcnt vmcnt(7) lgkmcnt(8)\n\t"
        "s_mov_b64 exec, s[32:33]\n\t"
        "v_mad_u32_u24 v72, v32, s66, v13\n\t"
        "v_mad_u32_u24 v73, v33, s66, v13\n\t"
        "v_mad_u32_u24 v74, v34, s66, v13\n\t"
        "v_mad_u32_u24 v75, v35, s66, v13\n\t"
        "v_lshlrev_b32_sdwa v72, 2, v72 dst_sel:DWORD dst_unused:UNUSED_PAD src0_sel:DWORD src1_sel:BYTE_3\n\t"
        "ds_add_rtn_u32 v92, v72, v14 offset:1024\n\t"
        "v_lshlrev_b32_sdwa v73, 2, v73 dst_sel:DWORD dst_unused:UNUSED_PAD src0_sel:DWORD src1_sel:BYTE_3\n\t"
        "ds_add_rtn_u32 v93, v73, v14 offset:1024\n\t"
        "v_lshlrev_b32_sdwa v74, 2, v74 dst_sel:DWORD dst_unused:UNUSED_PAD src0_sel:DWORD src1_sel:BYTE_3\n\t"
        "ds_add_rtn_u32 v94, v74, v14 offset:1024\n\t"
        "v_lshlrev_b32_sdwa v75, 2, v75 dst_sel:DWORD dst_unused:UNUSED_PAD src0_sel:DWORD src1_sel:BYTE_3\n\t"
        "ds_add_rtn_u32 v95, v75, v14 offset:1024\n\t"
        "s_mov_b64 exec, -1\n\t"
        "ds_read_b32 v4, v7 offset:1028\n\t"
        "ds_read_b32 v5, v7 offset:1284\n\t"
        "s_waitcnt vmcnt(5) lgkmcnt(12)\n\t"
        "v_xor_b32 v116, v116, v114\n\t"
        "v_bitop3_b32 v116, v117, v116, v114 bitop3:0xde\n\t"
        "v_bitop3_b32 v116, v118, v116, v114 bitop3:0xde\n\t"
        "v_bitop3_b32 v116, v119, v116, v114 bitop3:0xde\n\t"
        "v_xor_b32 v120, v120, v115\n\t"
        "v_bitop3_b32 v120, v121, v120, v115 bitop3:0xde\n\t"
        "v_bitop3_b32 v120, v122, v120, v115 bitop3:0xde\n\t"
        "v_bitop3_b32 v120, v123, v120, v115 bitop3:0xde\n\t"
        "v_cmp_ne_u32 s[40:41], 0, v116\n\t"
        "v_cmp_ne_u32 s[42:43], 0, v120\n\t"
        "s_and_b64 s[38:39], s[38:39], 0xffff\n\t"
        "s_and_b64 s[36:37], s[36:37], s[40:41]\n\t"
        "s_and_b64 s[38:39], s[38:39], s[42:43]\n\t"
        "s_or_b64 s[36:37], s[36:37], s[38:39]\n\t"
        "s_cselect_b32 s67, 1, s67\n"
        ".L7_w38:\n\t"
        "ds_read_b32 v0, v2 offset:104\n\t"
        "ds_read_b32 v6, v2 offset:76\n\t"
        "s_waitcnt lgkmcnt(1)\n\t"
        "v_cmp_gt_u16 vcc, 1, v0\n\t"
        "s_cbranch_vccz .L7_d39\n\t"
        "s_waitcnt lgkmcnt(0)\n\t"
        "s_sleep 0\n\t"
        "s_sub_i32 s83, s83, 1\n\t"
        "s_cmp_gt_i32 s83, 0\n\t"
        "s_cbranch_scc1 .L7_w38\n"
        ".L7_d39:\n\t"
        "v_cmp_gt_u32 s[34:35], v4, v112\n\t"
        "v_cmp_gt_u32 s[36:37], v5, v113\n\t"
        "s_or_b64 s[38:39], s[34:35], s[36:37]\n\t"
        "s_cbranch_scc1 .L7_ov\n"
        ".L7_ov1:\n\t"
        "s_waitcnt lgkmcnt(0)\n\t"
        "v_readfirstlane_b32 s68, v6\n\t"
        "s_waitcnt vmcnt(4) lgkmcnt(8)\n\t"
        "s_mov_b64 exec, s[24:25]\n\t"
        "v_lshl_add_u32 v16, v16, 16, v36\n\t"
        "v_lshl_add_u32 v17, v17, 16, v37\n\t"
        "v_lshl_add_u32 v18, v18, 16, v38\n\t"
        "v_lshl_add_u32 v19, v19, 16, v39\n\t"
        "v_mad_i32_i24 v16, v56, s54, v16\n\t"
        "ds_add_u32 v76, v16\n\t"
        "v_mad_i32_i24 v17, v57, s54, v17\n\t"
        "ds_add_u32 v77, v17\n\t"
        "v_mad_i32_i24 v18, v58, s54, v18\n\t"
        "ds_add_u32 v78, v18\n\t"
        "v_mad_i32_i24 v19, v59, s54, v19\n\t"
        "ds_add_u32 v79, v19\n\t"
        "s_waitcnt vmcnt(3) lgkmcnt(8)\n\t"
        "s_mov_b64 exec, s[26:27]\n\t"
        "v_lshl_add_u32 v20, v20, 16, v40\n\t"
        "v_lshl_add_u32 v21, v21, 16, v41\n\t"
        "v_lshl_add_u32 v22, v22, 16, v42\n\t"
        "v_lshl_add_u32 v23, v23, 16, v43\n\t"
        "v_mad_i32_i24 v20, v60, s54, v20\n\t"
        "ds_add_u32 v80, v20\n\t"
        "v_mad_i32_i24 v21, v61, s54, v21\n\t"
        "ds_add_u32 v81, v21\n\t"
        "v_mad_i32_i24 v22, v62, s54, v22\n\t"
        "ds_add_u32 v82, v22\n\t"
        "v_mad_i32_i24 v23, v63, s54, v23\n\t"
        "ds_add_u32 v83, v23\n\t"
        "s_waitcnt vmcnt(2) lgkmcnt(8)\n\t"
        "s_mov_b64 exec, s[28:29]\n\t"
        "v_lshl_add_u32 v24, v24, 16, v44\n\t"
        "v_lshl_add_u32 v25, v25, 16, v45\n\t"
        "v_lshl_add_u32 v26, v26, 16, v46\n\t"
        "v_lshl_add_u32 v27, v27, 16, v47\n\t"
        "v_mad_i32_i24 v24, v64, s54, v24\n\t"
        "ds_add_u32 v84, v24\n\t"
        "v_mad_i32_i24 v25, v65, s54, v25\n\t"
        "ds_add_u32 v85, v25\n\t"
        "v_mad_i32_i24 v26, v66, s54, v26\n\t"
        "ds_add_u32 v86, v26\n\t"
        "v_mad_i32_i24 v27, v67, s54, v27\n\t"
        "ds_add_u32 v87, v27\n\t"
        "s_waitcnt vmcnt(1) lgkmcnt(8)\n\t"
        "s_mov_b64 exec, s[30:31]\n\t"
        "v_lshl_add_u32 v28, v28, 16, v48\n\t"
        "v_lshl_add_u32 v29, v29, 16, v49\n\t"
        "v_lshl_add_u32 v30, v30, 16, v50\n\t"
        "v_lshl_add_u32 v31, v31, 16, v51\n\t"
        "v_mad_i32_i24 v28, v68, s54, v28\n\t"
        "ds_add_u32 v88, v28\n\t"
        "v_mad_i32_i24 v29, v69, s54, v29\n\t"
        "ds_add_u32 v89, v29\n\t"
        "v_mad_i32_i24 v30, v70, s54, v30\n\t"
        "ds_add_u32 v90, v30\n\t"
        "v_mad_i32_i24 v31, v71, s54, v31\n\t"
        "ds_add_u32 v91, v31\n\t"
        "s_waitcnt vmcnt(0) lgkmcnt(8)\n\t"
        "s_mov_b64 exec, s[32:33]\n\t"
        "v_lshl_add_u32 v32, v32, 16, v52\n\t"
        "v_lshl_add_u32 v33, v33, 16, v53\n\t"
        "v_lshl_add_u32 v34, v34, 16, v54\n\t"
        "v_lshl_add_u32 v35, v35, 16, v55\n\t"
        "v_mad_i32_i24 v32, v72, s54, v32\n\t"
        "ds_add_u32 v92, v32\n\t"
        "v_mad_i32_i24 v33, v73, s54, v33\n\t"
        "ds_add_u32 v93, v33\n\t"
        "v_mad_i32_i24 v34, v74, s54, v34\n\t"
        "ds_add_u32 v94, v34\n\t"
        "v_mad_i32_i24 v35, v75, s54, v35\n\t"
        "ds_add_u32 v95, v35\n\t"
        "s_mov_b64 exec, -1\n\t"
        "s_and_b32 s78, s67, 1\n\t"
        "s_or_b32 s78, s78, s38\n\t"
        "s_or_b32 s78, s78, s39\n\t"
        "s_cbranch_scc1 .L7_mk\n"
        ".L7_mk1:\n\t"
        "s_mov_b64 exec, 1\n\t"
        "ds_add_u32 v2, v3 offset:112\n\t"
        "s_mov_b64 exec, -1\n\t"
        "s_waitcnt lgkmcnt(3)\n\t"
        "ds_read2_b32 v[16:17], v15 offset0:0 offset1:8\n\t"
        "ds_read2_b32 v[18:19], v15 offset0:16 offset1:24\n\t"
        "ds_read2_b32 v[20:21], v97 offset0:0 offset1:8\n\t"
        "ds_read2_b32 v[22:23], v97 offset0:16 offset1:24\n\t"
        "ds_read2_b32 v[24:25], v98 offset0:0 offset1:8\n\t"
        "ds_read2_b32 v[26:27], v98 offset0:16 offset1:24\n\t"
        "ds_read2_b32 v[28:29], v99 offset0:0 offset1:8\n\t"
        "ds_read2_b32 v[30:31], v99 offset0:16 offset1:24\n\t"
        "ds_read2_b32 v[32:33], v100 offset0:0 offset1:8\n\t"
        "ds_read2_b32 v[34:35], v100 offset0:16 offset1:24\n\t"
        "ds_read2_b32 v[36:37], v101 offset0:0 offset1:8\n\t"
        "ds_read2_b32 v[38:39], v101 offset0:16 offset1:24\n\t"
        "s_waitcnt lgkmcnt(10)\n\t"
        "buffer_store_dwordx4 v[16:19], v12, s[20:23], s48 offen offset:0 sc1\n\t"
        "ds_read2_b32 v[40:41], v102 offset0:0 offset1:8\n\t"
        "ds_read2_b32 v[42:43], v102 offset0:16 offset1:24\n\t"
        "s_waitcnt lgkmcnt(10)\n\t"
        "buffer_store_dwordx4 v[20:23], v12, s[20:23], s48 offen offset:1024 sc1\n\t"
        "ds_read2_b32 v[44:45], v103 offset0:0 offset1:8\n\t"
        "ds_read2_b32 v[46:47], v103 offset0:16 offset1:24\n\t"
        "s_mov_b64 exec, 1\n\t"
        "ds_add_u32 v2, v3 offset:72\n\t"
        "s_mov_b64 exec, -1\n\t"
        "s_waitcnt lgkmcnt(11)\n\t"
        "buffer_store_dwordx4 v[24:27], v12, s[20:23], s48 offen offset:2048 sc1\n\t"
        "s_waitcnt lgkmcnt(9)\n\t"
        "buffer_store_dwordx4 v[28:31], v12, s[20:23], s48 offen offset:3072 sc1\n\t"
        "s_waitcnt lgkmcnt(7)\n\t"
        "buffer_store_dwordx4 v[32:35], v12, s[20:23], s49 offen offset:0 sc1\n\t"
        "s_waitcnt lgkmcnt(5)\n\t"
        "buffer_store_dwordx4 v[36:39], v12, s[20:23], s49 offen offset:1024 sc1\n\t"
        "s_waitcnt lgkmcnt(3)\n\t"
        "buffer_store_dwordx4 v[40:43], v12, s[20:23], s49 offen offset:2048 sc1\n\t"
        "s_waitcnt lgkmcnt(1)\n\t"
        "buffer_store_dwordx4 v[44:47], v12, s[20:23], s49 offen offset:3072 sc1\n\t"
        "s_mov_b64 exec, 1\n\t"
        "ds_add_u32 v2, v3 offset:44\n\t"
        "s_mov_b64 exec, -1\n\t"
        "s_branch .L7_Q\n"
        ".L7_ov:\n\t"
        "s_mov_b32 s78, 68828\n\t"
        "v_mov_b32 v9, 40\n\t"
        "v_mad_u32_u24 v8, v56, v9, s78\n\t"
        "v_min_u32 v76, v76, v8\n\t"
        "v_mad_u32_u24 v8, v57, v9, s78\n\t"
        "v_min_u32 v77, v77, v8\n\t"
        "v_mad_u32_u24 v8, v58, v9, s78\n\t"
        "v_min_u32 v78, v78, v8\n\t"
        "v_mad_u32_u24 v8, v59, v9, s78\n\t"
        "v_min_u32 v79, v79, v8\n\t"
        "v_mad_u32_u24 v8, v60, v9, s78\n\t"
        "v_min_u32 v80, v80, v8\n\t"
        "v_mad_u32_u24 v8, v61, v9, s78\n\t"
        "v_min_u32 v81, v81, v8\n\t"
        "v_mad_u32_u24 v8, v62, v9, s78\n\t"
        "v_min_u32 v82, v82, v8\n\t"
        "v_mad_u32_u24 v8, v63, v9, s78\n\t"
        "v_min_u32 v83, v83, v8\n\t"
        "v_mad_u32_u24 v8, v64, v9, s78\n\t"
        "v_min_u32 v84, v84, v8\n\t"
        "v_mad_u32_u24 v8, v65, v9, s78\n\t"
        "v_min_u32 v85, v85, v8\n\t"
        "v_mad_u32_u24 v8, v66, v9, s78\n\t"
        "v_min_u32 v86, v86, v8\n\t"
        "v_mad_u32_u24 v8, v67, v9, s78\n\t"
        "v_min_u32 v87, v87, v8\n\t"
        "v_mad_u32_u24 v8, v68, v9, s78\n\t"
        "v_min_u32 v88, v88, v8\n\t"
        "v_mad_u32_u24 v8, v69, v9, s78\n\t"
        "v_min_u32 v89, v89, v8\n\t"
        "v_mad_u32_u24 v8, v70, v9, s78\n\t"
        "v_min_u32 v90, v90, v8\n\t"
        "v_mad_u32_u24 v8, v71, v9, s78\n\t"
        "v_min_u32 v91, v91, v8\n\t"
        "v_mad_u32_u24 v8, v72, v9, s78\n\t"
        "v_min_u32 v92, v92, v8\n\t"
        "v_mad_u32_u24 v8, v73, v9, s78\n\t"
        "v_min_u32 v93, v93, v8\n\t"
        "v_mad_u32_u24 v8, v74, v9, s78\n\t"
        "v_min_u32 v94, v94, v8\n\t"
        "v_mad_u32_u24 v8, v75, v9, s78\n\t"
        "v_min_u32 v95, v95, v8\n\t"
        "s_branch .L7_ov1\n"
        ".L7_mk:\n\t"
        "s_mul_i32 s78, s68, 0x4000400\n\t"
        "s_or_b32 s78, s78, 0xffffff\n\t"
        "v_mov_b32 v6, s78\n\t"
        "v_add_u32 v112, -4, v112\n\t"
        "v_add_u32 v113, -4, v113\n\t"
        "s_mov_b64 exec, s[34:35]\n\t"
        "ds_write_b32 v112, v6\n\t"
        "s_mov_b64 exec, s[36:37]\n\t"
        "ds_write_b32 v113, v6\n\t"
        "s_mov_b64 exec, -1\n\t"
        "s_and_b32 s78, s67, 1\n\t"
        "s_lshl_b32 s78, s78, 24\n\t"
        "v_mov_b32 v6, s78\n\t"
        "ds_xor_b32 v112, v6\n\t"
        "ds_xor_b32 v113, v6\n\t"
        "s_branch .L7_mk1\n"
        ".L7_P2:\n\t"
        "s_setprio 3\n\t"
        "v_lshrrev_b32 v15, 3, v1\n\t"
        "v_mul_u32_u24 v15, 0xa0, v15\n\t"
        "v_add_u32 v15, 68864, v15\n\t"
        "v_and_b32 v4, 7, v1\n\t"
        "v_lshl_add_u32 v5, v4, 4, v15\n\t"
        "v_lshl_add_u32 v15, v4, 2, v15\n\t"
        "s_mov_b32 s34, 0x0c0c0c0c\n\t"
        "s_mov_b32 s35, s34\n\t"
        "s_lshl_b64 s[36:37], s[34:35], 2\n\t"
        "s_lshl_b64 s[38:39], s[34:35], 4\n\t"
        "ds_read_b32 v6, v2 offset:1984\n\t"
        "s_waitcnt lgkmcnt(0)\n\t"
        "s_add_u32 s68, s68, 1\n\t"
        "v_mov_b32 v4, s68\n\t"
        "ds_write_b32 v2, v4 offset:76\n\t"
        "s_cmp_eq_u32 s70, 0\n\t"
        "s_cbranch_scc0 .L7_P3\n\t"
        "s_lshl_b32 s78, s68, 24\n\t"
        "v_mov_b32 v8, s78\n\t"
        "s_xor_b32 s78, s78, 0x5a000000\n\t"
        "v_mov_b32 v9, s78\n\t"
        "v_cndmask_b32 v8, v8, v9, s[34:35]\n\t"
        "s_mul_i32 s78, s68, 0x01010000\n\t"
        "v_mov_b32 v9, s78\n\t"
        "v_cndmask_b32 v8, v8, v9, s[36:37]\n\t"
        "s_mul_i32 s55, s68, 0x4000400\n\t"
        "v_xor_b32 v9, s55, v6\n\t"
        "v_cndmask_b32 v8, v8, v9, s[38:39]\n\t"
        "v_and_b32 v8, 0xff000000, v8\n\t"
        "v_or_b32 v8, 0xff0000, v8\n\t"
        "v_mov_b32 v9, v8\n\t"
        "v_mov_b64 v[10:11], v[8:9]\n\t"
        "ds_write_b128 v5, v[8:11] offset:0\n\t"
        "ds_write_b128 v5, v[8:11] offset:1280\n\t"
        "ds_write_b128 v5, v[8:11] offset:2560\n\t"
        "ds_write_b128 v5, v[8:11] offset:3840\n\t"
        "ds_write_b128 v5, v[8:11] offset:5120\n\t"
        "ds_write_b128 v5, v[8:11] offset:6400\n\t"
        "ds_write_b128 v5, v[8:11] offset:7680\n\t"
        "ds_write_b128 v5, v[8:11] offset:8960\n\t"
        "ds_write_b128 v5, v[8:11] offset:10240\n\t"
        "ds_write_b128 v5, v[8:11] offset:11520\n\t"
        "ds_write_b128 v5, v[8:11] offset:12800\n\t"
        "ds_write_b128 v5, v[8:11] offset:14080\n\t"
        "s_waitcnt lgkmcnt(6)\n\t"
        "ds_write_b128 v5, v[8:11] offset:15360\n\t"
        "ds_write_b128 v5, v[8:11] offset:16640\n\t"
        "ds_write_b128 v5, v[8:11] offset:17920\n\t"
        "ds_write_b128 v5, v[8:11] offset:19200\n"
        ".L7_P3:\n\t"
        "s_mov_b64 exec, 1\n\t"
        "ds_add_u32 v2, v3 offset:104\n\t"
        "s_mov_b64 exec, -1\n\t"
        "v_lshlrev_b32 v12, 4, v1\n\t"
        "s_cmp_eq_u32 s70, 0\n\t"
        "s_cbranch_scc0 .L7_Q\n\t"
        "v_add_u32 v15, 10240, v15\n\t"
        "v_add_u32 v97, 1280, v15\n\t"
        "v_add_u32 v98, 2560, v15\n\t"
        "v_add_u32 v99, 3840, v15\n\t"
        "v_add_u32 v100, 5120, v15\n\t"
        "v_add_u32 v101, 6400, v15\n\t"
        "v_add_u32 v102, 7680, v15\n\t"
        "v_add_u32 v103, 8960, v15\n\t"
        "s_lshl_b32 s48, s71, 14\n\t"
        "s_add_u32 s48, s48, 1056768\n\t"
        "s_add_u32 s49, s48, 4096\n"
        ".L7_w44:\n\t"
        "ds_read_b32 v0, v2 offset:112\n\t"
        "s_waitcnt lgkmcnt(0)\n\t"
        "v_cmp_gt_u16 vcc, 1, v0\n\t"
        "s_cbranch_vccz .L7_d45\n\t"
        "s_sleep 0\n\t"
        "s_sub_i32 s83, s83, 1\n\t"
        "s_cmp_gt_i32 s83, 0\n\t"
        "s_cbranch_scc1 .L7_w44\n"
        ".L7_d45:\n\t"
        "ds_read2_b32 v[16:17], v15 offset0:0 offset1:8\n\t"
        "ds_read2_b32 v[18:19], v15 offset0:16 offset1:24\n\t"
        "ds_read2_b32 v[20:21], v97 offset0:0 offset1:8\n\t"
        "ds_read2_b32 v[22:23], v97 offset0:16 offset1:24\n\t"
        "ds_read2_b32 v[24:25], v98 offset0:0 offset1:8\n\t"
        "ds_read2_b32 v[26:27], v98 offset0:16 offset1:24\n\t"
        "ds_read2_b32 v[28:29], v99 offset0:0 offset1:8\n\t"
        "ds_read2_b32 v[30:31], v99 offset0:16 offset1:24\n\t"
        "ds_read2_b32 v[32:33], v100 offset0:0 offset1:8\n\t"
        "ds_read2_b32 v[34:35], v100 offset0:16 offset1:24\n\t"
        "ds_read2_b32 v[36:37], v101 offset0:0 offset1:8\n\t"
        "ds_read2_b32 v[38:39], v101 offset0:16 offset1:24\n\t"
        "s_waitcnt lgkmcnt(10)\n\t"
        "buffer_store_dwordx4 v[16:19], v12, s[20:23], s48 offen offset:0 sc1\n\t"
        "ds_read2_b32 v[40:41], v102 offset0:0 offset1:8\n\t"
        "ds_read2_b32 v[42:43], v102 offset0:16 offset1:24\n\t"
        "s_waitcnt lgkmcnt(10)\n\t"
        "buffer_store_dwordx4 v[20:23], v12, s[20:23], s48 offen offset:1024 sc1\n\t"
        "ds_read2_b32 v[44:45], v103 offset0:0 offset1:8\n\t"
        "ds_read2_b32 v[46:47], v103 offset0:16 offset1:24\n\t"
        "s_mov_b64 exec, 1\n\t"
        "ds_add_u32 v2, v3 offset:72\n\t"
        "s_mov_b64 exec, -1\n\t"
        "s_waitcnt lgkmcnt(11)\n\t"
        "buffer_store_dwordx4 v[24:27], v12, s[20:23], s48 offen offset:2048 sc1\n\t"
        "s_waitcnt lgkmcnt(9)\n\t"
        "buffer_store_dwordx4 v[28:31], v12, s[20:23], s48 offen offset:3072 sc1\n\t"
        "s_waitcnt lgkmcnt(7)\n\t"
        "buffer_store_dwordx4 v[32:35], v12, s[20:23], s49 offen offset:0 sc1\n\t"
        "s_waitcnt lgkmcnt(5)\n\t"
        "buffer_store_dwordx4 v[36:39], v12, s[20:23], s49 offen offset:1024 sc1\n\t"
        "s_waitcnt lgkmcnt(3)\n\t"
        "buffer_store_dwordx4 v[40:43], v12, s[20:23], s49 offen offset:2048 sc1\n\t"
        "s_waitcnt lgkmcnt(1)\n\t"
        "buffer_store_dwordx4 v[44:47], v12, s[20:23], s49 offen offset:3072 sc1\n\t"
        "s_mov_b64 exec, 1\n\t"
        "ds_add_u32 v2, v3 offset:44\n\t"
        "s_mov_b64 exec, -1\n\t"
        "s_branch .L7_Q\n"
        ".L7_V2:\n\t"
        "v_and_b32 v5, 7, v1\n\t"
        "v_mul_u32_u24 v5, 0x420, v5\n\t"
        "v_and_b32 v6, 8, v1\n\t"
        "v_lshl_add_u32 v5, v6, 6, v5\n\t"
        "v_and_b32 v6, 48, v1\n\t"
        "v_add_u32 v5, v5, v6\n\t"
        "v_add_u32 v5, 68864, v5\n\t"
        "s_lshr_b32 s78, s81, 1\n\t"
        "s_lshl3_add_u32 s78, s78, s80\n\t"
        "s_and_b32 s79, s81, 1\n\t"
        "s_lshl1_add_u32 s78, s78, s79\n\t"
        "s_lshl_b32 s82, s78, 10\n\t"
        "s_add_u32 s82, s82, 131072\n"
        ".L7_w40:\n\t"
        "ds_read_b32 v0, v2 offset:104\n\t"
        "ds_read_b32 v6, v2 offset:76\n\t"
        "s_waitcnt lgkmcnt(1)\n\t"
        "v_cmp_gt_u16 vcc, 1, v0\n\t"
        "s_cbranch_vccz .L7_d41\n\t"
        "s_waitcnt lgkmcnt(0)\n\t"
        "s_sleep 1\n\t"
        "s_sub_i32 s83, s83, 1\n\t"
        "s_cmp_gt_i32 s83, 0\n\t"
        "s_cbranch_scc1 .L7_w40\n"
        ".L7_d41:\n\t"
        "s_waitcnt lgkmcnt(0)\n\t"
        "v_readfirstlane_b32 s68, v6\n\t"
        "v_mov_b32 v7, 2\n\t"
        "s_and_b32 s73, s68, 1\n\t"
        "s_cselect_b32 s73, 1, 0x10000\n\t"
        "v_mov_b32 v16, s73\n\t"
        "v_and_b32 v17, 7, v1\n\t"
        "v_add_u32 v17, 1, v17\n\t"
        "v_lshrrev_b32 v17, v17, s68\n\t"
        "s_mov_b32 s49, 0x10001\n\t"
        "v_and_b32 v17, s49, v17\n\t"
        "s_mov_b32 s48, 0x3db8aa3b\n\t"
        "s_waitcnt vmcnt(0)\n\t"
        "ds_read_b128 v[32:35], v5 offset:0\n\t"
        "ds_read_b128 v[36:39], v5 offset:8448\n\t"
        "ds_read_b128 v[40:43], v5 offset:16896\n\t"
        "ds_read_b128 v[44:47], v5 offset:64\n\t"
        "ds_read_b128 v[48:51], v5 offset:8512\n\t"
        "ds_read_b128 v[52:55], v5 offset:16960\n\t"
        "ds_read_b128 v[56:59], v5 offset:128\n\t"
        "ds_read_b128 v[60:63], v5 offset:8576\n\t"
        "ds_read_b128 v[64:67], v5 offset:17024\n\t"
        "ds_read_b128 v[68:71], v5 offset:192\n\t"
        "ds_read_b128 v[72:75], v5 offset:8640\n\t"
        "ds_read_b128 v[76:79], v5 offset:17088\n\t"
        "s_waitcnt lgkmcnt(9)\n\t"
        "v_mfma_f32_16x16x32_bf16 v[20:23], v[36:39], v[32:35], 0\n\t"
        "v_mfma_f32_16x16x32_bf16 v[24:27], v[40:43], v[32:35], 0\n\t"
        "ds_read_b128 v[80:83], v5 offset:256\n\t"
        "ds_read_b128 v[84:87], v5 offset:8704\n\t"
        "ds_read_b128 v[88:91], v5 offset:17152\n\t"
        "s_waitcnt lgkmcnt(9)\n\t"
        "v_mfma_f32_16x16x32_bf16 v[20:23], v[48:51], v[44:47], v[20:23]\n\t"
        "v_mfma_f32_16x16x32_bf16 v[24:27], v[52:55], v[44:47], v[24:27]\n\t"
        "ds_read_b128 v[92:95], v5 offset:320\n\t"
        "ds_read_b128 v[96:99], v5 offset:8768\n\t"
        "ds_read_b128 v[100:103], v5 offset:17216\n\t"
        "s_waitcnt lgkmcnt(9)\n\t"
        "v_mfma_f32_16x16x32_bf16 v[20:23], v[60:63], v[56:59], v[20:23]\n\t"
        "v_mfma_f32_16x16x32_bf16 v[24:27], v[64:67], v[56:59], v[24:27]\n\t"
        "ds_read_b128 v[104:107], v5 offset:384\n\t"
        "ds_read_b128 v[108:111], v5 offset:8832\n\t"
        "ds_read_b128 v[112:115], v5 offset:17280\n\t"
        "s_waitcnt lgkmcnt(9)\n\t"
        "v_mfma_f32_16x16x32_bf16 v[20:23], v[72:75], v[68:71], v[20:23]\n\t"
        "v_mfma_f32_16x16x32_bf16 v[24:27], v[76:79], v[68:71], v[24:27]\n\t"
        "ds_read_b128 v[116:119], v5 offset:448\n\t"
        "ds_read_b128 v[120:123], v5 offset:8896\n\t"
        "ds_read_b128 v[124:127], v5 offset:17344\n\t"
        "s_waitcnt lgkmcnt(9)\n\t"
        "v_mfma_f32_16x16x32_bf16 v[20:23], v[84:87], v[80:83], v[20:23]\n\t"
        "v_mfma_f32_16x16x32_bf16 v[24:27], v[88:91], v[80:83], v[24:27]\n\t"
        "s_mov_b64 exec, 1\n\t"
        "ds_add_u32 v2, v7 offset:72\n\t"
        "s_mov_b64 exec, -1\n\t"
        "s_waitcnt lgkmcnt(7)\n\t"
        "v_mfma_f32_16x16x32_bf16 v[20:23], v[96:99], v[92:95], v[20:23]\n\t"
        "v_mfma_f32_16x16x32_bf16 v[24:27], v[100:103], v[92:95], v[24:27]\n\t"
        "s_waitcnt lgkmcnt(4)\n\t"
        "v_mfma_f32_16x16x32_bf16 v[20:23], v[108:111], v[104:107], v[20:23]\n\t"
        "v_mfma_f32_16x16x32_bf16 v[24:27], v[112:115], v[104:107], v[24:27]\n\t"
        "s_waitcnt lgkmcnt(1)\n\t"
        "v_mfma_f32_16x16x32_bf16 v[20:23], v[120:123], v[116:119], v[20:23]\n\t"
        "v_mfma_f32_16x16x32_bf16 v[24:27], v[124:127], v[116:119], v[24:27]\n\t"
        "s_nop 6\n\t"
        "v_mul_f32 v20, s48, v20\n\t"
        "v_mul_f32 v21, s48, v21\n\t"
        "v_mul_f32 v22, s48, v22\n\t"
        "v_mul_f32 v23, s48, v23\n\t"
        "v_mul_f32 v24, s48, v24\n\t"
        "v_mul_f32 v25, s48, v25\n\t"
        "v_mul_f32 v26, s48, v26\n\t"
        "v_mul_f32 v27, s48, v27\n\t"
        "s_mov_b32 s48, 0x07060302\n\t"
        "v_add_u32 v20, 0x10000, v20\n\t"
        "v_add_u32 v21, 0x10000, v21\n\t"
        "v_add_u32 v22, 0x10000, v22\n\t"
        "v_add_u32 v23, 0x10000, v23\n\t"
        "v_perm_b32 v20, v21, v20, s48\n\t"
        "v_perm_b32 v21, v23, v22, s48\n\t"
        "v_cvt_pk_bf16_f32 v22, v24, v25\n\t"
        "v_cvt_pk_bf16_f32 v23, v26, v27\n\t"
        "v_bfi_b32 v20, s49, v16, v20\n\t"
        "v_bfi_b32 v21, s49, v17, v21\n\t"
        "buffer_store_dwordx4 v[20:23], v12, s[20:23], s82 offen sc1\n\t"
        "s_mov_b64 exec, 1\n\t"
        "ds_add_u32 v2, v7 offset:44\n\t"
        "s_mov_b64 exec, -1\n\t"
        "s_branch .L7_Q\n"
        ".L7_T2:\n\t"
        "v_lshlrev_b32 v4, 4, v1\n\t"
        "v_add_u32 v4, 148160, v4\n\t"
        "v_mul_u32_u24 v5, 196, v1\n\t"
        "v_add_u32 v5, 148160, v5\n\t"
        "v_mov_b32 v12, 0xffff\n\t"
        "v_mov_b32 v13, v12\n\t"
        "v_mov_b64 v[14:15], v[12:13]\n\t"
        "ds_write_b128 v4, v[12:15] offset:0\n\t"
        "ds_write_b128 v4, v[12:15] offset:1024\n\t"
        "ds_write_b128 v4, v[12:15] offset:2048\n\t"
        "ds_write_b128 v4, v[12:15] offset:3072\n\t"
        "ds_write_b128 v4, v[12:15] offset:4096\n\t"
        "ds_write_b128 v4, v[12:15] offset:5120\n\t"
        "ds_write_b128 v4, v[12:15] offset:6144\n\t"
        "ds_write_b128 v4, v[12:15] offset:7168\n\t"
        "ds_write_b128 v4, v[12:15] offset:8192\n\t"
        "ds_write_b128 v4, v[12:15] offset:9216\n\t"
        "ds_write_b128 v4, v[12:15] offset:10240\n\t"
        "ds_write_b128 v4, v[12:15] offset:11264\n\t"
        "s_waitcnt lgkmcnt(6)\n\t"
        "ds_write_b128 v4, v[12:15] offset:12288\n\t"
        "ds_write_b128 v4, v[12:15] offset:13312\n\t"
        "ds_write_b128 v4, v[12:15] offset:14336\n\t"
        "ds_write_b128 v4, v[12:15] offset:15360\n\t"
        "ds_write_b32 v5, v2 offset:192\n\t"
        "s_mov_b64 exec, 0xffff\n\t"
        "ds_write_b32 v5, v2 offset:12736\n\t"
        "s_mov_b64 exec, 1\n\t"
        "ds_add_u32 v2, v3 offset:8\n\t"
        "s_mov_b64 exec, -1\n\t"
        "s_and_b32 s82, s82, -1024\n\t"
        "v_mov_b32 v9, 4\n"
        ".L7_g42:\n\t"
        "s_memrealtime s[78:79]\n\t"
        "s_waitcnt lgkmcnt(0)\n\t"
        "s_sub_u32 s78, s78, s74\n\t"
        "s_cmp_ge_i32 s78, 150\n\t"
        "s_cbranch_scc1 .L7_h43\n\t"
        "s_sleep 1\n\t"
        "s_branch .L7_g42\n"
        ".L7_h43:\n"
        ".L7_b1:\n\t"
        "s_buffer_load_dword s40, s[20:23], s82 offset:0 glc\n\t"
        "s_buffer_load_dword s41, s[20:23], s82 offset:128 glc\n\t"
        "s_buffer_load_dword s42, s[20:23], s82 offset:256 glc\n\t"
        "s_buffer_load_dword s43, s[20:23], s82 offset:384 glc\n\t"
        "s_buffer_load_dword s44, s[20:23], s82 offset:512 glc\n\t"
        "s_buffer_load_dword s45, s[20:23], s82 offset:640 glc\n\t"
        "s_buffer_load_dword s46, s[20:23], s82 offset:768 glc\n\t"
        "s_buffer_load_dword s47, s[20:23], s82 offset:896 glc\n\t"
        "s_waitcnt lgkmcnt(0)\n\t"
        "s_mov_b32 s80, 0\n\t"
        "s_mov_b32 s81, 0\n\t"
        "s_sub_u32 s40, s40, s74\n\t"
        "s_max_i32 s80, s80, s40\n\t"
        "s_min_i32 s81, s81, s40\n\t"
        "s_sub_u32 s41, s41, s74\n\t"
        "s_max_i32 s80, s80, s41\n\t"
        "s_min_i32 s81, s81, s41\n\t"
        "s_sub_u32 s42, s42, s74\n\t"
        "s_max_i32 s80, s80, s42\n\t"
        "s_min_i32 s81, s81, s42\n\t"
        "s_sub_u32 s43, s43, s74\n\t"
        "s_max_i32 s80, s80, s43\n\t"
        "s_min_i32 s81, s81, s43\n\t"
        "s_sub_u32 s44, s44, s74\n\t"
        "s_max_i32 s80, s80, s44\n\t"
        "s_min_i32 s81, s81, s44\n\t"
        "s_sub_u32 s45, s45, s74\n\t"
        "s_max_i32 s80, s80, s45\n\t"
        "s_min_i32 s81, s81, s45\n\t"
        "s_sub_u32 s46, s46, s74\n\t"
        "s_max_i32 s80, s80, s46\n\t"
        "s_min_i32 s81, s81, s46\n\t"
        "s_sub_u32 s47, s47, s74\n\t"
        "s_max_i32 s80, s80, s47\n\t"
        "s_min_i32 s81, s81, s47\n\t"
        "s_sub_u32 s81, s80, s81\n\t"
        "s_cmp_lt_u32 s81, 0x3e8\n\t"
        "s_cbranch_scc1 .L7_b2\n\t"
        "v_mov_b32 v9, 0\n\t"
        "s_mov_b32 s80, 0\n\t"
        "s_sleep 4\n\t"
        "s_memrealtime s[78:79]\n\t"
        "s_waitcnt lgkmcnt(0)\n\t"
        "s_sub_u32 s78, s78, s74\n\t"
        "s_cmp_lt_u32 s78, 0x4b0\n\t"
        "s_cbranch_scc1 .L7_b1\n"
        ".L7_b2:\n\t"
        "s_add_u32 s80, s80, 1\n\t"
        "v_mov_b32 v7, s80\n\t"
        "s_mov_b64 exec, 1\n\t"
        "ds_add_u32 v2, v7 offset:92\n\t"
        "s_mov_b64 exec, -1\n\t"
        "s_branch .L7_Q0\n"
        ".L7_R2:\n\t"
        "s_sub_u32 s79, s64, s72\n\t"
        "s_min_i32 s79, s79, s65\n\t"
        "s_max_i32 s79, s79, 0\n\t"
        "v_mov_b32 v32, v1\n\t"
        "v_add_u32 v33, 64, v1\n\t"
        "v_cmp_gt_u32 s[32:33], s79, v32\n\t"
        "v_cmp_gt_u32 s[34:35], s79, v33\n\t"
        "v_cmp_gt_u32 s[36:37], 40, v1\n\t"
        "s_sub_u32 s78, 0xff, s65\n\t"
        "v_add_u32 v34, s78, v32\n\t"
        "v_add_u32 v6, 0xffffffb0, v34\n\t"
        "v_cmp_gt_u32 vcc, 0xff, v34\n\t"
        "s_nop 1\n\t"
        "v_cndmask_b32 v34, v6, v34, vcc\n\t"
        "v_add_u32 v35, s78, v33\n\t"
        "v_add_u32 v6, 0xffffffb0, v35\n\t"
        "v_cmp_gt_u32 vcc, 0xff, v35\n\t"
        "s_nop 1\n\t"
        "v_cndmask_b32 v35, v6, v35, vcc\n\t"
        "v_mov_b32 v12, 0x8000\n\t"
        "v_cndmask_b32 v44, v12, 0, s[32:33]\n\t"
        "v_cndmask_b32 v45, v12, 0, s[34:35]\n\t"
        "v_cndmask_b32 v46, 63, 31, s[36:37]\n\t"
        "v_mov_b32 v47, 63\n\t"
        "s_waitcnt vmcnt(0)\n\t"
        "v_lshlrev_b32 v16, 16, v16\n\t"
        "v_lshlrev_b32 v17, 16, v17\n\t"
        "v_mul_f32 v36, v16, v16\n\t"
        "v_mul_f32 v37, v17, v17\n\t"
        "v_rcp_f32 v36, v36\n\t"
        "v_rcp_f32 v37, v37\n\t"
        "v_or3_b32 v16, v16, v34, v44\n\t"
        "v_or3_b32 v17, v17, v35, v45\n\t"
        "v_add_f32 v36, 0xc0600000, v36\n\t"
        "v_add_f32 v37, 0xc0600000, v37\n\t"
        "v_cvt_u32_f32 v36, v36\n\t"
        "v_cvt_u32_f32 v37, v37\n\t"
        "v_min_u32 v36, 31, v36\n\t"
        "v_min_u32 v37, 31, v37\n\t"
        "v_cndmask_b32 v36, 0, v36, s[32:33]\n\t"
        "v_cndmask_b32 v37, 0, v37, s[34:35]\n\t"
        "v_sub_u32 v36, v46, v36\n\t"
        "v_sub_u32 v37, v47, v37\n\t"
        "v_lshlrev_b32 v36, 2, v36\n\t"
        "v_lshlrev_b32 v37, 2, v37\n\t"
        "ds_add_rtn_u32 v40, v36, v3 offset:256\n\t"
        "s_mov_b64 exec, 0xffff\n\t"
        "ds_add_rtn_u32 v41, v37, v3 offset:256\n\t"
        "s_mov_b64 exec, -1\n\t"
        "v_lshlrev_b32 v9, 2, v1\n\t"
        "ds_read_b32 v10, v9 offset:256\n\t"
        "s_cmp_eq_u32 s70, 0\n\t"
        "s_cselect_b32 s78, 56, 0\n\t"
        "s_cselect_b32 s79, 0, 56\n\t"
        "v_mov_b32 v49, -40\n\t"
        "v_cndmask_b32 v48, v49, 0, s[36:37]\n\t"
        "s_mov_b32 s40, 0x23108954\n\t"
        "s_mov_b32 s41, 0x67\n\t"
        "s_mov_b32 s42, 0x1986732\n\t"
        "s_mov_b32 s43, 0x45\n\t"
        "v_mov_b32 v13, s78\n\t"
        "v_mov_b32 v14, s79\n\t"
        "v_cndmask_b32 v13, v14, v13, s[36:37]\n\t"
        "s_waitcnt lgkmcnt(0)\n\t"
        "v_mov_b32 v11, v10\n\t"
        "s_nop 1\n\t"
        "v_add_u32_dpp v11, v11, v11 row_shr:1 row_mask:0xf bank_mask:0xf bound_ctrl:1\n\t"
        "s_nop 1\n\t"
        "v_add_u32_dpp v11, v11, v11 row_shr:2 row_mask:0xf bank_mask:0xf bound_ctrl:1\n\t"
        "s_nop 1\n\t"
        "v_add_u32_dpp v11, v11, v11 row_shr:4 row_mask:0xf bank_mask:0xf bound_ctrl:1\n\t"
        "s_nop 1\n\t"
        "v_add_u32_dpp v11, v11, v11 row_shr:8 row_mask:0xf bank_mask:0xf bound_ctrl:1\n\t"
        "s_nop 1\n\t"
        "v_add_u32_dpp v11, v11, v11 row_bcast:15 row_mask:0xa bank_mask:0xf\n\t"
        "s_nop 1\n\t"
        "v_add_u32_dpp v11, v11, v11 row_bcast:31 row_mask:0xc bank_mask:0xf\n\t"
        "v_sub_u32 v11, v11, v10\n\t"
        "ds_bpermute_b32 v36, v36, v11\n\t"
        "ds_bpermute_b32 v37, v37, v11\n\t"
        "v_lshlrev_b32 v9, 1, v1\n\t"
        "s_waitcnt lgkmcnt(0)\n\t"
        "v_add3_u32 v36, v36, v40, v48\n\t"
        "v_and_b32 v6, -4, v36\n\t"
        "v_lshrrev_b64 v[10:11], v6, s[40:41]\n\t"
        "v_lshrrev_b64 v[50:51], v6, s[42:43]\n\t"
        "v_cmp_eq_u32 vcc, 0, v13\n\t"
        "s_nop 1\n\t"
        "v_cndmask_b32 v10, v10, v50, vcc\n\t"
        "v_and_b32 v10, 15, v10\n\t"
        "v_and_b32 v6, 3, v36\n\t"
        "v_lshl_add_u32 v6, v10, 2, v6\n\t"
        "v_add_u32 v36, v6, v13\n\t"
        "v_and_b32 v6, 15, v36\n\t"
        "v_lshlrev_b32 v6, 4, v6\n\t"
        "v_lshl_add_u32 v6, v36, 8, v6\n\t"
        "v_and_b32 v10, 8, v13\n\t"
        "v_lshlrev_b32 v10, 8, v10\n\t"
        "v_sub_u32 v6, v6, v10\n\t"
        "v_lshlrev_b32 v36, 2, v36\n\t"
        "ds_write_b32 v36, v16 offset:512\n\t"
        "ds_write_b16 v9, v6 offset:1792\n\t"
        "v_add3_u32 v37, v37, v41, v49\n\t"
        "v_and_b32 v6, -4, v37\n\t"
        "v_lshrrev_b64 v[10:11], v6, s[40:41]\n\t"
        "v_lshrrev_b64 v[50:51], v6, s[42:43]\n\t"
        "v_cmp_eq_u32 vcc, 0, v14\n\t"
        "s_nop 1\n\t"
        "v_cndmask_b32 v10, v10, v50, vcc\n\t"
        "v_and_b32 v10, 15, v10\n\t"
        "v_and_b32 v6, 3, v37\n\t"
        "v_lshl_add_u32 v6, v10, 2, v6\n\t"
        "v_add_u32 v37, v6, v14\n\t"
        "v_and_b32 v6, 15, v37\n\t"
        "v_lshlrev_b32 v6, 4, v6\n\t"
        "v_lshl_add_u32 v6, v37, 8, v6\n\t"
        "v_and_b32 v10, 8, v14\n\t"
        "v_lshlrev_b32 v10, 8, v10\n\t"
        "v_sub_u32 v6, v6, v10\n\t"
        "v_lshlrev_b32 v37, 2, v37\n\t"
        "s_mov_b64 exec, 0xffff\n\t"
        "ds_write_b32 v37, v17 offset:512\n\t"
        "ds_write_b16 v9, v6 offset:1920\n\t"
        "s_mov_b64 exec, 1\n\t"
        "ds_add_u32 v2, v3 offset:8\n\t"
        "s_mov_b64 exec, -1\n"
        ".L7_w46:\n\t"
        "ds_read_b64 v[4:5], v2 offset:72\n\t"
        "s_waitcnt lgkmcnt(0)\n\t"
        "v_cmp_gt_u16 vcc, 2, v4\n\t"
        "s_cbranch_vccz .L7_d47\n\t"
        "s_sleep 2\n\t"
        "s_sub_i32 s83, s83, 1\n\t"
        "s_cmp_gt_i32 s83, 0\n\t"
        "s_cbranch_scc1 .L7_w46\n"
        ".L7_d47:\n\t"
        "v_readfirstlane_b32 s68, v5\n\t"
        "v_lshlrev_b32 v22, 4, v1\n\t"
        "v_add_u32 v9, 142848, v22\n\t"
        "v_and_b32 v10, 60, v1\n\t"
        "ds_read_b32 v24, v10 offset:704\n\t"
        "ds_read_b32 v25, v10 offset:768\n\t"
        "ds_read_b32 v26, v10 offset:832\n\t"
        "v_and_b32 v11, 15, v1\n\t"
        "v_lshrrev_b32 v12, 4, v1\n\t"
        "v_xor_b32 v16, v11, v12\n\t"
        "v_lshlrev_b32 v16, 4, v16\n\t"
        "v_lshl_add_u32 v16, v11, 8, v16\n\t"
        "v_xor_b32 v17, 64, v16\n\t"
        "v_xor_b32 v18, 128, v16\n\t"
        "v_xor_b32 v19, 192, v16\n\t"
        "ds_write_b128 v9, v[16:19]\n\t"
        "s_and_b32 s73, s68, 1\n\t"
        "s_cselect_b32 s73, 1, 0x10000\n\t"
        "v_mov_b32 v20, s73\n\t"
        "v_and_b32 v21, 7, v1\n\t"
        "v_add_u32 v21, 1, v21\n\t"
        "v_lshrrev_b32 v21, v21, s68\n\t"
        "s_mov_b32 s49, 0x10001\n\t"
        "v_and_b32 v21, s49, v21\n\t"
        "v_and_b32 v23, 0x37f, v22\n\t"
        "ds_write_b128 v9, v[20:23] offset:1024\n\t"
        "s_add_u32 s78, s72, s65\n\t"
        "s_sub_u32 s78, s78, 0xff\n\t"
        "s_lshl_b32 s78, s78, 9\n\t"
        "v_and_b32 v13, 48, v22\n\t"
        "v_add_u32 v14, s78, v13\n\t"
        "s_movk_i32 s78, 0x200\n\t"
        "v_bfe_u32 v27, v1, 1, 2\n\t"
        "v_xor_b32 v27, v27, v12\n\t"
        "v_lshlrev_b32 v27, 4, v27\n\t"
        "v_lshl_add_u32 v27, v11, 6, v27\n\t"
        "v_lshlrev_b32 v28, 1, v1\n\t"
        "v_and_b32 v28, 48, v28\n\t"
        "v_xor_b32 v28, v28, v22\n\t"
        "s_waitcnt lgkmcnt(2)\n\t"
        "v_mad_u32_u16 v24, v24, s78, v14\n\t"
        "v_mad_u32_u16 v25, v25, s78, v14\n\t"
        "v_mad_u32_u16 v26, v26, s78, v14\n\t"
        "s_mov_b32 exec_hi, 0\n\t"
        "v_mov_b32 v24, -2.0\n\t"
        "s_mov_b64 exec, -1\n\t"
        "ds_write_b128 v9, v[24:27] offset:2048\n\t"
        "s_mov_b32 s78, 0x3f80\n\t"
        "s_mov_b32 s79, 0\n\t"
        "v_lshlrev_b64 v[30:31], v13, s[78:79]\n\t"
        "s_mov_b32 s78, 0x00f0000f\n\t"
        "s_mov_b32 s79, 0xf0000f00\n\t"
        "v_cndmask_b32 v30, 0, v30, s[78:79]\n\t"
        "v_cndmask_b32 v31, 0, v31, s[78:79]\n\t"
        "ds_write_b128 v9, v[28:31] offset:3072\n\t"
        "s_branch .L7_Q\n"
        ".L7_Q0:\n"
        ".L7_w48:\n\t"
        "ds_read_b64 v[4:5], v2 offset:72\n\t"
        "s_waitcnt lgkmcnt(0)\n\t"
        "v_cmp_gt_u16 vcc, 2, v4\n\t"
        "s_cbranch_vccz .L7_d49\n\t"
        "s_sleep 2\n\t"
        "s_sub_i32 s83, s83, 1\n\t"
        "s_cmp_gt_i32 s83, 0\n\t"
        "s_cbranch_scc1 .L7_w48\n"
        ".L7_d49:\n\t"
        "v_readfirstlane_b32 s68, v5\n"
        ".L7_Q:\n\t"
        "s_setprio 2\n\t"
        "v_and_b32 v5, 7, v1\n\t"
        "v_mul_u32_u24 v5, 0x420, v5\n\t"
        "v_and_b32 v6, 8, v1\n\t"
        "v_lshl_add_u32 v5, v6, 6, v5\n\t"
        "v_and_b32 v6, 48, v1\n\t"
        "v_add_u32 v5, v5, v6\n\t"
        "s_and_b32 s73, s68, 1\n\t"
        "s_cselect_b32 s73, 1, 0x10000\n\t"
        "v_and_b32 v6, 7, v1\n\t"
        "v_add_u32 v6, 1, v6\n\t"
        "v_lshrrev_b32 v6, v6, s68\n\t"
        "s_mov_b32 s49, 0x10001\n\t"
        "v_and_b32 v6, s49, v6\n\t"
        "s_add_u32 s80, s69, s76\n\t"
        "s_and_b32 s80, s80, 3\n\t"
        "s_lshl2_add_u32 s78, s70, s80\n\t"
        "s_lshl_b32 s78, s78, 14\n\t"
        "s_add_u32 s78, s78, 131072\n\t"
        "v_lshl_add_u32 v4, v1, 4, s78\n\t"
        "s_mov_b32 s24, 4096\n\t"
        "s_mov_b32 s25, 8192\n\t"
        "s_mov_b32 s26, 12288\n"
        ".L7_w50:\n\t"
        "ds_read_b32 v0, v2 offset:92\n\t"
        "s_waitcnt lgkmcnt(0)\n\t"
        "v_cmp_gt_u16 vcc, 1, v0\n\t"
        "s_cbranch_vccz .L7_d51\n\t"
        "s_sleep 1\n\t"
        "s_sub_i32 s83, s83, 1\n\t"
        "s_cmp_gt_i32 s83, 0\n\t"
        "s_cbranch_scc1 .L7_w50\n"
        ".L7_d51:\n\t"
        "v_readfirstlane_b32 s78, v0\n\t"
        "s_sub_u32 s78, s78, 1\n\t"
        "s_add_u32 s77, s74, s78\n"
        ".L7_g52:\n\t"
        "s_memrealtime s[78:79]\n\t"
        "s_waitcnt lgkmcnt(0)\n\t"
        "s_sub_u32 s78, s78, s77\n\t"
        "s_cmp_ge_i32 s78, 280\n\t"
        "s_cbranch_scc1 .L7_h53\n\t"
        "s_sleep 1\n\t"
        "s_branch .L7_g52\n"
        ".L7_h53:\n"
        ".L7_w54:\n\t"
        "ds_read_b32 v0, v2 offset:88\n\t"
        "s_waitcnt lgkmcnt(0)\n\t"
        "v_cmp_gt_u16 vcc, 8, v0\n\t"
        "s_cbranch_vccz .L7_d55\n\t"
        "s_sleep 1\n\t"
        "s_sub_i32 s83, s83, 1\n\t"
        "s_cmp_gt_i32 s83, 0\n\t"
        "s_cbranch_scc1 .L7_w54\n"
        ".L7_d55:\n\t"
        "buffer_load_dwordx4 v[64:67], v4, s[20:23], 0 offen offset:0 sc1\n\t"
        "buffer_load_dwordx4 v[68:71], v4, s[20:23], 0 offen offset:1024 sc1\n\t"
        "buffer_load_dwordx4 v[72:75], v4, s[20:23], 0 offen offset:2048 sc1\n\t"
        "buffer_load_dwordx4 v[76:79], v4, s[20:23], 0 offen offset:3072 sc1\n\t"
        "buffer_load_dwordx4 v[80:83], v4, s[20:23], s24 offen offset:0 sc1\n\t"
        "buffer_load_dwordx4 v[84:87], v4, s[20:23], s24 offen offset:1024 sc1\n\t"
        "buffer_load_dwordx4 v[88:91], v4, s[20:23], s24 offen offset:2048 sc1\n\t"
        "buffer_load_dwordx4 v[92:95], v4, s[20:23], s24 offen offset:3072 sc1\n\t"
        "buffer_load_dwordx4 v[96:99], v4, s[20:23], s25 offen offset:0 sc1\n\t"
        "buffer_load_dwordx4 v[100:103], v4, s[20:23], s25 offen offset:1024 sc1\n\t"
        "buffer_load_dwordx4 v[104:107], v4, s[20:23], s25 offen offset:2048 sc1\n\t"
        "buffer_load_dwordx4 v[108:111], v4, s[20:23], s25 offen offset:3072 sc1\n\t"
        "buffer_load_dwordx4 v[112:115], v4, s[20:23], s26 offen offset:0 sc1\n\t"
        "buffer_load_dwordx4 v[116:119], v4, s[20:23], s26 offen offset:1024 sc1\n\t"
        "buffer_load_dwordx4 v[120:123], v4, s[20:23], s26 offen offset:2048 sc1\n\t"
        "buffer_load_dwordx4 v[124:127], v4, s[20:23], s26 offen offset:3072 sc1\n\t"
        "s_setprio 3\n"
        ".L7_w56:\n\t"
        "ds_read_b32 v0, v2 offset:0\n\t"
        "s_waitcnt lgkmcnt(0)\n\t"
        "v_cmp_gt_u16 vcc, 4, v0\n\t"
        "s_cbranch_vccz .L7_d57\n\t"
        "s_sleep 1\n\t"
        "s_sub_i32 s83, s83, 1\n\t"
        "s_cmp_gt_i32 s83, 0\n\t"
        "s_cbranch_scc1 .L7_w56\n"
        ".L7_d57:\n\t"
        "s_branch .L7_qg\n"
        ".L7_qr:\n\t"
        "s_waitcnt lgkmcnt(0)\n\t"
        "s_sleep 8\n\t"
        "s_sub_i32 s83, s83, 64\n\t"
        "s_cmp_gt_i32 s83, 0\n\t"
        "s_cbranch_scc0 .L7_qd\n\t"
        "buffer_load_dwordx4 v[64:67], v4, s[20:23], 0 offen offset:0 sc1\n\t"
        "buffer_load_dwordx4 v[68:71], v4, s[20:23], 0 offen offset:1024 sc1\n\t"
        "buffer_load_dwordx4 v[72:75], v4, s[20:23], 0 offen offset:2048 sc1\n\t"
        "buffer_load_dwordx4 v[76:79], v4, s[20:23], 0 offen offset:3072 sc1\n\t"
        "buffer_load_dwordx4 v[80:83], v4, s[20:23], s24 offen offset:0 sc1\n\t"
        "buffer_load_dwordx4 v[84:87], v4, s[20:23], s24 offen offset:1024 sc1\n\t"
        "buffer_load_dwordx4 v[88:91], v4, s[20:23], s24 offen offset:2048 sc1\n\t"
        "buffer_load_dwordx4 v[92:95], v4, s[20:23], s24 offen offset:3072 sc1\n\t"
        "buffer_load_dwordx4 v[96:99], v4, s[20:23], s25 offen offset:0 sc1\n\t"
        "buffer_load_dwordx4 v[100:103], v4, s[20:23], s25 offen offset:1024 sc1\n\t"
        "buffer_load_dwordx4 v[104:107], v4, s[20:23], s25 offen offset:2048 sc1\n\t"
        "buffer_load_dwordx4 v[108:111], v4, s[20:23], s25 offen offset:3072 sc1\n\t"
        "buffer_load_dwordx4 v[112:115], v4, s[20:23], s26 offen offset:0 sc1\n\t"
        "buffer_load_dwordx4 v[116:119], v4, s[20:23], s26 offen offset:1024 sc1\n\t"
        "buffer_load_dwordx4 v[120:123], v4, s[20:23], s26 offen offset:2048 sc1\n\t"
        "buffer_load_dwordx4 v[124:127], v4, s[20:23], s26 offen offset:3072 sc1\n"
        ".L7_qg:\n\t"
        "v_mov_b32 v7, 0\n\t"
        "ds_read_b128 v[8:11], v5 offset:2048\n\t"
        "ds_read_b128 v[12:15], v5 offset:10496\n\t"
        "ds_read_b128 v[16:19], v5 offset:18944\n\t"
        "ds_read_b128 v[20:23], v5 offset:27392\n\t"
        "s_waitcnt vmcnt(14) lgkmcnt(3)\n\t"
        "v_xor_b32 v64, s73, v64\n\t"
        "v_xor_b32 v65, v6, v65\n\t"
        "v_xor_b32 v68, s73, v68\n\t"
        "v_xor_b32 v69, v6, v69\n\t"
        "v_mfma_f32_16x16x32_bf16 v[24:27], v[64:67], v[8:11], 0\n\t"
        "s_nop 0\n\t"
        "v_mfma_f32_16x16x32_bf16 v[28:31], v[68:71], v[8:11], 0\n\t"
        "v_or3_b32 v7, v7, v64, v65\n\t"
        "ds_read_b128 v[8:11], v5 offset:35840\n\t"
        "s_waitcnt lgkmcnt(3)\n\t"
        "v_mfma_f32_16x16x32_bf16 v[32:35], v[64:67], v[12:15], 0\n\t"
        "v_mfma_f32_16x16x32_bf16 v[36:39], v[68:71], v[12:15], 0\n\t"
        "v_or3_b32 v7, v7, v68, v69\n\t"
        "ds_read_b128 v[12:15], v5 offset:2112\n\t"
        "s_waitcnt lgkmcnt(3)\n\t"
        "v_mfma_f32_16x16x32_bf16 v[40:43], v[64:67], v[16:19], 0\n\t"
        "v_mfma_f32_16x16x32_bf16 v[44:47], v[68:71], v[16:19], 0\n\t"
        "ds_read_b128 v[16:19], v5 offset:10560\n\t"
        "s_waitcnt lgkmcnt(3)\n\t"
        "v_mfma_f32_16x16x32_bf16 v[48:51], v[64:67], v[20:23], 0\n\t"
        "v_mfma_f32_16x16x32_bf16 v[52:55], v[68:71], v[20:23], 0\n\t"
        "ds_read_b128 v[20:23], v5 offset:19008\n\t"
        "s_waitcnt vmcnt(12) lgkmcnt(3)\n\t"
        "v_xor_b32 v72, s73, v72\n\t"
        "v_xor_b32 v73, v6, v73\n\t"
        "v_xor_b32 v76, s73, v76\n\t"
        "v_xor_b32 v77, v6, v77\n\t"
        "v_mfma_f32_16x16x32_bf16 v[56:59], v[64:67], v[8:11], 0\n\t"
        "v_mfma_f32_16x16x32_bf16 v[60:63], v[68:71], v[8:11], 0\n\t"
        "ds_read_b128 v[8:11], v5 offset:27456\n\t"
        "ds_read_b128 v[64:67], v5 offset:35904\n\t"
        "ds_read_b128 v[68:71], v5 offset:2176\n\t"
        "s_waitcnt lgkmcnt(4)\n\t"
        "v_mfma_f32_16x16x32_bf16 v[24:27], v[72:75], v[12:15], v[24:27]\n\t"
        "v_mfma_f32_16x16x32_bf16 v[28:31], v[76:79], v[12:15], v[28:31]\n\t"
        "v_or3_b32 v7, v7, v72, v73\n\t"
        "ds_read_b128 v[12:15], v5 offset:10624\n\t"
        "v_mfma_f32_16x16x32_bf16 v[32:35], v[72:75], v[16:19], v[32:35]\n\t"
        "v_mfma_f32_16x16x32_bf16 v[36:39], v[76:79], v[16:19], v[36:39]\n\t"
        "v_or3_b32 v7, v7, v76, v77\n\t"
        "ds_read_b128 v[16:19], v5 offset:19072\n\t"
        "s_waitcnt lgkmcnt(4)\n\t"
        "v_mfma_f32_16x16x32_bf16 v[40:43], v[72:75], v[20:23], v[40:43]\n\t"
        "v_mfma_f32_16x16x32_bf16 v[44:47], v[76:79], v[20:23], v[44:47]\n\t"
        "ds_read_b128 v[20:23], v5 offset:27520\n\t"
        "v_mfma_f32_16x16x32_bf16 v[48:51], v[72:75], v[8:11], v[48:51]\n\t"
        "v_mfma_f32_16x16x32_bf16 v[52:55], v[76:79], v[8:11], v[52:55]\n\t"
        "ds_read_b128 v[8:11], v5 offset:35968\n\t"
        "s_waitcnt vmcnt(10) lgkmcnt(4)\n\t"
        "v_xor_b32 v80, s73, v80\n\t"
        "v_xor_b32 v81, v6, v81\n\t"
        "v_xor_b32 v84, s73, v84\n\t"
        "v_xor_b32 v85, v6, v85\n\t"
        "v_mfma_f32_16x16x32_bf16 v[56:59], v[72:75], v[64:67], v[56:59]\n\t"
        "v_mfma_f32_16x16x32_bf16 v[60:63], v[76:79], v[64:67], v[60:63]\n\t"
        "ds_read_b128 v[64:67], v5 offset:2240\n\t"
        "ds_read_b128 v[72:75], v5 offset:10688\n\t"
        "ds_read_b128 v[76:79], v5 offset:19136\n\t"
        "v_mfma_f32_16x16x32_bf16 v[24:27], v[80:83], v[68:71], v[24:27]\n\t"
        "v_mfma_f32_16x16x32_bf16 v[28:31], v[84:87], v[68:71], v[28:31]\n\t"
        "v_or3_b32 v7, v7, v80, v81\n\t"
        "ds_read_b128 v[68:71], v5 offset:27584\n\t"
        "s_waitcnt lgkmcnt(6)\n\t"
        "v_mfma_f32_16x16x32_bf16 v[32:35], v[80:83], v[12:15], v[32:35]\n\t"
        "v_mfma_f32_16x16x32_bf16 v[36:39], v[84:87], v[12:15], v[36:39]\n\t"
        "v_or3_b32 v7, v7, v84, v85\n\t"
        "ds_read_b128 v[12:15], v5 offset:36032\n\t"
        "v_mfma_f32_16x16x32_bf16 v[40:43], v[80:83], v[16:19], v[40:43]\n\t"
        "v_mfma_f32_16x16x32_bf16 v[44:47], v[84:87], v[16:19], v[44:47]\n\t"
        "ds_read_b128 v[16:19], v5 offset:2304\n\t"
        "s_waitcnt lgkmcnt(6)\n\t"
        "v_mfma_f32_16x16x32_bf16 v[48:51], v[80:83], v[20:23], v[48:51]\n\t"
        "v_mfma_f32_16x16x32_bf16 v[52:55], v[84:87], v[20:23], v[52:55]\n\t"
        "ds_read_b128 v[20:23], v5 offset:10752\n\t"
        "s_waitcnt vmcnt(8) lgkmcnt(6)\n\t"
        "v_xor_b32 v88, s73, v88\n\t"
        "v_xor_b32 v89, v6, v89\n\t"
        "v_xor_b32 v92, s73, v92\n\t"
        "v_xor_b32 v93, v6, v93\n\t"
        "v_mfma_f32_16x16x32_bf16 v[56:59], v[80:83], v[8:11], v[56:59]\n\t"
        "v_mfma_f32_16x16x32_bf16 v[60:63], v[84:87], v[8:11], v[60:63]\n\t"
        "ds_read_b128 v[8:11], v5 offset:19200\n\t"
        "ds_read_b128 v[80:83], v5 offset:27648\n\t"
        "v_mfma_f32_16x16x32_bf16 v[24:27], v[88:91], v[64:67], v[24:27]\n\t"
        "v_mfma_f32_16x16x32_bf16 v[28:31], v[92:95], v[64:67], v[28:31]\n\t"
        "v_or3_b32 v7, v7, v88, v89\n\t"
        "ds_read_b128 v[84:87], v5 offset:36096\n\t"
        "s_waitcnt lgkmcnt(7)\n\t"
        "v_mfma_f32_16x16x32_bf16 v[32:35], v[88:91], v[72:75], v[32:35]\n\t"
        "v_mfma_f32_16x16x32_bf16 v[36:39], v[92:95], v[72:75], v[36:39]\n\t"
        "v_or3_b32 v7, v7, v92, v93\n\t"
        "ds_read_b128 v[64:67], v5 offset:2368\n\t"
        "v_mfma_f32_16x16x32_bf16 v[40:43], v[88:91], v[76:79], v[40:43]\n\t"
        "v_mfma_f32_16x16x32_bf16 v[44:47], v[92:95], v[76:79], v[44:47]\n\t"
        "ds_read_b128 v[72:75], v5 offset:10816\n\t"
        "s_waitcnt lgkmcnt(7)\n\t"
        "v_mfma_f32_16x16x32_bf16 v[48:51], v[88:91], v[68:71], v[48:51]\n\t"
        "v_mfma_f32_16x16x32_bf16 v[52:55], v[92:95], v[68:71], v[52:55]\n\t"
        "ds_read_b128 v[76:79], v5 offset:19264\n\t"
        "s_waitcnt vmcnt(6) lgkmcnt(7)\n\t"
        "v_xor_b32 v96, s73, v96\n\t"
        "v_xor_b32 v97, v6, v97\n\t"
        "v_xor_b32 v100, s73, v100\n\t"
        "v_xor_b32 v101, v6, v101\n\t"
        "v_mfma_f32_16x16x32_bf16 v[56:59], v[88:91], v[12:15], v[56:59]\n\t"
        "v_mfma_f32_16x16x32_bf16 v[60:63], v[92:95], v[12:15], v[60:63]\n\t"
        "ds_read_b128 v[68:71], v5 offset:27712\n\t"
        "v_mfma_f32_16x16x32_bf16 v[24:27], v[96:99], v[16:19], v[24:27]\n\t"
        "v_mfma_f32_16x16x32_bf16 v[28:31], v[100:103], v[16:19], v[28:31]\n\t"
        "v_or3_b32 v7, v7, v96, v97\n\t"
        "ds_read_b128 v[12:15], v5 offset:36160\n\t"
        "s_waitcnt lgkmcnt(7)\n\t"
        "v_mfma_f32_16x16x32_bf16 v[32:35], v[96:99], v[20:23], v[32:35]\n\t"
        "v_mfma_f32_16x16x32_bf16 v[36:39], v[100:103], v[20:23], v[36:39]\n\t"
        "v_or3_b32 v7, v7, v100, v101\n\t"
        "ds_read_b128 v[88:91], v5 offset:2432\n\t"
        "v_mfma_f32_16x16x32_bf16 v[40:43], v[96:99], v[8:11], v[40:43]\n\t"
        "v_mfma_f32_16x16x32_bf16 v[44:47], v[100:103], v[8:11], v[44:47]\n\t"
        "ds_read_b128 v[92:95], v5 offset:10880\n\t"
        "s_waitcnt lgkmcnt(7)\n\t"
        "v_mfma_f32_16x16x32_bf16 v[48:51], v[96:99], v[80:83], v[48:51]\n\t"
        "v_mfma_f32_16x16x32_bf16 v[52:55], v[100:103], v[80:83], v[52:55]\n\t"
        "ds_read_b128 v[16:19], v5 offset:19328\n\t"
        "s_waitcnt vmcnt(4) lgkmcnt(7)\n\t"
        "v_xor_b32 v104, s73, v104\n\t"
        "v_xor_b32 v105, v6, v105\n\t"
        "v_xor_b32 v108, s73, v108\n\t"
        "v_xor_b32 v109, v6, v109\n\t"
        "v_mfma_f32_16x16x32_bf16 v[56:59], v[96:99], v[84:87], v[56:59]\n\t"
        "v_mfma_f32_16x16x32_bf16 v[60:63], v[100:103], v[84:87], v[60:63]\n\t"
        "ds_read_b128 v[20:23], v5 offset:27776\n\t"
        "v_mfma_f32_16x16x32_bf16 v[24:27], v[104:107], v[64:67], v[24:27]\n\t"
        "v_mfma_f32_16x16x32_bf16 v[28:31], v[108:111], v[64:67], v[28:31]\n\t"
        "v_or3_b32 v7, v7, v104, v105\n\t"
        "ds_read_b128 v[8:11], v5 offset:36224\n\t"
        "s_waitcnt lgkmcnt(7)\n\t"
        "v_mfma_f32_16x16x32_bf16 v[32:35], v[104:107], v[72:75], v[32:35]\n\t"
        "v_mfma_f32_16x16x32_bf16 v[36:39], v[108:111], v[72:75], v[36:39]\n\t"
        "v_or3_b32 v7, v7, v108, v109\n\t"
        "ds_read_b128 v[80:83], v5 offset:2496\n\t"
        "v_mfma_f32_16x16x32_bf16 v[40:43], v[104:107], v[76:79], v[40:43]\n\t"
        "v_mfma_f32_16x16x32_bf16 v[44:47], v[108:111], v[76:79], v[44:47]\n\t"
        "ds_read_b128 v[84:87], v5 offset:10944\n\t"
        "s_waitcnt lgkmcnt(7)\n\t"
        "v_mfma_f32_16x16x32_bf16 v[48:51], v[104:107], v[68:71], v[48:51]\n\t"
        "v_mfma_f32_16x16x32_bf16 v[52:55], v[108:111], v[68:71], v[52:55]\n\t"
        "ds_read_b128 v[96:99], v5 offset:19392\n\t"
        "s_waitcnt vmcnt(2) lgkmcnt(7)\n\t"
        "v_xor_b32 v112, s73, v112\n\t"
        "v_xor_b32 v113, v6, v113\n\t"
        "v_xor_b32 v116, s73, v116\n\t"
        "v_xor_b32 v117, v6, v117\n\t"
        "v_mfma_f32_16x16x32_bf16 v[56:59], v[104:107], v[12:15], v[56:59]\n\t"
        "v_mfma_f32_16x16x32_bf16 v[60:63], v[108:111], v[12:15], v[60:63]\n\t"
        "ds_read_b128 v[100:103], v5 offset:27840\n\t"
        "v_mfma_f32_16x16x32_bf16 v[24:27], v[112:115], v[88:91], v[24:27]\n\t"
        "v_mfma_f32_16x16x32_bf16 v[28:31], v[116:119], v[88:91], v[28:31]\n\t"
        "v_or3_b32 v7, v7, v112, v113\n\t"
        "ds_read_b128 v[64:67], v5 offset:36288\n\t"
        "s_waitcnt lgkmcnt(7)\n\t"
        "v_mfma_f32_16x16x32_bf16 v[32:35], v[112:115], v[92:95], v[32:35]\n\t"
        "v_mfma_f32_16x16x32_bf16 v[36:39], v[116:119], v[92:95], v[36:39]\n\t"
        "v_or3_b32 v7, v7, v116, v117\n\t"
        "v_mfma_f32_16x16x32_bf16 v[40:43], v[112:115], v[16:19], v[40:43]\n\t"
        "v_mfma_f32_16x16x32_bf16 v[44:47], v[116:119], v[16:19], v[44:47]\n\t"
        "s_waitcnt lgkmcnt(5)\n\t"
        "v_mfma_f32_16x16x32_bf16 v[48:51], v[112:115], v[20:23], v[48:51]\n\t"
        "v_mfma_f32_16x16x32_bf16 v[52:55], v[116:119], v[20:23], v[52:55]\n\t"
        "s_waitcnt vmcnt(0) lgkmcnt(4)\n\t"
        "v_xor_b32 v120, s73, v120\n\t"
        "v_xor_b32 v121, v6, v121\n\t"
        "v_xor_b32 v124, s73, v124\n\t"
        "v_xor_b32 v125, v6, v125\n\t"
        "v_mfma_f32_16x16x32_bf16 v[56:59], v[112:115], v[8:11], v[56:59]\n\t"
        "v_mfma_f32_16x16x32_bf16 v[60:63], v[116:119], v[8:11], v[60:63]\n\t"
        "v_mfma_f32_16x16x32_bf16 v[24:27], v[120:123], v[80:83], v[24:27]\n\t"
        "v_mfma_f32_16x16x32_bf16 v[28:31], v[124:127], v[80:83], v[28:31]\n\t"
        "v_or3_b32 v7, v7, v120, v121\n\t"
        "s_waitcnt lgkmcnt(3)\n\t"
        "v_mfma_f32_16x16x32_bf16 v[32:35], v[120:123], v[84:87], v[32:35]\n\t"
        "v_mfma_f32_16x16x32_bf16 v[36:39], v[124:127], v[84:87], v[36:39]\n\t"
        "v_or3_b32 v7, v7, v124, v125\n\t"
        "v_and_b32 v7, s49, v7\n\t"
        "v_cmp_ne_u32 vcc, 0, v7\n\t"
        "s_cbranch_vccnz .L7_qr\n"
        ".L7_qd:\n\t"
        "s_mov_b64 exec, 1\n\t"
        "ds_add_u32 v2, v3 offset:12\n\t"
        "s_mov_b64 exec, -1\n\t"
        "v_and_b32 v6, 15, v1\n\t"
        "v_mul_u32_u24 v6, 272, v6\n\t"
        "v_and_b32 v7, 48, v1\n\t"
        "s_lshl_b32 s78, s80, 6\n\t"
        "v_add3_u32 v6, v6, v7, s78\n\t"
        "s_waitcnt lgkmcnt(2)\n\t"
        "v_mfma_f32_16x16x32_bf16 v[40:43], v[120:123], v[96:99], v[40:43]\n\t"
        "v_mfma_f32_16x16x32_bf16 v[44:47], v[124:127], v[96:99], v[44:47]\n\t"
        "v_cvt_pk_bf16_f32 v24, v24, v25\n\t"
        "v_cvt_pk_bf16_f32 v25, v26, v27\n\t"
        "v_cvt_pk_bf16_f32 v26, v28, v29\n\t"
        "v_cvt_pk_bf16_f32 v27, v30, v31\n\t"
        "s_waitcnt lgkmcnt(0)\n\t"
        "v_mfma_f32_16x16x32_bf16 v[48:51], v[120:123], v[100:103], v[48:51]\n\t"
        "v_mfma_f32_16x16x32_bf16 v[52:55], v[124:127], v[100:103], v[52:55]\n\t"
        "v_cvt_pk_bf16_f32 v32, v32, v33\n\t"
        "v_cvt_pk_bf16_f32 v33, v34, v35\n\t"
        "v_cvt_pk_bf16_f32 v34, v36, v37\n\t"
        "v_cvt_pk_bf16_f32 v35, v38, v39\n\t"
        "v_mfma_f32_16x16x32_bf16 v[56:59], v[120:123], v[64:67], v[56:59]\n\t"
        "v_mfma_f32_16x16x32_bf16 v[60:63], v[124:127], v[64:67], v[60:63]\n\t"
        "v_cvt_pk_bf16_f32 v40, v40, v41\n\t"
        "v_cvt_pk_bf16_f32 v41, v42, v43\n\t"
        "v_cvt_pk_bf16_f32 v42, v44, v45\n\t"
        "v_cvt_pk_bf16_f32 v43, v46, v47\n\t"
        "ds_read_b32 v0, v2 offset:12\n\t"
        "v_cvt_pk_bf16_f32 v48, v48, v49\n\t"
        "v_cvt_pk_bf16_f32 v49, v50, v51\n\t"
        "v_cvt_pk_bf16_f32 v50, v52, v53\n\t"
        "v_cvt_pk_bf16_f32 v51, v54, v55\n\t"
        "v_cvt_pk_bf16_f32 v56, v56, v57\n\t"
        "v_cvt_pk_bf16_f32 v57, v58, v59\n\t"
        "v_cvt_pk_bf16_f32 v58, v60, v61\n\t"
        "v_cvt_pk_bf16_f32 v59, v62, v63\n\t"
        "s_waitcnt lgkmcnt(0)\n\t"
        "v_cmp_gt_u16 vcc, 4, v0\n\t"
        "s_cbranch_vccz .L7_qx\n"
        ".L7_w58:\n\t"
        "ds_read_b32 v0, v2 offset:12\n\t"
        "s_waitcnt lgkmcnt(0)\n\t"
        "v_cmp_gt_u16 vcc, 4, v0\n\t"
        "s_cbranch_vccz .L7_d59\n\t"
        "s_sleep 0\n\t"
        "s_sub_i32 s83, s83, 1\n\t"
        "s_cmp_gt_i32 s83, 0\n\t"
        "s_cbranch_scc1 .L7_w58\n"
        ".L7_d59:\n"
        ".L7_qx:\n\t"
        "ds_write_b128 v6, v[24:27] offset:2048\n\t"
        "ds_write_b128 v6, v[32:35] offset:6400\n\t"
        "ds_write_b128 v6, v[40:43] offset:10752\n\t"
        "ds_write_b128 v6, v[48:51] offset:15104\n\t"
        "ds_write_b128 v6, v[56:59] offset:19456\n\t"
        "s_lshl_b32 s78, s69, 9\n\t"
        "v_lshl_add_u32 v7, v1, 3, s78\n\t"
        "v_mov_b64 v[8:9], 0\n\t"
        "ds_write_b64 v7, v[8:9] offset:34048\n\t"
        "s_mov_b64 exec, 1\n\t"
        "ds_add_u32 v2, v3 offset:16\n\t"
        "s_mov_b64 exec, -1\n\t"
        "v_lshrrev_b32 v10, 3, v1\n\t"
        "v_lshlrev_b32 v10, 9, v10\n\t"
        "v_and_b32 v11, 7, v1\n\t"
        "v_lshl_add_u32 v10, v11, 4, v10\n\t"
        "v_and_b32 v11, 16, v1\n\t"
        "v_lshlrev_b32 v11, 2, v11\n\t"
        "v_xor_b32 v10, v10, v11\n\t"
        "s_load_dwordx4 s[24:27], %2, 32\n\t"
        "s_lshl_b32 s78, s80, 14\n\t"
        "s_add_u32 m0, s78, 68864\n\t"
        "s_lshl_b32 s78, s70, 9\n\t"
        "s_add_u32 s78, s78, s80\n\t"
        "s_lshl_b32 s78, s78, 7\n"
        ".L7_w60:\n\t"
        "ds_read_b32 v0, v2 offset:24\n\t"
        "s_waitcnt lgkmcnt(0)\n\t"
        "v_cmp_gt_u16 vcc, 8, v0\n\t"
        "s_cbranch_vccz .L7_d61\n\t"
        "s_sleep 1\n\t"
        "s_sub_i32 s83, s83, 1\n\t"
        "s_cmp_gt_i32 s83, 0\n\t"
        "s_cbranch_scc1 .L7_w60\n"
        ".L7_d61:\n"
        ".L7_w62:\n\t"
        "ds_read_b32 v0, v2 offset:88\n\t"
        "s_waitcnt lgkmcnt(0)\n\t"
        "v_cmp_gt_u16 vcc, 16, v0\n\t"
        "s_cbranch_vccz .L7_d63\n\t"
        "s_sleep 1\n\t"
        "s_sub_i32 s83, s83, 1\n\t"
        "s_cmp_gt_i32 s83, 0\n\t"
        "s_cbranch_scc1 .L7_w62\n"
        ".L7_d63:\n\t"
        "s_setprio 2\n\t"
        "buffer_load_dwordx4 v10, s[24:27], s78 offen lds\n\t"
        "s_add_u32 m0, m0, 0x400\n\t"
        "s_add_u32 s78, s78, 0x1000\n\t"
        "buffer_load_dwordx4 v10, s[24:27], s78 offen lds\n\t"
        "s_add_u32 m0, m0, 0x400\n\t"
        "s_add_u32 s78, s78, 0x1000\n\t"
        "buffer_load_dwordx4 v10, s[24:27], s78 offen lds\n\t"
        "s_add_u32 m0, m0, 0x400\n\t"
        "s_add_u32 s78, s78, 0x1000\n\t"
        "buffer_load_dwordx4 v10, s[24:27], s78 offen lds\n\t"
        "s_add_u32 m0, m0, 0x400\n\t"
        "s_add_u32 s78, s78, 0x1000\n\t"
        "buffer_load_dwordx4 v10, s[24:27], s78 offen lds\n\t"
        "s_add_u32 m0, m0, 0x400\n\t"
        "s_add_u32 s78, s78, 0x1000\n\t"
        "buffer_load_dwordx4 v10, s[24:27], s78 offen lds\n\t"
        "s_add_u32 m0, m0, 0x400\n\t"
        "s_add_u32 s78, s78, 0x1000\n\t"
        "buffer_load_dwordx4 v10, s[24:27], s78 offen lds\n\t"
        "s_add_u32 m0, m0, 0x400\n\t"
        "s_add_u32 s78, s78, 0x1000\n\t"
        "buffer_load_dwordx4 v10, s[24:27], s78 offen lds\n\t"
        "s_add_u32 m0, m0, 0x400\n\t"
        "s_add_u32 s78, s78, 0x1000\n\t"
        "buffer_load_dwordx4 v10, s[24:27], s78 offen lds\n\t"
        "s_add_u32 m0, m0, 0x400\n\t"
        "s_add_u32 s78, s78, 0x1000\n\t"
        "buffer_load_dwordx4 v10, s[24:27], s78 offen lds\n\t"
        "s_add_u32 m0, m0, 0x400\n\t"
        "s_add_u32 s78, s78, 0x1000\n\t"
        "buffer_load_dwordx4 v10, s[24:27], s78 offen lds\n\t"
        "s_add_u32 m0, m0, 0x400\n\t"
        "s_add_u32 s78, s78, 0x1000\n\t"
        "buffer_load_dwordx4 v10, s[24:27], s78 offen lds\n\t"
        "s_add_u32 m0, m0, 0x400\n\t"
        "s_add_u32 s78, s78, 0x1000\n\t"
        "buffer_load_dwordx4 v10, s[24:27], s78 offen lds\n\t"
        "s_add_u32 m0, m0, 0x400\n\t"
        "s_add_u32 s78, s78, 0x1000\n\t"
        "buffer_load_dwordx4 v10, s[24:27], s78 offen lds\n\t"
        "s_add_u32 m0, m0, 0x400\n\t"
        "s_add_u32 s78, s78, 0x1000\n\t"
        "buffer_load_dwordx4 v10, s[24:27], s78 offen lds\n\t"
        "s_add_u32 m0, m0, 0x400\n\t"
        "s_add_u32 s78, s78, 0x1000\n\t"
        "buffer_load_dwordx4 v10, s[24:27], s78 offen lds\n\t"
        "s_waitcnt vmcnt(0)\n"
        ".L7_vw:\n\t"
        "s_mov_b64 exec, 1\n\t"
        "ds_add_u32 v2, v3 offset:28\n\t"
        "s_mov_b64 exec, -1\n\t"
        "s_setprio 0\n"
        ".L7_w64:\n\t"
        "ds_read_b32 v0, v2 offset:36\n\t"
        "s_waitcnt lgkmcnt(0)\n\t"
        "v_cmp_gt_u16 vcc, 4, v0\n\t"
        "s_cbranch_vccz .L7_d65\n\t"
        "s_sleep 2\n\t"
        "s_sub_i32 s83, s83, 1\n\t"
        "s_cmp_gt_i32 s83, 0\n\t"
        "s_cbranch_scc1 .L7_w64\n"
        ".L7_d65:\n\t"
        "v_cmp_lt_u32 vcc, 0xfffff, v0\n\t"
        "s_cbranch_vccnz .L7_exh\n\t"
        "s_cmp_eq_u32 s69, 0\n\t"
        "s_cbranch_scc0 .L7_hz\n\t"
        "s_lshr_b32 s47, s68, 20\n\t"
        "s_add_u32 s47, s47, s68\n\t"
        "s_and_b32 s47, s47, 0xfff\n\t"
        "s_mul_i32 s46, s68, 0x9e377000\n\t"
        "s_or_b32 s46, s46, s47\n\t"
        "v_mov_b32 v4, s46\n\t"
        "s_xor_b32 s44, s70, 1\n\t"
        "s_lshl1_add_u32 s44, %3, s44\n\t"
        "s_lshl_b32 s44, s44, 10\n\t"
        "s_add_u32 s44, s44, 262144\n"
        ".L7_w66:\n\t"
        "ds_read_b32 v0, v2 offset:116\n\t"
        "s_waitcnt lgkmcnt(0)\n\t"
        "v_cmp_gt_u16 vcc, 1, v0\n\t"
        "s_cbranch_vccz .L7_d67\n\t"
        "s_sleep 1\n\t"
        "s_sub_i32 s83, s83, 1\n\t"
        "s_cmp_gt_i32 s83, 0\n\t"
        "s_cbranch_scc1 .L7_w66\n"
        ".L7_d67:\n\t"
        "v_readfirstlane_b32 s78, v0\n\t"
        "s_lshr_b32 s78, s78, 16\n\t"
        "s_cmp_gt_u32 s78, 16\n\t"
        "s_cselect_b32 s45, 0x2000, 0\n\t"
        "s_xor_b32 s44, s44, s45\n\t"
        "s_xor_b32 s45, s44, 0xc00\n"
        ".L7_w68:\n\t"
        "ds_read_b32 v0, v2 offset:56\n\t"
        "s_waitcnt lgkmcnt(0)\n\t"
        "v_cmp_gt_u16 vcc, 3, v0\n\t"
        "s_cbranch_vccz .L7_d69\n\t"
        "s_sleep 8\n\t"
        "s_sub_i32 s83, s83, 1\n\t"
        "s_cmp_gt_i32 s83, 0\n\t"
        "s_cbranch_scc1 .L7_w68\n"
        ".L7_d69:\n\t"
        "s_setprio 3\n"
        ".L7_w70:\n\t"
        "ds_read_b32 v0, v2 offset:96\n\t"
        "s_waitcnt lgkmcnt(0)\n\t"
        "v_cmp_gt_u16 vcc, 7, v0\n\t"
        "s_cbranch_vccz .L7_d71\n\t"
        "s_sleep 1\n\t"
        "s_sub_i32 s83, s83, 1\n\t"
        "s_cmp_gt_i32 s83, 0\n\t"
        "s_cbranch_scc1 .L7_w70\n"
        ".L7_d71:\n\t"
        "s_mov_b64 exec, 1\n\t"
        "buffer_store_dword v4, off, s[20:23], s44 offset:12 sc1\n\t"
        "s_mov_b32 s83, 64\n\t"
        "s_sleep 4\n"
        ".L7_h1:\n\t"
        "s_buffer_load_dword s47, s[20:23], s45 offset:12 glc\n\t"
        "s_waitcnt lgkmcnt(0)\n\t"
        "s_cmp_eq_u32 s47, s46\n\t"
        "s_cbranch_scc1 .L7_h2\n\t"
        "s_sleep 3\n\t"
        "s_sub_i32 s83, s83, 1\n\t"
        "s_cmp_gt_i32 s83, 0\n\t"
        "s_cbranch_scc1 .L7_h1\n"
        ".L7_h2:\n\t"
        "s_mov_b64 exec, 1\n\t"
        "ds_add_u32 v2, v3 offset:100\n\t"
        "s_mov_b64 exec, -1\n"
        ".L7_hz:\n\t"
        "s_endpgm\n"
        ".L7_exh:\n\t"
        "s_mov_b64 exec, 1\n\t"
        "ds_add_u32 v2, v3 offset:100\n\t"
        "s_mov_b64 exec, -1\n\t"
        "s_branch .L7_exit\n"
        ".L7_O2:\n\t"
        "s_load_dwordx8 s[24:31], %2, 32\n\t"
        "s_load_dwordx4 s[60:63], %2, 176\n\t"
        "v_lshrrev_b32 v6, 4, v1\n\t"
        "v_and_b32 v7, 48, v1\n\t"
        "v_and_b32 v4, 2, v6\n\t"
        "v_add_lshl_u32 v4, v4, v6, 2\n\t"
        "v_and_b32 v8, 15, v1\n\t"
        "s_lshl_b32 s78, s70, 8\n\t"
        "v_lshl_add_u32 v9, v8, 4, s78\n\t"
        "v_lshrrev_b32 v10, 3, v1\n\t"
        "v_lshlrev_b32 v10, 9, v10\n\t"
        "v_and_b32 v11, 7, v1\n\t"
        "v_lshl_add_u32 v10, v11, 4, v10\n\t"
        "v_and_b32 v11, 16, v1\n\t"
        "v_lshlrev_b32 v11, 2, v11\n\t"
        "v_xor_b32 v10, v10, v11\n\t"
        "s_add_u32 s81, s69, s76\n\t"
        "s_and_b32 s81, s81, 7\n\t"
        "s_lshr_b32 s78, s81, 1\n\t"
        "s_lshl_b32 s78, s78, 7\n\t"
        "s_and_b32 s79, s81, 1\n\t"
        "s_lshl_b32 s79, s79, 15\n\t"
        "s_add_u32 s78, s78, s79\n\t"
        "s_lshl_b32 s79, s70, 16\n\t"
        "s_add_u32 s78, s78, s79\n\t"
        "v_add_u32 v10, s78, v10\n\t"
        "s_lshl_b32 s82, s81, 13\n\t"
        "s_add_u32 s82, s82, 68864\n\t"
        "v_bfe_u32 v12, v1, 2, 2\n\t"
        "v_lshlrev_b32 v11, 10, v6\n\t"
        "v_lshl_add_u32 v11, v12, 7, v11\n\t"
        "v_lshrrev_b32 v12, 1, v12\n\t"
        "s_and_b32 s78, s81, 1\n\t"
        "v_xor_b32 v12, s78, v12\n\t"
        "v_lshl_add_u32 v11, v12, 6, v11\n\t"
        "v_and_b32 v12, 3, v1\n\t"
        "v_lshl_add_u32 v11, v12, 4, v11\n\t"
        "s_lshr_b32 s78, s81, 1\n\t"
        "s_lshl_b32 s78, s78, 14\n\t"
        "s_add_u32 s78, s78, 68864\n\t"
        "v_add_u32 v23, s78, v11\n\t"
        "s_waitcnt vmcnt(0)\n\t"
        "ds_bpermute_b32 v12, v7, v5 offset:0\n\t"
        "ds_bpermute_b32 v13, v7, v5 offset:4\n\t"
        "ds_bpermute_b32 v14, v7, v5 offset:8\n\t"
        "ds_bpermute_b32 v15, v7, v5 offset:12\n\t"
        "ds_bpermute_b32 v16, v7, v5 offset:64\n\t"
        "ds_bpermute_b32 v17, v7, v5 offset:68\n\t"
        "ds_bpermute_b32 v18, v7, v5 offset:72\n\t"
        "ds_bpermute_b32 v19, v7, v5 offset:76\n\t"
        "ds_bpermute_b32 v20, v4, v5 offset:128\n\t"
        "ds_bpermute_b32 v21, v4, v5 offset:136\n\t"
        "s_waitcnt lgkmcnt(0)\n\t"
        "v_lshl_add_u32 v12, v12, 9, v9\n\t"
        "v_lshl_add_u32 v13, v13, 9, v9\n\t"
        "v_lshl_add_u32 v14, v14, 9, v9\n\t"
        "v_lshl_add_u32 v15, v15, 9, v9\n\t"
        "v_lshl_add_u32 v16, v16, 9, v9\n\t"
        "v_lshl_add_u32 v17, v17, 9, v9\n\t"
        "v_lshl_add_u32 v18, v18, 9, v9\n\t"
        "v_lshl_add_u32 v19, v19, 9, v9\n\t"
        "v_lshl_add_u32 v20, v20, 9, v9\n\t"
        "v_lshl_add_u32 v21, v21, 9, v9\n\t"
        "s_setprio 0\n"
        ".L7_w1:\n\t"
        "ds_read_b32 v0, v2 offset:44\n\t"
        "s_waitcnt lgkmcnt(0)\n\t"
        "v_cmp_gt_u16 vcc, 2, v0\n\t"
        "s_cbranch_vccz .L7_d2\n\t"
        "s_sleep 1\n\t"
        "s_sub_i32 s83, s83, 1\n\t"
        "s_cmp_gt_i32 s83, 0\n\t"
        "s_cbranch_scc1 .L7_w1\n"
        ".L7_d2:\n"
        ".L7_w3:\n\t"
        "ds_read_b64 v[4:5], v2 offset:72\n\t"
        "s_waitcnt lgkmcnt(0)\n\t"
        "v_cmp_gt_u16 vcc, 2, v4\n\t"
        "s_cbranch_vccz .L7_d4\n\t"
        "s_sleep 1\n\t"
        "s_sub_i32 s83, s83, 1\n\t"
        "s_cmp_gt_i32 s83, 0\n\t"
        "s_cbranch_scc1 .L7_w3\n"
        ".L7_d4:\n\t"
        "v_readfirstlane_b32 s68, v5\n\t"
        "s_add_u32 m0, s82, 0\n\t"
        "s_mov_b32 s78, 0\n\t"
        "buffer_load_dwordx4 v10, s[28:31], s78 offen lds\n\t"
        "s_add_u32 m0, m0, 0x400\n\t"
        "s_add_u32 s78, s78, 0x1000\n\t"
        "buffer_load_dwordx4 v10, s[28:31], s78 offen lds\n\t"
        "s_add_u32 m0, m0, 0x400\n\t"
        "s_add_u32 s78, s78, 0x1000\n\t"
        "buffer_load_dwordx4 v10, s[28:31], s78 offen lds\n\t"
        "s_add_u32 m0, m0, 0x400\n\t"
        "s_add_u32 s78, s78, 0x1000\n\t"
        "buffer_load_dwordx4 v10, s[28:31], s78 offen lds\n\t"
        "s_add_u32 m0, m0, 0x400\n\t"
        "s_add_u32 s78, s78, 0x1000\n\t"
        "buffer_load_dwordx4 v10, s[28:31], s78 offen lds\n\t"
        "s_add_u32 m0, m0, 0x400\n\t"
        "s_add_u32 s78, s78, 0x1000\n\t"
        "buffer_load_dwordx4 v10, s[28:31], s78 offen lds\n\t"
        "s_add_u32 m0, m0, 0x400\n\t"
        "s_add_u32 s78, s78, 0x1000\n\t"
        "buffer_load_dwordx4 v10, s[28:31], s78 offen lds\n\t"
        "s_add_u32 m0, m0, 0x400\n\t"
        "s_add_u32 s78, s78, 0x1000\n\t"
        "buffer_load_dwordx4 v10, s[28:31], s78 offen lds\n\t"
        "s_mov_b64 exec, 1\n\t"
        "ds_add_u32 v2, v3 offset:88\n\t"
        "s_mov_b64 exec, -1\n\t"
        "s_mov_b32 s36, 0x22222222\n\t"
        "s_mov_b32 s37, s36\n\t"
        "s_lshl_b64 s[38:39], s[36:37], 1\n\t"
        "s_lshl_b64 s[40:41], s[36:37], 2\n\t"
        "s_and_b32 s44, s68, 0xfff\n\t"
        "s_xor_b32 s45, s44, 0x5a5\n\t"
        "s_lshr_b32 s46, s68, 12\n\t"
        "s_lshr_b32 s47, s68, 20\n\t"
        "s_add_u32 s46, s46, s68\n\t"
        "s_and_b32 s46, s46, 0xfff\n\t"
        "s_add_u32 s47, s47, s68\n\t"
        "s_and_b32 s47, s47, 0xfff\n\t"
        "v_mov_b32 v22, s44\n\t"
        "v_mov_b32 v0, s45\n\t"
        "v_cndmask_b32 v22, v22, v0, s[36:37]\n\t"
        "v_mov_b32 v0, s46\n\t"
        "v_cndmask_b32 v22, v22, v0, s[38:39]\n\t"
        "v_mov_b32 v0, s47\n\t"
        "v_cndmask_b32 v22, v22, v0, s[40:41]\n\t"
        "s_lshl3_add_u32 s42, %3, s77\n\t"
        "s_lshl_b32 s42, s42, 8\n\t"
        "s_add_u32 s42, s42, 262144\n\t"
        "s_xor_b32 s43, s42, 0x800\n\t"
        "s_movk_i32 s48, 0xfff\n\t"
        "s_waitcnt vmcnt(0)\n\t"
        "s_mov_b64 exec, 1\n\t"
        "ds_add_u32 v2, v3 offset:20\n\t"
        "s_mov_b64 exec, -1\n"
        ".L7_w5:\n\t"
        "ds_read_b32 v0, v2 offset:20\n\t"
        "s_waitcnt lgkmcnt(0)\n\t"
        "v_cmp_gt_u16 vcc, 8, v0\n\t"
        "s_cbranch_vccz .L7_d6\n\t"
        "s_sleep 1\n\t"
        "s_sub_i32 s83, s83, 1\n\t"
        "s_cmp_gt_i32 s83, 0\n\t"
        "s_cbranch_scc1 .L7_w5\n"
        ".L7_d6:\n\t"
        "ds_read_b64_tr_b16 v[64:65], v23 offset:0\n\t"
        "ds_read_b64_tr_b16 v[66:67], v23 offset:512\n\t"
        "ds_read_b64_tr_b16 v[68:69], v23 offset:8\n\t"
        "ds_read_b64_tr_b16 v[70:71], v23 offset:520\n\t"
        "ds_read_b64_tr_b16 v[72:73], v23 offset:4096\n\t"
        "ds_read_b64_tr_b16 v[74:75], v23 offset:4608\n\t"
        "ds_read_b64_tr_b16 v[76:77], v23 offset:4104\n\t"
        "ds_read_b64_tr_b16 v[78:79], v23 offset:4616\n\t"
        "ds_read_b64_tr_b16 v[80:81], v23 offset:8192\n\t"
        "ds_read_b64_tr_b16 v[82:83], v23 offset:8704\n\t"
        "ds_read_b64_tr_b16 v[84:85], v23 offset:8200\n\t"
        "ds_read_b64_tr_b16 v[86:87], v23 offset:8712\n\t"
        "ds_read_b64_tr_b16 v[88:89], v23 offset:12288\n\t"
        "ds_read_b64_tr_b16 v[90:91], v23 offset:12800\n\t"
        "s_waitcnt lgkmcnt(10)\n\t"
        "ds_read_b64_tr_b16 v[92:93], v23 offset:12296\n\t"
        "ds_read_b64_tr_b16 v[94:95], v23 offset:12808\n\t"
        "s_mov_b64 exec, 1\n\t"
        "ds_add_u32 v2, v3 offset:24\n\t"
        "s_mov_b64 exec, -1\n\t"
        "buffer_load_dwordx4 v[24:27], v12, s[16:19], 0 offen\n\t"
        "buffer_load_dwordx4 v[28:31], v13, s[16:19], 0 offen\n\t"
        "buffer_load_dwordx4 v[32:35], v14, s[16:19], 0 offen\n\t"
        "buffer_load_dwordx4 v[36:39], v15, s[16:19], 0 offen\n\t"
        "buffer_load_dwordx4 v[40:43], v16, s[16:19], 0 offen\n\t"
        "buffer_load_dwordx4 v[44:47], v17, s[16:19], 0 offen\n\t"
        "buffer_load_dwordx4 v[48:51], v18, s[16:19], 0 offen\n\t"
        "buffer_load_dwordx4 v[52:55], v19, s[16:19], 0 offen\n\t"
        "buffer_load_dwordx4 v[56:59], v20, s[16:19], 0 offen\n\t"
        "buffer_load_dwordx4 v[60:63], v21, s[16:19], 0 offen\n\t"
        "s_mov_b64 exec, 1\n\t"
        "ds_add_u32 v2, v3 offset:88\n\t"
        "s_mov_b64 exec, -1\n"
        ".L7_o2:\n\t"
        "s_setprio 2\n\t"
        "v_add_u32 v20, s80, v6\n\t"
        "v_lshrrev_b32 v21, 5, v1\n\t"
        "v_add_u32 v21, s80, v21\n\t"
        "v_lshlrev_b32 v8, 4, v8\n\t"
        "s_movk_i32 s78, 272\n\t"
        "v_mad_u32_u24 v10, v20, s78, v8\n\t"
        "v_mad_u32_u24 v11, v21, s78, v8\n\t"
        "v_lshlrev_b32 v108, 1, v20\n\t"
        "v_lshlrev_b32 v109, 1, v21\n\t"
        "s_mov_b32 s50, 0x0f0ff0f0\n\t"
        "s_mov_b32 s51, s50\n\t"
        "s_and_b32 s57, s81, 1\n\t"
        "s_lshl_b32 s57, s57, 13\n"
        ".L7_w7:\n\t"
        "ds_read_b32 v0, v2 offset:16\n\t"
        "s_waitcnt lgkmcnt(0)\n\t"
        "v_cmp_gt_u16 vcc, 4, v0\n\t"
        "s_cbranch_vccz .L7_d8\n\t"
        "s_sleep 1\n\t"
        "s_sub_i32 s83, s83, 1\n\t"
        "s_cmp_gt_i32 s83, 0\n\t"
        "s_cbranch_scc1 .L7_w7\n"
        ".L7_d8:\n\t"
        "ds_read_b128 v[96:99], v10 offset:2048\n\t"
        "ds_read_b128 v[100:103], v10 offset:3136\n\t"
        "ds_read_b128 v[104:107], v11 offset:4224\n\t"
        "ds_read_b32 v9, v2 offset:116\n\t"
        "ds_read_u16 v110, v108 offset:1792\n\t"
        "ds_read_u16 v111, v108 offset:1800\n\t"
        "ds_read_u16 v112, v109 offset:1808\n\t"
        "s_waitcnt vmcnt(0)\n\t"
        "s_waitcnt lgkmcnt(6)\n\t"
        "v_dot2_f32_bf16 v12, v24, v96, 0\n\t"
        "v_dot2_f32_bf16 v13, v28, v96, 0\n\t"
        "v_dot2_f32_bf16 v14, v32, v96, 0\n\t"
        "v_dot2_f32_bf16 v15, v36, v96, 0\n\t"
        "v_dot2_f32_bf16 v12, v25, v97, v12\n\t"
        "v_dot2_f32_bf16 v13, v29, v97, v13\n\t"
        "v_dot2_f32_bf16 v14, v33, v97, v14\n\t"
        "v_dot2_f32_bf16 v15, v37, v97, v15\n\t"
        "v_dot2_f32_bf16 v12, v26, v98, v12\n\t"
        "v_dot2_f32_bf16 v13, v30, v98, v13\n\t"
        "v_dot2_f32_bf16 v14, v34, v98, v14\n\t"
        "v_dot2_f32_bf16 v15, v38, v98, v15\n\t"
        "v_dot2_f32_bf16 v12, v27, v99, v12\n\t"
        "v_dot2_f32_bf16 v13, v31, v99, v13\n\t"
        "v_dot2_f32_bf16 v14, v35, v99, v14\n\t"
        "v_dot2_f32_bf16 v15, v39, v99, v15\n\t"
        "s_waitcnt lgkmcnt(5)\n\t"
        "v_dot2_f32_bf16 v16, v40, v100, 0\n\t"
        "v_dot2_f32_bf16 v17, v44, v100, 0\n\t"
        "v_dot2_f32_bf16 v18, v48, v100, 0\n\t"
        "v_dot2_f32_bf16 v19, v52, v100, 0\n\t"
        "v_dot2_f32_bf16 v16, v41, v101, v16\n\t"
        "v_dot2_f32_bf16 v17, v45, v101, v17\n\t"
        "v_dot2_f32_bf16 v18, v49, v101, v18\n\t"
        "v_dot2_f32_bf16 v19, v53, v101, v19\n\t"
        "v_dot2_f32_bf16 v16, v42, v102, v16\n\t"
        "v_dot2_f32_bf16 v17, v46, v102, v17\n\t"
        "v_dot2_f32_bf16 v18, v50, v102, v18\n\t"
        "v_dot2_f32_bf16 v19, v54, v102, v19\n\t"
        "v_dot2_f32_bf16 v16, v43, v103, v16\n\t"
        "v_dot2_f32_bf16 v17, v47, v103, v17\n\t"
        "v_dot2_f32_bf16 v18, v51, v103, v18\n\t"
        "v_dot2_f32_bf16 v19, v55, v103, v19\n\t"
        "s_waitcnt lgkmcnt(4)\n\t"
        "v_dot2_f32_bf16 v20, v56, v104, 0\n\t"
        "v_dot2_f32_bf16 v21, v60, v104, 0\n\t"
        "v_dot2_f32_bf16 v20, v57, v105, v20\n\t"
        "v_dot2_f32_bf16 v21, v61, v105, v21\n\t"
        "v_dot2_f32_bf16 v20, v58, v106, v20\n\t"
        "v_dot2_f32_bf16 v21, v62, v106, v21\n\t"
        "v_dot2_f32_bf16 v20, v59, v107, v20\n\t"
        "v_dot2_f32_bf16 v21, v63, v107, v21\n\t"
        "s_nop 1\n\t"
        "v_add_f32_dpp v20, v20, v20 row_ror:8 row_mask:0xf bank_mask:0x3\n\t"
        "v_add_f32_dpp v12, v12, v12 row_ror:8 row_mask:0xf bank_mask:0x3\n\t"
        "v_add_f32_dpp v14, v14, v14 row_ror:8 row_mask:0xf bank_mask:0x3\n\t"
        "v_add_f32_dpp v16, v16, v16 row_ror:8 row_mask:0xf bank_mask:0x3\n\t"
        "v_add_f32_dpp v18, v18, v18 row_ror:8 row_mask:0xf bank_mask:0x3\n\t"
        "v_add_f32_dpp v20, v21, v21 row_ror:8 row_mask:0xf bank_mask:0xc\n\t"
        "v_add_f32_dpp v12, v13, v13 row_ror:8 row_mask:0xf bank_mask:0xc\n\t"
        "v_add_f32_dpp v14, v15, v15 row_ror:8 row_mask:0xf bank_mask:0xc\n\t"
        "v_add_f32_dpp v16, v17, v17 row_ror:8 row_mask:0xf bank_mask:0xc\n\t"
        "v_add_f32_dpp v18, v19, v19 row_ror:8 row_mask:0xf bank_mask:0xc\n\t"
        "v_add_f32_dpp v20, v20, v20 row_half_mirror row_mask:0xf bank_mask:0xf\n\t"
        "v_add_f32_dpp v12, v12, v12 row_half_mirror row_mask:0xf bank_mask:0x5\n\t"
        "v_add_f32_dpp v16, v16, v16 row_half_mirror row_mask:0xf bank_mask:0x5\n\t"
        "v_mov_b32 v21, v20\n\t"
        "v_add_f32_dpp v12, v14, v14 row_half_mirror row_mask:0xf bank_mask:0xa\n\t"
        "v_add_f32_dpp v16, v18, v18 row_half_mirror row_mask:0xf bank_mask:0xa\n\t"
        "v_permlane16_swap_b32 v20, v21\n\t"
        "v_cndmask_b32 v20, v20, v21, s[50:51]\n\t"
        "v_add_f32_dpp v12, v12, v12 quad_perm:[1,0,3,2] row_mask:0xf bank_mask:0xf\n\t"
        "v_add_f32_dpp v16, v16, v16 quad_perm:[1,0,3,2] row_mask:0xf bank_mask:0xf\n\t"
        "v_add_f32_dpp v20, v20, v20 quad_perm:[1,0,3,2] row_mask:0xf bank_mask:0xf\n\t"
        "v_add_f32_dpp v12, v12, v12 quad_perm:[2,3,0,1] row_mask:0xf bank_mask:0xf\n\t"
        "v_add_f32_dpp v16, v16, v16 quad_perm:[2,3,0,1] row_mask:0xf bank_mask:0xf\n\t"
        "v_add_f32_dpp v20, v20, v20 quad_perm:[2,3,0,1] row_mask:0xf bank_mask:0xf\n\t"
        "s_waitcnt lgkmcnt(0)\n\t"
        "v_cmp_gt_u16 vcc, 1, v9\n\t"
        "s_cbranch_vccz .L7_sv\n"
        ".L7_w9:\n\t"
        "ds_read_b32 v9, v2 offset:116\n\t"
        "s_waitcnt lgkmcnt(0)\n\t"
        "v_cmp_gt_u16 vcc, 1, v9\n\t"
        "s_cbranch_vccz .L7_d10\n\t"
        "s_sleep 1\n\t"
        "s_sub_i32 s83, s83, 1\n\t"
        "s_cmp_gt_i32 s83, 0\n\t"
        "s_cbranch_scc1 .L7_w9\n"
        ".L7_d10:\n"
        ".L7_sv:\n\t"
        "v_readfirstlane_b32 s78, v9\n\t"
        "s_lshr_b32 s78, s78, 16\n\t"
        "s_cmp_gt_u32 s78, 16\n\t"
        "s_cselect_b32 s56, 0x2000, 0\n\t"
        "s_sub_u32 s78, s78, 13\n\t"
        "s_cmp_le_u32 s78, 6\n\t"
        "s_cselect_b32 s57, s57, s56\n\t"
        "v_cndmask_b32 v12, v12, v16, s[36:37]\n\t"
        "v_cndmask_b32 v12, v12, v20, s[38:39]\n\t"
        "v_add_u32 v12, 0x800, v12\n\t"
        "v_bfi_b32 v21, s48, v22, v12\n\t"
        "v_lshlrev_b32 v13, 2, v1\n\t"
        "s_xor_b32 s42, s42, s56\n\t"
        "s_setprio 3\n\t"
        "buffer_store_dword v21, v13, s[20:23], s42 offen sc1\n\t"
        "s_setprio 2\n\t"
        "s_xor_b32 s43, s43, s56\n\t"
        "s_lshr_b32 s78, %3, 3\n\t"
        "s_and_b32 s79, %3, 3\n\t"
        "s_lshl2_add_u32 s78, s78, s79\n\t"
        "s_and_b32 s79, %3, 4\n\t"
        "s_lshl_b32 s79, s79, 11\n\t"
        "s_lshl_b32 s30, s81, 10\n\t"
        "s_add_u32 s79, s79, s30\n\t"
        "s_add_u32 s79, s79, 3145728\n\t"
        "s_xor_b32 s79, s79, s57\n\t"
        "s_mul_i32 s24, s78, 0xc000\n\t"
        "s_xor_b32 s78, s78, 1\n\t"
        "s_mul_i32 s27, s78, 0xc000\n\t"
        "s_add_u32 s24, s24, s79\n\t"
        "s_add_u32 s25, s24, 0x4000\n\t"
        "s_add_u32 s26, s24, 0x8000\n\t"
        "s_add_u32 s27, s27, s79\n\t"
        "s_add_u32 s28, s27, 0x4000\n\t"
        "s_add_u32 s29, s27, 0x8000\n\t"
        "s_lshr_b32 s78, s24, 8\n\t"
        "s_and_b32 s78, s78, 0x2000\n\t"
        "s_xor_b32 s24, s24, s78\n\t"
        "s_lshr_b32 s78, s25, 8\n\t"
        "s_and_b32 s78, s78, 0x2000\n\t"
        "s_xor_b32 s25, s25, s78\n\t"
        "s_lshr_b32 s78, s26, 8\n\t"
        "s_and_b32 s78, s78, 0x2000\n\t"
        "s_xor_b32 s26, s26, s78\n\t"
        "s_lshr_b32 s78, s27, 8\n\t"
        "s_and_b32 s78, s78, 0x2000\n\t"
        "s_xor_b32 s27, s27, s78\n\t"
        "s_lshr_b32 s78, s28, 8\n\t"
        "s_and_b32 s78, s78, 0x2000\n\t"
        "s_xor_b32 s28, s28, s78\n\t"
        "s_lshr_b32 s78, s29, 8\n\t"
        "s_and_b32 s78, s78, 0x2000\n\t"
        "s_xor_b32 s29, s29, s78\n\t"
        "v_and_b32 v14, 48, v8\n\t"
        "s_mov_b32 s78, 0xffff\n\t"
        "s_mov_b32 s79, 0\n\t"
        "v_lshlrev_b64 v[4:5], v14, s[78:79]\n\t"
        "v_xor_b32 v18, v110, v8\n\t"
        "v_xor_b32 v19, v111, v8\n\t"
        "v_and_b32 v14, 1, v6\n\t"
        "v_lshl_add_u32 v14, v14, 3, v8\n\t"
        "v_xor_b32 v20, v112, v14\n\t"
        "v_xor_b32 v21, v21, v22\n"
        ".L7_w11:\n\t"
        "ds_read_b32 v0, v2 offset:36\n\t"
        "s_waitcnt lgkmcnt(0)\n\t"
        "v_cmp_gt_u16 vcc, 4, v0\n\t"
        "s_cbranch_vccz .L7_d12\n\t"
        "s_sleep 1\n\t"
        "s_sub_i32 s83, s83, 1\n\t"
        "s_cmp_gt_i32 s83, 0\n\t"
        "s_cbranch_scc1 .L7_w11\n"
        ".L7_d12:\n\t"
        "v_lshrrev_b32 v0, 20, v0\n\t"
        "s_nop 0\n\t"
        "v_readfirstlane_b32 s58, v0\n\t"
        "s_sleep 8\n"
        ".L7_sp:\n\t"
        "s_setprio 3\n\t"
        "buffer_load_dword v12, v13, s[20:23], s43 offen sc1\n\t"
        "s_setprio 2\n\t"
        "s_waitcnt vmcnt(0)\n\t"
        "v_xor_b32 v12, v12, v22\n\t"
        "v_and_b32 v0, s48, v12\n\t"
        "v_cmp_ne_u32 vcc, 0, v0\n\t"
        "s_cbranch_vccz .L7_sq\n\t"
        "s_sleep 8\n\t"
        "s_sub_i32 s83, s83, 1\n\t"
        "s_cmp_gt_i32 s83, 0\n\t"
        "s_cbranch_scc1 .L7_sp\n"
        ".L7_sq:\n\t"
        "v_add_f32 v21, v21, v12\n\t"
        "v_exp_f32 v21, v21\n\t"
        "v_lshlrev_b32 v22, 4, v1\n\t"
        "v_add_u32 v22, 142848, v22\n\t"
        "s_cmp_lg_u32 s58, 0\n\t"
        "s_cbranch_scc1 .L7_na\n\t"
        "v_add_f32_dpp v0, v21, v21 row_ror:4 row_mask:0xf bank_mask:0xf\n\t"
        "s_cmp_lt_u32 s69, 4\n\t"
        "s_cselect_b32 s78, 16, 1\n\t"
        "v_add_f32_dpp v0, v0, v0 row_ror:8 row_mask:0xf bank_mask:0xf\n\t"
        "v_add_f32 v0, 0x3089705f, v0\n\t"
        "v_rcp_f32 v0, v0\n\t"
        "s_nop 0\n\t"
        "v_mul_f32 v21, v21, v0\n\t"
        "v_cvt_pk_bf16_f32 v21, v21, v21\n\t"
        "v_mov_b32 v0, s78\n\t"
        "s_nop 0\n\t"
        "v_and_b32_dpp v6, v21, v4 quad_perm:[0,0,0,0] row_mask:0xf bank_mask:0xf\n\t"
        "v_and_b32_dpp v7, v21, v5 quad_perm:[0,0,0,0] row_mask:0xf bank_mask:0xf\n\t"
        "v_and_b32_dpp v16, v21, v4 quad_perm:[1,1,1,1] row_mask:0xf bank_mask:0xf\n\t"
        "v_and_b32_dpp v17, v21, v5 quad_perm:[1,1,1,1] row_mask:0xf bank_mask:0xf\n\t"
        "v_mfma_f32_4x4x4_16b_bf16 v[8:11], v[6:7], v[24:25], 0 cbsz:2 abid:0\n\t"
        "v_mfma_f32_4x4x4_16b_bf16 v[12:15], v[6:7], v[26:27], 0 cbsz:2 abid:0\n\t"
        "v_mfma_f32_4x4x4_16b_bf16 v[120:123], v[16:17], v[40:41], 0 cbsz:2 abid:0\n\t"
        "v_mfma_f32_4x4x4_16b_bf16 v[124:127], v[16:17], v[42:43], 0 cbsz:2 abid:0\n\t"
        "v_mfma_f32_4x4x4_16b_bf16 v[8:11], v[6:7], v[28:29], v[8:11] cbsz:2 abid:2\n\t"
        "v_mfma_f32_4x4x4_16b_bf16 v[12:15], v[6:7], v[30:31], v[12:15] cbsz:2 abid:2\n\t"
        "v_mfma_f32_4x4x4_16b_bf16 v[120:123], v[16:17], v[44:45], v[120:123] cbsz:2 abid:2\n\t"
        "v_mfma_f32_4x4x4_16b_bf16 v[124:127], v[16:17], v[46:47], v[124:127] cbsz:2 abid:2\n\t"
        "v_mfma_f32_4x4x4_16b_bf16 v[8:11], v[6:7], v[32:33], v[8:11] cbsz:2 abid:1\n\t"
        "v_mfma_f32_4x4x4_16b_bf16 v[12:15], v[6:7], v[34:35], v[12:15] cbsz:2 abid:1\n\t"
        "v_mfma_f32_4x4x4_16b_bf16 v[120:123], v[16:17], v[48:49], v[120:123] cbsz:2 abid:1\n\t"
        "v_mfma_f32_4x4x4_16b_bf16 v[124:127], v[16:17], v[50:51], v[124:127] cbsz:2 abid:1\n\t"
        "v_mfma_f32_4x4x4_16b_bf16 v[8:11], v[6:7], v[36:37], v[8:11] cbsz:2 abid:3\n\t"
        "v_mfma_f32_4x4x4_16b_bf16 v[12:15], v[6:7], v[38:39], v[12:15] cbsz:2 abid:3\n\t"
        "v_mfma_f32_4x4x4_16b_bf16 v[120:123], v[16:17], v[52:53], v[120:123] cbsz:2 abid:3\n\t"
        "v_mfma_f32_4x4x4_16b_bf16 v[124:127], v[16:17], v[54:55], v[124:127] cbsz:2 abid:3\n\t"
        "v_and_b32_dpp v6, v21, v4 quad_perm:[2,2,2,2] row_mask:0xf bank_mask:0xf\n\t"
        "v_and_b32_dpp v7, v21, v5 quad_perm:[2,2,2,2] row_mask:0xf bank_mask:0xf\n\t"
        "s_nop 0\n\t"
        "s_nop 0\n\t"
        "v_mfma_f32_4x4x4_16b_bf16 v[24:27], v[6:7], v[56:57], 0 cbsz:2 abid:0\n\t"
        "v_mfma_f32_4x4x4_16b_bf16 v[28:31], v[6:7], v[58:59], 0 cbsz:2 abid:0\n\t"
        "v_cvt_pk_bf16_f32 v8, v8, v9\n\t"
        "v_cvt_pk_bf16_f32 v9, v10, v11\n\t"
        "v_cvt_pk_bf16_f32 v10, v12, v13\n\t"
        "v_cvt_pk_bf16_f32 v11, v14, v15\n\t"
        "ds_write_b128 v18, v[8:11] offset:46336\n\t"
        "v_mfma_f32_4x4x4_16b_bf16 v[24:27], v[6:7], v[60:61], v[24:27] cbsz:2 abid:2\n\t"
        "v_mfma_f32_4x4x4_16b_bf16 v[28:31], v[6:7], v[62:63], v[28:31] cbsz:2 abid:2\n\t"
        "v_cvt_pk_bf16_f32 v120, v120, v121\n\t"
        "v_cvt_pk_bf16_f32 v121, v122, v123\n\t"
        "v_cvt_pk_bf16_f32 v122, v124, v125\n\t"
        "v_cvt_pk_bf16_f32 v123, v126, v127\n\t"
        "ds_write_b128 v19, v[120:123] offset:46336\n\t"
        "v_permlane16_swap_b32 v24, v28\n\t"
        "v_permlane16_swap_b32 v25, v29\n\t"
        "v_permlane16_swap_b32 v26, v30\n\t"
        "v_permlane16_swap_b32 v27, v31\n\t"
        "v_pk_add_f32 v[24:25], v[24:25], v[28:29]\n\t"
        "v_pk_add_f32 v[26:27], v[26:27], v[30:31]\n\t"
        "v_cvt_pk_bf16_f32 v24, v24, v25\n\t"
        "v_cvt_pk_bf16_f32 v25, v26, v27\n\t"
        "ds_write_b64 v20, v[24:25] offset:46336\n\t"
        "s_mov_b64 exec, 1\n\t"
        "ds_add_u32 v2, v0 offset:40\n\t"
        "s_mov_b64 exec, -1\n"
        ".L7_na:\n"
        ".L7_w13:\n\t"
        "ds_read_b32 v0, v2 offset:28\n\t"
        "s_waitcnt lgkmcnt(0)\n\t"
        "v_cmp_gt_u16 vcc, 4, v0\n\t"
        "s_cbranch_vccz .L7_d14\n\t"
        "s_sleep 1\n\t"
        "s_sub_i32 s83, s83, 1\n\t"
        "s_cmp_gt_i32 s83, 0\n\t"
        "s_cbranch_scc1 .L7_w13\n"
        ".L7_d14:\n\t"
        "ds_read_b64_tr_b16 v[96:97], v23 offset:0\n\t"
        "ds_read_b64_tr_b16 v[98:99], v23 offset:512\n\t"
        "ds_read_b64_tr_b16 v[100:101], v23 offset:8\n\t"
        "ds_read_b64_tr_b16 v[102:103], v23 offset:520\n\t"
        "ds_read_b64_tr_b16 v[104:105], v23 offset:4096\n\t"
        "ds_read_b64_tr_b16 v[106:107], v23 offset:4608\n\t"
        "ds_read_b64_tr_b16 v[108:109], v23 offset:4104\n\t"
        "ds_read_b64_tr_b16 v[110:111], v23 offset:4616\n\t"
        "ds_read_b64_tr_b16 v[112:113], v23 offset:8192\n\t"
        "ds_read_b64_tr_b16 v[114:115], v23 offset:8704\n\t"
        "ds_read_b64_tr_b16 v[116:117], v23 offset:8200\n\t"
        "ds_read_b64_tr_b16 v[118:119], v23 offset:8712\n\t"
        "s_waitcnt lgkmcnt(6)\n\t"
        "ds_read_b64_tr_b16 v[120:121], v23 offset:12288\n\t"
        "ds_read_b64_tr_b16 v[122:123], v23 offset:12800\n\t"
        "ds_read_b64_tr_b16 v[124:125], v23 offset:12296\n\t"
        "ds_read_b64_tr_b16 v[126:127], v23 offset:12808\n\t"
        "ds_read_b128 v[4:7], v22\n\t"
        "s_lshl_b32 s50, s81, 6\n\t"
        "s_lshl_b32 s51, s69, 10\n\t"
        "s_add_u32 s51, s51, 134656\n\t"
        "s_mov_b32 s49, 0x10001\n\t"
        "s_mov_b32 s48, 0x07060302\n\t"
        "s_mov_b32 s55, 142848\n"
        ".L7_w15:\n\t"
        "ds_read_b32 v0, v2 offset:40\n\t"
        "s_waitcnt lgkmcnt(0)\n\t"
        "v_cmp_gt_u16 vcc, 64, v0\n\t"
        "s_cbranch_vccz .L7_d16\n\t"
        "s_setprio 0\n\t"
        "s_sleep 1\n\t"
        "s_sub_i32 s83, s83, 1\n\t"
        "s_cmp_gt_i32 s83, 0\n\t"
        "s_cbranch_scc1 .L7_w15\n"
        ".L7_d16:\n\t"
        "s_setprio 2\n\t"
        "ds_read_b128 v[8:11], v4 offset:46336\n\t"
        "ds_read_b128 v[12:15], v5 offset:46336\n\t"
        "ds_read_b128 v[16:19], v6 offset:46336\n\t"
        "ds_read_b128 v[20:23], v7 offset:46336\n\t"
        "ds_read_b128 v[48:51], v4 offset:50432\n\t"
        "ds_read_b128 v[52:55], v5 offset:50432\n\t"
        "ds_read_b128 v[56:59], v6 offset:50432\n\t"
        "ds_read_b128 v[60:63], v7 offset:50432\n\t"
        "v_and_b32 v3, 8, v1\n\t"
        "s_waitcnt lgkmcnt(6)\n\t"
        "v_mfma_f32_16x16x32_bf16 v[32:35], v[64:67], v[8:11], 0\n\t"
        "v_mfma_f32_16x16x32_bf16 v[32:35], v[72:75], v[12:15], v[32:35]\n\t"
        "s_waitcnt lgkmcnt(4)\n\t"
        "v_mfma_f32_16x16x32_bf16 v[32:35], v[80:83], v[16:19], v[32:35]\n\t"
        "v_mfma_f32_16x16x32_bf16 v[32:35], v[88:91], v[20:23], v[32:35]\n\t"
        "v_mfma_f32_16x16x32_bf16 v[36:39], v[92:95], v[20:23], 0\n\t"
        "v_mfma_f32_16x16x32_bf16 v[36:39], v[84:87], v[16:19], v[36:39]\n\t"
        "v_mfma_f32_16x16x32_bf16 v[36:39], v[76:79], v[12:15], v[36:39]\n\t"
        "v_mfma_f32_16x16x32_bf16 v[36:39], v[68:71], v[8:11], v[36:39]\n\t"
        "ds_read_b32 v0, v2 offset:40\n\t"
        "v_lshl_add_u32 v8, v3, 8, v4\n\t"
        "v_lshl_add_u32 v12, v3, 8, v5\n\t"
        "v_lshl_add_u32 v16, v3, 8, v6\n\t"
        "v_lshl_add_u32 v20, v3, 8, v7\n\t"
        "ds_read_b128 v[8:11], v8 offset:54528\n\t"
        "ds_read_b128 v[12:15], v12 offset:54528\n\t"
        "ds_read_b128 v[16:19], v16 offset:54528\n\t"
        "ds_read_b128 v[20:23], v20 offset:54528\n\t"
        "s_waitcnt lgkmcnt(7)\n\t"
        "v_mfma_f32_16x16x32_bf16 v[40:43], v[64:67], v[48:51], 0\n\t"
        "v_mfma_f32_16x16x32_bf16 v[40:43], v[72:75], v[52:55], v[40:43]\n\t"
        "s_waitcnt lgkmcnt(5)\n\t"
        "v_mfma_f32_16x16x32_bf16 v[40:43], v[80:83], v[56:59], v[40:43]\n\t"
        "v_mfma_f32_16x16x32_bf16 v[40:43], v[88:91], v[60:63], v[40:43]\n\t"
        "v_mfma_f32_16x16x32_bf16 v[44:47], v[92:95], v[60:63], 0\n\t"
        "v_mfma_f32_16x16x32_bf16 v[44:47], v[84:87], v[56:59], v[44:47]\n\t"
        "v_mfma_f32_16x16x32_bf16 v[44:47], v[76:79], v[52:55], v[44:47]\n\t"
        "v_mfma_f32_16x16x32_bf16 v[44:47], v[68:71], v[48:51], v[44:47]\n\t"
        "s_waitcnt lgkmcnt(4)\n\t"
        "v_cmp_gt_u16 vcc, 68, v0\n\t"
        "s_cbranch_vccz .L7_t0\n"
        ".L7_w17:\n\t"
        "ds_read_b32 v0, v2 offset:40\n\t"
        "s_waitcnt lgkmcnt(0)\n\t"
        "v_cmp_gt_u16 vcc, 68, v0\n\t"
        "s_cbranch_vccz .L7_d18\n\t"
        "s_setprio 0\n\t"
        "s_sleep 1\n\t"
        "s_sub_i32 s83, s83, 1\n\t"
        "s_cmp_gt_i32 s83, 0\n\t"
        "s_cbranch_scc1 .L7_w17\n"
        ".L7_d18:\n\t"
        "s_setprio 2\n\t"
        "v_lshl_add_u32 v8, v3, 8, v4\n\t"
        "v_lshl_add_u32 v12, v3, 8, v5\n\t"
        "v_lshl_add_u32 v16, v3, 8, v6\n\t"
        "v_lshl_add_u32 v20, v3, 8, v7\n\t"
        "ds_read_b128 v[8:11], v8 offset:54528\n\t"
        "ds_read_b128 v[12:15], v12 offset:54528\n\t"
        "ds_read_b128 v[16:19], v16 offset:54528\n\t"
        "ds_read_b128 v[20:23], v20 offset:54528\n"
        ".L7_t0:\n\t"
        "ds_read_b32 v0, v2 offset:52\n\t"
        "ds_read_b128 v[48:51], v4 offset:23808\n\t"
        "ds_read_b128 v[52:55], v5 offset:23808\n\t"
        "ds_read_b128 v[56:59], v6 offset:23808\n\t"
        "ds_read_b128 v[60:63], v7 offset:23808\n\t"
        "s_waitcnt lgkmcnt(7)\n\t"
        "v_mfma_f32_16x16x32_bf16 v[24:27], v[64:67], v[8:11], 0\n\t"
        "v_mfma_f32_16x16x32_bf16 v[24:27], v[72:75], v[12:15], v[24:27]\n\t"
        "s_waitcnt lgkmcnt(5)\n\t"
        "v_mfma_f32_16x16x32_bf16 v[24:27], v[80:83], v[16:19], v[24:27]\n\t"
        "v_mfma_f32_16x16x32_bf16 v[24:27], v[88:91], v[20:23], v[24:27]\n\t"
        "v_mfma_f32_16x16x32_bf16 v[28:31], v[92:95], v[20:23], 0\n\t"
        "v_mfma_f32_16x16x32_bf16 v[28:31], v[84:87], v[16:19], v[28:31]\n\t"
        "v_mfma_f32_16x16x32_bf16 v[28:31], v[76:79], v[12:15], v[28:31]\n\t"
        "v_mfma_f32_16x16x32_bf16 v[28:31], v[68:71], v[8:11], v[28:31]\n\t"
        "ds_read_b128 v[8:11], v4 offset:32000\n\t"
        "ds_read_b128 v[12:15], v5 offset:32000\n\t"
        "ds_read_b128 v[16:19], v6 offset:32000\n\t"
        "ds_read_b128 v[20:23], v7 offset:32000\n\t"
        "s_waitcnt lgkmcnt(8)\n\t"
        "v_cmp_gt_u16 vcc, 6, v0\n\t"
        "s_cbranch_vccz .L7_t1\n"
        ".L7_w19:\n\t"
        "ds_read_b32 v0, v2 offset:52\n\t"
        "s_waitcnt lgkmcnt(0)\n\t"
        "v_cmp_gt_u16 vcc, 6, v0\n\t"
        "s_cbranch_vccz .L7_d20\n\t"
        "s_setprio 0\n\t"
        "s_sleep 1\n\t"
        "s_sub_i32 s83, s83, 1\n\t"
        "s_cmp_gt_i32 s83, 0\n\t"
        "s_cbranch_scc1 .L7_w19\n"
        ".L7_d20:\n\t"
        "s_setprio 2\n\t"
        "ds_read_b128 v[48:51], v4 offset:23808\n\t"
        "ds_read_b128 v[52:55], v5 offset:23808\n\t"
        "ds_read_b128 v[56:59], v6 offset:23808\n\t"
        "ds_read_b128 v[60:63], v7 offset:23808\n\t"
        "ds_read_b128 v[8:11], v4 offset:32000\n\t"
        "ds_read_b128 v[12:15], v5 offset:32000\n\t"
        "ds_read_b128 v[16:19], v6 offset:32000\n\t"
        "ds_read_b128 v[20:23], v7 offset:32000\n"
        ".L7_t1:\n\t"
        "v_lshl_add_u32 v3, v1, 4, s55\n\t"
        "s_waitcnt lgkmcnt(6)\n\t"
        "v_mfma_f32_16x16x32_bf16 v[32:35], v[96:99], v[48:51], v[32:35]\n\t"
        "v_mfma_f32_16x16x32_bf16 v[32:35], v[104:107], v[52:55], v[32:35]\n\t"
        "s_waitcnt lgkmcnt(4)\n\t"
        "v_mfma_f32_16x16x32_bf16 v[32:35], v[112:115], v[56:59], v[32:35]\n\t"
        "v_mfma_f32_16x16x32_bf16 v[32:35], v[120:123], v[60:63], v[32:35]\n\t"
        "v_mfma_f32_16x16x32_bf16 v[36:39], v[124:127], v[60:63], v[36:39]\n\t"
        "v_mfma_f32_16x16x32_bf16 v[36:39], v[116:119], v[56:59], v[36:39]\n\t"
        "v_mfma_f32_16x16x32_bf16 v[36:39], v[108:111], v[52:55], v[36:39]\n\t"
        "v_mfma_f32_16x16x32_bf16 v[36:39], v[100:103], v[48:51], v[36:39]\n\t"
        "ds_read_b128 v[60:63], v3 offset:1024\n\t"
        "s_waitcnt lgkmcnt(3)\n\t"
        "v_mfma_f32_16x16x32_bf16 v[24:27], v[96:99], v[8:11], v[24:27]\n\t"
        "v_mfma_f32_16x16x32_bf16 v[24:27], v[104:107], v[12:15], v[24:27]\n\t"
        "s_waitcnt lgkmcnt(1)\n\t"
        "v_mfma_f32_16x16x32_bf16 v[24:27], v[112:115], v[16:19], v[24:27]\n\t"
        "v_mfma_f32_16x16x32_bf16 v[24:27], v[120:123], v[20:23], v[24:27]\n\t"
        "v_mfma_f32_16x16x32_bf16 v[28:31], v[124:127], v[20:23], v[28:31]\n\t"
        "v_mfma_f32_16x16x32_bf16 v[28:31], v[116:119], v[16:19], v[28:31]\n\t"
        "v_mfma_f32_16x16x32_bf16 v[28:31], v[108:111], v[12:15], v[28:31]\n\t"
        "v_mfma_f32_16x16x32_bf16 v[28:31], v[100:103], v[8:11], v[28:31]\n\t"
        "ds_read_b32 v0, v2 offset:56\n\t"
        "ds_read_b128 v[8:11], v4 offset:27904\n\t"
        "ds_read_b128 v[12:15], v5 offset:27904\n\t"
        "ds_read_b128 v[16:19], v6 offset:27904\n\t"
        "ds_read_b128 v[20:23], v7 offset:27904\n\t"
        "s_waitcnt lgkmcnt(5)\n\t"
        "v_add_u32 v32, 0x10000, v32\n\t"
        "v_add_u32 v33, 0x10000, v33\n\t"
        "v_add_u32 v34, 0x10000, v34\n\t"
        "v_add_u32 v35, 0x10000, v35\n\t"
        "v_perm_b32 v52, v33, v32, s48\n\t"
        "v_perm_b32 v53, v35, v34, s48\n\t"
        "v_cvt_pk_bf16_f32 v54, v36, v37\n\t"
        "v_cvt_pk_bf16_f32 v55, v38, v39\n\t"
        "v_bfi_b32 v52, s49, v60, v52\n\t"
        "v_bfi_b32 v53, s49, v61, v53\n\t"
        "buffer_store_dwordx4 v[52:55], v62, s[20:23], s25 offen sc1\n\t"
        "s_mov_b32 exec_lo, 0x00ff00ff\n\t"
        "s_mov_b32 exec_hi, 0x00ff00ff\n\t"
        "v_add_u32 v24, 0x10000, v24\n\t"
        "v_add_u32 v25, 0x10000, v25\n\t"
        "v_add_u32 v26, 0x10000, v26\n\t"
        "v_add_u32 v27, 0x10000, v27\n\t"
        "v_perm_b32 v48, v25, v24, s48\n\t"
        "v_perm_b32 v49, v27, v26, s48\n\t"
        "v_cvt_pk_bf16_f32 v50, v28, v29\n\t"
        "v_cvt_pk_bf16_f32 v51, v30, v31\n\t"
        "v_bfi_b32 v48, s49, v60, v48\n\t"
        "v_bfi_b32 v49, s49, v61, v49\n\t"
        "buffer_store_dwordx4 v[48:51], v62, s[20:23], s24 offen sc1\n\t"
        "s_mov_b64 exec, -1\n\t"
        "s_waitcnt lgkmcnt(4)\n\t"
        "v_cmp_gt_u16 vcc, 4, v0\n\t"
        "s_cbranch_vccz .L7_t2\n"
        ".L7_w21:\n\t"
        "ds_read_b32 v0, v2 offset:56\n\t"
        "s_waitcnt lgkmcnt(0)\n\t"
        "v_cmp_gt_u16 vcc, 3, v0\n\t"
        "s_cbranch_vccz .L7_d22\n\t"
        "s_setprio 0\n\t"
        "s_sleep 1\n\t"
        "s_sub_i32 s83, s83, 1\n\t"
        "s_cmp_gt_i32 s83, 0\n\t"
        "s_cbranch_scc1 .L7_w21\n"
        ".L7_d22:\n\t"
        "s_setprio 2\n"
        ".L7_w23:\n\t"
        "ds_read_b32 v0, v2 offset:56\n\t"
        "ds_read_b128 v[8:11], v4 offset:27904\n\t"
        "ds_read_b128 v[12:15], v5 offset:27904\n\t"
        "ds_read_b128 v[16:19], v6 offset:27904\n\t"
        "ds_read_b128 v[20:23], v7 offset:27904\n\t"
        "s_waitcnt lgkmcnt(4)\n\t"
        "v_cmp_gt_u16 vcc, 4, v0\n\t"
        "s_cbranch_vccz .L7_d24\n\t"
        "s_waitcnt lgkmcnt(0)\n\t"
        "s_setprio 0\n\t"
        "s_sleep 1\n\t"
        "s_sub_i32 s83, s83, 1\n\t"
        "s_cmp_gt_i32 s83, 0\n\t"
        "s_cbranch_scc1 .L7_w23\n"
        ".L7_d24:\n\t"
        "s_setprio 2\n"
        ".L7_t2:\n\t"
        "s_waitcnt lgkmcnt(2)\n\t"
        "v_mfma_f32_16x16x32_bf16 v[40:43], v[96:99], v[8:11], v[40:43]\n\t"
        "v_mfma_f32_16x16x32_bf16 v[40:43], v[104:107], v[12:15], v[40:43]\n\t"
        "s_waitcnt lgkmcnt(0)\n\t"
        "v_mfma_f32_16x16x32_bf16 v[40:43], v[112:115], v[16:19], v[40:43]\n\t"
        "v_mfma_f32_16x16x32_bf16 v[40:43], v[120:123], v[20:23], v[40:43]\n\t"
        "v_mfma_f32_16x16x32_bf16 v[44:47], v[124:127], v[20:23], v[44:47]\n\t"
        "v_mfma_f32_16x16x32_bf16 v[44:47], v[116:119], v[16:19], v[44:47]\n\t"
        "v_mfma_f32_16x16x32_bf16 v[44:47], v[108:111], v[12:15], v[44:47]\n\t"
        "v_mfma_f32_16x16x32_bf16 v[44:47], v[100:103], v[8:11], v[44:47]\n\t"
        "ds_read_b128 v[8:11], v4 offset:60672\n\t"
        "ds_read_b128 v[12:15], v5 offset:60672\n\t"
        "ds_read_b128 v[16:19], v6 offset:60672\n\t"
        "ds_read_b128 v[20:23], v7 offset:60672\n\t"
        "v_add_u32 v40, 0x10000, v40\n\t"
        "v_add_u32 v41, 0x10000, v41\n\t"
        "v_add_u32 v42, 0x10000, v42\n\t"
        "v_add_u32 v43, 0x10000, v43\n\t"
        "v_perm_b32 v56, v41, v40, s48\n\t"
        "v_perm_b32 v57, v43, v42, s48\n\t"
        "v_cvt_pk_bf16_f32 v58, v44, v45\n\t"
        "v_cvt_pk_bf16_f32 v59, v46, v47\n\t"
        "v_bfi_b32 v56, s49, v60, v56\n\t"
        "v_bfi_b32 v57, s49, v61, v57\n\t"
        "buffer_store_dwordx4 v[56:59], v62, s[20:23], s26 offen sc1\n\t"
        "v_mov_b32 v0, 1\n\t"
        "s_mov_b64 exec, 1\n\t"
        "ds_add_u32 v2, v0 offset:96\n\t"
        "s_mov_b64 exec, -1\n\t"
        "s_waitcnt lgkmcnt(3)\n\t"
        "v_mfma_f32_16x16x32_bf16 v[32:35], v[64:67], v[8:11], 0\n\t"
        "v_mfma_f32_16x16x32_bf16 v[32:35], v[72:75], v[12:15], v[32:35]\n\t"
        "s_waitcnt lgkmcnt(1)\n\t"
        "v_mfma_f32_16x16x32_bf16 v[32:35], v[80:83], v[16:19], v[32:35]\n\t"
        "v_mfma_f32_16x16x32_bf16 v[32:35], v[88:91], v[20:23], v[32:35]\n\t"
        "v_mfma_f32_16x16x32_bf16 v[36:39], v[92:95], v[20:23], 0\n\t"
        "v_mfma_f32_16x16x32_bf16 v[36:39], v[84:87], v[16:19], v[36:39]\n\t"
        "v_mfma_f32_16x16x32_bf16 v[36:39], v[76:79], v[12:15], v[36:39]\n\t"
        "v_mfma_f32_16x16x32_bf16 v[36:39], v[68:71], v[8:11], v[36:39]\n\t"
        "ds_read_b128 v[8:11], v4 offset:64768\n\t"
        "ds_read_b128 v[12:15], v5 offset:64768\n\t"
        "ds_read_b128 v[16:19], v6 offset:64768\n\t"
        "ds_read_b128 v[20:23], v7 offset:64768\n\t"
        "s_waitcnt lgkmcnt(2)\n\t"
        "v_mfma_f32_16x16x32_bf16 v[40:43], v[64:67], v[8:11], 0\n\t"
        "v_mfma_f32_16x16x32_bf16 v[40:43], v[72:75], v[12:15], v[40:43]\n\t"
        "s_waitcnt lgkmcnt(0)\n\t"
        "v_mfma_f32_16x16x32_bf16 v[40:43], v[80:83], v[16:19], v[40:43]\n\t"
        "v_mfma_f32_16x16x32_bf16 v[40:43], v[88:91], v[20:23], v[40:43]\n\t"
        "v_mfma_f32_16x16x32_bf16 v[44:47], v[92:95], v[20:23], 0\n\t"
        "v_mfma_f32_16x16x32_bf16 v[44:47], v[84:87], v[16:19], v[44:47]\n\t"
        "v_mfma_f32_16x16x32_bf16 v[44:47], v[76:79], v[12:15], v[44:47]\n\t"
        "v_mfma_f32_16x16x32_bf16 v[44:47], v[68:71], v[8:11], v[44:47]\n\t"
        "ds_read_b128 v[68:71], v3 offset:2048\n\t"
        "ds_read_b128 v[74:77], v3 offset:3072\n"
        ".L7_w25:\n\t"
        "ds_read_b32 v0, v2 offset:60\n\t"
        "s_waitcnt lgkmcnt(0)\n\t"
        "v_cmp_gt_u16 vcc, 2, v0\n\t"
        "s_cbranch_vccz .L7_d26\n\t"
        "s_setprio 0\n\t"
        "s_sleep 1\n\t"
        "s_sub_i32 s83, s83, 1\n\t"
        "s_cmp_gt_i32 s83, 0\n\t"
        "s_cbranch_scc1 .L7_w25\n"
        ".L7_d26:\n\t"
        "s_setprio 2\n\t"
        "ds_read_b128 v[8:11], v4 offset:34048\n\t"
        "ds_read_b128 v[12:15], v5 offset:34048\n\t"
        "ds_read_b128 v[16:19], v6 offset:34048\n\t"
        "ds_read_b128 v[20:23], v7 offset:34048\n\t"
        "v_add_u32 v71, s51, v71\n\t"
        "v_add_u32 v72, s51, v74\n\t"
        "v_mov_b64 v[74:75], 0\n\t"
        "v_mov_b64 v[78:79], 0\n\t"
        "s_waitcnt lgkmcnt(2)\n\t"
        "v_mfma_f32_16x16x32_bf16 v[24:27], v[96:99], v[8:11], v[24:27]\n\t"
        "v_mfma_f32_16x16x32_bf16 v[24:27], v[104:107], v[12:15], v[24:27]\n\t"
        "s_waitcnt lgkmcnt(0)\n\t"
        "v_mfma_f32_16x16x32_bf16 v[24:27], v[112:115], v[16:19], v[24:27]\n\t"
        "v_mfma_f32_16x16x32_bf16 v[24:27], v[120:123], v[20:23], v[24:27]\n\t"
        "v_mfma_f32_16x16x32_bf16 v[28:31], v[124:127], v[20:23], v[28:31]\n\t"
        "v_mfma_f32_16x16x32_bf16 v[28:31], v[116:119], v[16:19], v[28:31]\n\t"
        "v_mfma_f32_16x16x32_bf16 v[28:31], v[108:111], v[12:15], v[28:31]\n\t"
        "v_mfma_f32_16x16x32_bf16 v[28:31], v[100:103], v[8:11], v[28:31]\n"
        ".L7_w27:\n\t"
        "ds_read_b32 v0, v2 offset:100\n\t"
        "s_waitcnt lgkmcnt(0)\n\t"
        "v_cmp_gt_u16 vcc, 1, v0\n\t"
        "s_cbranch_vccz .L7_d28\n\t"
        "s_setprio 0\n\t"
        "s_sleep 1\n\t"
        "s_sub_i32 s83, s83, 1\n\t"
        "s_cmp_gt_i32 s83, 0\n\t"
        "s_cbranch_scc1 .L7_w27\n"
        ".L7_d28:\n\t"
        "s_setprio 2\n\t"
        "buffer_load_dwordx4 v[48:51], v63, s[20:23], s27 offen sc1\n\t"
        "buffer_load_dwordx4 v[52:55], v62, s[20:23], s28 offen sc1\n\t"
        "buffer_load_dwordx4 v[56:59], v62, s[20:23], s29 offen sc1\n"
        ".L7_w29:\n\t"
        "ds_read_b32 v0, v2 offset:64\n\t"
        "s_waitcnt lgkmcnt(0)\n\t"
        "v_cmp_gt_u16 vcc, 4, v0\n\t"
        "s_cbranch_vccz .L7_d30\n\t"
        "s_setprio 0\n\t"
        "s_sleep 1\n\t"
        "s_sub_i32 s83, s83, 1\n\t"
        "s_cmp_gt_i32 s83, 0\n\t"
        "s_cbranch_scc1 .L7_w29\n"
        ".L7_d30:\n\t"
        "s_setprio 2\n\t"
        "ds_read_b128 v[8:11], v4 offset:38144\n\t"
        "ds_read_b128 v[12:15], v5 offset:38144\n\t"
        "ds_read_b128 v[16:19], v6 offset:38144\n\t"
        "ds_read_b128 v[20:23], v7 offset:38144\n\t"
        "s_waitcnt lgkmcnt(2)\n\t"
        "v_mfma_f32_16x16x32_bf16 v[32:35], v[96:99], v[8:11], v[32:35]\n\t"
        "v_mfma_f32_16x16x32_bf16 v[32:35], v[104:107], v[12:15], v[32:35]\n\t"
        "s_waitcnt lgkmcnt(0)\n\t"
        "v_mfma_f32_16x16x32_bf16 v[32:35], v[112:115], v[16:19], v[32:35]\n\t"
        "v_mfma_f32_16x16x32_bf16 v[32:35], v[120:123], v[20:23], v[32:35]\n\t"
        "v_mfma_f32_16x16x32_bf16 v[36:39], v[124:127], v[20:23], v[36:39]\n\t"
        "v_mfma_f32_16x16x32_bf16 v[36:39], v[116:119], v[16:19], v[36:39]\n\t"
        "v_mfma_f32_16x16x32_bf16 v[36:39], v[108:111], v[12:15], v[36:39]\n\t"
        "v_mfma_f32_16x16x32_bf16 v[36:39], v[100:103], v[8:11], v[36:39]\n\t"
        "s_waitcnt vmcnt(0)\n\t"
        "s_branch .L7_s32\n"
        ".L7_r31:\n\t"
        "buffer_load_dwordx4 v[48:51], v63, s[20:23], s27 offen sc1\n\t"
        "buffer_load_dwordx4 v[52:55], v62, s[20:23], s28 offen sc1\n\t"
        "buffer_load_dwordx4 v[56:59], v62, s[20:23], s29 offen sc1\n\t"
        "s_waitcnt vmcnt(0)\n"
        ".L7_s32:\n\t"
        "v_xor_b32 v48, v48, v60\n\t"
        "v_xor_b32 v49, v49, v61\n\t"
        "v_xor_b32 v52, v52, v60\n\t"
        "v_xor_b32 v53, v53, v61\n\t"
        "v_xor_b32 v56, v56, v60\n\t"
        "v_xor_b32 v57, v57, v61\n\t"
        "v_or_b32 v0, v48, v49\n\t"
        "v_or3_b32 v0, v0, v52, v53\n\t"
        "v_or3_b32 v0, v0, v56, v57\n\t"
        "v_and_b32 v0, s49, v0\n\t"
        "v_cmp_ne_u32 vcc, 0, v0\n\t"
        "s_cbranch_vccz .L7_t33\n\t"
        "s_sleep 8\n\t"
        "s_sub_i32 s83, s83, 1\n\t"
        "s_cmp_gt_i32 s83, 0\n\t"
        "s_cbranch_scc1 .L7_r31\n"
        ".L7_t33:\n\t"
        "v_mfma_f32_16x16x32_bf16 v[24:27], v[76:79], v[48:51], v[24:27]\n\t"
        "v_mfma_f32_16x16x32_bf16 v[28:31], v[74:77], v[48:51], v[28:31]\n\t"
        "v_mfma_f32_16x16x32_bf16 v[32:35], v[76:79], v[52:55], v[32:35]\n\t"
        "v_mfma_f32_16x16x32_bf16 v[36:39], v[74:77], v[52:55], v[36:39]\n\t"
        "v_mfma_f32_16x16x32_bf16 v[40:43], v[76:79], v[56:59], v[40:43]\n\t"
        "v_mfma_f32_16x16x32_bf16 v[44:47], v[74:77], v[56:59], v[44:47]\n\t"
        "s_nop 2\n\t"
        "v_cvt_pk_bf16_f32 v48, v24, v25\n\t"
        "v_cvt_pk_bf16_f32 v49, v26, v27\n\t"
        "v_cvt_pk_bf16_f32 v50, v28, v29\n\t"
        "v_cvt_pk_bf16_f32 v51, v30, v31\n\t"
        "ds_write_b128 v71, v[48:51]\n\t"
        "ds_read_b128 v[48:51], v72\n\t"
        "v_cvt_pk_bf16_f32 v52, v32, v33\n\t"
        "v_cvt_pk_bf16_f32 v53, v34, v35\n\t"
        "v_cvt_pk_bf16_f32 v54, v36, v37\n\t"
        "v_cvt_pk_bf16_f32 v55, v38, v39\n\t"
        "ds_write_b128 v71, v[52:55]\n\t"
        "ds_read_b128 v[52:55], v72\n\t"
        "ds_read_b32 v0, v2 offset:68\n\t"
        "ds_read_b128 v[8:11], v4 offset:42240\n\t"
        "ds_read_b128 v[12:15], v5 offset:42240\n\t"
        "ds_read_b128 v[16:19], v6 offset:42240\n\t"
        "ds_read_b128 v[20:23], v7 offset:42240\n\t"
        "s_waitcnt lgkmcnt(4)\n\t"
        "buffer_store_dwordx4 v[48:51], v68, s[60:63], s50 offen sc1\n\t"
        "buffer_store_dwordx4 v[52:55], v69, s[60:63], s50 offen sc1\n\t"
        "v_cmp_gt_u16 vcc, 4, v0\n\t"
        "s_cbranch_vccz .L7_t5\n"
        ".L7_w34:\n\t"
        "ds_read_b32 v0, v2 offset:68\n\t"
        "s_waitcnt lgkmcnt(0)\n\t"
        "v_cmp_gt_u16 vcc, 3, v0\n\t"
        "s_cbranch_vccz .L7_d35\n\t"
        "s_setprio 0\n\t"
        "s_sleep 1\n\t"
        "s_sub_i32 s83, s83, 1\n\t"
        "s_cmp_gt_i32 s83, 0\n\t"
        "s_cbranch_scc1 .L7_w34\n"
        ".L7_d35:\n\t"
        "s_setprio 2\n"
        ".L7_w36:\n\t"
        "ds_read_b32 v0, v2 offset:68\n\t"
        "ds_read_b128 v[8:11], v4 offset:42240\n\t"
        "ds_read_b128 v[12:15], v5 offset:42240\n\t"
        "ds_read_b128 v[16:19], v6 offset:42240\n\t"
        "ds_read_b128 v[20:23], v7 offset:42240\n\t"
        "s_waitcnt lgkmcnt(4)\n\t"
        "v_cmp_gt_u16 vcc, 4, v0\n\t"
        "s_cbranch_vccz .L7_d37\n\t"
        "s_waitcnt lgkmcnt(0)\n\t"
        "s_setprio 0\n\t"
        "s_sleep 0\n\t"
        "s_sub_i32 s83, s83, 1\n\t"
        "s_cmp_gt_i32 s83, 0\n\t"
        "s_cbranch_scc1 .L7_w36\n"
        ".L7_d37:\n\t"
        "s_setprio 2\n"
        ".L7_t5:\n\t"
        "s_waitcnt lgkmcnt(2)\n\t"
        "v_mfma_f32_16x16x32_bf16 v[40:43], v[96:99], v[8:11], v[40:43]\n\t"
        "v_mfma_f32_16x16x32_bf16 v[40:43], v[104:107], v[12:15], v[40:43]\n\t"
        "s_waitcnt lgkmcnt(0)\n\t"
        "v_mfma_f32_16x16x32_bf16 v[40:43], v[112:115], v[16:19], v[40:43]\n\t"
        "v_mfma_f32_16x16x32_bf16 v[40:43], v[120:123], v[20:23], v[40:43]\n\t"
        "v_mfma_f32_16x16x32_bf16 v[44:47], v[124:127], v[20:23], v[44:47]\n\t"
        "v_mfma_f32_16x16x32_bf16 v[44:47], v[116:119], v[16:19], v[44:47]\n\t"
        "v_mfma_f32_16x16x32_bf16 v[44:47], v[108:111], v[12:15], v[44:47]\n\t"
        "v_mfma_f32_16x16x32_bf16 v[44:47], v[100:103], v[8:11], v[44:47]\n\t"
        "s_nop 3\n\t"
        "v_cvt_pk_bf16_f32 v56, v40, v41\n\t"
        "v_cvt_pk_bf16_f32 v57, v42, v43\n\t"
        "s_nop 1\n\t"
        "v_cvt_pk_bf16_f32 v58, v44, v45\n\t"
        "v_cvt_pk_bf16_f32 v59, v46, v47\n\t"
        "ds_write_b128 v71, v[56:59]\n\t"
        "ds_read_b128 v[56:59], v72\n\t"
        "s_waitcnt lgkmcnt(0)\n\t"
        "buffer_store_dwordx4 v[56:59], v70, s[60:63], s50 offen sc1\n\t"
        "s_or_b32 s78, %3, s69\n\t"
        "s_cbranch_scc1 .L7_ng\n\t"
        "v_mov_b32 v0, s68\n\t"
        "buffer_store_dword v0, off, s[20:23], 0 sc1\n"
        ".L7_ng:\n\t"
        "s_endpgm\n"
        ".L7_G2:\n\t"
        "s_setprio 3\n\t"
        "v_lshlrev_b32 v5, 4, v1\n\t"
        "s_mul_i32 s82, s76, 13\n\t"
        "s_lshr_b32 s82, s82, 6\n\t"
        "s_mul_i32 s82, s82, 5\n\t"
        "s_sub_u32 s82, s76, s82\n\t"
        "s_waitcnt lgkmcnt(0)\n\t"
        "s_lshl2_add_u32 s78, s82, s69\n\t"
        "s_lshl_b32 s78, s78, 10\n\t"
        "s_add_u32 m0, s78, 46336\n\t"
        "s_add_u32 s82, s82, 1\n\t"
        "s_cmp_eq_u32 s82, 5\n\t"
        "buffer_load_dwordx4 v5, s[56:59], s78 offen lds\n\t"
        "s_cselect_b32 s82, 0, s82\n\t"
        "v_lshrrev_b32 v18, 3, v1\n\t"
        "v_lshlrev_b32 v18, 14, v18\n\t"
        "v_and_b32 v6, 0x70, v5\n\t"
        "s_lshl_b32 s78, s69, 19\n\t"
        "s_lshl_b32 s79, s71, 7\n\t"
        "s_add_u32 s78, s78, s79\n\t"
        "v_add3_u32 v18, v18, v6, s78\n\t"
        "s_mov_b32 s24, 1048576\n\t"
        "s_mov_b32 s25, 1179648\n\t"
        "s_mov_b32 s26, 1310720\n\t"
        "s_mov_b32 s27, 1441792\n\t"
        "v_and_b32 v6, 0xf0, v5\n\t"
        "s_lshl_b32 s78, s70, 8\n\t"
        "v_add_u32 v62, s78, v6\n\t"
        "v_mov_b32 v7, 113860\n\t"
        "s_movk_i32 s38, 196\n\t"
        "s_mov_b32 s39, 12845056\n\t"
        "s_lshl_b32 s33, s69, 3\n\t"
        "s_lshl_b32 s78, 1, s33\n\t"
        "v_mov_b32 v29, s78\n\t"
        "s_sub_u32 s32, s78, 1\n\t"
        "s_and_b32 s32, s32, 0x01010101\n\t"
        "s_mov_b32 s35, 0xffffff\n\t"
        "s_mov_b32 s40, 0x13050109\n\t"
        "s_mov_b32 s41, 23\n\t"
        "s_cmp_eq_u32 s69, 1\n\t"
        "s_cselect_b32 s40, 0x12040008, s40\n\t"
        "s_cselect_b32 s41, 22, s41\n\t"
        "s_cmp_eq_u32 s69, 2\n\t"
        "s_cselect_b32 s40, 0x100e0602, s40\n\t"
        "s_cselect_b32 s41, 20, s41\n\t"
        "s_cmp_eq_u32 s69, 3\n\t"
        "s_cselect_b32 s40, 0x110f0703, s40\n\t"
        "s_cselect_b32 s41, 21, s41\n\t"
        "s_mul_i32 s41, s41, 0x01010101\n\t"
        "s_bitcmp1_b32 s40, 3\n\t"
        "s_cselect_b32 s78, 0, 4\n\t"
        "v_mov_b32 v61, s78\n\t"
        "v_lshrrev_b32 v12, 4, v1\n\t"
        "s_bfe_u32 s78, s40, 0x80000\n\t"
        "s_lshl_b32 s78, s78, 2\n\t"
        "v_add_u32 v13, s78, v12\n\t"
        "v_lshlrev_b32 v24, 2, v13\n\t"
        "v_lshlrev_b32 v15, 4, v13\n\t"
        "v_and_b32 v15, 0xf0, v15\n\t"
        "v_xor_b32 v15, v15, v6\n\t"
        "v_lshl_add_u32 v56, v13, 8, v15\n\t"
        "s_bfe_u32 s78, s40, 0x80008\n\t"
        "s_lshl_b32 s78, s78, 2\n\t"
        "v_add_u32 v13, s78, v12\n\t"
        "v_lshlrev_b32 v25, 2, v13\n\t"
        "v_lshlrev_b32 v15, 4, v13\n\t"
        "v_and_b32 v15, 0xf0, v15\n\t"
        "v_xor_b32 v15, v15, v6\n\t"
        "v_lshl_add_u32 v57, v13, 8, v15\n\t"
        "s_bfe_u32 s78, s40, 0x80010\n\t"
        "s_lshl_b32 s78, s78, 2\n\t"
        "v_add_u32 v13, s78, v12\n\t"
        "v_lshlrev_b32 v26, 2, v13\n\t"
        "v_lshlrev_b32 v15, 4, v13\n\t"
        "v_and_b32 v15, 0xf0, v15\n\t"
        "v_xor_b32 v15, v15, v6\n\t"
        "v_lshl_add_u32 v58, v13, 8, v15\n\t"
        "v_lshlrev_b32 v15, 9, v61\n\t"
        "v_sub_u32 v58, v58, v15\n\t"
        "s_bfe_u32 s78, s40, 0x80018\n\t"
        "s_lshl_b32 s78, s78, 2\n\t"
        "v_add_u32 v13, s78, v12\n\t"
        "v_lshlrev_b32 v27, 2, v13\n\t"
        "v_lshlrev_b32 v15, 4, v13\n\t"
        "v_and_b32 v15, 0xf0, v15\n\t"
        "v_xor_b32 v15, v15, v6\n\t"
        "v_lshl_add_u32 v59, v13, 8, v15\n\t"
        "s_bfe_u32 s78, s41, 0x80000\n\t"
        "s_lshl_b32 s78, s78, 2\n\t"
        "v_add_u32 v13, s78, v12\n\t"
        "v_lshlrev_b32 v28, 2, v13\n\t"
        "v_lshlrev_b32 v15, 4, v13\n\t"
        "v_and_b32 v15, 0xf0, v15\n\t"
        "v_xor_b32 v15, v15, v6\n\t"
        "v_lshl_add_u32 v60, v13, 8, v15\n\t"
        "v_and_b32 v20, 28, v1\n\t"
        "v_lshlrev_b32 v20, 1, v20\n\t"
        "v_lshrrev_b64 v[12:13], v20, s[40:41]\n\t"
        "v_and_b32 v13, 0xff, v12\n\t"
        "v_and_b32 v12, 3, v1\n\t"
        "v_lshl_add_u32 v13, v13, 2, v12\n\t"
        "v_lshlrev_b32 v31, 2, v13\n\t"
        "s_and_b32 s78, s67, 0x10000\n\t"
        "s_or_b32 s78, s78, 1\n\t"
        "v_mov_b32 v30, s78\n\t"
        "s_mov_b32 s36, 0\n\t"
        "v_and_b32 v0, 48, v5\n\t"
        "s_mov_b32 s78, 0x0f0e\n\t"
        "s_mov_b32 s79, 0\n\t"
        "v_lshlrev_b64 v[8:9], v0, s[78:79]\n\t"
        "s_mov_b32 s78, 0x0b0a\n\t"
        "v_lshlrev_b64 v[12:13], v0, s[78:79]\n\t"
        "s_mov_b32 s78, 0xf0f0f0f0\n\t"
        "s_mov_b32 s79, s78\n\t"
        "v_cndmask_b32 v8, v8, v12, s[78:79]\n\t"
        "v_cndmask_b32 v9, v9, v13, s[78:79]\n\t"
        "v_xor_b32 v8, 0x0c0c0c0c, v8\n\t"
        "v_xor_b32 v9, 0x0c0c0c0c, v9\n\t"
        "s_movk_i32 s48, 0x200\n\t"
        "s_mov_b32 s37, 0x01010101\n\t"
        "s_mov_b32 s49, 0x05040100\n\t"
        "s_getpc_b64 s[50:51]\n"
        ".L7_pc:\n\t"
        "s_add_u32 s52, s50, .L7_ce-.L7_pc\n\t"
        "s_addc_u32 s53, s51, 0\n\t"
        "s_add_u32 s42, s50, .L7_ev-.L7_pc\n\t"
        "s_addc_u32 s43, s51, 0\n\t"
        "s_add_u32 s50, s50, .L7_ie-.L7_pc\n\t"
        "s_addc_u32 s51, s51, 0\n\t"
        "s_cmp_eq_u32 s80, 10\n\t"
        "s_cselect_b32 s80, 0, s80\n\t"
        "s_mul_i32 s78, s80, 0x1080\n\t"
        "s_add_u32 m0, s78, s81\n\t"
        "s_lshr_b32 s78, s80, 1\n\t"
        "s_lshl1_add_u32 s78, s78, s80\n\t"
        "s_lshl_b32 s78, s78, 11\n\t"
        "buffer_load_dwordx4 v4, s[16:19], s78 offen lds\n\t"
        "s_add_u32 s80, s80, 1\n\t"
        "s_mul_i32 s78, s80, 0x1080\n\t"
        "s_add_u32 m0, s78, s81\n\t"
        "s_lshr_b32 s78, s80, 1\n\t"
        "s_lshl1_add_u32 s78, s78, s80\n\t"
        "s_lshl_b32 s78, s78, 11\n\t"
        "buffer_load_dwordx4 v4, s[16:19], s78 offen lds\n\t"
        "s_add_u32 s80, s80, 1\n\t"
        "s_mul_i32 s78, s80, 0x1080\n\t"
        "s_add_u32 m0, s78, s81\n\t"
        "s_lshr_b32 s78, s80, 1\n\t"
        "s_lshl1_add_u32 s78, s78, s80\n\t"
        "s_lshl_b32 s78, s78, 11\n\t"
        "buffer_load_dwordx4 v4, s[16:19], s78 offen lds\n\t"
        "s_add_u32 s80, s80, 1\n\t"
        "s_mul_i32 s78, s80, 0x1080\n\t"
        "s_add_u32 m0, s78, s81\n\t"
        "s_lshr_b32 s78, s80, 1\n\t"
        "s_lshl1_add_u32 s78, s78, s80\n\t"
        "s_lshl_b32 s78, s78, 11\n\t"
        "buffer_load_dwordx4 v4, s[16:19], s78 offen lds\n\t"
        "s_add_u32 s80, s80, 1\n\t"
        "s_mul_i32 s78, s80, 0x1080\n\t"
        "s_add_u32 m0, s78, s81\n\t"
        "s_lshr_b32 s78, s80, 1\n\t"
        "s_lshl1_add_u32 s78, s78, s80\n\t"
        "s_lshl_b32 s78, s78, 11\n\t"
        "buffer_load_dwordx4 v4, s[16:19], s78 offen lds\n\t"
        "s_add_u32 s80, s80, 1\n\t"
        "s_lshl2_add_u32 s78, s82, s69\n\t"
        "s_lshl_b32 s78, s78, 10\n\t"
        "s_add_u32 m0, s78, 46336\n\t"
        "s_add_u32 s82, s82, 1\n\t"
        "s_cmp_eq_u32 s82, 5\n\t"
        "buffer_load_dwordx4 v5, s[56:59], s78 offen lds\n\t"
        "s_cselect_b32 s82, 0, s82\n\t"
        "s_lshl2_add_u32 s78, s82, s69\n\t"
        "s_lshl_b32 s78, s78, 10\n\t"
        "s_add_u32 m0, s78, 46336\n\t"
        "s_add_u32 s82, s82, 1\n\t"
        "s_cmp_eq_u32 s82, 5\n\t"
        "buffer_load_dwordx4 v5, s[56:59], s78 offen lds\n\t"
        "s_cselect_b32 s82, 0, s82\n\t"
        "s_lshl2_add_u32 s78, s82, s69\n\t"
        "s_lshl_b32 s78, s78, 10\n\t"
        "s_add_u32 m0, s78, 46336\n\t"
        "s_add_u32 s82, s82, 1\n\t"
        "s_cmp_eq_u32 s82, 5\n\t"
        "buffer_load_dwordx4 v5, s[56:59], s78 offen lds\n\t"
        "s_cselect_b32 s82, 0, s82\n\t"
        "s_lshl2_add_u32 s78, s82, s69\n\t"
        "s_lshl_b32 s78, s78, 10\n\t"
        "s_add_u32 m0, s78, 46336\n\t"
        "s_add_u32 s82, s82, 1\n\t"
        "s_cmp_eq_u32 s82, 5\n\t"
        "buffer_load_dwordx4 v5, s[56:59], s78 offen lds\n\t"
        "s_cselect_b32 s82, 0, s82\n\t"
        "s_waitcnt vmcnt(4)\n\t"
        "s_mov_b64 exec, 1\n\t"
        "ds_add_u32 v2, v3 offset:0\n\t"
        "s_mov_b64 exec, -1\n"
        ".L7_w72:\n\t"
        "ds_read_b64 v[12:13], v2 offset:72\n\t"
        "s_waitcnt lgkmcnt(0)\n\t"
        "v_cmp_gt_u16 vcc, 2, v12\n\t"
        "s_cbranch_vccz .L7_d73\n\t"
        "s_sleep 1\n\t"
        "s_sub_i32 s83, s83, 1\n\t"
        "s_cmp_gt_i32 s83, 0\n\t"
        "s_cbranch_scc1 .L7_w72\n"
        ".L7_d73:\n\t"
        "v_readfirstlane_b32 s68, v13\n"
        ".L7_w74:\n\t"
        "ds_read_b32 v0, v2 offset:8\n\t"
        "s_waitcnt lgkmcnt(0)\n\t"
        "v_cmp_gt_u16 vcc, 2, v0\n\t"
        "s_cbranch_vccz .L7_d75\n\t"
        "s_sleep 1\n\t"
        "s_sub_i32 s83, s83, 1\n\t"
        "s_cmp_gt_i32 s83, 0\n\t"
        "s_cbranch_scc1 .L7_w74\n"
        ".L7_d75:\n\t"
        "ds_read_b32 v5, v31 offset:512\n\t"
        "ds_read_b32 v24, v24 offset:512\n\t"
        "ds_read_b32 v25, v25 offset:512\n\t"
        "ds_read_b32 v26, v26 offset:512\n\t"
        "ds_read_b32 v27, v27 offset:512\n\t"
        "ds_read_b32 v28, v28 offset:512\n\t"
        "s_waitcnt lgkmcnt(0)\n\t"
        "v_dot4_u32_u8 v31, v5, s38, v7\n\t"
        "s_waitcnt vmcnt(0)\n\t"
        "s_mov_b64 exec, 1\n\t"
        "ds_add_u32 v2, v3 offset:4\n\t"
        "s_mov_b64 exec, -1\n\t"
        "s_setprio 3\n\t"
        "s_lshl_b32 s44, s68, 24\n\t"
        "s_xor_b32 s45, s44, 0x5a000000\n\t"
        "s_mul_i32 s46, s68, 0x01010000\n\t"
        "s_mul_i32 s47, s68, 0x4000400\n\t"
        "s_and_b32 s47, s47, 0xff000000\n\t"
        "s_or_b32 s47, s47, 0xff0000\n\t"
        "s_and_b32 s46, s46, 0xff000000\n\t"
        "s_mul_i32 s34, s32, 0xff\n\t"
        "s_lshl_b32 s78, 0xff, s33\n\t"
        "s_or_b32 s79, s78, s34\n\t"
        "s_andn2_b32 s79, 0x0c0c0c0c, s79\n\t"
        "s_and_b32 s78, s78, 0x03020100\n\t"
        "s_and_b32 s34, s34, 0x07060504\n\t"
        "s_or_b32 s34, s34, s78\n\t"
        "s_or_b32 s34, s34, s79\n"
        ".L7_w76:\n\t"
        "ds_read_b32 v0, v2 offset:92\n\t"
        "s_waitcnt lgkmcnt(0)\n\t"
        "v_cmp_gt_u16 vcc, 1, v0\n\t"
        "s_cbranch_vccz .L7_d77\n\t"
        "s_sleep 1\n\t"
        "s_sub_i32 s83, s83, 1\n\t"
        "s_cmp_gt_i32 s83, 0\n\t"
        "s_cbranch_scc1 .L7_w76\n"
        ".L7_d77:\n\t"
        "v_readfirstlane_b32 s78, v0\n\t"
        "s_sub_u32 s78, s78, 1\n\t"
        "s_add_u32 s77, s74, s78\n"
        ".L7_g78:\n\t"
        "s_memrealtime s[78:79]\n\t"
        "s_waitcnt lgkmcnt(0)\n\t"
        "s_sub_u32 s78, s78, s77\n\t"
        "s_cmp_ge_i32 s78, 305\n\t"
        "s_cbranch_scc1 .L7_h79\n\t"
        "s_sleep 1\n\t"
        "s_branch .L7_g78\n"
        ".L7_h79:\n\t"
        "buffer_load_dwordx4 v[64:67], v18, s[20:23], s24 offen sc1\n\t"
        "buffer_load_dwordx4 v[68:71], v18, s[20:23], s25 offen sc1\n\t"
        "buffer_load_dwordx4 v[72:75], v18, s[20:23], s26 offen sc1\n\t"
        "buffer_load_dwordx4 v[76:79], v18, s[20:23], s27 offen sc1\n"
        ".L7_w80:\n\t"
        "ds_read_b32 v0, v2 offset:4\n\t"
        "s_waitcnt lgkmcnt(0)\n\t"
        "v_cmp_gt_u16 vcc, 4, v0\n\t"
        "s_cbranch_vccz .L7_d81\n\t"
        "s_sleep 1\n\t"
        "s_sub_i32 s83, s83, 1\n\t"
        "s_cmp_gt_i32 s83, 0\n\t"
        "s_cbranch_scc1 .L7_w80\n"
        ".L7_d81:\n\t"
        "s_branch .L7_ll\n"
        ".L7_again:\n\t"
        "s_waitcnt lgkmcnt(0)\n\t"
        "s_sleep 8\n\t"
        "s_sub_i32 s83, s83, 16\n\t"
        "s_cmp_gt_i32 s83, 0\n\t"
        "s_cbranch_scc0 .L7_got\n\t"
        "buffer_load_dwordx4 v[64:67], v18, s[20:23], s24 offen sc1\n\t"
        "buffer_load_dwordx4 v[68:71], v18, s[20:23], s25 offen sc1\n\t"
        "buffer_load_dwordx4 v[72:75], v18, s[20:23], s26 offen sc1\n\t"
        "buffer_load_dwordx4 v[76:79], v18, s[20:23], s27 offen sc1\n"
        ".L7_ll:\n\t"
        "s_waitcnt vmcnt(3)\n\t"
        "v_xor_b32 v64, s44, v64\n\t"
        "v_xor_b32 v65, s45, v65\n\t"
        "v_xor_b32 v66, s46, v66\n\t"
        "v_xor_b32 v4, s47, v67\n\t"
        "v_or_b32 v5, v64, v65\n\t"
        "v_lshlrev_b32_sdwa v112, 1, v64 dst_sel:DWORD dst_unused:UNUSED_PAD src0_sel:DWORD src1_sel:WORD_0\n\t"
        "ds_read_u16 v112, v112 offset:46336\n\t"
        "v_lshlrev_b32_sdwa v113, 1, v65 dst_sel:DWORD dst_unused:UNUSED_PAD src0_sel:DWORD src1_sel:WORD_0\n\t"
        "ds_read_u16 v113, v113 offset:46336\n\t"
        "v_lshlrev_b32_sdwa v114, 1, v66 dst_sel:DWORD dst_unused:UNUSED_PAD src0_sel:DWORD src1_sel:WORD_0\n\t"
        "ds_read_u16 v114, v114 offset:46336\n\t"
        "v_mad_u32_u16 v80, v64, s38, v7 op_sel:[1,0,0,0]\n\t"
        "v_mad_u32_u16 v81, v65, s38, v7 op_sel:[1,0,0,0]\n\t"
        "v_mad_u32_u16 v82, v66, s38, v7 op_sel:[1,0,0,0]\n\t"
        "s_waitcnt vmcnt(2)\n\t"
        "v_xor_b32 v68, s44, v68\n\t"
        "v_xor_b32 v69, s45, v69\n\t"
        "v_xor_b32 v70, s46, v70\n\t"
        "v_bitop3_b32 v4, v71, v4, s47 bitop3:0xde\n\t"
        "v_or3_b32 v5, v5, v68, v69\n\t"
        "v_or3_b32 v5, v5, v66, v70\n\t"
        "v_lshlrev_b32_sdwa v116, 1, v68 dst_sel:DWORD dst_unused:UNUSED_PAD src0_sel:DWORD src1_sel:WORD_0\n\t"
        "ds_read_u16 v116, v116 offset:46336\n\t"
        "v_lshlrev_b32_sdwa v117, 1, v69 dst_sel:DWORD dst_unused:UNUSED_PAD src0_sel:DWORD src1_sel:WORD_0\n\t"
        "ds_read_u16 v117, v117 offset:46336\n\t"
        "v_lshlrev_b32_sdwa v118, 1, v70 dst_sel:DWORD dst_unused:UNUSED_PAD src0_sel:DWORD src1_sel:WORD_0\n\t"
        "ds_read_u16 v118, v118 offset:46336\n\t"
        "v_mad_u32_u16 v84, v68, s38, v7 op_sel:[1,0,0,0]\n\t"
        "v_mad_u32_u16 v85, v69, s38, v7 op_sel:[1,0,0,0]\n\t"
        "v_mad_u32_u16 v86, v70, s38, v7 op_sel:[1,0,0,0]\n\t"
        "s_waitcnt vmcnt(1)\n\t"
        "v_xor_b32 v72, s44, v72\n\t"
        "v_xor_b32 v73, s45, v73\n\t"
        "v_xor_b32 v74, s46, v74\n\t"
        "v_bitop3_b32 v4, v75, v4, s47 bitop3:0xde\n\t"
        "v_or3_b32 v5, v5, v72, v73\n\t"
        "v_lshlrev_b32_sdwa v120, 1, v72 dst_sel:DWORD dst_unused:UNUSED_PAD src0_sel:DWORD src1_sel:WORD_0\n\t"
        "ds_read_u16 v120, v120 offset:46336\n\t"
        "v_lshlrev_b32_sdwa v121, 1, v73 dst_sel:DWORD dst_unused:UNUSED_PAD src0_sel:DWORD src1_sel:WORD_0\n\t"
        "ds_read_u16 v121, v121 offset:46336\n\t"
        "v_lshlrev_b32_sdwa v122, 1, v74 dst_sel:DWORD dst_unused:UNUSED_PAD src0_sel:DWORD src1_sel:WORD_0\n\t"
        "ds_read_u16 v122, v122 offset:46336\n\t"
        "v_mad_u32_u16 v88, v72, s38, v7 op_sel:[1,0,0,0]\n\t"
        "v_mad_u32_u16 v89, v73, s38, v7 op_sel:[1,0,0,0]\n\t"
        "v_mad_u32_u16 v90, v74, s38, v7 op_sel:[1,0,0,0]\n\t"
        "s_waitcnt vmcnt(0)\n\t"
        "v_xor_b32 v76, s44, v76\n\t"
        "v_xor_b32 v77, s45, v77\n\t"
        "v_xor_b32 v78, s46, v78\n\t"
        "v_bitop3_b32 v4, v79, v4, s47 bitop3:0xde\n\t"
        "v_or3_b32 v5, v5, v76, v77\n\t"
        "v_or3_b32 v5, v5, v74, v78\n\t"
        "v_lshlrev_b32_sdwa v124, 1, v76 dst_sel:DWORD dst_unused:UNUSED_PAD src0_sel:DWORD src1_sel:WORD_0\n\t"
        "ds_read_u16 v124, v124 offset:46336\n\t"
        "v_lshlrev_b32_sdwa v125, 1, v77 dst_sel:DWORD dst_unused:UNUSED_PAD src0_sel:DWORD src1_sel:WORD_0\n\t"
        "ds_read_u16 v125, v125 offset:46336\n\t"
        "v_lshlrev_b32_sdwa v126, 1, v78 dst_sel:DWORD dst_unused:UNUSED_PAD src0_sel:DWORD src1_sel:WORD_0\n\t"
        "ds_read_u16 v126, v126 offset:46336\n\t"
        "v_mad_u32_u16 v92, v76, s38, v7 op_sel:[1,0,0,0]\n\t"
        "v_mad_u32_u16 v93, v77, s38, v7 op_sel:[1,0,0,0]\n\t"
        "v_mad_u32_u16 v94, v78, s38, v7 op_sel:[1,0,0,0]\n\t"
        "v_and_b32 v6, 0xfc000000, v4\n\t"
        "v_or_b32 v5, v5, v6\n\t"
        "v_cmp_lt_u32 vcc, s35, v5\n\t"
        "s_cbranch_vccnz .L7_again\n"
        ".L7_got:\n\t"
        "v_and_b32 v6, s35, v4\n\t"
        "s_waitcnt lgkmcnt(2)\n\t"
        "ds_add_rtn_u32 v96, v80, v29 offset:192\n\t"
        "ds_add_rtn_u32 v97, v81, v29 offset:192\n\t"
        "ds_add_rtn_u32 v98, v82, v29 offset:192\n\t"
        "ds_add_rtn_u32 v100, v84, v29 offset:192\n\t"
        "ds_add_rtn_u32 v101, v85, v29 offset:192\n\t"
        "ds_add_rtn_u32 v102, v86, v29 offset:192\n\t"
        "ds_add_rtn_u32 v104, v88, v29 offset:192\n\t"
        "ds_add_rtn_u32 v105, v89, v29 offset:192\n\t"
        "ds_add_rtn_u32 v106, v90, v29 offset:192\n\t"
        "ds_add_rtn_u32 v108, v92, v29 offset:192\n\t"
        "ds_add_rtn_u32 v109, v93, v29 offset:192\n\t"
        "ds_add_rtn_u32 v110, v94, v29 offset:192\n\t"
        "v_cmp_ne_u32 vcc, 0, v6\n\t"
        "s_cbranch_vccnz .L7_x\n"
        ".L7_y:\n\t"
        "s_mov_b64 exec, 1\n\t"
        "ds_add_u32 v2, v3 offset:32\n\t"
        "s_mov_b64 exec, -1\n\t"
        "v_perm_b32 v112, v112, v64, s49\n\t"
        "v_perm_b32 v113, v113, v65, s49\n\t"
        "v_perm_b32 v114, v114, v66, s49\n\t"
        "v_perm_b32 v116, v116, v68, s49\n\t"
        "v_perm_b32 v117, v117, v69, s49\n\t"
        "v_perm_b32 v118, v118, v70, s49\n\t"
        "v_perm_b32 v120, v120, v72, s49\n\t"
        "v_perm_b32 v121, v121, v73, s49\n\t"
        "v_perm_b32 v122, v122, v74, s49\n\t"
        "v_perm_b32 v124, v124, v76, s49\n\t"
        "s_cmp_lt_u32 s69, 1\n\t"
        "s_cbranch_scc0 .L7_nv\n\t"
        "s_mov_b32 s79, 0\n\t"
        "v_xor_b32 v32, s47, v67\n\t"
        "v_xor_b32 v33, s47, v71\n\t"
        "v_xor_b32 v34, s47, v75\n\t"
        "v_xor_b32 v35, s47, v79\n\t"
        "v_lshlrev_b32 v32, 6, v32\n\t"
        "v_lshlrev_b32 v33, 6, v33\n\t"
        "v_lshlrev_b32 v34, 6, v34\n\t"
        "v_lshlrev_b32 v35, 6, v35\n\t"
        "s_mov_b32 exec_lo, s37\n\t"
        "s_mov_b32 exec_hi, s37\n\t"
        "v_cmp_gt_i32 vcc, 0, v32\n\t"
        "s_bcnt1_i32_b64 s78, vcc\n\t"
        "s_add_u32 s79, s79, s78\n\t"
        "v_cmp_gt_i32 vcc, 0, v33\n\t"
        "s_bcnt1_i32_b64 s78, vcc\n\t"
        "s_add_u32 s79, s79, s78\n\t"
        "v_cmp_gt_i32 vcc, 0, v34\n\t"
        "s_bcnt1_i32_b64 s78, vcc\n\t"
        "s_add_u32 s79, s79, s78\n\t"
        "v_cmp_gt_i32 vcc, 0, v35\n\t"
        "s_bcnt1_i32_b64 s78, vcc\n\t"
        "s_add_u32 s79, s79, s78\n\t"
        "s_lshl_b32 s79, s79, 16\n\t"
        "s_or_b32 s79, s79, 1\n\t"
        "v_mov_b32 v32, s79\n\t"
        "s_waitcnt lgkmcnt(13)\n\t"
        "s_mov_b64 exec, 1\n\t"
        "ds_add_u32 v2, v32 offset:116\n\t"
        "s_mov_b64 exec, -1\n"
        ".L7_nv:\n\t"
        "v_lshlrev_b32 v4, 7, v4\n\t"
        "v_cmp_gt_i32 vcc, 0, v4\n\t"
        "s_cmp_lg_u64 vcc, 0\n\t"
        "s_cselect_b32 s78, 0x100000, 0\n\t"
        "v_or_b32 v30, s78, v30\n\t"
        "s_waitcnt lgkmcnt(13)\n\t"
        "v_perm_b32 v125, v125, v77, s49\n\t"
        "v_perm_b32 v126, v126, v78, s49\n"
        ".L7_w82:\n\t"
        "ds_read_b32 v0, v2 offset:32\n\t"
        "s_waitcnt lgkmcnt(0)\n\t"
        "v_cmp_gt_u16 vcc, 4, v0\n\t"
        "s_cbranch_vccz .L7_d83\n\t"
        "s_sleep 1\n\t"
        "s_sub_i32 s83, s83, 1\n\t"
        "s_cmp_gt_i32 s83, 0\n\t"
        "s_cbranch_scc1 .L7_w82\n"
        ".L7_d83:\n\t"
        "ds_read_b32 v32, v80 offset:192\n\t"
        "ds_read_b32 v33, v81 offset:192\n\t"
        "ds_read_b32 v34, v82 offset:192\n\t"
        "ds_read_b32 v35, v84 offset:192\n\t"
        "ds_read_b32 v36, v85 offset:192\n\t"
        "ds_read_b32 v37, v86 offset:192\n\t"
        "ds_read_b32 v38, v88 offset:192\n\t"
        "ds_read_b32 v39, v89 offset:192\n\t"
        "ds_read_b32 v40, v90 offset:192\n\t"
        "ds_read_b32 v41, v92 offset:192\n\t"
        "ds_read_b32 v42, v93 offset:192\n\t"
        "ds_read_b32 v43, v94 offset:192\n\t"
        "ds_read_b32 v6, v31 offset:192\n\t"
        "s_waitcnt lgkmcnt(10)\n\t"
        "v_perm_b32 v32, v32, v96, s34\n\t"
        "v_sad_u8 v96, v32, 0, 0\n\t"
        "v_perm_b32 v33, v33, v97, s34\n\t"
        "v_sad_u8 v97, v33, 0, 0\n\t"
        "v_perm_b32 v34, v34, v98, s34\n\t"
        "v_sad_u8 v98, v34, 0, 0\n\t"
        "s_waitcnt lgkmcnt(7)\n\t"
        "v_perm_b32 v35, v35, v100, s34\n\t"
        "v_sad_u8 v100, v35, 0, 0\n\t"
        "v_perm_b32 v36, v36, v101, s34\n\t"
        "v_sad_u8 v101, v36, 0, 0\n\t"
        "v_perm_b32 v37, v37, v102, s34\n\t"
        "v_sad_u8 v102, v37, 0, 0\n\t"
        "s_waitcnt lgkmcnt(4)\n\t"
        "v_perm_b32 v38, v38, v104, s34\n\t"
        "v_sad_u8 v104, v38, 0, 0\n\t"
        "v_perm_b32 v39, v39, v105, s34\n\t"
        "v_sad_u8 v105, v39, 0, 0\n\t"
        "v_perm_b32 v40, v40, v106, s34\n\t"
        "v_sad_u8 v106, v40, 0, 0\n\t"
        "s_waitcnt lgkmcnt(1)\n\t"
        "v_perm_b32 v41, v41, v108, s34\n\t"
        "v_sad_u8 v108, v41, 0, 0\n\t"
        "v_perm_b32 v42, v42, v109, s34\n\t"
        "v_sad_u8 v109, v42, 0, 0\n\t"
        "v_perm_b32 v43, v43, v110, s34\n\t"
        "v_sad_u8 v110, v43, 0, 0\n\t"
        "v_min_u32 v96, 47, v96\n\t"
        "v_min_u32 v97, 47, v97\n\t"
        "v_min_u32 v98, 47, v98\n\t"
        "v_min_u32 v100, 47, v100\n\t"
        "v_min_u32 v101, 47, v101\n\t"
        "v_min_u32 v102, 47, v102\n\t"
        "v_min_u32 v104, 47, v104\n\t"
        "v_min_u32 v105, 47, v105\n\t"
        "v_min_u32 v106, 47, v106\n\t"
        "v_min_u32 v108, 47, v108\n\t"
        "v_min_u32 v109, 47, v109\n\t"
        "v_min_u32 v110, 47, v110\n\t"
        "v_lshl_add_u32 v96, v96, 2, v80\n\t"
        "ds_write_b32 v96, v112\n\t"
        "v_lshl_add_u32 v97, v97, 2, v81\n\t"
        "ds_write_b32 v97, v113\n\t"
        "v_lshl_add_u32 v98, v98, 2, v82\n\t"
        "ds_write_b32 v98, v114\n\t"
        "v_lshl_add_u32 v100, v100, 2, v84\n\t"
        "ds_write_b32 v100, v116\n\t"
        "v_lshl_add_u32 v101, v101, 2, v85\n\t"
        "ds_write_b32 v101, v117\n\t"
        "v_lshl_add_u32 v102, v102, 2, v86\n\t"
        "ds_write_b32 v102, v118\n\t"
        "v_lshl_add_u32 v104, v104, 2, v88\n\t"
        "ds_write_b32 v104, v120\n\t"
        "v_lshl_add_u32 v105, v105, 2, v89\n\t"
        "ds_write_b32 v105, v121\n\t"
        "v_lshl_add_u32 v106, v106, 2, v90\n\t"
        "ds_write_b32 v106, v122\n\t"
        "v_lshl_add_u32 v108, v108, 2, v92\n\t"
        "ds_write_b32 v108, v124\n\t"
        "v_lshl_add_u32 v109, v109, 2, v93\n\t"
        "ds_write_b32 v109, v125\n\t"
        "v_lshl_add_u32 v110, v110, 2, v94\n\t"
        "ds_write_b32 v110, v126\n\t"
        "s_cmp_lg_u32 s36, 0\n\t"
        "s_cbranch_scc1 .L7_z\n"
        ".L7_zr:\n\t"
        "s_mov_b64 exec, 1\n\t"
        "ds_add_u32 v2, v30 offset:36\n\t"
        "s_mov_b64 exec, -1\n\t"
        "s_waitcnt lgkmcnt(13)\n\t"
        "v_sad_u8 v6, v6, 0, -1\n\t"
        "v_mov_b64 v[32:33], 0\n\t"
        "v_mov_b64 v[34:35], 0\n\t"
        "v_mov_b64 v[36:37], 0\n\t"
        "v_med3_i32 v6, v6, 0, 47\n\t"
        "v_lshrrev_b32 v6, 1, v6\n\t"
        "v_mov_b64 v[38:39], 0\n\t"
        "v_and_b32 v63, 0xff, v24\n\t"
        "v_mad_u32_u24 v63, v63, s38, v7\n\t"
        "v_max_u32_dpp v6, v6, v6 quad_perm:[1,0,3,2] row_mask:0xf bank_mask:0xf\n\t"
        "s_nop 1\n\t"
        "v_max_u32_dpp v6, v6, v6 quad_perm:[2,3,0,1] row_mask:0xf bank_mask:0xf\n\t"
        "s_nop 0\n\t"
        "v_readlane_b32 s24, v6, 0\n\t"
        "v_readlane_b32 s25, v6, 4\n\t"
        "v_readlane_b32 s26, v6, 8\n\t"
        "v_readlane_b32 s27, v6, 12\n\t"
        "v_readlane_b32 s28, v6, 16\n\t"
        "s_max_u32 s28, s28, 5\n\t"
        "s_mov_b32 s56, s24\n\t"
        "s_mov_b32 s57, s24\n\t"
        "s_mov_b32 s78, 0xf0000\n"
        ".L7_w84:\n\t"
        "ds_read_b32 v4, v2 offset:36\n\t"
        "ds_read_b32 v12, v31 offset:188\n\t"
        "s_waitcnt lgkmcnt(1)\n\t"
        "v_cmp_gt_u16 vcc, 4, v4\n\t"
        "s_cbranch_vccz .L7_d85\n\t"
        "s_waitcnt lgkmcnt(0)\n\t"
        "s_sleep 1\n\t"
        "s_sub_i32 s83, s83, 1\n\t"
        "s_cmp_gt_i32 s83, 0\n\t"
        "s_cbranch_scc1 .L7_w84\n"
        ".L7_d85:\n\t"
        "ds_read2_b32 v[40:41], v63 offset0:0 offset1:1\n\t"
        "s_sub_u32 s56, s56, 1\n\t"
        "s_cbranch_scc1 .L7_i86\n"
        ".L7_j87:\n\t"
        "ds_read2_b32 v[42:43], v63 offset0:2 offset1:3\n\t"
        "s_sub_u32 s56, s56, 1\n\t"
        "s_cbranch_scc1 .L7_i88\n"
        ".L7_j89:\n\t"
        "ds_read2_b32 v[44:45], v63 offset0:4 offset1:5\n\t"
        "s_sub_u32 s56, s56, 1\n\t"
        "s_cbranch_scc1 .L7_i90\n"
        ".L7_j91:\n\t"
        "ds_read2_b32 v[46:47], v63 offset0:6 offset1:7\n\t"
        "s_sub_u32 s56, s56, 1\n\t"
        "s_cbranch_scc1 .L7_i92\n"
        ".L7_j93:\n\t"
        "ds_read2_b32 v[48:49], v63 offset0:8 offset1:9\n\t"
        "s_sub_u32 s56, s56, 1\n\t"
        "s_cbranch_scc1 .L7_i94\n"
        ".L7_j95:\n\t"
        "ds_read2_b32 v[50:51], v63 offset0:10 offset1:11\n\t"
        "s_sub_u32 s56, s56, 1\n\t"
        "s_cbranch_scc1 .L7_i96\n"
        ".L7_j97:\n\t"
        "ds_read2_b32 v[52:53], v63 offset0:12 offset1:13\n\t"
        "s_sub_u32 s56, s56, 1\n\t"
        "s_cbranch_scc1 .L7_i98\n"
        ".L7_j99:\n\t"
        "ds_read2_b32 v[54:55], v63 offset0:14 offset1:15\n\t"
        "s_sub_u32 s56, s56, 1\n\t"
        "s_cbranch_scc1 .L7_i100\n"
        ".L7_j101:\n\t"
        "s_waitcnt lgkmcnt(8)\n\t"
        "v_and_or_b32 v12, v4, s78, v12\n\t"
        "v_cmp_ne_u32 vcc, 0xffff, v12\n\t"
        "s_cbranch_vccnz .L7_exit4\n\t"
        "s_waitcnt lgkmcnt(7)\n\t"
        "v_mad_u32_u16 v10, v40, s48, v62\n\t"
        "v_mad_u32_u16 v11, v41, s48, v62\n\t"
        "buffer_load_dwordx4 v[64:67], v10, s[16:19], 0 offen\n\t"
        "buffer_load_dwordx4 v[68:71], v11, s[16:19], 0 offen\n\t"
        "s_waitcnt lgkmcnt(6)\n\t"
        "v_mad_u32_u16 v10, v42, s48, v62\n\t"
        "v_mad_u32_u16 v11, v43, s48, v62\n\t"
        "buffer_load_dwordx4 v[72:75], v10, s[16:19], 0 offen\n\t"
        "buffer_load_dwordx4 v[76:79], v11, s[16:19], 0 offen\n\t"
        "s_waitcnt lgkmcnt(5)\n\t"
        "v_mad_u32_u16 v10, v44, s48, v62\n\t"
        "v_mad_u32_u16 v11, v45, s48, v62\n\t"
        "buffer_load_dwordx4 v[80:83], v10, s[16:19], 0 offen\n\t"
        "buffer_load_dwordx4 v[84:87], v11, s[16:19], 0 offen\n\t"
        "s_waitcnt lgkmcnt(4)\n\t"
        "v_mad_u32_u16 v10, v46, s48, v62\n\t"
        "v_mad_u32_u16 v11, v47, s48, v62\n\t"
        "buffer_load_dwordx4 v[88:91], v10, s[16:19], 0 offen\n\t"
        "buffer_load_dwordx4 v[92:95], v11, s[16:19], 0 offen\n\t"
        "s_waitcnt lgkmcnt(3)\n\t"
        "v_mad_u32_u16 v10, v48, s48, v62\n\t"
        "v_mad_u32_u16 v11, v49, s48, v62\n\t"
        "buffer_load_dwordx4 v[96:99], v10, s[16:19], 0 offen\n\t"
        "buffer_load_dwordx4 v[100:103], v11, s[16:19], 0 offen\n\t"
        "s_waitcnt lgkmcnt(2)\n\t"
        "v_mad_u32_u16 v10, v50, s48, v62\n\t"
        "v_mad_u32_u16 v11, v51, s48, v62\n\t"
        "buffer_load_dwordx4 v[104:107], v10, s[16:19], 0 offen\n\t"
        "buffer_load_dwordx4 v[108:111], v11, s[16:19], 0 offen\n\t"
        "s_min_u32 s59, s56, s57\n\t"
        "s_add_u32 s58, s59, 1\n\t"
        "s_setprio 2\n"
        ".L7_loop:\n\t"
        "v_add_u32 v63, 64, v63\n\t"
        "v_perm_b32 v12, v41, v40, v8\n\t"
        "v_perm_b32 v13, v41, v40, v9\n\t"
        "s_waitcnt vmcnt(10) lgkmcnt(1)\n\t"
        "v_mad_u32_u16 v10, v52, s48, v62\n\t"
        "v_mad_u32_u16 v11, v53, s48, v62\n\t"
        "buffer_load_dwordx4 v[112:115], v10, s[16:19], 0 offen\n\t"
        "buffer_load_dwordx4 v[116:119], v11, s[16:19], 0 offen\n\t"
        "v_mfma_f32_4x4x4_16b_bf16 v[32:35], v[12:13], v[64:65], v[32:35] cbsz:1 abid:0\n\t"
        "v_mfma_f32_4x4x4_16b_bf16 v[36:39], v[12:13], v[66:67], v[36:39] cbsz:1 abid:0\n\t"
        "ds_read2_b32 v[40:41], v63 offset0:0 offset1:1\n\t"
        "v_mfma_f32_4x4x4_16b_bf16 v[32:35], v[12:13], v[68:69], v[32:35] cbsz:1 abid:1\n\t"
        "v_mfma_f32_4x4x4_16b_bf16 v[36:39], v[12:13], v[70:71], v[36:39] cbsz:1 abid:1\n\t"
        "s_sub_u32 s59, s59, 1\n\t"
        "s_cbranch_scc1 .L7_i102\n"
        ".L7_j103:\n\t"
        "v_perm_b32 v12, v43, v42, v8\n\t"
        "v_perm_b32 v13, v43, v42, v9\n\t"
        "s_waitcnt vmcnt(10) lgkmcnt(1)\n\t"
        "v_mad_u32_u16 v10, v54, s48, v62\n\t"
        "v_mad_u32_u16 v11, v55, s48, v62\n\t"
        "buffer_load_dwordx4 v[120:123], v10, s[16:19], 0 offen\n\t"
        "buffer_load_dwordx4 v[124:127], v11, s[16:19], 0 offen\n\t"
        "v_mfma_f32_4x4x4_16b_bf16 v[32:35], v[12:13], v[72:73], v[32:35] cbsz:1 abid:0\n\t"
        "v_mfma_f32_4x4x4_16b_bf16 v[36:39], v[12:13], v[74:75], v[36:39] cbsz:1 abid:0\n\t"
        "ds_read2_b32 v[42:43], v63 offset0:2 offset1:3\n\t"
        "v_mfma_f32_4x4x4_16b_bf16 v[32:35], v[12:13], v[76:77], v[32:35] cbsz:1 abid:1\n\t"
        "v_mfma_f32_4x4x4_16b_bf16 v[36:39], v[12:13], v[78:79], v[36:39] cbsz:1 abid:1\n\t"
        "s_sub_u32 s59, s59, 1\n\t"
        "s_cbranch_scc1 .L7_i104\n"
        ".L7_j105:\n\t"
        "v_perm_b32 v12, v45, v44, v8\n\t"
        "v_perm_b32 v13, v45, v44, v9\n\t"
        "s_waitcnt vmcnt(10) lgkmcnt(1)\n\t"
        "v_mad_u32_u16 v10, v40, s48, v62\n\t"
        "v_mad_u32_u16 v11, v41, s48, v62\n\t"
        "buffer_load_dwordx4 v[64:67], v10, s[16:19], 0 offen\n\t"
        "buffer_load_dwordx4 v[68:71], v11, s[16:19], 0 offen\n\t"
        "v_mfma_f32_4x4x4_16b_bf16 v[32:35], v[12:13], v[80:81], v[32:35] cbsz:1 abid:0\n\t"
        "v_mfma_f32_4x4x4_16b_bf16 v[36:39], v[12:13], v[82:83], v[36:39] cbsz:1 abid:0\n\t"
        "ds_read2_b32 v[44:45], v63 offset0:4 offset1:5\n\t"
        "v_mfma_f32_4x4x4_16b_bf16 v[32:35], v[12:13], v[84:85], v[32:35] cbsz:1 abid:1\n\t"
        "v_mfma_f32_4x4x4_16b_bf16 v[36:39], v[12:13], v[86:87], v[36:39] cbsz:1 abid:1\n\t"
        "s_sub_u32 s59, s59, 1\n\t"
        "s_cbranch_scc1 .L7_i106\n"
        ".L7_j107:\n\t"
        "v_perm_b32 v12, v47, v46, v8\n\t"
        "v_perm_b32 v13, v47, v46, v9\n\t"
        "s_waitcnt vmcnt(10) lgkmcnt(1)\n\t"
        "v_mad_u32_u16 v10, v42, s48, v62\n\t"
        "v_mad_u32_u16 v11, v43, s48, v62\n\t"
        "buffer_load_dwordx4 v[72:75], v10, s[16:19], 0 offen\n\t"
        "buffer_load_dwordx4 v[76:79], v11, s[16:19], 0 offen\n\t"
        "v_mfma_f32_4x4x4_16b_bf16 v[32:35], v[12:13], v[88:89], v[32:35] cbsz:1 abid:0\n\t"
        "v_mfma_f32_4x4x4_16b_bf16 v[36:39], v[12:13], v[90:91], v[36:39] cbsz:1 abid:0\n\t"
        "ds_read2_b32 v[46:47], v63 offset0:6 offset1:7\n\t"
        "v_mfma_f32_4x4x4_16b_bf16 v[32:35], v[12:13], v[92:93], v[32:35] cbsz:1 abid:1\n\t"
        "v_mfma_f32_4x4x4_16b_bf16 v[36:39], v[12:13], v[94:95], v[36:39] cbsz:1 abid:1\n\t"
        "s_sub_u32 s59, s59, 1\n\t"
        "s_cbranch_scc1 .L7_i108\n"
        ".L7_j109:\n\t"
        "v_perm_b32 v12, v49, v48, v8\n\t"
        "v_perm_b32 v13, v49, v48, v9\n\t"
        "s_waitcnt vmcnt(10) lgkmcnt(1)\n\t"
        "v_mad_u32_u16 v10, v44, s48, v62\n\t"
        "v_mad_u32_u16 v11, v45, s48, v62\n\t"
        "buffer_load_dwordx4 v[80:83], v10, s[16:19], 0 offen\n\t"
        "buffer_load_dwordx4 v[84:87], v11, s[16:19], 0 offen\n\t"
        "v_mfma_f32_4x4x4_16b_bf16 v[32:35], v[12:13], v[96:97], v[32:35] cbsz:1 abid:0\n\t"
        "v_mfma_f32_4x4x4_16b_bf16 v[36:39], v[12:13], v[98:99], v[36:39] cbsz:1 abid:0\n\t"
        "ds_read2_b32 v[48:49], v63 offset0:8 offset1:9\n\t"
        "v_mfma_f32_4x4x4_16b_bf16 v[32:35], v[12:13], v[100:101], v[32:35] cbsz:1 abid:1\n\t"
        "v_mfma_f32_4x4x4_16b_bf16 v[36:39], v[12:13], v[102:103], v[36:39] cbsz:1 abid:1\n\t"
        "s_sub_u32 s59, s59, 1\n\t"
        "s_cbranch_scc1 .L7_i110\n"
        ".L7_j111:\n\t"
        "v_perm_b32 v12, v51, v50, v8\n\t"
        "v_perm_b32 v13, v51, v50, v9\n\t"
        "s_waitcnt vmcnt(10) lgkmcnt(1)\n\t"
        "v_mad_u32_u16 v10, v46, s48, v62\n\t"
        "v_mad_u32_u16 v11, v47, s48, v62\n\t"
        "buffer_load_dwordx4 v[88:91], v10, s[16:19], 0 offen\n\t"
        "buffer_load_dwordx4 v[92:95], v11, s[16:19], 0 offen\n\t"
        "v_mfma_f32_4x4x4_16b_bf16 v[32:35], v[12:13], v[104:105], v[32:35] cbsz:1 abid:0\n\t"
        "v_mfma_f32_4x4x4_16b_bf16 v[36:39], v[12:13], v[106:107], v[36:39] cbsz:1 abid:0\n\t"
        "ds_read2_b32 v[50:51], v63 offset0:10 offset1:11\n\t"
        "v_mfma_f32_4x4x4_16b_bf16 v[32:35], v[12:13], v[108:109], v[32:35] cbsz:1 abid:1\n\t"
        "v_mfma_f32_4x4x4_16b_bf16 v[36:39], v[12:13], v[110:111], v[36:39] cbsz:1 abid:1\n\t"
        "s_sub_u32 s59, s59, 1\n\t"
        "s_cbranch_scc1 .L7_i112\n"
        ".L7_j113:\n\t"
        "v_perm_b32 v12, v53, v52, v8\n\t"
        "v_perm_b32 v13, v53, v52, v9\n\t"
        "s_waitcnt vmcnt(10) lgkmcnt(1)\n\t"
        "v_mad_u32_u16 v10, v48, s48, v62\n\t"
        "v_mad_u32_u16 v11, v49, s48, v62\n\t"
        "buffer_load_dwordx4 v[96:99], v10, s[16:19], 0 offen\n\t"
        "buffer_load_dwordx4 v[100:103], v11, s[16:19], 0 offen\n\t"
        "v_mfma_f32_4x4x4_16b_bf16 v[32:35], v[12:13], v[112:113], v[32:35] cbsz:1 abid:0\n\t"
        "v_mfma_f32_4x4x4_16b_bf16 v[36:39], v[12:13], v[114:115], v[36:39] cbsz:1 abid:0\n\t"
        "ds_read2_b32 v[52:53], v63 offset0:12 offset1:13\n\t"
        "v_mfma_f32_4x4x4_16b_bf16 v[32:35], v[12:13], v[116:117], v[32:35] cbsz:1 abid:1\n\t"
        "v_mfma_f32_4x4x4_16b_bf16 v[36:39], v[12:13], v[118:119], v[36:39] cbsz:1 abid:1\n\t"
        "s_sub_u32 s59, s59, 1\n\t"
        "s_cbranch_scc1 .L7_i114\n"
        ".L7_j115:\n\t"
        "v_perm_b32 v12, v55, v54, v8\n\t"
        "v_perm_b32 v13, v55, v54, v9\n\t"
        "s_waitcnt vmcnt(10) lgkmcnt(1)\n\t"
        "v_mad_u32_u16 v10, v50, s48, v62\n\t"
        "v_mad_u32_u16 v11, v51, s48, v62\n\t"
        "buffer_load_dwordx4 v[104:107], v10, s[16:19], 0 offen\n\t"
        "buffer_load_dwordx4 v[108:111], v11, s[16:19], 0 offen\n\t"
        "v_mfma_f32_4x4x4_16b_bf16 v[32:35], v[12:13], v[120:121], v[32:35] cbsz:1 abid:0\n\t"
        "v_mfma_f32_4x4x4_16b_bf16 v[36:39], v[12:13], v[122:123], v[36:39] cbsz:1 abid:0\n\t"
        "ds_read2_b32 v[54:55], v63 offset0:14 offset1:15\n\t"
        "v_mfma_f32_4x4x4_16b_bf16 v[32:35], v[12:13], v[124:125], v[32:35] cbsz:1 abid:1\n\t"
        "v_mfma_f32_4x4x4_16b_bf16 v[36:39], v[12:13], v[126:127], v[36:39] cbsz:1 abid:1\n\t"
        "s_sub_u32 s59, s59, 1\n\t"
        "s_cbranch_scc0 .L7_loop\n\t"
        "s_branch .L7_i116\n"
        ".L7_i86:\n\t"
        "s_mov_b32 s60, 8\n\t"
        "s_swappc_b64 s[54:55], s[50:51]\n\t"
        "s_branch .L7_j87\n"
        ".L7_i88:\n\t"
        "s_mov_b32 s60, 16\n\t"
        "s_swappc_b64 s[54:55], s[50:51]\n\t"
        "s_branch .L7_j89\n"
        ".L7_i90:\n\t"
        "s_mov_b32 s60, 24\n\t"
        "s_swappc_b64 s[54:55], s[50:51]\n\t"
        "s_branch .L7_j91\n"
        ".L7_i92:\n\t"
        "s_mov_b32 s60, 32\n\t"
        "s_swappc_b64 s[54:55], s[50:51]\n\t"
        "s_branch .L7_j93\n"
        ".L7_i94:\n\t"
        "s_mov_b32 s60, 40\n\t"
        "s_swappc_b64 s[54:55], s[50:51]\n\t"
        "s_branch .L7_j95\n"
        ".L7_i96:\n\t"
        "s_mov_b32 s60, 48\n\t"
        "s_swappc_b64 s[54:55], s[50:51]\n\t"
        "s_branch .L7_j97\n"
        ".L7_i98:\n\t"
        "s_mov_b32 s60, 56\n\t"
        "s_swappc_b64 s[54:55], s[50:51]\n\t"
        "s_branch .L7_j99\n"
        ".L7_i100:\n\t"
        "s_mov_b32 s60, 64\n\t"
        "s_swappc_b64 s[54:55], s[50:51]\n\t"
        "s_branch .L7_j101\n"
        ".L7_i102:\n\t"
        "s_mov_b32 s60, 8\n\t"
        "s_swappc_b64 s[62:63], s[42:43]\n\t"
        "s_cbranch_vccnz .L7_dt1\n\t"
        "s_branch .L7_j103\n"
        ".L7_i104:\n\t"
        "s_mov_b32 s60, 16\n\t"
        "s_swappc_b64 s[62:63], s[42:43]\n\t"
        "s_cbranch_vccnz .L7_dt2\n\t"
        "s_branch .L7_j105\n"
        ".L7_i106:\n\t"
        "s_mov_b32 s60, 24\n\t"
        "s_swappc_b64 s[62:63], s[42:43]\n\t"
        "s_cbranch_vccnz .L7_dt3\n\t"
        "s_branch .L7_j107\n"
        ".L7_i108:\n\t"
        "s_mov_b32 s60, 32\n\t"
        "s_swappc_b64 s[62:63], s[42:43]\n\t"
        "s_cbranch_vccnz .L7_dt4\n\t"
        "s_branch .L7_j109\n"
        ".L7_i110:\n\t"
        "s_mov_b32 s60, 40\n\t"
        "s_swappc_b64 s[62:63], s[42:43]\n\t"
        "s_cbranch_vccnz .L7_dt5\n\t"
        "s_branch .L7_j111\n"
        ".L7_i112:\n\t"
        "s_mov_b32 s60, 48\n\t"
        "s_swappc_b64 s[62:63], s[42:43]\n\t"
        "s_cbranch_vccnz .L7_dt6\n\t"
        "s_branch .L7_j113\n"
        ".L7_i114:\n\t"
        "s_mov_b32 s60, 56\n\t"
        "s_swappc_b64 s[62:63], s[42:43]\n\t"
        "s_cbranch_vccnz .L7_dt7\n\t"
        "s_branch .L7_j115\n"
        ".L7_i116:\n\t"
        "s_mov_b32 s60, 64\n\t"
        "s_swappc_b64 s[62:63], s[42:43]\n\t"
        "s_cbranch_vccnz .L7_dt0\n\t"
        "s_branch .L7_loop\n"
        ".L7_ev:\n\t"
        "s_sub_u32 s56, s56, s58\n\t"
        "s_cbranch_scc0 .L7_e1\n\t"
        "s_swappc_b64 s[54:55], s[50:51]\n"
        ".L7_e1:\n\t"
        "s_sub_u32 s57, s57, s58\n\t"
        "s_cbranch_scc0 .L7_e2\n\t"
        "s_swappc_b64 s[54:55], s[52:53]\n"
        ".L7_e2:\n\t"
        "s_min_u32 s59, s56, s57\n\t"
        "s_add_u32 s58, s59, 1\n\t"
        "s_setpc_b64 s[62:63]\n"
        ".L7_dt0:\n\t"
        "v_perm_b32 v12, v41, v40, v8\n\t"
        "v_perm_b32 v13, v41, v40, v9\n\t"
        "s_waitcnt vmcnt(6)\n\t"
        "s_cmp_lt_u32 s57, 3\n\t"
        "s_cbranch_scc0 .L7_du0\n\t"
        "s_waitcnt vmcnt(2)\n\t"
        "s_cmp_eq_u32 s57, 0\n\t"
        "s_cbranch_scc0 .L7_du0\n\t"
        "s_waitcnt vmcnt(0)\n"
        ".L7_du0:\n\t"
        "v_mfma_f32_4x4x4_16b_bf16 v[32:35], v[12:13], v[64:65], v[32:35] cbsz:1 abid:0\n\t"
        "v_mfma_f32_4x4x4_16b_bf16 v[36:39], v[12:13], v[66:67], v[36:39] cbsz:1 abid:0\n\t"
        "s_sub_u32 s57, s57, 1\n\t"
        "v_mfma_f32_4x4x4_16b_bf16 v[32:35], v[12:13], v[68:69], v[32:35] cbsz:1 abid:1\n\t"
        "v_mfma_f32_4x4x4_16b_bf16 v[36:39], v[12:13], v[70:71], v[36:39] cbsz:1 abid:1\n\t"
        "s_cbranch_scc1 .L7_last\n"
        ".L7_dt1:\n\t"
        "v_perm_b32 v12, v43, v42, v8\n\t"
        "v_perm_b32 v13, v43, v42, v9\n\t"
        "s_waitcnt vmcnt(6)\n\t"
        "s_cmp_lt_u32 s57, 3\n\t"
        "s_cbranch_scc0 .L7_du1\n\t"
        "s_waitcnt vmcnt(2)\n\t"
        "s_cmp_eq_u32 s57, 0\n\t"
        "s_cbranch_scc0 .L7_du1\n\t"
        "s_waitcnt vmcnt(0)\n"
        ".L7_du1:\n\t"
        "v_mfma_f32_4x4x4_16b_bf16 v[32:35], v[12:13], v[72:73], v[32:35] cbsz:1 abid:0\n\t"
        "v_mfma_f32_4x4x4_16b_bf16 v[36:39], v[12:13], v[74:75], v[36:39] cbsz:1 abid:0\n\t"
        "s_sub_u32 s57, s57, 1\n\t"
        "v_mfma_f32_4x4x4_16b_bf16 v[32:35], v[12:13], v[76:77], v[32:35] cbsz:1 abid:1\n\t"
        "v_mfma_f32_4x4x4_16b_bf16 v[36:39], v[12:13], v[78:79], v[36:39] cbsz:1 abid:1\n\t"
        "s_cbranch_scc1 .L7_last\n"
        ".L7_dt2:\n\t"
        "v_perm_b32 v12, v45, v44, v8\n\t"
        "v_perm_b32 v13, v45, v44, v9\n\t"
        "s_waitcnt vmcnt(6)\n\t"
        "s_cmp_lt_u32 s57, 3\n\t"
        "s_cbranch_scc0 .L7_du2\n\t"
        "s_waitcnt vmcnt(2)\n\t"
        "s_cmp_eq_u32 s57, 0\n\t"
        "s_cbranch_scc0 .L7_du2\n\t"
        "s_waitcnt vmcnt(0)\n"
        ".L7_du2:\n\t"
        "v_mfma_f32_4x4x4_16b_bf16 v[32:35], v[12:13], v[80:81], v[32:35] cbsz:1 abid:0\n\t"
        "v_mfma_f32_4x4x4_16b_bf16 v[36:39], v[12:13], v[82:83], v[36:39] cbsz:1 abid:0\n\t"
        "s_sub_u32 s57, s57, 1\n\t"
        "v_mfma_f32_4x4x4_16b_bf16 v[32:35], v[12:13], v[84:85], v[32:35] cbsz:1 abid:1\n\t"
        "v_mfma_f32_4x4x4_16b_bf16 v[36:39], v[12:13], v[86:87], v[36:39] cbsz:1 abid:1\n\t"
        "s_cbranch_scc1 .L7_last\n"
        ".L7_dt3:\n\t"
        "v_perm_b32 v12, v47, v46, v8\n\t"
        "v_perm_b32 v13, v47, v46, v9\n\t"
        "s_waitcnt vmcnt(6)\n\t"
        "s_cmp_lt_u32 s57, 3\n\t"
        "s_cbranch_scc0 .L7_du3\n\t"
        "s_waitcnt vmcnt(2)\n\t"
        "s_cmp_eq_u32 s57, 0\n\t"
        "s_cbranch_scc0 .L7_du3\n\t"
        "s_waitcnt vmcnt(0)\n"
        ".L7_du3:\n\t"
        "v_mfma_f32_4x4x4_16b_bf16 v[32:35], v[12:13], v[88:89], v[32:35] cbsz:1 abid:0\n\t"
        "v_mfma_f32_4x4x4_16b_bf16 v[36:39], v[12:13], v[90:91], v[36:39] cbsz:1 abid:0\n\t"
        "s_sub_u32 s57, s57, 1\n\t"
        "v_mfma_f32_4x4x4_16b_bf16 v[32:35], v[12:13], v[92:93], v[32:35] cbsz:1 abid:1\n\t"
        "v_mfma_f32_4x4x4_16b_bf16 v[36:39], v[12:13], v[94:95], v[36:39] cbsz:1 abid:1\n\t"
        "s_cbranch_scc1 .L7_last\n"
        ".L7_dt4:\n\t"
        "v_perm_b32 v12, v49, v48, v8\n\t"
        "v_perm_b32 v13, v49, v48, v9\n\t"
        "s_waitcnt vmcnt(6)\n\t"
        "s_cmp_lt_u32 s57, 3\n\t"
        "s_cbranch_scc0 .L7_du4\n\t"
        "s_waitcnt vmcnt(2)\n\t"
        "s_cmp_eq_u32 s57, 0\n\t"
        "s_cbranch_scc0 .L7_du4\n\t"
        "s_waitcnt vmcnt(0)\n"
        ".L7_du4:\n\t"
        "v_mfma_f32_4x4x4_16b_bf16 v[32:35], v[12:13], v[96:97], v[32:35] cbsz:1 abid:0\n\t"
        "v_mfma_f32_4x4x4_16b_bf16 v[36:39], v[12:13], v[98:99], v[36:39] cbsz:1 abid:0\n\t"
        "s_sub_u32 s57, s57, 1\n\t"
        "v_mfma_f32_4x4x4_16b_bf16 v[32:35], v[12:13], v[100:101], v[32:35] cbsz:1 abid:1\n\t"
        "v_mfma_f32_4x4x4_16b_bf16 v[36:39], v[12:13], v[102:103], v[36:39] cbsz:1 abid:1\n\t"
        "s_cbranch_scc1 .L7_last\n"
        ".L7_dt5:\n\t"
        "v_perm_b32 v12, v51, v50, v8\n\t"
        "v_perm_b32 v13, v51, v50, v9\n\t"
        "s_waitcnt vmcnt(6)\n\t"
        "s_cmp_lt_u32 s57, 3\n\t"
        "s_cbranch_scc0 .L7_du5\n\t"
        "s_waitcnt vmcnt(2)\n\t"
        "s_cmp_eq_u32 s57, 0\n\t"
        "s_cbranch_scc0 .L7_du5\n\t"
        "s_waitcnt vmcnt(0)\n"
        ".L7_du5:\n\t"
        "v_mfma_f32_4x4x4_16b_bf16 v[32:35], v[12:13], v[104:105], v[32:35] cbsz:1 abid:0\n\t"
        "v_mfma_f32_4x4x4_16b_bf16 v[36:39], v[12:13], v[106:107], v[36:39] cbsz:1 abid:0\n\t"
        "s_sub_u32 s57, s57, 1\n\t"
        "v_mfma_f32_4x4x4_16b_bf16 v[32:35], v[12:13], v[108:109], v[32:35] cbsz:1 abid:1\n\t"
        "v_mfma_f32_4x4x4_16b_bf16 v[36:39], v[12:13], v[110:111], v[36:39] cbsz:1 abid:1\n\t"
        "s_cbranch_scc1 .L7_last\n"
        ".L7_dt6:\n\t"
        "v_perm_b32 v12, v53, v52, v8\n\t"
        "v_perm_b32 v13, v53, v52, v9\n\t"
        "s_waitcnt vmcnt(6)\n\t"
        "s_cmp_lt_u32 s57, 3\n\t"
        "s_cbranch_scc0 .L7_du6\n\t"
        "s_waitcnt vmcnt(2)\n\t"
        "s_cmp_eq_u32 s57, 0\n\t"
        "s_cbranch_scc0 .L7_du6\n\t"
        "s_waitcnt vmcnt(0)\n"
        ".L7_du6:\n\t"
        "v_mfma_f32_4x4x4_16b_bf16 v[32:35], v[12:13], v[112:113], v[32:35] cbsz:1 abid:0\n\t"
        "v_mfma_f32_4x4x4_16b_bf16 v[36:39], v[12:13], v[114:115], v[36:39] cbsz:1 abid:0\n\t"
        "s_sub_u32 s57, s57, 1\n\t"
        "v_mfma_f32_4x4x4_16b_bf16 v[32:35], v[12:13], v[116:117], v[32:35] cbsz:1 abid:1\n\t"
        "v_mfma_f32_4x4x4_16b_bf16 v[36:39], v[12:13], v[118:119], v[36:39] cbsz:1 abid:1\n\t"
        "s_cbranch_scc1 .L7_last\n"
        ".L7_dt7:\n\t"
        "v_perm_b32 v12, v55, v54, v8\n\t"
        "v_perm_b32 v13, v55, v54, v9\n\t"
        "s_waitcnt vmcnt(6)\n\t"
        "s_cmp_lt_u32 s57, 3\n\t"
        "s_cbranch_scc0 .L7_du7\n\t"
        "s_waitcnt vmcnt(2)\n\t"
        "s_cmp_eq_u32 s57, 0\n\t"
        "s_cbranch_scc0 .L7_du7\n\t"
        "s_waitcnt vmcnt(0)\n"
        ".L7_du7:\n\t"
        "v_mfma_f32_4x4x4_16b_bf16 v[32:35], v[12:13], v[120:121], v[32:35] cbsz:1 abid:0\n\t"
        "v_mfma_f32_4x4x4_16b_bf16 v[36:39], v[12:13], v[122:123], v[36:39] cbsz:1 abid:0\n\t"
        "s_sub_u32 s57, s57, 1\n\t"
        "v_mfma_f32_4x4x4_16b_bf16 v[32:35], v[12:13], v[124:125], v[32:35] cbsz:1 abid:1\n\t"
        "v_mfma_f32_4x4x4_16b_bf16 v[36:39], v[12:13], v[126:127], v[36:39] cbsz:1 abid:1\n\t"
        "s_cbranch_scc1 .L7_last\n\t"
        "s_branch .L7_dt0\n"
        ".L7_ie:\n\t"
        "v_and_b32 v63, 0xff, v25\n\t"
        "v_mad_u32_u24 v63, v63, s38, v7\n\t"
        "v_subrev_u32 v63, s60, v63\n\t"
        "s_mov_b32 s56, s25\n\t"
        "s_swappc_b64 s[50:51], s[54:55]\n\t"
        "v_and_b32 v63, 0xff, v26\n\t"
        "v_mad_u32_u24 v63, v63, s38, v7\n\t"
        "v_subrev_u32 v63, s60, v63\n\t"
        "s_mov_b32 s56, s26\n\t"
        "s_swappc_b64 s[50:51], s[54:55]\n\t"
        "v_and_b32 v63, 0xff, v27\n\t"
        "v_mad_u32_u24 v63, v63, s38, v7\n\t"
        "v_subrev_u32 v63, s60, v63\n\t"
        "s_mov_b32 s56, s27\n\t"
        "s_swappc_b64 s[50:51], s[54:55]\n\t"
        "v_and_b32 v63, 0xff, v28\n\t"
        "v_mad_u32_u24 v63, v63, s38, v7\n\t"
        "v_subrev_u32 v63, s60, v63\n\t"
        "s_add_u32 s56, s28, 2\n\t"
        "s_swappc_b64 s[50:51], s[54:55]\n\t"
        "s_mov_b64 vcc, -1\n\t"
        "s_setpc_b64 s[54:55]\n"
        ".L7_ce:\n\t"
        "v_and_b32 v4, 0xffff0000, v24\n\t"
        "s_nop 2\n\t"
        "v_pk_mul_f32 v[32:33], v[32:33], v[4:5] op_sel_hi:[1,0]\n\t"
        "v_pk_mul_f32 v[34:35], v[34:35], v[4:5] op_sel_hi:[1,0]\n\t"
        "v_pk_mul_f32 v[36:37], v[36:37], v[4:5] op_sel_hi:[1,0]\n\t"
        "v_pk_mul_f32 v[38:39], v[38:39], v[4:5] op_sel_hi:[1,0]\n\t"
        "v_cvt_pk_bf16_f32 v12, v32, v33\n\t"
        "v_cvt_pk_bf16_f32 v13, v34, v35\n\t"
        "v_cvt_pk_bf16_f32 v14, v36, v37\n\t"
        "v_cvt_pk_bf16_f32 v15, v38, v39\n"
        ".L7_w117:\n\t"
        "ds_read_b32 v4, v2 offset:12\n\t"
        "s_waitcnt lgkmcnt(0)\n\t"
        "v_cmp_gt_u16 vcc, 4, v4\n\t"
        "s_cbranch_vccz .L7_d118\n\t"
        "s_sleep 1\n\t"
        "s_sub_i32 s83, s83, 1\n\t"
        "s_cmp_gt_i32 s83, 0\n\t"
        "s_cbranch_scc1 .L7_w117\n"
        ".L7_d118:\n\t"
        "s_mov_b64 vcc, 0\n\t"
        "s_setprio 3\n\t"
        "ds_write_b128 v56, v[12:15] offset:23808\n\t"
        "s_mov_b64 exec, 1\n\t"
        "ds_add_u32 v2, v3 offset:52\n\t"
        "s_mov_b64 exec, -1\n\t"
        "v_mov_b64 v[32:33], 0\n\t"
        "v_mov_b64 v[34:35], 0\n\t"
        "v_mov_b64 v[36:37], 0\n\t"
        "v_mov_b64 v[38:39], 0\n\t"
        "s_mov_b32 s57, s25\n\t"
        "s_swappc_b64 s[52:53], s[54:55]\n\t"
        "v_and_b32 v4, 0xffff0000, v25\n\t"
        "s_nop 2\n\t"
        "v_pk_mul_f32 v[32:33], v[32:33], v[4:5] op_sel_hi:[1,0]\n\t"
        "v_pk_mul_f32 v[34:35], v[34:35], v[4:5] op_sel_hi:[1,0]\n\t"
        "v_pk_mul_f32 v[36:37], v[36:37], v[4:5] op_sel_hi:[1,0]\n\t"
        "v_pk_mul_f32 v[38:39], v[38:39], v[4:5] op_sel_hi:[1,0]\n\t"
        "v_cvt_pk_bf16_f32 v12, v32, v33\n\t"
        "v_cvt_pk_bf16_f32 v13, v34, v35\n\t"
        "v_cvt_pk_bf16_f32 v14, v36, v37\n\t"
        "v_cvt_pk_bf16_f32 v15, v38, v39\n\t"
        "ds_write_b128 v57, v[12:15] offset:23808\n\t"
        "s_mov_b64 exec, 1\n\t"
        "ds_add_u32 v61, v3 offset:52\n\t"
        "s_mov_b64 exec, -1\n\t"
        "v_mov_b64 v[32:33], 0\n\t"
        "v_mov_b64 v[34:35], 0\n\t"
        "v_mov_b64 v[36:37], 0\n\t"
        "v_mov_b64 v[38:39], 0\n\t"
        "s_mov_b32 s57, s26\n\t"
        "s_swappc_b64 s[52:53], s[54:55]\n\t"
        "v_and_b32 v4, 0xffff0000, v26\n\t"
        "s_nop 2\n\t"
        "v_pk_mul_f32 v[32:33], v[32:33], v[4:5] op_sel_hi:[1,0]\n\t"
        "v_pk_mul_f32 v[34:35], v[34:35], v[4:5] op_sel_hi:[1,0]\n\t"
        "v_pk_mul_f32 v[36:37], v[36:37], v[4:5] op_sel_hi:[1,0]\n\t"
        "v_pk_mul_f32 v[38:39], v[38:39], v[4:5] op_sel_hi:[1,0]\n\t"
        "v_cvt_pk_bf16_f32 v12, v32, v33\n\t"
        "v_cvt_pk_bf16_f32 v13, v34, v35\n\t"
        "v_cvt_pk_bf16_f32 v14, v36, v37\n\t"
        "v_cvt_pk_bf16_f32 v15, v38, v39\n\t"
        "ds_write_b128 v58, v[12:15] offset:23808\n\t"
        "s_mov_b64 exec, 1\n\t"
        "ds_add_u32 v61, v3 offset:56\n\t"
        "s_mov_b64 exec, -1\n\t"
        "v_mov_b64 v[32:33], 0\n\t"
        "v_mov_b64 v[34:35], 0\n\t"
        "v_mov_b64 v[36:37], 0\n\t"
        "v_mov_b64 v[38:39], 0\n\t"
        "s_mov_b32 s57, s27\n\t"
        "s_swappc_b64 s[52:53], s[54:55]\n\t"
        "v_and_b32 v4, 0xffff0000, v27\n\t"
        "s_nop 2\n\t"
        "v_pk_mul_f32 v[32:33], v[32:33], v[4:5] op_sel_hi:[1,0]\n\t"
        "v_pk_mul_f32 v[34:35], v[34:35], v[4:5] op_sel_hi:[1,0]\n\t"
        "v_pk_mul_f32 v[36:37], v[36:37], v[4:5] op_sel_hi:[1,0]\n\t"
        "v_pk_mul_f32 v[38:39], v[38:39], v[4:5] op_sel_hi:[1,0]\n\t"
        "v_cvt_pk_bf16_f32 v12, v32, v33\n\t"
        "v_cvt_pk_bf16_f32 v13, v34, v35\n\t"
        "v_cvt_pk_bf16_f32 v14, v36, v37\n\t"
        "v_cvt_pk_bf16_f32 v15, v38, v39\n\t"
        "ds_write_b128 v59, v[12:15] offset:21760\n\t"
        "s_mov_b64 exec, 1\n\t"
        "ds_add_u32 v2, v3 offset:64\n\t"
        "s_mov_b64 exec, -1\n\t"
        "v_mov_b64 v[32:33], 0\n\t"
        "v_mov_b64 v[34:35], 0\n\t"
        "v_mov_b64 v[36:37], 0\n\t"
        "v_mov_b64 v[38:39], 0\n\t"
        "s_mov_b32 s57, s28\n\t"
        "s_swappc_b64 s[52:53], s[54:55]\n"
        ".L7_last:\n\t"
        "v_and_b32 v4, 0xffff0000, v28\n\t"
        "s_nop 2\n\t"
        "v_pk_mul_f32 v[32:33], v[32:33], v[4:5] op_sel_hi:[1,0]\n\t"
        "v_pk_mul_f32 v[34:35], v[34:35], v[4:5] op_sel_hi:[1,0]\n\t"
        "v_pk_mul_f32 v[36:37], v[36:37], v[4:5] op_sel_hi:[1,0]\n\t"
        "v_pk_mul_f32 v[38:39], v[38:39], v[4:5] op_sel_hi:[1,0]\n\t"
        "v_cvt_pk_bf16_f32 v12, v32, v33\n\t"
        "v_cvt_pk_bf16_f32 v13, v34, v35\n\t"
        "v_cvt_pk_bf16_f32 v14, v36, v37\n\t"
        "v_cvt_pk_bf16_f32 v15, v38, v39\n\t"
        "ds_write_b128 v60, v[12:15] offset:21760\n\t"
        "s_mov_b64 exec, 1\n\t"
        "ds_add_u32 v2, v3 offset:68\n\t"
        "s_mov_b64 exec, -1\n\t"
        "s_endpgm\n"
        ".L7_x:\n\t"
        "s_mov_b32 s36, 1\n\t"
        "s_waitcnt lgkmcnt(0)\n\t"
        "v_dot4_u32_u8 v83, v67, s39, v7\n\t"
        "v_lshlrev_b32_sdwa v115, 1, v67 dst_sel:DWORD dst_unused:UNUSED_PAD src0_sel:DWORD src1_sel:WORD_0\n\t"
        "ds_read_u16 v115, v115 offset:46336\n\t"
        "v_cmp_eq_u16 vcc, -1, v67\n\t"
        "s_cmp_lg_u64 vcc, 0\n\t"
        "s_cselect_b32 s78, 0x10000, 0\n\t"
        "v_or_b32 v30, s78, v30\n\t"
        "ds_add_rtn_u32 v99, v83, v29 offset:192\n\t"
        "v_dot4_u32_u8 v87, v71, s39, v7\n\t"
        "v_lshlrev_b32_sdwa v119, 1, v71 dst_sel:DWORD dst_unused:UNUSED_PAD src0_sel:DWORD src1_sel:WORD_0\n\t"
        "ds_read_u16 v119, v119 offset:46336\n\t"
        "v_cmp_eq_u16 vcc, -1, v71\n\t"
        "s_cmp_lg_u64 vcc, 0\n\t"
        "s_cselect_b32 s78, 0x10000, 0\n\t"
        "v_or_b32 v30, s78, v30\n\t"
        "ds_add_rtn_u32 v103, v87, v29 offset:192\n\t"
        "v_dot4_u32_u8 v91, v75, s39, v7\n\t"
        "v_lshlrev_b32_sdwa v123, 1, v75 dst_sel:DWORD dst_unused:UNUSED_PAD src0_sel:DWORD src1_sel:WORD_0\n\t"
        "ds_read_u16 v123, v123 offset:46336\n\t"
        "v_cmp_eq_u16 vcc, -1, v75\n\t"
        "s_cmp_lg_u64 vcc, 0\n\t"
        "s_cselect_b32 s78, 0x10000, 0\n\t"
        "v_or_b32 v30, s78, v30\n\t"
        "ds_add_rtn_u32 v107, v91, v29 offset:192\n\t"
        "v_dot4_u32_u8 v95, v79, s39, v7\n\t"
        "v_lshlrev_b32_sdwa v127, 1, v79 dst_sel:DWORD dst_unused:UNUSED_PAD src0_sel:DWORD src1_sel:WORD_0\n\t"
        "ds_read_u16 v127, v127 offset:46336\n\t"
        "v_cmp_eq_u16 vcc, -1, v79\n\t"
        "s_cmp_lg_u64 vcc, 0\n\t"
        "s_cselect_b32 s78, 0x10000, 0\n\t"
        "v_or_b32 v30, s78, v30\n\t"
        "ds_add_rtn_u32 v111, v95, v29 offset:192\n\t"
        "s_branch .L7_y\n"
        ".L7_z:\n\t"
        "s_waitcnt lgkmcnt(0)\n\t"
        "ds_read_b32 v32, v83 offset:192\n\t"
        "ds_read_b32 v33, v87 offset:192\n\t"
        "ds_read_b32 v34, v91 offset:192\n\t"
        "ds_read_b32 v35, v95 offset:192\n\t"
        "v_perm_b32 v115, v115, v67, s49\n\t"
        "v_perm_b32 v119, v119, v71, s49\n\t"
        "v_perm_b32 v123, v123, v75, s49\n\t"
        "v_perm_b32 v127, v127, v79, s49\n\t"
        "s_waitcnt lgkmcnt(0)\n\t"
        "v_perm_b32 v32, v32, v99, s34\n\t"
        "v_sad_u8 v99, v32, 0, 0\n\t"
        "v_perm_b32 v33, v33, v103, s34\n\t"
        "v_sad_u8 v103, v33, 0, 0\n\t"
        "v_perm_b32 v34, v34, v107, s34\n\t"
        "v_sad_u8 v107, v34, 0, 0\n\t"
        "v_perm_b32 v35, v35, v111, s34\n\t"
        "v_sad_u8 v111, v35, 0, 0\n\t"
        "v_min_u32 v99, 47, v99\n\t"
        "v_min_u32 v103, 47, v103\n\t"
        "v_min_u32 v107, 47, v107\n\t"
        "v_min_u32 v111, 47, v111\n\t"
        "v_lshl_add_u32 v99, v99, 2, v83\n\t"
        "ds_write_b32 v99, v115\n\t"
        "v_lshl_add_u32 v103, v103, 2, v87\n\t"
        "ds_write_b32 v103, v119\n\t"
        "v_lshl_add_u32 v107, v107, 2, v91\n\t"
        "ds_write_b32 v107, v123\n\t"
        "v_lshl_add_u32 v111, v111, 2, v95\n\t"
        "ds_write_b32 v111, v127\n\t"
        "s_branch .L7_zr\n"
        ".L7_exit4:\n\t"
        "s_add_u32 s69, s69, 4\n"
        ".L7_exit:\n\t"
        "s_waitcnt vmcnt(0) lgkmcnt(0)\n\t"
        "s_setprio 0\n\t"
        : "+{v0}"(tid), "=&{s69}"(wv)
        : "s"(__builtin_amdgcn_kernarg_segment_ptr()), "s"(blockIdx.x)
        : "scc", "vcc", "memory", "s16", "s17", "s18", "s19", "s20", "s21", "s22", "s23", "s24", "s25", "s26", "s27", "s28", "s29", "s30", "s31", "s32", "s33", "s34", "s35",
          "s36", "s37", "s38", "s39", "s40", "s41", "s42", "s43", "s44", "s45", "s46", "s47", "s48", "s49", "s50", "s51", "s52", "s53", "s54", "s55",
          "s56", "s57", "s58", "s59", "s60", "s61", "s62", "s63", "s64", "s65", "s66", "s67", "s68", "s70", "s71", "s72", "s73", "s74", "s75", "s76",
          "s77", "s78", "s79", "s80", "s81", "s82", "s83", "s84", "s85", "s86", "s87", "s88", "s89", "s90", "s91", "s92", "s93", "s94", "s95", "s96",
          "s97", "s98", "s99", "s100", "s101", "v1", "v2", "v3", "v4", "v5", "v6", "v7", "v8", "v9", "v10", "v11", "v12", "v13", "v14", "v15",
          "v16", "v17", "v18", "v19", "v20", "v21", "v22", "v23", "v24", "v25", "v26", "v27", "v28", "v29", "v30", "v31", "v32", "v33", "v34", "v35",
          "v36", "v37", "v38", "v39", "v40", "v41", "v42", "v43", "v44", "v45", "v46", "v47", "v48", "v49", "v50", "v51", "v52", "v53", "v54", "v55",
          "v56", "v57", "v58", "v59", "v60", "v61", "v62", "v63", "v64", "v65", "v66", "v67", "v68", "v69", "v70", "v71", "v72", "v73", "v74", "v75",
          "v76", "v77", "v78", "v79", "v80", "v81", "v82", "v83", "v84", "v85", "v86", "v87", "v88", "v89", "v90", "v91", "v92", "v93", "v94", "v95",
          "v96", "v97", "v98", "v99", "v100", "v101", "v102", "v103", "v104", "v105", "v106", "v107", "v108", "v109", "v110", "v111", "v112", "v113", "v114", "v115",
          "v116", "v117", "v118", "v119", "v120", "v121", "v122", "v123", "v124", "v125", "v126", "v127");
    const __attribute__((address_space(4))) Args* q = (const __attribute__((address_space(4))) Args*)__builtin_amdgcn_kernarg_segment_ptr();
    asm volatile("" : "+s"(q));
    const u32 wave = __builtin_amdgcn_readfirstlane(wv);
    u32 lane;
    asm volatile("v_mbcnt_lo_u32_b32 %0, -1, 0\n\tv_mbcnt_hi_u32_b32 %0, -1, %0" : "=v"(lane));
    const u32 half = blockIdx.x & 1u, TN = q->TN, r0 = (blockIdx.x >> 1) * TN;
    const u16* x = q->xall;
    _Pragma("nounroll") for (u32 n = 0; n < (1u << 26) && *(volatile __attribute__((address_space(3))) u32*)S_QK < 4u; ++n) __builtin_amdgcn_s_sleep(2);
    if (wave >= 4u) {
        const int E = q->E, *dst = (const int*)q->dst.p, *src = (const int*)q->src.p;
        const u16* dis = q->disall;
        const u32 w = wave - 4u;
        _Pragma("nounroll") for (u32 k = 0; k < 5u; ++k) {
            const u32 grp = (u32)(((w == 0u ? 0x1713050109ull : w == 1u ? 0x1612040008ull : w == 2u ? 0x14100e0602ull : 0x15110f0703ull) >> (8u * k)) & 255u);
            const u32 row = grp * 4u + (lane >> 4), iv = *(const u32*)(smem + L_INV + row * 4u);
            const int me = (iv & 0x8000u) ? -1 : q->nbase + (int)(r0 + (iv & 255u) + TN - 255u);
            const int m0 = __builtin_amdgcn_readlane(me, 0), m1 = __builtin_amdgcn_readlane(me, 16), m2 = __builtin_amdgcn_readlane(me, 32), m3 = __builtin_amdgcn_readlane(me, 48);
            float s[8] = {0.f, 0.f, 0.f, 0.f, 0.f, 0.f, 0.f, 0.f};
            for (int e0 = 0; e0 < E; e0 += 64) {
                const int e = min(e0 + (int)lane, E - 1), d = dst[e], si = src[e];
                unsigned long long m = __ballot(e0 + (int)lane < E && (d == m0 || d == m1 || d == m2 || d == m3));
                while (m) {
                    const int l = __builtin_ctzll(m);
                    m &= m - 1;
                    const int sl = __builtin_amdgcn_readlane(si, l);
                    if (__builtin_amdgcn_readlane(d, l) == me) {
                        const u32x4 r = *(const u32x4*)(x + (size_t)sl * 256 + half * 128u + (lane & 15u) * 8u);
                        const float wt = bf2f(dis[sl]);
#pragma unroll
                        for (int j = 0; j < 4; ++j) { s[2 * j] += wt * lo(r[j]); s[2 * j + 1] += wt * hi(r[j]); }
                    }
                }
            }
            const float sc = bf2f(dis[max(me, 0)]);
            *(u32x4*)(smem + L_AL + (row < 48u ? row : row - 8u) * 256u + (((lane ^ row) & 15u) << 4)) =
                (u32x4){pack2(s[0] * sc, s[1] * sc), pack2(s[2] * sc, s[3] * sc), pack2(s[4] * sc, s[5] * sc), pack2(s[6] * sc, s[7] * sc)};
            lds_signal(S_T0 + 4u * (grp < 12u ? 1u + ((grp >> 2) & 1u) : grp >> 2), 1u);
        }
        return;
    }
    const int Ee = q->Ee, *edst = (const int*)q->edst.p, *esrc = (const int*)q->esrc.p;
    const u16 *wq = (const u16*)q->wq.p, *wk = (const u16*)q->wk.p;
    float* qs = (float*)(smem + L_QK + wave * 1024u);
    _Pragma("nounroll") for (u32 n = wave; n < TN && r0 + n < q->N; n += 4u) {
        const int me = q->nbase + (int)(r0 + n);
        float c0 = 0.f, c1 = 0.f, c2 = 0.f, c3 = 0.f;
        _Pragma("nounroll") for (int i = 0; i < 256; ++i) {
            const float xv = bf2f(x[(size_t)me * 256 + i]);
            const u32x2 r = *(const u32x2*)(wq + i * 256 + lane * 4u);
            c0 += xv * lo(r[0]); c1 += xv * hi(r[0]); c2 += xv * lo(r[1]); c3 += xv * hi(r[1]);
        }
        qs[lane * 4u] = c0; qs[lane * 4u + 1u] = c1; qs[lane * 4u + 2u] = c2; qs[lane * 4u + 3u] = c3;
        float k[4] = {0.f, 0.f, 0.f, 0.f};
        _Pragma("nounroll") for (int c = 0; c < 256; c += 2) {
            const float qa = qs[c], qb = qs[c + 1];
#pragma unroll
            for (int j = 0; j < 4; ++j) { const u32 r = *(const u32*)(wk + (lane * 4u + j) * 256u + c); k[j] += qa * lo(r) + qb * hi(r); }
        }
        float s0 = 0.f, s1 = 0.f, s2 = 0.f, s3 = 0.f, dn = 0.f;
        for (int e0 = 0; e0 < Ee; e0 += 64) {
            const int e = min(e0 + (int)lane, Ee - 1), si = esrc[e];
            unsigned long long m = __ballot(e0 + (int)lane < Ee && edst[e] == me);
            while (m) {
                const int l = __builtin_ctzll(m);
                m &= m - 1;
                const u32x2 r = *(const u32x2*)(x + (size_t)__builtin_amdgcn_readlane(si, l) * 256 + lane * 4u);
                float d = k[0] * lo(r[0]) + k[1] * hi(r[0]) + k[2] * lo(r[1]) + k[3] * hi(r[1]);
#pragma unroll
                for (int o = 1; o < 64; o <<= 1)
                    d += __uint_as_float((u32)__builtin_amdgcn_ds_bpermute((int)((lane ^ (u32)o) * 4u), (int)__float_as_uint(d)));
                const float wt = __builtin_amdgcn_exp2f(d * 0.09016844f);
                s0 += wt * lo(r[0]); s1 += wt * hi(r[0]); s2 += wt * lo(r[1]); s3 += wt * hi(r[1]);
                dn += wt;
            }
        }
        const float sc = __builtin_amdgcn_rcpf(dn + 1e-9f);
        if ((lane >> 5) == half)
            *(u32x2*)(smem + L_AA + ((u32)*(const u16*)(smem + L_MAP + n * 2u) ^ ((lane & 31u) * 8u))) =
                (u32x2){pack2(s0 * sc, s1 * sc), pack2(s2 * sc, s3 * sc)};
    }
    lds_signal(S_Z, 17u);
}

extern "C" void kernel_launch(void* const* d_in, const int* in_sizes, int n_in,
                              void* d_out, int out_size, void* d_ws, size_t ws_size,
                              hipStream_t stream) {
    const u32 N = in_sizes[5], E = in_sizes[1], Ee = in_sizes[3];
    const u16* x = (const u16*)d_in[0];
    const u16* dis = (const u16*)d_in[5];
    const u32 FL = 0x00020000u;
    const bool generic = N > NMAX || E > EMAX || (E & 3u) || (N & 1u);
    const bool irr = N > NMAX || Ee != 4u * N;
    (void)hipFuncSetAttribute((const void*)agg_kernel, hipFuncAttributeMaxDynamicSharedMemorySize, LDS_BYTES);
    for (u32 nbase = 0; nbase < N; nbase += NMAX) {
        const u32 n = N - nbase < NMAX ? N - nbase : NMAX;
        Args a;
        a.x = Desc{x + (size_t)nbase * 256, n * 512u, FL};
        a.ws = Desc{d_ws, WS_END, FL};
        a.wl = Desc{d_in[6], 131072u, FL}; a.wq = Desc{d_in[7], 131072u, FL}; a.wk = Desc{d_in[8], 131072u, FL}; a.wv = Desc{d_in[9], 131072u, FL};
        a.src = Desc{d_in[1], generic ? 0u : E * 4u, FL}; a.dst = Desc{d_in[2], generic ? 0u : E * 4u, FL};
        a.esrc = Desc{d_in[3], irr ? 0u : Ee * 4u, FL}; a.edst = Desc{d_in[4], irr ? 0u : Ee * 4u, FL};
        a.dis = Desc{dis + nbase, n * 2u, FL};
        a.out = Desc{(u16*)d_out + (size_t)nbase * 256, n * 512u, FL};
        a.N = n; a.TN = n > 256u ? (n + 127u) / 128u : 2u; a.M = ((1u << 24) + a.TN - 1u) / a.TN;
        a.irr = (irr ? 1u : 0u) | (generic ? 0x10000u : 0u);
        a.xall = x; a.disall = dis;
        a.E = (int)E; a.Ee = (int)Ee; a.nbase = (int)nbase; a.pad = 0;
        agg_kernel<<<256, 1024, LDS_BYTES, stream>>>(a);
    }
}
```

```cpp
#include <hip/hip_runtime.h>
#include <hip/hip_bf16.h>
#include <stdint.h>

typedef unsigned short u16;
typedef unsigned int u32;
typedef __attribute__((ext_vector_type(4))) u32 u32x4;
typedef __attribute__((ext_vector_type(2))) u32 u32x2;
struct Desc { const void* p; u32 bytes, flags; };
struct Args {
    Desc x, ws, wl, wv, wq, wk, src, dst, esrc, edst, dis, out;
    u32 N, TN, M, irr;
    const u16* xall;
    const u16* disall;
    int E, Ee, nbase, pad;
};
constexpr u32 NMAX = 10240, EMAX = 163840;
constexpr u32 WS_END = 9437184;
constexpr int LDS_BYTES = 163840;
constexpr u32 L_INV = 512, L_MAP = 1792, L_QK = 2048, L_AL = 23808, L_AA = 46336, S_QK = 16, S_Z = 40, S_T0 = 48;

__device__ __forceinline__ float bf2f(u32 h) { return __uint_as_float(h << 16); }
__device__ __forceinline__ float lo(u32 v) { return __uint_as_float(v << 16); }
__device__ __forceinline__ float hi(u32 v) { return __uint_as_float(v & 0xFFFF0000u); }
__device__ __forceinline__ u32 pack2(float a, float b) {
    return (u32)__builtin_bit_cast(u16, (__bf16)a) | ((u32)__builtin_bit_cast(u16, (__bf16)b) << 16);
}
__device__ __forceinline__ void lds_signal(const u32 addr, const u32 v) {
    asm volatile("s_mov_b64 exec, 1\n\tds_add_u32 %0, %1\n\ts_mov_b64 exec, -1" :: "v"(addr), "v"(v) : "memory");
}

extern "C" __global__ void __launch_bounds__(1024, 4)
agg_kernel(const Args a) {
    extern __shared__ __attribute__((aligned(16))) char smem[];
    u32 tid = threadIdx.x, wv;
    asm volatile(
        "s_memrealtime s[74:75]\n\t"
        "s_load_dwordx8 s[16:23], %2, 0\n\t"
        "s_load_dwordx4 s[64:67], %2, 192\n\t"
        "v_readfirstlane_b32 s69, v0\n\t"
        "v_and_b32 v1, 63, v0\n\t"
        "v_mov_b32 v2, 0\n\t"
        "v_mov_b32 v3, 1\n\t"
        "s_lshr_b32 s76, %3, 3\n\t"
        "s_and_b32 s70, %3, 1\n\t"
        "s_lshr_b32 s71, %3, 1\n\t"
        "s_mov_b32 s83, 0x2000000\n\t"
        "s_lshr_b32 s69, s69, 6\n\t"
        "s_cbranch_scc0 .L7_U\n\t"
        "s_cmp_lt_u32 s69, 8\n\t"
        "s_cbranch_scc1 .L7_G0\n\t"
        "s_load_dwordx4 s[48:51], %2, 128\n\t"
        "s_sub_u32 s69, s69, 8\n\t"
        "v_lshlrev_b32 v4, 2, v1\n\t"
        "s_lshl_b32 s77, s70, 2\n\t"
        "s_xor_b32 s77, s77, s69\n\t"
        "s_xor_b32 s77, s77, 4\n\t"
        "s_mul_i32 s80, s77, 10\n\t"
        "s_waitcnt lgkmcnt(0)\n\t"
        "s_mul_i32 s72, s71, s65\n\t"
        "s_add_u32 s78, s72, s80\n\t"
        "s_lshl_b32 s78, s78, 4\n\t"
        "s_mov_b32 exec_hi, 0xff\n\t"
        "buffer_load_dword v5, v4, s[48:51], s78 offen\n\t"
        "s_mov_b64 exec, -1\n\t"
        "s_barrier\n\t"
        "s_branch .L7_O2\n"
        ".L7_U:\n\t"
        "s_load_dwordx16 s[32:47], %2, 64\n\t"
        "s_load_dwordx16 s[48:63], %2, 128\n\t"
        "s_setprio 3\n\t"
        "s_getpc_b64 s[24:25]\n\t"
        "s_add_u32 s24, s24, agg_kernel@rel32@lo+4\n\t"
        "s_addc_u32 s25, s25, agg_kernel@rel32@hi+12\n\t"
        "s_and_b32 s25, s25, 0xffff\n\t"
        "s_mov_b32 s26, .L7_x-agg_kernel\n\t"
        "s_mov_b32 s27, 0x20000\n\t"
        "v_lshlrev_b32 v6, 12, v1\n\t"
        "s_lshl_b32 s78, s76, 7\n\t"
        "s_mov_b32 m0, 134400\n\t"
        "s_mov_b64 exec, 63\n\t"
        "buffer_load_dword v6, s[24:27], s78 offen lds\n\t"
        "s_mov_b64 exec, -1\n\t"
        "s_cmp_eq_u32 s70, 0\n\t"
        "s_cbranch_scc0 .L7_V\n\t"
        "v_lshrrev_b32 v4, 3, v1\n\t"
        "v_and_b32 v5, 7, v1\n\t"
        "v_lshlrev_b32 v4, 14, v4\n\t"
        "v_lshl_add_u32 v4, v5, 4, v4\n\t"
        "s_lshl_b32 s78, s71, 7\n\t"
        "v_add_u32 v4, s78, v4\n\t"
        "v_add_u32 v5, 0x20000, v4\n\t"
        "v_add_u32 v6, 0x40000, v4\n\t"
        "v_add_u32 v7, 0x60000, v4\n\t"
        "v_add_u32 v8, 0x80000, v4\n\t"
        "s_waitcnt lgkmcnt(0)\n\t"
        "s_mul_i32 s72, s71, s65\n\t"
        "buffer_load_dwordx4 v[16:19], v4, s[44:47], 0 offen\n\t"
        "buffer_load_dwordx4 v[20:23], v5, s[44:47], 0 offen\n\t"
        "buffer_load_dwordx4 v[24:27], v6, s[44:47], 0 offen\n\t"
        "buffer_load_dwordx4 v[28:31], v7, s[44:47], 0 offen\n\t"
        "buffer_load_dwordx4 v[32:35], v8, s[44:47], 0 offen\n\t"
        "s_mul_i32 s80, s71, 0x50\n\t"
        "s_lshl_b32 s78, s80, 4\n\t"
        "v_lshlrev_b32 v12, 4, v1\n\t"
        "buffer_load_dwordx4 v[116:119], v12, s[52:55], s78 offen\n\t"
        "s_mov_b64 exec, 0xffff\n\t"
        "buffer_load_dwordx4 v[120:123], v12, s[52:55], s78 offen offset:1024\n\t"
        "s_mov_b64 exec, -1\n\t"
        "buffer_load_dwordx4 v[36:39], v4, s[40:43], 0 offen\n\t"
        "buffer_load_dwordx4 v[40:43], v5, s[40:43], 0 offen\n\t"
        "buffer_load_dwordx4 v[44:47], v6, s[40:43], 0 offen\n\t"
        "buffer_load_dwordx4 v[48:51], v7, s[40:43], 0 offen\n\t"
        "buffer_load_dwordx4 v[52:55], v8, s[40:43], 0 offen\n\t"
        "s_barrier\n\t"
        "s_branch .L7_U2\n"
        ".L7_G0:\n\t"
        "s_cmp_lt_u32 s69, 4\n\t"
        "s_cbranch_scc1 .L7_L\n\t"
        "s_sub_u32 s69, s69, 4\n\t"
        "v_lshlrev_b32 v4, 4, v1\n\t"
        "v_and_b32 v5, 0x200, v4\n\t"
        "v_mad_u32_u24 v4, v5, 7, v4\n\t"
        "s_mul_i32 s80, s70, 5\n\t"
        "s_mul_i32 s81, s69, 0x420\n\t"
        "s_add_u32 s81, s81, 2048\n\t"
        "s_mul_i32 s78, s80, 0x1080\n\t"
        "s_add_u32 m0, s78, s81\n\t"
        "s_lshr_b32 s78, s80, 1\n\t"
        "s_lshl1_add_u32 s78, s78, s80\n\t"
        "s_lshl_b32 s78, s78, 11\n\t"
        "s_waitcnt lgkmcnt(0)\n\t"
        "s_mul_i32 s72, s71, s65\n\t"
        "s_add_u32 s79, s72, s69\n\t"
        "s_lshl_b32 s79, s79, 9\n\t"
        "v_add_u32 v4, s79, v4\n\t"
        "s_sleep 3\n\t"
        "s_cmp_eq_u32 s70, 0\n\t"
        "s_cbranch_scc1 .L7_gs\n\t"
        "s_sleep 4\n"
        ".L7_gs:\n\t"
        "buffer_load_dwordx4 v4, s[16:19], s78 offen lds\n\t"
        "s_add_u32 s80, s80, 1\n\t"
        "s_load_dwordx4 s[56:59], %2, 160\n\t"
        "s_mul_i32 s78, s80, 0x1080\n\t"
        "s_add_u32 m0, s78, s81\n\t"
        "s_lshr_b32 s78, s80, 1\n\t"
        "s_lshl1_add_u32 s78, s78, s80\n\t"
        "s_lshl_b32 s78, s78, 11\n\t"
        "buffer_load_dwordx4 v4, s[16:19], s78 offen lds\n\t"
        "s_add_u32 s80, s80, 1\n\t"
        "s_mul_i32 s78, s80, 0x1080\n\t"
        "s_add_u32 m0, s78, s81\n\t"
        "s_lshr_b32 s78, s80, 1\n\t"
        "s_lshl1_add_u32 s78, s78, s80\n\t"
        "s_lshl_b32 s78, s78, 11\n\t"
        "buffer_load_dwordx4 v4, s[16:19], s78 offen lds\n\t"
        "s_add_u32 s80, s80, 1\n\t"
        "s_mul_i32 s78, s80, 0x1080\n\t"
        "s_add_u32 m0, s78, s81\n\t"
        "s_lshr_b32 s78, s80, 1\n\t"
        "s_lshl1_add_u32 s78, s78, s80\n\t"
        "s_lshl_b32 s78, s78, 11\n\t"
        "buffer_load_dwordx4 v4, s[16:19], s78 offen lds\n\t"
        "s_add_u32 s80, s80, 1\n\t"
        "s_mul_i32 s78, s80, 0x1080\n\t"
        "s_add_u32 m0, s78, s81\n\t"
        "s_lshr_b32 s78, s80, 1\n\t"
        "s_lshl1_add_u32 s78, s78, s80\n\t"
        "s_lshl_b32 s78, s78, 11\n\t"
        "buffer_load_dwordx4 v4, s[16:19], s78 offen lds\n\t"
        "s_add_u32 s80, s80, 1\n\t"
        "s_barrier\n\t"
        "s_branch .L7_G2\n"
        ".L7_L:\n\t"
        "s_cmp_eq_u32 s69, 2\n\t"
        "s_cbranch_scc0 .L7_L1\n\t"
        "s_waitcnt lgkmcnt(0)\n\t"
        "s_mul_i32 s72, s71, s65\n\t"
        "s_buffer_load_dword s68, s[20:23], 0\n\t"
        "s_barrier\n\t"
        "s_branch .L7_P2\n"
        ".L7_L1:\n\t"
        "s_cmp_eq_u32 s69, 1\n\t"
        "s_cbranch_scc0 .L7_T\n\t"
        "s_load_dwordx4 s[56:59], %2, 160\n\t"
        "v_lshlrev_b32 v4, 3, v1\n\t"
        "v_mov_b64 v[8:9], 0\n\t"
        "ds_write_b64 v4, v[8:9]\n\t"
        "s_waitcnt lgkmcnt(0)\n\t"
        "s_mul_i32 s72, s71, s65\n\t"
        "v_add_lshl_u32 v4, v1, s72, 1\n\t"
        "buffer_load_ushort v16, v4, s[56:59], 0 offen\n\t"
        "buffer_load_ushort v17, v4, s[56:59], 0 offen offset:80\n\t"
        "s_barrier\n\t"
        "s_branch .L7_R2\n"
        ".L7_T:\n\t"
        "s_load_dwordx8 s[24:31], %2, 32\n\t"
        "v_lshlrev_b32 v6, 7, v1\n\t"
        "s_mov_b32 m0, 134400\n\t"
        "s_lshl_b32 s78, s70, 16\n\t"
        "s_lshl_b32 s79, s76, 11\n\t"
        "s_add_u32 s80, s78, s79\n\t"
        "s_mul_i32 s81, s76, 0x300\n\t"
        "s_lshl_b32 s82, %3, 7\n\t"
        "s_add_u32 s82, s82, 4096\n\t"
        "v_lshlrev_b32 v8, 2, v1\n\t"
        "s_and_b32 s78, s71, 63\n\t"
        "s_lshl_b32 s78, s78, 7\n\t"
        "s_and_b32 s79, s71, 64\n\t"
        "s_lshl_b32 s79, s79, 8\n\t"
        "s_add_u32 s42, s78, s79\n\t"
        "s_and_b32 s44, s76, 1\n\t"
        "s_lshl_b32 s78, s44, 13\n\t"
        "s_add_u32 s42, s42, s78\n\t"
        "s_lshl_b32 s78, s70, 15\n\t"
        "s_add_u32 s42, s42, s78\n\t"
        "s_add_u32 s42, s42, 65536\n\t"
        "s_xor_b32 s43, s42, 0x2000\n\t"
        "s_waitcnt lgkmcnt(0)\n\t"
        "s_mul_i32 s72, s71, s65\n\t"
        "s_buffer_load_dword s40, s[20:23], s42 glc\n\t"
        "s_buffer_load_dword s41, s[20:23], s43 glc\n\t"
        "v_mov_b32 v7, s74\n\t"
        "s_mov_b32 exec_hi, 0\n\t"
        "buffer_store_dword v7, v8, s[20:23], s82 offen sc1\n\t"
        "s_lshr_b32 s78, %3, 2\n\t"
        "s_xor_b32 s44, s44, s78\n\t"
        "s_waitcnt lgkmcnt(1)\n\t"
        "s_nop 8\n\t"
        "s_getreg_b32 s45, hwreg(HW_REG_IB_STS, 8, 4)\n\t"
        "s_xnor_b32 s44, s44, s45\n\t"
        "s_and_b32 s44, s44, 1\n\t"
        "s_lshl_b32 s44, s44, 25\n\t"
        "v_mov_b32 v7, s44\n\t"
        "ds_write_b32 v2, v7 offset:1984\n\t"
        "s_waitcnt lgkmcnt(0)\n\t"
        "s_barrier\n\t"
        "s_mov_b64 exec, -1\n\t"
        "s_mov_b64 exec, 0xffff\n\t"
        "buffer_load_dword v6, s[28:31], s80 offen lds\n\t"
        "buffer_load_dword v6, s[24:27], s80 offen lds\n\t"
        "s_mov_b64 exec, -1\n\t"
        "s_branch .L7_T2\n"
        ".L7_V:\n\t"
        "s_and_b32 s78, s71, 1\n\t"
        "s_and_b32 s79, s76, 3\n\t"
        "s_lshl2_add_u32 s80, s78, s79\n\t"
        "s_and_b32 s78, s71, 2\n\t"
        "s_lshr_b32 s79, s76, 2\n\t"
        "s_lshl2_add_u32 s81, s78, s79\n\t"
        "v_lshlrev_b32 v12, 4, v1\n\t"
        "v_and_b32 v5, 0x200, v12\n\t"
        "v_mad_u32_u24 v4, v5, 15, v12\n\t"
        "s_lshl_b32 s82, s80, 14\n\t"
        "s_lshr_b32 s78, s81, 1\n\t"
        "s_lshl_b32 s78, s78, 14\n\t"
        "s_and_b32 s79, s81, 1\n\t"
        "s_lshl_b32 s79, s79, 11\n\t"
        "s_add_u32 s77, s78, s79\n\t"
        "s_mov_b32 m0, 68864\n\t"
        "s_waitcnt lgkmcnt(0)\n\t"
        "s_mul_i32 s72, s71, s65\n\t"
        "buffer_load_dwordx4 v4, s[36:39], s77 offen offset:0 lds\n\t"
        "s_add_u32 m0, m0, 544\n\t"
        "s_nop 0\n\t"
        "buffer_load_dwordx4 v4, s[36:39], s77 offen offset:512 lds\n\t"
        "s_add_u32 m0, m0, 544\n\t"
        "s_nop 0\n\t"
        "buffer_load_dwordx4 v4, s[36:39], s77 offen offset:1024 lds\n\t"
        "s_add_u32 m0, m0, 544\n\t"
        "s_nop 0\n\t"
        "buffer_load_dwordx4 v4, s[36:39], s77 offen offset:1536 lds\n\t"
        "s_add_u32 m0, m0, 2592\n\t"
        "s_add_u32 s77, s77, 0x1000\n\t"
        "buffer_load_dwordx4 v4, s[36:39], s77 offen offset:0 lds\n\t"
        "s_add_u32 m0, m0, 544\n\t"
        "s_nop 0\n\t"
        "buffer_load_dwordx4 v4, s[36:39], s77 offen offset:512 lds\n\t"
        "s_add_u32 m0, m0, 544\n\t"
        "s_nop 0\n\t"
        "buffer_load_dwordx4 v4, s[36:39], s77 offen offset:1024 lds\n\t"
        "s_add_u32 m0, m0, 544\n\t"
        "s_nop 0\n\t"
        "buffer_load_dwordx4 v4, s[36:39], s77 offen offset:1536 lds\n\t"
        "s_add_u32 m0, m0, 2592\n\t"
        "s_nop 0\n\t"
        "buffer_load_dwordx4 v4, s[32:35], s82 offen offset:0 lds\n\t"
        "s_add_u32 m0, m0, 544\n\t"
        "s_nop 0\n\t"
        "buffer_load_dwordx4 v4, s[32:35], s82 offen offset:512 lds\n\t"
        "s_add_u32 m0, m0, 544\n\t"
        "s_nop 0\n\t"
        "buffer_load_dwordx4 v4, s[32:35], s82 offen offset:1024 lds\n\t"
        "s_add_u32 m0, m0, 544\n\t"
        "s_nop 0\n\t"
        "buffer_load_dwordx4 v4, s[32:35], s82 offen offset:1536 lds\n\t"
        "s_add_u32 m0, m0, 2592\n\t"
        "s_add_u32 s82, s82, 0x1000\n\t"
        "buffer_load_dwordx4 v4, s[32:35], s82 offen offset:0 lds\n\t"
        "s_add_u32 m0, m0, 544\n\t"
        "s_nop 0\n\t"
        "buffer_load_dwordx4 v4, s[32:35], s82 offen offset:512 lds\n\t"
        "s_add_u32 m0, m0, 544\n\t"
        "s_nop 0\n\t"
        "buffer_load_dwordx4 v4, s[32:35], s82 offen offset:1024 lds\n\t"
        "s_add_u32 m0, m0, 544\n\t"
        "s_nop 0\n\t"
        "buffer_load_dwordx4 v4, s[32:35], s82 offen offset:1536 lds\n\t"
        "s_add_u32 m0, m0, 2592\n\t"
        "s_sub_u32 s82, s82, 0x800\n\t"
        "buffer_load_dwordx4 v4, s[32:35], s82 offen offset:0 lds\n\t"
        "s_add_u32 m0, m0, 544\n\t"
        "s_nop 0\n\t"
        "buffer_load_dwordx4 v4, s[32:35], s82 offen offset:512 lds\n\t"
        "s_add_u32 m0, m0, 544\n\t"
        "s_nop 0\n\t"
        "buffer_load_dwordx4 v4, s[32:35], s82 offen offset:1024 lds\n\t"
        "s_add_u32 m0, m0, 544\n\t"
        "s_nop 0\n\t"
        "buffer_load_dwordx4 v4, s[32:35], s82 offen offset:1536 lds\n\t"
        "s_add_u32 m0, m0, 2592\n\t"
        "s_add_u32 s82, s82, 0x1000\n\t"
        "buffer_load_dwordx4 v4, s[32:35], s82 offen offset:0 lds\n\t"
        "s_add_u32 m0, m0, 544\n\t"
        "s_nop 0\n\t"
        "buffer_load_dwordx4 v4, s[32:35], s82 offen offset:512 lds\n\t"
        "s_add_u32 m0, m0, 544\n\t"
        "s_nop 0\n\t"
        "buffer_load_dwordx4 v4, s[32:35], s82 offen offset:1024 lds\n\t"
        "s_add_u32 m0, m0, 544\n\t"
        "s_nop 0\n\t"
        "buffer_load_dwordx4 v4, s[32:35], s82 offen offset:1536 lds\n\t"
        "s_barrier\n\t"
        "s_branch .L7_V2\n"
        ".L7_U2:\n\t"
        "v_cmp_gt_u32 s[24:25], s46, v4\n\t"
        "v_cmp_gt_u32 s[26:27], s46, v5\n\t"
        "v_cmp_gt_u32 s[28:29], s46, v6\n\t"
        "v_cmp_gt_u32 s[30:31], s46, v7\n\t"
        "v_cmp_gt_u32 s[32:33], s46, v8\n\t"
        "v_mul_u32_u24 v112, 0xa0, v1\n\t"
        "v_add_u32 v112, 68864, v112\n\t"
        "v_add_u32 v113, 10240, v112\n\t"
        "v_lshlrev_b32 v7, 2, v1\n\t"
        "ds_write_b32 v7, v112 offset:1028\n\t"
        "ds_write_b32 v7, v113 offset:1284\n\t"
        "v_lshrrev_b32 v15, 3, v1\n\t"
        "v_mul_u32_u24 v15, 0xa0, v15\n\t"
        "v_add_u32 v15, 68864, v15\n\t"
        "v_and_b32 v4, 7, v1\n\t"
        "v_lshl_add_u32 v15, v4, 2, v15\n\t"
        "v_mov_b32 v13, 0x1000000\n\t"
        "v_mov_b32 v14, 4\n\t"
        "s_lshl_b32 s54, s65, 14\n\t"
        "s_sub_u32 s54, 0, s54\n\t"
        "s_lshl_b32 s48, s71, 14\n\t"
        "s_add_u32 s48, s48, 1048576\n\t"
        "s_add_u32 s49, s48, 4096\n\t"
        "v_add_u32 v97, 1280, v15\n\t"
        "v_add_u32 v98, 2560, v15\n\t"
        "v_add_u32 v99, 3840, v15\n\t"
        "v_add_u32 v100, 5120, v15\n\t"
        "v_add_u32 v101, 6400, v15\n\t"
        "v_add_u32 v102, 7680, v15\n\t"
        "v_add_u32 v103, 8960, v15\n\t"
        "v_add_u32 v112, 0x80, v112\n\t"
        "v_add_u32 v113, 0x80, v113\n\t"
        "v_add_u32 v114, s80, v1\n\t"
        "v_add_u32 v115, 64, v114\n\t"
        "v_cmp_gt_u32 s[36:37], s64, v114\n\t"
        "v_cmp_gt_u32 s[38:39], s64, v115\n\t"
        "s_waitcnt vmcnt(11) lgkmcnt(8)\n\t"
        "s_mov_b64 exec, s[24:25]\n\t"
        "v_mad_u32_u24 v56, v16, s66, v13\n\t"
        "v_mad_u32_u24 v57, v17, s66, v13\n\t"
        "v_mad_u32_u24 v58, v18, s66, v13\n\t"
        "v_mad_u32_u24 v59, v19, s66, v13\n\t"
        "v_lshlrev_b32_sdwa v56, 2, v56 dst_sel:DWORD dst_unused:UNUSED_PAD src0_sel:DWORD src1_sel:BYTE_3\n\t"
        "ds_add_rtn_u32 v76, v56, v14 offset:1024\n\t"
        "v_lshlrev_b32_sdwa v57, 2, v57 dst_sel:DWORD dst_unused:UNUSED_PAD src0_sel:DWORD src1_sel:BYTE_3\n\t"
        "ds_add_rtn_u32 v77, v57, v14 offset:1024\n\t"
        "v_lshlrev_b32_sdwa v58, 2, v58 dst_sel:DWORD dst_unused:UNUSED_PAD src0_sel:DWORD src1_sel:BYTE_3\n\t"
        "ds_add_rtn_u32 v78, v58, v14 offset:1024\n\t"
        "v_lshlrev_b32_sdwa v59, 2, v59 dst_sel:DWORD dst_unused:UNUSED_PAD src0_sel:DWORD src1_sel:BYTE_3\n\t"
        "ds_add_rtn_u32 v79, v59, v14 offset:1024\n\t"
        "s_waitcnt vmcnt(10) lgkmcnt(8)\n\t"
        "s_mov_b64 exec, s[26:27]\n\t"
        "v_mad_u32_u24 v60, v20, s66, v13\n\t"
        "v_mad_u32_u24 v61, v21, s66, v13\n\t"
        "v_mad_u32_u24 v62, v22, s66, v13\n\t"
        "v_mad_u32_u24 v63, v23, s66, v13\n\t"
        "v_lshlrev_b32_sdwa v60, 2, v60 dst_sel:DWORD dst_unused:UNUSED_PAD src0_sel:DWORD src1_sel:BYTE_3\n\t"
        "ds_add_rtn_u32 v80, v60, v14 offset:1024\n\t"
        "v_lshlrev_b32_sdwa v61, 2, v61 dst_sel:DWORD dst_unused:UNUSED_PAD src0_sel:DWORD src1_sel:BYTE_3\n\t"
        "ds_add_rtn_u32 v81, v61, v14 offset:1024\n\t"
        "v_lshlrev_b32_sdwa v62, 2, v62 dst_sel:DWORD dst_unused:UNUSED_PAD src0_sel:DWORD src1_sel:BYTE_3\n\t"
        "ds_add_rtn_u32 v82, v62, v14 offset:1024\n\t"
        "v_lshlrev_b32_sdwa v63, 2, v63 dst_sel:DWORD dst_unused:UNUSED_PAD src0_sel:DWORD src1_sel:BYTE_3\n\t"
        "ds_add_rtn_u32 v83, v63, v14 offset:1024\n\t"
        "s_waitcnt vmcnt(9) lgkmcnt(8)\n\t"
        "s_mov_b64 exec, s[28:29]\n\t"
        "v_mad_u32_u24 v64, v24, s66, v13\n\t"
        "v_mad_u32_u24 v65, v25, s66, v13\n\t"
        "v_mad_u32_u24 v66, v26, s66, v13\n\t"
        "v_mad_u32_u24 v67, v27, s66, v13\n\t"
        "v_lshlrev_b32_sdwa v64, 2, v64 dst_sel:DWORD dst_unused:UNUSED_PAD src0_sel:DWORD src1_sel:BYTE_3\n\t"
        "ds_add_rtn_u32 v84, v64, v14 offset:1024\n\t"
        "v_lshlrev_b32_sdwa v65, 2, v65 dst_sel:DWORD dst_unused:UNUSED_PAD src0_sel:DWORD src1_sel:BYTE_3\n\t"
        "ds_add_rtn_u32 v85, v65, v14 offset:1024\n\t"
        "v_lshlrev_b32_sdwa v66, 2, v66 dst_sel:DWORD dst_unused:UNUSED_PAD src0_sel:DWORD src1_sel:BYTE_3\n\t"
        "ds_add_rtn_u32 v86, v66, v14 offset:1024\n\t"
        "v_lshlrev_b32_sdwa v67, 2, v67 dst_sel:DWORD dst_unused:UNUSED_PAD src0_sel:DWORD src1_sel:BYTE_3\n\t"
        "ds_add_rtn_u32 v87, v67, v14 offset:1024\n\t"
        "s_waitcnt vmcnt(8) lgkmcnt(8)\n\t"
        "s_mov_b64 exec, s[30:31]\n\t"
        "v_mad_u32_u24 v68, v28, s66, v13\n\t"
        "v_mad_u32_u24 v69, v29, s66, v13\n\t"
        "v_mad_u32_u24 v70, v30, s66, v13\n\t"
        "v_mad_u32_u24 v71, v31, s66, v13\n\t"
        "v_lshlrev_b32_sdwa v68, 2, v68 dst_sel:DWORD dst_unused:UNUSED_PAD src0_sel:DWORD src1_sel:BYTE_3\n\t"
        "ds_add_rtn_u32 v88, v68, v14 offset:1024\n\t"
        "v_lshlrev_b32_sdwa v69, 2, v69 dst_sel:DWORD dst_unused:UNUSED_PAD src0_sel:DWORD src1_sel:BYTE_3\n\t"
        "ds_add_rtn_u32 v89, v69, v14 offset:1024\n\t"
        "v_lshlrev_b32_sdwa v70, 2, v70 dst_sel:DWORD dst_unused:UNUSED_PAD src0_sel:DWORD src1_sel:BYTE_3\n\t"
        "ds_add_rtn_u32 v90, v70, v14 offset:1024\n\t"
        "v_lshlrev_b32_sdwa v71, 2, v71 dst_sel:DWORD dst_unused:UNUSED_PAD src0_sel:DWORD src1_sel:BYTE_3\n\t"
        "ds_add_rtn_u32 v91, v71, v14 offset:1024\n\t"
        "s_waitcnt vmcnt(7) lgkmcnt(8)\n\t"
        "s_mov_b64 exec, s[32:33]\n\t"
        "v_mad_u32_u24 v72, v32, s66, v13\n\t"
        "v_mad_u32_u24 v73, v33, s66, v13\n\t"
        "v_mad_u32_u24 v74, v34, s66, v13\n\t"
        "v_mad_u32_u24 v75, v35, s66, v13\n\t"
        "v_lshlrev_b32_sdwa v72, 2, v72 dst_sel:DWORD dst_unused:UNUSED_PAD src0_sel:DWORD src1_sel:BYTE_3\n\t"
        "ds_add_rtn_u32 v92, v72, v14 offset:1024\n\t"
        "v_lshlrev_b32_sdwa v73, 2, v73 dst_sel:DWORD dst_unused:UNUSED_PAD src0_sel:DWORD src1_sel:BYTE_3\n\t"
        "ds_add_rtn_u32 v93, v73, v14 offset:1024\n\t"
        "v_lshlrev_b32_sdwa v74, 2, v74 dst_sel:DWORD dst_unused:UNUSED_PAD src0_sel:DWORD src1_sel:BYTE_3\n\t"
        "ds_add_rtn_u32 v94, v74, v14 offset:1024\n\t"
        "v_lshlrev_b32_sdwa v75, 2, v75 dst_sel:DWORD dst_unused:UNUSED_PAD src0_sel:DWORD src1_sel:BYTE_3\n\t"
        "ds_add_rtn_u32 v95, v75, v14 offset:1024\n\t"
        "s_mov_b64 exec, -1\n\t"
        "ds_read_b32 v4, v7 offset:1028\n\t"
        "ds_read_b32 v5, v7 offset:1284\n\t"
        "s_waitcnt vmcnt(5) lgkmcnt(12)\n\t"
        "v_xor_b32 v116, v116, v114\n\t"
        "v_bitop3_b32 v116, v117, v116, v114 bitop3:0xde\n\t"
        "v_bitop3_b32 v116, v118, v116, v114 bitop3:0xde\n\t"
        "v_bitop3_b32 v116, v119, v116, v114 bitop3:0xde\n\t"
        "v_xor_b32 v120, v120, v115\n\t"
        "v_bitop3_b32 v120, v121, v120, v115 bitop3:0xde\n\t"
        "v_bitop3_b32 v120, v122, v120, v115 bitop3:0xde\n\t"
        "v_bitop3_b32 v120, v123, v120, v115 bitop3:0xde\n\t"
        "v_cmp_ne_u32 s[40:41], 0, v116\n\t"
        "v_cmp_ne_u32 s[42:43], 0, v120\n\t"
        "s_and_b64 s[38:39], s[38:39], 0xffff\n\t"
        "s_and_b64 s[36:37], s[36:37], s[40:41]\n\t"
        "s_and_b64 s[38:39], s[38:39], s[42:43]\n\t"
        "s_or_b64 s[36:37], s[36:37], s[38:39]\n\t"
        "s_cselect_b32 s67, 1, s67\n"
        ".L7_w38:\n\t"
        "ds_read_b32 v0, v2 offset:104\n\t"
        "ds_read_b32 v6, v2 offset:76\n\t"
        "s_waitcnt lgkmcnt(1)\n\t"
        "v_cmp_gt_u16 vcc, 1, v0\n\t"
        "s_cbranch_vccz .L7_d39\n\t"
        "s_waitcnt lgkmcnt(0)\n\t"
        "s_sleep 0\n\t"
        "s_sub_i32 s83, s83, 1\n\t"
        "s_cmp_gt_i32 s83, 0\n\t"
        "s_cbranch_scc1 .L7_w38\n"
        ".L7_d39:\n\t"
        "v_cmp_gt_u32 s[34:35], v4, v112\n\t"
        "v_cmp_gt_u32 s[36:37], v5, v113\n\t"
        "s_or_b64 s[38:39], s[34:35], s[36:37]\n\t"
        "s_cbranch_scc1 .L7_ov\n"
        ".L7_ov1:\n\t"
        "s_waitcnt lgkmcnt(0)\n\t"
        "v_readfirstlane_b32 s68, v6\n\t"
        "s_waitcnt vmcnt(4) lgkmcnt(8)\n\t"
        "s_mov_b64 exec, s[24:25]\n\t"
        "v_lshl_add_u32 v16, v16, 16, v36\n\t"
        "v_lshl_add_u32 v17, v17, 16, v37\n\t"
        "v_lshl_add_u32 v18, v18, 16, v38\n\t"
        "v_lshl_add_u32 v19, v19, 16, v39\n\t"
        "v_mad_i32_i24 v16, v56, s54, v16\n\t"
        "ds_add_u32 v76, v16\n\t"
        "v_mad_i32_i24 v17, v57, s54, v17\n\t"
        "ds_add_u32 v77, v17\n\t"
        "v_mad_i32_i24 v18, v58, s54, v18\n\t"
        "ds_add_u32 v78, v18\n\t"
        "v_mad_i32_i24 v19, v59, s54, v19\n\t"
        "ds_add_u32 v79, v19\n\t"
        "s_waitcnt vmcnt(3) lgkmcnt(8)\n\t"
        "s_mov_b64 exec, s[26:27]\n\t"
        "v_lshl_add_u32 v20, v20, 16, v40\n\t"
        "v_lshl_add_u32 v21, v21, 16, v41\n\t"
        "v_lshl_add_u32 v22, v22, 16, v42\n\t"
        "v_lshl_add_u32 v23, v23, 16, v43\n\t"
        "v_mad_i32_i24 v20, v60, s54, v20\n\t"
        "ds_add_u32 v80, v20\n\t"
        "v_mad_i32_i24 v21, v61, s54, v21\n\t"
        "ds_add_u32 v81, v21\n\t"
        "v_mad_i32_i24 v22, v62, s54, v22\n\t"
        "ds_add_u32 v82, v22\n\t"
        "v_mad_i32_i24 v23, v63, s54, v23\n\t"
        "ds_add_u32 v83, v23\n\t"
        "s_waitcnt vmcnt(2) lgkmcnt(8)\n\t"
        "s_mov_b64 exec, s[28:29]\n\t"
        "v_lshl_add_u32 v24, v24, 16, v44\n\t"
        "v_lshl_add_u32 v25, v25, 16, v45\n\t"
        "v_lshl_add_u32 v26, v26, 16, v46\n\t"
        "v_lshl_add_u32 v27, v27, 16, v47\n\t"
        "v_mad_i32_i24 v24, v64, s54, v24\n\t"
        "ds_add_u32 v84, v24\n\t"
        "v_mad_i32_i24 v25, v65, s54, v25\n\t"
        "ds_add_u32 v85, v25\n\t"
        "v_mad_i32_i24 v26, v66, s54, v26\n\t"
        "ds_add_u32 v86, v26\n\t"
        "v_mad_i32_i24 v27, v67, s54, v27\n\t"
        "ds_add_u32 v87, v27\n\t"
        "s_waitcnt vmcnt(1) lgkmcnt(8)\n\t"
        "s_mov_b64 exec, s[30:31]\n\t"
        "v_lshl_add_u32 v28, v28, 16, v48\n\t"
        "v_lshl_add_u32 v29, v29, 16, v49\n\t"
        "v_lshl_add_u32 v30, v30, 16, v50\n\t"
        "v_lshl_add_u32 v31, v31, 16, v51\n\t"
        "v_mad_i32_i24 v28, v68, s54, v28\n\t"
        "ds_add_u32 v88, v28\n\t"
        "v_mad_i32_i24 v29, v69, s54, v29\n\t"
        "ds_add_u32 v89, v29\n\t"
        "v_mad_i32_i24 v30, v70, s54, v30\n\t"
        "ds_add_u32 v90, v30\n\t"
        "v_mad_i32_i24 v31, v71, s54, v31\n\t"
        "ds_add_u32 v91, v31\n\t"
        "s_waitcnt vmcnt(0) lgkmcnt(8)\n\t"
        "s_mov_b64 exec, s[32:33]\n\t"
        "v_lshl_add_u32 v32, v32, 16, v52\n\t"
        "v_lshl_add_u32 v33, v33, 16, v53\n\t"
        "v_lshl_add_u32 v34, v34, 16, v54\n\t"
        "v_lshl_add_u32 v35, v35, 16, v55\n\t"
        "v_mad_i32_i24 v32, v72, s54, v32\n\t"
        "ds_add_u32 v92, v32\n\t"
        "v_mad_i32_i24 v33, v73, s54, v33\n\t"
        "ds_add_u32 v93, v33\n\t"
        "v_mad_i32_i24 v34, v74, s54, v34\n\t"
        "ds_add_u32 v94, v34\n\t"
        "v_mad_i32_i24 v35, v75, s54, v35\n\t"
        "ds_add_u32 v95, v35\n\t"
        "s_mov_b64 exec, -1\n\t"
        "s_and_b32 s78, s67, 1\n\t"
        "s_or_b32 s78, s78, s38\n\t"
        "s_or_b32 s78, s78, s39\n\t"
        "s_cbranch_scc1 .L7_mk\n"
        ".L7_mk1:\n\t"
        "s_mov_b64 exec, 1\n\t"
        "ds_add_u32 v2, v3 offset:112\n\t"
        "s_mov_b64 exec, -1\n\t"
        "s_waitcnt lgkmcnt(3)\n\t"
        "ds_read2_b32 v[16:17], v15 offset0:0 offset1:8\n\t"
        "ds_read2_b32 v[18:19], v15 offset0:16 offset1:24\n\t"
        "ds_read2_b32 v[20:21], v97 offset0:0 offset1:8\n\t"
        "ds_read2_b32 v[22:23], v97 offset0:16 offset1:24\n\t"
        "ds_read2_b32 v[24:25], v98 offset0:0 offset1:8\n\t"
        "ds_read2_b32 v[26:27], v98 offset0:16 offset1:24\n\t"
        "ds_read2_b32 v[28:29], v99 offset0:0 offset1:8\n\t"
        "ds_read2_b32 v[30:31], v99 offset0:16 offset1:24\n\t"
        "ds_read2_b32 v[32:33], v100 offset0:0 offset1:8\n\t"
        "ds_read2_b32 v[34:35], v100 offset0:16 offset1:24\n\t"
        "ds_read2_b32 v[36:37], v101 offset0:0 offset1:8\n\t"
        "ds_read2_b32 v[38:39], v101 offset0:16 offset1:24\n\t"
        "s_waitcnt lgkmcnt(10)\n\t"
        "buffer_store_dwordx4 v[16:19], v12, s[20:23], s48 offen offset:0 sc1\n\t"
        "ds_read2_b32 v[40:41], v102 offset0:0 offset1:8\n\t"
        "ds_read2_b32 v[42:43], v102 offset0:16 offset1:24\n\t"
        "s_waitcnt lgkmcnt(10)\n\t"
        "buffer_store_dwordx4 v[20:23], v12, s[20:23], s48 offen offset:1024 sc1\n\t"
        "ds_read2_b32 v[44:45], v103 offset0:0 offset1:8\n\t"
        "ds_read2_b32 v[46:47], v103 offset0:16 offset1:24\n\t"
        "s_mov_b64 exec, 1\n\t"
        "ds_add_u32 v2, v3 offset:72\n\t"
        "s_mov_b64 exec, -1\n\t"
        "s_waitcnt lgkmcnt(11)\n\t"
        "buffer_store_dwordx4 v[24:27], v12, s[20:23], s48 offen offset:2048 sc1\n\t"
        "s_waitcnt lgkmcnt(9)\n\t"
        "buffer_store_dwordx4 v[28:31], v12, s[20:23], s48 offen offset:3072 sc1\n\t"
        "s_waitcnt lgkmcnt(7)\n\t"
        "buffer_store_dwordx4 v[32:35], v12, s[20:23], s49 offen offset:0 sc1\n\t"
        "s_waitcnt lgkmcnt(5)\n\t"
        "buffer_store_dwordx4 v[36:39], v12, s[20:23], s49 offen offset:1024 sc1\n\t"
        "s_waitcnt lgkmcnt(3)\n\t"
        "buffer_store_dwordx4 v[40:43], v12, s[20:23], s49 offen offset:2048 sc1\n\t"
        "s_waitcnt lgkmcnt(1)\n\t"
        "buffer_store_dwordx4 v[44:47], v12, s[20:23], s49 offen offset:3072 sc1\n\t"
        "s_mov_b64 exec, 1\n\t"
        "ds_add_u32 v2, v3 offset:44\n\t"
        "s_mov_b64 exec, -1\n\t"
        "s_branch .L7_Q\n"
        ".L7_ov:\n\t"
        "s_mov_b32 s78, 68828\n\t"
        "v_mov_b32 v9, 40\n\t"
        "v_mad_u32_u24 v8, v56, v9, s78\n\t"
        "v_min_u32 v76, v76, v8\n\t"
        "v_mad_u32_u24 v8, v57, v9, s78\n\t"
        "v_min_u32 v77, v77, v8\n\t"
        "v_mad_u32_u24 v8, v58, v9, s78\n\t"
        "v_min_u32 v78, v78, v8\n\t"
        "v_mad_u32_u24 v8, v59, v9, s78\n\t"
        "v_min_u32 v79, v79, v8\n\t"
        "v_mad_u32_u24 v8, v60, v9, s78\n\t"
        "v_min_u32 v80, v80, v8\n\t"
        "v_mad_u32_u24 v8, v61, v9, s78\n\t"
        "v_min_u32 v81, v81, v8\n\t"
        "v_mad_u32_u24 v8, v62, v9, s78\n\t"
        "v_min_u32 v82, v82, v8\n\t"
        "v_mad_u32_u24 v8, v63, v9, s78\n\t"
        "v_min_u32 v83, v83, v8\n\t"
        "v_mad_u32_u24 v8, v64, v9, s78\n\t"
        "v_min_u32 v84, v84, v8\n\t"
        "v_mad_u32_u24 v8, v65, v9, s78\n\t"
        "v_min_u32 v85, v85, v8\n\t"
        "v_mad_u32_u24 v8, v66, v9, s78\n\t"
        "v_min_u32 v86, v86, v8\n\t"
        "v_mad_u32_u24 v8, v67, v9, s78\n\t"
        "v_min_u32 v87, v87, v8\n\t"
        "v_mad_u32_u24 v8, v68, v9, s78\n\t"
        "v_min_u32 v88, v88, v8\n\t"
        "v_mad_u32_u24 v8, v69, v9, s78\n\t"
        "v_min_u32 v89, v89, v8\n\t"
        "v_mad_u32_u24 v8, v70, v9, s78\n\t"
        "v_min_u32 v90, v90, v8\n\t"
        "v_mad_u32_u24 v8, v71, v9, s78\n\t"
        "v_min_u32 v91, v91, v8\n\t"
        "v_mad_u32_u24 v8, v72, v9, s78\n\t"
        "v_min_u32 v92, v92, v8\n\t"
        "v_mad_u32_u24 v8, v73, v9, s78\n\t"
        "v_min_u32 v93, v93, v8\n\t"
        "v_mad_u32_u24 v8, v74, v9, s78\n\t"
        "v_min_u32 v94, v94, v8\n\t"
        "v_mad_u32_u24 v8, v75, v9, s78\n\t"
        "v_min_u32 v95, v95, v8\n\t"
        "s_branch .L7_ov1\n"
        ".L7_mk:\n\t"
        "s_mul_i32 s78, s68, 0x4000400\n\t"
        "s_or_b32 s78, s78, 0xffffff\n\t"
        "v_mov_b32 v6, s78\n\t"
        "v_add_u32 v112, -4, v112\n\t"
        "v_add_u32 v113, -4, v113\n\t"
        "s_mov_b64 exec, s[34:35]\n\t"
        "ds_write_b32 v112, v6\n\t"
        "s_mov_b64 exec, s[36:37]\n\t"
        "ds_write_b32 v113, v6\n\t"
        "s_mov_b64 exec, -1\n\t"
        "s_and_b32 s78, s67, 1\n\t"
        "s_lshl_b32 s78, s78, 24\n\t"
        "v_mov_b32 v6, s78\n\t"
        "ds_xor_b32 v112, v6\n\t"
        "ds_xor_b32 v113, v6\n\t"
        "s_branch .L7_mk1\n"
        ".L7_P2:\n\t"
        "s_setprio 3\n\t"
        "v_lshrrev_b32 v15, 3, v1\n\t"
        "v_mul_u32_u24 v15, 0xa0, v15\n\t"
        "v_add_u32 v15, 68864, v15\n\t"
        "v_and_b32 v4, 7, v1\n\t"
        "v_lshl_add_u32 v5, v4, 4, v15\n\t"
        "v_lshl_add_u32 v15, v4, 2, v15\n\t"
        "s_mov_b32 s34, 0x0c0c0c0c\n\t"
        "s_mov_b32 s35, s34\n\t"
        "s_lshl_b64 s[36:37], s[34:35], 2\n\t"
        "s_lshl_b64 s[38:39], s[34:35], 4\n\t"
        "ds_read_b32 v6, v2 offset:1984\n\t"
        "s_waitcnt lgkmcnt(0)\n\t"
        "s_add_u32 s68, s68, 1\n\t"
        "v_mov_b32 v4, s68\n\t"
        "ds_write_b32 v2, v4 offset:76\n\t"
        "s_cmp_eq_u32 s70, 0\n\t"
        "s_cbranch_scc0 .L7_P3\n\t"
        "s_lshl_b32 s78, s68, 24\n\t"
        "v_mov_b32 v8, s78\n\t"
        "s_xor_b32 s78, s78, 0x5a000000\n\t"
        "v_mov_b32 v9, s78\n\t"
        "v_cndmask_b32 v8, v8, v9, s[34:35]\n\t"
        "s_mul_i32 s78, s68, 0x01010000\n\t"
        "v_mov_b32 v9, s78\n\t"
        "v_cndmask_b32 v8, v8, v9, s[36:37]\n\t"
        "s_mul_i32 s55, s68, 0x4000400\n\t"
        "v_xor_b32 v9, s55, v6\n\t"
        "v_cndmask_b32 v8, v8, v9, s[38:39]\n\t"
        "v_and_b32 v8, 0xff000000, v8\n\t"
        "v_or_b32 v8, 0xff0000, v8\n\t"
        "v_mov_b32 v9, v8\n\t"
        "v_mov_b64 v[10:11], v[8:9]\n\t"
        "ds_write_b128 v5, v[8:11] offset:0\n\t"
        "ds_write_b128 v5, v[8:11] offset:1280\n\t"
        "ds_write_b128 v5, v[8:11] offset:2560\n\t"
        "ds_write_b128 v5, v[8:11] offset:3840\n\t"
        "ds_write_b128 v5, v[8:11] offset:5120\n\t"
        "ds_write_b128 v5, v[8:11] offset:6400\n\t"
        "ds_write_b128 v5, v[8:11] offset:7680\n\t"
        "ds_write_b128 v5, v[8:11] offset:8960\n\t"
        "ds_write_b128 v5, v[8:11] offset:10240\n\t"
        "ds_write_b128 v5, v[8:11] offset:11520\n\t"
        "ds_write_b128 v5, v[8:11] offset:12800\n\t"
        "ds_write_b128 v5, v[8:11] offset:14080\n\t"
        "s_waitcnt lgkmcnt(6)\n\t"
        "ds_write_b128 v5, v[8:11] offset:15360\n\t"
        "ds_write_b128 v5, v[8:11] offset:16640\n\t"
        "ds_write_b128 v5, v[8:11] offset:17920\n\t"
        "ds_write_b128 v5, v[8:11] offset:19200\n"
        ".L7_P3:\n\t"
        "s_mov_b64 exec, 1\n\t"
        "ds_add_u32 v2, v3 offset:104\n\t"
        "s_mov_b64 exec, -1\n\t"
        "v_lshlrev_b32 v12, 4, v1\n\t"
        "s_cmp_eq_u32 s70, 0\n\t"
        "s_cbranch_scc0 .L7_Q\n\t"
        "v_add_u32 v15, 10240, v15\n\t"
        "v_add_u32 v97, 1280, v15\n\t"
        "v_add_u32 v98, 2560, v15\n\t"
        "v_add_u32 v99, 3840, v15\n\t"
        "v_add_u32 v100, 5120, v15\n\t"
        "v_add_u32 v101, 6400, v15\n\t"
        "v_add_u32 v102, 7680, v15\n\t"
        "v_add_u32 v103, 8960, v15\n\t"
        "s_lshl_b32 s48, s71, 14\n\t"
        "s_add_u32 s48, s48, 1056768\n\t"
        "s_add_u32 s49, s48, 4096\n"
        ".L7_w44:\n\t"
        "ds_read_b32 v0, v2 offset:112\n\t"
        "s_waitcnt lgkmcnt(0)\n\t"
        "v_cmp_gt_u16 vcc, 1, v0\n\t"
        "s_cbranch_vccz .L7_d45\n\t"
        "s_sleep 0\n\t"
        "s_sub_i32 s83, s83, 1\n\t"
        "s_cmp_gt_i32 s83, 0\n\t"
        "s_cbranch_scc1 .L7_w44\n"
        ".L7_d45:\n\t"
        "ds_read2_b32 v[16:17], v15 offset0:0 offset1:8\n\t"
        "ds_read2_b32 v[18:19], v15 offset0:16 offset1:24\n\t"
        "ds_read2_b32 v[20:21], v97 offset0:0 offset1:8\n\t"
        "ds_read2_b32 v[22:23], v97 offset0:16 offset1:24\n\t"
        "ds_read2_b32 v[24:25], v98 offset0:0 offset1:8\n\t"
        "ds_read2_b32 v[26:27], v98 offset0:16 offset1:24\n\t"
        "ds_read2_b32 v[28:29], v99 offset0:0 offset1:8\n\t"
        "ds_read2_b32 v[30:31], v99 offset0:16 offset1:24\n\t"
        "ds_read2_b32 v[32:33], v100 offset0:0 offset1:8\n\t"
        "ds_read2_b32 v[34:35], v100 offset0:16 offset1:24\n\t"
        "ds_read2_b32 v[36:37], v101 offset0:0 offset1:8\n\t"
        "ds_read2_b32 v[38:39], v101 offset0:16 offset1:24\n\t"
        "s_waitcnt lgkmcnt(10)\n\t"
        "buffer_store_dwordx4 v[16:19], v12, s[20:23], s48 offen offset:0 sc1\n\t"
        "ds_read2_b32 v[40:41], v102 offset0:0 offset1:8\n\t"
        "ds_read2_b32 v[42:43], v102 offset0:16 offset1:24\n\t"
        "s_waitcnt lgkmcnt(10)\n\t"
        "buffer_store_dwordx4 v[20:23], v12, s[20:23], s48 offen offset:1024 sc1\n\t"
        "ds_read2_b32 v[44:45], v103 offset0:0 offset1:8\n\t"
        "ds_read2_b32 v[46:47], v103 offset0:16 offset1:24\n\t"
        "s_mov_b64 exec, 1\n\t"
        "ds_add_u32 v2, v3 offset:72\n\t"
        "s_mov_b64 exec, -1\n\t"
        "s_waitcnt lgkmcnt(11)\n\t"
        "buffer_store_dwordx4 v[24:27], v12, s[20:23], s48 offen offset:2048 sc1\n\t"
        "s_waitcnt lgkmcnt(9)\n\t"
        "buffer_store_dwordx4 v[28:31], v12, s[20:23], s48 offen offset:3072 sc1\n\t"
        "s_waitcnt lgkmcnt(7)\n\t"
        "buffer_store_dwordx4 v[32:35], v12, s[20:23], s49 offen offset:0 sc1\n\t"
        "s_waitcnt lgkmcnt(5)\n\t"
        "buffer_store_dwordx4 v[36:39], v12, s[20:23], s49 offen offset:1024 sc1\n\t"
        "s_waitcnt lgkmcnt(3)\n\t"
        "buffer_store_dwordx4 v[40:43], v12, s[20:23], s49 offen offset:2048 sc1\n\t"
        "s_waitcnt lgkmcnt(1)\n\t"
        "buffer_store_dwordx4 v[44:47], v12, s[20:23], s49 offen offset:3072 sc1\n\t"
        "s_mov_b64 exec, 1\n\t"
        "ds_add_u32 v2, v3 offset:44\n\t"
        "s_mov_b64 exec, -1\n\t"
        "s_branch .L7_Q\n"
        ".L7_V2:\n\t"
        "v_and_b32 v5, 7, v1\n\t"
        "v_mul_u32_u24 v5, 0x420, v5\n\t"
        "v_and_b32 v6, 8, v1\n\t"
        "v_lshl_add_u32 v5, v6, 6, v5\n\t"
        "v_and_b32 v6, 48, v1\n\t"
        "v_add_u32 v5, v5, v6\n\t"
        "v_add_u32 v5, 68864, v5\n\t"
        "s_lshr_b32 s78, s81, 1\n\t"
        "s_lshl3_add_u32 s78, s78, s80\n\t"
        "s_and_b32 s79, s81, 1\n\t"
        "s_lshl1_add_u32 s78, s78, s79\n\t"
        "s_lshl_b32 s82, s78, 10\n\t"
        "s_add_u32 s82, s82, 131072\n"
        ".L7_w40:\n\t"
        "ds_read_b32 v0, v2 offset:104\n\t"
        "ds_read_b32 v6, v2 offset:76\n\t"
        "s_waitcnt lgkmcnt(1)\n\t"
        "v_cmp_gt_u16 vcc, 1, v0\n\t"
        "s_cbranch_vccz .L7_d41\n\t"
        "s_waitcnt lgkmcnt(0)\n\t"
        "s_sleep 1\n\t"
        "s_sub_i32 s83, s83, 1\n\t"
        "s_cmp_gt_i32 s83, 0\n\t"
        "s_cbranch_scc1 .L7_w40\n"
        ".L7_d41:\n\t"
        "s_waitcnt lgkmcnt(0)\n\t"
        "v_readfirstlane_b32 s68, v6\n\t"
        "v_mov_b32 v7, 2\n\t"
        "s_and_b32 s73, s68, 1\n\t"
        "s_cselect_b32 s73, 1, 0x10000\n\t"
        "v_mov_b32 v16, s73\n\t"
        "v_and_b32 v17, 7, v1\n\t"
        "v_add_u32 v17, 1, v17\n\t"
        "v_lshrrev_b32 v17, v17, s68\n\t"
        "s_mov_b32 s49, 0x10001\n\t"
        "v_and_b32 v17, s49, v17\n\t"
        "s_mov_b32 s48, 0x3db8aa3b\n\t"
        "s_waitcnt vmcnt(0)\n\t"
        "ds_read_b128 v[32:35], v5 offset:0\n\t"
        "ds_read_b128 v[36:39], v5 offset:8448\n\t"
        "ds_read_b128 v[40:43], v5 offset:16896\n\t"
        "ds_read_b128 v[44:47], v5 offset:64\n\t"
        "ds_read_b128 v[48:51], v5 offset:8512\n\t"
        "ds_read_b128 v[52:55], v5 offset:16960\n\t"
        "ds_read_b128 v[56:59], v5 offset:128\n\t"
        "ds_read_b128 v[60:63], v5 offset:8576\n\t"
        "ds_read_b128 v[64:67], v5 offset:17024\n\t"
        "ds_read_b128 v[68:71], v5 offset:192\n\t"
        "ds_read_b128 v[72:75], v5 offset:8640\n\t"
        "ds_read_b128 v[76:79], v5 offset:17088\n\t"
        "s_waitcnt lgkmcnt(9)\n\t"
        "v_mfma_f32_16x16x32_bf16 v[20:23], v[36:39], v[32:35], 0\n\t"
        "v_mfma_f32_16x16x32_bf16 v[24:27], v[40:43], v[32:35], 0\n\t"
        "ds_read_b128 v[80:83], v5 offset:256\n\t"
        "ds_read_b128 v[84:87], v5 offset:8704\n\t"
        "ds_read_b128 v[88:91], v5 offset:17152\n\t"
        "s_waitcnt lgkmcnt(9)\n\t"
        "v_mfma_f32_16x16x32_bf16 v[20:23], v[48:51], v[44:47], v[20:23]\n\t"
        "v_mfma_f32_16x16x32_bf16 v[24:27], v[52:55], v[44:47], v[24:27]\n\t"
        "ds_read_b128 v[92:95], v5 offset:320\n\t"
        "ds_read_b128 v[96:99], v5 offset:8768\n\t"
        "ds_read_b128 v[100:103], v5 offset:17216\n\t"
        "s_waitcnt lgkmcnt(9)\n\t"
        "v_mfma_f32_16x16x32_bf16 v[20:23], v[60:63], v[56:59], v[20:23]\n\t"
        "v_mfma_f32_16x16x32_bf16 v[24:27], v[64:67], v[56:59], v[24:27]\n\t"
        "ds_read_b128 v[104:107], v5 offset:384\n\t"
        "ds_read_b128 v[108:111], v5 offset:8832\n\t"
        "ds_read_b128 v[112:115], v5 offset:17280\n\t"
        "s_waitcnt lgkmcnt(9)\n\t"
        "v_mfma_f32_16x16x32_bf16 v[20:23], v[72:75], v[68:71], v[20:23]\n\t"
        "v_mfma_f32_16x16x32_bf16 v[24:27], v[76:79], v[68:71], v[24:27]\n\t"
        "ds_read_b128 v[116:119], v5 offset:448\n\t"
        "ds_read_b128 v[120:123], v5 offset:8896\n\t"
        "ds_read_b128 v[124:127], v5 offset:17344\n\t"
        "s_waitcnt lgkmcnt(9)\n\t"
        "v_mfma_f32_16x16x32_bf16 v[20:23], v[84:87], v[80:83], v[20:23]\n\t"
        "v_mfma_f32_16x16x32_bf16 v[24:27], v[88:91], v[80:83], v[24:27]\n\t"
        "s_mov_b64 exec, 1\n\t"
        "ds_add_u32 v2, v7 offset:72\n\t"
        "s_mov_b64 exec, -1\n\t"
        "s_waitcnt lgkmcnt(7)\n\t"
        "v_mfma_f32_16x16x32_bf16 v[20:23], v[96:99], v[92:95], v[20:23]\n\t"
        "v_mfma_f32_16x16x32_bf16 v[24:27], v[100:103], v[92:95], v[24:27]\n\t"
        "s_waitcnt lgkmcnt(4)\n\t"
        "v_mfma_f32_16x16x32_bf16 v[20:23], v[108:111], v[104:107], v[20:23]\n\t"
        "v_mfma_f32_16x16x32_bf16 v[24:27], v[112:115], v[104:107], v[24:27]\n\t"
        "s_waitcnt lgkmcnt(1)\n\t"
        "v_mfma_f32_16x16x32_bf16 v[20:23], v[120:123], v[116:119], v[20:23]\n\t"
        "v_mfma_f32_16x16x32_bf16 v[24:27], v[124:127], v[116:119], v[24:27]\n\t"
        "s_nop 6\n\t"
        "v_mul_f32 v20, s48, v20\n\t"
        "v_mul_f32 v21, s48, v21\n\t"
        "v_mul_f32 v22, s48, v22\n\t"
        "v_mul_f32 v23, s48, v23\n\t"
        "v_mul_f32 v24, s48, v24\n\t"
        "v_mul_f32 v25, s48, v25\n\t"
        "v_mul_f32 v26, s48, v26\n\t"
        "v_mul_f32 v27, s48, v27\n\t"
        "s_mov_b32 s48, 0x07060302\n\t"
        "v_add_u32 v20, 0x10000, v20\n\t"
        "v_add_u32 v21, 0x10000, v21\n\t"
        "v_add_u32 v22, 0x10000, v22\n\t"
        "v_add_u32 v23, 0x10000, v23\n\t"
        "v_perm_b32 v20, v21, v20, s48\n\t"
        "v_perm_b32 v21, v23, v22, s48\n\t"
        "v_cvt_pk_bf16_f32 v22, v24, v25\n\t"
        "v_cvt_pk_bf16_f32 v23, v26, v27\n\t"
        "v_bfi_b32 v20, s49, v16, v20\n\t"
        "v_bfi_b32 v21, s49, v17, v21\n\t"
        "buffer_store_dwordx4 v[20:23], v12, s[20:23], s82 offen sc1\n\t"
        "s_mov_b64 exec, 1\n\t"
        "ds_add_u32 v2, v7 offset:44\n\t"
        "s_mov_b64 exec, -1\n\t"
        "s_branch .L7_Q\n"
        ".L7_T2:\n\t"
        "v_lshlrev_b32 v4, 4, v1\n\t"
        "v_add_u32 v4, 148160, v4\n\t"
        "v_mul_u32_u24 v5, 196, v1\n\t"
        "v_add_u32 v5, 148160, v5\n\t"
        "v_mov_b32 v12, 0xffff\n\t"
        "v_mov_b32 v13, v12\n\t"
        "v_mov_b64 v[14:15], v[12:13]\n\t"
        "ds_write_b128 v4, v[12:15] offset:0\n\t"
        "ds_write_b128 v4, v[12:15] offset:1024\n\t"
        "ds_write_b128 v4, v[12:15] offset:2048\n\t"
        "ds_write_b128 v4, v[12:15] offset:3072\n\t"
        "ds_write_b128 v4, v[12:15] offset:4096\n\t"
        "ds_write_b128 v4, v[12:15] offset:5120\n\t"
        "ds_write_b128 v4, v[12:15] offset:6144\n\t"
        "ds_write_b128 v4, v[12:15] offset:7168\n\t"
        "ds_write_b128 v4, v[12:15] offset:8192\n\t"
        "ds_write_b128 v4, v[12:15] offset:9216\n\t"
        "ds_write_b128 v4, v[12:15] offset:10240\n\t"
        "ds_write_b128 v4, v[12:15] offset:11264\n\t"
        "s_waitcnt lgkmcnt(6)\n\t"
        "ds_write_b128 v4, v[12:15] offset:12288\n\t"
        "ds_write_b128 v4, v[12:15] offset:13312\n\t"
        "ds_write_b128 v4, v[12:15] offset:14336\n\t"
        "ds_write_b128 v4, v[12:15] offset:15360\n\t"
        "ds_write_b32 v5, v2 offset:192\n\t"
        "s_mov_b64 exec, 0xffff\n\t"
        "ds_write_b32 v5, v2 offset:12736\n\t"
        "s_mov_b64 exec, 1\n\t"
        "ds_add_u32 v2, v3 offset:8\n\t"
        "s_mov_b64 exec, -1\n\t"
        "s_and_b32 s82, s82, -1024\n\t"
        "v_mov_b32 v9, 4\n"
        ".L7_g42:\n\t"
        "s_memrealtime s[78:79]\n\t"
        "s_waitcnt lgkmcnt(0)\n\t"
        "s_sub_u32 s78, s78, s74\n\t"
        "s_cmp_ge_i32 s78, 150\n\t"
        "s_cbranch_scc1 .L7_h43\n\t"
        "s_sleep 1\n\t"
        "s_branch .L7_g42\n"
        ".L7_h43:\n"
        ".L7_b1:\n\t"
        "s_buffer_load_dword s40, s[20:23], s82 offset:0 glc\n\t"
        "s_buffer_load_dword s41, s[20:23], s82 offset:128 glc\n\t"
        "s_buffer_load_dword s42, s[20:23], s82 offset:256 glc\n\t"
        "s_buffer_load_dword s43, s[20:23], s82 offset:384 glc\n\t"
        "s_buffer_load_dword s44, s[20:23], s82 offset:512 glc\n\t"
        "s_buffer_load_dword s45, s[20:23], s82 offset:640 glc\n\t"
        "s_buffer_load_dword s46, s[20:23], s82 offset:768 glc\n\t"
        "s_buffer_load_dword s47, s[20:23], s82 offset:896 glc\n\t"
        "s_waitcnt lgkmcnt(0)\n\t"
        "s_mov_b32 s80, 0\n\t"
        "s_mov_b32 s81, 0\n\t"
        "s_sub_u32 s40, s40, s74\n\t"
        "s_max_i32 s80, s80, s40\n\t"
        "s_min_i32 s81, s81, s40\n\t"
        "s_sub_u32 s41, s41, s74\n\t"
        "s_max_i32 s80, s80, s41\n\t"
        "s_min_i32 s81, s81, s41\n\t"
        "s_sub_u32 s42, s42, s74\n\t"
        "s_max_i32 s80, s80, s42\n\t"
        "s_min_i32 s81, s81, s42\n\t"
        "s_sub_u32 s43, s43, s74\n\t"
        "s_max_i32 s80, s80, s43\n\t"
        "s_min_i32 s81, s81, s43\n\t"
        "s_sub_u32 s44, s44, s74\n\t"
        "s_max_i32 s80, s80, s44\n\t"
        "s_min_i32 s81, s81, s44\n\t"
        "s_sub_u32 s45, s45, s74\n\t"
        "s_max_i32 s80, s80, s45\n\t"
        "s_min_i32 s81, s81, s45\n\t"
        "s_sub_u32 s46, s46, s74\n\t"
        "s_max_i32 s80, s80, s46\n\t"
        "s_min_i32 s81, s81, s46\n\t"
        "s_sub_u32 s47, s47, s74\n\t"
        "s_max_i32 s80, s80, s47\n\t"
        "s_min_i32 s81, s81, s47\n\t"
        "s_sub_u32 s81, s80, s81\n\t"
        "s_cmp_lt_u32 s81, 0x3e8\n\t"
        "s_cbranch_scc1 .L7_b2\n\t"
        "v_mov_b32 v9, 0\n\t"
        "s_mov_b32 s80, 0\n\t"
        "s_sleep 4\n\t"
        "s_memrealtime s[78:79]\n\t"
        "s_waitcnt lgkmcnt(0)\n\t"
        "s_sub_u32 s78, s78, s74\n\t"
        "s_cmp_lt_u32 s78, 0x4b0\n\t"
        "s_cbranch_scc1 .L7_b1\n"
        ".L7_b2:\n\t"
        "s_add_u32 s80, s80, 1\n\t"
        "v_mov_b32 v7, s80\n\t"
        "s_mov_b64 exec, 1\n\t"
        "ds_add_u32 v2, v7 offset:92\n\t"
        "s_mov_b64 exec, -1\n\t"
        "s_branch .L7_Q0\n"
        ".L7_R2:\n\t"
        "s_sub_u32 s79, s64, s72\n\t"
        "s_min_i32 s79, s79, s65\n\t"
        "s_max_i32 s79, s79, 0\n\t"
        "s_mov_b32 exec_hi, 0xff\n\t"
        "v_mov_b32 v32, v1\n\t"
        "v_add_u32 v33, 40, v1\n\t"
        "v_cmp_gt_u32 s[32:33], s79, v32\n\t"
        "v_cmp_gt_u32 s[34:35], s79, v33\n\t"
        "s_sub_u32 s78, 0xff, s65\n\t"
        "v_add_u32 v34, s78, v32\n\t"
        "v_add_u32 v6, 0xffffffb0, v34\n\t"
        "v_cmp_gt_u32 vcc, 0xff, v34\n\t"
        "s_nop 1\n\t"
        "v_cndmask_b32 v34, v6, v34, vcc\n\t"
        "v_add_u32 v35, s78, v33\n\t"
        "v_add_u32 v6, 0xffffffb0, v35\n\t"
        "v_cmp_gt_u32 vcc, 0xff, v35\n\t"
        "s_nop 1\n\t"
        "v_cndmask_b32 v35, v6, v35, vcc\n\t"
        "v_mov_b32 v12, 0x8000\n\t"
        "v_cndmask_b32 v44, v12, 0, s[32:33]\n\t"
        "v_cndmask_b32 v45, v12, 0, s[34:35]\n\t"
        "v_mov_b32 v46, -1\n\t"
        "v_mov_b32 v47, 0xff\n\t"
        "v_mov_b32 v40, 0\n\t"
        "v_mov_b32 v48, -1\n\t"
        "v_mov_b32 v49, 0xff\n\t"
        "v_mov_b32 v41, 0\n\t"
        "s_mov_b32 s40, 0x23108954\n\t"
        "s_mov_b32 s41, 0x67\n\t"
        "s_mov_b32 s42, 0x1986732\n\t"
        "s_mov_b32 s43, 0x45\n\t"
        "s_cmp_eq_u32 s70, 0\n\t"
        "s_cselect_b64 s[44:45], s[40:41], s[42:43]\n\t"
        "s_cselect_b64 s[46:47], s[42:43], s[40:41]\n\t"
        "s_cselect_b32 s40, 56, 0\n\t"
        "s_cselect_b32 s41, 0, 56\n\t"
        "s_cselect_b32 s42, 0x800, 0\n\t"
        "s_cselect_b32 s43, 0, 0x800\n\t"
        "v_lshlrev_b32 v9, 1, v1\n\t"
        "s_waitcnt vmcnt(0)\n\t"
        "v_lshlrev_b32 v16, 16, v16\n\t"
        "v_lshlrev_b32 v17, 16, v17\n\t"
        "v_mul_f32 v36, v16, v16\n\t"
        "v_mul_f32 v37, v17, v17\n\t"
        "v_rcp_f32 v36, v36\n\t"
        "v_rcp_f32 v37, v37\n\t"
        "v_or3_b32 v16, v16, v34, v44\n\t"
        "v_or3_b32 v17, v17, v35, v45\n\t"
        "v_add_f32 v36, 0xc0600000, v36\n\t"
        "v_add_f32 v37, 0xc0600000, v37\n\t"
        "v_cvt_u32_f32 v36, v36\n\t"
        "v_cvt_u32_f32 v37, v37\n\t"
        "v_min_u32 v36, 31, v36\n\t"
        "v_min_u32 v37, 31, v37\n\t"
        "v_cndmask_b32 v36, 0, v36, s[32:33]\n\t"
        "v_cndmask_b32 v37, 0, v37, s[34:35]\n\t"
        "v_bfe_i32 v38, v36, 4, 1\n\t"
        "v_bfe_i32 v39, v37, 4, 1\n\t"
        "v_cmp_gt_i32 s[32:33], 0, v38\n\t"
        "v_cmp_gt_i32 s[34:35], 0, v39\n\t"
        "s_nop 0\n\t"
        "v_bitop3_b32 v10, v46, s32, v38 bitop3:0x40\n\t"
        "v_bitop3_b32 v46, v46, s32, v38 bitop3:0x90\n\t"
        "v_bcnt_u32_b32 v40, v10, v40\n\t"
        "v_bitop3_b32 v11, v47, s33, v38 bitop3:0x40\n\t"
        "v_bitop3_b32 v47, v47, s33, v38 bitop3:0x90\n\t"
        "v_bcnt_u32_b32 v40, v11, v40\n\t"
        "v_bitop3_b32 v10, v48, s34, v39 bitop3:0x40\n\t"
        "v_bitop3_b32 v48, v48, s34, v39 bitop3:0x90\n\t"
        "v_bcnt_u32_b32 v41, v10, v41\n\t"
        "v_bitop3_b32 v11, v49, s35, v39 bitop3:0x40\n\t"
        "v_bitop3_b32 v49, v49, s35, v39 bitop3:0x90\n\t"
        "v_bcnt_u32_b32 v41, v11, v41\n\t"
        "v_bfe_i32 v38, v36, 3, 1\n\t"
        "v_bfe_i32 v39, v37, 3, 1\n\t"
        "v_cmp_gt_i32 s[32:33], 0, v38\n\t"
        "v_cmp_gt_i32 s[34:35], 0, v39\n\t"
        "s_nop 0\n\t"
        "v_bitop3_b32 v10, v46, s32, v38 bitop3:0x40\n\t"
        "v_bitop3_b32 v46, v46, s32, v38 bitop3:0x90\n\t"
        "v_bcnt_u32_b32 v40, v10, v40\n\t"
        "v_bitop3_b32 v11, v47, s33, v38 bitop3:0x40\n\t"
        "v_bitop3_b32 v47, v47, s33, v38 bitop3:0x90\n\t"
        "v_bcnt_u32_b32 v40, v11, v40\n\t"
        "v_bitop3_b32 v10, v48, s34, v39 bitop3:0x40\n\t"
        "v_bitop3_b32 v48, v48, s34, v39 bitop3:0x90\n\t"
        "v_bcnt_u32_b32 v41, v10, v41\n\t"
        "v_bitop3_b32 v11, v49, s35, v39 bitop3:0x40\n\t"
        "v_bitop3_b32 v49, v49, s35, v39 bitop3:0x90\n\t"
        "v_bcnt_u32_b32 v41, v11, v41\n\t"
        "v_bfe_i32 v38, v36, 2, 1\n\t"
        "v_bfe_i32 v39, v37, 2, 1\n\t"
        "v_cmp_gt_i32 s[32:33], 0, v38\n\t"
        "v_cmp_gt_i32 s[34:35], 0, v39\n\t"
        "s_nop 0\n\t"
        "v_bitop3_b32 v10, v46, s32, v38 bitop3:0x40\n\t"
        "v_bitop3_b32 v46, v46, s32, v38 bitop3:0x90\n\t"
        "v_bcnt_u32_b32 v40, v10, v40\n\t"
        "v_bitop3_b32 v11, v47, s33, v38 bitop3:0x40\n\t"
        "v_bitop3_b32 v47, v47, s33, v38 bitop3:0x90\n\t"
        "v_bcnt_u32_b32 v40, v11, v40\n\t"
        "v_bitop3_b32 v10, v48, s34, v39 bitop3:0x40\n\t"
        "v_bitop3_b32 v48, v48, s34, v39 bitop3:0x90\n\t"
        "v_bcnt_u32_b32 v41, v10, v41\n\t"
        "v_bitop3_b32 v11, v49, s35, v39 bitop3:0x40\n\t"
        "v_bitop3_b32 v49, v49, s35, v39 bitop3:0x90\n\t"
        "v_bcnt_u32_b32 v41, v11, v41\n\t"
        "v_bfe_i32 v38, v36, 1, 1\n\t"
        "v_bfe_i32 v39, v37, 1, 1\n\t"
        "v_cmp_gt_i32 s[32:33], 0, v38\n\t"
        "v_cmp_gt_i32 s[34:35], 0, v39\n\t"
        "s_nop 0\n\t"
        "v_bitop3_b32 v10, v46, s32, v38 bitop3:0x40\n\t"
        "v_bitop3_b32 v46, v46, s32, v38 bitop3:0x90\n\t"
        "v_bcnt_u32_b32 v40, v10, v40\n\t"
        "v_bitop3_b32 v11, v47, s33, v38 bitop3:0x40\n\t"
        "v_bitop3_b32 v47, v47, s33, v38 bitop3:0x90\n\t"
        "v_bcnt_u32_b32 v40, v11, v40\n\t"
        "v_bitop3_b32 v10, v48, s34, v39 bitop3:0x40\n\t"
        "v_bitop3_b32 v48, v48, s34, v39 bitop3:0x90\n\t"
        "v_bcnt_u32_b32 v41, v10, v41\n\t"
        "v_bitop3_b32 v11, v49, s35, v39 bitop3:0x40\n\t"
        "v_bitop3_b32 v49, v49, s35, v39 bitop3:0x90\n\t"
        "v_bcnt_u32_b32 v41, v11, v41\n\t"
        "v_bfe_i32 v38, v36, 0, 1\n\t"
        "v_bfe_i32 v39, v37, 0, 1\n\t"
        "v_cmp_gt_i32 s[32:33], 0, v38\n\t"
        "v_cmp_gt_i32 s[34:35], 0, v39\n\t"
        "s_nop 0\n\t"
        "v_bitop3_b32 v10, v46, s32, v38 bitop3:0x40\n\t"
        "v_bitop3_b32 v46, v46, s32, v38 bitop3:0x90\n\t"
        "v_bcnt_u32_b32 v40, v10, v40\n\t"
        "v_bitop3_b32 v11, v47, s33, v38 bitop3:0x40\n\t"
        "v_bitop3_b32 v47, v47, s33, v38 bitop3:0x90\n\t"
        "v_bcnt_u32_b32 v40, v11, v40\n\t"
        "v_bitop3_b32 v10, v48, s34, v39 bitop3:0x40\n\t"
        "v_bitop3_b32 v48, v48, s34, v39 bitop3:0x90\n\t"
        "v_bcnt_u32_b32 v41, v10, v41\n\t"
        "v_bitop3_b32 v11, v49, s35, v39 bitop3:0x40\n\t"
        "v_bitop3_b32 v49, v49, s35, v39 bitop3:0x90\n\t"
        "v_bcnt_u32_b32 v41, v11, v41\n\t"
        "v_mbcnt_lo_u32_b32 v40, v46, v40\n\t"
        "v_mbcnt_hi_u32_b32 v40, v47, v40\n\t"
        "v_and_b32 v6, -4, v40\n\t"
        "v_lshrrev_b64 v[10:11], v6, s[44:45]\n\t"
        "v_and_b32 v10, 15, v10\n\t"
        "v_and_b32 v6, 3, v40\n\t"
        "v_lshl_add_u32 v6, v10, 2, v6\n\t"
        "v_add_u32 v36, s40, v6\n\t"
        "v_and_b32 v6, 15, v36\n\t"
        "v_lshlrev_b32 v6, 4, v6\n\t"
        "v_lshl_add_u32 v6, v36, 8, v6\n\t"
        "v_subrev_u32 v38, s42, v6\n\t"
        "v_lshlrev_b32 v36, 2, v36\n\t"
        "ds_write_b32 v36, v16 offset:512\n\t"
        "ds_write_b16 v9, v38 offset:1792\n\t"
        "v_mbcnt_lo_u32_b32 v41, v48, v41\n\t"
        "v_mbcnt_hi_u32_b32 v41, v49, v41\n\t"
        "v_and_b32 v6, -4, v41\n\t"
        "v_lshrrev_b64 v[10:11], v6, s[46:47]\n\t"
        "v_and_b32 v10, 15, v10\n\t"
        "v_and_b32 v6, 3, v41\n\t"
        "v_lshl_add_u32 v6, v10, 2, v6\n\t"
        "v_add_u32 v37, s41, v6\n\t"
        "v_and_b32 v6, 15, v37\n\t"
        "v_lshlrev_b32 v6, 4, v6\n\t"
        "v_lshl_add_u32 v6, v37, 8, v6\n\t"
        "v_subrev_u32 v39, s43, v6\n\t"
        "v_lshlrev_b32 v37, 2, v37\n\t"
        "ds_write_b32 v37, v17 offset:512\n\t"
        "ds_write_b16 v9, v39 offset:1872\n\t"
        "s_mov_b64 exec, 1\n\t"
        "ds_add_u32 v2, v3 offset:8\n\t"
        "s_mov_b64 exec, -1\n"
        ".L7_w46:\n\t"
        "ds_read_b64 v[4:5], v2 offset:72\n\t"
        "s_waitcnt lgkmcnt(0)\n\t"
        "v_cmp_gt_u16 vcc, 2, v4\n\t"
        "s_cbranch_vccz .L7_d47\n\t"
        "s_sleep 2\n\t"
        "s_sub_i32 s83, s83, 1\n\t"
        "s_cmp_gt_i32 s83, 0\n\t"
        "s_cbranch_scc1 .L7_w46\n"
        ".L7_d47:\n\t"
        "v_readfirstlane_b32 s68, v5\n\t"
        "v_lshlrev_b32 v22, 4, v1\n\t"
        "v_add_u32 v9, 142848, v22\n\t"
        "v_and_b32 v10, 60, v1\n\t"
        "ds_read_b32 v24, v10 offset:704\n\t"
        "ds_read_b32 v25, v10 offset:768\n\t"
        "ds_read_b32 v26, v10 offset:832\n\t"
        "v_and_b32 v11, 15, v1\n\t"
        "v_lshrrev_b32 v12, 4, v1\n\t"
        "v_xor_b32 v16, v11, v12\n\t"
        "v_lshlrev_b32 v16, 4, v16\n\t"
        "v_lshl_add_u32 v16, v11, 8, v16\n\t"
        "v_xor_b32 v17, 64, v16\n\t"
        "v_xor_b32 v18, 128, v16\n\t"
        "v_xor_b32 v19, 192, v16\n\t"
        "ds_write_b128 v9, v[16:19]\n\t"
        "s_and_b32 s73, s68, 1\n\t"
        "s_cselect_b32 s73, 1, 0x10000\n\t"
        "v_mov_b32 v20, s73\n\t"
        "v_and_b32 v21, 7, v1\n\t"
        "v_add_u32 v21, 1, v21\n\t"
        "v_lshrrev_b32 v21, v21, s68\n\t"
        "s_mov_b32 s49, 0x10001\n\t"
        "v_and_b32 v21, s49, v21\n\t"
        "v_and_b32 v23, 0x37f, v22\n\t"
        "ds_write_b128 v9, v[20:23] offset:1024\n\t"
        "s_add_u32 s78, s72, s65\n\t"
        "s_sub_u32 s78, s78, 0xff\n\t"
        "s_lshl_b32 s78, s78, 9\n\t"
        "v_and_b32 v13, 48, v22\n\t"
        "v_add_u32 v14, s78, v13\n\t"
        "s_movk_i32 s78, 0x200\n\t"
        "v_bfe_u32 v27, v1, 1, 2\n\t"
        "v_xor_b32 v27, v27, v12\n\t"
        "v_lshlrev_b32 v27, 4, v27\n\t"
        "v_lshl_add_u32 v27, v11, 6, v27\n\t"
        "v_lshlrev_b32 v28, 1, v1\n\t"
        "v_and_b32 v28, 48, v28\n\t"
        "v_xor_b32 v28, v28, v22\n\t"
        "s_waitcnt lgkmcnt(2)\n\t"
        "v_mad_u32_u16 v24, v24, s78, v14\n\t"
        "v_mad_u32_u16 v25, v25, s78, v14\n\t"
        "v_mad_u32_u16 v26, v26, s78, v14\n\t"
        "s_mov_b32 exec_hi, 0\n\t"
        "v_mov_b32 v24, -2.0\n\t"
        "s_mov_b64 exec, -1\n\t"
        "ds_write_b128 v9, v[24:27] offset:2048\n\t"
        "s_mov_b32 s78, 0x3f80\n\t"
        "s_mov_b32 s79, 0\n\t"
        "v_lshlrev_b64 v[30:31], v13, s[78:79]\n\t"
        "s_mov_b32 s78, 0x00f0000f\n\t"
        "s_mov_b32 s79, 0xf0000f00\n\t"
        "v_cndmask_b32 v30, 0, v30, s[78:79]\n\t"
        "v_cndmask_b32 v31, 0, v31, s[78:79]\n\t"
        "ds_write_b128 v9, v[28:31] offset:3072\n\t"
        "s_branch .L7_Q\n"
        ".L7_Q0:\n"
        ".L7_w48:\n\t"
        "ds_read_b64 v[4:5], v2 offset:72\n\t"
        "s_waitcnt lgkmcnt(0)\n\t"
        "v_cmp_gt_u16 vcc, 2, v4\n\t"
        "s_cbranch_vccz .L7_d49\n\t"
        "s_sleep 2\n\t"
        "s_sub_i32 s83, s83, 1\n\t"
        "s_cmp_gt_i32 s83, 0\n\t"
        "s_cbranch_scc1 .L7_w48\n"
        ".L7_d49:\n\t"
        "v_readfirstlane_b32 s68, v5\n"
        ".L7_Q:\n\t"
        "s_setprio 2\n\t"
        "v_and_b32 v5, 7, v1\n\t"
        "v_mul_u32_u24 v5, 0x420, v5\n\t"
        "v_and_b32 v6, 8, v1\n\t"
        "v_lshl_add_u32 v5, v6, 6, v5\n\t"
        "v_and_b32 v6, 48, v1\n\t"
        "v_add_u32 v5, v5, v6\n\t"
        "s_and_b32 s73, s68, 1\n\t"
        "s_cselect_b32 s73, 1, 0x10000\n\t"
        "v_and_b32 v6, 7, v1\n\t"
        "v_add_u32 v6, 1, v6\n\t"
        "v_lshrrev_b32 v6, v6, s68\n\t"
        "s_mov_b32 s49, 0x10001\n\t"
        "v_and_b32 v6, s49, v6\n\t"
        "s_add_u32 s80, s69, s76\n\t"
        "s_and_b32 s80, s80, 3\n\t"
        "s_lshl2_add_u32 s78, s70, s80\n\t"
        "s_lshl_b32 s78, s78, 14\n\t"
        "s_add_u32 s78, s78, 131072\n\t"
        "v_lshl_add_u32 v4, v1, 4, s78\n\t"
        "s_mov_b32 s24, 4096\n\t"
        "s_mov_b32 s25, 8192\n\t"
        "s_mov_b32 s26, 12288\n"
        ".L7_w50:\n\t"
        "ds_read_b32 v0, v2 offset:92\n\t"
        "s_waitcnt lgkmcnt(0)\n\t"
        "v_cmp_gt_u16 vcc, 1, v0\n\t"
        "s_cbranch_vccz .L7_d51\n\t"
        "s_sleep 1\n\t"
        "s_sub_i32 s83, s83, 1\n\t"
        "s_cmp_gt_i32 s83, 0\n\t"
        "s_cbranch_scc1 .L7_w50\n"
        ".L7_d51:\n\t"
        "v_readfirstlane_b32 s78, v0\n\t"
        "s_sub_u32 s78, s78, 1\n\t"
        "s_add_u32 s77, s74, s78\n"
        ".L7_g52:\n\t"
        "s_memrealtime s[78:79]\n\t"
        "s_waitcnt lgkmcnt(0)\n\t"
        "s_sub_u32 s78, s78, s77\n\t"
        "s_cmp_ge_i32 s78, 280\n\t"
        "s_cbranch_scc1 .L7_h53\n\t"
        "s_sleep 1\n\t"
        "s_branch .L7_g52\n"
        ".L7_h53:\n"
        ".L7_w54:\n\t"
        "ds_read_b32 v0, v2 offset:88\n\t"
        "s_waitcnt lgkmcnt(0)\n\t"
        "v_cmp_gt_u16 vcc, 8, v0\n\t"
        "s_cbranch_vccz .L7_d55\n\t"
        "s_sleep 1\n\t"
        "s_sub_i32 s83, s83, 1\n\t"
        "s_cmp_gt_i32 s83, 0\n\t"
        "s_cbranch_scc1 .L7_w54\n"
        ".L7_d55:\n\t"
        "buffer_load_dwordx4 v[64:67], v4, s[20:23], 0 offen offset:0 sc1\n\t"
        "buffer_load_dwordx4 v[68:71], v4, s[20:23], 0 offen offset:1024 sc1\n\t"
        "buffer_load_dwordx4 v[72:75], v4, s[20:23], 0 offen offset:2048 sc1\n\t"
        "buffer_load_dwordx4 v[76:79], v4, s[20:23], 0 offen offset:3072 sc1\n\t"
        "buffer_load_dwordx4 v[80:83], v4, s[20:23], s24 offen offset:0 sc1\n\t"
        "buffer_load_dwordx4 v[84:87], v4, s[20:23], s24 offen offset:1024 sc1\n\t"
        "buffer_load_dwordx4 v[88:91], v4, s[20:23], s24 offen offset:2048 sc1\n\t"
        "buffer_load_dwordx4 v[92:95], v4, s[20:23], s24 offen offset:3072 sc1\n\t"
        "buffer_load_dwordx4 v[96:99], v4, s[20:23], s25 offen offset:0 sc1\n\t"
        "buffer_load_dwordx4 v[100:103], v4, s[20:23], s25 offen offset:1024 sc1\n\t"
        "buffer_load_dwordx4 v[104:107], v4, s[20:23], s25 offen offset:2048 sc1\n\t"
        "buffer_load_dwordx4 v[108:111], v4, s[20:23], s25 offen offset:3072 sc1\n\t"
        "buffer_load_dwordx4 v[112:115], v4, s[20:23], s26 offen offset:0 sc1\n\t"
        "buffer_load_dwordx4 v[116:119], v4, s[20:23], s26 offen offset:1024 sc1\n\t"
        "buffer_load_dwordx4 v[120:123], v4, s[20:23], s26 offen offset:2048 sc1\n\t"
        "buffer_load_dwordx4 v[124:127], v4, s[20:23], s26 offen offset:3072 sc1\n\t"
        "s_setprio 3\n"
        ".L7_w56:\n\t"
        "ds_read_b32 v0, v2 offset:0\n\t"
        "s_waitcnt lgkmcnt(0)\n\t"
        "v_cmp_gt_u16 vcc, 4, v0\n\t"
        "s_cbranch_vccz .L7_d57\n\t"
        "s_sleep 1\n\t"
        "s_sub_i32 s83, s83, 1\n\t"
        "s_cmp_gt_i32 s83, 0\n\t"
        "s_cbranch_scc1 .L7_w56\n"
        ".L7_d57:\n\t"
        "s_branch .L7_qg\n"
        ".L7_qr:\n\t"
        "s_waitcnt lgkmcnt(0)\n\t"
        "s_sleep 8\n\t"
        "s_sub_i32 s83, s83, 64\n\t"
        "s_cmp_gt_i32 s83, 0\n\t"
        "s_cbranch_scc0 .L7_qd\n\t"
        "buffer_load_dwordx4 v[64:67], v4, s[20:23], 0 offen offset:0 sc1\n\t"
        "buffer_load_dwordx4 v[68:71], v4, s[20:23], 0 offen offset:1024 sc1\n\t"
        "buffer_load_dwordx4 v[72:75], v4, s[20:23], 0 offen offset:2048 sc1\n\t"
        "buffer_load_dwordx4 v[76:79], v4, s[20:23], 0 offen offset:3072 sc1\n\t"
        "buffer_load_dwordx4 v[80:83], v4, s[20:23], s24 offen offset:0 sc1\n\t"
        "buffer_load_dwordx4 v[84:87], v4, s[20:23], s24 offen offset:1024 sc1\n\t"
        "buffer_load_dwordx4 v[88:91], v4, s[20:23], s24 offen offset:2048 sc1\n\t"
        "buffer_load_dwordx4 v[92:95], v4, s[20:23], s24 offen offset:3072 sc1\n\t"
        "buffer_load_dwordx4 v[96:99], v4, s[20:23], s25 offen offset:0 sc1\n\t"
        "buffer_load_dwordx4 v[100:103], v4, s[20:23], s25 offen offset:1024 sc1\n\t"
        "buffer_load_dwordx4 v[104:107], v4, s[20:23], s25 offen offset:2048 sc1\n\t"
        "buffer_load_dwordx4 v[108:111], v4, s[20:23], s25 offen offset:3072 sc1\n\t"
        "buffer_load_dwordx4 v[112:115], v4, s[20:23], s26 offen offset:0 sc1\n\t"
        "buffer_load_dwordx4 v[116:119], v4, s[20:23], s26 offen offset:1024 sc1\n\t"
        "buffer_load_dwordx4 v[120:123], v4, s[20:23], s26 offen offset:2048 sc1\n\t"
        "buffer_load_dwordx4 v[124:127], v4, s[20:23], s26 offen offset:3072 sc1\n"
        ".L7_qg:\n\t"
        "v_mov_b32 v7, 0\n\t"
        "ds_read_b128 v[8:11], v5 offset:2048\n\t"
        "ds_read_b128 v[12:15], v5 offset:10496\n\t"
        "ds_read_b128 v[16:19], v5 offset:18944\n\t"
        "ds_read_b128 v[20:23], v5 offset:27392\n\t"
        "s_waitcnt vmcnt(14) lgkmcnt(3)\n\t"
        "v_xor_b32 v64, s73, v64\n\t"
        "v_xor_b32 v65, v6, v65\n\t"
        "v_xor_b32 v68, s73, v68\n\t"
        "v_xor_b32 v69, v6, v69\n\t"
        "v_mfma_f32_16x16x32_bf16 v[24:27], v[64:67], v[8:11], 0\n\t"
        "s_nop 0\n\t"
        "v_mfma_f32_16x16x32_bf16 v[28:31], v[68:71], v[8:11], 0\n\t"
        "v_or3_b32 v7, v7, v64, v65\n\t"
        "ds_read_b128 v[8:11], v5 offset:35840\n\t"
        "s_waitcnt lgkmcnt(3)\n\t"
        "v_mfma_f32_16x16x32_bf16 v[32:35], v[64:67], v[12:15], 0\n\t"
        "v_mfma_f32_16x16x32_bf16 v[36:39], v[68:71], v[12:15], 0\n\t"
        "v_or3_b32 v7, v7, v68, v69\n\t"
        "ds_read_b128 v[12:15], v5 offset:2112\n\t"
        "s_waitcnt lgkmcnt(3)\n\t"
        "v_mfma_f32_16x16x32_bf16 v[40:43], v[64:67], v[16:19], 0\n\t"
        "v_mfma_f32_16x16x32_bf16 v[44:47], v[68:71], v[16:19], 0\n\t"
        "ds_read_b128 v[16:19], v5 offset:10560\n\t"
        "s_waitcnt lgkmcnt(3)\n\t"
        "v_mfma_f32_16x16x32_bf16 v[48:51], v[64:67], v[20:23], 0\n\t"
        "v_mfma_f32_16x16x32_bf16 v[52:55], v[68:71], v[20:23], 0\n\t"
        "ds_read_b128 v[20:23], v5 offset:19008\n\t"
        "s_waitcnt vmcnt(12) lgkmcnt(3)\n\t"
        "v_xor_b32 v72, s73, v72\n\t"
        "v_xor_b32 v73, v6, v73\n\t"
        "v_xor_b32 v76, s73, v76\n\t"
        "v_xor_b32 v77, v6, v77\n\t"
        "v_mfma_f32_16x16x32_bf16 v[56:59], v[64:67], v[8:11], 0\n\t"
        "v_mfma_f32_16x16x32_bf16 v[60:63], v[68:71], v[8:11], 0\n\t"
        "ds_read_b128 v[8:11], v5 offset:27456\n\t"
        "ds_read_b128 v[64:67], v5 offset:35904\n\t"
        "ds_read_b128 v[68:71], v5 offset:2176\n\t"
        "s_waitcnt lgkmcnt(4)\n\t"
        "v_mfma_f32_16x16x32_bf16 v[24:27], v[72:75], v[12:15], v[24:27]\n\t"
        "v_mfma_f32_16x16x32_bf16 v[28:31], v[76:79], v[12:15], v[28:31]\n\t"
        "v_or3_b32 v7, v7, v72, v73\n\t"
        "ds_read_b128 v[12:15], v5 offset:10624\n\t"
        "v_mfma_f32_16x16x32_bf16 v[32:35], v[72:75], v[16:19], v[32:35]\n\t"
        "v_mfma_f32_16x16x32_bf16 v[36:39], v[76:79], v[16:19], v[36:39]\n\t"
        "v_or3_b32 v7, v7, v76, v77\n\t"
        "ds_read_b128 v[16:19], v5 offset:19072\n\t"
        "s_waitcnt lgkmcnt(4)\n\t"
        "v_mfma_f32_16x16x32_bf16 v[40:43], v[72:75], v[20:23], v[40:43]\n\t"
        "v_mfma_f32_16x16x32_bf16 v[44:47], v[76:79], v[20:23], v[44:47]\n\t"
        "ds_read_b128 v[20:23], v5 offset:27520\n\t"
        "v_mfma_f32_16x16x32_bf16 v[48:51], v[72:75], v[8:11], v[48:51]\n\t"
        "v_mfma_f32_16x16x32_bf16 v[52:55], v[76:79], v[8:11], v[52:55]\n\t"
        "ds_read_b128 v[8:11], v5 offset:35968\n\t"
        "s_waitcnt vmcnt(10) lgkmcnt(4)\n\t"
        "v_xor_b32 v80, s73, v80\n\t"
        "v_xor_b32 v81, v6, v81\n\t"
        "v_xor_b32 v84, s73, v84\n\t"
        "v_xor_b32 v85, v6, v85\n\t"
        "v_mfma_f32_16x16x32_bf16 v[56:59], v[72:75], v[64:67], v[56:59]\n\t"
        "v_mfma_f32_16x16x32_bf16 v[60:63], v[76:79], v[64:67], v[60:63]\n\t"
        "ds_read_b128 v[64:67], v5 offset:2240\n\t"
        "ds_read_b128 v[72:75], v5 offset:10688\n\t"
        "ds_read_b128 v[76:79], v5 offset:19136\n\t"
        "v_mfma_f32_16x16x32_bf16 v[24:27], v[80:83], v[68:71], v[24:27]\n\t"
        "v_mfma_f32_16x16x32_bf16 v[28:31], v[84:87], v[68:71], v[28:31]\n\t"
        "v_or3_b32 v7, v7, v80, v81\n\t"
        "ds_read_b128 v[68:71], v5 offset:27584\n\t"
        "s_waitcnt lgkmcnt(6)\n\t"
        "v_mfma_f32_16x16x32_bf16 v[32:35], v[80:83], v[12:15], v[32:35]\n\t"
        "v_mfma_f32_16x16x32_bf16 v[36:39], v[84:87], v[12:15], v[36:39]\n\t"
        "v_or3_b32 v7, v7, v84, v85\n\t"
        "ds_read_b128 v[12:15], v5 offset:36032\n\t"
        "v_mfma_f32_16x16x32_bf16 v[40:43], v[80:83], v[16:19], v[40:43]\n\t"
        "v_mfma_f32_16x16x32_bf16 v[44:47], v[84:87], v[16:19], v[44:47]\n\t"
        "ds_read_b128 v[16:19], v5 offset:2304\n\t"
        "s_waitcnt lgkmcnt(6)\n\t"
        "v_mfma_f32_16x16x32_bf16 v[48:51], v[80:83], v[20:23], v[48:51]\n\t"
        "v_mfma_f32_16x16x32_bf16 v[52:55], v[84:87], v[20:23], v[52:55]\n\t"
        "ds_read_b128 v[20:23], v5 offset:10752\n\t"
        "s_waitcnt vmcnt(8) lgkmcnt(6)\n\t"
        "v_xor_b32 v88, s73, v88\n\t"
        "v_xor_b32 v89, v6, v89\n\t"
        "v_xor_b32 v92, s73, v92\n\t"
        "v_xor_b32 v93, v6, v93\n\t"
        "v_mfma_f32_16x16x32_bf16 v[56:59], v[80:83], v[8:11], v[56:59]\n\t"
        "v_mfma_f32_16x16x32_bf16 v[60:63], v[84:87], v[8:11], v[60:63]\n\t"
        "ds_read_b128 v[8:11], v5 offset:19200\n\t"
        "ds_read_b128 v[80:83], v5 offset:27648\n\t"
        "v_mfma_f32_16x16x32_bf16 v[24:27], v[88:91], v[64:67], v[24:27]\n\t"
        "v_mfma_f32_16x16x32_bf16 v[28:31], v[92:95], v[64:67], v[28:31]\n\t"
        "v_or3_b32 v7, v7, v88, v89\n\t"
        "ds_read_b128 v[84:87], v5 offset:36096\n\t"
        "s_waitcnt lgkmcnt(7)\n\t"
        "v_mfma_f32_16x16x32_bf16 v[32:35], v[88:91], v[72:75], v[32:35]\n\t"
        "v_mfma_f32_16x16x32_bf16 v[36:39], v[92:95], v[72:75], v[36:39]\n\t"
        "v_or3_b32 v7, v7, v92, v93\n\t"
        "ds_read_b128 v[64:67], v5 offset:2368\n\t"
        "v_mfma_f32_16x16x32_bf16 v[40:43], v[88:91], v[76:79], v[40:43]\n\t"
        "v_mfma_f32_16x16x32_bf16 v[44:47], v[92:95], v[76:79], v[44:47]\n\t"
        "ds_read_b128 v[72:75], v5 offset:10816\n\t"
        "s_waitcnt lgkmcnt(7)\n\t"
        "v_mfma_f32_16x16x32_bf16 v[48:51], v[88:91], v[68:71], v[48:51]\n\t"
        "v_mfma_f32_16x16x32_bf16 v[52:55], v[92:95], v[68:71], v[52:55]\n\t"
        "ds_read_b128 v[76:79], v5 offset:19264\n\t"
        "s_waitcnt vmcnt(6) lgkmcnt(7)\n\t"
        "v_xor_b32 v96, s73, v96\n\t"
        "v_xor_b32 v97, v6, v97\n\t"
        "v_xor_b32 v100, s73, v100\n\t"
        "v_xor_b32 v101, v6, v101\n\t"
        "v_mfma_f32_16x16x32_bf16 v[56:59], v[88:91], v[12:15], v[56:59]\n\t"
        "v_mfma_f32_16x16x32_bf16 v[60:63], v[92:95], v[12:15], v[60:63]\n\t"
        "ds_read_b128 v[68:71], v5 offset:27712\n\t"
        "v_mfma_f32_16x16x32_bf16 v[24:27], v[96:99], v[16:19], v[24:27]\n\t"
        "v_mfma_f32_16x16x32_bf16 v[28:31], v[100:103], v[16:19], v[28:31]\n\t"
        "v_or3_b32 v7, v7, v96, v97\n\t"
        "ds_read_b128 v[12:15], v5 offset:36160\n\t"
        "s_waitcnt lgkmcnt(7)\n\t"
        "v_mfma_f32_16x16x32_bf16 v[32:35], v[96:99], v[20:23], v[32:35]\n\t"
        "v_mfma_f32_16x16x32_bf16 v[36:39], v[100:103], v[20:23], v[36:39]\n\t"
        "v_or3_b32 v7, v7, v100, v101\n\t"
        "ds_read_b128 v[88:91], v5 offset:2432\n\t"
        "v_mfma_f32_16x16x32_bf16 v[40:43], v[96:99], v[8:11], v[40:43]\n\t"
        "v_mfma_f32_16x16x32_bf16 v[44:47], v[100:103], v[8:11], v[44:47]\n\t"
        "ds_read_b128 v[92:95], v5 offset:10880\n\t"
        "s_waitcnt lgkmcnt(7)\n\t"
        "v_mfma_f32_16x16x32_bf16 v[48:51], v[96:99], v[80:83], v[48:51]\n\t"
        "v_mfma_f32_16x16x32_bf16 v[52:55], v[100:103], v[80:83], v[52:55]\n\t"
        "ds_read_b128 v[16:19], v5 offset:19328\n\t"
        "s_waitcnt vmcnt(4) lgkmcnt(7)\n\t"
        "v_xor_b32 v104, s73, v104\n\t"
        "v_xor_b32 v105, v6, v105\n\t"
        "v_xor_b32 v108, s73, v108\n\t"
        "v_xor_b32 v109, v6, v109\n\t"
        "v_mfma_f32_16x16x32_bf16 v[56:59], v[96:99], v[84:87], v[56:59]\n\t"
        "v_mfma_f32_16x16x32_bf16 v[60:63], v[100:103], v[84:87], v[60:63]\n\t"
        "ds_read_b128 v[20:23], v5 offset:27776\n\t"
        "v_mfma_f32_16x16x32_bf16 v[24:27], v[104:107], v[64:67], v[24:27]\n\t"
        "v_mfma_f32_16x16x32_bf16 v[28:31], v[108:111], v[64:67], v[28:31]\n\t"
        "v_or3_b32 v7, v7, v104, v105\n\t"
        "ds_read_b128 v[8:11], v5 offset:36224\n\t"
        "s_waitcnt lgkmcnt(7)\n\t"
        "v_mfma_f32_16x16x32_bf16 v[32:35], v[104:107], v[72:75], v[32:35]\n\t"
        "v_mfma_f32_16x16x32_bf16 v[36:39], v[108:111], v[72:75], v[36:39]\n\t"
        "v_or3_b32 v7, v7, v108, v109\n\t"
        "ds_read_b128 v[80:83], v5 offset:2496\n\t"
        "v_mfma_f32_16x16x32_bf16 v[40:43], v[104:107], v[76:79], v[40:43]\n\t"
        "v_mfma_f32_16x16x32_bf16 v[44:47], v[108:111], v[76:79], v[44:47]\n\t"
        "ds_read_b128 v[84:87], v5 offset:10944\n\t"
        "s_waitcnt lgkmcnt(7)\n\t"
        "v_mfma_f32_16x16x32_bf16 v[48:51], v[104:107], v[68:71], v[48:51]\n\t"
        "v_mfma_f32_16x16x32_bf16 v[52:55], v[108:111], v[68:71], v[52:55]\n\t"
        "ds_read_b128 v[96:99], v5 offset:19392\n\t"
        "s_waitcnt vmcnt(2) lgkmcnt(7)\n\t"
        "v_xor_b32 v112, s73, v112\n\t"
        "v_xor_b32 v113, v6, v113\n\t"
        "v_xor_b32 v116, s73, v116\n\t"
        "v_xor_b32 v117, v6, v117\n\t"
        "v_mfma_f32_16x16x32_bf16 v[56:59], v[104:107], v[12:15], v[56:59]\n\t"
        "v_mfma_f32_16x16x32_bf16 v[60:63], v[108:111], v[12:15], v[60:63]\n\t"
        "ds_read_b128 v[100:103], v5 offset:27840\n\t"
        "v_mfma_f32_16x16x32_bf16 v[24:27], v[112:115], v[88:91], v[24:27]\n\t"
        "v_mfma_f32_16x16x32_bf16 v[28:31], v[116:119], v[88:91], v[28:31]\n\t"
        "v_or3_b32 v7, v7, v112, v113\n\t"
        "ds_read_b128 v[64:67], v5 offset:36288\n\t"
        "s_waitcnt lgkmcnt(7)\n\t"
        "v_mfma_f32_16x16x32_bf16 v[32:35], v[112:115], v[92:95], v[32:35]\n\t"
        "v_mfma_f32_16x16x32_bf16 v[36:39], v[116:119], v[92:95], v[36:39]\n\t"
        "v_or3_b32 v7, v7, v116, v117\n\t"
        "v_mfma_f32_16x16x32_bf16 v[40:43], v[112:115], v[16:19], v[40:43]\n\t"
        "v_mfma_f32_16x16x32_bf16 v[44:47], v[116:119], v[16:19], v[44:47]\n\t"
        "s_waitcnt lgkmcnt(5)\n\t"
        "v_mfma_f32_16x16x32_bf16 v[48:51], v[112:115], v[20:23], v[48:51]\n\t"
        "v_mfma_f32_16x16x32_bf16 v[52:55], v[116:119], v[20:23], v[52:55]\n\t"
        "s_waitcnt vmcnt(0) lgkmcnt(4)\n\t"
        "v_xor_b32 v120, s73, v120\n\t"
        "v_xor_b32 v121, v6, v121\n\t"
        "v_xor_b32 v124, s73, v124\n\t"
        "v_xor_b32 v125, v6, v125\n\t"
        "v_mfma_f32_16x16x32_bf16 v[56:59], v[112:115], v[8:11], v[56:59]\n\t"
        "v_mfma_f32_16x16x32_bf16 v[60:63], v[116:119], v[8:11], v[60:63]\n\t"
        "v_mfma_f32_16x16x32_bf16 v[24:27], v[120:123], v[80:83], v[24:27]\n\t"
        "v_mfma_f32_16x16x32_bf16 v[28:31], v[124:127], v[80:83], v[28:31]\n\t"
        "v_or3_b32 v7, v7, v120, v121\n\t"
        "s_waitcnt lgkmcnt(3)\n\t"
        "v_mfma_f32_16x16x32_bf16 v[32:35], v[120:123], v[84:87], v[32:35]\n\t"
        "v_mfma_f32_16x16x32_bf16 v[36:39], v[124:127], v[84:87], v[36:39]\n\t"
        "v_or3_b32 v7, v7, v124, v125\n\t"
        "v_and_b32 v7, s49, v7\n\t"
        "v_cmp_ne_u32 vcc, 0, v7\n\t"
        "s_cbranch_vccnz .L7_qr\n"
        ".L7_qd:\n\t"
        "s_mov_b64 exec, 1\n\t"
        "ds_add_u32 v2, v3 offset:12\n\t"
        "s_mov_b64 exec, -1\n\t"
        "v_and_b32 v6, 15, v1\n\t"
        "v_mul_u32_u24 v6, 272, v6\n\t"
        "v_and_b32 v7, 48, v1\n\t"
        "s_lshl_b32 s78, s80, 6\n\t"
        "v_add3_u32 v6, v6, v7, s78\n\t"
        "s_waitcnt lgkmcnt(2)\n\t"
        "v_mfma_f32_16x16x32_bf16 v[40:43], v[120:123], v[96:99], v[40:43]\n\t"
        "v_mfma_f32_16x16x32_bf16 v[44:47], v[124:127], v[96:99], v[44:47]\n\t"
        "v_cvt_pk_bf16_f32 v24, v24, v25\n\t"
        "v_cvt_pk_bf16_f32 v25, v26, v27\n\t"
        "v_cvt_pk_bf16_f32 v26, v28, v29\n\t"
        "v_cvt_pk_bf16_f32 v27, v30, v31\n\t"
        "s_waitcnt lgkmcnt(0)\n\t"
        "v_mfma_f32_16x16x32_bf16 v[48:51], v[120:123], v[100:103], v[48:51]\n\t"
        "v_mfma_f32_16x16x32_bf16 v[52:55], v[124:127], v[100:103], v[52:55]\n\t"
        "v_cvt_pk_bf16_f32 v32, v32, v33\n\t"
        "v_cvt_pk_bf16_f32 v33, v34, v35\n\t"
        "v_cvt_pk_bf16_f32 v34, v36, v37\n\t"
        "v_cvt_pk_bf16_f32 v35, v38, v39\n\t"
        "v_mfma_f32_16x16x32_bf16 v[56:59], v[120:123], v[64:67], v[56:59]\n\t"
        "v_mfma_f32_16x16x32_bf16 v[60:63], v[124:127], v[64:67], v[60:63]\n\t"
        "v_cvt_pk_bf16_f32 v40, v40, v41\n\t"
        "v_cvt_pk_bf16_f32 v41, v42, v43\n\t"
        "v_cvt_pk_bf16_f32 v42, v44, v45\n\t"
        "v_cvt_pk_bf16_f32 v43, v46, v47\n\t"
        "ds_read_b32 v0, v2 offset:12\n\t"
        "v_cvt_pk_bf16_f32 v48, v48, v49\n\t"
        "v_cvt_pk_bf16_f32 v49, v50, v51\n\t"
        "v_cvt_pk_bf16_f32 v50, v52, v53\n\t"
        "v_cvt_pk_bf16_f32 v51, v54, v55\n\t"
        "v_cvt_pk_bf16_f32 v56, v56, v57\n\t"
        "v_cvt_pk_bf16_f32 v57, v58, v59\n\t"
        "v_cvt_pk_bf16_f32 v58, v60, v61\n\t"
        "v_cvt_pk_bf16_f32 v59, v62, v63\n\t"
        "s_waitcnt lgkmcnt(0)\n\t"
        "v_cmp_gt_u16 vcc, 4, v0\n\t"
        "s_cbranch_vccz .L7_qx\n"
        ".L7_w58:\n\t"
        "ds_read_b32 v0, v2 offset:12\n\t"
        "s_waitcnt lgkmcnt(0)\n\t"
        "v_cmp_gt_u16 vcc, 4, v0\n\t"
        "s_cbranch_vccz .L7_d59\n\t"
        "s_sleep 0\n\t"
        "s_sub_i32 s83, s83, 1\n\t"
        "s_cmp_gt_i32 s83, 0\n\t"
        "s_cbranch_scc1 .L7_w58\n"
        ".L7_d59:\n"
        ".L7_qx:\n\t"
        "ds_write_b128 v6, v[24:27] offset:2048\n\t"
        "ds_write_b128 v6, v[32:35] offset:6400\n\t"
        "ds_write_b128 v6, v[40:43] offset:10752\n\t"
        "ds_write_b128 v6, v[48:51] offset:15104\n\t"
        "ds_write_b128 v6, v[56:59] offset:19456\n\t"
        "s_lshl_b32 s78, s69, 9\n\t"
        "v_lshl_add_u32 v7, v1, 3, s78\n\t"
        "v_mov_b64 v[8:9], 0\n\t"
        "ds_write_b64 v7, v[8:9] offset:34048\n\t"
        "s_mov_b64 exec, 1\n\t"
        "ds_add_u32 v2, v3 offset:16\n\t"
        "s_mov_b64 exec, -1\n\t"
        "v_lshrrev_b32 v10, 3, v1\n\t"
        "v_lshlrev_b32 v10, 9, v10\n\t"
        "v_and_b32 v11, 7, v1\n\t"
        "v_lshl_add_u32 v10, v11, 4, v10\n\t"
        "v_and_b32 v11, 16, v1\n\t"
        "v_lshlrev_b32 v11, 2, v11\n\t"
        "v_xor_b32 v10, v10, v11\n\t"
        "s_load_dwordx4 s[24:27], %2, 32\n\t"
        "s_lshl_b32 s78, s80, 14\n\t"
        "s_add_u32 m0, s78, 68864\n\t"
        "s_lshl_b32 s78, s70, 9\n\t"
        "s_add_u32 s78, s78, s80\n\t"
        "s_lshl_b32 s78, s78, 7\n"
        ".L7_w60:\n\t"
        "ds_read_b32 v0, v2 offset:24\n\t"
        "s_waitcnt lgkmcnt(0)\n\t"
        "v_cmp_gt_u16 vcc, 8, v0\n\t"
        "s_cbranch_vccz .L7_d61\n\t"
        "s_sleep 1\n\t"
        "s_sub_i32 s83, s83, 1\n\t"
        "s_cmp_gt_i32 s83, 0\n\t"
        "s_cbranch_scc1 .L7_w60\n"
        ".L7_d61:\n"
        ".L7_w62:\n\t"
        "ds_read_b32 v0, v2 offset:88\n\t"
        "s_waitcnt lgkmcnt(0)\n\t"
        "v_cmp_gt_u16 vcc, 16, v0\n\t"
        "s_cbranch_vccz .L7_d63\n\t"
        "s_sleep 1\n\t"
        "s_sub_i32 s83, s83, 1\n\t"
        "s_cmp_gt_i32 s83, 0\n\t"
        "s_cbranch_scc1 .L7_w62\n"
        ".L7_d63:\n\t"
        "s_setprio 2\n\t"
        "buffer_load_dwordx4 v10, s[24:27], s78 offen lds\n\t"
        "s_add_u32 m0, m0, 0x400\n\t"
        "s_add_u32 s78, s78, 0x1000\n\t"
        "buffer_load_dwordx4 v10, s[24:27], s78 offen lds\n\t"
        "s_add_u32 m0, m0, 0x400\n\t"
        "s_add_u32 s78, s78, 0x1000\n\t"
        "buffer_load_dwordx4 v10, s[24:27], s78 offen lds\n\t"
        "s_add_u32 m0, m0, 0x400\n\t"
        "s_add_u32 s78, s78, 0x1000\n\t"
        "buffer_load_dwordx4 v10, s[24:27], s78 offen lds\n\t"
        "s_add_u32 m0, m0, 0x400\n\t"
        "s_add_u32 s78, s78, 0x1000\n\t"
        "buffer_load_dwordx4 v10, s[24:27], s78 offen lds\n\t"
        "s_add_u32 m0, m0, 0x400\n\t"
        "s_add_u32 s78, s78, 0x1000\n\t"
        "buffer_load_dwordx4 v10, s[24:27], s78 offen lds\n\t"
        "s_add_u32 m0, m0, 0x400\n\t"
        "s_add_u32 s78, s78, 0x1000\n\t"
        "buffer_load_dwordx4 v10, s[24:27], s78 offen lds\n\t"
        "s_add_u32 m0, m0, 0x400\n\t"
        "s_add_u32 s78, s78, 0x1000\n\t"
        "buffer_load_dwordx4 v10, s[24:27], s78 offen lds\n\t"
        "s_add_u32 m0, m0, 0x400\n\t"
        "s_add_u32 s78, s78, 0x1000\n\t"
        "buffer_load_dwordx4 v10, s[24:27], s78 offen lds\n\t"
        "s_add_u32 m0, m0, 0x400\n\t"
        "s_add_u32 s78, s78, 0x1000\n\t"
        "buffer_load_dwordx4 v10, s[24:27], s78 offen lds\n\t"
        "s_add_u32 m0, m0, 0x400\n\t"
        "s_add_u32 s78, s78, 0x1000\n\t"
        "buffer_load_dwordx4 v10, s[24:27], s78 offen lds\n\t"
        "s_add_u32 m0, m0, 0x400\n\t"
        "s_add_u32 s78, s78, 0x1000\n\t"
        "buffer_load_dwordx4 v10, s[24:27], s78 offen lds\n\t"
        "s_add_u32 m0, m0, 0x400\n\t"
        "s_add_u32 s78, s78, 0x1000\n\t"
        "buffer_load_dwordx4 v10, s[24:27], s78 offen lds\n\t"
        "s_add_u32 m0, m0, 0x400\n\t"
        "s_add_u32 s78, s78, 0x1000\n\t"
        "buffer_load_dwordx4 v10, s[24:27], s78 offen lds\n\t"
        "s_add_u32 m0, m0, 0x400\n\t"
        "s_add_u32 s78, s78, 0x1000\n\t"
        "buffer_load_dwordx4 v10, s[24:27], s78 offen lds\n\t"
        "s_add_u32 m0, m0, 0x400\n\t"
        "s_add_u32 s78, s78, 0x1000\n\t"
        "buffer_load_dwordx4 v10, s[24:27], s78 offen lds\n\t"
        "s_waitcnt vmcnt(0)\n"
        ".L7_vw:\n\t"
        "s_mov_b64 exec, 1\n\t"
        "ds_add_u32 v2, v3 offset:28\n\t"
        "s_mov_b64 exec, -1\n\t"
        "s_setprio 0\n"
        ".L7_w64:\n\t"
        "ds_read_b32 v0, v2 offset:36\n\t"
        "s_waitcnt lgkmcnt(0)\n\t"
        "v_cmp_gt_u16 vcc, 4, v0\n\t"
        "s_cbranch_vccz .L7_d65\n\t"
        "s_sleep 2\n\t"
        "s_sub_i32 s83, s83, 1\n\t"
        "s_cmp_gt_i32 s83, 0\n\t"
        "s_cbranch_scc1 .L7_w64\n"
        ".L7_d65:\n\t"
        "v_cmp_lt_u32 vcc, 0xfffff, v0\n\t"
        "s_cbranch_vccnz .L7_exh\n\t"
        "s_cmp_eq_u32 s69, 0\n\t"
        "s_cbranch_scc0 .L7_hz\n\t"
        "s_lshr_b32 s47, s68, 20\n\t"
        "s_add_u32 s47, s47, s68\n\t"
        "s_and_b32 s47, s47, 0xfff\n\t"
        "s_mul_i32 s46, s68, 0x9e377000\n\t"
        "s_or_b32 s46, s46, s47\n\t"
        "v_mov_b32 v4, s46\n\t"
        "s_xor_b32 s44, s70, 1\n\t"
        "s_lshl1_add_u32 s44, %3, s44\n\t"
        "s_lshl_b32 s44, s44, 10\n\t"
        "s_add_u32 s44, s44, 262144\n"
        ".L7_w66:\n\t"
        "ds_read_b32 v0, v2 offset:116\n\t"
        "s_waitcnt lgkmcnt(0)\n\t"
        "v_cmp_gt_u16 vcc, 1, v0\n\t"
        "s_cbranch_vccz .L7_d67\n\t"
        "s_sleep 1\n\t"
        "s_sub_i32 s83, s83, 1\n\t"
        "s_cmp_gt_i32 s83, 0\n\t"
        "s_cbranch_scc1 .L7_w66\n"
        ".L7_d67:\n\t"
        "v_readfirstlane_b32 s78, v0\n\t"
        "s_lshr_b32 s78, s78, 16\n\t"
        "s_cmp_gt_u32 s78, 16\n\t"
        "s_cselect_b32 s45, 0x2000, 0\n\t"
        "s_xor_b32 s44, s44, s45\n\t"
        "s_xor_b32 s45, s44, 0xc00\n"
        ".L7_w68:\n\t"
        "ds_read_b32 v0, v2 offset:56\n\t"
        "s_waitcnt lgkmcnt(0)\n\t"
        "v_cmp_gt_u16 vcc, 3, v0\n\t"
        "s_cbranch_vccz .L7_d69\n\t"
        "s_sleep 8\n\t"
        "s_sub_i32 s83, s83, 1\n\t"
        "s_cmp_gt_i32 s83, 0\n\t"
        "s_cbranch_scc1 .L7_w68\n"
        ".L7_d69:\n\t"
        "s_setprio 3\n"
        ".L7_w70:\n\t"
        "ds_read_b32 v0, v2 offset:96\n\t"
        "s_waitcnt lgkmcnt(0)\n\t"
        "v_cmp_gt_u16 vcc, 7, v0\n\t"
        "s_cbranch_vccz .L7_d71\n\t"
        "s_sleep 1\n\t"
        "s_sub_i32 s83, s83, 1\n\t"
        "s_cmp_gt_i32 s83, 0\n\t"
        "s_cbranch_scc1 .L7_w70\n"
        ".L7_d71:\n\t"
        "s_mov_b64 exec, 1\n\t"
        "buffer_store_dword v4, off, s[20:23], s44 offset:12 sc1\n\t"
        "s_mov_b32 s83, 64\n\t"
        "s_sleep 4\n"
        ".L7_h1:\n\t"
        "s_buffer_load_dword s47, s[20:23], s45 offset:12 glc\n\t"
        "s_waitcnt lgkmcnt(0)\n\t"
        "s_cmp_eq_u32 s47, s46\n\t"
        "s_cbranch_scc1 .L7_h2\n\t"
        "s_sleep 3\n\t"
        "s_sub_i32 s83, s83, 1\n\t"
        "s_cmp_gt_i32 s83, 0\n\t"
        "s_cbranch_scc1 .L7_h1\n"
        ".L7_h2:\n\t"
        "s_mov_b64 exec, 1\n\t"
        "ds_add_u32 v2, v3 offset:100\n\t"
        "s_mov_b64 exec, -1\n"
        ".L7_hz:\n\t"
        "s_endpgm\n"
        ".L7_exh:\n\t"
        "s_mov_b64 exec, 1\n\t"
        "ds_add_u32 v2, v3 offset:100\n\t"
        "s_mov_b64 exec, -1\n\t"
        "s_branch .L7_exit\n"
        ".L7_O2:\n\t"
        "s_load_dwordx8 s[24:31], %2, 32\n\t"
        "s_load_dwordx4 s[60:63], %2, 176\n\t"
        "v_lshrrev_b32 v6, 4, v1\n\t"
        "v_and_b32 v7, 48, v1\n\t"
        "v_and_b32 v4, 2, v6\n\t"
        "v_add_lshl_u32 v4, v4, v6, 2\n\t"
        "v_and_b32 v8, 15, v1\n\t"
        "s_lshl_b32 s78, s70, 8\n\t"
        "v_lshl_add_u32 v9, v8, 4, s78\n\t"
        "v_lshrrev_b32 v10, 3, v1\n\t"
        "v_lshlrev_b32 v10, 9, v10\n\t"
        "v_and_b32 v11, 7, v1\n\t"
        "v_lshl_add_u32 v10, v11, 4, v10\n\t"
        "v_and_b32 v11, 16, v1\n\t"
        "v_lshlrev_b32 v11, 2, v11\n\t"
        "v_xor_b32 v10, v10, v11\n\t"
        "s_add_u32 s81, s69, s76\n\t"
        "s_and_b32 s81, s81, 7\n\t"
        "s_lshr_b32 s78, s81, 1\n\t"
        "s_lshl_b32 s78, s78, 7\n\t"
        "s_and_b32 s79, s81, 1\n\t"
        "s_lshl_b32 s79, s79, 15\n\t"
        "s_add_u32 s78, s78, s79\n\t"
        "s_lshl_b32 s79, s70, 16\n\t"
        "s_add_u32 s78, s78, s79\n\t"
        "v_add_u32 v10, s78, v10\n\t"
        "s_lshl_b32 s82, s81, 13\n\t"
        "s_add_u32 s82, s82, 68864\n\t"
        "v_bfe_u32 v12, v1, 2, 2\n\t"
        "v_lshlrev_b32 v11, 10, v6\n\t"
        "v_lshl_add_u32 v11, v12, 7, v11\n\t"
        "v_lshrrev_b32 v12, 1, v12\n\t"
        "s_and_b32 s78, s81, 1\n\t"
        "v_xor_b32 v12, s78, v12\n\t"
        "v_lshl_add_u32 v11, v12, 6, v11\n\t"
        "v_and_b32 v12, 3, v1\n\t"
        "v_lshl_add_u32 v11, v12, 4, v11\n\t"
        "s_lshr_b32 s78, s81, 1\n\t"
        "s_lshl_b32 s78, s78, 14\n\t"
        "s_add_u32 s78, s78, 68864\n\t"
        "v_add_u32 v23, s78, v11\n\t"
        "s_waitcnt vmcnt(0)\n\t"
        "ds_bpermute_b32 v12, v7, v5 offset:0\n\t"
        "ds_bpermute_b32 v13, v7, v5 offset:4\n\t"
        "ds_bpermute_b32 v14, v7, v5 offset:8\n\t"
        "ds_bpermute_b32 v15, v7, v5 offset:12\n\t"
        "ds_bpermute_b32 v16, v7, v5 offset:64\n\t"
        "ds_bpermute_b32 v17, v7, v5 offset:68\n\t"
        "ds_bpermute_b32 v18, v7, v5 offset:72\n\t"
        "ds_bpermute_b32 v19, v7, v5 offset:76\n\t"
        "ds_bpermute_b32 v20, v4, v5 offset:128\n\t"
        "ds_bpermute_b32 v21, v4, v5 offset:136\n\t"
        "s_waitcnt lgkmcnt(0)\n\t"
        "v_lshl_add_u32 v12, v12, 9, v9\n\t"
        "v_lshl_add_u32 v13, v13, 9, v9\n\t"
        "v_lshl_add_u32 v14, v14, 9, v9\n\t"
        "v_lshl_add_u32 v15, v15, 9, v9\n\t"
        "v_lshl_add_u32 v16, v16, 9, v9\n\t"
        "v_lshl_add_u32 v17, v17, 9, v9\n\t"
        "v_lshl_add_u32 v18, v18, 9, v9\n\t"
        "v_lshl_add_u32 v19, v19, 9, v9\n\t"
        "v_lshl_add_u32 v20, v20, 9, v9\n\t"
        "v_lshl_add_u32 v21, v21, 9, v9\n\t"
        "s_setprio 0\n"
        ".L7_w1:\n\t"
        "ds_read_b32 v0, v2 offset:44\n\t"
        "s_waitcnt lgkmcnt(0)\n\t"
        "v_cmp_gt_u16 vcc, 2, v0\n\t"
        "s_cbranch_vccz .L7_d2\n\t"
        "s_sleep 1\n\t"
        "s_sub_i32 s83, s83, 1\n\t"
        "s_cmp_gt_i32 s83, 0\n\t"
        "s_cbranch_scc1 .L7_w1\n"
        ".L7_d2:\n"
        ".L7_w3:\n\t"
        "ds_read_b64 v[4:5], v2 offset:72\n\t"
        "s_waitcnt lgkmcnt(0)\n\t"
        "v_cmp_gt_u16 vcc, 2, v4\n\t"
        "s_cbranch_vccz .L7_d4\n\t"
        "s_sleep 1\n\t"
        "s_sub_i32 s83, s83, 1\n\t"
        "s_cmp_gt_i32 s83, 0\n\t"
        "s_cbranch_scc1 .L7_w3\n"
        ".L7_d4:\n\t"
        "v_readfirstlane_b32 s68, v5\n\t"
        "s_add_u32 m0, s82, 0\n\t"
        "s_mov_b32 s78, 0\n\t"
        "buffer_load_dwordx4 v10, s[28:31], s78 offen lds\n\t"
        "s_add_u32 m0, m0, 0x400\n\t"
        "s_add_u32 s78, s78, 0x1000\n\t"
        "buffer_load_dwordx4 v10, s[28:31], s78 offen lds\n\t"
        "s_add_u32 m0, m0, 0x400\n\t"
        "s_add_u32 s78, s78, 0x1000\n\t"
        "buffer_load_dwordx4 v10, s[28:31], s78 offen lds\n\t"
        "s_add_u32 m0, m0, 0x400\n\t"
        "s_add_u32 s78, s78, 0x1000\n\t"
        "buffer_load_dwordx4 v10, s[28:31], s78 offen lds\n\t"
        "s_add_u32 m0, m0, 0x400\n\t"
        "s_add_u32 s78, s78, 0x1000\n\t"
        "buffer_load_dwordx4 v10, s[28:31], s78 offen lds\n\t"
        "s_add_u32 m0, m0, 0x400\n\t"
        "s_add_u32 s78, s78, 0x1000\n\t"
        "buffer_load_dwordx4 v10, s[28:31], s78 offen lds\n\t"
        "s_add_u32 m0, m0, 0x400\n\t"
        "s_add_u32 s78, s78, 0x1000\n\t"
        "buffer_load_dwordx4 v10, s[28:31], s78 offen lds\n\t"
        "s_add_u32 m0, m0, 0x400\n\t"
        "s_add_u32 s78, s78, 0x1000\n\t"
        "buffer_load_dwordx4 v10, s[28:31], s78 offen lds\n\t"
        "s_mov_b64 exec, 1\n\t"
        "ds_add_u32 v2, v3 offset:88\n\t"
        "s_mov_b64 exec, -1\n\t"
        "s_mov_b32 s36, 0x22222222\n\t"
        "s_mov_b32 s37, s36\n\t"
        "s_lshl_b64 s[38:39], s[36:37], 1\n\t"
        "s_lshl_b64 s[40:41], s[36:37], 2\n\t"
        "s_and_b32 s44, s68, 0xfff\n\t"
        "s_xor_b32 s45, s44, 0x5a5\n\t"
        "s_lshr_b32 s46, s68, 12\n\t"
        "s_lshr_b32 s47, s68, 20\n\t"
        "s_add_u32 s46, s46, s68\n\t"
        "s_and_b32 s46, s46, 0xfff\n\t"
        "s_add_u32 s47, s47, s68\n\t"
        "s_and_b32 s47, s47, 0xfff\n\t"
        "v_mov_b32 v22, s44\n\t"
        "v_mov_b32 v0, s45\n\t"
        "v_cndmask_b32 v22, v22, v0, s[36:37]\n\t"
        "v_mov_b32 v0, s46\n\t"
        "v_cndmask_b32 v22, v22, v0, s[38:39]\n\t"
        "v_mov_b32 v0, s47\n\t"
        "v_cndmask_b32 v22, v22, v0, s[40:41]\n\t"
        "s_lshl3_add_u32 s42, %3, s77\n\t"
        "s_lshl_b32 s42, s42, 8\n\t"
        "s_add_u32 s42, s42, 262144\n\t"
        "s_xor_b32 s43, s42, 0x800\n\t"
        "s_movk_i32 s48, 0xfff\n\t"
        "s_waitcnt vmcnt(0)\n\t"
        "s_mov_b64 exec, 1\n\t"
        "ds_add_u32 v2, v3 offset:20\n\t"
        "s_mov_b64 exec, -1\n"
        ".L7_w5:\n\t"
        "ds_read_b32 v0, v2 offset:20\n\t"
        "s_waitcnt lgkmcnt(0)\n\t"
        "v_cmp_gt_u16 vcc, 8, v0\n\t"
        "s_cbranch_vccz .L7_d6\n\t"
        "s_sleep 1\n\t"
        "s_sub_i32 s83, s83, 1\n\t"
        "s_cmp_gt_i32 s83, 0\n\t"
        "s_cbranch_scc1 .L7_w5\n"
        ".L7_d6:\n\t"
        "ds_read_b64_tr_b16 v[64:65], v23 offset:0\n\t"
        "ds_read_b64_tr_b16 v[66:67], v23 offset:512\n\t"
        "ds_read_b64_tr_b16 v[68:69], v23 offset:8\n\t"
        "ds_read_b64_tr_b16 v[70:71], v23 offset:520\n\t"
        "ds_read_b64_tr_b16 v[72:73], v23 offset:4096\n\t"
        "ds_read_b64_tr_b16 v[74:75], v23 offset:4608\n\t"
        "ds_read_b64_tr_b16 v[76:77], v23 offset:4104\n\t"
        "ds_read_b64_tr_b16 v[78:79], v23 offset:4616\n\t"
        "ds_read_b64_tr_b16 v[80:81], v23 offset:8192\n\t"
        "ds_read_b64_tr_b16 v[82:83], v23 offset:8704\n\t"
        "ds_read_b64_tr_b16 v[84:85], v23 offset:8200\n\t"
        "ds_read_b64_tr_b16 v[86:87], v23 offset:8712\n\t"
        "ds_read_b64_tr_b16 v[88:89], v23 offset:12288\n\t"
        "ds_read_b64_tr_b16 v[90:91], v23 offset:12800\n\t"
        "s_waitcnt lgkmcnt(10)\n\t"
        "ds_read_b64_tr_b16 v[92:93], v23 offset:12296\n\t"
        "ds_read_b64_tr_b16 v[94:95], v23 offset:12808\n\t"
        "s_mov_b64 exec, 1\n\t"
        "ds_add_u32 v2, v3 offset:24\n\t"
        "s_mov_b64 exec, -1\n\t"
        "buffer_load_dwordx4 v[24:27], v12, s[16:19], 0 offen\n\t"
        "buffer_load_dwordx4 v[28:31], v13, s[16:19], 0 offen\n\t"
        "buffer_load_dwordx4 v[32:35], v14, s[16:19], 0 offen\n\t"
        "buffer_load_dwordx4 v[36:39], v15, s[16:19], 0 offen\n\t"
        "buffer_load_dwordx4 v[40:43], v16, s[16:19], 0 offen\n\t"
        "buffer_load_dwordx4 v[44:47], v17, s[16:19], 0 offen\n\t"
        "buffer_load_dwordx4 v[48:51], v18, s[16:19], 0 offen\n\t"
        "buffer_load_dwordx4 v[52:55], v19, s[16:19], 0 offen\n\t"
        "buffer_load_dwordx4 v[56:59], v20, s[16:19], 0 offen\n\t"
        "buffer_load_dwordx4 v[60:63], v21, s[16:19], 0 offen\n\t"
        "s_mov_b64 exec, 1\n\t"
        "ds_add_u32 v2, v3 offset:88\n\t"
        "s_mov_b64 exec, -1\n"
        ".L7_o2:\n\t"
        "s_setprio 2\n\t"
        "v_add_u32 v20, s80, v6\n\t"
        "v_lshrrev_b32 v21, 5, v1\n\t"
        "v_add_u32 v21, s80, v21\n\t"
        "v_lshlrev_b32 v8, 4, v8\n\t"
        "s_movk_i32 s78, 272\n\t"
        "v_mad_u32_u24 v10, v20, s78, v8\n\t"
        "v_mad_u32_u24 v11, v21, s78, v8\n\t"
        "v_lshlrev_b32 v108, 1, v20\n\t"
        "v_lshlrev_b32 v109, 1, v21\n\t"
        "s_mov_b32 s50, 0x0f0ff0f0\n\t"
        "s_mov_b32 s51, s50\n\t"
        "s_and_b32 s57, s81, 1\n\t"
        "s_lshl_b32 s57, s57, 13\n"
        ".L7_w7:\n\t"
        "ds_read_b32 v0, v2 offset:16\n\t"
        "s_waitcnt lgkmcnt(0)\n\t"
        "v_cmp_gt_u16 vcc, 4, v0\n\t"
        "s_cbranch_vccz .L7_d8\n\t"
        "s_sleep 1\n\t"
        "s_sub_i32 s83, s83, 1\n\t"
        "s_cmp_gt_i32 s83, 0\n\t"
        "s_cbranch_scc1 .L7_w7\n"
        ".L7_d8:\n\t"
        "ds_read_b128 v[96:99], v10 offset:2048\n\t"
        "ds_read_b128 v[100:103], v10 offset:3136\n\t"
        "ds_read_b128 v[104:107], v11 offset:4224\n\t"
        "ds_read_b32 v9, v2 offset:116\n\t"
        "ds_read_u16 v110, v108 offset:1792\n\t"
        "ds_read_u16 v111, v108 offset:1800\n\t"
        "ds_read_u16 v112, v109 offset:1808\n\t"
        "s_waitcnt vmcnt(0)\n\t"
        "s_waitcnt lgkmcnt(6)\n\t"
        "v_dot2_f32_bf16 v12, v24, v96, 0\n\t"
        "v_dot2_f32_bf16 v13, v28, v96, 0\n\t"
        "v_dot2_f32_bf16 v14, v32, v96, 0\n\t"
        "v_dot2_f32_bf16 v15, v36, v96, 0\n\t"
        "v_dot2_f32_bf16 v12, v25, v97, v12\n\t"
        "v_dot2_f32_bf16 v13, v29, v97, v13\n\t"
        "v_dot2_f32_bf16 v14, v33, v97, v14\n\t"
        "v_dot2_f32_bf16 v15, v37, v97, v15\n\t"
        "v_dot2_f32_bf16 v12, v26, v98, v12\n\t"
        "v_dot2_f32_bf16 v13, v30, v98, v13\n\t"
        "v_dot2_f32_bf16 v14, v34, v98, v14\n\t"
        "v_dot2_f32_bf16 v15, v38, v98, v15\n\t"
        "v_dot2_f32_bf16 v12, v27, v99, v12\n\t"
        "v_dot2_f32_bf16 v13, v31, v99, v13\n\t"
        "v_dot2_f32_bf16 v14, v35, v99, v14\n\t"
        "v_dot2_f32_bf16 v15, v39, v99, v15\n\t"
        "s_waitcnt lgkmcnt(5)\n\t"
        "v_dot2_f32_bf16 v16, v40, v100, 0\n\t"
        "v_dot2_f32_bf16 v17, v44, v100, 0\n\t"
        "v_dot2_f32_bf16 v18, v48, v100, 0\n\t"
        "v_dot2_f32_bf16 v19, v52, v100, 0\n\t"
        "v_dot2_f32_bf16 v16, v41, v101, v16\n\t"
        "v_dot2_f32_bf16 v17, v45, v101, v17\n\t"
        "v_dot2_f32_bf16 v18, v49, v101, v18\n\t"
        "v_dot2_f32_bf16 v19, v53, v101, v19\n\t"
        "v_dot2_f32_bf16 v16, v42, v102, v16\n\t"
        "v_dot2_f32_bf16 v17, v46, v102, v17\n\t"
        "v_dot2_f32_bf16 v18, v50, v102, v18\n\t"
        "v_dot2_f32_bf16 v19, v54, v102, v19\n\t"
        "v_dot2_f32_bf16 v16, v43, v103, v16\n\t"
        "v_dot2_f32_bf16 v17, v47, v103, v17\n\t"
        "v_dot2_f32_bf16 v18, v51, v103, v18\n\t"
        "v_dot2_f32_bf16 v19, v55, v103, v19\n\t"
        "s_waitcnt lgkmcnt(4)\n\t"
        "v_dot2_f32_bf16 v20, v56, v104, 0\n\t"
        "v_dot2_f32_bf16 v21, v60, v104, 0\n\t"
        "v_dot2_f32_bf16 v20, v57, v105, v20\n\t"
        "v_dot2_f32_bf16 v21, v61, v105, v21\n\t"
        "v_dot2_f32_bf16 v20, v58, v106, v20\n\t"
        "v_dot2_f32_bf16 v21, v62, v106, v21\n\t"
        "v_dot2_f32_bf16 v20, v59, v107, v20\n\t"
        "v_dot2_f32_bf16 v21, v63, v107, v21\n\t"
        "s_nop 1\n\t"
        "v_add_f32_dpp v20, v20, v20 row_ror:8 row_mask:0xf bank_mask:0x3\n\t"
        "v_add_f32_dpp v12, v12, v12 row_ror:8 row_mask:0xf bank_mask:0x3\n\t"
        "v_add_f32_dpp v14, v14, v14 row_ror:8 row_mask:0xf bank_mask:0x3\n\t"
        "v_add_f32_dpp v16, v16, v16 row_ror:8 row_mask:0xf bank_mask:0x3\n\t"
        "v_add_f32_dpp v18, v18, v18 row_ror:8 row_mask:0xf bank_mask:0x3\n\t"
        "v_add_f32_dpp v20, v21, v21 row_ror:8 row_mask:0xf bank_mask:0xc\n\t"
        "v_add_f32_dpp v12, v13, v13 row_ror:8 row_mask:0xf bank_mask:0xc\n\t"
        "v_add_f32_dpp v14, v15, v15 row_ror:8 row_mask:0xf bank_mask:0xc\n\t"
        "v_add_f32_dpp v16, v17, v17 row_ror:8 row_mask:0xf bank_mask:0xc\n\t"
        "v_add_f32_dpp v18, v19, v19 row_ror:8 row_mask:0xf bank_mask:0xc\n\t"
        "v_add_f32_dpp v20, v20, v20 row_half_mirror row_mask:0xf bank_mask:0xf\n\t"
        "v_add_f32_dpp v12, v12, v12 row_half_mirror row_mask:0xf bank_mask:0x5\n\t"
        "v_add_f32_dpp v16, v16, v16 row_half_mirror row_mask:0xf bank_mask:0x5\n\t"
        "v_mov_b32 v21, v20\n\t"
        "v_add_f32_dpp v12, v14, v14 row_half_mirror row_mask:0xf bank_mask:0xa\n\t"
        "v_add_f32_dpp v16, v18, v18 row_half_mirror row_mask:0xf bank_mask:0xa\n\t"
        "v_permlane16_swap_b32 v20, v21\n\t"
        "v_cndmask_b32 v20, v20, v21, s[50:51]\n\t"
        "v_add_f32_dpp v12, v12, v12 quad_perm:[1,0,3,2] row_mask:0xf bank_mask:0xf\n\t"
        "v_add_f32_dpp v16, v16, v16 quad_perm:[1,0,3,2] row_mask:0xf bank_mask:0xf\n\t"
        "v_add_f32_dpp v20, v20, v20 quad_perm:[1,0,3,2] row_mask:0xf bank_mask:0xf\n\t"
        "v_add_f32_dpp v12, v12, v12 quad_perm:[2,3,0,1] row_mask:0xf bank_mask:0xf\n\t"
        "v_add_f32_dpp v16, v16, v16 quad_perm:[2,3,0,1] row_mask:0xf bank_mask:0xf\n\t"
        "v_add_f32_dpp v20, v20, v20 quad_perm:[2,3,0,1] row_mask:0xf bank_mask:0xf\n\t"
        "s_waitcnt lgkmcnt(0)\n\t"
        "v_cmp_gt_u16 vcc, 1, v9\n\t"
        "s_cbranch_vccz .L7_sv\n"
        ".L7_w9:\n\t"
        "ds_read_b32 v9, v2 offset:116\n\t"
        "s_waitcnt lgkmcnt(0)\n\t"
        "v_cmp_gt_u16 vcc, 1, v9\n\t"
        "s_cbranch_vccz .L7_d10\n\t"
        "s_sleep 1\n\t"
        "s_sub_i32 s83, s83, 1\n\t"
        "s_cmp_gt_i32 s83, 0\n\t"
        "s_cbranch_scc1 .L7_w9\n"
        ".L7_d10:\n"
        ".L7_sv:\n\t"
        "v_readfirstlane_b32 s78, v9\n\t"
        "s_lshr_b32 s78, s78, 16\n\t"
        "s_cmp_gt_u32 s78, 16\n\t"
        "s_cselect_b32 s56, 0x2000, 0\n\t"
        "s_sub_u32 s78, s78, 13\n\t"
        "s_cmp_le_u32 s78, 6\n\t"
        "s_cselect_b32 s57, s57, s56\n\t"
        "v_cndmask_b32 v12, v12, v16, s[36:37]\n\t"
        "v_cndmask_b32 v12, v12, v20, s[38:39]\n\t"
        "v_add_u32 v12, 0x800, v12\n\t"
        "v_bfi_b32 v21, s48, v22, v12\n\t"
        "v_lshlrev_b32 v13, 2, v1\n\t"
        "s_xor_b32 s42, s42, s56\n\t"
        "s_setprio 3\n\t"
        "buffer_store_dword v21, v13, s[20:23], s42 offen sc1\n\t"
        "s_setprio 2\n\t"
        "s_xor_b32 s43, s43, s56\n\t"
        "s_lshr_b32 s78, %3, 3\n\t"
        "s_and_b32 s79, %3, 3\n\t"
        "s_lshl2_add_u32 s78, s78, s79\n\t"
        "s_and_b32 s79, %3, 4\n\t"
        "s_lshl_b32 s79, s79, 11\n\t"
        "s_lshl_b32 s30, s81, 10\n\t"
        "s_add_u32 s79, s79, s30\n\t"
        "s_add_u32 s79, s79, 3145728\n\t"
        "s_xor_b32 s79, s79, s57\n\t"
        "s_mul_i32 s24, s78, 0xc000\n\t"
        "s_xor_b32 s78, s78, 1\n\t"
        "s_mul_i32 s27, s78, 0xc000\n\t"
        "s_add_u32 s24, s24, s79\n\t"
        "s_add_u32 s25, s24, 0x4000\n\t"
        "s_add_u32 s26, s24, 0x8000\n\t"
        "s_add_u32 s27, s27, s79\n\t"
        "s_add_u32 s28, s27, 0x4000\n\t"
        "s_add_u32 s29, s27, 0x8000\n\t"
        "s_lshr_b32 s78, s24, 8\n\t"
        "s_and_b32 s78, s78, 0x2000\n\t"
        "s_xor_b32 s24, s24, s78\n\t"
        "s_lshr_b32 s78, s25, 8\n\t"
        "s_and_b32 s78, s78, 0x2000\n\t"
        "s_xor_b32 s25, s25, s78\n\t"
        "s_lshr_b32 s78, s26, 8\n\t"
        "s_and_b32 s78, s78, 0x2000\n\t"
        "s_xor_b32 s26, s26, s78\n\t"
        "s_lshr_b32 s78, s27, 8\n\t"
        "s_and_b32 s78, s78, 0x2000\n\t"
        "s_xor_b32 s27, s27, s78\n\t"
        "s_lshr_b32 s78, s28, 8\n\t"
        "s_and_b32 s78, s78, 0x2000\n\t"
        "s_xor_b32 s28, s28, s78\n\t"
        "s_lshr_b32 s78, s29, 8\n\t"
        "s_and_b32 s78, s78, 0x2000\n\t"
        "s_xor_b32 s29, s29, s78\n\t"
        "v_and_b32 v14, 48, v8\n\t"
        "s_mov_b32 s78, 0xffff\n\t"
        "s_mov_b32 s79, 0\n\t"
        "v_lshlrev_b64 v[4:5], v14, s[78:79]\n\t"
        "v_xor_b32 v18, v110, v8\n\t"
        "v_xor_b32 v19, v111, v8\n\t"
        "v_and_b32 v14, 1, v6\n\t"
        "v_lshl_add_u32 v14, v14, 3, v8\n\t"
        "v_xor_b32 v20, v112, v14\n\t"
        "v_xor_b32 v21, v21, v22\n"
        ".L7_w11:\n\t"
        "ds_read_b32 v0, v2 offset:36\n\t"
        "s_waitcnt lgkmcnt(0)\n\t"
        "v_cmp_gt_u16 vcc, 4, v0\n\t"
        "s_cbranch_vccz .L7_d12\n\t"
        "s_sleep 1\n\t"
        "s_sub_i32 s83, s83, 1\n\t"
        "s_cmp_gt_i32 s83, 0\n\t"
        "s_cbranch_scc1 .L7_w11\n"
        ".L7_d12:\n\t"
        "v_lshrrev_b32 v0, 20, v0\n\t"
        "s_nop 0\n\t"
        "v_readfirstlane_b32 s58, v0\n\t"
        "s_sleep 8\n"
        ".L7_sp:\n\t"
        "s_setprio 3\n\t"
        "buffer_load_dword v12, v13, s[20:23], s43 offen sc1\n\t"
        "s_setprio 2\n\t"
        "s_waitcnt vmcnt(0)\n\t"
        "v_xor_b32 v12, v12, v22\n\t"
        "v_and_b32 v0, s48, v12\n\t"
        "v_cmp_ne_u32 vcc, 0, v0\n\t"
        "s_cbranch_vccz .L7_sq\n\t"
        "s_sleep 8\n\t"
        "s_sub_i32 s83, s83, 1\n\t"
        "s_cmp_gt_i32 s83, 0\n\t"
        "s_cbranch_scc1 .L7_sp\n"
        ".L7_sq:\n\t"
        "v_add_f32 v21, v21, v12\n\t"
        "v_exp_f32 v21, v21\n\t"
        "v_lshlrev_b32 v22, 4, v1\n\t"
        "v_add_u32 v22, 142848, v22\n\t"
        "s_cmp_lg_u32 s58, 0\n\t"
        "s_cbranch_scc1 .L7_na\n\t"
        "v_add_f32_dpp v0, v21, v21 row_ror:4 row_mask:0xf bank_mask:0xf\n\t"
        "s_cmp_lt_u32 s69, 4\n\t"
        "s_cselect_b32 s78, 16, 1\n\t"
        "v_add_f32_dpp v0, v0, v0 row_ror:8 row_mask:0xf bank_mask:0xf\n\t"
        "v_add_f32 v0, 0x3089705f, v0\n\t"
        "v_rcp_f32 v0, v0\n\t"
        "s_nop 0\n\t"
        "v_mul_f32 v21, v21, v0\n\t"
        "v_cvt_pk_bf16_f32 v21, v21, v21\n\t"
        "v_mov_b32 v0, s78\n\t"
        "s_nop 0\n\t"
        "v_and_b32_dpp v6, v21, v4 quad_perm:[0,0,0,0] row_mask:0xf bank_mask:0xf\n\t"
        "v_and_b32_dpp v7, v21, v5 quad_perm:[0,0,0,0] row_mask:0xf bank_mask:0xf\n\t"
        "v_and_b32_dpp v16, v21, v4 quad_perm:[1,1,1,1] row_mask:0xf bank_mask:0xf\n\t"
        "v_and_b32_dpp v17, v21, v5 quad_perm:[1,1,1,1] row_mask:0xf bank_mask:0xf\n\t"
        "v_mfma_f32_4x4x4_16b_bf16 v[8:11], v[6:7], v[24:25], 0 cbsz:2 abid:0\n\t"
        "v_mfma_f32_4x4x4_16b_bf16 v[12:15], v[6:7], v[26:27], 0 cbsz:2 abid:0\n\t"
        "v_mfma_f32_4x4x4_16b_bf16 v[120:123], v[16:17], v[40:41], 0 cbsz:2 abid:0\n\t"
        "v_mfma_f32_4x4x4_16b_bf16 v[124:127], v[16:17], v[42:43], 0 cbsz:2 abid:0\n\t"
        "v_mfma_f32_4x4x4_16b_bf16 v[8:11], v[6:7], v[28:29], v[8:11] cbsz:2 abid:2\n\t"
        "v_mfma_f32_4x4x4_16b_bf16 v[12:15], v[6:7], v[30:31], v[12:15] cbsz:2 abid:2\n\t"
        "v_mfma_f32_4x4x4_16b_bf16 v[120:123], v[16:17], v[44:45], v[120:123] cbsz:2 abid:2\n\t"
        "v_mfma_f32_4x4x4_16b_bf16 v[124:127], v[16:17], v[46:47], v[124:127] cbsz:2 abid:2\n\t"
        "v_mfma_f32_4x4x4_16b_bf16 v[8:11], v[6:7], v[32:33], v[8:11] cbsz:2 abid:1\n\t"
        "v_mfma_f32_4x4x4_16b_bf16 v[12:15], v[6:7], v[34:35], v[12:15] cbsz:2 abid:1\n\t"
        "v_mfma_f32_4x4x4_16b_bf16 v[120:123], v[16:17], v[48:49], v[120:123] cbsz:2 abid:1\n\t"
        "v_mfma_f32_4x4x4_16b_bf16 v[124:127], v[16:17], v[50:51], v[124:127] cbsz:2 abid:1\n\t"
        "v_mfma_f32_4x4x4_16b_bf16 v[8:11], v[6:7], v[36:37], v[8:11] cbsz:2 abid:3\n\t"
        "v_mfma_f32_4x4x4_16b_bf16 v[12:15], v[6:7], v[38:39], v[12:15] cbsz:2 abid:3\n\t"
        "v_mfma_f32_4x4x4_16b_bf16 v[120:123], v[16:17], v[52:53], v[120:123] cbsz:2 abid:3\n\t"
        "v_mfma_f32_4x4x4_16b_bf16 v[124:127], v[16:17], v[54:55], v[124:127] cbsz:2 abid:3\n\t"
        "v_and_b32_dpp v6, v21, v4 quad_perm:[2,2,2,2] row_mask:0xf bank_mask:0xf\n\t"
        "v_and_b32_dpp v7, v21, v5 quad_perm:[2,2,2,2] row_mask:0xf bank_mask:0xf\n\t"
        "s_nop 0\n\t"
        "s_nop 0\n\t"
        "v_mfma_f32_4x4x4_16b_bf16 v[24:27], v[6:7], v[56:57], 0 cbsz:2 abid:0\n\t"
        "v_mfma_f32_4x4x4_16b_bf16 v[28:31], v[6:7], v[58:59], 0 cbsz:2 abid:0\n\t"
        "v_cvt_pk_bf16_f32 v8, v8, v9\n\t"
        "v_cvt_pk_bf16_f32 v9, v10, v11\n\t"
        "v_cvt_pk_bf16_f32 v10, v12, v13\n\t"
        "v_cvt_pk_bf16_f32 v11, v14, v15\n\t"
        "ds_write_b128 v18, v[8:11] offset:46336\n\t"
        "v_mfma_f32_4x4x4_16b_bf16 v[24:27], v[6:7], v[60:61], v[24:27] cbsz:2 abid:2\n\t"
        "v_mfma_f32_4x4x4_16b_bf16 v[28:31], v[6:7], v[62:63], v[28:31] cbsz:2 abid:2\n\t"
        "v_cvt_pk_bf16_f32 v120, v120, v121\n\t"
        "v_cvt_pk_bf16_f32 v121, v122, v123\n\t"
        "v_cvt_pk_bf16_f32 v122, v124, v125\n\t"
        "v_cvt_pk_bf16_f32 v123, v126, v127\n\t"
        "ds_write_b128 v19, v[120:123] offset:46336\n\t"
        "v_permlane16_swap_b32 v24, v28\n\t"
        "v_permlane16_swap_b32 v25, v29\n\t"
        "v_permlane16_swap_b32 v26, v30\n\t"
        "v_permlane16_swap_b32 v27, v31\n\t"
        "v_pk_add_f32 v[24:25], v[24:25], v[28:29]\n\t"
        "v_pk_add_f32 v[26:27], v[26:27], v[30:31]\n\t"
        "v_cvt_pk_bf16_f32 v24, v24, v25\n\t"
        "v_cvt_pk_bf16_f32 v25, v26, v27\n\t"
        "ds_write_b64 v20, v[24:25] offset:46336\n\t"
        "s_mov_b64 exec, 1\n\t"
        "ds_add_u32 v2, v0 offset:40\n\t"
        "s_mov_b64 exec, -1\n"
        ".L7_na:\n"
        ".L7_w13:\n\t"
        "ds_read_b32 v0, v2 offset:28\n\t"
        "s_waitcnt lgkmcnt(0)\n\t"
        "v_cmp_gt_u16 vcc, 4, v0\n\t"
        "s_cbranch_vccz .L7_d14\n\t"
        "s_sleep 1\n\t"
        "s_sub_i32 s83, s83, 1\n\t"
        "s_cmp_gt_i32 s83, 0\n\t"
        "s_cbranch_scc1 .L7_w13\n"
        ".L7_d14:\n\t"
        "ds_read_b64_tr_b16 v[96:97], v23 offset:0\n\t"
        "ds_read_b64_tr_b16 v[98:99], v23 offset:512\n\t"
        "ds_read_b64_tr_b16 v[100:101], v23 offset:8\n\t"
        "ds_read_b64_tr_b16 v[102:103], v23 offset:520\n\t"
        "ds_read_b64_tr_b16 v[104:105], v23 offset:4096\n\t"
        "ds_read_b64_tr_b16 v[106:107], v23 offset:4608\n\t"
        "ds_read_b64_tr_b16 v[108:109], v23 offset:4104\n\t"
        "ds_read_b64_tr_b16 v[110:111], v23 offset:4616\n\t"
        "ds_read_b64_tr_b16 v[112:113], v23 offset:8192\n\t"
        "ds_read_b64_tr_b16 v[114:115], v23 offset:8704\n\t"
        "ds_read_b64_tr_b16 v[116:117], v23 offset:8200\n\t"
        "ds_read_b64_tr_b16 v[118:119], v23 offset:8712\n\t"
        "s_waitcnt lgkmcnt(6)\n\t"
        "ds_read_b64_tr_b16 v[120:121], v23 offset:12288\n\t"
        "ds_read_b64_tr_b16 v[122:123], v23 offset:12800\n\t"
        "ds_read_b64_tr_b16 v[124:125], v23 offset:12296\n\t"
        "ds_read_b64_tr_b16 v[126:127], v23 offset:12808\n\t"
        "ds_read_b128 v[4:7], v22\n\t"
        "s_lshl_b32 s50, s81, 6\n\t"
        "s_lshl_b32 s51, s69, 10\n\t"
        "s_add_u32 s51, s51, 134656\n\t"
        "s_mov_b32 s49, 0x10001\n\t"
        "s_mov_b32 s48, 0x07060302\n\t"
        "s_mov_b32 s55, 142848\n"
        ".L7_w15:\n\t"
        "ds_read_b32 v0, v2 offset:40\n\t"
        "s_waitcnt lgkmcnt(0)\n\t"
        "v_cmp_gt_u16 vcc, 64, v0\n\t"
        "s_cbranch_vccz .L7_d16\n\t"
        "s_setprio 0\n\t"
        "s_sleep 1\n\t"
        "s_sub_i32 s83, s83, 1\n\t"
        "s_cmp_gt_i32 s83, 0\n\t"
        "s_cbranch_scc1 .L7_w15\n"
        ".L7_d16:\n\t"
        "s_setprio 2\n\t"
        "ds_read_b128 v[8:11], v4 offset:46336\n\t"
        "ds_read_b128 v[12:15], v5 offset:46336\n\t"
        "ds_read_b128 v[16:19], v6 offset:46336\n\t"
        "ds_read_b128 v[20:23], v7 offset:46336\n\t"
        "ds_read_b128 v[48:51], v4 offset:50432\n\t"
        "ds_read_b128 v[52:55], v5 offset:50432\n\t"
        "ds_read_b128 v[56:59], v6 offset:50432\n\t"
        "ds_read_b128 v[60:63], v7 offset:50432\n\t"
        "v_and_b32 v3, 8, v1\n\t"
        "s_waitcnt lgkmcnt(6)\n\t"
        "v_mfma_f32_16x16x32_bf16 v[32:35], v[64:67], v[8:11], 0\n\t"
        "v_mfma_f32_16x16x32_bf16 v[32:35], v[72:75], v[12:15], v[32:35]\n\t"
        "s_waitcnt lgkmcnt(4)\n\t"
        "v_mfma_f32_16x16x32_bf16 v[32:35], v[80:83], v[16:19], v[32:35]\n\t"
        "v_mfma_f32_16x16x32_bf16 v[32:35], v[88:91], v[20:23], v[32:35]\n\t"
        "v_mfma_f32_16x16x32_bf16 v[36:39], v[92:95], v[20:23], 0\n\t"
        "v_mfma_f32_16x16x32_bf16 v[36:39], v[84:87], v[16:19], v[36:39]\n\t"
        "v_mfma_f32_16x16x32_bf16 v[36:39], v[76:79], v[12:15], v[36:39]\n\t"
        "v_mfma_f32_16x16x32_bf16 v[36:39], v[68:71], v[8:11], v[36:39]\n\t"
        "ds_read_b32 v0, v2 offset:40\n\t"
        "v_lshl_add_u32 v8, v3, 8, v4\n\t"
        "v_lshl_add_u32 v12, v3, 8, v5\n\t"
        "v_lshl_add_u32 v16, v3, 8, v6\n\t"
        "v_lshl_add_u32 v20, v3, 8, v7\n\t"
        "ds_read_b128 v[8:11], v8 offset:54528\n\t"
        "ds_read_b128 v[12:15], v12 offset:54528\n\t"
        "ds_read_b128 v[16:19], v16 offset:54528\n\t"
        "ds_read_b128 v[20:23], v20 offset:54528\n\t"
        "s_waitcnt lgkmcnt(7)\n\t"
        "v_mfma_f32_16x16x32_bf16 v[40:43], v[64:67], v[48:51], 0\n\t"
        "v_mfma_f32_16x16x32_bf16 v[40:43], v[72:75], v[52:55], v[40:43]\n\t"
        "s_waitcnt lgkmcnt(5)\n\t"
        "v_mfma_f32_16x16x32_bf16 v[40:43], v[80:83], v[56:59], v[40:43]\n\t"
        "v_mfma_f32_16x16x32_bf16 v[40:43], v[88:91], v[60:63], v[40:43]\n\t"
        "v_mfma_f32_16x16x32_bf16 v[44:47], v[92:95], v[60:63], 0\n\t"
        "v_mfma_f32_16x16x32_bf16 v[44:47], v[84:87], v[56:59], v[44:47]\n\t"
        "v_mfma_f32_16x16x32_bf16 v[44:47], v[76:79], v[52:55], v[44:47]\n\t"
        "v_mfma_f32_16x16x32_bf16 v[44:47], v[68:71], v[48:51], v[44:47]\n\t"
        "s_waitcnt lgkmcnt(4)\n\t"
        "v_cmp_gt_u16 vcc, 68, v0\n\t"
        "s_cbranch_vccz .L7_t0\n"
        ".L7_w17:\n\t"
        "ds_read_b32 v0, v2 offset:40\n\t"
        "s_waitcnt lgkmcnt(0)\n\t"
        "v_cmp_gt_u16 vcc, 68, v0\n\t"
        "s_cbranch_vccz .L7_d18\n\t"
        "s_setprio 0\n\t"
        "s_sleep 1\n\t"
        "s_sub_i32 s83, s83, 1\n\t"
        "s_cmp_gt_i32 s83, 0\n\t"
        "s_cbranch_scc1 .L7_w17\n"
        ".L7_d18:\n\t"
        "s_setprio 2\n\t"
        "v_lshl_add_u32 v8, v3, 8, v4\n\t"
        "v_lshl_add_u32 v12, v3, 8, v5\n\t"
        "v_lshl_add_u32 v16, v3, 8, v6\n\t"
        "v_lshl_add_u32 v20, v3, 8, v7\n\t"
        "ds_read_b128 v[8:11], v8 offset:54528\n\t"
        "ds_read_b128 v[12:15], v12 offset:54528\n\t"
        "ds_read_b128 v[16:19], v16 offset:54528\n\t"
        "ds_read_b128 v[20:23], v20 offset:54528\n"
        ".L7_t0:\n\t"
        "ds_read_b32 v0, v2 offset:52\n\t"
        "ds_read_b128 v[48:51], v4 offset:23808\n\t"
        "ds_read_b128 v[52:55], v5 offset:23808\n\t"
        "ds_read_b128 v[56:59], v6 offset:23808\n\t"
        "ds_read_b128 v[60:63], v7 offset:23808\n\t"
        "s_waitcnt lgkmcnt(7)\n\t"
        "v_mfma_f32_16x16x32_bf16 v[24:27], v[64:67], v[8:11], 0\n\t"
        "v_mfma_f32_16x16x32_bf16 v[24:27], v[72:75], v[12:15], v[24:27]\n\t"
        "s_waitcnt lgkmcnt(5)\n\t"
        "v_mfma_f32_16x16x32_bf16 v[24:27], v[80:83], v[16:19], v[24:27]\n\t"
        "v_mfma_f32_16x16x32_bf16 v[24:27], v[88:91], v[20:23], v[24:27]\n\t"
        "v_mfma_f32_16x16x32_bf16 v[28:31], v[92:95], v[20:23], 0\n\t"
        "v_mfma_f32_16x16x32_bf16 v[28:31], v[84:87], v[16:19], v[28:31]\n\t"
        "v_mfma_f32_16x16x32_bf16 v[28:31], v[76:79], v[12:15], v[28:31]\n\t"
        "v_mfma_f32_16x16x32_bf16 v[28:31], v[68:71], v[8:11], v[28:31]\n\t"
        "ds_read_b128 v[8:11], v4 offset:32000\n\t"
        "ds_read_b128 v[12:15], v5 offset:32000\n\t"
        "ds_read_b128 v[16:19], v6 offset:32000\n\t"
        "ds_read_b128 v[20:23], v7 offset:32000\n\t"
        "s_waitcnt lgkmcnt(8)\n\t"
        "v_cmp_gt_u16 vcc, 6, v0\n\t"
        "s_cbranch_vccz .L7_t1\n"
        ".L7_w19:\n\t"
        "ds_read_b32 v0, v2 offset:52\n\t"
        "s_waitcnt lgkmcnt(0)\n\t"
        "v_cmp_gt_u16 vcc, 6, v0\n\t"
        "s_cbranch_vccz .L7_d20\n\t"
        "s_setprio 0\n\t"
        "s_sleep 1\n\t"
        "s_sub_i32 s83, s83, 1\n\t"
        "s_cmp_gt_i32 s83, 0\n\t"
        "s_cbranch_scc1 .L7_w19\n"
        ".L7_d20:\n\t"
        "s_setprio 2\n\t"
        "ds_read_b128 v[48:51], v4 offset:23808\n\t"
        "ds_read_b128 v[52:55], v5 offset:23808\n\t"
        "ds_read_b128 v[56:59], v6 offset:23808\n\t"
        "ds_read_b128 v[60:63], v7 offset:23808\n\t"
        "ds_read_b128 v[8:11], v4 offset:32000\n\t"
        "ds_read_b128 v[12:15], v5 offset:32000\n\t"
        "ds_read_b128 v[16:19], v6 offset:32000\n\t"
        "ds_read_b128 v[20:23], v7 offset:32000\n"
        ".L7_t1:\n\t"
        "v_lshl_add_u32 v3, v1, 4, s55\n\t"
        "s_waitcnt lgkmcnt(6)\n\t"
        "v_mfma_f32_16x16x32_bf16 v[32:35], v[96:99], v[48:51], v[32:35]\n\t"
        "v_mfma_f32_16x16x32_bf16 v[32:35], v[104:107], v[52:55], v[32:35]\n\t"
        "s_waitcnt lgkmcnt(4)\n\t"
        "v_mfma_f32_16x16x32_bf16 v[32:35], v[112:115], v[56:59], v[32:35]\n\t"
        "v_mfma_f32_16x16x32_bf16 v[32:35], v[120:123], v[60:63], v[32:35]\n\t"
        "v_mfma_f32_16x16x32_bf16 v[36:39], v[124:127], v[60:63], v[36:39]\n\t"
        "v_mfma_f32_16x16x32_bf16 v[36:39], v[116:119], v[56:59], v[36:39]\n\t"
        "v_mfma_f32_16x16x32_bf16 v[36:39], v[108:111], v[52:55], v[36:39]\n\t"
        "v_mfma_f32_16x16x32_bf16 v[36:39], v[100:103], v[48:51], v[36:39]\n\t"
        "ds_read_b128 v[60:63], v3 offset:1024\n\t"
        "s_waitcnt lgkmcnt(3)\n\t"
        "v_mfma_f32_16x16x32_bf16 v[24:27], v[96:99], v[8:11], v[24:27]\n\t"
        "v_mfma_f32_16x16x32_bf16 v[24:27], v[104:107], v[12:15], v[24:27]\n\t"
        "s_waitcnt lgkmcnt(1)\n\t"
        "v_mfma_f32_16x16x32_bf16 v[24:27], v[112:115], v[16:19], v[24:27]\n\t"
        "v_mfma_f32_16x16x32_bf16 v[24:27], v[120:123], v[20:23], v[24:27]\n\t"
        "v_mfma_f32_16x16x32_bf16 v[28:31], v[124:127], v[20:23], v[28:31]\n\t"
        "v_mfma_f32_16x16x32_bf16 v[28:31], v[116:119], v[16:19], v[28:31]\n\t"
        "v_mfma_f32_16x16x32_bf16 v[28:31], v[108:111], v[12:15], v[28:31]\n\t"
        "v_mfma_f32_16x16x32_bf16 v[28:31], v[100:103], v[8:11], v[28:31]\n\t"
        "ds_read_b32 v0, v2 offset:56\n\t"
        "ds_read_b128 v[8:11], v4 offset:27904\n\t"
        "ds_read_b128 v[12:15], v5 offset:27904\n\t"
        "ds_read_b128 v[16:19], v6 offset:27904\n\t"
        "ds_read_b128 v[20:23], v7 offset:27904\n\t"
        "s_waitcnt lgkmcnt(5)\n\t"
        "v_add_u32 v32, 0x10000, v32\n\t"
        "v_add_u32 v33, 0x10000, v33\n\t"
        "v_add_u32 v34, 0x10000, v34\n\t"
        "v_add_u32 v35, 0x10000, v35\n\t"
        "v_perm_b32 v52, v33, v32, s48\n\t"
        "v_perm_b32 v53, v35, v34, s48\n\t"
        "v_cvt_pk_bf16_f32 v54, v36, v37\n\t"
        "v_cvt_pk_bf16_f32 v55, v38, v39\n\t"
        "v_bfi_b32 v52, s49, v60, v52\n\t"
        "v_bfi_b32 v53, s49, v61, v53\n\t"
        "buffer_store_dwordx4 v[52:55], v62, s[20:23], s25 offen sc1\n\t"
        "s_mov_b32 exec_lo, 0x00ff00ff\n\t"
        "s_mov_b32 exec_hi, 0x00ff00ff\n\t"
        "v_add_u32 v24, 0x10000, v24\n\t"
        "v_add_u32 v25, 0x10000, v25\n\t"
        "v_add_u32 v26, 0x10000, v26\n\t"
        "v_add_u32 v27, 0x10000, v27\n\t"
        "v_perm_b32 v48, v25, v24, s48\n\t"
        "v_perm_b32 v49, v27, v26, s48\n\t"
        "v_cvt_pk_bf16_f32 v50, v28, v29\n\t"
        "v_cvt_pk_bf16_f32 v51, v30, v31\n\t"
        "v_bfi_b32 v48, s49, v60, v48\n\t"
        "v_bfi_b32 v49, s49, v61, v49\n\t"
        "buffer_store_dwordx4 v[48:51], v62, s[20:23], s24 offen sc1\n\t"
        "s_mov_b64 exec, -1\n\t"
        "s_waitcnt lgkmcnt(4)\n\t"
        "v_cmp_gt_u16 vcc, 4, v0\n\t"
        "s_cbranch_vccz .L7_t2\n"
        ".L7_w21:\n\t"
        "ds_read_b32 v0, v2 offset:56\n\t"
        "s_waitcnt lgkmcnt(0)\n\t"
        "v_cmp_gt_u16 vcc, 3, v0\n\t"
        "s_cbranch_vccz .L7_d22\n\t"
        "s_setprio 0\n\t"
        "s_sleep 1\n\t"
        "s_sub_i32 s83, s83, 1\n\t"
        "s_cmp_gt_i32 s83, 0\n\t"
        "s_cbranch_scc1 .L7_w21\n"
        ".L7_d22:\n\t"
        "s_setprio 2\n"
        ".L7_w23:\n\t"
        "ds_read_b32 v0, v2 offset:56\n\t"
        "ds_read_b128 v[8:11], v4 offset:27904\n\t"
        "ds_read_b128 v[12:15], v5 offset:27904\n\t"
        "ds_read_b128 v[16:19], v6 offset:27904\n\t"
        "ds_read_b128 v[20:23], v7 offset:27904\n\t"
        "s_waitcnt lgkmcnt(4)\n\t"
        "v_cmp_gt_u16 vcc, 4, v0\n\t"
        "s_cbranch_vccz .L7_d24\n\t"
        "s_waitcnt lgkmcnt(0)\n\t"
        "s_setprio 0\n\t"
        "s_sleep 1\n\t"
        "s_sub_i32 s83, s83, 1\n\t"
        "s_cmp_gt_i32 s83, 0\n\t"
        "s_cbranch_scc1 .L7_w23\n"
        ".L7_d24:\n\t"
        "s_setprio 2\n"
        ".L7_t2:\n\t"
        "s_waitcnt lgkmcnt(2)\n\t"
        "v_mfma_f32_16x16x32_bf16 v[40:43], v[96:99], v[8:11], v[40:43]\n\t"
        "v_mfma_f32_16x16x32_bf16 v[40:43], v[104:107], v[12:15], v[40:43]\n\t"
        "s_waitcnt lgkmcnt(0)\n\t"
        "v_mfma_f32_16x16x32_bf16 v[40:43], v[112:115], v[16:19], v[40:43]\n\t"
        "v_mfma_f32_16x16x32_bf16 v[40:43], v[120:123], v[20:23], v[40:43]\n\t"
        "v_mfma_f32_16x16x32_bf16 v[44:47], v[124:127], v[20:23], v[44:47]\n\t"
        "v_mfma_f32_16x16x32_bf16 v[44:47], v[116:119], v[16:19], v[44:47]\n\t"
        "v_mfma_f32_16x16x32_bf16 v[44:47], v[108:111], v[12:15], v[44:47]\n\t"
        "v_mfma_f32_16x16x32_bf16 v[44:47], v[100:103], v[8:11], v[44:47]\n\t"
        "ds_read_b128 v[8:11], v4 offset:60672\n\t"
        "ds_read_b128 v[12:15], v5 offset:60672\n\t"
        "ds_read_b128 v[16:19], v6 offset:60672\n\t"
        "ds_read_b128 v[20:23], v7 offset:60672\n\t"
        "v_add_u32 v40, 0x10000, v40\n\t"
        "v_add_u32 v41, 0x10000, v41\n\t"
        "v_add_u32 v42, 0x10000, v42\n\t"
        "v_add_u32 v43, 0x10000, v43\n\t"
        "v_perm_b32 v56, v41, v40, s48\n\t"
        "v_perm_b32 v57, v43, v42, s48\n\t"
        "v_cvt_pk_bf16_f32 v58, v44, v45\n\t"
        "v_cvt_pk_bf16_f32 v59, v46, v47\n\t"
        "v_bfi_b32 v56, s49, v60, v56\n\t"
        "v_bfi_b32 v57, s49, v61, v57\n\t"
        "buffer_store_dwordx4 v[56:59], v62, s[20:23], s26 offen sc1\n\t"
        "v_mov_b32 v0, 1\n\t"
        "s_mov_b64 exec, 1\n\t"
        "ds_add_u32 v2, v0 offset:96\n\t"
        "s_mov_b64 exec, -1\n\t"
        "s_waitcnt lgkmcnt(3)\n\t"
        "v_mfma_f32_16x16x32_bf16 v[32:35], v[64:67], v[8:11], 0\n\t"
        "v_mfma_f32_16x16x32_bf16 v[32:35], v[72:75], v[12:15], v[32:35]\n\t"
        "s_waitcnt lgkmcnt(1)\n\t"
        "v_mfma_f32_16x16x32_bf16 v[32:35], v[80:83], v[16:19], v[32:35]\n\t"
        "v_mfma_f32_16x16x32_bf16 v[32:35], v[88:91], v[20:23], v[32:35]\n\t"
        "v_mfma_f32_16x16x32_bf16 v[36:39], v[92:95], v[20:23], 0\n\t"
        "v_mfma_f32_16x16x32_bf16 v[36:39], v[84:87], v[16:19], v[36:39]\n\t"
        "v_mfma_f32_16x16x32_bf16 v[36:39], v[76:79], v[12:15], v[36:39]\n\t"
        "v_mfma_f32_16x16x32_bf16 v[36:39], v[68:71], v[8:11], v[36:39]\n\t"
        "ds_read_b128 v[8:11], v4 offset:64768\n\t"
        "ds_read_b128 v[12:15], v5 offset:64768\n\t"
        "ds_read_b128 v[16:19], v6 offset:64768\n\t"
        "ds_read_b128 v[20:23], v7 offset:64768\n\t"
        "s_waitcnt lgkmcnt(2)\n\t"
        "v_mfma_f32_16x16x32_bf16 v[40:43], v[64:67], v[8:11], 0\n\t"
        "v_mfma_f32_16x16x32_bf16 v[40:43], v[72:75], v[12:15], v[40:43]\n\t"
        "s_waitcnt lgkmcnt(0)\n\t"
        "v_mfma_f32_16x16x32_bf16 v[40:43], v[80:83], v[16:19], v[40:43]\n\t"
        "v_mfma_f32_16x16x32_bf16 v[40:43], v[88:91], v[20:23], v[40:43]\n\t"
        "v_mfma_f32_16x16x32_bf16 v[44:47], v[92:95], v[20:23], 0\n\t"
        "v_mfma_f32_16x16x32_bf16 v[44:47], v[84:87], v[16:19], v[44:47]\n\t"
        "v_mfma_f32_16x16x32_bf16 v[44:47], v[76:79], v[12:15], v[44:47]\n\t"
        "v_mfma_f32_16x16x32_bf16 v[44:47], v[68:71], v[8:11], v[44:47]\n\t"
        "ds_read_b128 v[68:71], v3 offset:2048\n\t"
        "ds_read_b128 v[74:77], v3 offset:3072\n"
        ".L7_w25:\n\t"
        "ds_read_b32 v0, v2 offset:60\n\t"
        "s_waitcnt lgkmcnt(0)\n\t"
        "v_cmp_gt_u16 vcc, 2, v0\n\t"
        "s_cbranch_vccz .L7_d26\n\t"
        "s_setprio 0\n\t"
        "s_sleep 1\n\t"
        "s_sub_i32 s83, s83, 1\n\t"
        "s_cmp_gt_i32 s83, 0\n\t"
        "s_cbranch_scc1 .L7_w25\n"
        ".L7_d26:\n\t"
        "s_setprio 2\n\t"
        "ds_read_b128 v[8:11], v4 offset:34048\n\t"
        "ds_read_b128 v[12:15], v5 offset:34048\n\t"
        "ds_read_b128 v[16:19], v6 offset:34048\n\t"
        "ds_read_b128 v[20:23], v7 offset:34048\n\t"
        "v_add_u32 v71, s51, v71\n\t"
        "v_add_u32 v72, s51, v74\n\t"
        "v_mov_b64 v[74:75], 0\n\t"
        "v_mov_b64 v[78:79], 0\n\t"
        "s_waitcnt lgkmcnt(2)\n\t"
        "v_mfma_f32_16x16x32_bf16 v[24:27], v[96:99], v[8:11], v[24:27]\n\t"
        "v_mfma_f32_16x16x32_bf16 v[24:27], v[104:107], v[12:15], v[24:27]\n\t"
        "s_waitcnt lgkmcnt(0)\n\t"
        "v_mfma_f32_16x16x32_bf16 v[24:27], v[112:115], v[16:19], v[24:27]\n\t"
        "v_mfma_f32_16x16x32_bf16 v[24:27], v[120:123], v[20:23], v[24:27]\n\t"
        "v_mfma_f32_16x16x32_bf16 v[28:31], v[124:127], v[20:23], v[28:31]\n\t"
        "v_mfma_f32_16x16x32_bf16 v[28:31], v[116:119], v[16:19], v[28:31]\n\t"
        "v_mfma_f32_16x16x32_bf16 v[28:31], v[108:111], v[12:15], v[28:31]\n\t"
        "v_mfma_f32_16x16x32_bf16 v[28:31], v[100:103], v[8:11], v[28:31]\n"
        ".L7_w27:\n\t"
        "ds_read_b32 v0, v2 offset:100\n\t"
        "s_waitcnt lgkmcnt(0)\n\t"
        "v_cmp_gt_u16 vcc, 1, v0\n\t"
        "s_cbranch_vccz .L7_d28\n\t"
        "s_setprio 0\n\t"
        "s_sleep 1\n\t"
        "s_sub_i32 s83, s83, 1\n\t"
        "s_cmp_gt_i32 s83, 0\n\t"
        "s_cbranch_scc1 .L7_w27\n"
        ".L7_d28:\n\t"
        "s_setprio 2\n\t"
        "buffer_load_dwordx4 v[48:51], v63, s[20:23], s27 offen sc1\n\t"
        "buffer_load_dwordx4 v[52:55], v62, s[20:23], s28 offen sc1\n\t"
        "buffer_load_dwordx4 v[56:59], v62, s[20:23], s29 offen sc1\n"
        ".L7_w29:\n\t"
        "ds_read_b32 v0, v2 offset:64\n\t"
        "s_waitcnt lgkmcnt(0)\n\t"
        "v_cmp_gt_u16 vcc, 4, v0\n\t"
        "s_cbranch_vccz .L7_d30\n\t"
        "s_setprio 0\n\t"
        "s_sleep 1\n\t"
        "s_sub_i32 s83, s83, 1\n\t"
        "s_cmp_gt_i32 s83, 0\n\t"
        "s_cbranch_scc1 .L7_w29\n"
        ".L7_d30:\n\t"
        "s_setprio 2\n\t"
        "ds_read_b128 v[8:11], v4 offset:38144\n\t"
        "ds_read_b128 v[12:15], v5 offset:38144\n\t"
        "ds_read_b128 v[16:19], v6 offset:38144\n\t"
        "ds_read_b128 v[20:23], v7 offset:38144\n\t"
        "s_waitcnt lgkmcnt(2)\n\t"
        "v_mfma_f32_16x16x32_bf16 v[32:35], v[96:99], v[8:11], v[32:35]\n\t"
        "v_mfma_f32_16x16x32_bf16 v[32:35], v[104:107], v[12:15], v[32:35]\n\t"
        "s_waitcnt lgkmcnt(0)\n\t"
        "v_mfma_f32_16x16x32_bf16 v[32:35], v[112:115], v[16:19], v[32:35]\n\t"
        "v_mfma_f32_16x16x32_bf16 v[32:35], v[120:123], v[20:23], v[32:35]\n\t"
        "v_mfma_f32_16x16x32_bf16 v[36:39], v[124:127], v[20:23], v[36:39]\n\t"
        "v_mfma_f32_16x16x32_bf16 v[36:39], v[116:119], v[16:19], v[36:39]\n\t"
        "v_mfma_f32_16x16x32_bf16 v[36:39], v[108:111], v[12:15], v[36:39]\n\t"
        "v_mfma_f32_16x16x32_bf16 v[36:39], v[100:103], v[8:11], v[36:39]\n\t"
        "s_waitcnt vmcnt(0)\n\t"
        "s_branch .L7_s32\n"
        ".L7_r31:\n\t"
        "buffer_load_dwordx4 v[48:51], v63, s[20:23], s27 offen sc1\n\t"
        "buffer_load_dwordx4 v[52:55], v62, s[20:23], s28 offen sc1\n\t"
        "buffer_load_dwordx4 v[56:59], v62, s[20:23], s29 offen sc1\n\t"
        "s_waitcnt vmcnt(0)\n"
        ".L7_s32:\n\t"
        "v_xor_b32 v48, v48, v60\n\t"
        "v_xor_b32 v49, v49, v61\n\t"
        "v_xor_b32 v52, v52, v60\n\t"
        "v_xor_b32 v53, v53, v61\n\t"
        "v_xor_b32 v56, v56, v60\n\t"
        "v_xor_b32 v57, v57, v61\n\t"
        "v_or_b32 v0, v48, v49\n\t"
        "v_or3_b32 v0, v0, v52, v53\n\t"
        "v_or3_b32 v0, v0, v56, v57\n\t"
        "v_and_b32 v0, s49, v0\n\t"
        "v_cmp_ne_u32 vcc, 0, v0\n\t"
        "s_cbranch_vccz .L7_t33\n\t"
        "s_sleep 8\n\t"
        "s_sub_i32 s83, s83, 1\n\t"
        "s_cmp_gt_i32 s83, 0\n\t"
        "s_cbranch_scc1 .L7_r31\n"
        ".L7_t33:\n\t"
        "v_mfma_f32_16x16x32_bf16 v[24:27], v[76:79], v[48:51], v[24:27]\n\t"
        "v_mfma_f32_16x16x32_bf16 v[28:31], v[74:77], v[48:51], v[28:31]\n\t"
        "v_mfma_f32_16x16x32_bf16 v[32:35], v[76:79], v[52:55], v[32:35]\n\t"
        "v_mfma_f32_16x16x32_bf16 v[36:39], v[74:77], v[52:55], v[36:39]\n\t"
        "v_mfma_f32_16x16x32_bf16 v[40:43], v[76:79], v[56:59], v[40:43]\n\t"
        "v_mfma_f32_16x16x32_bf16 v[44:47], v[74:77], v[56:59], v[44:47]\n\t"
        "s_nop 2\n\t"
        "v_cvt_pk_bf16_f32 v48, v24, v25\n\t"
        "v_cvt_pk_bf16_f32 v49, v26, v27\n\t"
        "v_cvt_pk_bf16_f32 v50, v28, v29\n\t"
        "v_cvt_pk_bf16_f32 v51, v30, v31\n\t"
        "ds_write_b128 v71, v[48:51]\n\t"
        "ds_read_b128 v[48:51], v72\n\t"
        "v_cvt_pk_bf16_f32 v52, v32, v33\n\t"
        "v_cvt_pk_bf16_f32 v53, v34, v35\n\t"
        "v_cvt_pk_bf16_f32 v54, v36, v37\n\t"
        "v_cvt_pk_bf16_f32 v55, v38, v39\n\t"
        "ds_write_b128 v71, v[52:55]\n\t"
        "ds_read_b128 v[52:55], v72\n\t"
        "ds_read_b32 v0, v2 offset:68\n\t"
        "ds_read_b128 v[8:11], v4 offset:42240\n\t"
        "ds_read_b128 v[12:15], v5 offset:42240\n\t"
        "ds_read_b128 v[16:19], v6 offset:42240\n\t"
        "ds_read_b128 v[20:23], v7 offset:42240\n\t"
        "s_waitcnt lgkmcnt(4)\n\t"
        "buffer_store_dwordx4 v[48:51], v68, s[60:63], s50 offen sc1\n\t"
        "buffer_store_dwordx4 v[52:55], v69, s[60:63], s50 offen sc1\n\t"
        "v_cmp_gt_u16 vcc, 4, v0\n\t"
        "s_cbranch_vccz .L7_t5\n"
        ".L7_w34:\n\t"
        "ds_read_b32 v0, v2 offset:68\n\t"
        "s_waitcnt lgkmcnt(0)\n\t"
        "v_cmp_gt_u16 vcc, 3, v0\n\t"
        "s_cbranch_vccz .L7_d35\n\t"
        "s_setprio 0\n\t"
        "s_sleep 1\n\t"
        "s_sub_i32 s83, s83, 1\n\t"
        "s_cmp_gt_i32 s83, 0\n\t"
        "s_cbranch_scc1 .L7_w34\n"
        ".L7_d35:\n\t"
        "s_setprio 2\n"
        ".L7_w36:\n\t"
        "ds_read_b32 v0, v2 offset:68\n\t"
        "ds_read_b128 v[8:11], v4 offset:42240\n\t"
        "ds_read_b128 v[12:15], v5 offset:42240\n\t"
        "ds_read_b128 v[16:19], v6 offset:42240\n\t"
        "ds_read_b128 v[20:23], v7 offset:42240\n\t"
        "s_waitcnt lgkmcnt(4)\n\t"
        "v_cmp_gt_u16 vcc, 4, v0\n\t"
        "s_cbranch_vccz .L7_d37\n\t"
        "s_waitcnt lgkmcnt(0)\n\t"
        "s_setprio 0\n\t"
        "s_sleep 0\n\t"
        "s_sub_i32 s83, s83, 1\n\t"
        "s_cmp_gt_i32 s83, 0\n\t"
        "s_cbranch_scc1 .L7_w36\n"
        ".L7_d37:\n\t"
        "s_setprio 2\n"
        ".L7_t5:\n\t"
        "s_waitcnt lgkmcnt(2)\n\t"
        "v_mfma_f32_16x16x32_bf16 v[40:43], v[96:99], v[8:11], v[40:43]\n\t"
        "v_mfma_f32_16x16x32_bf16 v[40:43], v[104:107], v[12:15], v[40:43]\n\t"
        "s_waitcnt lgkmcnt(0)\n\t"
        "v_mfma_f32_16x16x32_bf16 v[40:43], v[112:115], v[16:19], v[40:43]\n\t"
        "v_mfma_f32_16x16x32_bf16 v[40:43], v[120:123], v[20:23], v[40:43]\n\t"
        "v_mfma_f32_16x16x32_bf16 v[44:47], v[124:127], v[20:23], v[44:47]\n\t"
        "v_mfma_f32_16x16x32_bf16 v[44:47], v[116:119], v[16:19], v[44:47]\n\t"
        "v_mfma_f32_16x16x32_bf16 v[44:47], v[108:111], v[12:15], v[44:47]\n\t"
        "v_mfma_f32_16x16x32_bf16 v[44:47], v[100:103], v[8:11], v[44:47]\n\t"
        "s_nop 3\n\t"
        "v_cvt_pk_bf16_f32 v56, v40, v41\n\t"
        "v_cvt_pk_bf16_f32 v57, v42, v43\n\t"
        "s_nop 1\n\t"
        "v_cvt_pk_bf16_f32 v58, v44, v45\n\t"
        "v_cvt_pk_bf16_f32 v59, v46, v47\n\t"
        "ds_write_b128 v71, v[56:59]\n\t"
        "ds_read_b128 v[56:59], v72\n\t"
        "s_waitcnt lgkmcnt(0)\n\t"
        "buffer_store_dwordx4 v[56:59], v70, s[60:63], s50 offen sc1\n\t"
        "s_or_b32 s78, %3, s69\n\t"
        "s_cbranch_scc1 .L7_ng\n\t"
        "v_mov_b32 v0, s68\n\t"
        "buffer_store_dword v0, off, s[20:23], 0 sc1\n"
        ".L7_ng:\n\t"
        "s_endpgm\n"
        ".L7_G2:\n\t"
        "s_setprio 3\n\t"
        "v_lshlrev_b32 v5, 4, v1\n\t"
        "s_mul_i32 s82, s76, 13\n\t"
        "s_lshr_b32 s82, s82, 6\n\t"
        "s_mul_i32 s82, s82, 5\n\t"
        "s_sub_u32 s82, s76, s82\n\t"
        "s_waitcnt lgkmcnt(0)\n\t"
        "s_lshl2_add_u32 s78, s82, s69\n\t"
        "s_lshl_b32 s78, s78, 10\n\t"
        "s_add_u32 m0, s78, 46336\n\t"
        "s_add_u32 s82, s82, 1\n\t"
        "s_cmp_eq_u32 s82, 5\n\t"
        "buffer_load_dwordx4 v5, s[56:59], s78 offen lds\n\t"
        "s_cselect_b32 s82, 0, s82\n\t"
        "v_lshrrev_b32 v18, 3, v1\n\t"
        "v_lshlrev_b32 v18, 14, v18\n\t"
        "v_and_b32 v6, 0x70, v5\n\t"
        "s_lshl_b32 s78, s69, 19\n\t"
        "s_lshl_b32 s79, s71, 7\n\t"
        "s_add_u32 s78, s78, s79\n\t"
        "v_add3_u32 v18, v18, v6, s78\n\t"
        "s_mov_b32 s24, 1048576\n\t"
        "s_mov_b32 s25, 1179648\n\t"
        "s_mov_b32 s26, 1310720\n\t"
        "s_mov_b32 s27, 1441792\n\t"
        "v_and_b32 v6, 0xf0, v5\n\t"
        "s_lshl_b32 s78, s70, 8\n\t"
        "v_add_u32 v62, s78, v6\n\t"
        "v_mov_b32 v7, 113860\n\t"
        "s_movk_i32 s38, 196\n\t"
        "s_mov_b32 s39, 12845056\n\t"
        "s_lshl_b32 s33, s69, 3\n\t"
        "s_lshl_b32 s78, 1, s33\n\t"
        "v_mov_b32 v29, s78\n\t"
        "s_sub_u32 s32, s78, 1\n\t"
        "s_and_b32 s32, s32, 0x01010101\n\t"
        "s_mov_b32 s35, 0xffffff\n\t"
        "s_mov_b32 s40, 0x13050109\n\t"
        "s_mov_b32 s41, 23\n\t"
        "s_cmp_eq_u32 s69, 1\n\t"
        "s_cselect_b32 s40, 0x12040008, s40\n\t"
        "s_cselect_b32 s41, 22, s41\n\t"
        "s_cmp_eq_u32 s69, 2\n\t"
        "s_cselect_b32 s40, 0x100e0602, s40\n\t"
        "s_cselect_b32 s41, 20, s41\n\t"
        "s_cmp_eq_u32 s69, 3\n\t"
        "s_cselect_b32 s40, 0x110f0703, s40\n\t"
        "s_cselect_b32 s41, 21, s41\n\t"
        "s_mul_i32 s41, s41, 0x01010101\n\t"
        "s_bitcmp1_b32 s40, 3\n\t"
        "s_cselect_b32 s78, 0, 4\n\t"
        "v_mov_b32 v61, s78\n\t"
        "v_lshrrev_b32 v12, 4, v1\n\t"
        "s_bfe_u32 s78, s40, 0x80000\n\t"
        "s_lshl_b32 s78, s78, 2\n\t"
        "v_add_u32 v13, s78, v12\n\t"
        "v_lshlrev_b32 v24, 2, v13\n\t"
        "v_lshlrev_b32 v15, 4, v13\n\t"
        "v_and_b32 v15, 0xf0, v15\n\t"
        "v_xor_b32 v15, v15, v6\n\t"
        "v_lshl_add_u32 v56, v13, 8, v15\n\t"
        "s_bfe_u32 s78, s40, 0x80008\n\t"
        "s_lshl_b32 s78, s78, 2\n\t"
        "v_add_u32 v13, s78, v12\n\t"
        "v_lshlrev_b32 v25, 2, v13\n\t"
        "v_lshlrev_b32 v15, 4, v13\n\t"
        "v_and_b32 v15, 0xf0, v15\n\t"
        "v_xor_b32 v15, v15, v6\n\t"
        "v_lshl_add_u32 v57, v13, 8, v15\n\t"
        "s_bfe_u32 s78, s40, 0x80010\n\t"
        "s_lshl_b32 s78, s78, 2\n\t"
        "v_add_u32 v13, s78, v12\n\t"
        "v_lshlrev_b32 v26, 2, v13\n\t"
        "v_lshlrev_b32 v15, 4, v13\n\t"
        "v_and_b32 v15, 0xf0, v15\n\t"
        "v_xor_b32 v15, v15, v6\n\t"
        "v_lshl_add_u32 v58, v13, 8, v15\n\t"
        "v_lshlrev_b32 v15, 9, v61\n\t"
        "v_sub_u32 v58, v58, v15\n\t"
        "s_bfe_u32 s78, s40, 0x80018\n\t"
        "s_lshl_b32 s78, s78, 2\n\t"
        "v_add_u32 v13, s78, v12\n\t"
        "v_lshlrev_b32 v27, 2, v13\n\t"
        "v_lshlrev_b32 v15, 4, v13\n\t"
        "v_and_b32 v15, 0xf0, v15\n\t"
        "v_xor_b32 v15, v15, v6\n\t"
        "v_lshl_add_u32 v59, v13, 8, v15\n\t"
        "s_bfe_u32 s78, s41, 0x80000\n\t"
        "s_lshl_b32 s78, s78, 2\n\t"
        "v_add_u32 v13, s78, v12\n\t"
        "v_lshlrev_b32 v28, 2, v13\n\t"
        "v_lshlrev_b32 v15, 4, v13\n\t"
        "v_and_b32 v15, 0xf0, v15\n\t"
        "v_xor_b32 v15, v15, v6\n\t"
        "v_lshl_add_u32 v60, v13, 8, v15\n\t"
        "v_and_b32 v20, 28, v1\n\t"
        "v_lshlrev_b32 v20, 1, v20\n\t"
        "v_lshrrev_b64 v[12:13], v20, s[40:41]\n\t"
        "v_and_b32 v13, 0xff, v12\n\t"
        "v_and_b32 v12, 3, v1\n\t"
        "v_lshl_add_u32 v13, v13, 2, v12\n\t"
        "v_lshlrev_b32 v31, 2, v13\n\t"
        "s_and_b32 s78, s67, 0x10000\n\t"
        "s_or_b32 s78, s78, 1\n\t"
        "v_mov_b32 v30, s78\n\t"
        "s_mov_b32 s36, 0\n\t"
        "v_and_b32 v0, 48, v5\n\t"
        "s_mov_b32 s78, 0x0f0e\n\t"
        "s_mov_b32 s79, 0\n\t"
        "v_lshlrev_b64 v[8:9], v0, s[78:79]\n\t"
        "s_mov_b32 s78, 0x0b0a\n\t"
        "v_lshlrev_b64 v[12:13], v0, s[78:79]\n\t"
        "s_mov_b32 s78, 0xf0f0f0f0\n\t"
        "s_mov_b32 s79, s78\n\t"
        "v_cndmask_b32 v8, v8, v12, s[78:79]\n\t"
        "v_cndmask_b32 v9, v9, v13, s[78:79]\n\t"
        "v_xor_b32 v8, 0x0c0c0c0c, v8\n\t"
        "v_xor_b32 v9, 0x0c0c0c0c, v9\n\t"
        "s_movk_i32 s48, 0x200\n\t"
        "s_mov_b32 s37, 0x01010101\n\t"
        "s_mov_b32 s49, 0x05040100\n\t"
        "s_getpc_b64 s[50:51]\n"
        ".L7_pc:\n\t"
        "s_add_u32 s52, s50, .L7_ce-.L7_pc\n\t"
        "s_addc_u32 s53, s51, 0\n\t"
        "s_add_u32 s42, s50, .L7_ev-.L7_pc\n\t"
        "s_addc_u32 s43, s51, 0\n\t"
        "s_add_u32 s50, s50, .L7_ie-.L7_pc\n\t"
        "s_addc_u32 s51, s51, 0\n\t"
        "s_cmp_eq_u32 s80, 10\n\t"
        "s_cselect_b32 s80, 0, s80\n\t"
        "s_mul_i32 s78, s80, 0x1080\n\t"
        "s_add_u32 m0, s78, s81\n\t"
        "s_lshr_b32 s78, s80, 1\n\t"
        "s_lshl1_add_u32 s78, s78, s80\n\t"
        "s_lshl_b32 s78, s78, 11\n\t"
        "buffer_load_dwordx4 v4, s[16:19], s78 offen lds\n\t"
        "s_add_u32 s80, s80, 1\n\t"
        "s_mul_i32 s78, s80, 0x1080\n\t"
        "s_add_u32 m0, s78, s81\n\t"
        "s_lshr_b32 s78, s80, 1\n\t"
        "s_lshl1_add_u32 s78, s78, s80\n\t"
        "s_lshl_b32 s78, s78, 11\n\t"
        "buffer_load_dwordx4 v4, s[16:19], s78 offen lds\n\t"
        "s_add_u32 s80, s80, 1\n\t"
        "s_mul_i32 s78, s80, 0x1080\n\t"
        "s_add_u32 m0, s78, s81\n\t"
        "s_lshr_b32 s78, s80, 1\n\t"
        "s_lshl1_add_u32 s78, s78, s80\n\t"
        "s_lshl_b32 s78, s78, 11\n\t"
        "buffer_load_dwordx4 v4, s[16:19], s78 offen lds\n\t"
        "s_add_u32 s80, s80, 1\n\t"
        "s_mul_i32 s78, s80, 0x1080\n\t"
        "s_add_u32 m0, s78, s81\n\t"
        "s_lshr_b32 s78, s80, 1\n\t"
        "s_lshl1_add_u32 s78, s78, s80\n\t"
        "s_lshl_b32 s78, s78, 11\n\t"
        "buffer_load_dwordx4 v4, s[16:19], s78 offen lds\n\t"
        "s_add_u32 s80, s80, 1\n\t"
        "s_mul_i32 s78, s80, 0x1080\n\t"
        "s_add_u32 m0, s78, s81\n\t"
        "s_lshr_b32 s78, s80, 1\n\t"
        "s_lshl1_add_u32 s78, s78, s80\n\t"
        "s_lshl_b32 s78, s78, 11\n\t"
        "buffer_load_dwordx4 v4, s[16:19], s78 offen lds\n\t"
        "s_add_u32 s80, s80, 1\n\t"
        "s_lshl2_add_u32 s78, s82, s69\n\t"
        "s_lshl_b32 s78, s78, 10\n\t"
        "s_add_u32 m0, s78, 46336\n\t"
        "s_add_u32 s82, s82, 1\n\t"
        "s_cmp_eq_u32 s82, 5\n\t"
        "buffer_load_dwordx4 v5, s[56:59], s78 offen lds\n\t"
        "s_cselect_b32 s82, 0, s82\n\t"
        "s_lshl2_add_u32 s78, s82, s69\n\t"
        "s_lshl_b32 s78, s78, 10\n\t"
        "s_add_u32 m0, s78, 46336\n\t"
        "s_add_u32 s82, s82, 1\n\t"
        "s_cmp_eq_u32 s82, 5\n\t"
        "buffer_load_dwordx4 v5, s[56:59], s78 offen lds\n\t"
        "s_cselect_b32 s82, 0, s82\n\t"
        "s_lshl2_add_u32 s78, s82, s69\n\t"
        "s_lshl_b32 s78, s78, 10\n\t"
        "s_add_u32 m0, s78, 46336\n\t"
        "s_add_u32 s82, s82, 1\n\t"
        "s_cmp_eq_u32 s82, 5\n\t"
        "buffer_load_dwordx4 v5, s[56:59], s78 offen lds\n\t"
        "s_cselect_b32 s82, 0, s82\n\t"
        "s_lshl2_add_u32 s78, s82, s69\n\t"
        "s_lshl_b32 s78, s78, 10\n\t"
        "s_add_u32 m0, s78, 46336\n\t"
        "s_add_u32 s82, s82, 1\n\t"
        "s_cmp_eq_u32 s82, 5\n\t"
        "buffer_load_dwordx4 v5, s[56:59], s78 offen lds\n\t"
        "s_cselect_b32 s82, 0, s82\n\t"
        "s_waitcnt vmcnt(4)\n\t"
        "s_mov_b64 exec, 1\n\t"
        "ds_add_u32 v2, v3 offset:0\n\t"
        "s_mov_b64 exec, -1\n"
        ".L7_w72:\n\t"
        "ds_read_b64 v[12:13], v2 offset:72\n\t"
        "s_waitcnt lgkmcnt(0)\n\t"
        "v_cmp_gt_u16 vcc, 2, v12\n\t"
        "s_cbranch_vccz .L7_d73\n\t"
        "s_sleep 1\n\t"
        "s_sub_i32 s83, s83, 1\n\t"
        "s_cmp_gt_i32 s83, 0\n\t"
        "s_cbranch_scc1 .L7_w72\n"
        ".L7_d73:\n\t"
        "v_readfirstlane_b32 s68, v13\n"
        ".L7_w74:\n\t"
        "ds_read_b32 v0, v2 offset:8\n\t"
        "s_waitcnt lgkmcnt(0)\n\t"
        "v_cmp_gt_u16 vcc, 2, v0\n\t"
        "s_cbranch_vccz .L7_d75\n\t"
        "s_sleep 1\n\t"
        "s_sub_i32 s83, s83, 1\n\t"
        "s_cmp_gt_i32 s83, 0\n\t"
        "s_cbranch_scc1 .L7_w74\n"
        ".L7_d75:\n\t"
        "ds_read_b32 v5, v31 offset:512\n\t"
        "ds_read_b32 v24, v24 offset:512\n\t"
        "ds_read_b32 v25, v25 offset:512\n\t"
        "ds_read_b32 v26, v26 offset:512\n\t"
        "ds_read_b32 v27, v27 offset:512\n\t"
        "ds_read_b32 v28, v28 offset:512\n\t"
        "s_waitcnt lgkmcnt(0)\n\t"
        "v_dot4_u32_u8 v31, v5, s38, v7\n\t"
        "s_waitcnt vmcnt(0)\n\t"
        "s_mov_b64 exec, 1\n\t"
        "ds_add_u32 v2, v3 offset:4\n\t"
        "s_mov_b64 exec, -1\n\t"
        "s_setprio 3\n\t"
        "s_lshl_b32 s44, s68, 24\n\t"
        "s_xor_b32 s45, s44, 0x5a000000\n\t"
        "s_mul_i32 s46, s68, 0x01010000\n\t"
        "s_mul_i32 s47, s68, 0x4000400\n\t"
        "s_and_b32 s47, s47, 0xff000000\n\t"
        "s_or_b32 s47, s47, 0xff0000\n\t"
        "s_and_b32 s46, s46, 0xff000000\n\t"
        "s_mul_i32 s34, s32, 0xff\n\t"
        "s_lshl_b32 s78, 0xff, s33\n\t"
        "s_or_b32 s79, s78, s34\n\t"
        "s_andn2_b32 s79, 0x0c0c0c0c, s79\n\t"
        "s_and_b32 s78, s78, 0x03020100\n\t"
        "s_and_b32 s34, s34, 0x07060504\n\t"
        "s_or_b32 s34, s34, s78\n\t"
        "s_or_b32 s34, s34, s79\n"
        ".L7_w76:\n\t"
        "ds_read_b32 v0, v2 offset:92\n\t"
        "s_waitcnt lgkmcnt(0)\n\t"
        "v_cmp_gt_u16 vcc, 1, v0\n\t"
        "s_cbranch_vccz .L7_d77\n\t"
        "s_sleep 1\n\t"
        "s_sub_i32 s83, s83, 1\n\t"
        "s_cmp_gt_i32 s83, 0\n\t"
        "s_cbranch_scc1 .L7_w76\n"
        ".L7_d77:\n\t"
        "v_readfirstlane_b32 s78, v0\n\t"
        "s_sub_u32 s78, s78, 1\n\t"
        "s_add_u32 s77, s74, s78\n"
        ".L7_g78:\n\t"
        "s_memrealtime s[78:79]\n\t"
        "s_waitcnt lgkmcnt(0)\n\t"
        "s_sub_u32 s78, s78, s77\n\t"
        "s_cmp_ge_i32 s78, 305\n\t"
        "s_cbranch_scc1 .L7_h79\n\t"
        "s_sleep 1\n\t"
        "s_branch .L7_g78\n"
        ".L7_h79:\n\t"
        "buffer_load_dwordx4 v[64:67], v18, s[20:23], s24 offen sc1\n\t"
        "buffer_load_dwordx4 v[68:71], v18, s[20:23], s25 offen sc1\n\t"
        "buffer_load_dwordx4 v[72:75], v18, s[20:23], s26 offen sc1\n\t"
        "buffer_load_dwordx4 v[76:79], v18, s[20:23], s27 offen sc1\n"
        ".L7_w80:\n\t"
        "ds_read_b32 v0, v2 offset:4\n\t"
        "s_waitcnt lgkmcnt(0)\n\t"
        "v_cmp_gt_u16 vcc, 4, v0\n\t"
        "s_cbranch_vccz .L7_d81\n\t"
        "s_sleep 1\n\t"
        "s_sub_i32 s83, s83, 1\n\t"
        "s_cmp_gt_i32 s83, 0\n\t"
        "s_cbranch_scc1 .L7_w80\n"
        ".L7_d81:\n\t"
        "s_branch .L7_ll\n"
        ".L7_again:\n\t"
        "s_waitcnt lgkmcnt(0)\n\t"
        "s_sleep 8\n\t"
        "s_sub_i32 s83, s83, 16\n\t"
        "s_cmp_gt_i32 s83, 0\n\t"
        "s_cbranch_scc0 .L7_got\n\t"
        "buffer_load_dwordx4 v[64:67], v18, s[20:23], s24 offen sc1\n\t"
        "buffer_load_dwordx4 v[68:71], v18, s[20:23], s25 offen sc1\n\t"
        "buffer_load_dwordx4 v[72:75], v18, s[20:23], s26 offen sc1\n\t"
        "buffer_load_dwordx4 v[76:79], v18, s[20:23], s27 offen sc1\n"
        ".L7_ll:\n\t"
        "s_waitcnt vmcnt(3)\n\t"
        "v_xor_b32 v64, s44, v64\n\t"
        "v_xor_b32 v65, s45, v65\n\t"
        "v_xor_b32 v66, s46, v66\n\t"
        "v_xor_b32 v4, s47, v67\n\t"
        "v_or_b32 v5, v64, v65\n\t"
        "v_lshlrev_b32_sdwa v112, 1, v64 dst_sel:DWORD dst_unused:UNUSED_PAD src0_sel:DWORD src1_sel:WORD_0\n\t"
        "ds_read_u16 v112, v112 offset:46336\n\t"
        "v_lshlrev_b32_sdwa v113, 1, v65 dst_sel:DWORD dst_unused:UNUSED_PAD src0_sel:DWORD src1_sel:WORD_0\n\t"
        "ds_read_u16 v113, v113 offset:46336\n\t"
        "v_lshlrev_b32_sdwa v114, 1, v66 dst_sel:DWORD dst_unused:UNUSED_PAD src0_sel:DWORD src1_sel:WORD_0\n\t"
        "ds_read_u16 v114, v114 offset:46336\n\t"
        "v_mad_u32_u16 v80, v64, s38, v7 op_sel:[1,0,0,0]\n\t"
        "v_mad_u32_u16 v81, v65, s38, v7 op_sel:[1,0,0,0]\n\t"
        "v_mad_u32_u16 v82, v66, s38, v7 op_sel:[1,0,0,0]\n\t"
        "s_waitcnt vmcnt(2)\n\t"
        "v_xor_b32 v68, s44, v68\n\t"
        "v_xor_b32 v69, s45, v69\n\t"
        "v_xor_b32 v70, s46, v70\n\t"
        "v_bitop3_b32 v4, v71, v4, s47 bitop3:0xde\n\t"
        "v_or3_b32 v5, v5, v68, v69\n\t"
        "v_or3_b32 v5, v5, v66, v70\n\t"
        "v_lshlrev_b32_sdwa v116, 1, v68 dst_sel:DWORD dst_unused:UNUSED_PAD src0_sel:DWORD src1_sel:WORD_0\n\t"
        "ds_read_u16 v116, v116 offset:46336\n\t"
        "v_lshlrev_b32_sdwa v117, 1, v69 dst_sel:DWORD dst_unused:UNUSED_PAD src0_sel:DWORD src1_sel:WORD_0\n\t"
        "ds_read_u16 v117, v117 offset:46336\n\t"
        "v_lshlrev_b32_sdwa v118, 1, v70 dst_sel:DWORD dst_unused:UNUSED_PAD src0_sel:DWORD src1_sel:WORD_0\n\t"
        "ds_read_u16 v118, v118 offset:46336\n\t"
        "v_mad_u32_u16 v84, v68, s38, v7 op_sel:[1,0,0,0]\n\t"
        "v_mad_u32_u16 v85, v69, s38, v7 op_sel:[1,0,0,0]\n\t"
        "v_mad_u32_u16 v86, v70, s38, v7 op_sel:[1,0,0,0]\n\t"
        "s_waitcnt vmcnt(1)\n\t"
        "v_xor_b32 v72, s44, v72\n\t"
        "v_xor_b32 v73, s45, v73\n\t"
        "v_xor_b32 v74, s46, v74\n\t"
        "v_bitop3_b32 v4, v75, v4, s47 bitop3:0xde\n\t"
        "v_or3_b32 v5, v5, v72, v73\n\t"
        "v_lshlrev_b32_sdwa v120, 1, v72 dst_sel:DWORD dst_unused:UNUSED_PAD src0_sel:DWORD src1_sel:WORD_0\n\t"
        "ds_read_u16 v120, v120 offset:46336\n\t"
        "v_lshlrev_b32_sdwa v121, 1, v73 dst_sel:DWORD dst_unused:UNUSED_PAD src0_sel:DWORD src1_sel:WORD_0\n\t"
        "ds_read_u16 v121, v121 offset:46336\n\t"
        "v_lshlrev_b32_sdwa v122, 1, v74 dst_sel:DWORD dst_unused:UNUSED_PAD src0_sel:DWORD src1_sel:WORD_0\n\t"
        "ds_read_u16 v122, v122 offset:46336\n\t"
        "v_mad_u32_u16 v88, v72, s38, v7 op_sel:[1,0,0,0]\n\t"
        "v_mad_u32_u16 v89, v73, s38, v7 op_sel:[1,0,0,0]\n\t"
        "v_mad_u32_u16 v90, v74, s38, v7 op_sel:[1,0,0,0]\n\t"
        "s_waitcnt vmcnt(0)\n\t"
        "v_xor_b32 v76, s44, v76\n\t"
        "v_xor_b32 v77, s45, v77\n\t"
        "v_xor_b32 v78, s46, v78\n\t"
        "v_bitop3_b32 v4, v79, v4, s47 bitop3:0xde\n\t"
        "v_or3_b32 v5, v5, v76, v77\n\t"
        "v_or3_b32 v5, v5, v74, v78\n\t"
        "v_lshlrev_b32_sdwa v124, 1, v76 dst_sel:DWORD dst_unused:UNUSED_PAD src0_sel:DWORD src1_sel:WORD_0\n\t"
        "ds_read_u16 v124, v124 offset:46336\n\t"
        "v_lshlrev_b32_sdwa v125, 1, v77 dst_sel:DWORD dst_unused:UNUSED_PAD src0_sel:DWORD src1_sel:WORD_0\n\t"
        "ds_read_u16 v125, v125 offset:46336\n\t"
        "v_lshlrev_b32_sdwa v126, 1, v78 dst_sel:DWORD dst_unused:UNUSED_PAD src0_sel:DWORD src1_sel:WORD_0\n\t"
        "ds_read_u16 v126, v126 offset:46336\n\t"
        "v_mad_u32_u16 v92, v76, s38, v7 op_sel:[1,0,0,0]\n\t"
        "v_mad_u32_u16 v93, v77, s38, v7 op_sel:[1,0,0,0]\n\t"
        "v_mad_u32_u16 v94, v78, s38, v7 op_sel:[1,0,0,0]\n\t"
        "v_and_b32 v6, 0xfc000000, v4\n\t"
        "v_or_b32 v5, v5, v6\n\t"
        "v_cmp_lt_u32 vcc, s35, v5\n\t"
        "s_cbranch_vccnz .L7_again\n"
        ".L7_got:\n\t"
        "v_and_b32 v6, s35, v4\n\t"
        "s_waitcnt lgkmcnt(2)\n\t"
        "ds_add_rtn_u32 v96, v80, v29 offset:192\n\t"
        "ds_add_rtn_u32 v97, v81, v29 offset:192\n\t"
        "ds_add_rtn_u32 v98, v82, v29 offset:192\n\t"
        "ds_add_rtn_u32 v100, v84, v29 offset:192\n\t"
        "ds_add_rtn_u32 v101, v85, v29 offset:192\n\t"
        "ds_add_rtn_u32 v102, v86, v29 offset:192\n\t"
        "ds_add_rtn_u32 v104, v88, v29 offset:192\n\t"
        "ds_add_rtn_u32 v105, v89, v29 offset:192\n\t"
        "ds_add_rtn_u32 v106, v90, v29 offset:192\n\t"
        "ds_add_rtn_u32 v108, v92, v29 offset:192\n\t"
        "ds_add_rtn_u32 v109, v93, v29 offset:192\n\t"
        "ds_add_rtn_u32 v110, v94, v29 offset:192\n\t"
        "v_cmp_ne_u32 vcc, 0, v6\n\t"
        "s_cbranch_vccnz .L7_x\n"
        ".L7_y:\n\t"
        "s_mov_b64 exec, 1\n\t"
        "ds_add_u32 v2, v3 offset:32\n\t"
        "s_mov_b64 exec, -1\n\t"
        "v_perm_b32 v112, v112, v64, s49\n\t"
        "v_perm_b32 v113, v113, v65, s49\n\t"
        "v_perm_b32 v114, v114, v66, s49\n\t"
        "v_perm_b32 v116, v116, v68, s49\n\t"
        "v_perm_b32 v117, v117, v69, s49\n\t"
        "v_perm_b32 v118, v118, v70, s49\n\t"
        "v_perm_b32 v120, v120, v72, s49\n\t"
        "v_perm_b32 v121, v121, v73, s49\n\t"
        "v_perm_b32 v122, v122, v74, s49\n\t"
        "v_perm_b32 v124, v124, v76, s49\n\t"
        "s_cmp_lt_u32 s69, 1\n\t"
        "s_cbranch_scc0 .L7_nv\n\t"
        "s_mov_b32 s79, 0\n\t"
        "v_xor_b32 v32, s47, v67\n\t"
        "v_xor_b32 v33, s47, v71\n\t"
        "v_xor_b32 v34, s47, v75\n\t"
        "v_xor_b32 v35, s47, v79\n\t"
        "v_lshlrev_b32 v32, 6, v32\n\t"
        "v_lshlrev_b32 v33, 6, v33\n\t"
        "v_lshlrev_b32 v34, 6, v34\n\t"
        "v_lshlrev_b32 v35, 6, v35\n\t"
        "s_mov_b32 exec_lo, s37\n\t"
        "s_mov_b32 exec_hi, s37\n\t"
        "v_cmp_gt_i32 vcc, 0, v32\n\t"
        "s_bcnt1_i32_b64 s78, vcc\n\t"
        "s_add_u32 s79, s79, s78\n\t"
        "v_cmp_gt_i32 vcc, 0, v33\n\t"
        "s_bcnt1_i32_b64 s78, vcc\n\t"
        "s_add_u32 s79, s79, s78\n\t"
        "v_cmp_gt_i32 vcc, 0, v34\n\t"
        "s_bcnt1_i32_b64 s78, vcc\n\t"
        "s_add_u32 s79, s79, s78\n\t"
        "v_cmp_gt_i32 vcc, 0, v35\n\t"
        "s_bcnt1_i32_b64 s78, vcc\n\t"
        "s_add_u32 s79, s79, s78\n\t"
        "s_lshl_b32 s79, s79, 16\n\t"
        "s_or_b32 s79, s79, 1\n\t"
        "v_mov_b32 v32, s79\n\t"
        "s_waitcnt lgkmcnt(13)\n\t"
        "s_mov_b64 exec, 1\n\t"
        "ds_add_u32 v2, v32 offset:116\n\t"
        "s_mov_b64 exec, -1\n"
        ".L7_nv:\n\t"
        "v_lshlrev_b32 v4, 7, v4\n\t"
        "v_cmp_gt_i32 vcc, 0, v4\n\t"
        "s_cmp_lg_u64 vcc, 0\n\t"
        "s_cselect_b32 s78, 0x100000, 0\n\t"
        "v_or_b32 v30, s78, v30\n\t"
        "s_waitcnt lgkmcnt(13)\n\t"
        "v_perm_b32 v125, v125, v77, s49\n\t"
        "v_perm_b32 v126, v126, v78, s49\n"
        ".L7_w82:\n\t"
        "ds_read_b32 v0, v2 offset:32\n\t"
        "s_waitcnt lgkmcnt(0)\n\t"
        "v_cmp_gt_u16 vcc, 4, v0\n\t"
        "s_cbranch_vccz .L7_d83\n\t"
        "s_sleep 1\n\t"
        "s_sub_i32 s83, s83, 1\n\t"
        "s_cmp_gt_i32 s83, 0\n\t"
        "s_cbranch_scc1 .L7_w82\n"
        ".L7_d83:\n\t"
        "ds_read_b32 v32, v80 offset:192\n\t"
        "ds_read_b32 v33, v81 offset:192\n\t"
        "ds_read_b32 v34, v82 offset:192\n\t"
        "ds_read_b32 v35, v84 offset:192\n\t"
        "ds_read_b32 v36, v85 offset:192\n\t"
        "ds_read_b32 v37, v86 offset:192\n\t"
        "ds_read_b32 v38, v88 offset:192\n\t"
        "ds_read_b32 v39, v89 offset:192\n\t"
        "ds_read_b32 v40, v90 offset:192\n\t"
        "ds_read_b32 v41, v92 offset:192\n\t"
        "ds_read_b32 v42, v93 offset:192\n\t"
        "ds_read_b32 v43, v94 offset:192\n\t"
        "ds_read_b32 v6, v31 offset:192\n\t"
        "s_waitcnt lgkmcnt(10)\n\t"
        "v_perm_b32 v32, v32, v96, s34\n\t"
        "v_sad_u8 v96, v32, 0, 0\n\t"
        "v_perm_b32 v33, v33, v97, s34\n\t"
        "v_sad_u8 v97, v33, 0, 0\n\t"
        "v_perm_b32 v34, v34, v98, s34\n\t"
        "v_sad_u8 v98, v34, 0, 0\n\t"
        "s_waitcnt lgkmcnt(7)\n\t"
        "v_perm_b32 v35, v35, v100, s34\n\t"
        "v_sad_u8 v100, v35, 0, 0\n\t"
        "v_perm_b32 v36, v36, v101, s34\n\t"
        "v_sad_u8 v101, v36, 0, 0\n\t"
        "v_perm_b32 v37, v37, v102, s34\n\t"
        "v_sad_u8 v102, v37, 0, 0\n\t"
        "s_waitcnt lgkmcnt(4)\n\t"
        "v_perm_b32 v38, v38, v104, s34\n\t"
        "v_sad_u8 v104, v38, 0, 0\n\t"
        "v_perm_b32 v39, v39, v105, s34\n\t"
        "v_sad_u8 v105, v39, 0, 0\n\t"
        "v_perm_b32 v40, v40, v106, s34\n\t"
        "v_sad_u8 v106, v40, 0, 0\n\t"
        "s_waitcnt lgkmcnt(1)\n\t"
        "v_perm_b32 v41, v41, v108, s34\n\t"
        "v_sad_u8 v108, v41, 0, 0\n\t"
        "v_perm_b32 v42, v42, v109, s34\n\t"
        "v_sad_u8 v109, v42, 0, 0\n\t"
        "v_perm_b32 v43, v43, v110, s34\n\t"
        "v_sad_u8 v110, v43, 0, 0\n\t"
        "v_min_u32 v96, 47, v96\n\t"
        "v_min_u32 v97, 47, v97\n\t"
        "v_min_u32 v98, 47, v98\n\t"
        "v_min_u32 v100, 47, v100\n\t"
        "v_min_u32 v101, 47, v101\n\t"
        "v_min_u32 v102, 47, v102\n\t"
        "v_min_u32 v104, 47, v104\n\t"
        "v_min_u32 v105, 47, v105\n\t"
        "v_min_u32 v106, 47, v106\n\t"
        "v_min_u32 v108, 47, v108\n\t"
        "v_min_u32 v109, 47, v109\n\t"
        "v_min_u32 v110, 47, v110\n\t"
        "v_lshl_add_u32 v96, v96, 2, v80\n\t"
        "ds_write_b32 v96, v112\n\t"
        "v_lshl_add_u32 v97, v97, 2, v81\n\t"
        "ds_write_b32 v97, v113\n\t"
        "v_lshl_add_u32 v98, v98, 2, v82\n\t"
        "ds_write_b32 v98, v114\n\t"
        "v_lshl_add_u32 v100, v100, 2, v84\n\t"
        "ds_write_b32 v100, v116\n\t"
        "v_lshl_add_u32 v101, v101, 2, v85\n\t"
        "ds_write_b32 v101, v117\n\t"
        "v_lshl_add_u32 v102, v102, 2, v86\n\t"
        "ds_write_b32 v102, v118\n\t"
        "v_lshl_add_u32 v104, v104, 2, v88\n\t"
        "ds_write_b32 v104, v120\n\t"
        "v_lshl_add_u32 v105, v105, 2, v89\n\t"
        "ds_write_b32 v105, v121\n\t"
        "v_lshl_add_u32 v106, v106, 2, v90\n\t"
        "ds_write_b32 v106, v122\n\t"
        "v_lshl_add_u32 v108, v108, 2, v92\n\t"
        "ds_write_b32 v108, v124\n\t"
        "v_lshl_add_u32 v109, v109, 2, v93\n\t"
        "ds_write_b32 v109, v125\n\t"
        "v_lshl_add_u32 v110, v110, 2, v94\n\t"
        "ds_write_b32 v110, v126\n\t"
        "s_cmp_lg_u32 s36, 0\n\t"
        "s_cbranch_scc1 .L7_z\n"
        ".L7_zr:\n\t"
        "s_mov_b64 exec, 1\n\t"
        "ds_add_u32 v2, v30 offset:36\n\t"
        "s_mov_b64 exec, -1\n\t"
        "s_waitcnt lgkmcnt(13)\n\t"
        "v_sad_u8 v6, v6, 0, -1\n\t"
        "v_mov_b64 v[32:33], 0\n\t"
        "v_mov_b64 v[34:35], 0\n\t"
        "v_mov_b64 v[36:37], 0\n\t"
        "v_med3_i32 v6, v6, 0, 47\n\t"
        "v_lshrrev_b32 v6, 1, v6\n\t"
        "v_mov_b64 v[38:39], 0\n\t"
        "v_and_b32 v63, 0xff, v24\n\t"
        "v_mad_u32_u24 v63, v63, s38, v7\n\t"
        "v_max_u32_dpp v6, v6, v6 quad_perm:[1,0,3,2] row_mask:0xf bank_mask:0xf\n\t"
        "s_nop 1\n\t"
        "v_max_u32_dpp v6, v6, v6 quad_perm:[2,3,0,1] row_mask:0xf bank_mask:0xf\n\t"
        "s_nop 0\n\t"
        "v_readlane_b32 s24, v6, 0\n\t"
        "v_readlane_b32 s25, v6, 4\n\t"
        "v_readlane_b32 s26, v6, 8\n\t"
        "v_readlane_b32 s27, v6, 12\n\t"
        "v_readlane_b32 s28, v6, 16\n\t"
        "s_max_u32 s28, s28, 5\n\t"
        "s_mov_b32 s56, s24\n\t"
        "s_mov_b32 s57, s24\n\t"
        "s_mov_b32 s78, 0xf0000\n"
        ".L7_w84:\n\t"
        "ds_read_b32 v4, v2 offset:36\n\t"
        "ds_read_b32 v12, v31 offset:188\n\t"
        "s_waitcnt lgkmcnt(1)\n\t"
        "v_cmp_gt_u16 vcc, 4, v4\n\t"
        "s_cbranch_vccz .L7_d85\n\t"
        "s_waitcnt lgkmcnt(0)\n\t"
        "s_sleep 1\n\t"
        "s_sub_i32 s83, s83, 1\n\t"
        "s_cmp_gt_i32 s83, 0\n\t"
        "s_cbranch_scc1 .L7_w84\n"
        ".L7_d85:\n\t"
        "ds_read2_b32 v[40:41], v63 offset0:0 offset1:1\n\t"
        "s_sub_u32 s56, s56, 1\n\t"
        "s_cbranch_scc1 .L7_i86\n"
        ".L7_j87:\n\t"
        "ds_read2_b32 v[42:43], v63 offset0:2 offset1:3\n\t"
        "s_sub_u32 s56, s56, 1\n\t"
        "s_cbranch_scc1 .L7_i88\n"
        ".L7_j89:\n\t"
        "ds_read2_b32 v[44:45], v63 offset0:4 offset1:5\n\t"
        "s_sub_u32 s56, s56, 1\n\t"
        "s_cbranch_scc1 .L7_i90\n"
        ".L7_j91:\n\t"
        "ds_read2_b32 v[46:47], v63 offset0:6 offset1:7\n\t"
        "s_sub_u32 s56, s56, 1\n\t"
        "s_cbranch_scc1 .L7_i92\n"
        ".L7_j93:\n\t"
        "ds_read2_b32 v[48:49], v63 offset0:8 offset1:9\n\t"
        "s_sub_u32 s56, s56, 1\n\t"
        "s_cbranch_scc1 .L7_i94\n"
        ".L7_j95:\n\t"
        "ds_read2_b32 v[50:51], v63 offset0:10 offset1:11\n\t"
        "s_sub_u32 s56, s56, 1\n\t"
        "s_cbranch_scc1 .L7_i96\n"
        ".L7_j97:\n\t"
        "ds_read2_b32 v[52:53], v63 offset0:12 offset1:13\n\t"
        "s_sub_u32 s56, s56, 1\n\t"
        "s_cbranch_scc1 .L7_i98\n"
        ".L7_j99:\n\t"
        "ds_read2_b32 v[54:55], v63 offset0:14 offset1:15\n\t"
        "s_sub_u32 s56, s56, 1\n\t"
        "s_cbranch_scc1 .L7_i100\n"
        ".L7_j101:\n\t"
        "s_waitcnt lgkmcnt(8)\n\t"
        "v_and_or_b32 v12, v4, s78, v12\n\t"
        "v_cmp_ne_u32 vcc, 0xffff, v12\n\t"
        "s_cbranch_vccnz .L7_exit4\n\t"
        "s_waitcnt lgkmcnt(7)\n\t"
        "v_mad_u32_u16 v10, v40, s48, v62\n\t"
        "v_mad_u32_u16 v11, v41, s48, v62\n\t"
        "buffer_load_dwordx4 v[64:67], v10, s[16:19], 0 offen\n\t"
        "buffer_load_dwordx4 v[68:71], v11, s[16:19], 0 offen\n\t"
        "s_waitcnt lgkmcnt(6)\n\t"
        "v_mad_u32_u16 v10, v42, s48, v62\n\t"
        "v_mad_u32_u16 v11, v43, s48, v62\n\t"
        "buffer_load_dwordx4 v[72:75], v10, s[16:19], 0 offen\n\t"
        "buffer_load_dwordx4 v[76:79], v11, s[16:19], 0 offen\n\t"
        "s_waitcnt lgkmcnt(5)\n\t"
        "v_mad_u32_u16 v10, v44, s48, v62\n\t"
        "v_mad_u32_u16 v11, v45, s48, v62\n\t"
        "buffer_load_dwordx4 v[80:83], v10, s[16:19], 0 offen\n\t"
        "buffer_load_dwordx4 v[84:87], v11, s[16:19], 0 offen\n\t"
        "s_waitcnt lgkmcnt(4)\n\t"
        "v_mad_u32_u16 v10, v46, s48, v62\n\t"
        "v_mad_u32_u16 v11, v47, s48, v62\n\t"
        "buffer_load_dwordx4 v[88:91], v10, s[16:19], 0 offen\n\t"
        "buffer_load_dwordx4 v[92:95], v11, s[16:19], 0 offen\n\t"
        "s_waitcnt lgkmcnt(3)\n\t"
        "v_mad_u32_u16 v10, v48, s48, v62\n\t"
        "v_mad_u32_u16 v11, v49, s48, v62\n\t"
        "buffer_load_dwordx4 v[96:99], v10, s[16:19], 0 offen\n\t"
        "buffer_load_dwordx4 v[100:103], v11, s[16:19], 0 offen\n\t"
        "s_waitcnt lgkmcnt(2)\n\t"
        "v_mad_u32_u16 v10, v50, s48, v62\n\t"
        "v_mad_u32_u16 v11, v51, s48, v62\n\t"
        "buffer_load_dwordx4 v[104:107], v10, s[16:19], 0 offen\n\t"
        "buffer_load_dwordx4 v[108:111], v11, s[16:19], 0 offen\n\t"
        "s_min_u32 s59, s56, s57\n\t"
        "s_add_u32 s58, s59, 1\n\t"
        "s_setprio 2\n"
        ".L7_loop:\n\t"
        "v_add_u32 v63, 64, v63\n\t"
        "v_perm_b32 v12, v41, v40, v8\n\t"
        "v_perm_b32 v13, v41, v40, v9\n\t"
        "s_waitcnt vmcnt(10) lgkmcnt(1)\n\t"
        "v_mad_u32_u16 v10, v52, s48, v62\n\t"
        "v_mad_u32_u16 v11, v53, s48, v62\n\t"
        "buffer_load_dwordx4 v[112:115], v10, s[16:19], 0 offen\n\t"
        "buffer_load_dwordx4 v[116:119], v11, s[16:19], 0 offen\n\t"
        "v_mfma_f32_4x4x4_16b_bf16 v[32:35], v[12:13], v[64:65], v[32:35] cbsz:1 abid:0\n\t"
        "v_mfma_f32_4x4x4_16b_bf16 v[36:39], v[12:13], v[66:67], v[36:39] cbsz:1 abid:0\n\t"
        "ds_read2_b32 v[40:41], v63 offset0:0 offset1:1\n\t"
        "v_mfma_f32_4x4x4_16b_bf16 v[32:35], v[12:13], v[68:69], v[32:35] cbsz:1 abid:1\n\t"
        "v_mfma_f32_4x4x4_16b_bf16 v[36:39], v[12:13], v[70:71], v[36:39] cbsz:1 abid:1\n\t"
        "s_sub_u32 s59, s59, 1\n\t"
        "s_cbranch_scc1 .L7_i102\n"
        ".L7_j103:\n\t"
        "v_perm_b32 v12, v43, v42, v8\n\t"
        "v_perm_b32 v13, v43, v42, v9\n\t"
        "s_waitcnt vmcnt(10) lgkmcnt(1)\n\t"
        "v_mad_u32_u16 v10, v54, s48, v62\n\t"
        "v_mad_u32_u16 v11, v55, s48, v62\n\t"
        "buffer_load_dwordx4 v[120:123], v10, s[16:19], 0 offen\n\t"
        "buffer_load_dwordx4 v[124:127], v11, s[16:19], 0 offen\n\t"
        "v_mfma_f32_4x4x4_16b_bf16 v[32:35], v[12:13], v[72:73], v[32:35] cbsz:1 abid:0\n\t"
        "v_mfma_f32_4x4x4_16b_bf16 v[36:39], v[12:13], v[74:75], v[36:39] cbsz:1 abid:0\n\t"
        "ds_read2_b32 v[42:43], v63 offset0:2 offset1:3\n\t"
        "v_mfma_f32_4x4x4_16b_bf16 v[32:35], v[12:13], v[76:77], v[32:35] cbsz:1 abid:1\n\t"
        "v_mfma_f32_4x4x4_16b_bf16 v[36:39], v[12:13], v[78:79], v[36:39] cbsz:1 abid:1\n\t"
        "s_sub_u32 s59, s59, 1\n\t"
        "s_cbranch_scc1 .L7_i104\n"
        ".L7_j105:\n\t"
        "v_perm_b32 v12, v45, v44, v8\n\t"
        "v_perm_b32 v13, v45, v44, v9\n\t"
        "s_waitcnt vmcnt(10) lgkmcnt(1)\n\t"
        "v_mad_u32_u16 v10, v40, s48, v62\n\t"
        "v_mad_u32_u16 v11, v41, s48, v62\n\t"
        "buffer_load_dwordx4 v[64:67], v10, s[16:19], 0 offen\n\t"
        "buffer_load_dwordx4 v[68:71], v11, s[16:19], 0 offen\n\t"
        "v_mfma_f32_4x4x4_16b_bf16 v[32:35], v[12:13], v[80:81], v[32:35] cbsz:1 abid:0\n\t"
        "v_mfma_f32_4x4x4_16b_bf16 v[36:39], v[12:13], v[82:83], v[36:39] cbsz:1 abid:0\n\t"
        "ds_read2_b32 v[44:45], v63 offset0:4 offset1:5\n\t"
        "v_mfma_f32_4x4x4_16b_bf16 v[32:35], v[12:13], v[84:85], v[32:35] cbsz:1 abid:1\n\t"
        "v_mfma_f32_4x4x4_16b_bf16 v[36:39], v[12:13], v[86:87], v[36:39] cbsz:1 abid:1\n\t"
        "s_sub_u32 s59, s59, 1\n\t"
        "s_cbranch_scc1 .L7_i106\n"
        ".L7_j107:\n\t"
        "v_perm_b32 v12, v47, v46, v8\n\t"
        "v_perm_b32 v13, v47, v46, v9\n\t"
        "s_waitcnt vmcnt(10) lgkmcnt(1)\n\t"
        "v_mad_u32_u16 v10, v42, s48, v62\n\t"
        "v_mad_u32_u16 v11, v43, s48, v62\n\t"
        "buffer_load_dwordx4 v[72:75], v10, s[16:19], 0 offen\n\t"
        "buffer_load_dwordx4 v[76:79], v11, s[16:19], 0 offen\n\t"
        "v_mfma_f32_4x4x4_16b_bf16 v[32:35], v[12:13], v[88:89], v[32:35] cbsz:1 abid:0\n\t"
        "v_mfma_f32_4x4x4_16b_bf16 v[36:39], v[12:13], v[90:91], v[36:39] cbsz:1 abid:0\n\t"
        "ds_read2_b32 v[46:47], v63 offset0:6 offset1:7\n\t"
        "v_mfma_f32_4x4x4_16b_bf16 v[32:35], v[12:13], v[92:93], v[32:35] cbsz:1 abid:1\n\t"
        "v_mfma_f32_4x4x4_16b_bf16 v[36:39], v[12:13], v[94:95], v[36:39] cbsz:1 abid:1\n\t"
        "s_sub_u32 s59, s59, 1\n\t"
        "s_cbranch_scc1 .L7_i108\n"
        ".L7_j109:\n\t"
        "v_perm_b32 v12, v49, v48, v8\n\t"
        "v_perm_b32 v13, v49, v48, v9\n\t"
        "s_waitcnt vmcnt(10) lgkmcnt(1)\n\t"
        "v_mad_u32_u16 v10, v44, s48, v62\n\t"
        "v_mad_u32_u16 v11, v45, s48, v62\n\t"
        "buffer_load_dwordx4 v[80:83], v10, s[16:19], 0 offen\n\t"
        "buffer_load_dwordx4 v[84:87], v11, s[16:19], 0 offen\n\t"
        "v_mfma_f32_4x4x4_16b_bf16 v[32:35], v[12:13], v[96:97], v[32:35] cbsz:1 abid:0\n\t"
        "v_mfma_f32_4x4x4_16b_bf16 v[36:39], v[12:13], v[98:99], v[36:39] cbsz:1 abid:0\n\t"
        "ds_read2_b32 v[48:49], v63 offset0:8 offset1:9\n\t"
        "v_mfma_f32_4x4x4_16b_bf16 v[32:35], v[12:13], v[100:101], v[32:35] cbsz:1 abid:1\n\t"
        "v_mfma_f32_4x4x4_16b_bf16 v[36:39], v[12:13], v[102:103], v[36:39] cbsz:1 abid:1\n\t"
        "s_sub_u32 s59, s59, 1\n\t"
        "s_cbranch_scc1 .L7_i110\n"
        ".L7_j111:\n\t"
        "v_perm_b32 v12, v51, v50, v8\n\t"
        "v_perm_b32 v13, v51, v50, v9\n\t"
        "s_waitcnt vmcnt(10) lgkmcnt(1)\n\t"
        "v_mad_u32_u16 v10, v46, s48, v62\n\t"
        "v_mad_u32_u16 v11, v47, s48, v62\n\t"
        "buffer_load_dwordx4 v[88:91], v10, s[16:19], 0 offen\n\t"
        "buffer_load_dwordx4 v[92:95], v11, s[16:19], 0 offen\n\t"
        "v_mfma_f32_4x4x4_16b_bf16 v[32:35], v[12:13], v[104:105], v[32:35] cbsz:1 abid:0\n\t"
        "v_mfma_f32_4x4x4_16b_bf16 v[36:39], v[12:13], v[106:107], v[36:39] cbsz:1 abid:0\n\t"
        "ds_read2_b32 v[50:51], v63 offset0:10 offset1:11\n\t"
        "v_mfma_f32_4x4x4_16b_bf16 v[32:35], v[12:13], v[108:109], v[32:35] cbsz:1 abid:1\n\t"
        "v_mfma_f32_4x4x4_16b_bf16 v[36:39], v[12:13], v[110:111], v[36:39] cbsz:1 abid:1\n\t"
        "s_sub_u32 s59, s59, 1\n\t"
        "s_cbranch_scc1 .L7_i112\n"
        ".L7_j113:\n\t"
        "v_perm_b32 v12, v53, v52, v8\n\t"
        "v_perm_b32 v13, v53, v52, v9\n\t"
        "s_waitcnt vmcnt(10) lgkmcnt(1)\n\t"
        "v_mad_u32_u16 v10, v48, s48, v62\n\t"
        "v_mad_u32_u16 v11, v49, s48, v62\n\t"
        "buffer_load_dwordx4 v[96:99], v10, s[16:19], 0 offen\n\t"
        "buffer_load_dwordx4 v[100:103], v11, s[16:19], 0 offen\n\t"
        "v_mfma_f32_4x4x4_16b_bf16 v[32:35], v[12:13], v[112:113], v[32:35] cbsz:1 abid:0\n\t"
        "v_mfma_f32_4x4x4_16b_bf16 v[36:39], v[12:13], v[114:115], v[36:39] cbsz:1 abid:0\n\t"
        "ds_read2_b32 v[52:53], v63 offset0:12 offset1:13\n\t"
        "v_mfma_f32_4x4x4_16b_bf16 v[32:35], v[12:13], v[116:117], v[32:35] cbsz:1 abid:1\n\t"
        "v_mfma_f32_4x4x4_16b_bf16 v[36:39], v[12:13], v[118:119], v[36:39] cbsz:1 abid:1\n\t"
        "s_sub_u32 s59, s59, 1\n\t"
        "s_cbranch_scc1 .L7_i114\n"
        ".L7_j115:\n\t"
        "v_perm_b32 v12, v55, v54, v8\n\t"
        "v_perm_b32 v13, v55, v54, v9\n\t"
        "s_waitcnt vmcnt(10) lgkmcnt(1)\n\t"
        "v_mad_u32_u16 v10, v50, s48, v62\n\t"
        "v_mad_u32_u16 v11, v51, s48, v62\n\t"
        "buffer_load_dwordx4 v[104:107], v10, s[16:19], 0 offen\n\t"
        "buffer_load_dwordx4 v[108:111], v11, s[16:19], 0 offen\n\t"
        "v_mfma_f32_4x4x4_16b_bf16 v[32:35], v[12:13], v[120:121], v[32:35] cbsz:1 abid:0\n\t"
        "v_mfma_f32_4x4x4_16b_bf16 v[36:39], v[12:13], v[122:123], v[36:39] cbsz:1 abid:0\n\t"
        "ds_read2_b32 v[54:55], v63 offset0:14 offset1:15\n\t"
        "v_mfma_f32_4x4x4_16b_bf16 v[32:35], v[12:13], v[124:125], v[32:35] cbsz:1 abid:1\n\t"
        "v_mfma_f32_4x4x4_16b_bf16 v[36:39], v[12:13], v[126:127], v[36:39] cbsz:1 abid:1\n\t"
        "s_sub_u32 s59, s59, 1\n\t"
        "s_cbranch_scc0 .L7_loop\n\t"
        "s_branch .L7_i116\n"
        ".L7_i86:\n\t"
        "s_mov_b32 s60, 8\n\t"
        "s_swappc_b64 s[54:55], s[50:51]\n\t"
        "s_branch .L7_j87\n"
        ".L7_i88:\n\t"
        "s_mov_b32 s60, 16\n\t"
        "s_swappc_b64 s[54:55], s[50:51]\n\t"
        "s_branch .L7_j89\n"
        ".L7_i90:\n\t"
        "s_mov_b32 s60, 24\n\t"
        "s_swappc_b64 s[54:55], s[50:51]\n\t"
        "s_branch .L7_j91\n"
        ".L7_i92:\n\t"
        "s_mov_b32 s60, 32\n\t"
        "s_swappc_b64 s[54:55], s[50:51]\n\t"
        "s_branch .L7_j93\n"
        ".L7_i94:\n\t"
        "s_mov_b32 s60, 40\n\t"
        "s_swappc_b64 s[54:55], s[50:51]\n\t"
        "s_branch .L7_j95\n"
        ".L7_i96:\n\t"
        "s_mov_b32 s60, 48\n\t"
        "s_swappc_b64 s[54:55], s[50:51]\n\t"
        "s_branch .L7_j97\n"
        ".L7_i98:\n\t"
        "s_mov_b32 s60, 56\n\t"
        "s_swappc_b64 s[54:55], s[50:51]\n\t"
        "s_branch .L7_j99\n"
        ".L7_i100:\n\t"
        "s_mov_b32 s60, 64\n\t"
        "s_swappc_b64 s[54:55], s[50:51]\n\t"
        "s_branch .L7_j101\n"
        ".L7_i102:\n\t"
        "s_mov_b32 s60, 8\n\t"
        "s_swappc_b64 s[62:63], s[42:43]\n\t"
        "s_cbranch_vccnz .L7_dt1\n\t"
        "s_branch .L7_j103\n"
        ".L7_i104:\n\t"
        "s_mov_b32 s60, 16\n\t"
        "s_swappc_b64 s[62:63], s[42:43]\n\t"
        "s_cbranch_vccnz .L7_dt2\n\t"
        "s_branch .L7_j105\n"
        ".L7_i106:\n\t"
        "s_mov_b32 s60, 24\n\t"
        "s_swappc_b64 s[62:63], s[42:43]\n\t"
        "s_cbranch_vccnz .L7_dt3\n\t"
        "s_branch .L7_j107\n"
        ".L7_i108:\n\t"
        "s_mov_b32 s60, 32\n\t"
        "s_swappc_b64 s[62:63], s[42:43]\n\t"
        "s_cbranch_vccnz .L7_dt4\n\t"
        "s_branch .L7_j109\n"
        ".L7_i110:\n\t"
        "s_mov_b32 s60, 40\n\t"
        "s_swappc_b64 s[62:63], s[42:43]\n\t"
        "s_cbranch_vccnz .L7_dt5\n\t"
        "s_branch .L7_j111\n"
        ".L7_i112:\n\t"
        "s_mov_b32 s60, 48\n\t"
        "s_swappc_b64 s[62:63], s[42:43]\n\t"
        "s_cbranch_vccnz .L7_dt6\n\t"
        "s_branch .L7_j113\n"
        ".L7_i114:\n\t"
        "s_mov_b32 s60, 56\n\t"
        "s_swappc_b64 s[62:63], s[42:43]\n\t"
        "s_cbranch_vccnz .L7_dt7\n\t"
        "s_branch .L7_j115\n"
        ".L7_i116:\n\t"
        "s_mov_b32 s60, 64\n\t"
        "s_swappc_b64 s[62:63], s[42:43]\n\t"
        "s_cbranch_vccnz .L7_dt0\n\t"
        "s_branch .L7_loop\n"
        ".L7_ev:\n\t"
        "s_sub_u32 s56, s56, s58\n\t"
        "s_cbranch_scc0 .L7_e1\n\t"
        "s_swappc_b64 s[54:55], s[50:51]\n"
        ".L7_e1:\n\t"
        "s_sub_u32 s57, s57, s58\n\t"
        "s_cbranch_scc0 .L7_e2\n\t"
        "s_swappc_b64 s[54:55], s[52:53]\n"
        ".L7_e2:\n\t"
        "s_min_u32 s59, s56, s57\n\t"
        "s_add_u32 s58, s59, 1\n\t"
        "s_setpc_b64 s[62:63]\n"
        ".L7_dt0:\n\t"
        "v_perm_b32 v12, v41, v40, v8\n\t"
        "v_perm_b32 v13, v41, v40, v9\n\t"
        "s_waitcnt vmcnt(6)\n\t"
        "s_cmp_lt_u32 s57, 3\n\t"
        "s_cbranch_scc0 .L7_du0\n\t"
        "s_waitcnt vmcnt(2)\n\t"
        "s_cmp_eq_u32 s57, 0\n\t"
        "s_cbranch_scc0 .L7_du0\n\t"
        "s_waitcnt vmcnt(0)\n"
        ".L7_du0:\n\t"
        "v_mfma_f32_4x4x4_16b_bf16 v[32:35], v[12:13], v[64:65], v[32:35] cbsz:1 abid:0\n\t"
        "v_mfma_f32_4x4x4_16b_bf16 v[36:39], v[12:13], v[66:67], v[36:39] cbsz:1 abid:0\n\t"
        "s_sub_u32 s57, s57, 1\n\t"
        "v_mfma_f32_4x4x4_16b_bf16 v[32:35], v[12:13], v[68:69], v[32:35] cbsz:1 abid:1\n\t"
        "v_mfma_f32_4x4x4_16b_bf16 v[36:39], v[12:13], v[70:71], v[36:39] cbsz:1 abid:1\n\t"
        "s_cbranch_scc1 .L7_last\n"
        ".L7_dt1:\n\t"
        "v_perm_b32 v12, v43, v42, v8\n\t"
        "v_perm_b32 v13, v43, v42, v9\n\t"
        "s_waitcnt vmcnt(6)\n\t"
        "s_cmp_lt_u32 s57, 3\n\t"
        "s_cbranch_scc0 .L7_du1\n\t"
        "s_waitcnt vmcnt(2)\n\t"
        "s_cmp_eq_u32 s57, 0\n\t"
        "s_cbranch_scc0 .L7_du1\n\t"
        "s_waitcnt vmcnt(0)\n"
        ".L7_du1:\n\t"
        "v_mfma_f32_4x4x4_16b_bf16 v[32:35], v[12:13], v[72:73], v[32:35] cbsz:1 abid:0\n\t"
        "v_mfma_f32_4x4x4_16b_bf16 v[36:39], v[12:13], v[74:75], v[36:39] cbsz:1 abid:0\n\t"
        "s_sub_u32 s57, s57, 1\n\t"
        "v_mfma_f32_4x4x4_16b_bf16 v[32:35], v[12:13], v[76:77], v[32:35] cbsz:1 abid:1\n\t"
        "v_mfma_f32_4x4x4_16b_bf16 v[36:39], v[12:13], v[78:79], v[36:39] cbsz:1 abid:1\n\t"
        "s_cbranch_scc1 .L7_last\n"
        ".L7_dt2:\n\t"
        "v_perm_b32 v12, v45, v44, v8\n\t"
        "v_perm_b32 v13, v45, v44, v9\n\t"
        "s_waitcnt vmcnt(6)\n\t"
        "s_cmp_lt_u32 s57, 3\n\t"
        "s_cbranch_scc0 .L7_du2\n\t"
        "s_waitcnt vmcnt(2)\n\t"
        "s_cmp_eq_u32 s57, 0\n\t"
        "s_cbranch_scc0 .L7_du2\n\t"
        "s_waitcnt vmcnt(0)\n"
        ".L7_du2:\n\t"
        "v_mfma_f32_4x4x4_16b_bf16 v[32:35], v[12:13], v[80:81], v[32:35] cbsz:1 abid:0\n\t"
        "v_mfma_f32_4x4x4_16b_bf16 v[36:39], v[12:13], v[82:83], v[36:39] cbsz:1 abid:0\n\t"
        "s_sub_u32 s57, s57, 1\n\t"
        "v_mfma_f32_4x4x4_16b_bf16 v[32:35], v[12:13], v[84:85], v[32:35] cbsz:1 abid:1\n\t"
        "v_mfma_f32_4x4x4_16b_bf16 v[36:39], v[12:13], v[86:87], v[36:39] cbsz:1 abid:1\n\t"
        "s_cbranch_scc1 .L7_last\n"
        ".L7_dt3:\n\t"
        "v_perm_b32 v12, v47, v46, v8\n\t"
        "v_perm_b32 v13, v47, v46, v9\n\t"
        "s_waitcnt vmcnt(6)\n\t"
        "s_cmp_lt_u32 s57, 3\n\t"
        "s_cbranch_scc0 .L7_du3\n\t"
        "s_waitcnt vmcnt(2)\n\t"
        "s_cmp_eq_u32 s57, 0\n\t"
        "s_cbranch_scc0 .L7_du3\n\t"
        "s_waitcnt vmcnt(0)\n"
        ".L7_du3:\n\t"
        "v_mfma_f32_4x4x4_16b_bf16 v[32:35], v[12:13], v[88:89], v[32:35] cbsz:1 abid:0\n\t"
        "v_mfma_f32_4x4x4_16b_bf16 v[36:39], v[12:13], v[90:91], v[36:39] cbsz:1 abid:0\n\t"
        "s_sub_u32 s57, s57, 1\n\t"
        "v_mfma_f32_4x4x4_16b_bf16 v[32:35], v[12:13], v[92:93], v[32:35] cbsz:1 abid:1\n\t"
        "v_mfma_f32_4x4x4_16b_bf16 v[36:39], v[12:13], v[94:95], v[36:39] cbsz:1 abid:1\n\t"
        "s_cbranch_scc1 .L7_last\n"
        ".L7_dt4:\n\t"
        "v_perm_b32 v12, v49, v48, v8\n\t"
        "v_perm_b32 v13, v49, v48, v9\n\t"
        "s_waitcnt vmcnt(6)\n\t"
        "s_cmp_lt_u32 s57, 3\n\t"
        "s_cbranch_scc0 .L7_du4\n\t"
        "s_waitcnt vmcnt(2)\n\t"
        "s_cmp_eq_u32 s57, 0\n\t"
        "s_cbranch_scc0 .L7_du4\n\t"
        "s_waitcnt vmcnt(0)\n"
        ".L7_du4:\n\t"
        "v_mfma_f32_4x4x4_16b_bf16 v[32:35], v[12:13], v[96:97], v[32:35] cbsz:1 abid:0\n\t"
        "v_mfma_f32_4x4x4_16b_bf16 v[36:39], v[12:13], v[98:99], v[36:39] cbsz:1 abid:0\n\t"
        "s_sub_u32 s57, s57, 1\n\t"
        "v_mfma_f32_4x4x4_16b_bf16 v[32:35], v[12:13], v[100:101], v[32:35] cbsz:1 abid:1\n\t"
        "v_mfma_f32_4x4x4_16b_bf16 v[36:39], v[12:13], v[102:103], v[36:39] cbsz:1 abid:1\n\t"
        "s_cbranch_scc1 .L7_last\n"
        ".L7_dt5:\n\t"
        "v_perm_b32 v12, v51, v50, v8\n\t"
        "v_perm_b32 v13, v51, v50, v9\n\t"
        "s_waitcnt vmcnt(6)\n\t"
        "s_cmp_lt_u32 s57, 3\n\t"
        "s_cbranch_scc0 .L7_du5\n\t"
        "s_waitcnt vmcnt(2)\n\t"
        "s_cmp_eq_u32 s57, 0\n\t"
        "s_cbranch_scc0 .L7_du5\n\t"
        "s_waitcnt vmcnt(0)\n"
        ".L7_du5:\n\t"
        "v_mfma_f32_4x4x4_16b_bf16 v[32:35], v[12:13], v[104:105], v[32:35] cbsz:1 abid:0\n\t"
        "v_mfma_f32_4x4x4_16b_bf16 v[36:39], v[12:13], v[106:107], v[36:39] cbsz:1 abid:0\n\t"
        "s_sub_u32 s57, s57, 1\n\t"
        "v_mfma_f32_4x4x4_16b_bf16 v[32:35], v[12:13], v[108:109], v[32:35] cbsz:1 abid:1\n\t"
        "v_mfma_f32_4x4x4_16b_bf16 v[36:39], v[12:13], v[110:111], v[36:39] cbsz:1 abid:1\n\t"
        "s_cbranch_scc1 .L7_last\n"
        ".L7_dt6:\n\t"
        "v_perm_b32 v12, v53, v52, v8\n\t"
        "v_perm_b32 v13, v53, v52, v9\n\t"
        "s_waitcnt vmcnt(6)\n\t"
        "s_cmp_lt_u32 s57, 3\n\t"
        "s_cbranch_scc0 .L7_du6\n\t"
        "s_waitcnt vmcnt(2)\n\t"
        "s_cmp_eq_u32 s57, 0\n\t"
        "s_cbranch_scc0 .L7_du6\n\t"
        "s_waitcnt vmcnt(0)\n"
        ".L7_du6:\n\t"
        "v_mfma_f32_4x4x4_16b_bf16 v[32:35], v[12:13], v[112:113], v[32:35] cbsz:1 abid:0\n\t"
        "v_mfma_f32_4x4x4_16b_bf16 v[36:39], v[12:13], v[114:115], v[36:39] cbsz:1 abid:0\n\t"
        "s_sub_u32 s57, s57, 1\n\t"
        "v_mfma_f32_4x4x4_16b_bf16 v[32:35], v[12:13], v[116:117], v[32:35] cbsz:1 abid:1\n\t"
        "v_mfma_f32_4x4x4_16b_bf16 v[36:39], v[12:13], v[118:119], v[36:39] cbsz:1 abid:1\n\t"
        "s_cbranch_scc1 .L7_last\n"
        ".L7_dt7:\n\t"
        "v_perm_b32 v12, v55, v54, v8\n\t"
        "v_perm_b32 v13, v55, v54, v9\n\t"
        "s_waitcnt vmcnt(6)\n\t"
        "s_cmp_lt_u32 s57, 3\n\t"
        "s_cbranch_scc0 .L7_du7\n\t"
        "s_waitcnt vmcnt(2)\n\t"
        "s_cmp_eq_u32 s57, 0\n\t"
        "s_cbranch_scc0 .L7_du7\n\t"
        "s_waitcnt vmcnt(0)\n"
        ".L7_du7:\n\t"
        "v_mfma_f32_4x4x4_16b_bf16 v[32:35], v[12:13], v[120:121], v[32:35] cbsz:1 abid:0\n\t"
        "v_mfma_f32_4x4x4_16b_bf16 v[36:39], v[12:13], v[122:123], v[36:39] cbsz:1 abid:0\n\t"
        "s_sub_u32 s57, s57, 1\n\t"
        "v_mfma_f32_4x4x4_16b_bf16 v[32:35], v[12:13], v[124:125], v[32:35] cbsz:1 abid:1\n\t"
        "v_mfma_f32_4x4x4_16b_bf16 v[36:39], v[12:13], v[126:127], v[36:39] cbsz:1 abid:1\n\t"
        "s_cbranch_scc1 .L7_last\n\t"
        "s_branch .L7_dt0\n"
        ".L7_ie:\n\t"
        "v_and_b32 v63, 0xff, v25\n\t"
        "v_mad_u32_u24 v63, v63, s38, v7\n\t"
        "v_subrev_u32 v63, s60, v63\n\t"
        "s_mov_b32 s56, s25\n\t"
        "s_swappc_b64 s[50:51], s[54:55]\n\t"
        "v_and_b32 v63, 0xff, v26\n\t"
        "v_mad_u32_u24 v63, v63, s38, v7\n\t"
        "v_subrev_u32 v63, s60, v63\n\t"
        "s_mov_b32 s56, s26\n\t"
        "s_swappc_b64 s[50:51], s[54:55]\n\t"
        "v_and_b32 v63, 0xff, v27\n\t"
        "v_mad_u32_u24 v63, v63, s38, v7\n\t"
        "v_subrev_u32 v63, s60, v63\n\t"
        "s_mov_b32 s56, s27\n\t"
        "s_swappc_b64 s[50:51], s[54:55]\n\t"
        "v_and_b32 v63, 0xff, v28\n\t"
        "v_mad_u32_u24 v63, v63, s38, v7\n\t"
        "v_subrev_u32 v63, s60, v63\n\t"
        "s_add_u32 s56, s28, 2\n\t"
        "s_swappc_b64 s[50:51], s[54:55]\n\t"
        "s_mov_b64 vcc, -1\n\t"
        "s_setpc_b64 s[54:55]\n"
        ".L7_ce:\n\t"
        "v_and_b32 v4, 0xffff0000, v24\n\t"
        "s_nop 2\n\t"
        "v_pk_mul_f32 v[32:33], v[32:33], v[4:5] op_sel_hi:[1,0]\n\t"
        "v_pk_mul_f32 v[34:35], v[34:35], v[4:5] op_sel_hi:[1,0]\n\t"
        "v_pk_mul_f32 v[36:37], v[36:37], v[4:5] op_sel_hi:[1,0]\n\t"
        "v_pk_mul_f32 v[38:39], v[38:39], v[4:5] op_sel_hi:[1,0]\n\t"
        "v_cvt_pk_bf16_f32 v12, v32, v33\n\t"
        "v_cvt_pk_bf16_f32 v13, v34, v35\n\t"
        "v_cvt_pk_bf16_f32 v14, v36, v37\n\t"
        "v_cvt_pk_bf16_f32 v15, v38, v39\n"
        ".L7_w117:\n\t"
        "ds_read_b32 v4, v2 offset:12\n\t"
        "s_waitcnt lgkmcnt(0)\n\t"
        "v_cmp_gt_u16 vcc, 4, v4\n\t"
        "s_cbranch_vccz .L7_d118\n\t"
        "s_sleep 1\n\t"
        "s_sub_i32 s83, s83, 1\n\t"
        "s_cmp_gt_i32 s83, 0\n\t"
        "s_cbranch_scc1 .L7_w117\n"
        ".L7_d118:\n\t"
        "s_mov_b64 vcc, 0\n\t"
        "s_setprio 3\n\t"
        "ds_write_b128 v56, v[12:15] offset:23808\n\t"
        "s_mov_b64 exec, 1\n\t"
        "ds_add_u32 v2, v3 offset:52\n\t"
        "s_mov_b64 exec, -1\n\t"
        "v_mov_b64 v[32:33], 0\n\t"
        "v_mov_b64 v[34:35], 0\n\t"
        "v_mov_b64 v[36:37], 0\n\t"
        "v_mov_b64 v[38:39], 0\n\t"
        "s_mov_b32 s57, s25\n\t"
        "s_swappc_b64 s[52:53], s[54:55]\n\t"
        "v_and_b32 v4, 0xffff0000, v25\n\t"
        "s_nop 2\n\t"
        "v_pk_mul_f32 v[32:33], v[32:33], v[4:5] op_sel_hi:[1,0]\n\t"
        "v_pk_mul_f32 v[34:35], v[34:35], v[4:5] op_sel_hi:[1,0]\n\t"
        "v_pk_mul_f32 v[36:37], v[36:37], v[4:5] op_sel_hi:[1,0]\n\t"
        "v_pk_mul_f32 v[38:39], v[38:39], v[4:5] op_sel_hi:[1,0]\n\t"
        "v_cvt_pk_bf16_f32 v12, v32, v33\n\t"
        "v_cvt_pk_bf16_f32 v13, v34, v35\n\t"
        "v_cvt_pk_bf16_f32 v14, v36, v37\n\t"
        "v_cvt_pk_bf16_f32 v15, v38, v39\n\t"
        "ds_write_b128 v57, v[12:15] offset:23808\n\t"
        "s_mov_b64 exec, 1\n\t"
        "ds_add_u32 v61, v3 offset:52\n\t"
        "s_mov_b64 exec, -1\n\t"
        "v_mov_b64 v[32:33], 0\n\t"
        "v_mov_b64 v[34:35], 0\n\t"
        "v_mov_b64 v[36:37], 0\n\t"
        "v_mov_b64 v[38:39], 0\n\t"
        "s_mov_b32 s57, s26\n\t"
        "s_swappc_b64 s[52:53], s[54:55]\n\t"
        "v_and_b32 v4, 0xffff0000, v26\n\t"
        "s_nop 2\n\t"
        "v_pk_mul_f32 v[32:33], v[32:33], v[4:5] op_sel_hi:[1,0]\n\t"
        "v_pk_mul_f32 v[34:35], v[34:35], v[4:5] op_sel_hi:[1,0]\n\t"
        "v_pk_mul_f32 v[36:37], v[36:37], v[4:5] op_sel_hi:[1,0]\n\t"
        "v_pk_mul_f32 v[38:39], v[38:39], v[4:5] op_sel_hi:[1,0]\n\t"
        "v_cvt_pk_bf16_f32 v12, v32, v33\n\t"
        "v_cvt_pk_bf16_f32 v13, v34, v35\n\t"
        "v_cvt_pk_bf16_f32 v14, v36, v37\n\t"
        "v_cvt_pk_bf16_f32 v15, v38, v39\n\t"
        "ds_write_b128 v58, v[12:15] offset:23808\n\t"
        "s_mov_b64 exec, 1\n\t"
        "ds_add_u32 v61, v3 offset:56\n\t"
        "s_mov_b64 exec, -1\n\t"
        "v_mov_b64 v[32:33], 0\n\t"
        "v_mov_b64 v[34:35], 0\n\t"
        "v_mov_b64 v[36:37], 0\n\t"
        "v_mov_b64 v[38:39], 0\n\t"
        "s_mov_b32 s57, s27\n\t"
        "s_swappc_b64 s[52:53], s[54:55]\n\t"
        "v_and_b32 v4, 0xffff0000, v27\n\t"
        "s_nop 2\n\t"
        "v_pk_mul_f32 v[32:33], v[32:33], v[4:5] op_sel_hi:[1,0]\n\t"
        "v_pk_mul_f32 v[34:35], v[34:35], v[4:5] op_sel_hi:[1,0]\n\t"
        "v_pk_mul_f32 v[36:37], v[36:37], v[4:5] op_sel_hi:[1,0]\n\t"
        "v_pk_mul_f32 v[38:39], v[38:39], v[4:5] op_sel_hi:[1,0]\n\t"
        "v_cvt_pk_bf16_f32 v12, v32, v33\n\t"
        "v_cvt_pk_bf16_f32 v13, v34, v35\n\t"
        "v_cvt_pk_bf16_f32 v14, v36, v37\n\t"
        "v_cvt_pk_bf16_f32 v15, v38, v39\n\t"
        "ds_write_b128 v59, v[12:15] offset:21760\n\t"
        "s_mov_b64 exec, 1\n\t"
        "ds_add_u32 v2, v3 offset:64\n\t"
        "s_mov_b64 exec, -1\n\t"
        "v_mov_b64 v[32:33], 0\n\t"
        "v_mov_b64 v[34:35], 0\n\t"
        "v_mov_b64 v[36:37], 0\n\t"
        "v_mov_b64 v[38:39], 0\n\t"
        "s_mov_b32 s57, s28\n\t"
        "s_swappc_b64 s[52:53], s[54:55]\n"
        ".L7_last:\n\t"
        "v_and_b32 v4, 0xffff0000, v28\n\t"
        "s_nop 2\n\t"
        "v_pk_mul_f32 v[32:33], v[32:33], v[4:5] op_sel_hi:[1,0]\n\t"
        "v_pk_mul_f32 v[34:35], v[34:35], v[4:5] op_sel_hi:[1,0]\n\t"
        "v_pk_mul_f32 v[36:37], v[36:37], v[4:5] op_sel_hi:[1,0]\n\t"
        "v_pk_mul_f32 v[38:39], v[38:39], v[4:5] op_sel_hi:[1,0]\n\t"
        "v_cvt_pk_bf16_f32 v12, v32, v33\n\t"
        "v_cvt_pk_bf16_f32 v13, v34, v35\n\t"
        "v_cvt_pk_bf16_f32 v14, v36, v37\n\t"
        "v_cvt_pk_bf16_f32 v15, v38, v39\n\t"
        "ds_write_b128 v60, v[12:15] offset:21760\n\t"
        "s_mov_b64 exec, 1\n\t"
        "ds_add_u32 v2, v3 offset:68\n\t"
        "s_mov_b64 exec, -1\n\t"
        "s_endpgm\n"
        ".L7_x:\n\t"
        "s_mov_b32 s36, 1\n\t"
        "s_waitcnt lgkmcnt(0)\n\t"
        "v_dot4_u32_u8 v83, v67, s39, v7\n\t"
        "v_lshlrev_b32_sdwa v115, 1, v67 dst_sel:DWORD dst_unused:UNUSED_PAD src0_sel:DWORD src1_sel:WORD_0\n\t"
        "ds_read_u16 v115, v115 offset:46336\n\t"
        "v_cmp_eq_u16 vcc, -1, v67\n\t"
        "s_cmp_lg_u64 vcc, 0\n\t"
        "s_cselect_b32 s78, 0x10000, 0\n\t"
        "v_or_b32 v30, s78, v30\n\t"
        "ds_add_rtn_u32 v99, v83, v29 offset:192\n\t"
        "v_dot4_u32_u8 v87, v71, s39, v7\n\t"
        "v_lshlrev_b32_sdwa v119, 1, v71 dst_sel:DWORD dst_unused:UNUSED_PAD src0_sel:DWORD src1_sel:WORD_0\n\t"
        "ds_read_u16 v119, v119 offset:46336\n\t"
        "v_cmp_eq_u16 vcc, -1, v71\n\t"
        "s_cmp_lg_u64 vcc, 0\n\t"
        "s_cselect_b32 s78, 0x10000, 0\n\t"
        "v_or_b32 v30, s78, v30\n\t"
        "ds_add_rtn_u32 v103, v87, v29 offset:192\n\t"
        "v_dot4_u32_u8 v91, v75, s39, v7\n\t"
        "v_lshlrev_b32_sdwa v123, 1, v75 dst_sel:DWORD dst_unused:UNUSED_PAD src0_sel:DWORD src1_sel:WORD_0\n\t"
        "ds_read_u16 v123, v123 offset:46336\n\t"
        "v_cmp_eq_u16 vcc, -1, v75\n\t"
        "s_cmp_lg_u64 vcc, 0\n\t"
        "s_cselect_b32 s78, 0x10000, 0\n\t"
        "v_or_b32 v30, s78, v30\n\t"
        "ds_add_rtn_u32 v107, v91, v29 offset:192\n\t"
        "v_dot4_u32_u8 v95, v79, s39, v7\n\t"
        "v_lshlrev_b32_sdwa v127, 1, v79 dst_sel:DWORD dst_unused:UNUSED_PAD src0_sel:DWORD src1_sel:WORD_0\n\t"
        "ds_read_u16 v127, v127 offset:46336\n\t"
        "v_cmp_eq_u16 vcc, -1, v79\n\t"
        "s_cmp_lg_u64 vcc, 0\n\t"
        "s_cselect_b32 s78, 0x10000, 0\n\t"
        "v_or_b32 v30, s78, v30\n\t"
        "ds_add_rtn_u32 v111, v95, v29 offset:192\n\t"
        "s_branch .L7_y\n"
        ".L7_z:\n\t"
        "s_waitcnt lgkmcnt(0)\n\t"
        "ds_read_b32 v32, v83 offset:192\n\t"
        "ds_read_b32 v33, v87 offset:192\n\t"
        "ds_read_b32 v34, v91 offset:192\n\t"
        "ds_read_b32 v35, v95 offset:192\n\t"
        "v_perm_b32 v115, v115, v67, s49\n\t"
        "v_perm_b32 v119, v119, v71, s49\n\t"
        "v_perm_b32 v123, v123, v75, s49\n\t"
        "v_perm_b32 v127, v127, v79, s49\n\t"
        "s_waitcnt lgkmcnt(0)\n\t"
        "v_perm_b32 v32, v32, v99, s34\n\t"
        "v_sad_u8 v99, v32, 0, 0\n\t"
        "v_perm_b32 v33, v33, v103, s34\n\t"
        "v_sad_u8 v103, v33, 0, 0\n\t"
        "v_perm_b32 v34, v34, v107, s34\n\t"
        "v_sad_u8 v107, v34, 0, 0\n\t"
        "v_perm_b32 v35, v35, v111, s34\n\t"
        "v_sad_u8 v111, v35, 0, 0\n\t"
        "v_min_u32 v99, 47, v99\n\t"
        "v_min_u32 v103, 47, v103\n\t"
        "v_min_u32 v107, 47, v107\n\t"
        "v_min_u32 v111, 47, v111\n\t"
        "v_lshl_add_u32 v99, v99, 2, v83\n\t"
        "ds_write_b32 v99, v115\n\t"
        "v_lshl_add_u32 v103, v103, 2, v87\n\t"
        "ds_write_b32 v103, v119\n\t"
        "v_lshl_add_u32 v107, v107, 2, v91\n\t"
        "ds_write_b32 v107, v123\n\t"
        "v_lshl_add_u32 v111, v111, 2, v95\n\t"
        "ds_write_b32 v111, v127\n\t"
        "s_branch .L7_zr\n"
        ".L7_exit4:\n\t"
        "s_add_u32 s69, s69, 4\n"
        ".L7_exit:\n\t"
        "s_waitcnt vmcnt(0) lgkmcnt(0)\n\t"
        "s_setprio 0\n\t"
        : "+{v0}"(tid), "=&{s69}"(wv)
        : "s"(__builtin_amdgcn_kernarg_segment_ptr()), "s"(blockIdx.x)
        : "scc", "vcc", "memory", "s16", "s17", "s18", "s19", "s20", "s21", "s22", "s23", "s24", "s25", "s26", "s27", "s28", "s29", "s30", "s31", "s32", "s33", "s34", "s35",
          "s36", "s37", "s38", "s39", "s40", "s41", "s42", "s43", "s44", "s45", "s46", "s47", "s48", "s49", "s50", "s51", "s52", "s53", "s54", "s55",
          "s56", "s57", "s58", "s59", "s60", "s61", "s62", "s63", "s64", "s65", "s66", "s67", "s68", "s70", "s71", "s72", "s73", "s74", "s75", "s76",
          "s77", "s78", "s79", "s80", "s81", "s82", "s83", "s84", "s85", "s86", "s87", "s88", "s89", "s90", "s91", "s92", "s93", "s94", "s95", "s96",
          "s97", "s98", "s99", "s100", "s101", "v1", "v2", "v3", "v4", "v5", "v6", "v7", "v8", "v9", "v10", "v11", "v12", "v13", "v14", "v15",
          "v16", "v17", "v18", "v19", "v20", "v21", "v22", "v23", "v24", "v25", "v26", "v27", "v28", "v29", "v30", "v31", "v32", "v33", "v34", "v35",
          "v36", "v37", "v38", "v39", "v40", "v41", "v42", "v43", "v44", "v45", "v46", "v47", "v48", "v49", "v50", "v51", "v52", "v53", "v54", "v55",
          "v56", "v57", "v58", "v59", "v60", "v61", "v62", "v63", "v64", "v65", "v66", "v67", "v68", "v69", "v70", "v71", "v72", "v73", "v74", "v75",
          "v76", "v77", "v78", "v79", "v80", "v81", "v82", "v83", "v84", "v85", "v86", "v87", "v88", "v89", "v90", "v91", "v92", "v93", "v94", "v95",
          "v96", "v97", "v98", "v99", "v100", "v101", "v102", "v103", "v104", "v105", "v106", "v107", "v108", "v109", "v110", "v111", "v112", "v113", "v114", "v115",
          "v116", "v117", "v118", "v119", "v120", "v121", "v122", "v123", "v124", "v125", "v126", "v127");
    const __attribute__((address_space(4))) Args* q = (const __attribute__((address_space(4))) Args*)__builtin_amdgcn_kernarg_segment_ptr();
    asm volatile("" : "+s"(q));
    const u32 wave = __builtin_amdgcn_readfirstlane(wv);
    u32 lane;
    asm volatile("v_mbcnt_lo_u32_b32 %0, -1, 0\n\tv_mbcnt_hi_u32_b32 %0, -1, %0" : "=v"(lane));
    const u32 half = blockIdx.x & 1u, TN = q->TN, r0 = (blockIdx.x >> 1) * TN;
    const u16* x = q->xall;
    _Pragma("nounroll") for (u32 n = 0; n < (1u << 26) && *(volatile __attribute__((address_space(3))) u32*)S_QK < 4u; ++n) __builtin_amdgcn_s_sleep(2);
    if (wave >= 4u) {
        const int E = q->E, *dst = (const int*)q->dst.p, *src = (const int*)q->src.p;
        const u16* dis = q->disall;
        const u32 w = wave - 4u;
        _Pragma("nounroll") for (u32 k = 0; k < 5u; ++k) {
            const u32 grp = (u32)(((w == 0u ? 0x1713050109ull : w == 1u ? 0x1612040008ull : w == 2u ? 0x14100e0602ull : 0x15110f0703ull) >> (8u * k)) & 255u);
            const u32 row = grp * 4u + (lane >> 4), iv = *(const u32*)(smem + L_INV + row * 4u);
            const int me = (iv & 0x8000u) ? -1 : q->nbase + (int)(r0 + (iv & 255u) + TN - 255u);
            const int m0 = __builtin_amdgcn_readlane(me, 0), m1 = __builtin_amdgcn_readlane(me, 16), m2 = __builtin_amdgcn_readlane(me, 32), m3 = __builtin_amdgcn_readlane(me, 48);
            float s[8] = {0.f, 0.f, 0.f, 0.f, 0.f, 0.f, 0.f, 0.f};
            for (int e0 = 0; e0 < E; e0 += 64) {
                const int e = min(e0 + (int)lane, E - 1), d = dst[e], si = src[e];
                unsigned long long m = __ballot(e0 + (int)lane < E && (d == m0 || d == m1 || d == m2 || d == m3));
                while (m) {
                    const int l = __builtin_ctzll(m);
                    m &= m - 1;
                    const int sl = __builtin_amdgcn_readlane(si, l);
                    if (__builtin_amdgcn_readlane(d, l) == me) {
                        const u32x4 r = *(const u32x4*)(x + (size_t)sl * 256 + half * 128u + (lane & 15u) * 8u);
                        const float wt = bf2f(dis[sl]);
#pragma unroll
                        for (int j = 0; j < 4; ++j) { s[2 * j] += wt * lo(r[j]); s[2 * j + 1] += wt * hi(r[j]); }
                    }
                }
            }
            const float sc = bf2f(dis[max(me, 0)]);
            *(u32x4*)(smem + L_AL + (row < 48u ? row : row - 8u) * 256u + (((lane ^ row) & 15u) << 4)) =
                (u32x4){pack2(s[0] * sc, s[1] * sc), pack2(s[2] * sc, s[3] * sc), pack2(s[4] * sc, s[5] * sc), pack2(s[6] * sc, s[7] * sc)};
            lds_signal(S_T0 + 4u * (grp < 12u ? 1u + ((grp >> 2) & 1u) : grp >> 2), 1u);
        }
        return;
    }
    const int Ee = q->Ee, *edst = (const int*)q->edst.p, *esrc = (const int*)q->esrc.p;
    const u16 *wq = (const u16*)q->wq.p, *wk = (const u16*)q->wk.p;
    float* qs = (float*)(smem + L_QK + wave * 1024u);
    _Pragma("nounroll") for (u32 n = wave; n < TN && r0 + n < q->N; n += 4u) {
        const int me = q->nbase + (int)(r0 + n);
        float c0 = 0.f, c1 = 0.f, c2 = 0.f, c3 = 0.f;
        _Pragma("nounroll") for (int i = 0; i < 256; ++i) {
            const float xv = bf2f(x[(size_t)me * 256 + i]);
            const u32x2 r = *(const u32x2*)(wq + i * 256 + lane * 4u);
            c0 += xv * lo(r[0]); c1 += xv * hi(r[0]); c2 += xv * lo(r[1]); c3 += xv * hi(r[1]);
        }
        qs[lane * 4u] = c0; qs[lane * 4u + 1u] = c1; qs[lane * 4u + 2u] = c2; qs[lane * 4u + 3u] = c3;
        float k[4] = {0.f, 0.f, 0.f, 0.f};
        _Pragma("nounroll") for (int c = 0; c < 256; c += 2) {
            const float qa = qs[c], qb = qs[c + 1];
#pragma unroll
            for (int j = 0; j < 4; ++j) { const u32 r = *(const u32*)(wk + (lane * 4u + j) * 256u + c); k[j] += qa * lo(r) + qb * hi(r); }
        }
        float s0 = 0.f, s1 = 0.f, s2 = 0.f, s3 = 0.f, dn = 0.f;
        for (int e0 = 0; e0 < Ee; e0 += 64) {
            const int e = min(e0 + (int)lane, Ee - 1), si = esrc[e];
            unsigned long long m = __ballot(e0 + (int)lane < Ee && edst[e] == me);
            while (m) {
                const int l = __builtin_ctzll(m);
                m &= m - 1;
                const u32x2 r = *(const u32x2*)(x + (size_t)__builtin_amdgcn_readlane(si, l) * 256 + lane * 4u);
                float d = k[0] * lo(r[0]) + k[1] * hi(r[0]) + k[2] * lo(r[1]) + k[3] * hi(r[1]);
#pragma unroll
                for (int o = 1; o < 64; o <<= 1)
                    d += __uint_as_float((u32)__builtin_amdgcn_ds_bpermute((int)((lane ^ (u32)o) * 4u), (int)__float_as_uint(d)));
                const float wt = __builtin_amdgcn_exp2f(d * 0.09016844f);
                s0 += wt * lo(r[0]); s1 += wt * hi(r[0]); s2 += wt * lo(r[1]); s3 += wt * hi(r[1]);
                dn += wt;
            }
        }
        const float sc = __builtin_amdgcn_rcpf(dn + 1e-9f);
        if ((lane >> 5) == half)
            *(u32x2*)(smem + L_AA + ((u32)*(const u16*)(smem + L_MAP + n * 2u) ^ ((lane & 31u) * 8u))) =
                (u32x2){pack2(s0 * sc, s1 * sc), pack2(s2 * sc, s3 * sc)};
    }
    lds_signal(S_Z, 17u);
}

extern "C" void kernel_launch(void* const* d_in, const int* in_sizes, int n_in,
                              void* d_out, int out_size, void* d_ws, size_t ws_size,
                              hipStream_t stream) {
    const u32 N = in_sizes[5], E = in_sizes[1], Ee = in_sizes[3];
    const u16* x = (const u16*)d_in[0];
    const u16* dis = (const u16*)d_in[5];
    const u32 FL = 0x00020000u;
    const bool generic = N > NMAX || E > EMAX || (E & 3u) || (N & 1u);
    const bool irr = N > NMAX || Ee != 4u * N;
    (void)hipFuncSetAttribute((const void*)agg_kernel, hipFuncAttributeMaxDynamicSharedMemorySize, LDS_BYTES);
    for (u32 nbase = 0; nbase < N; nbase += NMAX) {
        const u32 n = N - nbase < NMAX ? N - nbase : NMAX;
        Args a;
        a.x = Desc{x + (size_t)nbase * 256, n * 512u, FL};
        a.ws = Desc{d_ws, WS_END, FL};
        a.wl = Desc{d_in[6], 131072u, FL}; a.wq = Desc{d_in[7], 131072u, FL}; a.wk = Desc{d_in[8], 131072u, FL}; a.wv = Desc{d_in[9], 131072u, FL};
        a.src = Desc{d_in[1], generic ? 0u : E * 4u, FL}; a.dst = Desc{d_in[2], generic ? 0u : E * 4u, FL};
        a.esrc = Desc{d_in[3], irr ? 0u : Ee * 4u, FL}; a.edst = Desc{d_in[4], irr ? 0u : Ee * 4u, FL};
        a.dis = Desc{dis + nbase, n * 2u, FL};
        a.out = Desc{(u16*)d_out + (size_t)nbase * 256, n * 512u, FL};
        a.N = n; a.TN = n > 256u ? (n + 127u) / 128u : 2u; a.M = ((1u << 24) + a.TN - 1u) / a.TN;
        a.irr = (irr ? 1u : 0u) | (generic ? 0x10000u : 0u);
        a.xall = x; a.disall = dis;
        a.E = (int)E; a.Ee = (int)Ee; a.nbase = (int)nbase; a.pad = 0;
        agg_kernel<<<256, 1024, LDS_BYTES, stream>>>(a);
    }
}
```
